```python
import torch

def _softmax(x, axis=-1):
    return torch.softmax(x, dim=axis)
def _gelu(x):
    return torch.nn.functional.gelu(x, approximate="tanh")
def _silu(x):
    return torch.nn.functional.silu(x)
def _sigmoid(x):
    return torch.sigmoid(x)
def _layernorm(x, axis=-1, eps=1e-5):
    m = x.mean(dim=axis, keepdim=True)
    v = x.var(dim=axis, keepdim=True, unbiased=False)
    return (x - m) / torch.sqrt(v + eps)
def _scatter_add(tgt, idx, src, axis=0):
    return tgt.clone().index_add_(axis, idx, src)
def _segment_sum(src, seg, n):
    out = torch.zeros((n,) + src.shape[1:], dtype=src.dtype)
    return out.index_add_(0, seg, src)
def _pad(x, pw):
    flat = tuple(v for pair in reversed(pw) for v in pair)
    return torch.nn.functional.pad(x, flat)

kernel_name = 'recurrent_scan__ssm_conv_h3__mixer_token_mlp__prompt'

def setup_inputs():

    _w = (lambda a: (lambda t: t.to(torch.bfloat16) if t.is_floating_point() else t)(torch.as_tensor(a)))
    def _A_setup():
        B, S, D = 1, 2048, 576
        ks = 4
        return {
            "q": _w((lambda *s: torch.randn(*s, dtype=torch.bfloat16) * 0.05)(B, S, D)),
            "k": _w((lambda *s: torch.randn(*s, dtype=torch.bfloat16) * 0.8)(B, S, D)),
            "v": _w((lambda *s: torch.randn(*s, dtype=torch.bfloat16) * 0.8)(B, S, D)),
            "shift_k": _w((lambda *s: torch.randn(*s, dtype=torch.bfloat16) * 0.8)(D, ks) * 0.5),
            "ssm_k": _w((lambda *s: torch.randn(*s, dtype=torch.bfloat16) * 0.8)(D, S) * (1.0 / S ** 0.5)),
            "ssm_D": _w((lambda *s: torch.randn(*s, dtype=torch.bfloat16) * 0.8)(D)),
        }
    _A_in = _A_setup()

    def _B_setup():
        B, S, D, F = 1, 2048, 576, 4608
        return {
            "x": _w((lambda *s: torch.randn(*s, dtype=torch.bfloat16) * 0.05)(B, S, D)),
            "w1": _w((lambda *s: torch.randn(*s, dtype=torch.bfloat16) * 0.8)(S, F) * (S ** -0.5)),
            "b1": _w((lambda *s: torch.randn(*s, dtype=torch.bfloat16) * 12.8)(F) * 0.02),
            "w2": _w((lambda *s: torch.randn(*s, dtype=torch.bfloat16) * 0.2)(F, S) * (F ** -0.5)),
            "b2": _w((lambda *s: torch.randn(*s, dtype=torch.bfloat16) * 3.2)(S) * 0.02),
        }
    _B_in = _B_setup()

    return {
        "A_q": _A_in["q"],
        "A_k": _A_in["k"],
        "A_v": _A_in["v"],
        "A_shift_k": _A_in["shift_k"],
        "A_ssm_k": _A_in["ssm_k"],
        "A_ssm_D": _A_in["ssm_D"],
        "B_x": _B_in["x"],
        "B_w1": _B_in["w1"],
        "B_b1": _B_in["b1"],
        "B_w2": _B_in["w2"],
        "B_b2": _B_in["b2"],
    }

def reference(A_q, A_k, A_v, A_shift_k, A_ssm_k, A_ssm_D, B_x, B_w1, B_b1, B_w2, B_b2):
    def _A_ref(q, k, v, shift_k, ssm_k, ssm_D):
        B, S, D = q.shape
        ks = shift_k.shape[-1]
        k_pad = _pad(k, ((0, 0), (ks - 1, 0), (0, 0)))
        k_shift = torch.zeros_like(k)
        for t in range(ks):
            k_shift = k_shift + k_pad[:, t:t + S, :] * shift_k[:, ks - 1 - t][None, None, :]
        kv = k_shift * v
        L = 2 * S
        kv_pad = _pad(kv, ((0, 0), (0, S), (0, 0)))
        kf_pad = _pad(ssm_k, ((0, 0), (0, S)))
        kv_f = (lambda x, axes=(-2,-1): torch.fft.rfft2(x.float(), dim=axes))(kv_pad[:, None, :, :], axes=(1, 2))
        kf_f = (lambda x, axes=(-2,-1): torch.fft.rfft2(x.float(), dim=axes))(kf_pad[:, None, :], axes=(1, 2))
        kf_f = (lambda x, *d: x.permute(*d))(kf_f, 1, 2, 0)
        y_f = kv_f * kf_f[None, :, :, :]
        y = (lambda x, s, axes=(-2,-1): torch.fft.irfft2(x, s=s, dim=axes).to(torch.bfloat16))(y_f, (1, L), axes=(1, 2))[:, 0, :S, :]
        y = y + kv * ssm_D[None, None, :]
        return q * y

    def _B_ref(x, w1, b1, w2, b2):
        B, S, D = x.shape
        h = _layernorm(x, axis=-1)
        u = torch.einsum("bsd,sf->bdf", h, w1) + b1
        u = _gelu(u)
        y = torch.einsum("bdf,fs->bds", u, w2) + b2
        return (lambda x, *d: x.permute(*d))(y, 0, 2, 1)

    x = A_q
    xn = _layernorm(x, axis=-1)
    s = _A_ref(xn, A_k, A_v, A_shift_k, A_ssm_k, A_ssm_D)
    g = _silu(_B_ref(xn, B_w1, B_b1, B_w2, B_b2))
    return x + s * g
```

```cpp
#include <hip/hip_runtime.h>
#include <hip/hip_bf16.h>

typedef unsigned short u16;
typedef unsigned long long u64;
typedef __attribute__((ext_vector_type(2))) float f32x2;
typedef __attribute__((ext_vector_type(4))) float f32x4;
typedef __attribute__((ext_vector_type(16))) float f32x16;
typedef __attribute__((ext_vector_type(4))) unsigned u32x4;
typedef __attribute__((ext_vector_type(2))) unsigned u32x2;
typedef __attribute__((ext_vector_type(2))) __bf16 bf16x2_t;

#define S_LEN 2048
#define D_DIM 576
#define F_DIM 4608
#define ROWB (D_DIM * 2)

#define WS_E 0u
#define WS_HF 1024u
#define WS_KVF (WS_HF + 16384u)
#define WS_CF (WS_KVF + 8192u + 24576u)
#define WS_XR (WS_CF + 2048u)
#define WS_ST (WS_XR + 6144u)
#define WS_HT 131072u
#define WS_KV (WS_HT + 2359296u)
#define WS_SG (WS_KV + 2359296u)
#define WS_XB (WS_SG + 2359296u)
#define WS_U (WS_XB + 9437184u)
#define WS_UF (WS_U + 5308416u)
#define WS_END (WS_UF + 393216u)
#define OOB 0x7ff00000u

__device__ __forceinline__ float bf2f(u16 v) { return __uint_as_float(((unsigned)v) << 16); }
__device__ __forceinline__ float lo_f(unsigned u) { return __uint_as_float(u << 16); }
__device__ __forceinline__ float hi_f(unsigned u) { return __uint_as_float(u & 0xffff0000u); }
__device__ __forceinline__ unsigned pk2(float a, float b) {
    bf16x2_t r = __builtin_convertvector(f32x2{a, b}, bf16x2_t);
    return __builtin_bit_cast(unsigned, r);
}
__device__ __forceinline__ unsigned pk2(f32x2 v) { return pk2(v[0], v[1]); }
__device__ __forceinline__ f32x2 unpk(unsigned u) { return f32x2{lo_f(u), hi_f(u)}; }
__device__ __forceinline__ f32x2 rbf2(f32x2 v) { return unpk(pk2(v)); }
__device__ __forceinline__ float rbf(float f) { return lo_f(pk2(f, f)); }

template <int CTRL>
__device__ __forceinline__ float dpp_add(float v) {
    int t = __builtin_amdgcn_update_dpp(0, __float_as_int(v), CTRL, 0xf, 0xf, false);
    return v + __int_as_float(t);
}
__device__ __forceinline__ float half_sum(float v) {
    v = dpp_add<0x128>(v);
    v = dpp_add<0x124>(v);
    v = dpp_add<0x122>(v);
    v = dpp_add<0x121>(v);
    const unsigned u = __float_as_uint(v);
    const auto r = __builtin_amdgcn_permlane16_swap(u, u, false, false);
    return __uint_as_float(r[0]) + __uint_as_float(r[1]);
}

__device__ __forceinline__ u32x4 make_srd(const void* base, unsigned nbytes) {
    u64 p = (u64)base;
    u32x4 r;
    r[0] = __builtin_amdgcn_readfirstlane((unsigned)p);
    r[1] = __builtin_amdgcn_readfirstlane((unsigned)(p >> 32)) & 0xffffu;
    r[2] = __builtin_amdgcn_readfirstlane(nbytes);
    r[3] = 0x00020000u;
    return r;
}
__device__ __forceinline__ int lane_id() {
    int l;
    asm volatile("v_mbcnt_lo_u32_b32 %0, -1, 0\n\tv_mbcnt_hi_u32_b32 %0, -1, %0" : "=v"(l));
    return l & 63;
}
__device__ __forceinline__ void dma16(unsigned voff, u32x4 srd, unsigned soff, unsigned dst) {
    asm volatile("s_mov_b32 m0, %3\n\ts_nop 0\n\tbuffer_load_dwordx4 %0, %1, %2 offen lds" ::"v"(voff), "s"(srd), "s"(soff), "s"(dst) : "memory");
}
#define RFL(x) __builtin_amdgcn_readfirstlane((unsigned)(x))
#define ST16_SC1(v, off, srd) asm volatile("buffer_store_dwordx4 %0, %1, %2, 0 offen sc1\n\ts_nop 1" ::"v"(v), "v"(off), "s"(srd) : "memory")
#define ST8_SC1(v, off, srd) asm volatile("buffer_store_dwordx2 %0, %1, %2, 0 offen sc1" ::"v"(v), "v"(off), "s"(srd) : "memory")
#define BARRIER() asm volatile("s_waitcnt lgkmcnt(0)\n\ts_barrier" ::: "memory")
#define DRAIN() asm volatile("s_waitcnt vmcnt(0)" ::: "memory")

__device__ __forceinline__ void wait_flags(unsigned off, u32x4 srd, u32x2 tag) {
#pragma unroll 1
    for (unsigned spins = 0; spins < (1u << 20); ++spins) {
        u32x2 f;
        asm volatile("buffer_load_dwordx2 %0, %1, %2, 0 offen sc1\n\ts_waitcnt vmcnt(0)" : "=v"(f) : "v"(off), "s"(srd) : "memory");
        if (__all(f[0] == tag[0] && f[1] == tag[1])) break;
        __builtin_amdgcn_s_sleep(1);
    }
}

#define HSTAMP(i)
#define CSTAMP(i)

#define CODE_BYTES 65536
#define T1S 20
#define MAIN_ASM \
    "s_load_dwordx16 s[8:23], s[0:1], 0\n\ts_load_dwordx8 s[76:83], s[0:1], 112\n\ts_load_dwordx4 s[84:87], s[0:1], 144\n\ts_load_dwordx8 s[24:31], s[0:1], 64\n\ts_load_dwordx4 s[32:35], s[0:1], 96\n\ts_getpc_b64 s[2:3]\n\tv_lshrrev_b32 v245, 6, v240\n\tv_and_b32 v240, 63, v240\n\t" \
    "v_readfirstlane_b32 s38, v245\n\ts_and_b32 s2, s2, 0xffffff80\n\ts_mov_b32 s4, s2\n\ts_mov_b32 s5, s3\n\ts_mov_b32 s6, 65536\n\ts_mov_b32 s7, 0x20000\n\tv_lshl_add_u32 v16, v245, 16, v240\n\tv_lshlrev_b32 v16, 7, v16\n\t" \
    "s_lshr_b32 s65, s40, 3\n\ts_lshl_b32 s73, s65, 13\n\tbuffer_load_dword v57, v16, s[4:7], s73 offen\n\tv_lshlrev_b32 v8, 4, v240\n\tv_and_b32 v244, 0xf0, v8\n\tv_lshrrev_b32 v245, 4, v240\n\tv_lshrrev_b32 v20, 5, v240\n\tv_mul_u32_u24 v20, 1152, v20\n\t" \
    "v_and_b32 v21, 31, v240\n\tv_lshl_add_u32 v22, v21, 2, v20\n\tv_lshl_add_u32 v21, v21, 4, v20\n\ts_lshl_b32 s68, s40, 2\n\ts_add_u32 s68, s68, s38\n\ts_mul_i32 s68, s68, 2304\n\ts_lshr_b32 s57, s38, 1\n\ts_and_b32 s58, s38, 1\n\t" \
    "s_cmp_lt_u32 s65, 27\n\ts_cselect_b32 s39, 1, 0\n\ts_cmp_lt_u32 s65, 24\n\ts_cselect_b32 s39, 2, s39\n\ts_sub_u32 s66, s65, 27\n\ts_mul_i32 s66, s66, 6\n\ts_cmp_lt_u32 s65, 27\n\ts_cselect_b32 s65, s65, s66\n\t" \
    "s_and_b32 s66, s40, 7\n\ts_mul_i32 s66, s66, 27\n\ts_add_u32 s65, s65, s66\n\ts_mul_i32 s42, s65, 171\n\ts_lshr_b32 s42, s42, 10\n\ts_mul_i32 s66, s42, 6\n\ts_sub_u32 s41, s65, s66\n\ts_mul_i32 s65, s42, 57\n\t" \
    "s_lshr_b32 s65, s65, 9\n\ts_mul_i32 s65, s65, 9\n\ts_sub_u32 s65, s42, s65\n\ts_lshl_b32 s66, s41, 2\n\ts_lshr_b32 s66, 5444640, s66\n\ts_and_b32 s66, s66, 15\n\ts_cmp_eq_u32 s65, 4\n\ts_cselect_b32 s43, s66, s41\n\t" \
    "s_mul_i32 s43, s43, 516\n\ts_lshr_b32 s43, s43, 8\n\ts_and_b32 s65, s40, 7\n\ts_mul_i32 s52, s65, 0\n\ts_mul_i32 s65, s65, 0\n\ts_lshr_b32 s65, s65, 1\n\ts_add_u32 s43, s43, s65\n\ts_cmp_ge_u32 s43, 12\n\t" \
    "s_cselect_b32 s66, 12, 0\n\ts_sub_u32 s43, s43, s66\n\ts_cmp_eq_u32 s39, 0\n\ts_cselect_b32 s43, 0, s43\n\ts_cselect_b32 s67, 3538944, 0\n\ts_sub_u32 s70, s43, 12\n\ts_mul_i32 s65, s57, 9437184\n\ts_add_u32 s67, s67, s65\n\t" \
    "s_lshl_b32 s66, s42, 8\n\ts_add_u32 s67, s67, s66\n\tv_lshrrev_b32 v16, 3, v240\n\ts_lshl_b32 s73, s58, 4\n\tv_add_u32 v16, s73, v16\n\tv_mul_u32_u24 v16, 9216, v16\n\tv_and_b32 v17, 0x70, v8\n\tv_and_b32 v18, 16, v240\n\t" \
    "v_lshlrev_b32 v18, 1, v18\n\tv_xor_b32 v17, v17, v18\n\tv_add_u32 v10, v16, v17\n\tv_xor_b32 v11, 64, v10\n\tv_add_u32 v11, 73728, v11\n\ts_lshl_b32 s72, s57, 16\n\ts_lshl_b32 s73, s58, 11\n\ts_add_u32 s72, s72, s73\n\t" \
    "s_lshl_b32 s54, s38, 10\n\tv_add_u32 v9, 4096, v8\n\tv_lshlrev_b32 v239, 3, v240\n\ts_mul_i32 s73, s40, 9216\n\ts_add_u32 s73, s73, s54\n\tv_add_u32 v23, s73, v8\n\tv_subrev_u32 v19, 3456, v23\n\tv_and_b32 v88, 31, v240\n\t" \
    "v_lshrrev_b32 v89, 5, v240\n\ts_lshl_b32 s73, s38, 1\n\tv_add_u32 v89, s73, v89\n\tv_mul_u32_u24 v90, 1168, v89\n\tv_add_u32 v90, 153600, v90\n\tv_lshl_add_u32 v91, v88, 2, v90\n\tv_lshl_add_u32 v90, v88, 4, v90\n\tv_cmp_eq_u32 vcc, 0, v88\n\t" \
    "v_lshlrev_b32 v89, 3, v89\n\tv_mov_b32 v92, 0x7ff00000\n\tv_cndmask_b32 v89, v92, v89, vcc\n\tv_bfe_u32 v88, v240, 2, 2\n\tv_and_b32 v92, 3, v240\n\tv_lshlrev_b32 v92, 3, v92\n\tv_lshl_add_u32 v92, v245, 5, v92\n\ts_lshl_b32 s73, s38, 7\n\t" \
    "v_add_u32 v93, s73, v92\n\tv_mul_u32_u24 v94, 1168, v88\n\tv_add_u32 v94, 153600, v94\n\tv_add_u32 v95, v94, v92\n\tv_add_u32 v94, v94, v93\n\tv_mul_u32_u24 v88, 1152, v88\n\tv_add_u32 v88, 131072, v88\n\tv_add_u32 v92, v88, v92\n\t" \
    "v_add_u32 v93, v88, v93\n\tv_lshl_add_u32 v88, v245, 10, v244\n\ts_lshr_b32 s61, s40, 2\n\ts_mul_i32 s61, s61, 36864\n\ts_and_b32 s65, s40, 3\n\ts_lshl_b32 s65, s65, 8\n\ts_add_u32 s61, s61, s65\n\ts_add_u32 s61, s61, 131072\n\t" \
    "s_mul_i32 s62, s40, 9216\n\ts_add_u32 s62, s62, 2490368\n\ts_lshl_b32 s63, s40, 3\n\ts_lshl_b32 s48, s38, 11\n\ts_add_u32 s48, s48, s63\n\ts_add_u32 s48, s48, 17408\n\ts_add_u32 s63, s63, 1024\n\ts_cmp_eq_u32 s38, 0\n\t" \
    "s_cselect_b32 s63, s63, 2146435072\n\ts_lshl_b32 s64, s40, 6\n\ts_add_u32 s64, s64, 58368\n\ts_waitcnt lgkmcnt(0)\n\tbuffer_load_dwordx4 v[48:51], v21, s[20:23], s68 offen\n\tbuffer_load_dwordx4 v[52:55], v21, s[20:23], s68 offen offset:512\n\tbuffer_load_dword v56, v22, s[20:23], s68 offen offset:1024\n\tbuffer_load_dwordx2 v[58:59], off, s[8:11], 0 sc1\n\t" \
    "s_add_u32 s12, s12, s67\n\ts_addc_u32 s13, s13, 0\n\ts_sub_u32 s14, s14, s67\n\ts_add_u32 m0, s54, 131072\n\ts_nop 0\n\tbuffer_load_dwordx4 v19, s[76:79], 0 offen lds\n\ts_and_b32 s4, s40, 7\n\ts_lshl_b32 s47, s4, 11\n\t" \
    "s_lshl_b32 s65, s57, 10\n\ts_add_u32 s47, s47, s65\n\tv_add_u32 v19, 4096, v19\n\ts_add_u32 m0, s54, 135168\n\ts_nop 0\n\tbuffer_load_dwordx4 v19, s[76:79], 0 offen lds\n\ts_add_u32 s47, s47, 1024\n\ts_mul_i32 s44, s57, 1179648\n\t" \
    "s_mul_i32 s65, s41, 6144\n\ts_add_u32 s44, s44, s65\n\tv_add_u32 v19, 4096, v19\n\ts_add_u32 m0, s54, 139264\n\ts_nop 0\n\tbuffer_load_dwordx4 v19, s[76:79], 0 offen lds\n\ts_mul_i32 s65, s58, 3072\n\ts_add_u32 s44, s44, s65\n\t" \
    "s_add_u32 s44, s44, 131072\n\ts_lshr_b32 s5, s4, 1\n\tv_add_u32 v19, 4096, v19\n\ts_add_u32 m0, s54, 144384\n\ts_nop 0\n\tbuffer_load_dwordx4 v23, s[80:83], 0 offen lds\n\tv_bfe_u32 v16, v240, 2, 2\n\tv_and_b32 v17, 3, v240\n\t" \
    "v_lshlrev_b32 v17, 3, v17\n\tv_lshl_add_u32 v16, v16, 7, v17\n\tv_add_u32 v23, 4096, v23\n\ts_add_u32 m0, s54, 148480\n\ts_nop 0\n\tbuffer_load_dwordx4 v23, s[80:83], 0 offen lds\n\tv_bfe_u32 v17, v240, 3, 2\n\tv_lshl_add_u32 v16, v17, 5, v16\n\t" \
    "s_mul_i32 s65, s57, 65600\n\tv_xor_b32 v17, s65, v16\n\tv_add_u32 v23, 4096, v23\n\tv_subrev_u32 v19, s54, v19\n\tv_subrev_u32 v23, s54, v23\n\ts_cmp_eq_u32 s58, 1\n\ts_cselect_b64 vcc, -1, 0\n\ts_cselect_b64 s[76:77], s[80:81], s[76:77]\n\t" \
    "s_mov_b32 m0, 143360\n\ts_cselect_b32 m0, 152576, m0\n\tv_cndmask_b32 v19, v19, v23, vcc\n\tbuffer_load_dwordx4 v19, s[76:79], 0 offen lds\n\tv_lshl_add_u32 v0, v245, 10, v17\n\ts_and_b32 s65, s5, 1\n\ts_xor_b32 s65, s65, 1\n\ts_lshl_b32 s65, s65, 6\n\t" \
    "s_lshr_b32 s71, s54, 1\n\tbuffer_load_dwordx2 v[60:61], v239, s[84:87], s71 offen\n\tv_xor_b32 v17, s65, v16\n\tv_lshl_add_u32 v4, v245, 9, v17\n\tv_xor_b32 v1, 32, v0\n\tv_xor_b32 v5, 32, v4\n\tbuffer_load_dwordx2 v[62:63], v239, s[84:87], s71 offen offset:2048\n\tv_xor_b32 v2, 64, v0\n\t" \
    "v_xor_b32 v6, 64, v4\n\tv_xor_b32 v3, 96, v0\n\tv_xor_b32 v7, 96, v4\n\ts_mov_b32 s71, 4096\n\tbuffer_load_dwordx2 v[64:65], v239, s[84:87], s71 offen\n\tv_add_u32 v224, 0x10000, v4\n\tv_add_u32 v225, 0x10000, v5\n\tv_add_u32 v226, 0x10000, v6\n\t" \
    "v_add_u32 v227, 0x10000, v7\n\ts_cmp_ge_u32 s43, 12\n\ts_cselect_b32 s69, s70, s43\n\ts_mul_i32 s69, s69, 294912\n\ts_add_u32 s69, s69, 0\n\ts_add_u32 m0, s72, 0\n\ts_nop 0\n\tbuffer_load_dwordx4 v10, s[12:15], s69 offen lds\n\t" \
    "s_mul_i32 s65, s38, 6144\n\ts_add_u32 s65, s65, 131072\n\tv_add_u32 v237, s65, v8\n\ts_xor_b32 s65, s38, 2\n\ts_add_u32 m0, s72, 1024\n\ts_nop 0\n\tbuffer_load_dwordx4 v11, s[12:15], s69 offen lds\n\ts_mul_i32 s65, s65, 6144\n\t" \
    "s_add_u32 s65, s65, 131072\n\tv_add_u32 v238, s65, v8\n\tv_mov_b32 v246, 0xbdd2d3e8\n\tv_mov_b32 v247, 0xc0135761\n\ts_lshr_b32 s65, s40, 3\n\ts_mul_i32 s66, s65, 43\n\ts_lshr_b32 s66, s66, 7\n\ts_mul_i32 s6, s66, 3\n\t" \
    "s_sub_u32 s6, s65, s6\n\ts_and_b32 s66, s66, 7\n\ts_and_b32 s7, s4, 1\n\ts_lshl_b32 s7, s7, 3\n\ts_add_u32 s7, s7, s66\n\ts_mul_i32 s65, s5, 4718592\n\ts_lshl_b32 s67, s7, 8\n\ts_add_u32 s65, s65, s67\n\t" \
    "s_add_u32 s16, s16, s65\n\ts_addc_u32 s17, s17, 0\n\ts_sub_u32 s18, s18, s65\n\ts_cmp_eq_u32 s39, 2\n\ts_cselect_b32 s18, s18, 0\n\tv_lshrrev_b32 v16, 3, v240\n\ts_lshl_b32 s65, s38, 3\n\tv_add_u32 v16, s65, v16\n\t" \
    "v_and_b32 v17, 0x70, v8\n\tv_lshlrev_b32 v18, 5, v245\n\tv_xor_b32 v17, v17, v18\n\tv_lshl_add_u32 v16, v16, 12, v17\n\ts_lshr_b32 s65, s5, 1\n\ts_lshl_b32 s65, s65, 7\n\tv_add_u32 v13, s65, v16\n\tv_xor_b32 v12, 128, v13\n\t" \
    "s_lshl_b32 s65, s7, 7\n\ts_lshl_b32 s66, s5, 5\n\ts_add_u32 s65, s65, s66\n\ts_lshl_b32 s59, s65, 3\n\ts_add_u32 s59, s59, 58368\n\ts_lshl_b32 s66, s65, 1\n\ts_add_u32 s32, s32, s66\n\ts_addc_u32 s33, s33, 0\n\t" \
    "s_sub_u32 s34, s34, s66\n\ts_lshl_b32 s66, s6, 2\n\ts_add_u32 s66, s66, s38\n\ts_mul_i32 s67, s66, 96\n\ts_mul_i32 s65, s65, 1152\n\ts_add_u32 s65, s65, s67\n\ts_add_u32 s20, s20, s65\n\ts_addc_u32 s21, s21, 0\n\t" \
    "s_sub_u32 s22, s22, s65\n\ts_add_u32 s24, s24, s65\n\ts_addc_u32 s25, s25, 0\n\ts_sub_u32 s26, s26, s65\n\ts_add_u32 s51, s67, 50176\n\ts_lshl_b32 s66, s66, 2\n\ts_add_u32 s66, s66, s5\n\ts_mul_i32 s46, s66, 110592\n\t" \
    "s_add_u32 s46, s46, 16646144\n\ts_cmp_eq_u32 s39, 2\n\ts_cselect_b32 s46, s46, 2146435072\n\ts_lshl_b32 s50, s66, 13\n\ts_add_u32 s50, s50, 21954560\n\tv_and_b32 v16, 31, v240\n\tv_mul_u32_u24 v243, 1152, v16\n\tv_lshrrev_b32 v16, 5, v240\n\t" \
    "v_lshl_add_u32 v243, v16, 4, v243\n\tv_lshrrev_b32 v242, 1, v244\n\ts_mul_i32 s65, s42, 57\n\ts_lshr_b32 s65, s65, 9\n\ts_mul_i32 s66, s65, 9\n\ts_sub_u32 s66, s42, s66\n\ts_lshl_b32 s66, s66, 1\n\ts_add_u32 s66, s66, s57\n\t" \
    "s_lshl_b32 s67, s41, 1\n\ts_add_u32 s67, s67, s58\n\ts_lshl_b32 s67, s67, 2\n\ts_add_u32 s65, s65, s67\n\ts_lshl_b32 s49, s65, 13\n\ts_lshl_b32 s67, s66, 7\n\ts_add_u32 s49, s49, s67\n\ts_add_u32 s49, s49, 21954560\n\t" \
    "s_mul_i32 s45, s65, 36\n\ts_add_u32 s45, s45, s66\n\ts_mul_i32 s45, s45, 3072\n\ts_add_u32 s45, s45, 16646144\n\tv_min_u32 v241, 17, v240\n\tv_lshlrev_b32 v241, 7, v241\n\ts_lshl_b32 s65, s6, 4\n\ts_add_u32 s65, s65, s7\n\t" \
    "s_lshl_b32 s65, s65, 2\n\ts_add_u32 s65, s65, s5\n\ts_lshl_b32 s65, s65, 2\n\ts_add_u32 s53, s65, s38\n\ts_mul_i32 s55, s53, 12288\n\ts_add_u32 s55, s55, 7208960\n\ts_cmp_eq_u32 s39, 2\n\ts_cselect_b32 s55, s55, 2146435072\n\t" \
    "s_mul_i32 s56, s53, 3072\n\ts_add_u32 s56, s56, 4849664\n\ts_or_b32 s65, s40, s38\n\ts_cselect_b32 s60, 2146435072, 0\n\ts_waitcnt vmcnt(12)\n\tv_lshlrev_b32 v24, 16, v48\n\tv_and_b32 v25, 0xffff0000, v48\n\tv_lshlrev_b32 v26, 16, v49\n\t" \
    "v_and_b32 v27, 0xffff0000, v49\n\tv_lshlrev_b32 v28, 16, v50\n\tv_and_b32 v29, 0xffff0000, v50\n\tv_lshlrev_b32 v30, 16, v51\n\tv_and_b32 v31, 0xffff0000, v51\n\tv_lshlrev_b32 v32, 16, v52\n\tv_and_b32 v33, 0xffff0000, v52\n\tv_lshlrev_b32 v34, 16, v53\n\t" \
    "v_and_b32 v35, 0xffff0000, v53\n\tv_lshlrev_b32 v36, 16, v54\n\tv_and_b32 v37, 0xffff0000, v54\n\tv_lshlrev_b32 v38, 16, v55\n\tv_and_b32 v39, 0xffff0000, v55\n\tv_lshlrev_b32 v40, 16, v56\n\tv_and_b32 v41, 0xffff0000, v56\n\tv_pk_add_f32 v[70:71], v[24:25], v[26:27]\n\t" \
    "v_pk_add_f32 v[72:73], v[28:29], v[30:31]\n\tv_pk_add_f32 v[74:75], v[32:33], v[34:35]\n\tv_pk_add_f32 v[76:77], v[36:37], v[38:39]\n\tv_pk_add_f32 v[70:71], v[70:71], v[72:73]\n\tv_pk_add_f32 v[74:75], v[74:75], v[76:77]\n\tv_pk_add_f32 v[70:71], v[70:71], v[40:41]\n\tv_pk_add_f32 v[70:71], v[70:71], v[74:75]\n\tv_add_f32 v66, v70, v71\n\t" \
    "s_nop 1\n\tv_add_f32_dpp v66, v66, v66 row_ror:8 row_mask:0xf bank_mask:0xf\n\ts_nop 1\n\tv_add_f32_dpp v66, v66, v66 row_ror:4 row_mask:0xf bank_mask:0xf\n\ts_nop 1\n\tv_add_f32_dpp v66, v66, v66 row_ror:2 row_mask:0xf bank_mask:0xf\n\ts_nop 1\n\tv_add_f32_dpp v66, v66, v66 row_ror:1 row_mask:0xf bank_mask:0xf\n\t" \
    "v_mov_b32 v67, v66\n\ts_nop 1\n\tv_permlane16_swap_b32 v66, v67\n\tv_add_f32 v66, v66, v67\n\tv_mul_f32 v66, 0x3ae38e39, v66\n\tv_pk_add_f32 v[70:71], v[24:25], v[66:67] op_sel_hi:[1,0] neg_lo:[0,1] neg_hi:[0,1]\n\tv_pk_add_f32 v[72:73], v[26:27], v[66:67] op_sel_hi:[1,0] neg_lo:[0,1] neg_hi:[0,1]\n\tv_pk_add_f32 v[74:75], v[28:29], v[66:67] op_sel_hi:[1,0] neg_lo:[0,1] neg_hi:[0,1]\n\t" \
    "v_pk_add_f32 v[76:77], v[30:31], v[66:67] op_sel_hi:[1,0] neg_lo:[0,1] neg_hi:[0,1]\n\tv_pk_add_f32 v[78:79], v[32:33], v[66:67] op_sel_hi:[1,0] neg_lo:[0,1] neg_hi:[0,1]\n\tv_pk_add_f32 v[80:81], v[34:35], v[66:67] op_sel_hi:[1,0] neg_lo:[0,1] neg_hi:[0,1]\n\tv_pk_add_f32 v[82:83], v[36:37], v[66:67] op_sel_hi:[1,0] neg_lo:[0,1] neg_hi:[0,1]\n\tv_pk_add_f32 v[84:85], v[38:39], v[66:67] op_sel_hi:[1,0] neg_lo:[0,1] neg_hi:[0,1]\n\tv_pk_add_f32 v[86:87], v[40:41], v[66:67] op_sel_hi:[1,0] neg_lo:[0,1] neg_hi:[0,1]\n\tv_pk_mul_f32 v[44:45], v[70:71], v[70:71]\n\tv_pk_mul_f32 v[46:47], v[72:73], v[72:73]\n\t" \
    "v_pk_fma_f32 v[44:45], v[74:75], v[74:75], v[44:45]\n\tv_pk_fma_f32 v[46:47], v[76:77], v[76:77], v[46:47]\n\tv_pk_fma_f32 v[44:45], v[78:79], v[78:79], v[44:45]\n\tv_pk_fma_f32 v[46:47], v[80:81], v[80:81], v[46:47]\n\tv_pk_fma_f32 v[44:45], v[82:83], v[82:83], v[44:45]\n\tv_pk_fma_f32 v[46:47], v[84:85], v[84:85], v[46:47]\n\tv_pk_fma_f32 v[44:45], v[86:87], v[86:87], v[44:45]\n\tv_pk_add_f32 v[44:45], v[44:45], v[46:47]\n\t" \
    "v_add_f32 v68, v44, v45\n\ts_nop 1\n\tv_add_f32_dpp v68, v68, v68 row_ror:8 row_mask:0xf bank_mask:0xf\n\ts_nop 1\n\tv_add_f32_dpp v68, v68, v68 row_ror:4 row_mask:0xf bank_mask:0xf\n\ts_nop 1\n\tv_add_f32_dpp v68, v68, v68 row_ror:2 row_mask:0xf bank_mask:0xf\n\ts_nop 1\n\t" \
    "v_add_f32_dpp v68, v68, v68 row_ror:1 row_mask:0xf bank_mask:0xf\n\tv_mov_b32 v69, v68\n\ts_nop 1\n\tv_permlane16_swap_b32 v68, v69\n\tv_add_f32 v68, v68, v69\n\tv_mul_f32 v68, 0x3ae38e39, v68\n\tv_cvt_pk_bf16_f32 v68, v68, v68\n\tv_lshlrev_b32 v68, 16, v68\n\t" \
    "v_add_f32 v68, 0x3727c5ac, v68\n\tv_cvt_pk_bf16_f32 v68, v68, v68\n\tv_lshlrev_b32 v68, 16, v68\n\tv_sqrt_f32 v68, v68\n\tv_cvt_pk_bf16_f32 v66, v66, v66\n\tv_lshlrev_b32 v66, 16, v66\n\tv_cvt_pk_bf16_f32 v68, v68, v68\n\tv_lshlrev_b32 v68, 16, v68\n\t" \
    "v_rcp_f32 v68, v68\n\ts_nop 0\n\tv_mul_f32 v69, v66, v68\n\tv_xor_b32 v69, 0x80000000, v69\n\tv_pk_fma_f32 v[24:25], v[24:25], v[68:69], v[68:69] op_sel:[0,0,1] op_sel_hi:[1,0,1]\n\tv_pk_fma_f32 v[26:27], v[26:27], v[68:69], v[68:69] op_sel:[0,0,1] op_sel_hi:[1,0,1]\n\tv_pk_fma_f32 v[28:29], v[28:29], v[68:69], v[68:69] op_sel:[0,0,1] op_sel_hi:[1,0,1]\n\tv_pk_fma_f32 v[30:31], v[30:31], v[68:69], v[68:69] op_sel:[0,0,1] op_sel_hi:[1,0,1]\n\t" \
    "v_pk_fma_f32 v[32:33], v[32:33], v[68:69], v[68:69] op_sel:[0,0,1] op_sel_hi:[1,0,1]\n\tv_pk_fma_f32 v[34:35], v[34:35], v[68:69], v[68:69] op_sel:[0,0,1] op_sel_hi:[1,0,1]\n\tv_pk_fma_f32 v[36:37], v[36:37], v[68:69], v[68:69] op_sel:[0,0,1] op_sel_hi:[1,0,1]\n\tv_pk_fma_f32 v[38:39], v[38:39], v[68:69], v[68:69] op_sel:[0,0,1] op_sel_hi:[1,0,1]\n\tv_pk_fma_f32 v[40:41], v[40:41], v[68:69], v[68:69] op_sel:[0,0,1] op_sel_hi:[1,0,1]\n\tv_cvt_pk_bf16_f32 v48, v24, v25\n\tv_cvt_pk_bf16_f32 v49, v26, v27\n\tv_cvt_pk_bf16_f32 v50, v28, v29\n\t" \
    "v_cvt_pk_bf16_f32 v51, v30, v31\n\tv_cvt_pk_bf16_f32 v52, v32, v33\n\tv_cvt_pk_bf16_f32 v53, v34, v35\n\tv_cvt_pk_bf16_f32 v54, v36, v37\n\tv_cvt_pk_bf16_f32 v55, v38, v39\n\tv_cvt_pk_bf16_f32 v56, v40, v41\n\tv_lshlrev_b32 v24, 16, v48\n\tv_and_b32 v25, 0xffff0000, v48\n\t" \
    "v_lshlrev_b32 v26, 16, v49\n\tv_and_b32 v27, 0xffff0000, v49\n\tv_lshlrev_b32 v28, 16, v50\n\tv_and_b32 v29, 0xffff0000, v50\n\tv_lshlrev_b32 v30, 16, v51\n\tv_and_b32 v31, 0xffff0000, v51\n\tv_lshlrev_b32 v32, 16, v52\n\tv_and_b32 v33, 0xffff0000, v52\n\t" \
    "v_lshlrev_b32 v34, 16, v53\n\tv_and_b32 v35, 0xffff0000, v53\n\tv_lshlrev_b32 v36, 16, v54\n\tv_and_b32 v37, 0xffff0000, v54\n\tv_lshlrev_b32 v38, 16, v55\n\tv_and_b32 v39, 0xffff0000, v55\n\tv_lshlrev_b32 v40, 16, v56\n\tv_and_b32 v41, 0xffff0000, v56\n\t" \
    "v_pk_add_f32 v[70:71], v[24:25], v[26:27]\n\tv_pk_add_f32 v[72:73], v[28:29], v[30:31]\n\tv_pk_add_f32 v[74:75], v[32:33], v[34:35]\n\tv_pk_add_f32 v[76:77], v[36:37], v[38:39]\n\tv_pk_mul_f32 v[44:45], v[24:25], v[24:25]\n\tv_pk_mul_f32 v[46:47], v[26:27], v[26:27]\n\tv_pk_add_f32 v[70:71], v[70:71], v[72:73]\n\tv_pk_add_f32 v[74:75], v[74:75], v[76:77]\n\t" \
    "v_pk_fma_f32 v[44:45], v[28:29], v[28:29], v[44:45]\n\tv_pk_fma_f32 v[46:47], v[30:31], v[30:31], v[46:47]\n\tv_pk_add_f32 v[70:71], v[70:71], v[40:41]\n\tv_pk_fma_f32 v[44:45], v[32:33], v[32:33], v[44:45]\n\tv_pk_fma_f32 v[46:47], v[34:35], v[34:35], v[46:47]\n\tv_pk_add_f32 v[70:71], v[70:71], v[74:75]\n\tv_pk_fma_f32 v[44:45], v[36:37], v[36:37], v[44:45]\n\tv_pk_fma_f32 v[46:47], v[38:39], v[38:39], v[46:47]\n\t" \
    "v_pk_fma_f32 v[44:45], v[40:41], v[40:41], v[44:45]\n\tv_pk_add_f32 v[44:45], v[44:45], v[46:47]\n\tv_add_f32 v66, v70, v71\n\tv_add_f32 v42, v44, v45\n\ts_nop 0\n\tv_add_f32_dpp v66, v66, v66 row_ror:8 row_mask:0xf bank_mask:0xf\n\tv_add_f32_dpp v42, v42, v42 row_ror:8 row_mask:0xf bank_mask:0xf\n\ts_nop 0\n\t" \
    "v_add_f32_dpp v66, v66, v66 row_ror:4 row_mask:0xf bank_mask:0xf\n\tv_add_f32_dpp v42, v42, v42 row_ror:4 row_mask:0xf bank_mask:0xf\n\ts_nop 0\n\tv_add_f32_dpp v66, v66, v66 row_ror:2 row_mask:0xf bank_mask:0xf\n\tv_add_f32_dpp v42, v42, v42 row_ror:2 row_mask:0xf bank_mask:0xf\n\ts_nop 0\n\tv_add_f32_dpp v66, v66, v66 row_ror:1 row_mask:0xf bank_mask:0xf\n\tv_add_f32_dpp v42, v42, v42 row_ror:1 row_mask:0xf bank_mask:0xf\n\t" \
    "v_mov_b32 v67, v66\n\tv_mov_b32 v43, v42\n\ts_nop 0\n\tv_permlane16_swap_b32 v66, v67\n\tv_permlane16_swap_b32 v42, v43\n\tv_add_f32 v66, v66, v67\n\tv_add_f32 v42, v42, v43\n\tv_mul_f32 v66, 0x3ae38e39, v66\n\t" \
    "v_mul_f32 v42, 0x3ae38e39, v42\n\tv_fma_f32 v42, -v66, v66, v42\n\tv_max_f32 v42, 0, v42\n\tv_cvt_pk_bf16_f32 v42, v42, v42\n\tv_lshlrev_b32 v42, 16, v42\n\tv_add_f32 v42, 0x3727c5ac, v42\n\tv_cvt_pk_bf16_f32 v42, v42, v42\n\tv_lshlrev_b32 v42, 16, v42\n\t" \
    "v_sqrt_f32 v42, v42\n\tv_cvt_pk_bf16_f32 v66, v66, v66\n\tv_lshlrev_b32 v66, 16, v66\n\tv_cvt_pk_bf16_f32 v42, v42, v42\n\tv_lshlrev_b32 v42, 16, v42\n\tv_rcp_f32 v42, v42\n\ts_nop 0\n\tv_mul_f32 v43, v66, v42\n\t" \
    "v_xor_b32 v43, 0x80000000, v43\n\tv_pk_fma_f32 v[24:25], v[24:25], v[42:43], v[42:43] op_sel:[0,0,1] op_sel_hi:[1,0,1]\n\tv_pk_fma_f32 v[26:27], v[26:27], v[42:43], v[42:43] op_sel:[0,0,1] op_sel_hi:[1,0,1]\n\tv_pk_fma_f32 v[28:29], v[28:29], v[42:43], v[42:43] op_sel:[0,0,1] op_sel_hi:[1,0,1]\n\tv_pk_fma_f32 v[30:31], v[30:31], v[42:43], v[42:43] op_sel:[0,0,1] op_sel_hi:[1,0,1]\n\tv_pk_fma_f32 v[32:33], v[32:33], v[42:43], v[42:43] op_sel:[0,0,1] op_sel_hi:[1,0,1]\n\tv_pk_fma_f32 v[34:35], v[34:35], v[42:43], v[42:43] op_sel:[0,0,1] op_sel_hi:[1,0,1]\n\tv_pk_fma_f32 v[36:37], v[36:37], v[42:43], v[42:43] op_sel:[0,0,1] op_sel_hi:[1,0,1]\n\t" \
    "v_pk_fma_f32 v[38:39], v[38:39], v[42:43], v[42:43] op_sel:[0,0,1] op_sel_hi:[1,0,1]\n\tv_pk_fma_f32 v[40:41], v[40:41], v[42:43], v[42:43] op_sel:[0,0,1] op_sel_hi:[1,0,1]\n\tv_cvt_pk_bf16_f32 v48, v24, v25\n\tv_cvt_pk_bf16_f32 v49, v26, v27\n\tv_cvt_pk_bf16_f32 v50, v28, v29\n\tv_cvt_pk_bf16_f32 v51, v30, v31\n\tv_cvt_pk_bf16_f32 v52, v32, v33\n\tv_cvt_pk_bf16_f32 v53, v34, v35\n\t" \
    "v_cvt_pk_bf16_f32 v54, v36, v37\n\tv_cvt_pk_bf16_f32 v55, v38, v39\n\tv_cvt_pk_bf16_f32 v56, v40, v41\n\tds_write_b128 v90, v[48:51]\n\tds_write_b128 v90, v[52:55] offset:512\n\tds_write_b32 v91, v56 offset:1024\n\ts_waitcnt vmcnt(11) lgkmcnt(0)\n\ts_barrier\n\t" \
    "ds_read_b64_tr_b16 v[24:25], v94 offset:0\n\tds_read_b64_tr_b16 v[26:27], v94 offset:4672\n\tds_read_b64_tr_b16 v[28:29], v94 offset:512\n\tds_read_b64_tr_b16 v[30:31], v94 offset:5184\n\tds_read_b64_tr_b16 v[32:33], v95 offset:1024\n\tds_read_b64_tr_b16 v[34:35], v95 offset:5696\n\tbuffer_store_dwordx2 v[68:69], v89, s[8:11], s64 offen sc1\n\tv_readfirstlane_b32 s36, v58\n\t" \
    "v_readfirstlane_b32 s37, v59\n\ts_lshl_b32 s73, s38, 12\n\ts_add_u32 s68, s61, s73\n\ts_add_u32 s36, s36, 0x7f4a7c15\n\ts_addc_u32 s37, s37, 0x9e3779b9\n\ts_waitcnt lgkmcnt(4)\n\tbuffer_store_dwordx4 v[24:27], v88, s[8:11], s68 offen sc1\n\ts_add_u32 s68, s68, 16384\n\t" \
    "s_waitcnt lgkmcnt(2)\n\tbuffer_store_dwordx4 v[28:31], v88, s[8:11], s68 offen sc1\n\ts_add_u32 s68, s61, 32768\n\ts_cmp_eq_u32 s38, 0\n\ts_cselect_b32 s68, s68, 0x7ff00000\n\ts_waitcnt lgkmcnt(0)\n\tbuffer_store_dwordx4 v[32:35], v88, s[8:11], s68 offen sc1\n\tv_mov_b64 v[248:249], s[36:37]\n\t" \
    "v_and_b32 v89, 7, v240\n\tv_lshlrev_b32 v89, 11, v89\n\ts_cmp_ge_u32 s43, 12\n\ts_cselect_b32 s69, s70, s43\n\ts_mul_i32 s69, s69, 294912\n\ts_add_u32 s69, s69, 128\n\ts_add_u32 m0, s72, 4096\n\ts_nop 0\n\t" \
    "buffer_load_dwordx4 v10, s[12:15], s69 offen lds\n\ts_add_u32 m0, s72, 5120\n\ts_nop 0\n\tbuffer_load_dwordx4 v11, s[12:15], s69 offen lds\n\ts_cmp_ge_u32 s43, 11\n\ts_cselect_b32 s69, s70, s43\n\ts_mul_i32 s69, s69, 294912\n\ts_add_u32 s69, s69, 294912\n\t" \
    "s_add_u32 m0, s72, 8192\n\ts_nop 0\n\tbuffer_load_dwordx4 v10, s[12:15], s69 offen lds\n\ts_add_u32 m0, s72, 9216\n\ts_nop 0\n\tbuffer_load_dwordx4 v11, s[12:15], s69 offen lds\n\ts_waitcnt vmcnt(10)\n\ts_barrier\n\t" \
    "ds_read_b64_tr_b16 v[36:37], v93 offset:0\n\tds_read_b64_tr_b16 v[38:39], v93 offset:4608\n\tds_read_b64_tr_b16 v[40:41], v93 offset:9216\n\tds_read_b64_tr_b16 v[42:43], v93 offset:13312\n\tds_read_b64_tr_b16 v[44:45], v93 offset:17920\n\tds_read_b64_tr_b16 v[46:47], v93 offset:512\n\tds_read_b64_tr_b16 v[48:49], v93 offset:5120\n\tds_read_b64_tr_b16 v[50:51], v93 offset:9728\n\t" \
    "ds_read_b64_tr_b16 v[52:53], v93 offset:13824\n\tds_read_b64_tr_b16 v[54:55], v93 offset:18432\n\tds_read_b64_tr_b16 v[120:121], v92 offset:1024\n\tds_read_b64_tr_b16 v[122:123], v92 offset:5632\n\tds_read_b64_tr_b16 v[124:125], v92 offset:10240\n\tds_read_b64_tr_b16 v[126:127], v92 offset:14336\n\tds_read_b64_tr_b16 v[128:129], v92 offset:18944\n\tv_lshlrev_b32 v78, 16, v60\n\t" \
    "s_cmp_ge_u32 s43, 11\n\ts_cselect_b32 s69, s70, s43\n\ts_mul_i32 s69, s69, 294912\n\ts_add_u32 s69, s69, 295040\n\ts_add_u32 m0, s72, 12288\n\tv_and_b32 v79, 0xffff0000, v60\n\tv_lshlrev_b32 v80, 16, v61\n\tv_and_b32 v81, 0xffff0000, v61\n\t" \
    "s_waitcnt lgkmcnt(10)\n\tv_lshlrev_b32 v66, 16, v36\n\tv_and_b32 v67, 0xffff0000, v36\n\tv_lshlrev_b32 v68, 16, v37\n\tbuffer_load_dwordx4 v10, s[12:15], s69 offen lds\n\ts_add_u32 m0, s72, 13312\n\tv_and_b32 v69, 0xffff0000, v37\n\tv_lshlrev_b32 v70, 16, v38\n\t" \
    "v_and_b32 v71, 0xffff0000, v38\n\tv_lshlrev_b32 v72, 16, v39\n\tv_and_b32 v73, 0xffff0000, v39\n\tv_lshlrev_b32 v74, 16, v40\n\tv_and_b32 v75, 0xffff0000, v40\n\tv_lshlrev_b32 v76, 16, v41\n\tv_mul_f32 v16, v66, v81\n\tv_mul_f32 v17, v67, v81\n\t" \
    "v_mul_f32 v18, v68, v81\n\tv_mul_f32 v19, v69, v81\n\tv_mul_f32 v20, v70, v81\n\tv_mul_f32 v21, v71, v81\n\tv_mul_f32 v22, v72, v81\n\tv_mul_f32 v23, v73, v81\n\tv_fmac_f32 v16, v67, v80\n\tv_fmac_f32 v17, v68, v80\n\t" \
    "v_fmac_f32 v18, v69, v80\n\tv_fmac_f32 v19, v70, v80\n\tv_fmac_f32 v20, v71, v80\n\tv_fmac_f32 v21, v72, v80\n\tv_fmac_f32 v22, v73, v80\n\tv_fmac_f32 v23, v74, v80\n\tv_fmac_f32 v16, v68, v79\n\tv_fmac_f32 v17, v69, v79\n\t" \
    "v_fmac_f32 v18, v70, v79\n\tv_fmac_f32 v19, v71, v79\n\tv_fmac_f32 v20, v72, v79\n\tbuffer_load_dwordx4 v11, s[12:15], s69 offen lds\n\ts_cmp_ge_u32 s43, 10\n\ts_cselect_b32 s69, s70, s43\n\ts_mul_i32 s69, s69, 294912\n\ts_add_u32 s69, s69, 589824\n\t" \
    "s_add_u32 m0, s72, 16384\n\tv_fmac_f32 v21, v73, v79\n\tv_fmac_f32 v22, v74, v79\n\tv_fmac_f32 v23, v75, v79\n\tv_fmac_f32 v16, v69, v78\n\tv_fmac_f32 v17, v70, v78\n\tv_fmac_f32 v18, v71, v78\n\tv_fmac_f32 v19, v72, v78\n\t" \
    "v_fmac_f32 v20, v73, v78\n\tv_fmac_f32 v21, v74, v78\n\tv_fmac_f32 v22, v75, v78\n\tv_fmac_f32 v23, v76, v78\n\tv_cvt_pk_bf16_f32 v66, v16, v17\n\tv_cvt_pk_bf16_f32 v67, v18, v19\n\tv_cvt_pk_bf16_f32 v68, v20, v21\n\tv_cvt_pk_bf16_f32 v69, v22, v23\n\t" \
    "v_lshlrev_b32 v16, 16, v66\n\tv_and_b32 v17, 0xffff0000, v66\n\tv_lshlrev_b32 v18, 16, v67\n\tv_and_b32 v19, 0xffff0000, v67\n\tv_lshlrev_b32 v20, 16, v68\n\tv_and_b32 v21, 0xffff0000, v68\n\tv_lshlrev_b32 v22, 16, v69\n\tv_and_b32 v23, 0xffff0000, v69\n\t" \
    "v_lshlrev_b32 v66, 16, v42\n\tv_and_b32 v67, 0xffff0000, v42\n\tbuffer_load_dwordx4 v10, s[12:15], s69 offen lds\n\ts_add_u32 m0, s72, 17408\n\tv_lshlrev_b32 v68, 16, v43\n\tv_and_b32 v69, 0xffff0000, v43\n\tv_lshlrev_b32 v70, 16, v44\n\tv_and_b32 v71, 0xffff0000, v44\n\t" \
    "v_lshlrev_b32 v72, 16, v45\n\tv_and_b32 v73, 0xffff0000, v45\n\tv_pk_mul_f32 v[16:17], v[16:17], v[66:67]\n\tv_pk_mul_f32 v[18:19], v[18:19], v[68:69]\n\tv_pk_mul_f32 v[20:21], v[20:21], v[70:71]\n\tv_pk_mul_f32 v[22:23], v[22:23], v[72:73]\n\tv_cvt_pk_bf16_f32 v24, v16, v17\n\tv_cvt_pk_bf16_f32 v25, v18, v19\n\t" \
    "v_cvt_pk_bf16_f32 v26, v20, v21\n\tv_cvt_pk_bf16_f32 v27, v22, v23\n\tv_lshlrev_b32 v78, 16, v62\n\tv_and_b32 v79, 0xffff0000, v62\n\tv_lshlrev_b32 v80, 16, v63\n\tv_and_b32 v81, 0xffff0000, v63\n\ts_waitcnt lgkmcnt(5)\n\tv_lshlrev_b32 v66, 16, v46\n\t" \
    "v_and_b32 v67, 0xffff0000, v46\n\tv_lshlrev_b32 v68, 16, v47\n\tv_and_b32 v69, 0xffff0000, v47\n\tv_lshlrev_b32 v70, 16, v48\n\tv_and_b32 v71, 0xffff0000, v48\n\tv_lshlrev_b32 v72, 16, v49\n\tv_and_b32 v73, 0xffff0000, v49\n\tv_lshlrev_b32 v74, 16, v50\n\t" \
    "v_and_b32 v75, 0xffff0000, v50\n\tbuffer_load_dwordx4 v11, s[12:15], s69 offen lds\n\ts_cmp_ge_u32 s43, 10\n\ts_cselect_b32 s69, s70, s43\n\ts_mul_i32 s69, s69, 294912\n\ts_add_u32 s69, s69, 589952\n\ts_add_u32 m0, s72, 20480\n\tv_lshlrev_b32 v76, 16, v51\n\t" \
    "v_mul_f32 v16, v66, v81\n\tv_mul_f32 v17, v67, v81\n\tv_mul_f32 v18, v68, v81\n\tv_mul_f32 v19, v69, v81\n\tv_mul_f32 v20, v70, v81\n\tv_mul_f32 v21, v71, v81\n\tv_mul_f32 v22, v72, v81\n\tv_mul_f32 v23, v73, v81\n\t" \
    "v_fmac_f32 v16, v67, v80\n\tv_fmac_f32 v17, v68, v80\n\tv_fmac_f32 v18, v69, v80\n\tv_fmac_f32 v19, v70, v80\n\tv_fmac_f32 v20, v71, v80\n\tv_fmac_f32 v21, v72, v80\n\tv_fmac_f32 v22, v73, v80\n\tv_fmac_f32 v23, v74, v80\n\t" \
    "v_fmac_f32 v16, v68, v79\n\tv_fmac_f32 v17, v69, v79\n\tv_fmac_f32 v18, v70, v79\n\tv_fmac_f32 v19, v71, v79\n\tv_fmac_f32 v20, v72, v79\n\tv_fmac_f32 v21, v73, v79\n\tv_fmac_f32 v22, v74, v79\n\tv_fmac_f32 v23, v75, v79\n\t" \
    "buffer_load_dwordx4 v10, s[12:15], s69 offen lds\n\ts_add_u32 m0, s72, 21504\n\tv_fmac_f32 v16, v69, v78\n\tv_fmac_f32 v17, v70, v78\n\tv_fmac_f32 v18, v71, v78\n\tv_fmac_f32 v19, v72, v78\n\tv_fmac_f32 v20, v73, v78\n\tv_fmac_f32 v21, v74, v78\n\t" \
    "v_fmac_f32 v22, v75, v78\n\tv_fmac_f32 v23, v76, v78\n\tv_cvt_pk_bf16_f32 v66, v16, v17\n\tv_cvt_pk_bf16_f32 v67, v18, v19\n\tv_cvt_pk_bf16_f32 v68, v20, v21\n\tv_cvt_pk_bf16_f32 v69, v22, v23\n\tv_lshlrev_b32 v16, 16, v66\n\tv_and_b32 v17, 0xffff0000, v66\n\t" \
    "v_lshlrev_b32 v18, 16, v67\n\tv_and_b32 v19, 0xffff0000, v67\n\tv_lshlrev_b32 v20, 16, v68\n\tv_and_b32 v21, 0xffff0000, v68\n\tv_lshlrev_b32 v22, 16, v69\n\tv_and_b32 v23, 0xffff0000, v69\n\tv_lshlrev_b32 v66, 16, v52\n\tv_and_b32 v67, 0xffff0000, v52\n\t" \
    "v_lshlrev_b32 v68, 16, v53\n\tv_and_b32 v69, 0xffff0000, v53\n\tv_lshlrev_b32 v70, 16, v54\n\tv_and_b32 v71, 0xffff0000, v54\n\tv_lshlrev_b32 v72, 16, v55\n\tv_and_b32 v73, 0xffff0000, v55\n\tv_pk_mul_f32 v[16:17], v[16:17], v[66:67]\n\tbuffer_load_dwordx4 v11, s[12:15], s69 offen lds\n\t" \
    "v_pk_mul_f32 v[18:19], v[18:19], v[68:69]\n\tv_pk_mul_f32 v[20:21], v[20:21], v[70:71]\n\tv_pk_mul_f32 v[22:23], v[22:23], v[72:73]\n\tv_cvt_pk_bf16_f32 v28, v16, v17\n\tv_cvt_pk_bf16_f32 v29, v18, v19\n\tv_cvt_pk_bf16_f32 v30, v20, v21\n\tv_cvt_pk_bf16_f32 v31, v22, v23\n\ts_waitcnt vmcnt(10)\n\t" \
    "s_barrier\n\tbuffer_store_dwordx2 v[248:249], v89, s[8:11], s63 offen sc1\n\ts_waitcnt lgkmcnt(0)\n\tv_lshlrev_b32 v78, 16, v64\n\ts_cmp_ge_u32 s43, 9\n\ts_cselect_b32 s69, s70, s43\n\ts_mul_i32 s69, s69, 294912\n\ts_add_u32 s69, s69, 884736\n\t" \
    "s_add_u32 m0, s72, 24576\n\tds_read_b64_tr_b16 a[48:49], v0 offset:0\n\tv_and_b32 v79, 0xffff0000, v64\n\tds_read_b64_tr_b16 a[50:51], v0 offset:512\n\tv_lshlrev_b32 v80, 16, v65\n\tds_read_b64_tr_b16 a[52:53], v1 offset:0\n\tv_and_b32 v81, 0xffff0000, v65\n\tds_read_b64_tr_b16 a[54:55], v1 offset:512\n\t" \
    "v_lshlrev_b32 v66, 16, v120\n\tds_read_b64_tr_b16 a[56:57], v2 offset:0\n\tv_and_b32 v67, 0xffff0000, v120\n\tds_read_b64_tr_b16 a[58:59], v2 offset:512\n\tv_lshlrev_b32 v68, 16, v121\n\tds_read_b64_tr_b16 a[60:61], v3 offset:0\n\tv_and_b32 v69, 0xffff0000, v121\n\tds_read_b64_tr_b16 a[62:63], v3 offset:512\n\t" \
    "v_lshlrev_b32 v70, 16, v122\n\tv_and_b32 v71, 0xffff0000, v122\n\tv_lshlrev_b32 v72, 16, v123\n\tv_and_b32 v73, 0xffff0000, v123\n\tv_lshlrev_b32 v74, 16, v124\n\tv_and_b32 v75, 0xffff0000, v124\n\tv_lshlrev_b32 v76, 16, v125\n\tv_mul_f32 v16, v66, v81\n\t" \
    "buffer_load_dwordx4 v10, s[12:15], s69 offen lds\n\ts_add_u32 m0, s72, 25600\n\tv_mul_f32 v17, v67, v81\n\tv_mul_f32 v18, v68, v81\n\tv_mul_f32 v19, v69, v81\n\tv_mul_f32 v20, v70, v81\n\tv_mul_f32 v21, v71, v81\n\tv_mul_f32 v22, v72, v81\n\t" \
    "v_mul_f32 v23, v73, v81\n\tv_fmac_f32 v16, v67, v80\n\tv_fmac_f32 v17, v68, v80\n\tv_fmac_f32 v18, v69, v80\n\tv_fmac_f32 v19, v70, v80\n\tv_fmac_f32 v20, v71, v80\n\tv_fmac_f32 v21, v72, v80\n\tv_fmac_f32 v22, v73, v80\n\t" \
    "v_fmac_f32 v23, v74, v80\n\tv_fmac_f32 v16, v68, v79\n\tv_fmac_f32 v17, v69, v79\n\tv_fmac_f32 v18, v70, v79\n\tv_fmac_f32 v19, v71, v79\n\tv_fmac_f32 v20, v72, v79\n\tv_fmac_f32 v21, v73, v79\n\tv_fmac_f32 v22, v74, v79\n\t" \
    "v_fmac_f32 v23, v75, v79\n\tv_fmac_f32 v16, v69, v78\n\tv_fmac_f32 v17, v70, v78\n\tv_fmac_f32 v18, v71, v78\n\tv_fmac_f32 v19, v72, v78\n\tv_fmac_f32 v20, v73, v78\n\tv_fmac_f32 v21, v74, v78\n\tbuffer_load_dwordx4 v11, s[12:15], s69 offen lds\n\t" \
    "s_xor_b32 s71, s57, 1\n\ts_lshl_b32 s71, s71, 6\n\ts_lshl_b32 s69, s42, 8\n\ts_add_u32 s71, s71, s69\n\tv_lshlrev_b32 v96, 3, v245\n\tv_fmac_f32 v22, v75, v78\n\tv_fmac_f32 v23, v76, v78\n\tv_cvt_pk_bf16_f32 v66, v16, v17\n\t" \
    "v_cvt_pk_bf16_f32 v67, v18, v19\n\tv_cvt_pk_bf16_f32 v68, v20, v21\n\tv_cvt_pk_bf16_f32 v69, v22, v23\n\tv_lshlrev_b32 v16, 16, v66\n\tv_and_b32 v17, 0xffff0000, v66\n\tv_lshlrev_b32 v18, 16, v67\n\tv_and_b32 v19, 0xffff0000, v67\n\tv_lshlrev_b32 v20, 16, v68\n\t" \
    "v_and_b32 v21, 0xffff0000, v68\n\tv_lshlrev_b32 v22, 16, v69\n\tv_and_b32 v23, 0xffff0000, v69\n\tv_lshlrev_b32 v66, 16, v126\n\tv_and_b32 v67, 0xffff0000, v126\n\tv_lshlrev_b32 v68, 16, v127\n\tv_and_b32 v69, 0xffff0000, v127\n\tv_lshlrev_b32 v70, 16, v128\n\t" \
    "v_and_b32 v71, 0xffff0000, v128\n\tv_lshlrev_b32 v72, 16, v129\n\tv_and_b32 v73, 0xffff0000, v129\n\tv_pk_mul_f32 v[16:17], v[16:17], v[66:67]\n\tv_pk_mul_f32 v[18:19], v[18:19], v[68:69]\n\tv_pk_mul_f32 v[20:21], v[20:21], v[70:71]\n\tbuffer_load_dwordx2 v[112:113], v96, s[28:31], s71 offen offset:0\n\tv_pk_mul_f32 v[22:23], v[22:23], v[72:73]\n\t" \
    "v_cvt_pk_bf16_f32 v32, v16, v17\n\tv_cvt_pk_bf16_f32 v33, v18, v19\n\tv_cvt_pk_bf16_f32 v34, v20, v21\n\tv_cvt_pk_bf16_f32 v35, v22, v23\n\tbuffer_load_dwordx2 v[114:115], v96, s[28:31], s71 offen offset:32\n\tbuffer_load_dwordx2 v[116:117], v96, s[28:31], s71 offen offset:128\n\tbuffer_load_dwordx2 v[118:119], v96, s[28:31], s71 offen offset:160\n\ts_cmp_ge_u32 s43, 9\n\t" \
    "s_cselect_b32 s69, s70, s43\n\ts_mul_i32 s69, s69, 294912\n\ts_add_u32 s69, s69, 884864\n\ts_add_u32 m0, s72, 28672\n\ts_nop 0\n\tbuffer_load_dwordx4 v10, s[12:15], s69 offen lds\n\ts_add_u32 m0, s72, 29696\n\ts_nop 0\n\t" \
    "buffer_load_dwordx4 v11, s[12:15], s69 offen lds\n\ts_cmp_ge_u32 s43, 8\n\ts_cselect_b32 s69, s70, s43\n\ts_mul_i32 s69, s69, 294912\n\ts_add_u32 s69, s69, 1179648\n\ts_add_u32 m0, s72, 32768\n\ts_nop 0\n\tbuffer_load_dwordx4 v10, s[12:15], s69 offen lds\n\t" \
    "s_add_u32 m0, s72, 33792\n\ts_nop 0\n\tbuffer_load_dwordx4 v11, s[12:15], s69 offen lds\n\ts_cmp_ge_u32 s43, 8\n\ts_cselect_b32 s69, s70, s43\n\ts_mul_i32 s69, s69, 294912\n\ts_add_u32 s69, s69, 1179776\n\ts_add_u32 m0, s72, 36864\n\t" \
    "s_nop 0\n\tbuffer_load_dwordx4 v10, s[12:15], s69 offen lds\n\ts_add_u32 m0, s72, 37888\n\ts_nop 0\n\tbuffer_load_dwordx4 v11, s[12:15], s69 offen lds\n\ts_cmp_ge_u32 s43, 7\n\ts_cselect_b32 s69, s70, s43\n\ts_mul_i32 s69, s69, 294912\n\t" \
    "s_add_u32 s69, s69, 1474560\n\ts_add_u32 m0, s72, 40960\n\ts_nop 0\n\tbuffer_load_dwordx4 v10, s[12:15], s69 offen lds\n\ts_add_u32 m0, s72, 41984\n\ts_nop 0\n\tbuffer_load_dwordx4 v11, s[12:15], s69 offen lds\n\ts_cmp_ge_u32 s43, 7\n\t" \
    "s_cselect_b32 s69, s70, s43\n\ts_mul_i32 s69, s69, 294912\n\ts_add_u32 s69, s69, 1474688\n\ts_add_u32 m0, s72, 45056\n\ts_nop 0\n\tbuffer_load_dwordx4 v10, s[12:15], s69 offen lds\n\ts_add_u32 m0, s72, 46080\n\ts_nop 0\n\t" \
    "buffer_load_dwordx4 v11, s[12:15], s69 offen lds\n\ts_cmp_ge_u32 s43, 6\n\ts_cselect_b32 s69, s70, s43\n\ts_mul_i32 s69, s69, 294912\n\ts_add_u32 s69, s69, 1769472\n\ts_add_u32 m0, s72, 49152\n\ts_nop 0\n\tbuffer_load_dwordx4 v10, s[12:15], s69 offen lds\n\t" \
    "s_add_u32 m0, s72, 50176\n\ts_nop 0\n\tbuffer_load_dwordx4 v11, s[12:15], s69 offen lds\n\ts_cmp_ge_u32 s43, 6\n\ts_cselect_b32 s69, s70, s43\n\ts_mul_i32 s69, s69, 294912\n\ts_add_u32 s69, s69, 1769600\n\ts_add_u32 m0, s72, 53248\n\t" \
    "s_nop 0\n\tbuffer_load_dwordx4 v10, s[12:15], s69 offen lds\n\ts_add_u32 m0, s72, 54272\n\ts_nop 0\n\tbuffer_load_dwordx4 v11, s[12:15], s69 offen lds\n\ts_cmp_ge_u32 s43, 5\n\ts_cselect_b32 s69, s70, s43\n\ts_mul_i32 s69, s69, 294912\n\t" \
    "s_add_u32 s69, s69, 2064384\n\ts_add_u32 m0, s72, 57344\n\ts_nop 0\n\tbuffer_load_dwordx4 v10, s[12:15], s69 offen lds\n\ts_add_u32 m0, s72, 58368\n\ts_nop 0\n\tbuffer_load_dwordx4 v11, s[12:15], s69 offen lds\n\ts_cmp_ge_u32 s43, 5\n\t" \
    "s_cselect_b32 s69, s70, s43\n\ts_mul_i32 s69, s69, 294912\n\ts_add_u32 s69, s69, 2064512\n\ts_add_u32 m0, s72, 61440\n\ts_nop 0\n\tbuffer_load_dwordx4 v10, s[12:15], s69 offen lds\n\ts_add_u32 m0, s72, 62464\n\ts_nop 0\n\t" \
    "buffer_load_dwordx4 v11, s[12:15], s69 offen lds\n\ts_lshl_b32 s73, s38, 10\n\ts_add_u32 s68, s62, s73\n\tbuffer_store_dwordx4 v[24:27], v8, s[8:11], s68 offen sc1\n\tbuffer_store_dwordx4 v[28:31], v9, s[8:11], s68 offen sc1\n\ts_add_u32 s68, s62, 8192\n\ts_cmp_eq_u32 s38, 1\n\ts_cselect_b32 s68, s68, 0x7ff00000\n\t" \
    "buffer_store_dwordx4 v[32:35], v8, s[8:11], s68 offen sc1\n\ts_cmp_eq_u32 s39, 0\n\ts_cbranch_scc1 101f\n\ts_waitcnt vmcnt(19) lgkmcnt(0)\n\ts_barrier\n\tds_read_b64_tr_b16 a[64:65], v0 offset:4096\n\tds_read_b64_tr_b16 a[66:67], v0 offset:4608\n\tds_read_b64_tr_b16 a[68:69], v1 offset:4096\n\t" \
    "ds_read_b64_tr_b16 a[70:71], v1 offset:4608\n\tds_read_b64_tr_b16 a[72:73], v2 offset:4096\n\tds_read_b64_tr_b16 a[74:75], v2 offset:4608\n\tds_read_b64_tr_b16 a[76:77], v3 offset:4096\n\tds_read_b64_tr_b16 a[78:79], v3 offset:4608\n\tds_read_b64_tr_b16 a[80:81], v0 offset:8192\n\tds_read_b64_tr_b16 a[82:83], v0 offset:8704\n\tds_read_b64_tr_b16 a[84:85], v1 offset:8192\n\t" \
    "ds_read_b64_tr_b16 a[86:87], v1 offset:8704\n\tds_read_b64_tr_b16 a[88:89], v2 offset:8192\n\tds_read_b64_tr_b16 a[90:91], v2 offset:8704\n\tds_read_b64_tr_b16 a[92:93], v3 offset:8192\n\tds_read_b64_tr_b16 a[94:95], v3 offset:8704\n\tds_read_b64_tr_b16 a[96:97], v0 offset:12288\n\tds_read_b64_tr_b16 a[98:99], v0 offset:12800\n\tds_read_b64_tr_b16 a[100:101], v1 offset:12288\n\t" \
    "ds_read_b64_tr_b16 a[102:103], v1 offset:12800\n\tds_read_b64_tr_b16 a[104:105], v2 offset:12288\n\tds_read_b64_tr_b16 a[106:107], v2 offset:12800\n\tds_read_b64_tr_b16 a[108:109], v3 offset:12288\n\tds_read_b64_tr_b16 a[110:111], v3 offset:12800\n\tds_read_b64_tr_b16 a[112:113], v0 offset:16384\n\tds_read_b64_tr_b16 a[114:115], v0 offset:16896\n\tds_read_b64_tr_b16 a[116:117], v1 offset:16384\n\t" \
    "ds_read_b64_tr_b16 a[118:119], v1 offset:16896\n\tds_read_b64_tr_b16 a[120:121], v2 offset:16384\n\tds_read_b64_tr_b16 a[122:123], v2 offset:16896\n\tds_read_b64_tr_b16 a[124:125], v3 offset:16384\n\tds_read_b64_tr_b16 a[126:127], v3 offset:16896\n\tds_read_b64_tr_b16 a[128:129], v0 offset:20480\n\tds_read_b64_tr_b16 a[130:131], v0 offset:20992\n\tds_read_b64_tr_b16 a[132:133], v1 offset:20480\n\t" \
    "ds_read_b64_tr_b16 a[134:135], v1 offset:20992\n\tds_read_b64_tr_b16 a[136:137], v2 offset:20480\n\tds_read_b64_tr_b16 a[138:139], v2 offset:20992\n\tds_read_b64_tr_b16 a[140:141], v3 offset:20480\n\tds_read_b64_tr_b16 a[142:143], v3 offset:20992\n\tds_read_b64_tr_b16 a[144:145], v0 offset:24576\n\tds_read_b64_tr_b16 a[146:147], v0 offset:25088\n\tds_read_b64_tr_b16 a[148:149], v1 offset:24576\n\t" \
    "ds_read_b64_tr_b16 a[150:151], v1 offset:25088\n\tds_read_b64_tr_b16 a[152:153], v2 offset:24576\n\tds_read_b64_tr_b16 a[154:155], v2 offset:25088\n\tds_read_b64_tr_b16 a[156:157], v3 offset:24576\n\tds_read_b64_tr_b16 a[158:159], v3 offset:25088\n\tds_read_b64_tr_b16 a[160:161], v0 offset:28672\n\tds_read_b64_tr_b16 a[162:163], v0 offset:29184\n\tds_read_b64_tr_b16 a[164:165], v1 offset:28672\n\t" \
    "ds_read_b64_tr_b16 a[166:167], v1 offset:29184\n\tds_read_b64_tr_b16 a[168:169], v2 offset:28672\n\tds_read_b64_tr_b16 a[170:171], v2 offset:29184\n\tds_read_b64_tr_b16 a[172:173], v3 offset:28672\n\tds_read_b64_tr_b16 a[174:175], v3 offset:29184\n\ts_cmp_ge_u32 s43, 4\n\ts_cselect_b32 s69, s70, s43\n\ts_mul_i32 s69, s69, 294912\n\t" \
    "s_add_u32 s69, s69, 2359296\n\ts_add_u32 m0, s72, 0\n\ts_nop 0\n\tbuffer_load_dwordx4 v10, s[12:15], s69 offen lds\n\ts_add_u32 m0, s72, 1024\n\ts_nop 0\n\tbuffer_load_dwordx4 v11, s[12:15], s69 offen lds\n\ts_waitcnt lgkmcnt(0)\n\t" \
    "s_barrier\n\tbuffer_load_dwordx4 v[250:253], v8, s[8:11], s47 offen sc1\n\ts_cmp_ge_u32 s43, 4\n\ts_cselect_b32 s69, s70, s43\n\ts_mul_i32 s69, s69, 294912\n\ts_add_u32 s69, s69, 2359424\n\ts_add_u32 m0, s72, 4096\n\ts_nop 0\n\t" \
    "buffer_load_dwordx4 v10, s[12:15], s69 offen lds\n\ts_add_u32 m0, s72, 5120\n\ts_nop 0\n\tbuffer_load_dwordx4 v11, s[12:15], s69 offen lds\n\ts_cmp_ge_u32 s43, 3\n\ts_cselect_b32 s69, s70, s43\n\ts_mul_i32 s69, s69, 294912\n\ts_add_u32 s69, s69, 2654208\n\t" \
    "s_add_u32 m0, s72, 8192\n\ts_nop 0\n\tbuffer_load_dwordx4 v10, s[12:15], s69 offen lds\n\ts_add_u32 m0, s72, 9216\n\ts_nop 0\n\tbuffer_load_dwordx4 v11, s[12:15], s69 offen lds\n\ts_cmp_ge_u32 s43, 3\n\ts_cselect_b32 s69, s70, s43\n\t" \
    "s_mul_i32 s69, s69, 294912\n\ts_add_u32 s69, s69, 2654336\n\ts_add_u32 m0, s72, 12288\n\ts_nop 0\n\tbuffer_load_dwordx4 v10, s[12:15], s69 offen lds\n\ts_add_u32 m0, s72, 13312\n\ts_nop 0\n\tbuffer_load_dwordx4 v11, s[12:15], s69 offen lds\n\t" \
    "s_cmp_ge_u32 s43, 2\n\ts_cselect_b32 s69, s70, s43\n\ts_mul_i32 s69, s69, 294912\n\ts_add_u32 s69, s69, 2949120\n\ts_add_u32 m0, s72, 16384\n\ts_nop 0\n\tbuffer_load_dwordx4 v10, s[12:15], s69 offen lds\n\ts_add_u32 m0, s72, 17408\n\t" \
    "s_nop 0\n\tbuffer_load_dwordx4 v11, s[12:15], s69 offen lds\n\ts_cmp_ge_u32 s43, 2\n\ts_cselect_b32 s69, s70, s43\n\ts_mul_i32 s69, s69, 294912\n\ts_add_u32 s69, s69, 2949248\n\ts_add_u32 m0, s72, 20480\n\ts_nop 0\n\t" \
    "buffer_load_dwordx4 v10, s[12:15], s69 offen lds\n\ts_add_u32 m0, s72, 21504\n\ts_nop 0\n\tbuffer_load_dwordx4 v11, s[12:15], s69 offen lds\n\ts_cmp_ge_u32 s43, 1\n\ts_cselect_b32 s69, s70, s43\n\ts_mul_i32 s69, s69, 294912\n\ts_add_u32 s69, s69, 3244032\n\t" \
    "s_add_u32 m0, s72, 24576\n\ts_nop 0\n\tbuffer_load_dwordx4 v10, s[12:15], s69 offen lds\n\ts_add_u32 m0, s72, 25600\n\ts_nop 0\n\tbuffer_load_dwordx4 v11, s[12:15], s69 offen lds\n\ts_cmp_ge_u32 s43, 1\n\ts_cselect_b32 s69, s70, s43\n\t" \
    "s_mul_i32 s69, s69, 294912\n\ts_add_u32 s69, s69, 3244160\n\ts_add_u32 m0, s72, 28672\n\ts_nop 0\n\tbuffer_load_dwordx4 v10, s[12:15], s69 offen lds\n\ts_add_u32 m0, s72, 29696\n\ts_nop 0\n\tbuffer_load_dwordx4 v11, s[12:15], s69 offen lds\n\t" \
    "s_waitcnt vmcnt(14)\n\tv_cmp_ne_u64 vcc, s[36:37], v[250:251]\n\tv_cmp_ne_u64 s[74:75], s[36:37], v[252:253]\n\ts_or_b64 s[74:75], s[74:75], vcc\n\ts_cbranch_scc0 105f\n\ts_mov_b32 s71, 1048576\n\t104:\n\ts_sleep 1\n\t" \
    "buffer_load_dwordx4 v[250:253], v8, s[8:11], s47 offen sc1\n\ts_waitcnt vmcnt(0)\n\tv_cmp_ne_u64 vcc, s[36:37], v[250:251]\n\tv_cmp_ne_u64 s[74:75], s[36:37], v[252:253]\n\ts_or_b64 s[74:75], s[74:75], vcc\n\ts_cbranch_scc0 105f\n\ts_sub_u32 s71, s71, 1\n\ts_cbranch_scc0 104b\n\t" \
    "105:\n\ts_cmp_ge_u32 s43, 12\n\ts_cselect_b32 s68, s70, s43\n\ts_mul_i32 s68, s68, 36864\n\ts_add_u32 s68, s68, s44\n\ts_add_u32 s68, s68, 0\n\tbuffer_load_dwordx4 v[120:123], v8, s[8:11], s68 offen offset:0 sc1\n\tbuffer_load_dwordx4 v[124:127], v8, s[8:11], s68 offen offset:1024 sc1\n\t" \
    "buffer_load_dwordx4 v[128:131], v8, s[8:11], s68 offen offset:2048 sc1\n\ts_cmp_ge_u32 s43, 11\n\ts_cselect_b32 s68, s70, s43\n\ts_mul_i32 s68, s68, 36864\n\ts_add_u32 s68, s68, s44\n\ts_add_u32 s68, s68, 36864\n\tbuffer_load_dwordx4 v[132:135], v8, s[8:11], s68 offen offset:0 sc1\n\tbuffer_load_dwordx4 v[136:139], v8, s[8:11], s68 offen offset:1024 sc1\n\t" \
    "buffer_load_dwordx4 v[140:143], v8, s[8:11], s68 offen offset:2048 sc1\n\ts_cmp_ge_u32 s43, 10\n\ts_cselect_b32 s68, s70, s43\n\ts_mul_i32 s68, s68, 36864\n\ts_add_u32 s68, s68, s44\n\ts_add_u32 s68, s68, 73728\n\tbuffer_load_dwordx4 v[144:147], v8, s[8:11], s68 offen offset:0 sc1\n\tbuffer_load_dwordx4 v[148:151], v8, s[8:11], s68 offen offset:1024 sc1\n\t" \
    "buffer_load_dwordx4 v[152:155], v8, s[8:11], s68 offen offset:2048 sc1\n\ts_cmp_ge_u32 s43, 9\n\ts_cselect_b32 s68, s70, s43\n\ts_mul_i32 s68, s68, 36864\n\ts_add_u32 s68, s68, s44\n\ts_add_u32 s68, s68, 110592\n\tbuffer_load_dwordx4 v[156:159], v8, s[8:11], s68 offen offset:0 sc1\n\tbuffer_load_dwordx4 v[160:163], v8, s[8:11], s68 offen offset:1024 sc1\n\t" \
    "buffer_load_dwordx4 v[164:167], v8, s[8:11], s68 offen offset:2048 sc1\n\tv_lshlrev_b32 v96, 16, v112\n\tv_and_b32 v97, 0xffff0000, v112\n\tv_lshlrev_b32 v98, 16, v113\n\tv_and_b32 v99, 0xffff0000, v113\n\tv_lshlrev_b32 v100, 16, v114\n\tv_and_b32 v101, 0xffff0000, v114\n\tv_lshlrev_b32 v102, 16, v115\n\t" \
    "v_and_b32 v103, 0xffff0000, v115\n\tv_lshlrev_b32 v104, 16, v116\n\tv_and_b32 v105, 0xffff0000, v116\n\tv_lshlrev_b32 v106, 16, v117\n\tv_and_b32 v107, 0xffff0000, v117\n\tv_lshlrev_b32 v108, 16, v118\n\tv_and_b32 v109, 0xffff0000, v118\n\tv_lshlrev_b32 v110, 16, v119\n\t" \
    "v_and_b32 v111, 0xffff0000, v119\n\tbuffer_store_dwordx2 v[248:249], off, s[8:11], s48 sc1\n\ts_barrier\n\ts_waitcnt vmcnt(10)\n\tv_mfma_f32_16x16x32_bf16 a[0:3], a[48:51], v[120:123], 0\n\ts_cmp_ge_u32 s43, 8\n\ts_cselect_b32 s68, s70, s43\n\ts_mul_i32 s68, s68, 36864\n\t" \
    "s_add_u32 s68, s68, s44\n\tv_mfma_f32_16x16x32_bf16 a[8:11], a[48:51], v[124:127], 0\n\ts_add_u32 s68, s68, 147456\n\tv_mfma_f32_16x16x32_bf16 a[16:19], a[48:51], v[128:131], 0\n\tv_mfma_f32_16x16x32_bf16 a[4:7], a[52:55], v[120:123], 0\n\tv_mfma_f32_16x16x32_bf16 a[12:15], a[52:55], v[124:127], 0\n\tbuffer_load_dwordx4 v[168:171], v8, s[8:11], s68 offen offset:0 sc1\n\tv_mfma_f32_16x16x32_bf16 a[20:23], a[52:55], v[128:131], 0\n\t" \
    "v_mfma_f32_16x16x32_bf16 v[48:51], a[56:59], v[120:123], v[96:99]\n\tv_mfma_f32_16x16x32_bf16 v[56:59], a[56:59], v[124:127], v[96:99]\n\tbuffer_load_dwordx4 v[172:175], v8, s[8:11], s68 offen offset:1024 sc1\n\tv_mfma_f32_16x16x32_bf16 v[64:67], a[56:59], v[128:131], v[96:99]\n\tv_mfma_f32_16x16x32_bf16 v[52:55], a[60:63], v[120:123], v[100:103]\n\tv_mfma_f32_16x16x32_bf16 v[60:63], a[60:63], v[124:127], v[100:103]\n\tbuffer_load_dwordx4 v[176:179], v8, s[8:11], s68 offen offset:2048 sc1\n\ts_cmp_ge_u32 s43, 7\n\t" \
    "s_cselect_b32 s68, s70, s43\n\ts_mul_i32 s68, s68, 36864\n\tv_mfma_f32_16x16x32_bf16 v[68:71], a[60:63], v[128:131], v[100:103]\n\ts_add_u32 s68, s68, s44\n\ts_add_u32 s68, s68, 184320\n\tv_mfma_f32_16x16x32_bf16 a[24:27], a[64:67], v[120:123], 0\n\tv_mfma_f32_16x16x32_bf16 a[32:35], a[64:67], v[124:127], 0\n\tbuffer_load_dwordx4 v[180:183], v8, s[8:11], s68 offen offset:0 sc1\n\t" \
    "v_mfma_f32_16x16x32_bf16 a[40:43], a[64:67], v[128:131], 0\n\tv_mfma_f32_16x16x32_bf16 a[28:31], a[68:71], v[120:123], 0\n\tv_mfma_f32_16x16x32_bf16 a[36:39], a[68:71], v[124:127], 0\n\tbuffer_load_dwordx4 v[184:187], v8, s[8:11], s68 offen offset:1024 sc1\n\tv_mfma_f32_16x16x32_bf16 a[44:47], a[68:71], v[128:131], 0\n\tv_mfma_f32_16x16x32_bf16 v[72:75], a[72:75], v[120:123], v[104:107]\n\tv_mfma_f32_16x16x32_bf16 v[80:83], a[72:75], v[124:127], v[104:107]\n\tbuffer_load_dwordx4 v[188:191], v8, s[8:11], s68 offen offset:2048 sc1\n\t" \
    "s_cmp_ge_u32 s43, 6\n\ts_cselect_b32 s68, s70, s43\n\ts_mul_i32 s68, s68, 36864\n\tv_mfma_f32_16x16x32_bf16 v[88:91], a[72:75], v[128:131], v[104:107]\n\ts_add_u32 s68, s68, s44\n\ts_add_u32 s68, s68, 221184\n\tv_mfma_f32_16x16x32_bf16 v[76:79], a[76:79], v[120:123], v[108:111]\n\tv_mfma_f32_16x16x32_bf16 v[84:87], a[76:79], v[124:127], v[108:111]\n\t" \
    "buffer_load_dwordx4 v[192:195], v8, s[8:11], s68 offen offset:0 sc1\n\tv_mfma_f32_16x16x32_bf16 v[92:95], a[76:79], v[128:131], v[108:111]\n\ts_barrier\n\ts_waitcnt vmcnt(14)\n\tv_mfma_f32_16x16x32_bf16 a[0:3], a[80:83], v[132:135], a[0:3]\n\tv_mfma_f32_16x16x32_bf16 a[8:11], a[80:83], v[136:139], a[8:11]\n\tbuffer_load_dwordx4 v[196:199], v8, s[8:11], s68 offen offset:1024 sc1\n\tv_mfma_f32_16x16x32_bf16 a[16:19], a[80:83], v[140:143], a[16:19]\n\t" \
    "v_mfma_f32_16x16x32_bf16 a[4:7], a[84:87], v[132:135], a[4:7]\n\tv_mfma_f32_16x16x32_bf16 a[12:15], a[84:87], v[136:139], a[12:15]\n\tbuffer_load_dwordx4 v[200:203], v8, s[8:11], s68 offen offset:2048 sc1\n\ts_cmp_ge_u32 s43, 5\n\ts_cselect_b32 s68, s70, s43\n\ts_mul_i32 s68, s68, 36864\n\tv_mfma_f32_16x16x32_bf16 a[20:23], a[84:87], v[140:143], a[20:23]\n\ts_add_u32 s68, s68, s44\n\t" \
    "s_add_u32 s68, s68, 258048\n\tv_mfma_f32_16x16x32_bf16 v[48:51], a[88:91], v[132:135], v[48:51]\n\tv_mfma_f32_16x16x32_bf16 v[56:59], a[88:91], v[136:139], v[56:59]\n\tv_mfma_f32_16x16x32_bf16 v[64:67], a[88:91], v[140:143], v[64:67]\n\tv_mfma_f32_16x16x32_bf16 v[52:55], a[92:95], v[132:135], v[52:55]\n\tv_mfma_f32_16x16x32_bf16 v[60:63], a[92:95], v[136:139], v[60:63]\n\tbuffer_load_dwordx4 v[120:123], v8, s[8:11], s68 offen offset:0 sc1\n\tv_mfma_f32_16x16x32_bf16 v[68:71], a[92:95], v[140:143], v[68:71]\n\t" \
    "v_mfma_f32_16x16x32_bf16 a[24:27], a[96:99], v[132:135], a[24:27]\n\tv_mfma_f32_16x16x32_bf16 a[32:35], a[96:99], v[136:139], a[32:35]\n\tbuffer_load_dwordx4 v[124:127], v8, s[8:11], s68 offen offset:1024 sc1\n\tv_mfma_f32_16x16x32_bf16 a[40:43], a[96:99], v[140:143], a[40:43]\n\tv_mfma_f32_16x16x32_bf16 a[28:31], a[100:103], v[132:135], a[28:31]\n\tv_mfma_f32_16x16x32_bf16 a[36:39], a[100:103], v[136:139], a[36:39]\n\tbuffer_load_dwordx4 v[128:131], v8, s[8:11], s68 offen offset:2048 sc1\n\ts_cmp_ge_u32 s43, 4\n\t" \
    "s_cselect_b32 s68, s70, s43\n\ts_mul_i32 s68, s68, 36864\n\tv_mfma_f32_16x16x32_bf16 a[44:47], a[100:103], v[140:143], a[44:47]\n\ts_add_u32 s68, s68, s44\n\ts_add_u32 s68, s68, 294912\n\tv_mfma_f32_16x16x32_bf16 v[72:75], a[104:107], v[132:135], v[72:75]\n\tv_mfma_f32_16x16x32_bf16 v[80:83], a[104:107], v[136:139], v[80:83]\n\tv_mfma_f32_16x16x32_bf16 v[88:91], a[104:107], v[140:143], v[88:91]\n\t" \
    "v_mfma_f32_16x16x32_bf16 v[76:79], a[108:111], v[132:135], v[76:79]\n\tv_mfma_f32_16x16x32_bf16 v[84:87], a[108:111], v[136:139], v[84:87]\n\tbuffer_load_dwordx4 v[204:207], v8, s[8:11], s68 offen offset:0 sc1\n\tv_mfma_f32_16x16x32_bf16 v[92:95], a[108:111], v[140:143], v[92:95]\n\ts_barrier\n\ts_waitcnt vmcnt(17)\n\tv_mfma_f32_16x16x32_bf16 a[0:3], a[112:115], v[144:147], a[0:3]\n\tv_mfma_f32_16x16x32_bf16 a[8:11], a[112:115], v[148:151], a[8:11]\n\t" \
    "buffer_load_dwordx4 v[208:211], v8, s[8:11], s68 offen offset:1024 sc1\n\tv_mfma_f32_16x16x32_bf16 a[16:19], a[112:115], v[152:155], a[16:19]\n\tv_mfma_f32_16x16x32_bf16 a[4:7], a[116:119], v[144:147], a[4:7]\n\tv_mfma_f32_16x16x32_bf16 a[12:15], a[116:119], v[148:151], a[12:15]\n\tbuffer_load_dwordx4 v[212:215], v8, s[8:11], s68 offen offset:2048 sc1\n\ts_cmp_ge_u32 s43, 3\n\ts_cselect_b32 s68, s70, s43\n\ts_mul_i32 s68, s68, 36864\n\t" \
    "v_mfma_f32_16x16x32_bf16 a[20:23], a[116:119], v[152:155], a[20:23]\n\ts_add_u32 s68, s68, s44\n\ts_add_u32 s68, s68, 331776\n\tv_mfma_f32_16x16x32_bf16 v[48:51], a[120:123], v[144:147], v[48:51]\n\tv_mfma_f32_16x16x32_bf16 v[56:59], a[120:123], v[148:151], v[56:59]\n\tbuffer_load_dwordx4 v[132:135], v8, s[8:11], s68 offen offset:0 sc1\n\tv_mfma_f32_16x16x32_bf16 v[64:67], a[120:123], v[152:155], v[64:67]\n\tv_mfma_f32_16x16x32_bf16 v[52:55], a[124:127], v[144:147], v[52:55]\n\t" \
    "v_mfma_f32_16x16x32_bf16 v[60:63], a[124:127], v[148:151], v[60:63]\n\tv_mfma_f32_16x16x32_bf16 v[68:71], a[124:127], v[152:155], v[68:71]\n\tv_mfma_f32_16x16x32_bf16 a[24:27], a[128:131], v[144:147], a[24:27]\n\tv_mfma_f32_16x16x32_bf16 a[32:35], a[128:131], v[148:151], a[32:35]\n\tbuffer_load_dwordx4 v[136:139], v8, s[8:11], s68 offen offset:1024 sc1\n\tv_mfma_f32_16x16x32_bf16 a[40:43], a[128:131], v[152:155], a[40:43]\n\tv_mfma_f32_16x16x32_bf16 a[28:31], a[132:135], v[144:147], a[28:31]\n\tv_mfma_f32_16x16x32_bf16 a[36:39], a[132:135], v[148:151], a[36:39]\n\t" \
    "buffer_load_dwordx4 v[140:143], v8, s[8:11], s68 offen offset:2048 sc1\n\ts_cmp_ge_u32 s43, 2\n\ts_cselect_b32 s68, s70, s43\n\ts_mul_i32 s68, s68, 36864\n\tv_mfma_f32_16x16x32_bf16 a[44:47], a[132:135], v[152:155], a[44:47]\n\ts_add_u32 s68, s68, s44\n\ts_add_u32 s68, s68, 368640\n\tv_mfma_f32_16x16x32_bf16 v[72:75], a[136:139], v[144:147], v[72:75]\n\t" \
    "v_mfma_f32_16x16x32_bf16 v[80:83], a[136:139], v[148:151], v[80:83]\n\tbuffer_load_dwordx4 v[216:219], v8, s[8:11], s68 offen offset:0 sc1\n\tv_mfma_f32_16x16x32_bf16 v[88:91], a[136:139], v[152:155], v[88:91]\n\tv_mfma_f32_16x16x32_bf16 v[76:79], a[140:143], v[144:147], v[76:79]\n\tv_mfma_f32_16x16x32_bf16 v[84:87], a[140:143], v[148:151], v[84:87]\n\tv_mfma_f32_16x16x32_bf16 v[92:95], a[140:143], v[152:155], v[92:95]\n\ts_barrier\n\ts_waitcnt vmcnt(20)\n\t" \
    "v_mfma_f32_16x16x32_bf16 a[0:3], a[144:147], v[156:159], a[0:3]\n\tv_mfma_f32_16x16x32_bf16 a[8:11], a[144:147], v[160:163], a[8:11]\n\tbuffer_load_dwordx4 a[48:51], v8, s[8:11], s68 offen offset:1024 sc1\n\tv_mfma_f32_16x16x32_bf16 a[16:19], a[144:147], v[164:167], a[16:19]\n\tv_mfma_f32_16x16x32_bf16 a[4:7], a[148:151], v[156:159], a[4:7]\n\tv_mfma_f32_16x16x32_bf16 a[12:15], a[148:151], v[160:163], a[12:15]\n\tbuffer_load_dwordx4 a[52:55], v8, s[8:11], s68 offen offset:2048 sc1\n\ts_cmp_ge_u32 s43, 1\n\t" \
    "s_cselect_b32 s68, s70, s43\n\ts_mul_i32 s68, s68, 36864\n\tv_mfma_f32_16x16x32_bf16 a[20:23], a[148:151], v[164:167], a[20:23]\n\ts_add_u32 s68, s68, s44\n\ts_add_u32 s68, s68, 405504\n\tv_mfma_f32_16x16x32_bf16 v[48:51], a[152:155], v[156:159], v[48:51]\n\tv_mfma_f32_16x16x32_bf16 v[56:59], a[152:155], v[160:163], v[56:59]\n\tbuffer_load_dwordx4 v[144:147], v8, s[8:11], s68 offen offset:0 sc1\n\t" \
    "v_mfma_f32_16x16x32_bf16 v[64:67], a[152:155], v[164:167], v[64:67]\n\tv_mfma_f32_16x16x32_bf16 v[52:55], a[156:159], v[156:159], v[52:55]\n\tds_read_b64_tr_b16 v[16:17], v0 offset:32768\n\tds_read_b64_tr_b16 v[18:19], v0 offset:33280\n\tv_mfma_f32_16x16x32_bf16 v[60:63], a[156:159], v[160:163], v[60:63]\n\tbuffer_load_dwordx4 v[148:151], v8, s[8:11], s68 offen offset:1024 sc1\n\tv_mfma_f32_16x16x32_bf16 v[68:71], a[156:159], v[164:167], v[68:71]\n\tv_mfma_f32_16x16x32_bf16 a[24:27], a[160:163], v[156:159], a[24:27]\n\t" \
    "ds_read_b64_tr_b16 v[20:21], v1 offset:32768\n\tds_read_b64_tr_b16 v[22:23], v1 offset:33280\n\tv_mfma_f32_16x16x32_bf16 a[32:35], a[160:163], v[160:163], a[32:35]\n\tv_mfma_f32_16x16x32_bf16 a[40:43], a[160:163], v[164:167], a[40:43]\n\tv_mfma_f32_16x16x32_bf16 a[28:31], a[164:167], v[156:159], a[28:31]\n\tds_read_b64_tr_b16 v[24:25], v2 offset:32768\n\tds_read_b64_tr_b16 v[26:27], v2 offset:33280\n\tv_mfma_f32_16x16x32_bf16 a[36:39], a[164:167], v[160:163], a[36:39]\n\t" \
    "buffer_load_dwordx4 v[152:155], v8, s[8:11], s68 offen offset:2048 sc1\n\ts_add_u32 s68, s44, 442368\n\tv_mfma_f32_16x16x32_bf16 a[44:47], a[164:167], v[164:167], a[44:47]\n\tv_mfma_f32_16x16x32_bf16 v[72:75], a[168:171], v[156:159], v[72:75]\n\tds_read_b64_tr_b16 v[28:29], v3 offset:32768\n\tds_read_b64_tr_b16 v[30:31], v3 offset:33280\n\tv_mfma_f32_16x16x32_bf16 v[80:83], a[168:171], v[160:163], v[80:83]\n\tbuffer_load_dwordx4 a[56:59], v8, s[8:11], s68 offen offset:0 sc1\n\t" \
    "v_mfma_f32_16x16x32_bf16 v[88:91], a[168:171], v[164:167], v[88:91]\n\tv_mfma_f32_16x16x32_bf16 v[76:79], a[172:175], v[156:159], v[76:79]\n\tds_read_b64_tr_b16 v[32:33], v0 offset:36864\n\tds_read_b64_tr_b16 v[34:35], v0 offset:37376\n\tv_mfma_f32_16x16x32_bf16 v[84:87], a[172:175], v[160:163], v[84:87]\n\tbuffer_load_dwordx4 a[60:63], v8, s[8:11], s68 offen offset:1024 sc1\n\tv_mfma_f32_16x16x32_bf16 v[92:95], a[172:175], v[164:167], v[92:95]\n\ts_barrier\n\t" \
    "s_waitcnt vmcnt(23) lgkmcnt(8)\n\tv_mfma_f32_16x16x32_bf16 a[0:3], v[16:19], v[168:171], a[0:3]\n\tds_read_b64_tr_b16 v[36:37], v1 offset:36864\n\tds_read_b64_tr_b16 v[38:39], v1 offset:37376\n\tv_mfma_f32_16x16x32_bf16 a[8:11], v[16:19], v[172:175], a[8:11]\n\tbuffer_load_dwordx4 a[64:67], v8, s[8:11], s68 offen offset:2048 sc1\n\ts_add_u32 s68, s44, 479232\n\tv_mfma_f32_16x16x32_bf16 a[16:19], v[16:19], v[176:179], a[16:19]\n\t" \
    "s_waitcnt lgkmcnt(8)\n\tv_mfma_f32_16x16x32_bf16 a[4:7], v[20:23], v[168:171], a[4:7]\n\tds_read_b64_tr_b16 v[40:41], v2 offset:36864\n\tds_read_b64_tr_b16 v[42:43], v2 offset:37376\n\tv_mfma_f32_16x16x32_bf16 a[12:15], v[20:23], v[172:175], a[12:15]\n\tbuffer_load_dwordx4 v[156:159], v8, s[8:11], s68 offen offset:0 sc1\n\tv_mfma_f32_16x16x32_bf16 a[20:23], v[20:23], v[176:179], a[20:23]\n\ts_waitcnt lgkmcnt(8)\n\t" \
    "v_mfma_f32_16x16x32_bf16 v[48:51], v[24:27], v[168:171], v[48:51]\n\tds_read_b64_tr_b16 v[44:45], v3 offset:36864\n\tds_read_b64_tr_b16 v[46:47], v3 offset:37376\n\tv_mfma_f32_16x16x32_bf16 v[56:59], v[24:27], v[172:175], v[56:59]\n\tv_mfma_f32_16x16x32_bf16 v[64:67], v[24:27], v[176:179], v[64:67]\n\ts_waitcnt lgkmcnt(8)\n\tv_mfma_f32_16x16x32_bf16 v[52:55], v[28:31], v[168:171], v[52:55]\n\tds_read_b64_tr_b16 v[16:17], v0 offset:40960\n\t" \
    "ds_read_b64_tr_b16 v[18:19], v0 offset:41472\n\tv_mfma_f32_16x16x32_bf16 v[60:63], v[28:31], v[172:175], v[60:63]\n\tbuffer_load_dwordx4 v[160:163], v8, s[8:11], s68 offen offset:1024 sc1\n\tv_mfma_f32_16x16x32_bf16 v[68:71], v[28:31], v[176:179], v[68:71]\n\ts_waitcnt lgkmcnt(8)\n\tv_mfma_f32_16x16x32_bf16 a[24:27], v[32:35], v[168:171], a[24:27]\n\tds_read_b64_tr_b16 v[20:21], v1 offset:40960\n\tds_read_b64_tr_b16 v[22:23], v1 offset:41472\n\t" \
    "v_mfma_f32_16x16x32_bf16 a[32:35], v[32:35], v[172:175], a[32:35]\n\tbuffer_load_dwordx4 v[164:167], v8, s[8:11], s68 offen offset:2048 sc1\n\ts_add_u32 s68, s44, 516096\n\tv_mfma_f32_16x16x32_bf16 a[40:43], v[32:35], v[176:179], a[40:43]\n\ts_waitcnt lgkmcnt(8)\n\tv_mfma_f32_16x16x32_bf16 a[28:31], v[36:39], v[168:171], a[28:31]\n\tds_read_b64_tr_b16 v[24:25], v2 offset:40960\n\tds_read_b64_tr_b16 v[26:27], v2 offset:41472\n\t" \
    "v_mfma_f32_16x16x32_bf16 a[36:39], v[36:39], v[172:175], a[36:39]\n\tv_mfma_f32_16x16x32_bf16 a[44:47], v[36:39], v[176:179], a[44:47]\n\ts_waitcnt lgkmcnt(8)\n\tv_mfma_f32_16x16x32_bf16 v[72:75], v[40:43], v[168:171], v[72:75]\n\tds_read_b64_tr_b16 v[28:29], v3 offset:40960\n\tds_read_b64_tr_b16 v[30:31], v3 offset:41472\n\tv_mfma_f32_16x16x32_bf16 v[80:83], v[40:43], v[172:175], v[80:83]\n\tbuffer_load_dwordx4 a[68:71], v8, s[8:11], s68 offen offset:0 sc1\n\t" \
    "v_mfma_f32_16x16x32_bf16 v[88:91], v[40:43], v[176:179], v[88:91]\n\ts_waitcnt lgkmcnt(8)\n\tv_mfma_f32_16x16x32_bf16 v[76:79], v[44:47], v[168:171], v[76:79]\n\tds_read_b64_tr_b16 v[32:33], v0 offset:45056\n\tds_read_b64_tr_b16 v[34:35], v0 offset:45568\n\tv_mfma_f32_16x16x32_bf16 v[84:87], v[44:47], v[172:175], v[84:87]\n\tbuffer_load_dwordx4 a[72:75], v8, s[8:11], s68 offen offset:1024 sc1\n\tv_mfma_f32_16x16x32_bf16 v[92:95], v[44:47], v[176:179], v[92:95]\n\t" \
    "s_barrier\n\ts_waitcnt vmcnt(26) lgkmcnt(8)\n\tv_mfma_f32_16x16x32_bf16 a[0:3], v[16:19], v[180:183], a[0:3]\n\tds_read_b64_tr_b16 v[36:37], v1 offset:45056\n\tds_read_b64_tr_b16 v[38:39], v1 offset:45568\n\tv_mfma_f32_16x16x32_bf16 a[8:11], v[16:19], v[184:187], a[8:11]\n\tbuffer_load_dwordx4 a[76:79], v8, s[8:11], s68 offen offset:2048 sc1\n\ts_mov_b32 s69, 3538944\n\t" \
    "s_add_u32 m0, s72, 32768\n\tv_mfma_f32_16x16x32_bf16 a[16:19], v[16:19], v[188:191], a[16:19]\n\ts_waitcnt lgkmcnt(8)\n\tv_mfma_f32_16x16x32_bf16 a[4:7], v[20:23], v[180:183], a[4:7]\n\tds_read_b64_tr_b16 v[40:41], v2 offset:45056\n\tds_read_b64_tr_b16 v[42:43], v2 offset:45568\n\tv_mfma_f32_16x16x32_bf16 a[12:15], v[20:23], v[184:187], a[12:15]\n\tbuffer_load_dwordx4 v10, s[12:15], s69 offen lds\n\t" \
    "s_add_u32 m0, s72, 33792\n\tv_mfma_f32_16x16x32_bf16 a[20:23], v[20:23], v[188:191], a[20:23]\n\ts_waitcnt lgkmcnt(8)\n\tv_mfma_f32_16x16x32_bf16 v[48:51], v[24:27], v[180:183], v[48:51]\n\tds_read_b64_tr_b16 v[44:45], v3 offset:45056\n\tds_read_b64_tr_b16 v[46:47], v3 offset:45568\n\tv_mfma_f32_16x16x32_bf16 v[56:59], v[24:27], v[184:187], v[56:59]\n\tbuffer_load_dwordx4 v11, s[12:15], s69 offen lds\n\t" \
    "s_mov_b32 s69, 3539072\n\ts_add_u32 m0, s72, 36864\n\tv_mfma_f32_16x16x32_bf16 v[64:67], v[24:27], v[188:191], v[64:67]\n\ts_waitcnt lgkmcnt(8)\n\tv_mfma_f32_16x16x32_bf16 v[52:55], v[28:31], v[180:183], v[52:55]\n\tds_read_b64_tr_b16 v[16:17], v0 offset:49152\n\tds_read_b64_tr_b16 v[18:19], v0 offset:49664\n\tv_mfma_f32_16x16x32_bf16 v[60:63], v[28:31], v[184:187], v[60:63]\n\t" \
    "v_mfma_f32_16x16x32_bf16 v[68:71], v[28:31], v[188:191], v[68:71]\n\ts_waitcnt lgkmcnt(8)\n\tv_mfma_f32_16x16x32_bf16 a[24:27], v[32:35], v[180:183], a[24:27]\n\tds_read_b64_tr_b16 v[20:21], v1 offset:49152\n\tds_read_b64_tr_b16 v[22:23], v1 offset:49664\n\tv_mfma_f32_16x16x32_bf16 a[32:35], v[32:35], v[184:187], a[32:35]\n\tbuffer_load_dwordx4 v10, s[12:15], s69 offen lds\n\ts_add_u32 m0, s72, 37888\n\t" \
    "v_mfma_f32_16x16x32_bf16 a[40:43], v[32:35], v[188:191], a[40:43]\n\ts_waitcnt lgkmcnt(8)\n\tv_mfma_f32_16x16x32_bf16 a[28:31], v[36:39], v[180:183], a[28:31]\n\tds_read_b64_tr_b16 v[24:25], v2 offset:49152\n\tds_read_b64_tr_b16 v[26:27], v2 offset:49664\n\tv_mfma_f32_16x16x32_bf16 a[36:39], v[36:39], v[184:187], a[36:39]\n\tbuffer_load_dwordx4 v11, s[12:15], s69 offen lds\n\ts_add_u32 s68, s44, 552960\n\t" \
    "v_mfma_f32_16x16x32_bf16 a[44:47], v[36:39], v[188:191], a[44:47]\n\ts_waitcnt lgkmcnt(8)\n\tv_mfma_f32_16x16x32_bf16 v[72:75], v[40:43], v[180:183], v[72:75]\n\tds_read_b64_tr_b16 v[28:29], v3 offset:49152\n\tds_read_b64_tr_b16 v[30:31], v3 offset:49664\n\tv_mfma_f32_16x16x32_bf16 v[80:83], v[40:43], v[184:187], v[80:83]\n\tv_mfma_f32_16x16x32_bf16 v[88:91], v[40:43], v[188:191], v[88:91]\n\ts_waitcnt lgkmcnt(8)\n\t" \
    "v_mfma_f32_16x16x32_bf16 v[76:79], v[44:47], v[180:183], v[76:79]\n\tds_read_b64_tr_b16 v[32:33], v0 offset:53248\n\tds_read_b64_tr_b16 v[34:35], v0 offset:53760\n\tv_mfma_f32_16x16x32_bf16 v[84:87], v[44:47], v[184:187], v[84:87]\n\tbuffer_load_dwordx4 v[168:171], v8, s[8:11], s68 offen offset:0 sc1\n\tv_mfma_f32_16x16x32_bf16 v[92:95], v[44:47], v[188:191], v[92:95]\n\ts_barrier\n\ts_waitcnt vmcnt(29) lgkmcnt(8)\n\t" \
    "v_mfma_f32_16x16x32_bf16 a[0:3], v[16:19], v[192:195], a[0:3]\n\tds_read_b64_tr_b16 v[36:37], v1 offset:53248\n\tds_read_b64_tr_b16 v[38:39], v1 offset:53760\n\tv_mfma_f32_16x16x32_bf16 a[8:11], v[16:19], v[196:199], a[8:11]\n\tbuffer_load_dwordx4 v[172:175], v8, s[8:11], s68 offen offset:1024 sc1\n\tv_mfma_f32_16x16x32_bf16 a[16:19], v[16:19], v[200:203], a[16:19]\n\ts_waitcnt lgkmcnt(8)\n\tv_mfma_f32_16x16x32_bf16 a[4:7], v[20:23], v[192:195], a[4:7]\n\t" \
    "ds_read_b64_tr_b16 v[40:41], v2 offset:53248\n\tds_read_b64_tr_b16 v[42:43], v2 offset:53760\n\tv_mfma_f32_16x16x32_bf16 a[12:15], v[20:23], v[196:199], a[12:15]\n\tbuffer_load_dwordx4 v[176:179], v8, s[8:11], s68 offen offset:2048 sc1\n\ts_mov_b32 s69, 3833856\n\ts_add_u32 m0, s72, 40960\n\tv_mfma_f32_16x16x32_bf16 a[20:23], v[20:23], v[200:203], a[20:23]\n\ts_waitcnt lgkmcnt(8)\n\t" \
    "v_mfma_f32_16x16x32_bf16 v[48:51], v[24:27], v[192:195], v[48:51]\n\tds_read_b64_tr_b16 v[44:45], v3 offset:53248\n\tds_read_b64_tr_b16 v[46:47], v3 offset:53760\n\tv_mfma_f32_16x16x32_bf16 v[56:59], v[24:27], v[196:199], v[56:59]\n\tbuffer_load_dwordx4 v10, s[12:15], s69 offen lds\n\ts_add_u32 m0, s72, 41984\n\tv_mfma_f32_16x16x32_bf16 v[64:67], v[24:27], v[200:203], v[64:67]\n\ts_waitcnt lgkmcnt(8)\n\t" \
    "v_mfma_f32_16x16x32_bf16 v[52:55], v[28:31], v[192:195], v[52:55]\n\tds_read_b64_tr_b16 v[16:17], v0 offset:57344\n\tds_read_b64_tr_b16 v[18:19], v0 offset:57856\n\tv_mfma_f32_16x16x32_bf16 v[60:63], v[28:31], v[196:199], v[60:63]\n\tv_mfma_f32_16x16x32_bf16 v[68:71], v[28:31], v[200:203], v[68:71]\n\ts_waitcnt lgkmcnt(8)\n\tv_mfma_f32_16x16x32_bf16 a[24:27], v[32:35], v[192:195], a[24:27]\n\tds_read_b64_tr_b16 v[20:21], v1 offset:57344\n\t" \
    "ds_read_b64_tr_b16 v[22:23], v1 offset:57856\n\tv_mfma_f32_16x16x32_bf16 a[32:35], v[32:35], v[196:199], a[32:35]\n\tbuffer_load_dwordx4 v11, s[12:15], s69 offen lds\n\ts_mov_b32 s69, 3833984\n\ts_add_u32 m0, s72, 45056\n\tv_mfma_f32_16x16x32_bf16 a[40:43], v[32:35], v[200:203], a[40:43]\n\ts_waitcnt lgkmcnt(8)\n\tv_mfma_f32_16x16x32_bf16 a[28:31], v[36:39], v[192:195], a[28:31]\n\t" \
    "ds_read_b64_tr_b16 v[24:25], v2 offset:57344\n\tds_read_b64_tr_b16 v[26:27], v2 offset:57856\n\tv_mfma_f32_16x16x32_bf16 a[36:39], v[36:39], v[196:199], a[36:39]\n\tbuffer_load_dwordx4 v10, s[12:15], s69 offen lds\n\ts_add_u32 m0, s72, 46080\n\tv_mfma_f32_16x16x32_bf16 a[44:47], v[36:39], v[200:203], a[44:47]\n\ts_waitcnt lgkmcnt(8)\n\tv_mfma_f32_16x16x32_bf16 v[72:75], v[40:43], v[192:195], v[72:75]\n\t" \
    "ds_read_b64_tr_b16 v[28:29], v3 offset:57344\n\tds_read_b64_tr_b16 v[30:31], v3 offset:57856\n\tv_mfma_f32_16x16x32_bf16 v[80:83], v[40:43], v[196:199], v[80:83]\n\tbuffer_load_dwordx4 v11, s[12:15], s69 offen lds\n\ts_add_u32 s68, s44, 589824\n\tv_mfma_f32_16x16x32_bf16 v[88:91], v[40:43], v[200:203], v[88:91]\n\ts_waitcnt lgkmcnt(8)\n\tv_mfma_f32_16x16x32_bf16 v[76:79], v[44:47], v[192:195], v[76:79]\n\t" \
    "ds_read_b64_tr_b16 v[32:33], v0 offset:61440\n\tds_read_b64_tr_b16 v[34:35], v0 offset:61952\n\tv_mfma_f32_16x16x32_bf16 v[84:87], v[44:47], v[196:199], v[84:87]\n\tv_mfma_f32_16x16x32_bf16 v[92:95], v[44:47], v[200:203], v[92:95]\n\ts_barrier\n\ts_waitcnt vmcnt(32) lgkmcnt(8)\n\tv_mfma_f32_16x16x32_bf16 a[0:3], v[16:19], v[120:123], a[0:3]\n\tds_read_b64_tr_b16 v[36:37], v1 offset:61440\n\t" \
    "ds_read_b64_tr_b16 v[38:39], v1 offset:61952\n\tv_mfma_f32_16x16x32_bf16 a[8:11], v[16:19], v[124:127], a[8:11]\n\tbuffer_load_dwordx4 v[180:183], v8, s[8:11], s68 offen offset:0 sc1\n\tv_mfma_f32_16x16x32_bf16 a[16:19], v[16:19], v[128:131], a[16:19]\n\ts_waitcnt lgkmcnt(8)\n\tv_mfma_f32_16x16x32_bf16 a[4:7], v[20:23], v[120:123], a[4:7]\n\tds_read_b64_tr_b16 v[40:41], v2 offset:61440\n\tds_read_b64_tr_b16 v[42:43], v2 offset:61952\n\t" \
    "v_mfma_f32_16x16x32_bf16 a[12:15], v[20:23], v[124:127], a[12:15]\n\tbuffer_load_dwordx4 v[184:187], v8, s[8:11], s68 offen offset:1024 sc1\n\tv_mfma_f32_16x16x32_bf16 a[20:23], v[20:23], v[128:131], a[20:23]\n\ts_waitcnt lgkmcnt(8)\n\tv_mfma_f32_16x16x32_bf16 v[48:51], v[24:27], v[120:123], v[48:51]\n\tds_read_b64_tr_b16 v[44:45], v3 offset:61440\n\tds_read_b64_tr_b16 v[46:47], v3 offset:61952\n\tv_mfma_f32_16x16x32_bf16 v[56:59], v[24:27], v[124:127], v[56:59]\n\t" \
    "buffer_load_dwordx4 v[188:191], v8, s[8:11], s68 offen offset:2048 sc1\n\ts_mov_b32 s69, 4128768\n\ts_add_u32 m0, s72, 49152\n\tv_mfma_f32_16x16x32_bf16 v[64:67], v[24:27], v[128:131], v[64:67]\n\ts_waitcnt lgkmcnt(8)\n\tv_mfma_f32_16x16x32_bf16 v[52:55], v[28:31], v[120:123], v[52:55]\n\tds_read_b64_tr_b16 v[16:17], v0 offset:0\n\tds_read_b64_tr_b16 v[18:19], v0 offset:512\n\t" \
    "v_mfma_f32_16x16x32_bf16 v[60:63], v[28:31], v[124:127], v[60:63]\n\tbuffer_load_dwordx4 v10, s[12:15], s69 offen lds\n\ts_add_u32 m0, s72, 50176\n\tv_mfma_f32_16x16x32_bf16 v[68:71], v[28:31], v[128:131], v[68:71]\n\ts_waitcnt lgkmcnt(8)\n\tv_mfma_f32_16x16x32_bf16 a[24:27], v[32:35], v[120:123], a[24:27]\n\tds_read_b64_tr_b16 v[20:21], v1 offset:0\n\tds_read_b64_tr_b16 v[22:23], v1 offset:512\n\t" \
    "v_mfma_f32_16x16x32_bf16 a[32:35], v[32:35], v[124:127], a[32:35]\n\tv_mfma_f32_16x16x32_bf16 a[40:43], v[32:35], v[128:131], a[40:43]\n\ts_waitcnt lgkmcnt(8)\n\tv_mfma_f32_16x16x32_bf16 a[28:31], v[36:39], v[120:123], a[28:31]\n\tds_read_b64_tr_b16 v[24:25], v2 offset:0\n\tds_read_b64_tr_b16 v[26:27], v2 offset:512\n\tv_mfma_f32_16x16x32_bf16 a[36:39], v[36:39], v[124:127], a[36:39]\n\tbuffer_load_dwordx4 v11, s[12:15], s69 offen lds\n\t" \
    "s_mov_b32 s69, 4128896\n\ts_add_u32 m0, s72, 53248\n\tv_mfma_f32_16x16x32_bf16 a[44:47], v[36:39], v[128:131], a[44:47]\n\ts_waitcnt lgkmcnt(8)\n\tv_mfma_f32_16x16x32_bf16 v[72:75], v[40:43], v[120:123], v[72:75]\n\tds_read_b64_tr_b16 v[28:29], v3 offset:0\n\tds_read_b64_tr_b16 v[30:31], v3 offset:512\n\tv_mfma_f32_16x16x32_bf16 v[80:83], v[40:43], v[124:127], v[80:83]\n\t" \
    "buffer_load_dwordx4 v10, s[12:15], s69 offen lds\n\ts_add_u32 m0, s72, 54272\n\tv_mfma_f32_16x16x32_bf16 v[88:91], v[40:43], v[128:131], v[88:91]\n\ts_waitcnt lgkmcnt(8)\n\tv_mfma_f32_16x16x32_bf16 v[76:79], v[44:47], v[120:123], v[76:79]\n\tds_read_b64_tr_b16 v[32:33], v0 offset:4096\n\tds_read_b64_tr_b16 v[34:35], v0 offset:4608\n\tv_mfma_f32_16x16x32_bf16 v[84:87], v[44:47], v[124:127], v[84:87]\n\t" \
    "buffer_load_dwordx4 v11, s[12:15], s69 offen lds\n\ts_add_u32 s68, s44, 626688\n\tv_mfma_f32_16x16x32_bf16 v[92:95], v[44:47], v[128:131], v[92:95]\n\ts_barrier\n\ts_waitcnt vmcnt(36) lgkmcnt(8)\n\tv_mfma_f32_16x16x32_bf16 a[0:3], v[16:19], v[204:207], a[0:3]\n\tds_read_b64_tr_b16 v[36:37], v1 offset:4096\n\tds_read_b64_tr_b16 v[38:39], v1 offset:4608\n\t" \
    "v_mfma_f32_16x16x32_bf16 a[8:11], v[16:19], v[208:211], a[8:11]\n\tbuffer_load_dwordx4 v[192:195], v8, s[8:11], s68 offen offset:0 sc1\n\tv_mfma_f32_16x16x32_bf16 a[16:19], v[16:19], v[212:215], a[16:19]\n\ts_waitcnt lgkmcnt(8)\n\tv_mfma_f32_16x16x32_bf16 a[4:7], v[20:23], v[204:207], a[4:7]\n\tds_read_b64_tr_b16 v[40:41], v2 offset:4096\n\tds_read_b64_tr_b16 v[42:43], v2 offset:4608\n\tv_mfma_f32_16x16x32_bf16 a[12:15], v[20:23], v[208:211], a[12:15]\n\t" \
    "buffer_load_dwordx4 v[196:199], v8, s[8:11], s68 offen offset:1024 sc1\n\tv_mfma_f32_16x16x32_bf16 a[20:23], v[20:23], v[212:215], a[20:23]\n\ts_waitcnt lgkmcnt(8)\n\tv_mfma_f32_16x16x32_bf16 v[48:51], v[24:27], v[204:207], v[48:51]\n\tds_read_b64_tr_b16 v[44:45], v3 offset:4096\n\tds_read_b64_tr_b16 v[46:47], v3 offset:4608\n\tv_mfma_f32_16x16x32_bf16 v[56:59], v[24:27], v[208:211], v[56:59]\n\tv_mfma_f32_16x16x32_bf16 v[64:67], v[24:27], v[212:215], v[64:67]\n\t" \
    "s_waitcnt lgkmcnt(8)\n\tv_mfma_f32_16x16x32_bf16 v[52:55], v[28:31], v[204:207], v[52:55]\n\tds_read_b64_tr_b16 v[16:17], v0 offset:8192\n\tds_read_b64_tr_b16 v[18:19], v0 offset:8704\n\tv_mfma_f32_16x16x32_bf16 v[60:63], v[28:31], v[208:211], v[60:63]\n\tbuffer_load_dwordx4 v[200:203], v8, s[8:11], s68 offen offset:2048 sc1\n\ts_mov_b32 s69, 4423680\n\ts_add_u32 m0, s72, 57344\n\t" \
    "v_mfma_f32_16x16x32_bf16 v[68:71], v[28:31], v[212:215], v[68:71]\n\ts_waitcnt lgkmcnt(8)\n\tv_mfma_f32_16x16x32_bf16 a[24:27], v[32:35], v[204:207], a[24:27]\n\tds_read_b64_tr_b16 v[20:21], v1 offset:8192\n\tds_read_b64_tr_b16 v[22:23], v1 offset:8704\n\tv_mfma_f32_16x16x32_bf16 a[32:35], v[32:35], v[208:211], a[32:35]\n\tbuffer_load_dwordx4 v10, s[12:15], s69 offen lds\n\ts_add_u32 m0, s72, 58368\n\t" \
    "v_mfma_f32_16x16x32_bf16 a[40:43], v[32:35], v[212:215], a[40:43]\n\ts_waitcnt lgkmcnt(8)\n\tv_mfma_f32_16x16x32_bf16 a[28:31], v[36:39], v[204:207], a[28:31]\n\tds_read_b64_tr_b16 v[24:25], v2 offset:8192\n\tds_read_b64_tr_b16 v[26:27], v2 offset:8704\n\tv_mfma_f32_16x16x32_bf16 a[36:39], v[36:39], v[208:211], a[36:39]\n\tv_mfma_f32_16x16x32_bf16 a[44:47], v[36:39], v[212:215], a[44:47]\n\ts_waitcnt lgkmcnt(8)\n\t" \
    "v_mfma_f32_16x16x32_bf16 v[72:75], v[40:43], v[204:207], v[72:75]\n\tds_read_b64_tr_b16 v[28:29], v3 offset:8192\n\tds_read_b64_tr_b16 v[30:31], v3 offset:8704\n\tv_mfma_f32_16x16x32_bf16 v[80:83], v[40:43], v[208:211], v[80:83]\n\tbuffer_load_dwordx4 v11, s[12:15], s69 offen lds\n\ts_mov_b32 s69, 4423808\n\ts_add_u32 m0, s72, 61440\n\tv_mfma_f32_16x16x32_bf16 v[88:91], v[40:43], v[212:215], v[88:91]\n\t" \
    "s_waitcnt lgkmcnt(8)\n\tv_mfma_f32_16x16x32_bf16 v[76:79], v[44:47], v[204:207], v[76:79]\n\tds_read_b64_tr_b16 v[32:33], v0 offset:12288\n\tds_read_b64_tr_b16 v[34:35], v0 offset:12800\n\tv_mfma_f32_16x16x32_bf16 v[84:87], v[44:47], v[208:211], v[84:87]\n\tbuffer_load_dwordx4 v10, s[12:15], s69 offen lds\n\ts_add_u32 m0, s72, 62464\n\tv_mfma_f32_16x16x32_bf16 v[92:95], v[44:47], v[212:215], v[92:95]\n\t" \
    "s_barrier\n\ts_waitcnt vmcnt(39) lgkmcnt(8)\n\tv_mfma_f32_16x16x32_bf16 a[0:3], v[16:19], v[132:135], a[0:3]\n\tds_read_b64_tr_b16 v[36:37], v1 offset:12288\n\tds_read_b64_tr_b16 v[38:39], v1 offset:12800\n\tv_mfma_f32_16x16x32_bf16 a[8:11], v[16:19], v[136:139], a[8:11]\n\tbuffer_load_dwordx4 v11, s[12:15], s69 offen lds\n\ts_mov_b32 s69, 4718592\n\t" \
    "s_add_u32 m0, s72, 0\n\tv_mfma_f32_16x16x32_bf16 a[16:19], v[16:19], v[140:143], a[16:19]\n\ts_waitcnt lgkmcnt(8)\n\tv_mfma_f32_16x16x32_bf16 a[4:7], v[20:23], v[132:135], a[4:7]\n\tds_read_b64_tr_b16 v[40:41], v2 offset:12288\n\tds_read_b64_tr_b16 v[42:43], v2 offset:12800\n\tv_mfma_f32_16x16x32_bf16 a[12:15], v[20:23], v[136:139], a[12:15]\n\tbuffer_load_dwordx4 v10, s[12:15], s69 offen lds\n\t" \
    "s_add_u32 m0, s72, 1024\n\tv_mfma_f32_16x16x32_bf16 a[20:23], v[20:23], v[140:143], a[20:23]\n\ts_waitcnt lgkmcnt(8)\n\tv_mfma_f32_16x16x32_bf16 v[48:51], v[24:27], v[132:135], v[48:51]\n\tds_read_b64_tr_b16 v[44:45], v3 offset:12288\n\tds_read_b64_tr_b16 v[46:47], v3 offset:12800\n\tv_mfma_f32_16x16x32_bf16 v[56:59], v[24:27], v[136:139], v[56:59]\n\tv_mfma_f32_16x16x32_bf16 v[64:67], v[24:27], v[140:143], v[64:67]\n\t" \
    "s_waitcnt lgkmcnt(8)\n\tv_mfma_f32_16x16x32_bf16 v[52:55], v[28:31], v[132:135], v[52:55]\n\tds_read_b64_tr_b16 v[16:17], v0 offset:16384\n\tds_read_b64_tr_b16 v[18:19], v0 offset:16896\n\tv_mfma_f32_16x16x32_bf16 v[60:63], v[28:31], v[136:139], v[60:63]\n\tbuffer_load_dwordx4 v11, s[12:15], s69 offen lds\n\ts_mov_b32 s69, 5013504\n\ts_add_u32 m0, s72, 4096\n\t" \
    "v_mfma_f32_16x16x32_bf16 v[68:71], v[28:31], v[140:143], v[68:71]\n\ts_waitcnt lgkmcnt(8)\n\tv_mfma_f32_16x16x32_bf16 a[24:27], v[32:35], v[132:135], a[24:27]\n\tds_read_b64_tr_b16 v[20:21], v1 offset:16384\n\tds_read_b64_tr_b16 v[22:23], v1 offset:16896\n\tv_mfma_f32_16x16x32_bf16 a[32:35], v[32:35], v[136:139], a[32:35]\n\tbuffer_load_dwordx4 v10, s[12:15], s69 offen lds\n\ts_add_u32 m0, s72, 5120\n\t" \
    "v_mfma_f32_16x16x32_bf16 a[40:43], v[32:35], v[140:143], a[40:43]\n\ts_waitcnt lgkmcnt(8)\n\tv_mfma_f32_16x16x32_bf16 a[28:31], v[36:39], v[132:135], a[28:31]\n\tds_read_b64_tr_b16 v[24:25], v2 offset:16384\n\tds_read_b64_tr_b16 v[26:27], v2 offset:16896\n\tv_mfma_f32_16x16x32_bf16 a[36:39], v[36:39], v[136:139], a[36:39]\n\tbuffer_load_dwordx4 v11, s[12:15], s69 offen lds\n\ts_add_u32 s68, s44, 663552\n\t" \
    "v_mfma_f32_16x16x32_bf16 a[44:47], v[36:39], v[140:143], a[44:47]\n\ts_waitcnt lgkmcnt(8)\n\tv_mfma_f32_16x16x32_bf16 v[72:75], v[40:43], v[132:135], v[72:75]\n\tds_read_b64_tr_b16 v[28:29], v3 offset:16384\n\tds_read_b64_tr_b16 v[30:31], v3 offset:16896\n\tv_mfma_f32_16x16x32_bf16 v[80:83], v[40:43], v[136:139], v[80:83]\n\tv_mfma_f32_16x16x32_bf16 v[88:91], v[40:43], v[140:143], v[88:91]\n\ts_waitcnt lgkmcnt(8)\n\t" \
    "v_mfma_f32_16x16x32_bf16 v[76:79], v[44:47], v[132:135], v[76:79]\n\tds_read_b64_tr_b16 v[32:33], v0 offset:20480\n\tds_read_b64_tr_b16 v[34:35], v0 offset:20992\n\tv_mfma_f32_16x16x32_bf16 v[84:87], v[44:47], v[136:139], v[84:87]\n\tbuffer_load_dwordx4 v[120:123], v8, s[8:11], s68 offen offset:0 sc1\n\tv_mfma_f32_16x16x32_bf16 v[92:95], v[44:47], v[140:143], v[92:95]\n\ts_barrier\n\ts_waitcnt vmcnt(42) lgkmcnt(8)\n\t" \
    "v_mfma_f32_16x16x32_bf16 a[0:3], v[16:19], v[216:219], a[0:3]\n\tds_read_b64_tr_b16 v[36:37], v1 offset:20480\n\tds_read_b64_tr_b16 v[38:39], v1 offset:20992\n\tv_mfma_f32_16x16x32_bf16 a[8:11], v[16:19], a[48:51], a[8:11]\n\tbuffer_load_dwordx4 v[124:127], v8, s[8:11], s68 offen offset:1024 sc1\n\tv_mfma_f32_16x16x32_bf16 a[16:19], v[16:19], a[52:55], a[16:19]\n\ts_waitcnt lgkmcnt(8)\n\tv_mfma_f32_16x16x32_bf16 a[4:7], v[20:23], v[216:219], a[4:7]\n\t" \
    "ds_read_b64_tr_b16 v[40:41], v2 offset:20480\n\tds_read_b64_tr_b16 v[42:43], v2 offset:20992\n\tv_mfma_f32_16x16x32_bf16 a[12:15], v[20:23], a[48:51], a[12:15]\n\tbuffer_load_dwordx4 v[128:131], v8, s[8:11], s68 offen offset:2048 sc1\n\ts_mov_b32 s69, 5308416\n\ts_add_u32 m0, s72, 8192\n\tv_mfma_f32_16x16x32_bf16 a[20:23], v[20:23], a[52:55], a[20:23]\n\ts_waitcnt lgkmcnt(8)\n\t" \
    "v_mfma_f32_16x16x32_bf16 v[48:51], v[24:27], v[216:219], v[48:51]\n\tds_read_b64_tr_b16 v[44:45], v3 offset:20480\n\tds_read_b64_tr_b16 v[46:47], v3 offset:20992\n\tv_mfma_f32_16x16x32_bf16 v[56:59], v[24:27], a[48:51], v[56:59]\n\tbuffer_load_dwordx4 v10, s[12:15], s69 offen lds\n\ts_add_u32 m0, s72, 9216\n\tv_mfma_f32_16x16x32_bf16 v[64:67], v[24:27], a[52:55], v[64:67]\n\ts_waitcnt lgkmcnt(8)\n\t" \
    "v_mfma_f32_16x16x32_bf16 v[52:55], v[28:31], v[216:219], v[52:55]\n\tds_read_b64_tr_b16 v[16:17], v0 offset:24576\n\tds_read_b64_tr_b16 v[18:19], v0 offset:25088\n\tv_mfma_f32_16x16x32_bf16 v[60:63], v[28:31], a[48:51], v[60:63]\n\tv_mfma_f32_16x16x32_bf16 v[68:71], v[28:31], a[52:55], v[68:71]\n\ts_waitcnt lgkmcnt(8)\n\tv_mfma_f32_16x16x32_bf16 a[24:27], v[32:35], v[216:219], a[24:27]\n\tds_read_b64_tr_b16 v[20:21], v1 offset:24576\n\t" \
    "ds_read_b64_tr_b16 v[22:23], v1 offset:25088\n\tv_mfma_f32_16x16x32_bf16 a[32:35], v[32:35], a[48:51], a[32:35]\n\tbuffer_load_dwordx4 v11, s[12:15], s69 offen lds\n\ts_mov_b32 s69, 5603328\n\ts_add_u32 m0, s72, 12288\n\tv_mfma_f32_16x16x32_bf16 a[40:43], v[32:35], a[52:55], a[40:43]\n\ts_waitcnt lgkmcnt(8)\n\tv_mfma_f32_16x16x32_bf16 a[28:31], v[36:39], v[216:219], a[28:31]\n\t" \
    "ds_read_b64_tr_b16 v[24:25], v2 offset:24576\n\tds_read_b64_tr_b16 v[26:27], v2 offset:25088\n\tv_mfma_f32_16x16x32_bf16 a[36:39], v[36:39], a[48:51], a[36:39]\n\tbuffer_load_dwordx4 v10, s[12:15], s69 offen lds\n\ts_add_u32 m0, s72, 13312\n\tv_mfma_f32_16x16x32_bf16 a[44:47], v[36:39], a[52:55], a[44:47]\n\ts_waitcnt lgkmcnt(8)\n\tv_mfma_f32_16x16x32_bf16 v[72:75], v[40:43], v[216:219], v[72:75]\n\t" \
    "ds_read_b64_tr_b16 v[28:29], v3 offset:24576\n\tds_read_b64_tr_b16 v[30:31], v3 offset:25088\n\tv_mfma_f32_16x16x32_bf16 v[80:83], v[40:43], a[48:51], v[80:83]\n\tbuffer_load_dwordx4 v11, s[12:15], s69 offen lds\n\ts_add_u32 s68, s44, 700416\n\tv_mfma_f32_16x16x32_bf16 v[88:91], v[40:43], a[52:55], v[88:91]\n\ts_waitcnt lgkmcnt(8)\n\tv_mfma_f32_16x16x32_bf16 v[76:79], v[44:47], v[216:219], v[76:79]\n\t" \
    "ds_read_b64_tr_b16 v[32:33], v0 offset:28672\n\tds_read_b64_tr_b16 v[34:35], v0 offset:29184\n\tv_mfma_f32_16x16x32_bf16 v[84:87], v[44:47], a[48:51], v[84:87]\n\tv_mfma_f32_16x16x32_bf16 v[92:95], v[44:47], a[52:55], v[92:95]\n\ts_waitcnt vmcnt(32)\n\ts_barrier\n\ts_waitcnt lgkmcnt(8)\n\tv_mfma_f32_16x16x32_bf16 a[0:3], v[16:19], v[144:147], a[0:3]\n\t" \
    "ds_read_b64_tr_b16 v[36:37], v1 offset:28672\n\tds_read_b64_tr_b16 v[38:39], v1 offset:29184\n\tv_mfma_f32_16x16x32_bf16 a[8:11], v[16:19], v[148:151], a[8:11]\n\tbuffer_load_dwordx4 v[132:135], v8, s[8:11], s68 offen offset:0 sc1\n\tv_mfma_f32_16x16x32_bf16 a[16:19], v[16:19], v[152:155], a[16:19]\n\ts_waitcnt lgkmcnt(8)\n\tv_mfma_f32_16x16x32_bf16 a[4:7], v[20:23], v[144:147], a[4:7]\n\tds_read_b64_tr_b16 v[40:41], v2 offset:28672\n\t" \
    "ds_read_b64_tr_b16 v[42:43], v2 offset:29184\n\tv_mfma_f32_16x16x32_bf16 a[12:15], v[20:23], v[148:151], a[12:15]\n\tbuffer_load_dwordx4 v[136:139], v8, s[8:11], s68 offen offset:1024 sc1\n\tv_mfma_f32_16x16x32_bf16 a[20:23], v[20:23], v[152:155], a[20:23]\n\ts_waitcnt lgkmcnt(8)\n\tv_mfma_f32_16x16x32_bf16 v[48:51], v[24:27], v[144:147], v[48:51]\n\tds_read_b64_tr_b16 v[44:45], v3 offset:28672\n\tds_read_b64_tr_b16 v[46:47], v3 offset:29184\n\t" \
    "v_mfma_f32_16x16x32_bf16 v[56:59], v[24:27], v[148:151], v[56:59]\n\tbuffer_load_dwordx4 v[140:143], v8, s[8:11], s68 offen offset:2048 sc1\n\ts_mov_b32 s69, 5898240\n\ts_add_u32 m0, s72, 16384\n\tv_mfma_f32_16x16x32_bf16 v[64:67], v[24:27], v[152:155], v[64:67]\n\ts_waitcnt lgkmcnt(8)\n\tv_mfma_f32_16x16x32_bf16 v[52:55], v[28:31], v[144:147], v[52:55]\n\tds_read_b64_tr_b16 v[16:17], v0 offset:32768\n\t" \
    "ds_read_b64_tr_b16 v[18:19], v0 offset:33280\n\tv_mfma_f32_16x16x32_bf16 v[60:63], v[28:31], v[148:151], v[60:63]\n\tbuffer_load_dwordx4 v10, s[12:15], s69 offen lds\n\ts_add_u32 m0, s72, 17408\n\tv_mfma_f32_16x16x32_bf16 v[68:71], v[28:31], v[152:155], v[68:71]\n\ts_waitcnt lgkmcnt(8)\n\tv_mfma_f32_16x16x32_bf16 a[24:27], v[32:35], v[144:147], a[24:27]\n\tds_read_b64_tr_b16 v[20:21], v1 offset:32768\n\t" \
    "ds_read_b64_tr_b16 v[22:23], v1 offset:33280\n\tv_mfma_f32_16x16x32_bf16 a[32:35], v[32:35], v[148:151], a[32:35]\n\tv_mfma_f32_16x16x32_bf16 a[40:43], v[32:35], v[152:155], a[40:43]\n\ts_waitcnt lgkmcnt(8)\n\tv_mfma_f32_16x16x32_bf16 a[28:31], v[36:39], v[144:147], a[28:31]\n\tds_read_b64_tr_b16 v[24:25], v2 offset:32768\n\tds_read_b64_tr_b16 v[26:27], v2 offset:33280\n\tv_mfma_f32_16x16x32_bf16 a[36:39], v[36:39], v[148:151], a[36:39]\n\t" \
    "buffer_load_dwordx4 v11, s[12:15], s69 offen lds\n\ts_mov_b32 s69, 6193152\n\ts_add_u32 m0, s72, 20480\n\tv_mfma_f32_16x16x32_bf16 a[44:47], v[36:39], v[152:155], a[44:47]\n\ts_waitcnt lgkmcnt(8)\n\tv_mfma_f32_16x16x32_bf16 v[72:75], v[40:43], v[144:147], v[72:75]\n\tds_read_b64_tr_b16 v[28:29], v3 offset:32768\n\tds_read_b64_tr_b16 v[30:31], v3 offset:33280\n\t" \
    "v_mfma_f32_16x16x32_bf16 v[80:83], v[40:43], v[148:151], v[80:83]\n\tbuffer_load_dwordx4 v10, s[12:15], s69 offen lds\n\ts_add_u32 m0, s72, 21504\n\tv_mfma_f32_16x16x32_bf16 v[88:91], v[40:43], v[152:155], v[88:91]\n\ts_waitcnt lgkmcnt(8)\n\tv_mfma_f32_16x16x32_bf16 v[76:79], v[44:47], v[144:147], v[76:79]\n\tds_read_b64_tr_b16 v[32:33], v0 offset:36864\n\tds_read_b64_tr_b16 v[34:35], v0 offset:37376\n\t" \
    "v_mfma_f32_16x16x32_bf16 v[84:87], v[44:47], v[148:151], v[84:87]\n\tv_mfma_f32_16x16x32_bf16 v[92:95], v[44:47], v[152:155], v[92:95]\n\ts_waitcnt vmcnt(31)\n\ts_barrier\n\ts_waitcnt lgkmcnt(8)\n\tv_mfma_f32_16x16x32_bf16 a[0:3], v[16:19], a[56:59], a[0:3]\n\tds_read_b64_tr_b16 v[36:37], v1 offset:36864\n\tds_read_b64_tr_b16 v[38:39], v1 offset:37376\n\t" \
    "v_mfma_f32_16x16x32_bf16 a[8:11], v[16:19], a[60:63], a[8:11]\n\tbuffer_load_dwordx4 v11, s[12:15], s69 offen lds\n\ts_mov_b32 s69, 6488064\n\ts_add_u32 m0, s72, 24576\n\tv_mfma_f32_16x16x32_bf16 a[16:19], v[16:19], a[64:67], a[16:19]\n\ts_waitcnt lgkmcnt(8)\n\tv_mfma_f32_16x16x32_bf16 a[4:7], v[20:23], a[56:59], a[4:7]\n\tds_read_b64_tr_b16 v[40:41], v2 offset:36864\n\t" \
    "ds_read_b64_tr_b16 v[42:43], v2 offset:37376\n\tv_mfma_f32_16x16x32_bf16 a[12:15], v[20:23], a[60:63], a[12:15]\n\tbuffer_load_dwordx4 v10, s[12:15], s69 offen lds\n\ts_add_u32 m0, s72, 25600\n\tv_mfma_f32_16x16x32_bf16 a[20:23], v[20:23], a[64:67], a[20:23]\n\ts_waitcnt lgkmcnt(8)\n\tv_mfma_f32_16x16x32_bf16 v[48:51], v[24:27], a[56:59], v[48:51]\n\tds_read_b64_tr_b16 v[44:45], v3 offset:36864\n\t" \
    "ds_read_b64_tr_b16 v[46:47], v3 offset:37376\n\tv_mfma_f32_16x16x32_bf16 v[56:59], v[24:27], a[60:63], v[56:59]\n\tbuffer_load_dwordx4 v11, s[12:15], s69 offen lds\n\ts_mov_b32 s69, 6782976\n\ts_add_u32 m0, s72, 28672\n\tv_mfma_f32_16x16x32_bf16 v[64:67], v[24:27], a[64:67], v[64:67]\n\ts_waitcnt lgkmcnt(8)\n\tv_mfma_f32_16x16x32_bf16 v[52:55], v[28:31], a[56:59], v[52:55]\n\t" \
    "ds_read_b64_tr_b16 v[16:17], v0 offset:40960\n\tds_read_b64_tr_b16 v[18:19], v0 offset:41472\n\tv_mfma_f32_16x16x32_bf16 v[60:63], v[28:31], a[60:63], v[60:63]\n\tbuffer_load_dwordx4 v10, s[12:15], s69 offen lds\n\ts_add_u32 m0, s72, 29696\n\tv_mfma_f32_16x16x32_bf16 v[68:71], v[28:31], a[64:67], v[68:71]\n\ts_waitcnt lgkmcnt(8)\n\tv_mfma_f32_16x16x32_bf16 a[24:27], v[32:35], a[56:59], a[24:27]\n\t" \
    "ds_read_b64_tr_b16 v[20:21], v1 offset:40960\n\tds_read_b64_tr_b16 v[22:23], v1 offset:41472\n\tv_mfma_f32_16x16x32_bf16 a[32:35], v[32:35], a[60:63], a[32:35]\n\tbuffer_load_dwordx4 v11, s[12:15], s69 offen lds\n\ts_add_u32 s68, s44, 737280\n\tv_mfma_f32_16x16x32_bf16 a[40:43], v[32:35], a[64:67], a[40:43]\n\ts_waitcnt lgkmcnt(8)\n\tv_mfma_f32_16x16x32_bf16 a[28:31], v[36:39], a[56:59], a[28:31]\n\t" \
    "ds_read_b64_tr_b16 v[24:25], v2 offset:40960\n\tds_read_b64_tr_b16 v[26:27], v2 offset:41472\n\tv_mfma_f32_16x16x32_bf16 a[36:39], v[36:39], a[60:63], a[36:39]\n\tv_mfma_f32_16x16x32_bf16 a[44:47], v[36:39], a[64:67], a[44:47]\n\ts_waitcnt lgkmcnt(8)\n\tv_mfma_f32_16x16x32_bf16 v[72:75], v[40:43], a[56:59], v[72:75]\n\tds_read_b64_tr_b16 v[28:29], v3 offset:40960\n\tds_read_b64_tr_b16 v[30:31], v3 offset:41472\n\t" \
    "v_mfma_f32_16x16x32_bf16 v[80:83], v[40:43], a[60:63], v[80:83]\n\tbuffer_load_dwordx4 v[144:147], v8, s[8:11], s68 offen offset:0 sc1\n\tv_mfma_f32_16x16x32_bf16 v[88:91], v[40:43], a[64:67], v[88:91]\n\ts_waitcnt lgkmcnt(8)\n\tv_mfma_f32_16x16x32_bf16 v[76:79], v[44:47], a[56:59], v[76:79]\n\tds_read_b64_tr_b16 v[32:33], v0 offset:45056\n\tds_read_b64_tr_b16 v[34:35], v0 offset:45568\n\tv_mfma_f32_16x16x32_bf16 v[84:87], v[44:47], a[60:63], v[84:87]\n\t" \
    "buffer_load_dwordx4 v[148:151], v8, s[8:11], s68 offen offset:1024 sc1\n\tv_mfma_f32_16x16x32_bf16 v[92:95], v[44:47], a[64:67], v[92:95]\n\ts_waitcnt vmcnt(31)\n\ts_barrier\n\ts_waitcnt lgkmcnt(8)\n\tv_mfma_f32_16x16x32_bf16 a[0:3], v[16:19], v[156:159], a[0:3]\n\tds_read_b64_tr_b16 v[36:37], v1 offset:45056\n\tds_read_b64_tr_b16 v[38:39], v1 offset:45568\n\t" \
    "v_mfma_f32_16x16x32_bf16 a[8:11], v[16:19], v[160:163], a[8:11]\n\tbuffer_load_dwordx4 v[152:155], v8, s[8:11], s68 offen offset:2048 sc1\n\ts_add_u32 s68, s44, 774144\n\tv_mfma_f32_16x16x32_bf16 a[16:19], v[16:19], v[164:167], a[16:19]\n\ts_waitcnt lgkmcnt(8)\n\tv_mfma_f32_16x16x32_bf16 a[4:7], v[20:23], v[156:159], a[4:7]\n\tds_read_b64_tr_b16 v[40:41], v2 offset:45056\n\tds_read_b64_tr_b16 v[42:43], v2 offset:45568\n\t" \
    "v_mfma_f32_16x16x32_bf16 a[12:15], v[20:23], v[160:163], a[12:15]\n\tbuffer_load_dwordx4 v[204:207], v8, s[8:11], s68 offen offset:0 sc1\n\tv_mfma_f32_16x16x32_bf16 a[20:23], v[20:23], v[164:167], a[20:23]\n\ts_waitcnt lgkmcnt(8)\n\tv_mfma_f32_16x16x32_bf16 v[48:51], v[24:27], v[156:159], v[48:51]\n\tds_read_b64_tr_b16 v[44:45], v3 offset:45056\n\tds_read_b64_tr_b16 v[46:47], v3 offset:45568\n\tv_mfma_f32_16x16x32_bf16 v[56:59], v[24:27], v[160:163], v[56:59]\n\t" \
    "v_mfma_f32_16x16x32_bf16 v[64:67], v[24:27], v[164:167], v[64:67]\n\ts_waitcnt lgkmcnt(8)\n\tv_mfma_f32_16x16x32_bf16 v[52:55], v[28:31], v[156:159], v[52:55]\n\tds_read_b64_tr_b16 v[16:17], v0 offset:49152\n\tds_read_b64_tr_b16 v[18:19], v0 offset:49664\n\tv_mfma_f32_16x16x32_bf16 v[60:63], v[28:31], v[160:163], v[60:63]\n\tbuffer_load_dwordx4 v[208:211], v8, s[8:11], s68 offen offset:1024 sc1\n\tv_mfma_f32_16x16x32_bf16 v[68:71], v[28:31], v[164:167], v[68:71]\n\t" \
    "s_waitcnt lgkmcnt(8)\n\tv_mfma_f32_16x16x32_bf16 a[24:27], v[32:35], v[156:159], a[24:27]\n\tds_read_b64_tr_b16 v[20:21], v1 offset:49152\n\tds_read_b64_tr_b16 v[22:23], v1 offset:49664\n\tv_mfma_f32_16x16x32_bf16 a[32:35], v[32:35], v[160:163], a[32:35]\n\tbuffer_load_dwordx4 v[212:215], v8, s[8:11], s68 offen offset:2048 sc1\n\ts_mov_b32 s69, 7077888\n\ts_add_u32 m0, s72, 32768\n\t" \
    "v_mfma_f32_16x16x32_bf16 a[40:43], v[32:35], v[164:167], a[40:43]\n\ts_waitcnt lgkmcnt(8)\n\tv_mfma_f32_16x16x32_bf16 a[28:31], v[36:39], v[156:159], a[28:31]\n\tds_read_b64_tr_b16 v[24:25], v2 offset:49152\n\tds_read_b64_tr_b16 v[26:27], v2 offset:49664\n\tv_mfma_f32_16x16x32_bf16 a[36:39], v[36:39], v[160:163], a[36:39]\n\tbuffer_load_dwordx4 v10, s[12:15], s69 offen lds\n\ts_add_u32 m0, s72, 33792\n\t" \
    "v_mfma_f32_16x16x32_bf16 a[44:47], v[36:39], v[164:167], a[44:47]\n\ts_waitcnt lgkmcnt(8)\n\tv_mfma_f32_16x16x32_bf16 v[72:75], v[40:43], v[156:159], v[72:75]\n\tds_read_b64_tr_b16 v[28:29], v3 offset:49152\n\tds_read_b64_tr_b16 v[30:31], v3 offset:49664\n\tv_mfma_f32_16x16x32_bf16 v[80:83], v[40:43], v[160:163], v[80:83]\n\tv_mfma_f32_16x16x32_bf16 v[88:91], v[40:43], v[164:167], v[88:91]\n\ts_waitcnt lgkmcnt(8)\n\t" \
    "v_mfma_f32_16x16x32_bf16 v[76:79], v[44:47], v[156:159], v[76:79]\n\tds_read_b64_tr_b16 v[32:33], v0 offset:53248\n\tds_read_b64_tr_b16 v[34:35], v0 offset:53760\n\tv_mfma_f32_16x16x32_bf16 v[84:87], v[44:47], v[160:163], v[84:87]\n\tbuffer_load_dwordx4 v11, s[12:15], s69 offen lds\n\ts_mov_b32 s69, 7372800\n\ts_add_u32 m0, s72, 36864\n\tv_mfma_f32_16x16x32_bf16 v[92:95], v[44:47], v[164:167], v[92:95]\n\t" \
    "s_waitcnt vmcnt(30)\n\ts_barrier\n\ts_waitcnt lgkmcnt(8)\n\tv_mfma_f32_16x16x32_bf16 a[0:3], v[16:19], a[68:71], a[0:3]\n\tds_read_b64_tr_b16 v[36:37], v1 offset:53248\n\tds_read_b64_tr_b16 v[38:39], v1 offset:53760\n\tv_mfma_f32_16x16x32_bf16 a[8:11], v[16:19], a[72:75], a[8:11]\n\tbuffer_load_dwordx4 v10, s[12:15], s69 offen lds\n\t" \
    "s_add_u32 m0, s72, 37888\n\tv_mfma_f32_16x16x32_bf16 a[16:19], v[16:19], a[76:79], a[16:19]\n\ts_waitcnt lgkmcnt(8)\n\tv_mfma_f32_16x16x32_bf16 a[4:7], v[20:23], a[68:71], a[4:7]\n\tds_read_b64_tr_b16 v[40:41], v2 offset:53248\n\tds_read_b64_tr_b16 v[42:43], v2 offset:53760\n\tv_mfma_f32_16x16x32_bf16 a[12:15], v[20:23], a[72:75], a[12:15]\n\tbuffer_load_dwordx4 v11, s[12:15], s69 offen lds\n\t" \
    "s_add_u32 s68, s44, 811008\n\tv_mfma_f32_16x16x32_bf16 a[20:23], v[20:23], a[76:79], a[20:23]\n\ts_waitcnt lgkmcnt(8)\n\tv_mfma_f32_16x16x32_bf16 v[48:51], v[24:27], a[68:71], v[48:51]\n\tds_read_b64_tr_b16 v[44:45], v3 offset:53248\n\tds_read_b64_tr_b16 v[46:47], v3 offset:53760\n\tv_mfma_f32_16x16x32_bf16 v[56:59], v[24:27], a[72:75], v[56:59]\n\tbuffer_load_dwordx4 v[156:159], v8, s[8:11], s68 offen offset:0 sc1\n\t" \
    "v_mfma_f32_16x16x32_bf16 v[64:67], v[24:27], a[76:79], v[64:67]\n\ts_waitcnt lgkmcnt(8)\n\tv_mfma_f32_16x16x32_bf16 v[52:55], v[28:31], a[68:71], v[52:55]\n\tds_read_b64_tr_b16 v[16:17], v0 offset:57344\n\tds_read_b64_tr_b16 v[18:19], v0 offset:57856\n\tv_mfma_f32_16x16x32_bf16 v[60:63], v[28:31], a[72:75], v[60:63]\n\tv_mfma_f32_16x16x32_bf16 v[68:71], v[28:31], a[76:79], v[68:71]\n\ts_waitcnt lgkmcnt(8)\n\t" \
    "v_mfma_f32_16x16x32_bf16 a[24:27], v[32:35], a[68:71], a[24:27]\n\tds_read_b64_tr_b16 v[20:21], v1 offset:57344\n\tds_read_b64_tr_b16 v[22:23], v1 offset:57856\n\tv_mfma_f32_16x16x32_bf16 a[32:35], v[32:35], a[72:75], a[32:35]\n\tbuffer_load_dwordx4 v[160:163], v8, s[8:11], s68 offen offset:1024 sc1\n\tv_mfma_f32_16x16x32_bf16 a[40:43], v[32:35], a[76:79], a[40:43]\n\ts_waitcnt lgkmcnt(8)\n\tv_mfma_f32_16x16x32_bf16 a[28:31], v[36:39], a[68:71], a[28:31]\n\t" \
    "ds_read_b64_tr_b16 v[24:25], v2 offset:57344\n\tds_read_b64_tr_b16 v[26:27], v2 offset:57856\n\tv_mfma_f32_16x16x32_bf16 a[36:39], v[36:39], a[72:75], a[36:39]\n\tbuffer_load_dwordx4 v[164:167], v8, s[8:11], s68 offen offset:2048 sc1\n\ts_add_u32 s68, s44, 847872\n\tv_mfma_f32_16x16x32_bf16 a[44:47], v[36:39], a[76:79], a[44:47]\n\ts_waitcnt lgkmcnt(8)\n\tv_mfma_f32_16x16x32_bf16 v[72:75], v[40:43], a[68:71], v[72:75]\n\t" \
    "ds_read_b64_tr_b16 v[28:29], v3 offset:57344\n\tds_read_b64_tr_b16 v[30:31], v3 offset:57856\n\tv_mfma_f32_16x16x32_bf16 v[80:83], v[40:43], a[72:75], v[80:83]\n\tv_mfma_f32_16x16x32_bf16 v[88:91], v[40:43], a[76:79], v[88:91]\n\ts_waitcnt lgkmcnt(8)\n\tv_mfma_f32_16x16x32_bf16 v[76:79], v[44:47], a[68:71], v[76:79]\n\tds_read_b64_tr_b16 v[32:33], v0 offset:61440\n\tds_read_b64_tr_b16 v[34:35], v0 offset:61952\n\t" \
    "v_mfma_f32_16x16x32_bf16 v[84:87], v[44:47], a[72:75], v[84:87]\n\tbuffer_load_dwordx4 v[216:219], v8, s[8:11], s68 offen offset:0 sc1\n\tv_mfma_f32_16x16x32_bf16 v[92:95], v[44:47], a[76:79], v[92:95]\n\ts_waitcnt vmcnt(32)\n\ts_barrier\n\ts_waitcnt lgkmcnt(8)\n\tv_mfma_f32_16x16x32_bf16 a[0:3], v[16:19], v[168:171], a[0:3]\n\tds_read_b64_tr_b16 v[36:37], v1 offset:61440\n\t" \
    "ds_read_b64_tr_b16 v[38:39], v1 offset:61952\n\tv_mfma_f32_16x16x32_bf16 a[8:11], v[16:19], v[172:175], a[8:11]\n\tbuffer_load_dwordx4 a[48:51], v8, s[8:11], s68 offen offset:1024 sc1\n\tv_mfma_f32_16x16x32_bf16 a[16:19], v[16:19], v[176:179], a[16:19]\n\ts_waitcnt lgkmcnt(8)\n\tv_mfma_f32_16x16x32_bf16 a[4:7], v[20:23], v[168:171], a[4:7]\n\tds_read_b64_tr_b16 v[40:41], v2 offset:61440\n\tds_read_b64_tr_b16 v[42:43], v2 offset:61952\n\t" \
    "v_mfma_f32_16x16x32_bf16 a[12:15], v[20:23], v[172:175], a[12:15]\n\tbuffer_load_dwordx4 a[52:55], v8, s[8:11], s68 offen offset:2048 sc1\n\ts_mov_b32 s69, 7667712\n\ts_add_u32 m0, s72, 40960\n\tv_mfma_f32_16x16x32_bf16 a[20:23], v[20:23], v[176:179], a[20:23]\n\ts_waitcnt lgkmcnt(8)\n\tv_mfma_f32_16x16x32_bf16 v[48:51], v[24:27], v[168:171], v[48:51]\n\tds_read_b64_tr_b16 v[44:45], v3 offset:61440\n\t" \
    "ds_read_b64_tr_b16 v[46:47], v3 offset:61952\n\tv_mfma_f32_16x16x32_bf16 v[56:59], v[24:27], v[172:175], v[56:59]\n\tbuffer_load_dwordx4 v10, s[12:15], s69 offen lds\n\ts_add_u32 m0, s72, 41984\n\tv_mfma_f32_16x16x32_bf16 v[64:67], v[24:27], v[176:179], v[64:67]\n\ts_waitcnt lgkmcnt(8)\n\tv_mfma_f32_16x16x32_bf16 v[52:55], v[28:31], v[168:171], v[52:55]\n\tds_read_b64_tr_b16 v[16:17], v0 offset:0\n\t" \
    "ds_read_b64_tr_b16 v[18:19], v0 offset:512\n\tv_mfma_f32_16x16x32_bf16 v[60:63], v[28:31], v[172:175], v[60:63]\n\tbuffer_load_dwordx4 v11, s[12:15], s69 offen lds\n\ts_mov_b32 s69, 7962624\n\ts_add_u32 m0, s72, 45056\n\tv_mfma_f32_16x16x32_bf16 v[68:71], v[28:31], v[176:179], v[68:71]\n\ts_waitcnt lgkmcnt(8)\n\tv_mfma_f32_16x16x32_bf16 a[24:27], v[32:35], v[168:171], a[24:27]\n\t" \
    "ds_read_b64_tr_b16 v[20:21], v1 offset:0\n\tds_read_b64_tr_b16 v[22:23], v1 offset:512\n\tv_mfma_f32_16x16x32_bf16 a[32:35], v[32:35], v[172:175], a[32:35]\n\tv_mfma_f32_16x16x32_bf16 a[40:43], v[32:35], v[176:179], a[40:43]\n\ts_waitcnt lgkmcnt(8)\n\tv_mfma_f32_16x16x32_bf16 a[28:31], v[36:39], v[168:171], a[28:31]\n\tds_read_b64_tr_b16 v[24:25], v2 offset:0\n\tds_read_b64_tr_b16 v[26:27], v2 offset:512\n\t" \
    "v_mfma_f32_16x16x32_bf16 a[36:39], v[36:39], v[172:175], a[36:39]\n\tbuffer_load_dwordx4 v10, s[12:15], s69 offen lds\n\ts_add_u32 m0, s72, 46080\n\tv_mfma_f32_16x16x32_bf16 a[44:47], v[36:39], v[176:179], a[44:47]\n\ts_waitcnt lgkmcnt(8)\n\tv_mfma_f32_16x16x32_bf16 v[72:75], v[40:43], v[168:171], v[72:75]\n\tds_read_b64_tr_b16 v[28:29], v3 offset:0\n\tds_read_b64_tr_b16 v[30:31], v3 offset:512\n\t" \
    "v_mfma_f32_16x16x32_bf16 v[80:83], v[40:43], v[172:175], v[80:83]\n\tbuffer_load_dwordx4 v11, s[12:15], s69 offen lds\n\ts_add_u32 s68, s44, 884736\n\tv_mfma_f32_16x16x32_bf16 v[88:91], v[40:43], v[176:179], v[88:91]\n\ts_waitcnt lgkmcnt(8)\n\tv_mfma_f32_16x16x32_bf16 v[76:79], v[44:47], v[168:171], v[76:79]\n\tds_read_b64_tr_b16 v[32:33], v0 offset:4096\n\tds_read_b64_tr_b16 v[34:35], v0 offset:4608\n\t" \
    "v_mfma_f32_16x16x32_bf16 v[84:87], v[44:47], v[172:175], v[84:87]\n\tv_mfma_f32_16x16x32_bf16 v[92:95], v[44:47], v[176:179], v[92:95]\n\ts_waitcnt vmcnt(31)\n\ts_barrier\n\ts_waitcnt lgkmcnt(8)\n\tv_mfma_f32_16x16x32_bf16 a[0:3], v[16:19], v[180:183], a[0:3]\n\tds_read_b64_tr_b16 v[36:37], v1 offset:4096\n\tds_read_b64_tr_b16 v[38:39], v1 offset:4608\n\t" \
    "v_mfma_f32_16x16x32_bf16 a[8:11], v[16:19], v[184:187], a[8:11]\n\tbuffer_load_dwordx4 a[56:59], v8, s[8:11], s68 offen offset:0 sc1\n\tv_mfma_f32_16x16x32_bf16 a[16:19], v[16:19], v[188:191], a[16:19]\n\ts_waitcnt lgkmcnt(8)\n\tv_mfma_f32_16x16x32_bf16 a[4:7], v[20:23], v[180:183], a[4:7]\n\tds_read_b64_tr_b16 v[40:41], v2 offset:4096\n\tds_read_b64_tr_b16 v[42:43], v2 offset:4608\n\tv_mfma_f32_16x16x32_bf16 a[12:15], v[20:23], v[184:187], a[12:15]\n\t" \
    "buffer_load_dwordx4 a[60:63], v8, s[8:11], s68 offen offset:1024 sc1\n\tv_mfma_f32_16x16x32_bf16 a[20:23], v[20:23], v[188:191], a[20:23]\n\ts_waitcnt lgkmcnt(8)\n\tv_mfma_f32_16x16x32_bf16 v[48:51], v[24:27], v[180:183], v[48:51]\n\tds_read_b64_tr_b16 v[44:45], v3 offset:4096\n\tds_read_b64_tr_b16 v[46:47], v3 offset:4608\n\tv_mfma_f32_16x16x32_bf16 v[56:59], v[24:27], v[184:187], v[56:59]\n\tbuffer_load_dwordx4 a[64:67], v8, s[8:11], s68 offen offset:2048 sc1\n\t" \
    "s_add_u32 s68, s44, 921600\n\tv_mfma_f32_16x16x32_bf16 v[64:67], v[24:27], v[188:191], v[64:67]\n\ts_waitcnt lgkmcnt(8)\n\tv_mfma_f32_16x16x32_bf16 v[52:55], v[28:31], v[180:183], v[52:55]\n\tds_read_b64_tr_b16 v[16:17], v0 offset:8192\n\tds_read_b64_tr_b16 v[18:19], v0 offset:8704\n\tv_mfma_f32_16x16x32_bf16 v[60:63], v[28:31], v[184:187], v[60:63]\n\tbuffer_load_dwordx4 v[168:171], v8, s[8:11], s68 offen offset:0 sc1\n\t" \
    "v_mfma_f32_16x16x32_bf16 v[68:71], v[28:31], v[188:191], v[68:71]\n\ts_waitcnt lgkmcnt(8)\n\tv_mfma_f32_16x16x32_bf16 a[0:3], v[32:35], v[192:195], a[0:3]\n\tds_read_b64_tr_b16 v[20:21], v1 offset:8192\n\tds_read_b64_tr_b16 v[22:23], v1 offset:8704\n\tv_mfma_f32_16x16x32_bf16 a[8:11], v[32:35], v[196:199], a[8:11]\n\tbuffer_load_dwordx4 v[172:175], v8, s[8:11], s68 offen offset:1024 sc1\n\tv_mfma_f32_16x16x32_bf16 a[16:19], v[32:35], v[200:203], a[16:19]\n\t" \
    "s_waitcnt lgkmcnt(8)\n\tv_mfma_f32_16x16x32_bf16 a[4:7], v[36:39], v[192:195], a[4:7]\n\tds_read_b64_tr_b16 v[24:25], v2 offset:8192\n\tds_read_b64_tr_b16 v[26:27], v2 offset:8704\n\tv_mfma_f32_16x16x32_bf16 a[12:15], v[36:39], v[196:199], a[12:15]\n\tv_mfma_f32_16x16x32_bf16 a[20:23], v[36:39], v[200:203], a[20:23]\n\ts_waitcnt lgkmcnt(8)\n\tv_mfma_f32_16x16x32_bf16 v[48:51], v[40:43], v[192:195], v[48:51]\n\t" \
    "ds_read_b64_tr_b16 v[28:29], v3 offset:8192\n\tds_read_b64_tr_b16 v[30:31], v3 offset:8704\n\tv_mfma_f32_16x16x32_bf16 v[56:59], v[40:43], v[196:199], v[56:59]\n\tbuffer_load_dwordx4 v[176:179], v8, s[8:11], s68 offen offset:2048 sc1\n\ts_mov_b32 s69, 8257536\n\ts_add_u32 m0, s72, 49152\n\tv_mfma_f32_16x16x32_bf16 v[64:67], v[40:43], v[200:203], v[64:67]\n\ts_waitcnt lgkmcnt(8)\n\t" \
    "v_mfma_f32_16x16x32_bf16 v[52:55], v[44:47], v[192:195], v[52:55]\n\tds_read_b64_tr_b16 v[32:33], v0 offset:12288\n\tds_read_b64_tr_b16 v[34:35], v0 offset:12800\n\tv_mfma_f32_16x16x32_bf16 v[60:63], v[44:47], v[196:199], v[60:63]\n\tbuffer_load_dwordx4 v10, s[12:15], s69 offen lds\n\ts_add_u32 m0, s72, 50176\n\tv_mfma_f32_16x16x32_bf16 v[68:71], v[44:47], v[200:203], v[68:71]\n\ts_waitcnt vmcnt(31)\n\t" \
    "s_barrier\n\ts_waitcnt lgkmcnt(8)\n\tv_mfma_f32_16x16x32_bf16 a[0:3], v[16:19], v[120:123], a[0:3]\n\tds_read_b64_tr_b16 v[36:37], v1 offset:12288\n\tds_read_b64_tr_b16 v[38:39], v1 offset:12800\n\tv_mfma_f32_16x16x32_bf16 a[8:11], v[16:19], v[124:127], a[8:11]\n\tbuffer_load_dwordx4 v11, s[12:15], s69 offen lds\n\ts_mov_b32 s69, 8552448\n\t" \
    "s_add_u32 m0, s72, 53248\n\tv_mfma_f32_16x16x32_bf16 a[16:19], v[16:19], v[128:131], a[16:19]\n\ts_waitcnt lgkmcnt(8)\n\tv_mfma_f32_16x16x32_bf16 a[4:7], v[20:23], v[120:123], a[4:7]\n\tds_read_b64_tr_b16 v[40:41], v2 offset:12288\n\tds_read_b64_tr_b16 v[42:43], v2 offset:12800\n\tv_mfma_f32_16x16x32_bf16 a[12:15], v[20:23], v[124:127], a[12:15]\n\tbuffer_load_dwordx4 v10, s[12:15], s69 offen lds\n\t" \
    "s_add_u32 m0, s72, 54272\n\tv_mfma_f32_16x16x32_bf16 a[20:23], v[20:23], v[128:131], a[20:23]\n\ts_waitcnt lgkmcnt(8)\n\tv_mfma_f32_16x16x32_bf16 v[48:51], v[24:27], v[120:123], v[48:51]\n\tds_read_b64_tr_b16 v[44:45], v3 offset:12288\n\tds_read_b64_tr_b16 v[46:47], v3 offset:12800\n\tv_mfma_f32_16x16x32_bf16 v[56:59], v[24:27], v[124:127], v[56:59]\n\tv_mfma_f32_16x16x32_bf16 v[64:67], v[24:27], v[128:131], v[64:67]\n\t" \
    "s_waitcnt lgkmcnt(8)\n\tv_mfma_f32_16x16x32_bf16 v[52:55], v[28:31], v[120:123], v[52:55]\n\tds_read_b64_tr_b16 v[16:17], v0 offset:16384\n\tds_read_b64_tr_b16 v[18:19], v0 offset:16896\n\tv_mfma_f32_16x16x32_bf16 v[60:63], v[28:31], v[124:127], v[60:63]\n\tbuffer_load_dwordx4 v11, s[12:15], s69 offen lds\n\ts_mov_b32 s69, 8847360\n\ts_add_u32 m0, s72, 57344\n\t" \
    "v_mfma_f32_16x16x32_bf16 v[68:71], v[28:31], v[128:131], v[68:71]\n\ts_waitcnt lgkmcnt(8)\n\tv_mfma_f32_16x16x32_bf16 a[0:3], v[32:35], v[132:135], a[0:3]\n\tds_read_b64_tr_b16 v[20:21], v1 offset:16384\n\tds_read_b64_tr_b16 v[22:23], v1 offset:16896\n\tv_mfma_f32_16x16x32_bf16 a[8:11], v[32:35], v[136:139], a[8:11]\n\tbuffer_load_dwordx4 v10, s[12:15], s69 offen lds\n\ts_add_u32 m0, s72, 58368\n\t" \
    "v_mfma_f32_16x16x32_bf16 a[16:19], v[32:35], v[140:143], a[16:19]\n\ts_waitcnt lgkmcnt(8)\n\tv_mfma_f32_16x16x32_bf16 a[4:7], v[36:39], v[132:135], a[4:7]\n\tds_read_b64_tr_b16 v[24:25], v2 offset:16384\n\tds_read_b64_tr_b16 v[26:27], v2 offset:16896\n\tv_mfma_f32_16x16x32_bf16 a[12:15], v[36:39], v[136:139], a[12:15]\n\tv_mfma_f32_16x16x32_bf16 a[20:23], v[36:39], v[140:143], a[20:23]\n\ts_waitcnt lgkmcnt(8)\n\t" \
    "v_mfma_f32_16x16x32_bf16 v[48:51], v[40:43], v[132:135], v[48:51]\n\tds_read_b64_tr_b16 v[28:29], v3 offset:16384\n\tds_read_b64_tr_b16 v[30:31], v3 offset:16896\n\tv_mfma_f32_16x16x32_bf16 v[56:59], v[40:43], v[136:139], v[56:59]\n\tbuffer_load_dwordx4 v11, s[12:15], s69 offen lds\n\ts_add_u32 s68, s44, 958464\n\tv_mfma_f32_16x16x32_bf16 v[64:67], v[40:43], v[140:143], v[64:67]\n\ts_waitcnt lgkmcnt(8)\n\t" \
    "v_mfma_f32_16x16x32_bf16 v[52:55], v[44:47], v[132:135], v[52:55]\n\tds_read_b64_tr_b16 v[32:33], v0 offset:20480\n\tds_read_b64_tr_b16 v[34:35], v0 offset:20992\n\tv_mfma_f32_16x16x32_bf16 v[60:63], v[44:47], v[136:139], v[60:63]\n\tbuffer_load_dwordx4 a[68:71], v8, s[8:11], s68 offen offset:0 sc1\n\tv_mfma_f32_16x16x32_bf16 v[68:71], v[44:47], v[140:143], v[68:71]\n\ts_waitcnt vmcnt(33)\n\ts_barrier\n\t" \
    "s_waitcnt vmcnt(30) lgkmcnt(8)\n\tv_mfma_f32_16x16x32_bf16 a[0:3], v[16:19], v[144:147], a[0:3]\n\tds_read_b64_tr_b16 v[36:37], v1 offset:20480\n\tds_read_b64_tr_b16 v[38:39], v1 offset:20992\n\tv_mfma_f32_16x16x32_bf16 a[8:11], v[16:19], v[148:151], a[8:11]\n\tbuffer_load_dwordx4 a[72:75], v8, s[8:11], s68 offen offset:1024 sc1\n\tv_mfma_f32_16x16x32_bf16 a[16:19], v[16:19], v[152:155], a[16:19]\n\ts_waitcnt lgkmcnt(8)\n\t" \
    "v_mfma_f32_16x16x32_bf16 a[4:7], v[20:23], v[144:147], a[4:7]\n\tds_read_b64_tr_b16 v[40:41], v2 offset:20480\n\tds_read_b64_tr_b16 v[42:43], v2 offset:20992\n\tv_mfma_f32_16x16x32_bf16 a[12:15], v[20:23], v[148:151], a[12:15]\n\tbuffer_load_dwordx4 a[76:79], v8, s[8:11], s68 offen offset:2048 sc1\n\ts_add_u32 s68, s44, 995328\n\tv_mfma_f32_16x16x32_bf16 a[20:23], v[20:23], v[152:155], a[20:23]\n\ts_waitcnt lgkmcnt(8)\n\t" \
    "v_mfma_f32_16x16x32_bf16 v[48:51], v[24:27], v[144:147], v[48:51]\n\tds_read_b64_tr_b16 v[44:45], v3 offset:20480\n\tds_read_b64_tr_b16 v[46:47], v3 offset:20992\n\tv_mfma_f32_16x16x32_bf16 v[56:59], v[24:27], v[148:151], v[56:59]\n\tbuffer_load_dwordx4 a[80:83], v8, s[8:11], s68 offen offset:0 sc1\n\tv_mfma_f32_16x16x32_bf16 v[64:67], v[24:27], v[152:155], v[64:67]\n\ts_waitcnt lgkmcnt(8)\n\tv_mfma_f32_16x16x32_bf16 v[52:55], v[28:31], v[144:147], v[52:55]\n\t" \
    "ds_read_b64_tr_b16 v[16:17], v0 offset:24576\n\tds_read_b64_tr_b16 v[18:19], v0 offset:25088\n\tv_mfma_f32_16x16x32_bf16 v[60:63], v[28:31], v[148:151], v[60:63]\n\tv_mfma_f32_16x16x32_bf16 v[68:71], v[28:31], v[152:155], v[68:71]\n\ts_waitcnt vmcnt(30) lgkmcnt(8)\n\tv_mfma_f32_16x16x32_bf16 a[0:3], v[32:35], v[204:207], a[0:3]\n\tds_read_b64_tr_b16 v[20:21], v1 offset:24576\n\tds_read_b64_tr_b16 v[22:23], v1 offset:25088\n\t" \
    "v_mfma_f32_16x16x32_bf16 a[8:11], v[32:35], v[208:211], a[8:11]\n\tbuffer_load_dwordx4 a[84:87], v8, s[8:11], s68 offen offset:1024 sc1\n\tv_mfma_f32_16x16x32_bf16 a[16:19], v[32:35], v[212:215], a[16:19]\n\ts_waitcnt lgkmcnt(8)\n\tv_mfma_f32_16x16x32_bf16 a[4:7], v[36:39], v[204:207], a[4:7]\n\tds_read_b64_tr_b16 v[24:25], v2 offset:24576\n\tds_read_b64_tr_b16 v[26:27], v2 offset:25088\n\tv_mfma_f32_16x16x32_bf16 a[12:15], v[36:39], v[208:211], a[12:15]\n\t" \
    "buffer_load_dwordx4 a[88:91], v8, s[8:11], s68 offen offset:2048 sc1\n\ts_mov_b32 s69, 9142272\n\ts_add_u32 m0, s72, 61440\n\tv_mfma_f32_16x16x32_bf16 a[20:23], v[36:39], v[212:215], a[20:23]\n\ts_waitcnt lgkmcnt(8)\n\tv_mfma_f32_16x16x32_bf16 v[48:51], v[40:43], v[204:207], v[48:51]\n\tds_read_b64_tr_b16 v[28:29], v3 offset:24576\n\tds_read_b64_tr_b16 v[30:31], v3 offset:25088\n\t" \
    "v_mfma_f32_16x16x32_bf16 v[56:59], v[40:43], v[208:211], v[56:59]\n\tv_mfma_f32_16x16x32_bf16 v[64:67], v[40:43], v[212:215], v[64:67]\n\ts_waitcnt lgkmcnt(8)\n\tv_mfma_f32_16x16x32_bf16 v[52:55], v[44:47], v[204:207], v[52:55]\n\tds_read_b64_tr_b16 v[32:33], v0 offset:28672\n\tds_read_b64_tr_b16 v[34:35], v0 offset:29184\n\tv_mfma_f32_16x16x32_bf16 v[60:63], v[44:47], v[208:211], v[60:63]\n\tbuffer_load_dwordx4 v10, s[12:15], s69 offen lds\n\t" \
    "s_add_u32 m0, s72, 62464\n\tv_mfma_f32_16x16x32_bf16 v[68:71], v[44:47], v[212:215], v[68:71]\n\ts_waitcnt vmcnt(29)\n\ts_barrier\n\ts_waitcnt vmcnt(26) lgkmcnt(8)\n\tv_mfma_f32_16x16x32_bf16 a[0:3], v[16:19], v[156:159], a[0:3]\n\tds_read_b64_tr_b16 v[36:37], v1 offset:28672\n\tds_read_b64_tr_b16 v[38:39], v1 offset:29184\n\t" \
    "v_mfma_f32_16x16x32_bf16 a[8:11], v[16:19], v[160:163], a[8:11]\n\tbuffer_load_dwordx4 v11, s[12:15], s69 offen lds\n\ts_add_u32 s68, s44, 1032192\n\tv_mfma_f32_16x16x32_bf16 a[16:19], v[16:19], v[164:167], a[16:19]\n\ts_waitcnt lgkmcnt(8)\n\tv_mfma_f32_16x16x32_bf16 a[4:7], v[20:23], v[156:159], a[4:7]\n\tds_read_b64_tr_b16 v[40:41], v2 offset:28672\n\tds_read_b64_tr_b16 v[42:43], v2 offset:29184\n\t" \
    "v_mfma_f32_16x16x32_bf16 a[12:15], v[20:23], v[160:163], a[12:15]\n\tbuffer_load_dwordx4 a[92:95], v8, s[8:11], s68 offen offset:0 sc1\n\tv_mfma_f32_16x16x32_bf16 a[20:23], v[20:23], v[164:167], a[20:23]\n\ts_waitcnt lgkmcnt(8)\n\tv_mfma_f32_16x16x32_bf16 v[48:51], v[24:27], v[156:159], v[48:51]\n\tds_read_b64_tr_b16 v[44:45], v3 offset:28672\n\tds_read_b64_tr_b16 v[46:47], v3 offset:29184\n\tv_mfma_f32_16x16x32_bf16 v[56:59], v[24:27], v[160:163], v[56:59]\n\t" \
    "buffer_load_dwordx4 a[96:99], v8, s[8:11], s68 offen offset:1024 sc1\n\tv_mfma_f32_16x16x32_bf16 v[64:67], v[24:27], v[164:167], v[64:67]\n\ts_waitcnt lgkmcnt(8)\n\tv_mfma_f32_16x16x32_bf16 v[52:55], v[28:31], v[156:159], v[52:55]\n\tds_read_b64_tr_b16 v[16:17], v0 offset:32768\n\tds_read_b64_tr_b16 v[18:19], v0 offset:33280\n\tv_mfma_f32_16x16x32_bf16 v[60:63], v[28:31], v[160:163], v[60:63]\n\tbuffer_load_dwordx4 a[100:103], v8, s[8:11], s68 offen offset:2048 sc1\n\t" \
    "s_add_u32 s68, s44, 1069056\n\tv_mfma_f32_16x16x32_bf16 v[68:71], v[28:31], v[164:167], v[68:71]\n\ts_waitcnt vmcnt(27) lgkmcnt(8)\n\tv_mfma_f32_16x16x32_bf16 a[0:3], v[32:35], v[216:219], a[0:3]\n\tds_read_b64_tr_b16 v[20:21], v1 offset:32768\n\tds_read_b64_tr_b16 v[22:23], v1 offset:33280\n\tv_mfma_f32_16x16x32_bf16 a[8:11], v[32:35], a[48:51], a[8:11]\n\tv_mfma_f32_16x16x32_bf16 a[16:19], v[32:35], a[52:55], a[16:19]\n\t" \
    "s_waitcnt lgkmcnt(8)\n\tv_mfma_f32_16x16x32_bf16 a[4:7], v[36:39], v[216:219], a[4:7]\n\tds_read_b64_tr_b16 v[24:25], v2 offset:32768\n\tds_read_b64_tr_b16 v[26:27], v2 offset:33280\n\tv_mfma_f32_16x16x32_bf16 a[12:15], v[36:39], a[48:51], a[12:15]\n\tbuffer_load_dwordx4 a[104:107], v8, s[8:11], s68 offen offset:0 sc1\n\tv_mfma_f32_16x16x32_bf16 a[20:23], v[36:39], a[52:55], a[20:23]\n\ts_waitcnt lgkmcnt(8)\n\t" \
    "v_mfma_f32_16x16x32_bf16 v[48:51], v[40:43], v[216:219], v[48:51]\n\tds_read_b64_tr_b16 v[28:29], v3 offset:32768\n\tds_read_b64_tr_b16 v[30:31], v3 offset:33280\n\tv_mfma_f32_16x16x32_bf16 v[56:59], v[40:43], a[48:51], v[56:59]\n\tbuffer_load_dwordx4 a[108:111], v8, s[8:11], s68 offen offset:1024 sc1\n\tv_mfma_f32_16x16x32_bf16 v[64:67], v[40:43], a[52:55], v[64:67]\n\ts_waitcnt lgkmcnt(8)\n\tv_mfma_f32_16x16x32_bf16 v[52:55], v[44:47], v[216:219], v[52:55]\n\t" \
    "ds_read_b64_tr_b16 v[32:33], v0 offset:36864\n\tds_read_b64_tr_b16 v[34:35], v0 offset:37376\n\tv_mfma_f32_16x16x32_bf16 v[60:63], v[44:47], a[48:51], v[60:63]\n\tv_mfma_f32_16x16x32_bf16 v[68:71], v[44:47], a[52:55], v[68:71]\n\ts_waitcnt vmcnt(25)\n\ts_barrier\n\ts_waitcnt vmcnt(22) lgkmcnt(8)\n\tv_mfma_f32_16x16x32_bf16 a[0:3], v[16:19], a[56:59], a[0:3]\n\t" \
    "ds_read_b64_tr_b16 v[36:37], v1 offset:36864\n\tds_read_b64_tr_b16 v[38:39], v1 offset:37376\n\tv_mfma_f32_16x16x32_bf16 a[8:11], v[16:19], a[60:63], a[8:11]\n\tbuffer_load_dwordx4 a[112:115], v8, s[8:11], s68 offen offset:2048 sc1\n\ts_add_u32 s68, s44, 1105920\n\tv_mfma_f32_16x16x32_bf16 a[16:19], v[16:19], a[64:67], a[16:19]\n\ts_waitcnt lgkmcnt(8)\n\tv_mfma_f32_16x16x32_bf16 a[4:7], v[20:23], a[56:59], a[4:7]\n\t" \
    "ds_read_b64_tr_b16 v[40:41], v2 offset:36864\n\tds_read_b64_tr_b16 v[42:43], v2 offset:37376\n\tv_mfma_f32_16x16x32_bf16 a[12:15], v[20:23], a[60:63], a[12:15]\n\tbuffer_load_dwordx4 a[116:119], v8, s[8:11], s68 offen offset:0 sc1\n\tv_mfma_f32_16x16x32_bf16 a[20:23], v[20:23], a[64:67], a[20:23]\n\ts_waitcnt lgkmcnt(8)\n\tv_mfma_f32_16x16x32_bf16 v[48:51], v[24:27], a[56:59], v[48:51]\n\tds_read_b64_tr_b16 v[44:45], v3 offset:36864\n\t" \
    "ds_read_b64_tr_b16 v[46:47], v3 offset:37376\n\tv_mfma_f32_16x16x32_bf16 v[56:59], v[24:27], a[60:63], v[56:59]\n\tbuffer_load_dwordx4 a[120:123], v8, s[8:11], s68 offen offset:1024 sc1\n\tv_mfma_f32_16x16x32_bf16 v[64:67], v[24:27], a[64:67], v[64:67]\n\ts_waitcnt lgkmcnt(8)\n\tv_mfma_f32_16x16x32_bf16 v[52:55], v[28:31], a[56:59], v[52:55]\n\tds_read_b64_tr_b16 v[16:17], v0 offset:40960\n\tds_read_b64_tr_b16 v[18:19], v0 offset:41472\n\t" \
    "v_mfma_f32_16x16x32_bf16 v[60:63], v[28:31], a[60:63], v[60:63]\n\tbuffer_load_dwordx4 a[124:127], v8, s[8:11], s68 offen offset:2048 sc1\n\ts_add_u32 s68, s44, 1142784\n\tv_mfma_f32_16x16x32_bf16 v[68:71], v[28:31], a[64:67], v[68:71]\n\ts_waitcnt vmcnt(23) lgkmcnt(8)\n\tv_mfma_f32_16x16x32_bf16 a[0:3], v[32:35], v[168:171], a[0:3]\n\tds_read_b64_tr_b16 v[20:21], v1 offset:40960\n\tds_read_b64_tr_b16 v[22:23], v1 offset:41472\n\t" \
    "v_mfma_f32_16x16x32_bf16 a[8:11], v[32:35], v[172:175], a[8:11]\n\tv_mfma_f32_16x16x32_bf16 a[16:19], v[32:35], v[176:179], a[16:19]\n\ts_waitcnt lgkmcnt(8)\n\tv_mfma_f32_16x16x32_bf16 a[4:7], v[36:39], v[168:171], a[4:7]\n\tds_read_b64_tr_b16 v[24:25], v2 offset:40960\n\tds_read_b64_tr_b16 v[26:27], v2 offset:41472\n\tv_mfma_f32_16x16x32_bf16 a[12:15], v[36:39], v[172:175], a[12:15]\n\tbuffer_load_dwordx4 a[128:131], v8, s[8:11], s68 offen offset:0 sc1\n\t" \
    "v_mfma_f32_16x16x32_bf16 a[20:23], v[36:39], v[176:179], a[20:23]\n\ts_waitcnt lgkmcnt(8)\n\tv_mfma_f32_16x16x32_bf16 v[48:51], v[40:43], v[168:171], v[48:51]\n\tds_read_b64_tr_b16 v[28:29], v3 offset:40960\n\tds_read_b64_tr_b16 v[30:31], v3 offset:41472\n\tv_mfma_f32_16x16x32_bf16 v[56:59], v[40:43], v[172:175], v[56:59]\n\tbuffer_load_dwordx4 a[132:135], v8, s[8:11], s68 offen offset:1024 sc1\n\tv_mfma_f32_16x16x32_bf16 v[64:67], v[40:43], v[176:179], v[64:67]\n\t" \
    "s_waitcnt lgkmcnt(8)\n\tv_mfma_f32_16x16x32_bf16 v[52:55], v[44:47], v[168:171], v[52:55]\n\tds_read_b64_tr_b16 v[32:33], v0 offset:45056\n\tds_read_b64_tr_b16 v[34:35], v0 offset:45568\n\tv_mfma_f32_16x16x32_bf16 v[60:63], v[44:47], v[172:175], v[60:63]\n\tbuffer_load_dwordx4 a[136:139], v8, s[8:11], s68 offen offset:2048 sc1\n\ts_mov_b32 s69, 4718720\n\ts_add_u32 m0, s72, 0\n\t" \
    "v_mfma_f32_16x16x32_bf16 v[68:71], v[44:47], v[176:179], v[68:71]\n\ts_waitcnt vmcnt(20)\n\ts_barrier\n\ts_waitcnt vmcnt(17) lgkmcnt(8)\n\tv_mfma_f32_16x16x32_bf16 a[0:3], v[16:19], a[68:71], a[0:3]\n\tds_read_b64_tr_b16 v[36:37], v1 offset:45056\n\tds_read_b64_tr_b16 v[38:39], v1 offset:45568\n\tv_mfma_f32_16x16x32_bf16 a[8:11], v[16:19], a[72:75], a[8:11]\n\t" \
    "buffer_load_dwordx4 v10, s[12:15], s69 offen lds\n\ts_add_u32 m0, s72, 1024\n\tv_mfma_f32_16x16x32_bf16 a[16:19], v[16:19], a[76:79], a[16:19]\n\ts_waitcnt lgkmcnt(8)\n\tv_mfma_f32_16x16x32_bf16 a[4:7], v[20:23], a[68:71], a[4:7]\n\tds_read_b64_tr_b16 v[40:41], v2 offset:45056\n\tds_read_b64_tr_b16 v[42:43], v2 offset:45568\n\tv_mfma_f32_16x16x32_bf16 a[12:15], v[20:23], a[72:75], a[12:15]\n\t" \
    "buffer_load_dwordx4 v11, s[12:15], s69 offen lds\n\ts_mov_b32 s69, 5013632\n\ts_add_u32 m0, s72, 4096\n\tv_mfma_f32_16x16x32_bf16 a[20:23], v[20:23], a[76:79], a[20:23]\n\ts_waitcnt lgkmcnt(8)\n\tv_mfma_f32_16x16x32_bf16 v[48:51], v[24:27], a[68:71], v[48:51]\n\tds_read_b64_tr_b16 v[44:45], v3 offset:45056\n\tds_read_b64_tr_b16 v[46:47], v3 offset:45568\n\t" \
    "v_mfma_f32_16x16x32_bf16 v[56:59], v[24:27], a[72:75], v[56:59]\n\tv_mfma_f32_16x16x32_bf16 v[64:67], v[24:27], a[76:79], v[64:67]\n\ts_waitcnt lgkmcnt(8)\n\tv_mfma_f32_16x16x32_bf16 v[52:55], v[28:31], a[68:71], v[52:55]\n\tds_read_b64_tr_b16 v[16:17], v0 offset:49152\n\tds_read_b64_tr_b16 v[18:19], v0 offset:49664\n\tv_mfma_f32_16x16x32_bf16 v[60:63], v[28:31], a[72:75], v[60:63]\n\tbuffer_load_dwordx4 v10, s[12:15], s69 offen lds\n\t" \
    "s_add_u32 m0, s72, 5120\n\tv_mfma_f32_16x16x32_bf16 v[68:71], v[28:31], a[76:79], v[68:71]\n\ts_waitcnt vmcnt(17) lgkmcnt(8)\n\tv_mfma_f32_16x16x32_bf16 a[0:3], v[32:35], a[80:83], a[0:3]\n\tds_read_b64_tr_b16 v[20:21], v1 offset:49152\n\tds_read_b64_tr_b16 v[22:23], v1 offset:49664\n\tv_mfma_f32_16x16x32_bf16 a[8:11], v[32:35], a[84:87], a[8:11]\n\tbuffer_load_dwordx4 v11, s[12:15], s69 offen lds\n\t" \
    "s_mov_b32 s69, 5308544\n\ts_add_u32 m0, s72, 8192\n\tv_mfma_f32_16x16x32_bf16 a[16:19], v[32:35], a[88:91], a[16:19]\n\ts_waitcnt lgkmcnt(8)\n\tv_mfma_f32_16x16x32_bf16 a[4:7], v[36:39], a[80:83], a[4:7]\n\tds_read_b64_tr_b16 v[24:25], v0 offset:53248\n\tds_read_b64_tr_b16 v[26:27], v0 offset:53760\n\tv_mfma_f32_16x16x32_bf16 a[12:15], v[36:39], a[84:87], a[12:15]\n\t" \
    "v_mfma_f32_16x16x32_bf16 a[20:23], v[36:39], a[88:91], a[20:23]\n\ts_waitcnt lgkmcnt(8)\n\tv_mfma_f32_16x16x32_bf16 v[48:51], v[40:43], a[80:83], v[48:51]\n\tds_read_b64_tr_b16 v[28:29], v1 offset:53248\n\tds_read_b64_tr_b16 v[30:31], v1 offset:53760\n\tv_mfma_f32_16x16x32_bf16 v[56:59], v[40:43], a[84:87], v[56:59]\n\tbuffer_load_dwordx4 v10, s[12:15], s69 offen lds\n\ts_add_u32 m0, s72, 9216\n\t" \
    "v_mfma_f32_16x16x32_bf16 v[64:67], v[40:43], a[88:91], v[64:67]\n\ts_waitcnt lgkmcnt(8)\n\tv_mfma_f32_16x16x32_bf16 v[52:55], v[44:47], a[80:83], v[52:55]\n\tds_read_b64_tr_b16 v[32:33], v0 offset:57344\n\tds_read_b64_tr_b16 v[34:35], v0 offset:57856\n\tv_mfma_f32_16x16x32_bf16 v[60:63], v[44:47], a[84:87], v[60:63]\n\tbuffer_load_dwordx4 v11, s[12:15], s69 offen lds\n\ts_mov_b32 s69, 5603456\n\t" \
    "s_add_u32 m0, s72, 12288\n\tv_mfma_f32_16x16x32_bf16 v[68:71], v[44:47], a[88:91], v[68:71]\n\ts_waitcnt vmcnt(18)\n\ts_barrier\n\ts_waitcnt vmcnt(15) lgkmcnt(8)\n\tv_mfma_f32_16x16x32_bf16 a[0:3], v[16:19], a[92:95], a[0:3]\n\tds_read_b64_tr_b16 v[36:37], v1 offset:57344\n\tds_read_b64_tr_b16 v[38:39], v1 offset:57856\n\t" \
    "v_mfma_f32_16x16x32_bf16 a[8:11], v[16:19], a[96:99], a[8:11]\n\tbuffer_load_dwordx4 v10, s[12:15], s69 offen lds\n\ts_add_u32 m0, s72, 13312\n\tv_mfma_f32_16x16x32_bf16 a[16:19], v[16:19], a[100:103], a[16:19]\n\ts_waitcnt lgkmcnt(8)\n\tv_mfma_f32_16x16x32_bf16 a[4:7], v[20:23], a[92:95], a[4:7]\n\tds_read_b64_tr_b16 v[40:41], v0 offset:61440\n\tds_read_b64_tr_b16 v[42:43], v0 offset:61952\n\t" \
    "v_mfma_f32_16x16x32_bf16 a[12:15], v[20:23], a[96:99], a[12:15]\n\tbuffer_load_dwordx4 v11, s[12:15], s69 offen lds\n\ts_mov_b32 s69, 5898368\n\ts_add_u32 m0, s72, 16384\n\tv_mfma_f32_16x16x32_bf16 a[20:23], v[20:23], a[100:103], a[20:23]\n\ts_waitcnt vmcnt(14) lgkmcnt(8)\n\tv_mfma_f32_16x16x32_bf16 a[0:3], v[24:27], a[104:107], a[0:3]\n\tds_read_b64_tr_b16 v[44:45], v1 offset:61440\n\t" \
    "ds_read_b64_tr_b16 v[46:47], v1 offset:61952\n\tv_mfma_f32_16x16x32_bf16 a[8:11], v[24:27], a[108:111], a[8:11]\n\tbuffer_load_dwordx4 v10, s[12:15], s69 offen lds\n\ts_add_u32 m0, s72, 17408\n\tv_mfma_f32_16x16x32_bf16 a[16:19], v[24:27], a[112:115], a[16:19]\n\ts_waitcnt lgkmcnt(8)\n\tv_mfma_f32_16x16x32_bf16 a[4:7], v[28:31], a[104:107], a[4:7]\n\tds_read_b64_tr_b16 v[16:17], v2 offset:49152\n\t" \
    "ds_read_b64_tr_b16 v[18:19], v2 offset:49664\n\tv_mfma_f32_16x16x32_bf16 a[12:15], v[28:31], a[108:111], a[12:15]\n\tv_mfma_f32_16x16x32_bf16 a[20:23], v[28:31], a[112:115], a[20:23]\n\ts_waitcnt vmcnt(12) lgkmcnt(8)\n\tv_mfma_f32_16x16x32_bf16 a[0:3], v[32:35], a[116:119], a[0:3]\n\tds_read_b64_tr_b16 v[20:21], v3 offset:49152\n\tds_read_b64_tr_b16 v[22:23], v3 offset:49664\n\tv_mfma_f32_16x16x32_bf16 a[8:11], v[32:35], a[120:123], a[8:11]\n\t" \
    "buffer_load_dwordx4 v11, s[12:15], s69 offen lds\n\tv_mfma_f32_16x16x32_bf16 a[16:19], v[32:35], a[124:127], a[16:19]\n\ts_waitcnt lgkmcnt(8)\n\tv_mfma_f32_16x16x32_bf16 a[4:7], v[36:39], a[116:119], a[4:7]\n\tds_read_b64_tr_b16 v[24:25], v2 offset:53248\n\tds_read_b64_tr_b16 v[26:27], v2 offset:53760\n\tv_mfma_f32_16x16x32_bf16 a[12:15], v[36:39], a[120:123], a[12:15]\n\tbuffer_store_dwordx2 v[248:249], off, s[8:11], s60 sc1\n\t" \
    "v_mfma_f32_16x16x32_bf16 a[20:23], v[36:39], a[124:127], a[20:23]\n\ts_waitcnt vmcnt(11) lgkmcnt(8)\n\tv_mfma_f32_16x16x32_bf16 a[0:3], v[40:43], a[128:131], a[0:3]\n\tds_read_b64_tr_b16 v[28:29], v3 offset:53248\n\tds_read_b64_tr_b16 v[30:31], v3 offset:53760\n\tv_mfma_f32_16x16x32_bf16 a[8:11], v[40:43], a[132:135], a[8:11]\n\tv_mfma_f32_16x16x32_bf16 a[16:19], v[40:43], a[136:139], a[16:19]\n\ts_waitcnt lgkmcnt(8)\n\t" \
    "v_mfma_f32_16x16x32_bf16 a[4:7], v[44:47], a[128:131], a[4:7]\n\tds_read_b64_tr_b16 v[32:33], v2 offset:57344\n\tds_read_b64_tr_b16 v[34:35], v2 offset:57856\n\tv_mfma_f32_16x16x32_bf16 a[12:15], v[44:47], a[132:135], a[12:15]\n\tv_mfma_f32_16x16x32_bf16 a[20:23], v[44:47], a[136:139], a[20:23]\n\ts_waitcnt vmcnt(9)\n\ts_barrier\n\ts_waitcnt lgkmcnt(8)\n\t" \
    "v_mfma_f32_16x16x32_bf16 v[48:51], v[16:19], a[92:95], v[48:51]\n\tds_read_b64_tr_b16 v[36:37], v3 offset:57344\n\tds_read_b64_tr_b16 v[38:39], v3 offset:57856\n\tv_mfma_f32_16x16x32_bf16 v[56:59], v[16:19], a[96:99], v[56:59]\n\tv_mfma_f32_16x16x32_bf16 v[64:67], v[16:19], a[100:103], v[64:67]\n\tds_write_b128 v237, a[0:3] offset:0\n\ts_waitcnt lgkmcnt(9)\n\tv_mfma_f32_16x16x32_bf16 v[52:55], v[20:23], a[92:95], v[52:55]\n\t" \
    "ds_read_b64_tr_b16 v[40:41], v2 offset:61440\n\tds_read_b64_tr_b16 v[42:43], v2 offset:61952\n\tv_mfma_f32_16x16x32_bf16 v[60:63], v[20:23], a[96:99], v[60:63]\n\tv_mfma_f32_16x16x32_bf16 v[68:71], v[20:23], a[100:103], v[68:71]\n\tds_write_b128 v237, a[8:11] offset:2048\n\ts_waitcnt lgkmcnt(10)\n\tv_mfma_f32_16x16x32_bf16 v[48:51], v[24:27], a[104:107], v[48:51]\n\tds_read_b64_tr_b16 v[44:45], v3 offset:61440\n\t" \
    "ds_read_b64_tr_b16 v[46:47], v3 offset:61952\n\ts_mov_b32 s69, 6193280\n\tv_mfma_f32_16x16x32_bf16 v[56:59], v[24:27], a[108:111], v[56:59]\n\ts_add_u32 m0, s72, 20480\n\tv_mfma_f32_16x16x32_bf16 v[64:67], v[24:27], a[112:115], v[64:67]\n\tds_write_b128 v237, a[16:19] offset:4096\n\ts_waitcnt lgkmcnt(11)\n\tv_mfma_f32_16x16x32_bf16 v[52:55], v[28:31], a[104:107], v[52:55]\n\t" \
    "ds_read_b64_tr_b16 v[16:17], v0 offset:0\n\tds_read_b64_tr_b16 v[18:19], v0 offset:512\n\tv_mfma_f32_16x16x32_bf16 v[60:63], v[28:31], a[108:111], v[60:63]\n\tbuffer_load_dwordx4 v10, s[12:15], s69 offen lds\n\ts_add_u32 m0, s72, 21504\n\tv_mfma_f32_16x16x32_bf16 v[68:71], v[28:31], a[112:115], v[68:71]\n\tds_write_b128 v237, a[4:7] offset:1024\n\tds_write_b128 v237, a[12:15] offset:3072\n\t" \
    "ds_write_b128 v237, a[20:23] offset:5120\n\ts_waitcnt vmcnt(8) lgkmcnt(0)\n\ts_barrier\n\tv_mfma_f32_16x16x32_bf16 v[48:51], v[32:35], a[116:119], v[48:51]\n\tds_read_b64_tr_b16 v[20:21], v1 offset:0\n\tds_read_b64_tr_b16 v[22:23], v1 offset:512\n\tv_mfma_f32_16x16x32_bf16 v[56:59], v[32:35], a[120:123], v[56:59]\n\tbuffer_load_dwordx4 v11, s[12:15], s69 offen lds\n\t" \
    "ds_read_b128 v[96:99], v238 offset:0\n\ts_mov_b32 s69, 6488192\n\tv_mfma_f32_16x16x32_bf16 v[64:67], v[32:35], a[124:127], v[64:67]\n\tds_read_b128 v[100:103], v238 offset:1024\n\tds_read_b128 v[104:107], v238 offset:2048\n\tv_mfma_f32_16x16x32_bf16 v[52:55], v[36:39], a[116:119], v[52:55]\n\tds_read_b64_tr_b16 v[24:25], v2 offset:0\n\tds_read_b64_tr_b16 v[26:27], v2 offset:512\n\t" \
    "s_add_u32 m0, s72, 24576\n\tv_mfma_f32_16x16x32_bf16 v[60:63], v[36:39], a[120:123], v[60:63]\n\tbuffer_load_dwordx4 v10, s[12:15], s69 offen lds\n\tds_read_b128 v[108:111], v238 offset:3072\n\ts_add_u32 m0, s72, 25600\n\tv_mfma_f32_16x16x32_bf16 v[68:71], v[36:39], a[124:127], v[68:71]\n\tds_read_b128 v[112:115], v238 offset:4096\n\tds_read_b128 v[116:119], v238 offset:5120\n\t" \
    "v_mfma_f32_16x16x32_bf16 v[48:51], v[40:43], a[128:131], v[48:51]\n\tds_read_b64_tr_b16 v[28:29], v3 offset:0\n\tds_read_b64_tr_b16 v[30:31], v3 offset:512\n\tv_mfma_f32_16x16x32_bf16 v[56:59], v[40:43], a[132:135], v[56:59]\n\tbuffer_load_dwordx4 v11, s[12:15], s69 offen lds\n\ts_mov_b32 s69, 6783104\n\ts_add_u32 m0, s72, 28672\n\tv_mfma_f32_16x16x32_bf16 v[64:67], v[40:43], a[136:139], v[64:67]\n\t" \
    "v_mfma_f32_16x16x32_bf16 v[52:55], v[44:47], a[128:131], v[52:55]\n\tds_read_b64_tr_b16 v[32:33], v0 offset:4096\n\tds_read_b64_tr_b16 v[34:35], v0 offset:4608\n\tv_mfma_f32_16x16x32_bf16 v[60:63], v[44:47], a[132:135], v[60:63]\n\tbuffer_load_dwordx4 v10, s[12:15], s69 offen lds\n\ts_add_u32 m0, s72, 29696\n\tv_mfma_f32_16x16x32_bf16 v[68:71], v[44:47], a[136:139], v[68:71]\n\ts_waitcnt lgkmcnt(10)\n\t" \
    "v_pk_add_f32 v[48:49], v[48:49], v[96:97]\n\tv_pk_add_f32 v[50:51], v[50:51], v[98:99]\n\tv_pk_add_f32 v[52:53], v[52:53], v[100:101]\n\tbuffer_load_dwordx4 v11, s[12:15], s69 offen lds\n\tv_pk_add_f32 v[54:55], v[54:55], v[102:103]\n\ts_mov_b32 s69, 7078016\n\tv_pk_mul_f32 v[96:97], v[48:49], v[48:49]\n\ts_add_u32 m0, s72, 32768\n\t" \
    "v_pk_mul_f32 v[98:99], v[50:51], v[50:51]\n\tv_pk_mul_f32 v[100:101], v[52:53], v[52:53]\n\tv_pk_mul_f32 v[102:103], v[54:55], v[54:55]\n\tv_pk_fma_f32 v[96:97], v[96:97], v[246:247], v[246:247] op_sel:[0,0,1] op_sel_hi:[1,0,1]\n\tv_pk_fma_f32 v[98:99], v[98:99], v[246:247], v[246:247] op_sel:[0,0,1] op_sel_hi:[1,0,1]\n\tv_pk_fma_f32 v[100:101], v[100:101], v[246:247], v[246:247] op_sel:[0,0,1] op_sel_hi:[1,0,1]\n\tv_pk_fma_f32 v[102:103], v[102:103], v[246:247], v[246:247] op_sel:[0,0,1] op_sel_hi:[1,0,1]\n\tv_pk_mul_f32 v[96:97], v[96:97], v[48:49]\n\t" \
    "v_pk_mul_f32 v[98:99], v[98:99], v[50:51]\n\tbuffer_load_dwordx4 v10, s[12:15], s69 offen lds\n\tv_pk_mul_f32 v[100:101], v[100:101], v[52:53]\n\ts_add_u32 m0, s72, 33792\n\tv_pk_mul_f32 v[102:103], v[102:103], v[54:55]\n\ts_waitcnt lgkmcnt(6)\n\tv_exp_f32 v96, v96\n\tv_pk_add_f32 v[56:57], v[56:57], v[104:105]\n\t" \
    "v_exp_f32 v97, v97\n\tv_pk_add_f32 v[58:59], v[58:59], v[106:107]\n\tv_exp_f32 v98, v98\n\tv_pk_add_f32 v[60:61], v[60:61], v[108:109]\n\tv_exp_f32 v99, v99\n\tbuffer_load_dwordx4 v11, s[12:15], s69 offen lds\n\tv_pk_add_f32 v[62:63], v[62:63], v[110:111]\n\ts_mov_b32 s69, 7372928\n\t" \
    "v_exp_f32 v100, v100\n\ts_add_u32 m0, s72, 36864\n\tv_pk_mul_f32 v[104:105], v[56:57], v[56:57]\n\tv_exp_f32 v101, v101\n\tv_pk_mul_f32 v[106:107], v[58:59], v[58:59]\n\tv_exp_f32 v102, v102\n\tv_pk_mul_f32 v[108:109], v[60:61], v[60:61]\n\tv_exp_f32 v103, v103\n\t" \
    "v_pk_mul_f32 v[110:111], v[62:63], v[62:63]\n\tv_pk_add_f32 v[96:97], v[96:97], 1.0 op_sel_hi:[1,0]\n\tbuffer_load_dwordx4 v10, s[12:15], s69 offen lds\n\tv_pk_add_f32 v[98:99], v[98:99], 1.0 op_sel_hi:[1,0]\n\ts_add_u32 m0, s72, 37888\n\tv_pk_add_f32 v[100:101], v[100:101], 1.0 op_sel_hi:[1,0]\n\tv_pk_add_f32 v[102:103], v[102:103], 1.0 op_sel_hi:[1,0]\n\tv_pk_fma_f32 v[104:105], v[104:105], v[246:247], v[246:247] op_sel:[0,0,1] op_sel_hi:[1,0,1]\n\t" \
    "v_rcp_f32 v96, v96\n\tv_pk_fma_f32 v[106:107], v[106:107], v[246:247], v[246:247] op_sel:[0,0,1] op_sel_hi:[1,0,1]\n\tv_rcp_f32 v97, v97\n\tv_pk_fma_f32 v[108:109], v[108:109], v[246:247], v[246:247] op_sel:[0,0,1] op_sel_hi:[1,0,1]\n\tv_rcp_f32 v98, v98\n\tv_pk_fma_f32 v[110:111], v[110:111], v[246:247], v[246:247] op_sel:[0,0,1] op_sel_hi:[1,0,1]\n\tv_rcp_f32 v99, v99\n\tbuffer_load_dwordx4 v11, s[12:15], s69 offen lds\n\t" \
    "v_pk_mul_f32 v[104:105], v[104:105], v[56:57]\n\ts_mov_b32 s69, 7667840\n\tv_rcp_f32 v100, v100\n\ts_add_u32 m0, s72, 40960\n\tv_pk_mul_f32 v[106:107], v[106:107], v[58:59]\n\tv_rcp_f32 v101, v101\n\tv_pk_mul_f32 v[108:109], v[108:109], v[60:61]\n\tv_rcp_f32 v102, v102\n\t" \
    "v_pk_mul_f32 v[110:111], v[110:111], v[62:63]\n\tv_rcp_f32 v103, v103\n\ts_waitcnt lgkmcnt(4)\n\tv_exp_f32 v104, v104\n\tbuffer_load_dwordx4 v10, s[12:15], s69 offen lds\n\tv_pk_mul_f32 v[96:97], v[96:97], v[48:49]\n\ts_add_u32 m0, s72, 41984\n\tv_exp_f32 v105, v105\n\t" \
    "v_pk_mul_f32 v[98:99], v[98:99], v[50:51]\n\tv_exp_f32 v106, v106\n\tv_pk_mul_f32 v[100:101], v[100:101], v[52:53]\n\tv_exp_f32 v107, v107\n\tv_pk_mul_f32 v[102:103], v[102:103], v[54:55]\n\tv_exp_f32 v108, v108\n\tbuffer_load_dwordx4 v11, s[12:15], s69 offen lds\n\tv_cvt_pk_bf16_f32 v48, v96, v97\n\t" \
    "s_mov_b32 s69, 7962752\n\tv_exp_f32 v109, v109\n\ts_add_u32 m0, s72, 45056\n\tv_cvt_pk_bf16_f32 v49, v98, v99\n\tv_exp_f32 v110, v110\n\tv_cvt_pk_bf16_f32 v50, v100, v101\n\tv_exp_f32 v111, v111\n\tv_cvt_pk_bf16_f32 v51, v102, v103\n\t" \
    "buffer_store_dwordx4 v[48:51], v8, s[8:11], s45 offen offset:0 sc1\n\tv_pk_add_f32 v[104:105], v[104:105], 1.0 op_sel_hi:[1,0]\n\tv_pk_add_f32 v[106:107], v[106:107], 1.0 op_sel_hi:[1,0]\n\tv_pk_add_f32 v[108:109], v[108:109], 1.0 op_sel_hi:[1,0]\n\tv_pk_add_f32 v[110:111], v[110:111], 1.0 op_sel_hi:[1,0]\n\tv_pk_add_f32 v[64:65], v[64:65], v[112:113]\n\tv_rcp_f32 v104, v104\n\tv_pk_add_f32 v[66:67], v[66:67], v[114:115]\n\t" \
    "v_rcp_f32 v105, v105\n\tv_pk_add_f32 v[68:69], v[68:69], v[116:117]\n\tv_rcp_f32 v106, v106\n\tv_pk_add_f32 v[70:71], v[70:71], v[118:119]\n\tv_rcp_f32 v107, v107\n\tv_pk_mul_f32 v[112:113], v[64:65], v[64:65]\n\tv_rcp_f32 v108, v108\n\tv_pk_mul_f32 v[114:115], v[66:67], v[66:67]\n\t" \
    "v_rcp_f32 v109, v109\n\tbuffer_load_dwordx4 v10, s[12:15], s69 offen lds\n\tv_pk_mul_f32 v[116:117], v[68:69], v[68:69]\n\ts_add_u32 m0, s72, 46080\n\tv_rcp_f32 v110, v110\n\tv_pk_mul_f32 v[118:119], v[70:71], v[70:71]\n\tv_rcp_f32 v111, v111\n\tv_pk_fma_f32 v[112:113], v[112:113], v[246:247], v[246:247] op_sel:[0,0,1] op_sel_hi:[1,0,1]\n\t" \
    "v_pk_mul_f32 v[104:105], v[104:105], v[56:57]\n\tv_pk_mul_f32 v[106:107], v[106:107], v[58:59]\n\tv_pk_mul_f32 v[108:109], v[108:109], v[60:61]\n\tv_pk_mul_f32 v[110:111], v[110:111], v[62:63]\n\tv_cvt_pk_bf16_f32 v56, v104, v105\n\tv_cvt_pk_bf16_f32 v57, v106, v107\n\tv_cvt_pk_bf16_f32 v58, v108, v109\n\tbuffer_load_dwordx4 v11, s[12:15], s69 offen lds\n\t" \
    "v_cvt_pk_bf16_f32 v59, v110, v111\n\ts_mov_b32 s69, 8257664\n\tbuffer_store_dwordx4 v[56:59], v8, s[8:11], s45 offen offset:1024 sc1\n\tv_pk_fma_f32 v[114:115], v[114:115], v[246:247], v[246:247] op_sel:[0,0,1] op_sel_hi:[1,0,1]\n\ts_add_u32 m0, s72, 49152\n\tv_pk_fma_f32 v[116:117], v[116:117], v[246:247], v[246:247] op_sel:[0,0,1] op_sel_hi:[1,0,1]\n\tv_pk_fma_f32 v[118:119], v[118:119], v[246:247], v[246:247] op_sel:[0,0,1] op_sel_hi:[1,0,1]\n\tv_pk_mul_f32 v[112:113], v[112:113], v[64:65]\n\t" \
    "v_pk_mul_f32 v[114:115], v[114:115], v[66:67]\n\tv_pk_mul_f32 v[116:117], v[116:117], v[68:69]\n\tv_pk_mul_f32 v[118:119], v[118:119], v[70:71]\n\tv_exp_f32 v112, v112\n\tv_exp_f32 v113, v113\n\tv_exp_f32 v114, v114\n\tv_exp_f32 v115, v115\n\tv_exp_f32 v116, v116\n\t" \
    "v_exp_f32 v117, v117\n\tv_exp_f32 v118, v118\n\tv_exp_f32 v119, v119\n\tv_pk_add_f32 v[112:113], v[112:113], 1.0 op_sel_hi:[1,0]\n\tv_pk_add_f32 v[114:115], v[114:115], 1.0 op_sel_hi:[1,0]\n\tv_pk_add_f32 v[116:117], v[116:117], 1.0 op_sel_hi:[1,0]\n\tv_pk_add_f32 v[118:119], v[118:119], 1.0 op_sel_hi:[1,0]\n\tv_rcp_f32 v112, v112\n\t" \
    "buffer_load_dwordx4 v10, s[12:15], s69 offen lds\n\tv_rcp_f32 v113, v113\n\ts_add_u32 m0, s72, 50176\n\tv_rcp_f32 v114, v114\n\tv_rcp_f32 v115, v115\n\tv_rcp_f32 v116, v116\n\tv_rcp_f32 v117, v117\n\tv_rcp_f32 v118, v118\n\t" \
    "v_rcp_f32 v119, v119\n\tv_pk_mul_f32 v[112:113], v[112:113], v[64:65]\n\tbuffer_load_dwordx4 v11, s[12:15], s69 offen lds\n\tv_pk_mul_f32 v[114:115], v[114:115], v[66:67]\n\ts_mov_b32 s69, 8552576\n\tv_pk_mul_f32 v[116:117], v[116:117], v[68:69]\n\ts_add_u32 m0, s72, 53248\n\tv_pk_mul_f32 v[118:119], v[118:119], v[70:71]\n\t" \
    "v_cvt_pk_bf16_f32 v64, v112, v113\n\tv_cvt_pk_bf16_f32 v65, v114, v115\n\tv_cvt_pk_bf16_f32 v66, v116, v117\n\tv_cvt_pk_bf16_f32 v67, v118, v119\n\tbuffer_store_dwordx4 v[64:67], v8, s[8:11], s45 offen offset:2048 sc1\n\ts_waitcnt vmcnt(22)\n\ts_barrier\n\tv_mfma_f32_16x16x32_bf16 a[24:27], v[16:19], v[180:183], a[24:27]\n\t" \
    "ds_read_b64_tr_b16 v[36:37], v1 offset:4096\n\tds_read_b64_tr_b16 v[38:39], v1 offset:4608\n\tv_mfma_f32_16x16x32_bf16 a[32:35], v[16:19], v[184:187], a[32:35]\n\tv_mfma_f32_16x16x32_bf16 a[40:43], v[16:19], v[188:191], a[40:43]\n\tv_mfma_f32_16x16x32_bf16 a[28:31], v[20:23], v[180:183], a[28:31]\n\tds_read_b64_tr_b16 v[40:41], v2 offset:4096\n\tds_read_b64_tr_b16 v[42:43], v2 offset:4608\n\tv_mfma_f32_16x16x32_bf16 a[36:39], v[20:23], v[184:187], a[36:39]\n\t" \
    "buffer_load_dwordx4 v10, s[12:15], s69 offen lds\n\ts_add_u32 m0, s72, 54272\n\tv_mfma_f32_16x16x32_bf16 a[44:47], v[20:23], v[188:191], a[44:47]\n\tv_mfma_f32_16x16x32_bf16 v[72:75], v[24:27], v[180:183], v[72:75]\n\tds_read_b64_tr_b16 v[44:45], v3 offset:4096\n\tds_read_b64_tr_b16 v[46:47], v3 offset:4608\n\tv_mfma_f32_16x16x32_bf16 v[80:83], v[24:27], v[184:187], v[80:83]\n\tbuffer_load_dwordx4 v11, s[12:15], s69 offen lds\n\t" \
    "v_lshrrev_b32 v229, 3, v244\n\tv_mfma_f32_16x16x32_bf16 v[88:91], v[24:27], v[188:191], v[88:91]\n\ts_waitcnt lgkmcnt(8)\n\tv_mfma_f32_16x16x32_bf16 v[76:79], v[28:31], v[180:183], v[76:79]\n\tds_read_b64_tr_b16 v[16:17], v0 offset:8192\n\tds_read_b64_tr_b16 v[18:19], v0 offset:8704\n\tv_mfma_f32_16x16x32_bf16 v[84:87], v[28:31], v[184:187], v[84:87]\n\tv_mfma_f32_16x16x32_bf16 v[92:95], v[28:31], v[188:191], v[92:95]\n\t" \
    "s_waitcnt lgkmcnt(8)\n\tv_mfma_f32_16x16x32_bf16 a[24:27], v[32:35], v[192:195], a[24:27]\n\tds_read_b64_tr_b16 v[20:21], v1 offset:8192\n\tds_read_b64_tr_b16 v[22:23], v1 offset:8704\n\tv_mfma_f32_16x16x32_bf16 a[32:35], v[32:35], v[196:199], a[32:35]\n\tbuffer_load_ushort v228, v229, s[32:35], 0 offen offset:0\n\tv_mfma_f32_16x16x32_bf16 a[40:43], v[32:35], v[200:203], a[40:43]\n\ts_waitcnt lgkmcnt(8)\n\t" \
    "v_mfma_f32_16x16x32_bf16 a[28:31], v[36:39], v[192:195], a[28:31]\n\tds_read_b64_tr_b16 v[24:25], v2 offset:8192\n\tds_read_b64_tr_b16 v[26:27], v2 offset:8704\n\tv_mfma_f32_16x16x32_bf16 a[36:39], v[36:39], v[196:199], a[36:39]\n\tbuffer_load_ushort v232, v229, s[32:35], 0 offen offset:32\n\ts_mov_b32 s69, 8847488\n\ts_add_u32 m0, s72, 57344\n\tv_mfma_f32_16x16x32_bf16 a[44:47], v[36:39], v[200:203], a[44:47]\n\t" \
    "s_waitcnt lgkmcnt(8)\n\tv_mfma_f32_16x16x32_bf16 v[72:75], v[40:43], v[192:195], v[72:75]\n\tds_read_b64_tr_b16 v[28:29], v3 offset:8192\n\tds_read_b64_tr_b16 v[30:31], v3 offset:8704\n\tv_mfma_f32_16x16x32_bf16 v[80:83], v[40:43], v[196:199], v[80:83]\n\tv_mfma_f32_16x16x32_bf16 v[88:91], v[40:43], v[200:203], v[88:91]\n\ts_waitcnt lgkmcnt(8)\n\tv_mfma_f32_16x16x32_bf16 v[76:79], v[44:47], v[192:195], v[76:79]\n\t" \
    "ds_read_b64_tr_b16 v[32:33], v0 offset:12288\n\tds_read_b64_tr_b16 v[34:35], v0 offset:12800\n\tv_mfma_f32_16x16x32_bf16 v[84:87], v[44:47], v[196:199], v[84:87]\n\tbuffer_load_dwordx4 v10, s[12:15], s69 offen lds\n\ts_add_u32 m0, s72, 58368\n\tv_mfma_f32_16x16x32_bf16 v[92:95], v[44:47], v[200:203], v[92:95]\n\ts_waitcnt vmcnt(22)\n\ts_barrier\n\t" \
    "s_waitcnt lgkmcnt(8)\n\tv_mfma_f32_16x16x32_bf16 a[24:27], v[16:19], v[120:123], a[24:27]\n\tds_read_b64_tr_b16 v[36:37], v1 offset:12288\n\tds_read_b64_tr_b16 v[38:39], v1 offset:12800\n\tv_mfma_f32_16x16x32_bf16 a[32:35], v[16:19], v[124:127], a[32:35]\n\tbuffer_load_dwordx4 v11, s[12:15], s69 offen lds\n\ts_mov_b32 s69, 9142400\n\ts_add_u32 m0, s72, 61440\n\t" \
    "v_mfma_f32_16x16x32_bf16 a[40:43], v[16:19], v[128:131], a[40:43]\n\ts_waitcnt lgkmcnt(8)\n\tv_mfma_f32_16x16x32_bf16 a[28:31], v[20:23], v[120:123], a[28:31]\n\tds_read_b64_tr_b16 v[40:41], v2 offset:12288\n\tds_read_b64_tr_b16 v[42:43], v2 offset:12800\n\tv_mfma_f32_16x16x32_bf16 a[36:39], v[20:23], v[124:127], a[36:39]\n\tbuffer_load_dwordx4 v10, s[12:15], s69 offen lds\n\ts_add_u32 m0, s72, 62464\n\t" \
    "v_mfma_f32_16x16x32_bf16 a[44:47], v[20:23], v[128:131], a[44:47]\n\ts_waitcnt lgkmcnt(8)\n\tv_mfma_f32_16x16x32_bf16 v[72:75], v[24:27], v[120:123], v[72:75]\n\tds_read_b64_tr_b16 v[44:45], v3 offset:12288\n\tds_read_b64_tr_b16 v[46:47], v3 offset:12800\n\tv_mfma_f32_16x16x32_bf16 v[80:83], v[24:27], v[124:127], v[80:83]\n\tbuffer_load_dwordx4 v11, s[12:15], s69 offen lds\n\ts_mov_b32 s69, 131072\n\t" \
    "s_add_u32 m0, s54, 0\n\tv_mfma_f32_16x16x32_bf16 v[88:91], v[24:27], v[128:131], v[88:91]\n\ts_waitcnt lgkmcnt(8)\n\tv_mfma_f32_16x16x32_bf16 v[76:79], v[28:31], v[120:123], v[76:79]\n\tds_read_b64_tr_b16 v[16:17], v0 offset:16384\n\tds_read_b64_tr_b16 v[18:19], v0 offset:16896\n\tv_mfma_f32_16x16x32_bf16 v[84:87], v[28:31], v[124:127], v[84:87]\n\tbuffer_load_dwordx4 v12, s[16:19], s69 offen lds\n\t" \
    "s_mov_b32 s69, 131072\n\ts_add_u32 m0, s54, 65536\n\tv_mfma_f32_16x16x32_bf16 v[92:95], v[28:31], v[128:131], v[92:95]\n\ts_waitcnt lgkmcnt(8)\n\tv_mfma_f32_16x16x32_bf16 a[24:27], v[32:35], v[132:135], a[24:27]\n\tds_read_b64_tr_b16 v[20:21], v1 offset:16384\n\tds_read_b64_tr_b16 v[22:23], v1 offset:16896\n\tv_mfma_f32_16x16x32_bf16 a[32:35], v[32:35], v[136:139], a[32:35]\n\t" \
    "v_mfma_f32_16x16x32_bf16 a[40:43], v[32:35], v[140:143], a[40:43]\n\ts_waitcnt lgkmcnt(8)\n\tv_mfma_f32_16x16x32_bf16 a[28:31], v[36:39], v[132:135], a[28:31]\n\tds_read_b64_tr_b16 v[24:25], v2 offset:16384\n\tds_read_b64_tr_b16 v[26:27], v2 offset:16896\n\tv_mfma_f32_16x16x32_bf16 a[36:39], v[36:39], v[136:139], a[36:39]\n\tbuffer_load_dwordx4 v13, s[16:19], s69 offen lds\n\ts_mov_b32 s69, 0\n\t" \
    "s_add_u32 m0, s54, 4096\n\tv_mfma_f32_16x16x32_bf16 a[44:47], v[36:39], v[140:143], a[44:47]\n\ts_waitcnt lgkmcnt(8)\n\tv_mfma_f32_16x16x32_bf16 v[72:75], v[40:43], v[132:135], v[72:75]\n\tds_read_b64_tr_b16 v[28:29], v3 offset:16384\n\tds_read_b64_tr_b16 v[30:31], v3 offset:16896\n\tv_mfma_f32_16x16x32_bf16 v[80:83], v[40:43], v[136:139], v[80:83]\n\tbuffer_load_dwordx4 v12, s[16:19], s69 offen lds\n\t" \
    "s_mov_b32 s69, 0\n\ts_add_u32 m0, s54, 69632\n\tv_mfma_f32_16x16x32_bf16 v[88:91], v[40:43], v[140:143], v[88:91]\n\ts_waitcnt lgkmcnt(8)\n\tv_mfma_f32_16x16x32_bf16 v[76:79], v[44:47], v[132:135], v[76:79]\n\tds_read_b64_tr_b16 v[32:33], v0 offset:20480\n\tds_read_b64_tr_b16 v[34:35], v0 offset:20992\n\tv_mfma_f32_16x16x32_bf16 v[84:87], v[44:47], v[136:139], v[84:87]\n\t" \
    "v_mfma_f32_16x16x32_bf16 v[92:95], v[44:47], v[140:143], v[92:95]\n\ts_waitcnt vmcnt(24)\n\ts_barrier\n\ts_waitcnt lgkmcnt(8)\n\tv_mfma_f32_16x16x32_bf16 a[24:27], v[16:19], v[144:147], a[24:27]\n\tds_read_b64_tr_b16 v[36:37], v1 offset:20480\n\tds_read_b64_tr_b16 v[38:39], v1 offset:20992\n\tv_mfma_f32_16x16x32_bf16 a[32:35], v[16:19], v[148:151], a[32:35]\n\t" \
    "buffer_load_dwordx4 v13, s[16:19], s69 offen lds\n\ts_mov_b32 s69, 655360\n\ts_add_u32 m0, s54, 8192\n\tv_mfma_f32_16x16x32_bf16 a[40:43], v[16:19], v[152:155], a[40:43]\n\ts_waitcnt lgkmcnt(8)\n\tv_mfma_f32_16x16x32_bf16 a[28:31], v[20:23], v[144:147], a[28:31]\n\tds_read_b64_tr_b16 v[40:41], v2 offset:20480\n\tds_read_b64_tr_b16 v[42:43], v2 offset:20992\n\t" \
    "v_mfma_f32_16x16x32_bf16 a[36:39], v[20:23], v[148:151], a[36:39]\n\tbuffer_load_dwordx4 v12, s[16:19], s69 offen lds\n\ts_mov_b32 s69, 655360\n\ts_add_u32 m0, s54, 73728\n\tv_mfma_f32_16x16x32_bf16 a[44:47], v[20:23], v[152:155], a[44:47]\n\ts_waitcnt lgkmcnt(8)\n\tv_mfma_f32_16x16x32_bf16 v[72:75], v[24:27], v[144:147], v[72:75]\n\tds_read_b64_tr_b16 v[44:45], v3 offset:20480\n\t" \
    "ds_read_b64_tr_b16 v[46:47], v3 offset:20992\n\tv_mfma_f32_16x16x32_bf16 v[80:83], v[24:27], v[148:151], v[80:83]\n\tbuffer_load_dwordx4 v13, s[16:19], s69 offen lds\n\ts_mov_b32 s69, 524288\n\ts_add_u32 m0, s54, 12288\n\tv_mfma_f32_16x16x32_bf16 v[88:91], v[24:27], v[152:155], v[88:91]\n\ts_waitcnt lgkmcnt(8)\n\tv_mfma_f32_16x16x32_bf16 v[76:79], v[28:31], v[144:147], v[76:79]\n\t" \
    "ds_read_b64_tr_b16 v[16:17], v0 offset:24576\n\tds_read_b64_tr_b16 v[18:19], v0 offset:25088\n\tv_mfma_f32_16x16x32_bf16 v[84:87], v[28:31], v[148:151], v[84:87]\n\tbuffer_load_dwordx4 v12, s[16:19], s69 offen lds\n\ts_mov_b32 s69, 524288\n\ts_add_u32 m0, s54, 77824\n\tv_mfma_f32_16x16x32_bf16 v[92:95], v[28:31], v[152:155], v[92:95]\n\ts_waitcnt lgkmcnt(8)\n\t" \
    "v_mfma_f32_16x16x32_bf16 a[24:27], v[32:35], v[204:207], a[24:27]\n\tds_read_b64_tr_b16 v[20:21], v1 offset:24576\n\tds_read_b64_tr_b16 v[22:23], v1 offset:25088\n\tv_mfma_f32_16x16x32_bf16 a[32:35], v[32:35], v[208:211], a[32:35]\n\tbuffer_load_dwordx4 v13, s[16:19], s69 offen lds\n\tv_mfma_f32_16x16x32_bf16 a[40:43], v[32:35], v[212:215], a[40:43]\n\ts_waitcnt lgkmcnt(8)\n\tv_mfma_f32_16x16x32_bf16 a[28:31], v[36:39], v[204:207], a[28:31]\n\t" \
    "ds_read_b64_tr_b16 v[24:25], v2 offset:24576\n\tds_read_b64_tr_b16 v[26:27], v2 offset:25088\n\tv_mfma_f32_16x16x32_bf16 a[36:39], v[36:39], v[208:211], a[36:39]\n\tv_mfma_f32_16x16x32_bf16 a[44:47], v[36:39], v[212:215], a[44:47]\n\ts_waitcnt lgkmcnt(8)\n\tv_mfma_f32_16x16x32_bf16 v[72:75], v[40:43], v[204:207], v[72:75]\n\tds_read_b64_tr_b16 v[28:29], v3 offset:24576\n\tds_read_b64_tr_b16 v[30:31], v3 offset:25088\n\t" \
    "v_mfma_f32_16x16x32_bf16 v[80:83], v[40:43], v[208:211], v[80:83]\n\tv_mfma_f32_16x16x32_bf16 v[88:91], v[40:43], v[212:215], v[88:91]\n\ts_waitcnt lgkmcnt(8)\n\tv_mfma_f32_16x16x32_bf16 v[76:79], v[44:47], v[204:207], v[76:79]\n\tds_read_b64_tr_b16 v[32:33], v0 offset:28672\n\tds_read_b64_tr_b16 v[34:35], v0 offset:29184\n\tv_mfma_f32_16x16x32_bf16 v[84:87], v[44:47], v[208:211], v[84:87]\n\tv_mfma_f32_16x16x32_bf16 v[92:95], v[44:47], v[212:215], v[92:95]\n\t" \
    "s_waitcnt vmcnt(16)\n\tbuffer_store_dwordx2 v[248:249], off, s[8:11], s49 sc1\n\ts_waitcnt vmcnt(13)\n\ts_barrier\n\ts_waitcnt lgkmcnt(8)\n\tv_mfma_f32_16x16x32_bf16 a[24:27], v[16:19], v[156:159], a[24:27]\n\tds_read_b64_tr_b16 v[36:37], v1 offset:28672\n\tds_read_b64_tr_b16 v[38:39], v1 offset:29184\n\t" \
    "s_mov_b32 s69, 1179648\n\tv_mfma_f32_16x16x32_bf16 a[32:35], v[16:19], v[160:163], a[32:35]\n\tv_lshlrev_b32 v228, 16, v228\n\tv_mov_b32 v229, v228\n\ts_add_u32 m0, s54, 16384\n\tv_mfma_f32_16x16x32_bf16 a[40:43], v[16:19], v[164:167], a[40:43]\n\tv_mov_b32 v230, v228\n\tv_mov_b32 v231, v228\n\t" \
    "s_waitcnt lgkmcnt(8)\n\tv_mfma_f32_16x16x32_bf16 a[28:31], v[20:23], v[156:159], a[28:31]\n\tds_read_b64_tr_b16 v[40:41], v2 offset:28672\n\tds_read_b64_tr_b16 v[42:43], v2 offset:29184\n\tv_mfma_f32_16x16x32_bf16 a[36:39], v[20:23], v[160:163], a[36:39]\n\tbuffer_load_dwordx4 v12, s[16:19], s69 offen lds\n\tv_lshlrev_b32 v232, 16, v232\n\ts_mov_b32 s69, 1179648\n\t" \
    "v_mfma_f32_16x16x32_bf16 a[44:47], v[20:23], v[164:167], a[44:47]\n\tv_mov_b32 v233, v232\n\tv_mov_b32 v234, v232\n\ts_waitcnt lgkmcnt(8)\n\tv_mfma_f32_16x16x32_bf16 v[72:75], v[24:27], v[156:159], v[72:75]\n\tds_read_b64_tr_b16 v[44:45], v3 offset:28672\n\tds_read_b64_tr_b16 v[46:47], v3 offset:29184\n\ts_add_u32 m0, s54, 81920\n\t" \
    "v_mfma_f32_16x16x32_bf16 v[80:83], v[24:27], v[160:163], v[80:83]\n\tbuffer_load_dwordx4 v13, s[16:19], s69 offen lds\n\tv_mov_b32 v235, v232\n\ts_mov_b32 s69, 1048576\n\tv_mfma_f32_16x16x32_bf16 v[88:91], v[24:27], v[164:167], v[88:91]\n\ts_add_u32 m0, s54, 20480\n\ts_waitcnt lgkmcnt(8)\n\tv_mfma_f32_16x16x32_bf16 v[76:79], v[28:31], v[156:159], v[76:79]\n\t" \
    "ds_read_b64_tr_b16 v[16:17], v0 offset:32768\n\tds_read_b64_tr_b16 v[18:19], v0 offset:33280\n\tv_mfma_f32_16x16x32_bf16 v[84:87], v[28:31], v[160:163], v[84:87]\n\tbuffer_load_dwordx4 v12, s[16:19], s69 offen lds\n\ts_mov_b32 s69, 1048576\n\ts_add_u32 m0, s54, 86016\n\tv_mfma_f32_16x16x32_bf16 v[92:95], v[28:31], v[164:167], v[92:95]\n\ts_waitcnt lgkmcnt(8)\n\t" \
    "v_mfma_f32_16x16x32_bf16 a[24:27], v[32:35], v[216:219], a[24:27]\n\tds_read_b64_tr_b16 v[20:21], v1 offset:32768\n\tds_read_b64_tr_b16 v[22:23], v1 offset:33280\n\tv_mfma_f32_16x16x32_bf16 a[32:35], v[32:35], a[48:51], a[32:35]\n\tbuffer_load_dwordx4 v13, s[16:19], s69 offen lds\n\tv_mfma_f32_16x16x32_bf16 a[40:43], v[32:35], a[52:55], a[40:43]\n\ts_waitcnt lgkmcnt(8)\n\tv_mfma_f32_16x16x32_bf16 a[28:31], v[36:39], v[216:219], a[28:31]\n\t" \
    "ds_read_b64_tr_b16 v[24:25], v2 offset:32768\n\tds_read_b64_tr_b16 v[26:27], v2 offset:33280\n\tv_mfma_f32_16x16x32_bf16 a[36:39], v[36:39], a[48:51], a[36:39]\n\tv_mfma_f32_16x16x32_bf16 a[44:47], v[36:39], a[52:55], a[44:47]\n\ts_waitcnt lgkmcnt(8)\n\tv_mfma_f32_16x16x32_bf16 v[72:75], v[40:43], v[216:219], v[72:75]\n\tds_read_b64_tr_b16 v[28:29], v3 offset:32768\n\tds_read_b64_tr_b16 v[30:31], v3 offset:33280\n\t" \
    "v_mfma_f32_16x16x32_bf16 v[80:83], v[40:43], a[48:51], v[80:83]\n\tv_mfma_f32_16x16x32_bf16 v[88:91], v[40:43], a[52:55], v[88:91]\n\ts_waitcnt lgkmcnt(8)\n\tv_mfma_f32_16x16x32_bf16 v[76:79], v[44:47], v[216:219], v[76:79]\n\tds_read_b64_tr_b16 v[32:33], v0 offset:36864\n\tds_read_b64_tr_b16 v[34:35], v0 offset:37376\n\tv_mfma_f32_16x16x32_bf16 v[84:87], v[44:47], a[48:51], v[84:87]\n\tv_mfma_f32_16x16x32_bf16 v[92:95], v[44:47], a[52:55], v[92:95]\n\t" \
    "s_barrier\n\ts_waitcnt lgkmcnt(8)\n\tv_mfma_f32_16x16x32_bf16 a[24:27], v[16:19], a[56:59], a[24:27]\n\tds_read_b64_tr_b16 v[36:37], v1 offset:36864\n\tds_read_b64_tr_b16 v[38:39], v1 offset:37376\n\ts_mov_b32 s69, 1703936\n\tv_mfma_f32_16x16x32_bf16 a[32:35], v[16:19], a[60:63], a[32:35]\n\ts_add_u32 m0, s54, 24576\n\t" \
    "v_mfma_f32_16x16x32_bf16 a[40:43], v[16:19], a[64:67], a[40:43]\n\ts_waitcnt lgkmcnt(8)\n\tv_mfma_f32_16x16x32_bf16 a[28:31], v[20:23], a[56:59], a[28:31]\n\tds_read_b64_tr_b16 v[40:41], v2 offset:36864\n\tds_read_b64_tr_b16 v[42:43], v2 offset:37376\n\tv_mfma_f32_16x16x32_bf16 a[36:39], v[20:23], a[60:63], a[36:39]\n\tbuffer_load_dwordx4 v12, s[16:19], s69 offen lds\n\ts_mov_b32 s69, 1703936\n\t" \
    "s_add_u32 m0, s54, 90112\n\tv_mfma_f32_16x16x32_bf16 a[44:47], v[20:23], a[64:67], a[44:47]\n\ts_waitcnt lgkmcnt(8)\n\tv_mfma_f32_16x16x32_bf16 v[72:75], v[24:27], a[56:59], v[72:75]\n\tds_read_b64_tr_b16 v[44:45], v3 offset:36864\n\tds_read_b64_tr_b16 v[46:47], v3 offset:37376\n\tv_mfma_f32_16x16x32_bf16 v[80:83], v[24:27], a[60:63], v[80:83]\n\tbuffer_load_dwordx4 v13, s[16:19], s69 offen lds\n\t" \
    "s_mov_b32 s69, 1572864\n\ts_add_u32 m0, s54, 28672\n\tv_mfma_f32_16x16x32_bf16 v[88:91], v[24:27], a[64:67], v[88:91]\n\ts_waitcnt lgkmcnt(8)\n\tv_mfma_f32_16x16x32_bf16 v[76:79], v[28:31], a[56:59], v[76:79]\n\tds_read_b64_tr_b16 v[16:17], v0 offset:40960\n\tds_read_b64_tr_b16 v[18:19], v0 offset:41472\n\tv_mfma_f32_16x16x32_bf16 v[84:87], v[28:31], a[60:63], v[84:87]\n\t" \
    "buffer_load_dwordx4 v12, s[16:19], s69 offen lds\n\ts_mov_b32 s69, 1572864\n\ts_add_u32 m0, s54, 94208\n\tv_mfma_f32_16x16x32_bf16 v[92:95], v[28:31], a[64:67], v[92:95]\n\ts_waitcnt lgkmcnt(8)\n\tv_mfma_f32_16x16x32_bf16 a[24:27], v[32:35], v[168:171], a[24:27]\n\tds_read_b64_tr_b16 v[20:21], v1 offset:40960\n\tds_read_b64_tr_b16 v[22:23], v1 offset:41472\n\t" \
    "v_mfma_f32_16x16x32_bf16 a[32:35], v[32:35], v[172:175], a[32:35]\n\tbuffer_load_dwordx4 v13, s[16:19], s69 offen lds\n\tv_mfma_f32_16x16x32_bf16 a[40:43], v[32:35], v[176:179], a[40:43]\n\ts_waitcnt lgkmcnt(8)\n\tv_mfma_f32_16x16x32_bf16 a[28:31], v[36:39], v[168:171], a[28:31]\n\tds_read_b64_tr_b16 v[24:25], v2 offset:40960\n\tds_read_b64_tr_b16 v[26:27], v2 offset:41472\n\tv_mfma_f32_16x16x32_bf16 a[36:39], v[36:39], v[172:175], a[36:39]\n\t" \
    "v_mfma_f32_16x16x32_bf16 a[44:47], v[36:39], v[176:179], a[44:47]\n\ts_waitcnt lgkmcnt(8)\n\tv_mfma_f32_16x16x32_bf16 v[72:75], v[40:43], v[168:171], v[72:75]\n\tds_read_b64_tr_b16 v[28:29], v3 offset:40960\n\tds_read_b64_tr_b16 v[30:31], v3 offset:41472\n\tv_mfma_f32_16x16x32_bf16 v[80:83], v[40:43], v[172:175], v[80:83]\n\tv_mfma_f32_16x16x32_bf16 v[88:91], v[40:43], v[176:179], v[88:91]\n\ts_waitcnt lgkmcnt(8)\n\t" \
    "v_mfma_f32_16x16x32_bf16 v[76:79], v[44:47], v[168:171], v[76:79]\n\tds_read_b64_tr_b16 v[32:33], v0 offset:45056\n\tds_read_b64_tr_b16 v[34:35], v0 offset:45568\n\tv_mfma_f32_16x16x32_bf16 v[84:87], v[44:47], v[172:175], v[84:87]\n\tv_mfma_f32_16x16x32_bf16 v[92:95], v[44:47], v[176:179], v[92:95]\n\ts_waitcnt vmcnt(19)\n\ts_barrier\n\ts_waitcnt lgkmcnt(8)\n\t" \
    "v_mfma_f32_16x16x32_bf16 a[24:27], v[16:19], a[68:71], a[24:27]\n\tds_read_b64_tr_b16 v[36:37], v1 offset:45056\n\tds_read_b64_tr_b16 v[38:39], v1 offset:45568\n\ts_mov_b32 s69, 2228224\n\tv_mfma_f32_16x16x32_bf16 a[32:35], v[16:19], a[72:75], a[32:35]\n\ts_add_u32 m0, s54, 32768\n\tv_mfma_f32_16x16x32_bf16 a[40:43], v[16:19], a[76:79], a[40:43]\n\ts_waitcnt lgkmcnt(8)\n\t" \
    "v_mfma_f32_16x16x32_bf16 a[28:31], v[20:23], a[68:71], a[28:31]\n\tds_read_b64_tr_b16 v[40:41], v2 offset:45056\n\tds_read_b64_tr_b16 v[42:43], v2 offset:45568\n\tv_mfma_f32_16x16x32_bf16 a[36:39], v[20:23], a[72:75], a[36:39]\n\tbuffer_load_dwordx4 v12, s[16:19], s69 offen lds\n\ts_mov_b32 s69, 2228224\n\ts_add_u32 m0, s54, 98304\n\tv_mfma_f32_16x16x32_bf16 a[44:47], v[20:23], a[76:79], a[44:47]\n\t" \
    "s_waitcnt lgkmcnt(8)\n\tv_mfma_f32_16x16x32_bf16 v[72:75], v[24:27], a[68:71], v[72:75]\n\tds_read_b64_tr_b16 v[44:45], v3 offset:45056\n\tds_read_b64_tr_b16 v[46:47], v3 offset:45568\n\tv_mfma_f32_16x16x32_bf16 v[80:83], v[24:27], a[72:75], v[80:83]\n\tbuffer_load_dwordx4 v13, s[16:19], s69 offen lds\n\ts_mov_b32 s69, 2097152\n\ts_add_u32 m0, s54, 36864\n\t" \
    "v_mfma_f32_16x16x32_bf16 v[88:91], v[24:27], a[76:79], v[88:91]\n\ts_waitcnt lgkmcnt(8)\n\tv_mfma_f32_16x16x32_bf16 v[76:79], v[28:31], a[68:71], v[76:79]\n\tds_read_b64_tr_b16 v[16:17], v0 offset:49152\n\tds_read_b64_tr_b16 v[18:19], v0 offset:49664\n\tv_mfma_f32_16x16x32_bf16 v[84:87], v[28:31], a[72:75], v[84:87]\n\tbuffer_load_dwordx4 v12, s[16:19], s69 offen lds\n\ts_mov_b32 s69, 2097152\n\t" \
    "s_add_u32 m0, s54, 102400\n\tv_mfma_f32_16x16x32_bf16 v[92:95], v[28:31], a[76:79], v[92:95]\n\ts_waitcnt lgkmcnt(8)\n\tv_mfma_f32_16x16x32_bf16 a[24:27], v[32:35], a[80:83], a[24:27]\n\tds_read_b64_tr_b16 v[20:21], v1 offset:49152\n\tds_read_b64_tr_b16 v[22:23], v1 offset:49664\n\tv_mfma_f32_16x16x32_bf16 a[32:35], v[32:35], a[84:87], a[32:35]\n\tbuffer_load_dwordx4 v13, s[16:19], s69 offen lds\n\t" \
    "v_mfma_f32_16x16x32_bf16 a[40:43], v[32:35], a[88:91], a[40:43]\n\ts_waitcnt lgkmcnt(8)\n\tv_mfma_f32_16x16x32_bf16 a[28:31], v[36:39], a[80:83], a[28:31]\n\tds_read_b64_tr_b16 v[24:25], v0 offset:53248\n\tds_read_b64_tr_b16 v[26:27], v0 offset:53760\n\tv_mfma_f32_16x16x32_bf16 a[36:39], v[36:39], a[84:87], a[36:39]\n\tv_mfma_f32_16x16x32_bf16 a[44:47], v[36:39], a[88:91], a[44:47]\n\ts_waitcnt lgkmcnt(8)\n\t" \
    "v_mfma_f32_16x16x32_bf16 v[72:75], v[40:43], a[80:83], v[72:75]\n\tds_read_b64_tr_b16 v[28:29], v1 offset:53248\n\tds_read_b64_tr_b16 v[30:31], v1 offset:53760\n\tv_mfma_f32_16x16x32_bf16 v[80:83], v[40:43], a[84:87], v[80:83]\n\tv_mfma_f32_16x16x32_bf16 v[88:91], v[40:43], a[88:91], v[88:91]\n\ts_waitcnt lgkmcnt(8)\n\tv_mfma_f32_16x16x32_bf16 v[76:79], v[44:47], a[80:83], v[76:79]\n\tds_read_b64_tr_b16 v[32:33], v0 offset:57344\n\t" \
    "ds_read_b64_tr_b16 v[34:35], v0 offset:57856\n\tv_mfma_f32_16x16x32_bf16 v[84:87], v[44:47], a[84:87], v[84:87]\n\tv_mfma_f32_16x16x32_bf16 v[92:95], v[44:47], a[88:91], v[92:95]\n\ts_waitcnt vmcnt(21)\n\ts_barrier\n\ts_waitcnt lgkmcnt(8)\n\tv_mfma_f32_16x16x32_bf16 a[24:27], v[16:19], a[92:95], a[24:27]\n\tds_read_b64_tr_b16 v[36:37], v1 offset:57344\n\t" \
    "ds_read_b64_tr_b16 v[38:39], v1 offset:57856\n\ts_mov_b32 s69, 2752512\n\tv_mfma_f32_16x16x32_bf16 a[32:35], v[16:19], a[96:99], a[32:35]\n\ts_add_u32 m0, s54, 40960\n\tv_mfma_f32_16x16x32_bf16 a[40:43], v[16:19], a[100:103], a[40:43]\n\ts_waitcnt lgkmcnt(8)\n\tv_mfma_f32_16x16x32_bf16 a[28:31], v[20:23], a[92:95], a[28:31]\n\tds_read_b64_tr_b16 v[40:41], v0 offset:61440\n\t" \
    "ds_read_b64_tr_b16 v[42:43], v0 offset:61952\n\tv_mfma_f32_16x16x32_bf16 a[36:39], v[20:23], a[96:99], a[36:39]\n\tbuffer_load_dwordx4 v12, s[16:19], s69 offen lds\n\ts_mov_b32 s69, 2752512\n\ts_add_u32 m0, s54, 106496\n\tv_mfma_f32_16x16x32_bf16 a[44:47], v[20:23], a[100:103], a[44:47]\n\ts_waitcnt lgkmcnt(8)\n\tv_mfma_f32_16x16x32_bf16 a[24:27], v[24:27], a[104:107], a[24:27]\n\t" \
    "ds_read_b64_tr_b16 v[44:45], v1 offset:61440\n\tds_read_b64_tr_b16 v[46:47], v1 offset:61952\n\tv_mfma_f32_16x16x32_bf16 a[32:35], v[24:27], a[108:111], a[32:35]\n\tbuffer_load_dwordx4 v13, s[16:19], s69 offen lds\n\ts_mov_b32 s69, 2621440\n\ts_add_u32 m0, s54, 45056\n\tv_mfma_f32_16x16x32_bf16 a[40:43], v[24:27], a[112:115], a[40:43]\n\ts_waitcnt lgkmcnt(8)\n\t" \
    "v_mfma_f32_16x16x32_bf16 a[28:31], v[28:31], a[104:107], a[28:31]\n\tds_read_b64_tr_b16 v[16:17], v2 offset:49152\n\tds_read_b64_tr_b16 v[18:19], v2 offset:49664\n\tv_mfma_f32_16x16x32_bf16 a[36:39], v[28:31], a[108:111], a[36:39]\n\tbuffer_load_dwordx4 v12, s[16:19], s69 offen lds\n\ts_mov_b32 s69, 2621440\n\ts_add_u32 m0, s54, 110592\n\tv_mfma_f32_16x16x32_bf16 a[44:47], v[28:31], a[112:115], a[44:47]\n\t" \
    "buffer_load_dwordx2 v[250:251], v241, s[8:11], s50 offen sc1\n\ts_waitcnt lgkmcnt(8)\n\tv_mfma_f32_16x16x32_bf16 a[24:27], v[32:35], a[116:119], a[24:27]\n\tds_read_b64_tr_b16 v[20:21], v3 offset:49152\n\tds_read_b64_tr_b16 v[22:23], v3 offset:49664\n\tv_mfma_f32_16x16x32_bf16 a[32:35], v[32:35], a[120:123], a[32:35]\n\tbuffer_load_dwordx4 v13, s[16:19], s69 offen lds\n\tv_mfma_f32_16x16x32_bf16 a[40:43], v[32:35], a[124:127], a[40:43]\n\t" \
    "s_waitcnt lgkmcnt(8)\n\tv_mfma_f32_16x16x32_bf16 a[28:31], v[36:39], a[116:119], a[28:31]\n\tds_read_b64_tr_b16 v[24:25], v2 offset:53248\n\tds_read_b64_tr_b16 v[26:27], v2 offset:53760\n\tv_mfma_f32_16x16x32_bf16 a[36:39], v[36:39], a[120:123], a[36:39]\n\tv_mfma_f32_16x16x32_bf16 a[44:47], v[36:39], a[124:127], a[44:47]\n\ts_waitcnt lgkmcnt(8)\n\tv_mfma_f32_16x16x32_bf16 a[24:27], v[40:43], a[128:131], a[24:27]\n\t" \
    "ds_read_b64_tr_b16 v[28:29], v3 offset:53248\n\tds_read_b64_tr_b16 v[30:31], v3 offset:53760\n\tv_mfma_f32_16x16x32_bf16 a[32:35], v[40:43], a[132:135], a[32:35]\n\tv_mfma_f32_16x16x32_bf16 a[40:43], v[40:43], a[136:139], a[40:43]\n\ts_waitcnt lgkmcnt(8)\n\tv_mfma_f32_16x16x32_bf16 a[28:31], v[44:47], a[128:131], a[28:31]\n\tds_read_b64_tr_b16 v[32:33], v2 offset:57344\n\tds_read_b64_tr_b16 v[34:35], v2 offset:57856\n\t" \
    "v_mfma_f32_16x16x32_bf16 a[36:39], v[44:47], a[132:135], a[36:39]\n\tv_mfma_f32_16x16x32_bf16 a[44:47], v[44:47], a[136:139], a[44:47]\n\ts_waitcnt vmcnt(25)\n\ts_barrier\n\ts_waitcnt lgkmcnt(8)\n\tv_mfma_f32_16x16x32_bf16 v[72:75], v[16:19], a[92:95], v[72:75]\n\tds_read_b64_tr_b16 v[36:37], v3 offset:57344\n\tds_read_b64_tr_b16 v[38:39], v3 offset:57856\n\t" \
    "v_mfma_f32_16x16x32_bf16 v[80:83], v[16:19], a[96:99], v[80:83]\n\tv_mfma_f32_16x16x32_bf16 v[88:91], v[16:19], a[100:103], v[88:91]\n\tds_write_b128 v237, a[24:27] offset:0\n\ts_waitcnt lgkmcnt(9)\n\tv_mfma_f32_16x16x32_bf16 v[76:79], v[20:23], a[92:95], v[76:79]\n\tds_read_b64_tr_b16 v[40:41], v2 offset:61440\n\tds_read_b64_tr_b16 v[42:43], v2 offset:61952\n\tv_mfma_f32_16x16x32_bf16 v[84:87], v[20:23], a[96:99], v[84:87]\n\t" \
    "v_mfma_f32_16x16x32_bf16 v[92:95], v[20:23], a[100:103], v[92:95]\n\tds_write_b128 v237, a[32:35] offset:2048\n\ts_waitcnt lgkmcnt(10)\n\tv_mfma_f32_16x16x32_bf16 v[72:75], v[24:27], a[104:107], v[72:75]\n\tds_read_b64_tr_b16 v[44:45], v3 offset:61440\n\tds_read_b64_tr_b16 v[46:47], v3 offset:61952\n\tv_mfma_f32_16x16x32_bf16 v[80:83], v[24:27], a[108:111], v[80:83]\n\tv_mfma_f32_16x16x32_bf16 v[88:91], v[24:27], a[112:115], v[88:91]\n\t" \
    "ds_write_b128 v237, a[40:43] offset:4096\n\ts_waitcnt lgkmcnt(11)\n\tv_mfma_f32_16x16x32_bf16 v[76:79], v[28:31], a[104:107], v[76:79]\n\tds_read_b64_tr_b16 v[16:17], v4 offset:0\n\tds_read_b64_tr_b16 v[18:19], v4 offset:2048\n\tv_mfma_f32_16x16x32_bf16 v[84:87], v[28:31], a[108:111], v[84:87]\n\tv_mfma_f32_16x16x32_bf16 v[92:95], v[28:31], a[112:115], v[92:95]\n\tds_write_b128 v237, a[28:31] offset:1024\n\t" \
    "ds_write_b128 v237, a[36:39] offset:3072\n\tds_write_b128 v237, a[44:47] offset:5120\n\ts_waitcnt vmcnt(24) lgkmcnt(0)\n\ts_barrier\n\tv_mfma_f32_16x16x32_bf16 v[72:75], v[32:35], a[116:119], v[72:75]\n\tds_read_b64_tr_b16 v[20:21], v5 offset:0\n\tds_read_b64_tr_b16 v[22:23], v5 offset:2048\n\ts_mov_b32 s69, 3276800\n\t" \
    "v_mfma_f32_16x16x32_bf16 v[80:83], v[32:35], a[120:123], v[80:83]\n\tds_read_b128 v[96:99], v238 offset:0\n\tds_read_b128 v[100:103], v238 offset:1024\n\ts_add_u32 m0, s54, 49152\n\tv_mfma_f32_16x16x32_bf16 v[88:91], v[32:35], a[124:127], v[88:91]\n\tds_read_b128 v[104:107], v238 offset:2048\n\tds_read_b128 v[108:111], v238 offset:3072\n\tv_mfma_f32_16x16x32_bf16 v[76:79], v[36:39], a[116:119], v[76:79]\n\t" \
    "ds_read_b64_tr_b16 v[24:25], v6 offset:0\n\tds_read_b64_tr_b16 v[26:27], v6 offset:2048\n\tv_mfma_f32_16x16x32_bf16 v[84:87], v[36:39], a[120:123], v[84:87]\n\tbuffer_load_dwordx4 v12, s[16:19], s69 offen lds\n\tds_read_b128 v[112:115], v238 offset:4096\n\ts_mov_b32 s69, 3276800\n\tv_mfma_f32_16x16x32_bf16 v[92:95], v[36:39], a[124:127], v[92:95]\n\tds_read_b128 v[116:119], v238 offset:5120\n\t" \
    "s_add_u32 m0, s54, 114688\n\tv_mfma_f32_16x16x32_bf16 v[72:75], v[40:43], a[128:131], v[72:75]\n\tds_read_b64_tr_b16 v[28:29], v7 offset:0\n\tds_read_b64_tr_b16 v[30:31], v7 offset:2048\n\tv_mfma_f32_16x16x32_bf16 v[80:83], v[40:43], a[132:135], v[80:83]\n\tbuffer_load_dwordx4 v13, s[16:19], s69 offen lds\n\tv_mfma_f32_16x16x32_bf16 v[88:91], v[40:43], a[136:139], v[88:91]\n\tv_mfma_f32_16x16x32_bf16 v[76:79], v[44:47], a[128:131], v[76:79]\n\t" \
    "ds_read_b64_tr_b16 v[32:33], v224 offset:0\n\tds_read_b64_tr_b16 v[34:35], v224 offset:2048\n\tv_mfma_f32_16x16x32_bf16 v[84:87], v[44:47], a[132:135], v[84:87]\n\tv_mfma_f32_16x16x32_bf16 v[92:95], v[44:47], a[136:139], v[92:95]\n\ts_cmp_lg_u32 s39, 2\n\ts_cbranch_scc1 106f\n\ts_waitcnt vmcnt(3)\n\tv_cmp_ne_u64 vcc, s[36:37], v[250:251]\n\t" \
    "s_cbranch_vccz 106f\n\ts_add_u32 s73, s45, 55296\n\ts_waitcnt lgkmcnt(10)\n\tv_pk_add_f32 v[72:73], v[72:73], v[96:97]\n\tv_pk_add_f32 v[74:75], v[74:75], v[98:99]\n\tv_pk_add_f32 v[76:77], v[76:77], v[100:101]\n\tv_pk_add_f32 v[78:79], v[78:79], v[102:103]\n\tv_pk_mul_f32 v[96:97], v[72:73], v[72:73]\n\t" \
    "v_pk_mul_f32 v[98:99], v[74:75], v[74:75]\n\tv_pk_mul_f32 v[100:101], v[76:77], v[76:77]\n\tv_pk_mul_f32 v[102:103], v[78:79], v[78:79]\n\tv_pk_fma_f32 v[96:97], v[96:97], v[246:247], v[246:247] op_sel:[0,0,1] op_sel_hi:[1,0,1]\n\tv_pk_fma_f32 v[98:99], v[98:99], v[246:247], v[246:247] op_sel:[0,0,1] op_sel_hi:[1,0,1]\n\tv_pk_fma_f32 v[100:101], v[100:101], v[246:247], v[246:247] op_sel:[0,0,1] op_sel_hi:[1,0,1]\n\tv_pk_fma_f32 v[102:103], v[102:103], v[246:247], v[246:247] op_sel:[0,0,1] op_sel_hi:[1,0,1]\n\tv_pk_mul_f32 v[96:97], v[96:97], v[72:73]\n\t" \
    "v_pk_mul_f32 v[98:99], v[98:99], v[74:75]\n\tv_pk_mul_f32 v[100:101], v[100:101], v[76:77]\n\tv_pk_mul_f32 v[102:103], v[102:103], v[78:79]\n\ts_waitcnt lgkmcnt(8)\n\tv_exp_f32 v96, v96\n\tv_pk_add_f32 v[80:81], v[80:81], v[104:105]\n\tv_exp_f32 v97, v97\n\tv_pk_add_f32 v[82:83], v[82:83], v[106:107]\n\t" \
    "v_exp_f32 v98, v98\n\tv_pk_add_f32 v[84:85], v[84:85], v[108:109]\n\tv_exp_f32 v99, v99\n\tv_pk_add_f32 v[86:87], v[86:87], v[110:111]\n\tv_exp_f32 v100, v100\n\tv_pk_mul_f32 v[104:105], v[80:81], v[80:81]\n\tv_exp_f32 v101, v101\n\tv_pk_mul_f32 v[106:107], v[82:83], v[82:83]\n\t" \
    "v_exp_f32 v102, v102\n\tv_pk_mul_f32 v[108:109], v[84:85], v[84:85]\n\tv_exp_f32 v103, v103\n\tv_pk_mul_f32 v[110:111], v[86:87], v[86:87]\n\tv_pk_add_f32 v[96:97], v[96:97], 1.0 op_sel_hi:[1,0]\n\tv_pk_add_f32 v[98:99], v[98:99], 1.0 op_sel_hi:[1,0]\n\tv_pk_add_f32 v[100:101], v[100:101], 1.0 op_sel_hi:[1,0]\n\tv_pk_add_f32 v[102:103], v[102:103], 1.0 op_sel_hi:[1,0]\n\t" \
    "v_pk_fma_f32 v[104:105], v[104:105], v[246:247], v[246:247] op_sel:[0,0,1] op_sel_hi:[1,0,1]\n\tv_rcp_f32 v96, v96\n\tv_pk_fma_f32 v[106:107], v[106:107], v[246:247], v[246:247] op_sel:[0,0,1] op_sel_hi:[1,0,1]\n\tv_rcp_f32 v97, v97\n\tv_pk_fma_f32 v[108:109], v[108:109], v[246:247], v[246:247] op_sel:[0,0,1] op_sel_hi:[1,0,1]\n\tv_rcp_f32 v98, v98\n\tv_pk_fma_f32 v[110:111], v[110:111], v[246:247], v[246:247] op_sel:[0,0,1] op_sel_hi:[1,0,1]\n\tv_rcp_f32 v99, v99\n\t" \
    "v_pk_mul_f32 v[104:105], v[104:105], v[80:81]\n\tv_rcp_f32 v100, v100\n\tv_pk_mul_f32 v[106:107], v[106:107], v[82:83]\n\tv_rcp_f32 v101, v101\n\tv_pk_mul_f32 v[108:109], v[108:109], v[84:85]\n\tv_rcp_f32 v102, v102\n\tv_pk_mul_f32 v[110:111], v[110:111], v[86:87]\n\tv_rcp_f32 v103, v103\n\t" \
    "s_waitcnt lgkmcnt(4)\n\tv_exp_f32 v104, v104\n\tv_pk_mul_f32 v[96:97], v[96:97], v[72:73]\n\tv_exp_f32 v105, v105\n\tv_pk_mul_f32 v[98:99], v[98:99], v[74:75]\n\tv_exp_f32 v106, v106\n\tv_pk_mul_f32 v[100:101], v[100:101], v[76:77]\n\tv_exp_f32 v107, v107\n\t" \
    "v_pk_mul_f32 v[102:103], v[102:103], v[78:79]\n\tv_exp_f32 v108, v108\n\tv_cvt_pk_bf16_f32 v72, v96, v97\n\tv_exp_f32 v109, v109\n\tv_cvt_pk_bf16_f32 v73, v98, v99\n\tv_exp_f32 v110, v110\n\tv_cvt_pk_bf16_f32 v74, v100, v101\n\tv_exp_f32 v111, v111\n\t" \
    "v_cvt_pk_bf16_f32 v75, v102, v103\n\tv_pk_add_f32 v[104:105], v[104:105], 1.0 op_sel_hi:[1,0]\n\tv_pk_add_f32 v[106:107], v[106:107], 1.0 op_sel_hi:[1,0]\n\tv_pk_add_f32 v[108:109], v[108:109], 1.0 op_sel_hi:[1,0]\n\tv_pk_add_f32 v[110:111], v[110:111], 1.0 op_sel_hi:[1,0]\n\tv_pk_add_f32 v[88:89], v[88:89], v[112:113]\n\tv_rcp_f32 v104, v104\n\tv_pk_add_f32 v[90:91], v[90:91], v[114:115]\n\t" \
    "v_rcp_f32 v105, v105\n\tv_pk_add_f32 v[92:93], v[92:93], v[116:117]\n\tv_rcp_f32 v106, v106\n\tv_pk_add_f32 v[94:95], v[94:95], v[118:119]\n\tv_rcp_f32 v107, v107\n\tv_pk_mul_f32 v[112:113], v[88:89], v[88:89]\n\tv_rcp_f32 v108, v108\n\tv_pk_mul_f32 v[114:115], v[90:91], v[90:91]\n\t" \
    "v_rcp_f32 v109, v109\n\tv_pk_mul_f32 v[116:117], v[92:93], v[92:93]\n\tv_rcp_f32 v110, v110\n\tv_pk_mul_f32 v[118:119], v[94:95], v[94:95]\n\tv_rcp_f32 v111, v111\n\tv_pk_fma_f32 v[112:113], v[112:113], v[246:247], v[246:247] op_sel:[0,0,1] op_sel_hi:[1,0,1]\n\tv_pk_mul_f32 v[104:105], v[104:105], v[80:81]\n\tv_pk_mul_f32 v[106:107], v[106:107], v[82:83]\n\t" \
    "v_pk_mul_f32 v[108:109], v[108:109], v[84:85]\n\tv_pk_mul_f32 v[110:111], v[110:111], v[86:87]\n\tv_cvt_pk_bf16_f32 v80, v104, v105\n\tv_cvt_pk_bf16_f32 v81, v106, v107\n\tv_cvt_pk_bf16_f32 v82, v108, v109\n\tv_cvt_pk_bf16_f32 v83, v110, v111\n\tbuffer_load_dwordx2 v[250:251], v241, s[8:11], s50 offen sc1\n\tv_pk_fma_f32 v[114:115], v[114:115], v[246:247], v[246:247] op_sel:[0,0,1] op_sel_hi:[1,0,1]\n\t" \
    "v_pk_fma_f32 v[116:117], v[116:117], v[246:247], v[246:247] op_sel:[0,0,1] op_sel_hi:[1,0,1]\n\tv_pk_fma_f32 v[118:119], v[118:119], v[246:247], v[246:247] op_sel:[0,0,1] op_sel_hi:[1,0,1]\n\tv_pk_mul_f32 v[112:113], v[112:113], v[88:89]\n\tv_pk_mul_f32 v[114:115], v[114:115], v[90:91]\n\tv_pk_mul_f32 v[116:117], v[116:117], v[92:93]\n\tv_pk_mul_f32 v[118:119], v[118:119], v[94:95]\n\tv_exp_f32 v112, v112\n\tv_exp_f32 v113, v113\n\t" \
    "v_exp_f32 v114, v114\n\tv_exp_f32 v115, v115\n\tv_exp_f32 v116, v116\n\tv_exp_f32 v117, v117\n\tv_exp_f32 v118, v118\n\tv_exp_f32 v119, v119\n\tv_pk_add_f32 v[112:113], v[112:113], 1.0 op_sel_hi:[1,0]\n\tv_pk_add_f32 v[114:115], v[114:115], 1.0 op_sel_hi:[1,0]\n\t" \
    "v_pk_add_f32 v[116:117], v[116:117], 1.0 op_sel_hi:[1,0]\n\tv_pk_add_f32 v[118:119], v[118:119], 1.0 op_sel_hi:[1,0]\n\tv_rcp_f32 v112, v112\n\tv_rcp_f32 v113, v113\n\tv_rcp_f32 v114, v114\n\tv_rcp_f32 v115, v115\n\tv_rcp_f32 v116, v116\n\tv_rcp_f32 v117, v117\n\t" \
    "v_rcp_f32 v118, v118\n\tv_rcp_f32 v119, v119\n\tv_pk_mul_f32 v[112:113], v[112:113], v[88:89]\n\tv_pk_mul_f32 v[114:115], v[114:115], v[90:91]\n\tv_pk_mul_f32 v[116:117], v[116:117], v[92:93]\n\tv_pk_mul_f32 v[118:119], v[118:119], v[94:95]\n\tv_cvt_pk_bf16_f32 v88, v112, v113\n\tv_cvt_pk_bf16_f32 v89, v114, v115\n\t" \
    "v_cvt_pk_bf16_f32 v90, v116, v117\n\tv_cvt_pk_bf16_f32 v91, v118, v119\n\ts_waitcnt vmcnt(0)\n\tv_cmp_ne_u64 vcc, s[36:37], v[250:251]\n\ts_cbranch_vccz 109f\n\ts_mov_b32 s71, 1048576\n\t108:\n\ts_sleep 1\n\t" \
    "buffer_load_dwordx2 v[250:251], v241, s[8:11], s50 offen sc1\n\ts_waitcnt vmcnt(0)\n\tv_cmp_ne_u64 vcc, s[36:37], v[250:251]\n\ts_cbranch_vccz 109f\n\ts_sub_u32 s71, s71, 1\n\ts_cbranch_scc0 108b\n\t109:\n\ts_add_u32 s68, s46, 0\n\t" \
    "buffer_load_dwordx4 v[144:147], v8, s[8:11], s68 offen offset:0 sc1\n\tbuffer_load_dwordx4 v[148:151], v8, s[8:11], s68 offen offset:1024 sc1\n\tbuffer_load_dwordx4 v[152:155], v8, s[8:11], s68 offen offset:2048 sc1\n\ts_add_u32 s68, s46, 3072\n\tbuffer_load_dwordx4 v[156:159], v8, s[8:11], s68 offen offset:0 sc1\n\tbuffer_load_dwordx4 v[160:163], v8, s[8:11], s68 offen offset:1024 sc1\n\tbuffer_load_dwordx4 v[164:167], v8, s[8:11], s68 offen offset:2048 sc1\n\ts_add_u32 s68, s46, 6144\n\t" \
    "buffer_load_dwordx4 v[168:171], v8, s[8:11], s68 offen offset:0 sc1\n\tbuffer_load_dwordx4 v[172:175], v8, s[8:11], s68 offen offset:1024 sc1\n\tbuffer_load_dwordx4 v[176:179], v8, s[8:11], s68 offen offset:2048 sc1\n\tbuffer_store_dwordx4 v[72:75], v8, s[8:11], s73 offen offset:0 sc1\n\tbuffer_store_dwordx4 v[80:83], v8, s[8:11], s73 offen offset:1024 sc1\n\tbuffer_store_dwordx4 v[88:91], v8, s[8:11], s73 offen offset:2048 sc1\n\ts_branch 107f\n\t106:\n\t" \
    "s_add_u32 s68, s46, 0\n\tbuffer_load_dwordx4 v[144:147], v8, s[8:11], s68 offen offset:0 sc1\n\tbuffer_load_dwordx4 v[148:151], v8, s[8:11], s68 offen offset:1024 sc1\n\tbuffer_load_dwordx4 v[152:155], v8, s[8:11], s68 offen offset:2048 sc1\n\ts_add_u32 s73, s45, 55296\n\ts_waitcnt lgkmcnt(10)\n\tv_pk_add_f32 v[72:73], v[72:73], v[96:97]\n\ts_add_u32 s68, s46, 3072\n\t" \
    "v_pk_add_f32 v[74:75], v[74:75], v[98:99]\n\tv_pk_add_f32 v[76:77], v[76:77], v[100:101]\n\tv_pk_add_f32 v[78:79], v[78:79], v[102:103]\n\tv_pk_mul_f32 v[96:97], v[72:73], v[72:73]\n\tv_pk_mul_f32 v[98:99], v[74:75], v[74:75]\n\tv_pk_mul_f32 v[100:101], v[76:77], v[76:77]\n\tv_pk_mul_f32 v[102:103], v[78:79], v[78:79]\n\tv_pk_fma_f32 v[96:97], v[96:97], v[246:247], v[246:247] op_sel:[0,0,1] op_sel_hi:[1,0,1]\n\t" \
    "v_pk_fma_f32 v[98:99], v[98:99], v[246:247], v[246:247] op_sel:[0,0,1] op_sel_hi:[1,0,1]\n\tv_pk_fma_f32 v[100:101], v[100:101], v[246:247], v[246:247] op_sel:[0,0,1] op_sel_hi:[1,0,1]\n\tv_pk_fma_f32 v[102:103], v[102:103], v[246:247], v[246:247] op_sel:[0,0,1] op_sel_hi:[1,0,1]\n\tv_pk_mul_f32 v[96:97], v[96:97], v[72:73]\n\tv_pk_mul_f32 v[98:99], v[98:99], v[74:75]\n\tv_pk_mul_f32 v[100:101], v[100:101], v[76:77]\n\tv_pk_mul_f32 v[102:103], v[102:103], v[78:79]\n\ts_waitcnt lgkmcnt(8)\n\t" \
    "v_exp_f32 v96, v96\n\tv_pk_add_f32 v[80:81], v[80:81], v[104:105]\n\tv_exp_f32 v97, v97\n\tv_pk_add_f32 v[82:83], v[82:83], v[106:107]\n\tv_exp_f32 v98, v98\n\tv_pk_add_f32 v[84:85], v[84:85], v[108:109]\n\tv_exp_f32 v99, v99\n\tbuffer_load_dwordx4 v[156:159], v8, s[8:11], s68 offen offset:0 sc1\n\t" \
    "v_pk_add_f32 v[86:87], v[86:87], v[110:111]\n\tv_exp_f32 v100, v100\n\tv_pk_mul_f32 v[104:105], v[80:81], v[80:81]\n\tv_exp_f32 v101, v101\n\tv_pk_mul_f32 v[106:107], v[82:83], v[82:83]\n\tv_exp_f32 v102, v102\n\tv_pk_mul_f32 v[108:109], v[84:85], v[84:85]\n\tv_exp_f32 v103, v103\n\t" \
    "v_pk_mul_f32 v[110:111], v[86:87], v[86:87]\n\tv_pk_add_f32 v[96:97], v[96:97], 1.0 op_sel_hi:[1,0]\n\tv_pk_add_f32 v[98:99], v[98:99], 1.0 op_sel_hi:[1,0]\n\tv_pk_add_f32 v[100:101], v[100:101], 1.0 op_sel_hi:[1,0]\n\tv_pk_add_f32 v[102:103], v[102:103], 1.0 op_sel_hi:[1,0]\n\tv_pk_fma_f32 v[104:105], v[104:105], v[246:247], v[246:247] op_sel:[0,0,1] op_sel_hi:[1,0,1]\n\tv_rcp_f32 v96, v96\n\tv_pk_fma_f32 v[106:107], v[106:107], v[246:247], v[246:247] op_sel:[0,0,1] op_sel_hi:[1,0,1]\n\t" \
    "v_rcp_f32 v97, v97\n\tv_pk_fma_f32 v[108:109], v[108:109], v[246:247], v[246:247] op_sel:[0,0,1] op_sel_hi:[1,0,1]\n\tv_rcp_f32 v98, v98\n\tv_pk_fma_f32 v[110:111], v[110:111], v[246:247], v[246:247] op_sel:[0,0,1] op_sel_hi:[1,0,1]\n\tv_rcp_f32 v99, v99\n\tv_pk_mul_f32 v[104:105], v[104:105], v[80:81]\n\tbuffer_load_dwordx4 v[160:163], v8, s[8:11], s68 offen offset:1024 sc1\n\tv_rcp_f32 v100, v100\n\t" \
    "v_pk_mul_f32 v[106:107], v[106:107], v[82:83]\n\tv_rcp_f32 v101, v101\n\tv_pk_mul_f32 v[108:109], v[108:109], v[84:85]\n\tv_rcp_f32 v102, v102\n\tv_pk_mul_f32 v[110:111], v[110:111], v[86:87]\n\tv_rcp_f32 v103, v103\n\ts_waitcnt lgkmcnt(4)\n\tv_exp_f32 v104, v104\n\t" \
    "v_pk_mul_f32 v[96:97], v[96:97], v[72:73]\n\tv_exp_f32 v105, v105\n\tv_pk_mul_f32 v[98:99], v[98:99], v[74:75]\n\tv_exp_f32 v106, v106\n\tv_pk_mul_f32 v[100:101], v[100:101], v[76:77]\n\tv_exp_f32 v107, v107\n\tv_pk_mul_f32 v[102:103], v[102:103], v[78:79]\n\tv_exp_f32 v108, v108\n\t" \
    "v_cvt_pk_bf16_f32 v72, v96, v97\n\tv_exp_f32 v109, v109\n\tv_cvt_pk_bf16_f32 v73, v98, v99\n\tbuffer_load_dwordx4 v[164:167], v8, s[8:11], s68 offen offset:2048 sc1\n\ts_add_u32 s68, s46, 6144\n\tv_exp_f32 v110, v110\n\tv_cvt_pk_bf16_f32 v74, v100, v101\n\tv_exp_f32 v111, v111\n\t" \
    "v_cvt_pk_bf16_f32 v75, v102, v103\n\tv_pk_add_f32 v[104:105], v[104:105], 1.0 op_sel_hi:[1,0]\n\tv_pk_add_f32 v[106:107], v[106:107], 1.0 op_sel_hi:[1,0]\n\tv_pk_add_f32 v[108:109], v[108:109], 1.0 op_sel_hi:[1,0]\n\tv_pk_add_f32 v[110:111], v[110:111], 1.0 op_sel_hi:[1,0]\n\tv_pk_add_f32 v[88:89], v[88:89], v[112:113]\n\tv_rcp_f32 v104, v104\n\tv_pk_add_f32 v[90:91], v[90:91], v[114:115]\n\t" \
    "v_rcp_f32 v105, v105\n\tv_pk_add_f32 v[92:93], v[92:93], v[116:117]\n\tv_rcp_f32 v106, v106\n\tv_pk_add_f32 v[94:95], v[94:95], v[118:119]\n\tv_rcp_f32 v107, v107\n\tv_pk_mul_f32 v[112:113], v[88:89], v[88:89]\n\tv_rcp_f32 v108, v108\n\tv_pk_mul_f32 v[114:115], v[90:91], v[90:91]\n\t" \
    "v_rcp_f32 v109, v109\n\tv_pk_mul_f32 v[116:117], v[92:93], v[92:93]\n\tbuffer_load_dwordx4 v[168:171], v8, s[8:11], s68 offen offset:0 sc1\n\tv_rcp_f32 v110, v110\n\tv_pk_mul_f32 v[118:119], v[94:95], v[94:95]\n\tv_rcp_f32 v111, v111\n\tv_pk_fma_f32 v[112:113], v[112:113], v[246:247], v[246:247] op_sel:[0,0,1] op_sel_hi:[1,0,1]\n\tv_pk_mul_f32 v[104:105], v[104:105], v[80:81]\n\t" \
    "v_pk_mul_f32 v[106:107], v[106:107], v[82:83]\n\tv_pk_mul_f32 v[108:109], v[108:109], v[84:85]\n\tv_pk_mul_f32 v[110:111], v[110:111], v[86:87]\n\tv_cvt_pk_bf16_f32 v80, v104, v105\n\tv_cvt_pk_bf16_f32 v81, v106, v107\n\tv_cvt_pk_bf16_f32 v82, v108, v109\n\tv_cvt_pk_bf16_f32 v83, v110, v111\n\tv_pk_fma_f32 v[114:115], v[114:115], v[246:247], v[246:247] op_sel:[0,0,1] op_sel_hi:[1,0,1]\n\t" \
    "v_pk_fma_f32 v[116:117], v[116:117], v[246:247], v[246:247] op_sel:[0,0,1] op_sel_hi:[1,0,1]\n\tv_pk_fma_f32 v[118:119], v[118:119], v[246:247], v[246:247] op_sel:[0,0,1] op_sel_hi:[1,0,1]\n\tv_pk_mul_f32 v[112:113], v[112:113], v[88:89]\n\tv_pk_mul_f32 v[114:115], v[114:115], v[90:91]\n\tv_pk_mul_f32 v[116:117], v[116:117], v[92:93]\n\tv_pk_mul_f32 v[118:119], v[118:119], v[94:95]\n\tv_exp_f32 v112, v112\n\tv_exp_f32 v113, v113\n\t" \
    "v_exp_f32 v114, v114\n\tv_exp_f32 v115, v115\n\tv_exp_f32 v116, v116\n\tbuffer_load_dwordx4 v[172:175], v8, s[8:11], s68 offen offset:1024 sc1\n\tv_exp_f32 v117, v117\n\tv_exp_f32 v118, v118\n\tv_exp_f32 v119, v119\n\tv_pk_add_f32 v[112:113], v[112:113], 1.0 op_sel_hi:[1,0]\n\t" \
    "v_pk_add_f32 v[114:115], v[114:115], 1.0 op_sel_hi:[1,0]\n\tv_pk_add_f32 v[116:117], v[116:117], 1.0 op_sel_hi:[1,0]\n\tv_pk_add_f32 v[118:119], v[118:119], 1.0 op_sel_hi:[1,0]\n\tv_rcp_f32 v112, v112\n\tv_rcp_f32 v113, v113\n\tv_rcp_f32 v114, v114\n\tv_rcp_f32 v115, v115\n\tv_rcp_f32 v116, v116\n\t" \
    "v_rcp_f32 v117, v117\n\tv_rcp_f32 v118, v118\n\tv_rcp_f32 v119, v119\n\tv_pk_mul_f32 v[112:113], v[112:113], v[88:89]\n\tv_pk_mul_f32 v[114:115], v[114:115], v[90:91]\n\tv_pk_mul_f32 v[116:117], v[116:117], v[92:93]\n\tv_pk_mul_f32 v[118:119], v[118:119], v[94:95]\n\tbuffer_load_dwordx4 v[176:179], v8, s[8:11], s68 offen offset:2048 sc1\n\t" \
    "v_cvt_pk_bf16_f32 v88, v112, v113\n\tv_cvt_pk_bf16_f32 v89, v114, v115\n\tv_cvt_pk_bf16_f32 v90, v116, v117\n\tv_cvt_pk_bf16_f32 v91, v118, v119\n\tbuffer_store_dwordx4 v[72:75], v8, s[8:11], s73 offen offset:0 sc1\n\tbuffer_store_dwordx4 v[80:83], v8, s[8:11], s73 offen offset:1024 sc1\n\tbuffer_store_dwordx4 v[88:91], v8, s[8:11], s73 offen offset:2048 sc1\n\ts_cmp_lg_u32 s39, 2\n\t" \
    "s_cbranch_scc1 102f\n\t107:\n\ts_waitcnt vmcnt(9)\n\ts_barrier\n\tv_mfma_f32_16x16x32_bf16 v[72:75], v[144:147], v[16:19], 0\n\tds_read_b64_tr_b16 v[36:37], v225 offset:0\n\tds_read_b64_tr_b16 v[38:39], v225 offset:2048\n\ts_add_u32 s68, s46, 9216\n\t" \
    "v_mfma_f32_16x16x32_bf16 v[80:83], v[148:151], v[16:19], 0\n\tv_mfma_f32_16x16x32_bf16 v[88:91], v[152:155], v[16:19], 0\n\tv_mfma_f32_16x16x32_bf16 v[76:79], v[144:147], v[20:23], 0\n\tds_read_b64_tr_b16 v[40:41], v226 offset:0\n\tds_read_b64_tr_b16 v[42:43], v226 offset:2048\n\tv_mfma_f32_16x16x32_bf16 v[84:87], v[148:151], v[20:23], 0\n\tbuffer_load_dwordx4 v[192:195], v8, s[8:11], s68 offen offset:0 sc1\n\tv_mfma_f32_16x16x32_bf16 v[92:95], v[152:155], v[20:23], 0\n\t" \
    "v_mfma_f32_16x16x32_bf16 v[96:99], v[144:147], v[24:27], 0\n\tds_read_b64_tr_b16 v[44:45], v227 offset:0\n\tds_read_b64_tr_b16 v[46:47], v227 offset:2048\n\tv_mfma_f32_16x16x32_bf16 v[104:107], v[148:151], v[24:27], 0\n\tbuffer_load_dwordx4 v[196:199], v8, s[8:11], s68 offen offset:1024 sc1\n\tv_mfma_f32_16x16x32_bf16 v[112:115], v[152:155], v[24:27], 0\n\ts_waitcnt lgkmcnt(8)\n\tv_mfma_f32_16x16x32_bf16 v[100:103], v[144:147], v[28:31], 0\n\t" \
    "ds_read_b64_tr_b16 v[16:17], v4 offset:4096\n\tds_read_b64_tr_b16 v[18:19], v4 offset:6144\n\tv_mfma_f32_16x16x32_bf16 v[108:111], v[148:151], v[28:31], 0\n\tv_mfma_f32_16x16x32_bf16 v[116:119], v[152:155], v[28:31], 0\n\ts_waitcnt lgkmcnt(8)\n\tv_mfma_f32_16x16x32_bf16 v[120:123], v[144:147], v[32:35], 0\n\tds_read_b64_tr_b16 v[20:21], v5 offset:4096\n\tds_read_b64_tr_b16 v[22:23], v5 offset:6144\n\t" \
    "v_mfma_f32_16x16x32_bf16 v[128:131], v[148:151], v[32:35], 0\n\tbuffer_load_dwordx4 v[200:203], v8, s[8:11], s68 offen offset:2048 sc1\n\ts_add_u32 s68, s46, 12288\n\tv_mfma_f32_16x16x32_bf16 v[136:139], v[152:155], v[32:35], 0\n\ts_waitcnt lgkmcnt(8)\n\tv_mfma_f32_16x16x32_bf16 v[124:127], v[144:147], v[36:39], 0\n\tds_read_b64_tr_b16 v[24:25], v6 offset:4096\n\tds_read_b64_tr_b16 v[26:27], v6 offset:6144\n\t" \
    "v_mfma_f32_16x16x32_bf16 v[132:135], v[148:151], v[36:39], 0\n\tbuffer_load_dwordx4 v[216:219], v8, s[8:11], s68 offen offset:0 sc1\n\tv_mfma_f32_16x16x32_bf16 v[140:143], v[152:155], v[36:39], 0\n\ts_waitcnt lgkmcnt(8)\n\tv_mfma_f32_16x16x32_bf16 v[48:51], v[144:147], v[40:43], v[228:231]\n\tds_read_b64_tr_b16 v[28:29], v7 offset:4096\n\tds_read_b64_tr_b16 v[30:31], v7 offset:6144\n\tv_mfma_f32_16x16x32_bf16 v[56:59], v[148:151], v[40:43], v[228:231]\n\t" \
    "buffer_load_dwordx4 v[204:207], v8, s[8:11], s68 offen offset:1024 sc1\n\tv_mfma_f32_16x16x32_bf16 v[64:67], v[152:155], v[40:43], v[228:231]\n\ts_waitcnt lgkmcnt(8)\n\tv_mfma_f32_16x16x32_bf16 v[52:55], v[144:147], v[44:47], v[232:235]\n\tds_read_b64_tr_b16 v[32:33], v224 offset:4096\n\tds_read_b64_tr_b16 v[34:35], v224 offset:6144\n\tv_mfma_f32_16x16x32_bf16 v[60:63], v[148:151], v[44:47], v[232:235]\n\tv_mfma_f32_16x16x32_bf16 v[68:71], v[152:155], v[44:47], v[232:235]\n\t" \
    "s_waitcnt vmcnt(11) lgkmcnt(8)\n\tv_mfma_f32_16x16x32_bf16 v[72:75], v[156:159], v[16:19], v[72:75]\n\tds_read_b64_tr_b16 v[36:37], v225 offset:4096\n\tds_read_b64_tr_b16 v[38:39], v225 offset:6144\n\tv_mfma_f32_16x16x32_bf16 v[80:83], v[160:163], v[16:19], v[80:83]\n\tbuffer_load_dwordx4 v[208:211], v8, s[8:11], s68 offen offset:2048 sc1\n\ts_add_u32 s68, s46, 15360\n\tv_mfma_f32_16x16x32_bf16 v[88:91], v[164:167], v[16:19], v[88:91]\n\t" \
    "s_waitcnt lgkmcnt(8)\n\tv_mfma_f32_16x16x32_bf16 v[76:79], v[156:159], v[20:23], v[76:79]\n\tds_read_b64_tr_b16 v[40:41], v226 offset:4096\n\tds_read_b64_tr_b16 v[42:43], v226 offset:6144\n\tv_mfma_f32_16x16x32_bf16 v[84:87], v[160:163], v[20:23], v[84:87]\n\tbuffer_load_dwordx4 v[212:215], v8, s[8:11], s68 offen offset:0 sc1\n\tv_mfma_f32_16x16x32_bf16 v[92:95], v[164:167], v[20:23], v[92:95]\n\ts_waitcnt lgkmcnt(8)\n\t" \
    "v_mfma_f32_16x16x32_bf16 v[96:99], v[156:159], v[24:27], v[96:99]\n\tds_read_b64_tr_b16 v[44:45], v227 offset:4096\n\tds_read_b64_tr_b16 v[46:47], v227 offset:6144\n\tv_mfma_f32_16x16x32_bf16 v[104:107], v[160:163], v[24:27], v[104:107]\n\tbuffer_load_dwordx4 v[144:147], v8, s[8:11], s68 offen offset:1024 sc1\n\tv_mfma_f32_16x16x32_bf16 v[112:115], v[164:167], v[24:27], v[112:115]\n\ts_waitcnt lgkmcnt(8)\n\tv_mfma_f32_16x16x32_bf16 v[100:103], v[156:159], v[28:31], v[100:103]\n\t" \
    "ds_read_b64_tr_b16 v[16:17], v4 offset:8192\n\tds_read_b64_tr_b16 v[18:19], v4 offset:10240\n\tv_mfma_f32_16x16x32_bf16 v[108:111], v[160:163], v[28:31], v[108:111]\n\tv_mfma_f32_16x16x32_bf16 v[116:119], v[164:167], v[28:31], v[116:119]\n\ts_waitcnt lgkmcnt(8)\n\tv_mfma_f32_16x16x32_bf16 v[120:123], v[156:159], v[32:35], v[120:123]\n\tds_read_b64_tr_b16 v[20:21], v5 offset:8192\n\tds_read_b64_tr_b16 v[22:23], v5 offset:10240\n\t" \
    "v_mfma_f32_16x16x32_bf16 v[128:131], v[160:163], v[32:35], v[128:131]\n\tbuffer_load_dwordx4 v[148:151], v8, s[8:11], s68 offen offset:2048 sc1\n\ts_add_u32 s68, s46, 18432\n\tv_mfma_f32_16x16x32_bf16 v[136:139], v[164:167], v[32:35], v[136:139]\n\ts_waitcnt lgkmcnt(8)\n\tv_mfma_f32_16x16x32_bf16 v[124:127], v[156:159], v[36:39], v[124:127]\n\tds_read_b64_tr_b16 v[24:25], v6 offset:8192\n\tds_read_b64_tr_b16 v[26:27], v6 offset:10240\n\t" \
    "v_mfma_f32_16x16x32_bf16 v[132:135], v[160:163], v[36:39], v[132:135]\n\tbuffer_load_dwordx4 v[152:155], v8, s[8:11], s68 offen offset:0 sc1\n\tv_mfma_f32_16x16x32_bf16 v[140:143], v[164:167], v[36:39], v[140:143]\n\ts_waitcnt lgkmcnt(8)\n\tv_mfma_f32_16x16x32_bf16 v[48:51], v[156:159], v[40:43], v[48:51]\n\tds_read_b64_tr_b16 v[28:29], v7 offset:8192\n\tds_read_b64_tr_b16 v[30:31], v7 offset:10240\n\tv_mfma_f32_16x16x32_bf16 v[56:59], v[160:163], v[40:43], v[56:59]\n\t" \
    "v_mfma_f32_16x16x32_bf16 v[64:67], v[164:167], v[40:43], v[64:67]\n\ts_waitcnt lgkmcnt(8)\n\tv_mfma_f32_16x16x32_bf16 v[52:55], v[156:159], v[44:47], v[52:55]\n\tds_read_b64_tr_b16 v[32:33], v224 offset:8192\n\tds_read_b64_tr_b16 v[34:35], v224 offset:10240\n\tv_mfma_f32_16x16x32_bf16 v[60:63], v[160:163], v[44:47], v[60:63]\n\tbuffer_load_dwordx4 v[180:183], v8, s[8:11], s68 offen offset:1024 sc1\n\tv_mfma_f32_16x16x32_bf16 v[68:71], v[164:167], v[44:47], v[68:71]\n\t" \
    "s_barrier\n\ts_waitcnt vmcnt(14) lgkmcnt(8)\n\tv_mfma_f32_16x16x32_bf16 v[72:75], v[168:171], v[16:19], v[72:75]\n\tds_read_b64_tr_b16 v[36:37], v225 offset:8192\n\tds_read_b64_tr_b16 v[38:39], v225 offset:10240\n\tv_mfma_f32_16x16x32_bf16 v[80:83], v[172:175], v[16:19], v[80:83]\n\tbuffer_load_dwordx4 v[184:187], v8, s[8:11], s68 offen offset:2048 sc1\n\ts_add_u32 s68, s46, 21504\n\t" \
    "v_mfma_f32_16x16x32_bf16 v[88:91], v[176:179], v[16:19], v[88:91]\n\ts_waitcnt lgkmcnt(8)\n\tv_mfma_f32_16x16x32_bf16 v[76:79], v[168:171], v[20:23], v[76:79]\n\tds_read_b64_tr_b16 v[40:41], v226 offset:8192\n\tds_read_b64_tr_b16 v[42:43], v226 offset:10240\n\tv_mfma_f32_16x16x32_bf16 v[84:87], v[172:175], v[20:23], v[84:87]\n\tbuffer_load_dwordx4 v[156:159], v8, s[8:11], s68 offen offset:0 sc1\n\tv_mfma_f32_16x16x32_bf16 v[92:95], v[176:179], v[20:23], v[92:95]\n\t" \
    "s_waitcnt lgkmcnt(8)\n\tv_mfma_f32_16x16x32_bf16 v[96:99], v[168:171], v[24:27], v[96:99]\n\tds_read_b64_tr_b16 v[44:45], v227 offset:8192\n\tds_read_b64_tr_b16 v[46:47], v227 offset:10240\n\tv_mfma_f32_16x16x32_bf16 v[104:107], v[172:175], v[24:27], v[104:107]\n\tbuffer_load_dwordx4 v[160:163], v8, s[8:11], s68 offen offset:1024 sc1\n\tv_mfma_f32_16x16x32_bf16 v[112:115], v[176:179], v[24:27], v[112:115]\n\ts_waitcnt lgkmcnt(8)\n\t" \
    "v_mfma_f32_16x16x32_bf16 v[100:103], v[168:171], v[28:31], v[100:103]\n\tds_read_b64_tr_b16 v[16:17], v4 offset:12288\n\tds_read_b64_tr_b16 v[18:19], v4 offset:14336\n\tv_mfma_f32_16x16x32_bf16 v[108:111], v[172:175], v[28:31], v[108:111]\n\tbuffer_load_dwordx4 v[164:167], v8, s[8:11], s68 offen offset:2048 sc1\n\ts_add_u32 s68, s46, 24576\n\tv_mfma_f32_16x16x32_bf16 v[116:119], v[176:179], v[28:31], v[116:119]\n\ts_waitcnt lgkmcnt(8)\n\t" \
    "v_mfma_f32_16x16x32_bf16 v[120:123], v[168:171], v[32:35], v[120:123]\n\tds_read_b64_tr_b16 v[20:21], v5 offset:12288\n\tds_read_b64_tr_b16 v[22:23], v5 offset:14336\n\tv_mfma_f32_16x16x32_bf16 v[128:131], v[172:175], v[32:35], v[128:131]\n\tv_mfma_f32_16x16x32_bf16 v[136:139], v[176:179], v[32:35], v[136:139]\n\ts_waitcnt lgkmcnt(8)\n\tv_mfma_f32_16x16x32_bf16 v[124:127], v[168:171], v[36:39], v[124:127]\n\tds_read_b64_tr_b16 v[24:25], v6 offset:12288\n\t" \
    "ds_read_b64_tr_b16 v[26:27], v6 offset:14336\n\tv_mfma_f32_16x16x32_bf16 v[132:135], v[172:175], v[36:39], v[132:135]\n\tbuffer_load_dwordx4 v[188:191], v8, s[8:11], s68 offen offset:0 sc1\n\tv_mfma_f32_16x16x32_bf16 v[140:143], v[176:179], v[36:39], v[140:143]\n\ts_waitcnt lgkmcnt(8)\n\tv_mfma_f32_16x16x32_bf16 v[48:51], v[168:171], v[40:43], v[48:51]\n\tds_read_b64_tr_b16 v[28:29], v7 offset:12288\n\tds_read_b64_tr_b16 v[30:31], v7 offset:14336\n\t" \
    "v_mfma_f32_16x16x32_bf16 v[56:59], v[172:175], v[40:43], v[56:59]\n\tbuffer_load_dwordx4 a[0:3], v8, s[8:11], s68 offen offset:1024 sc1\n\tv_mfma_f32_16x16x32_bf16 v[64:67], v[176:179], v[40:43], v[64:67]\n\ts_waitcnt lgkmcnt(8)\n\tv_mfma_f32_16x16x32_bf16 v[52:55], v[168:171], v[44:47], v[52:55]\n\tds_read_b64_tr_b16 v[32:33], v224 offset:12288\n\tds_read_b64_tr_b16 v[34:35], v224 offset:14336\n\tv_mfma_f32_16x16x32_bf16 v[60:63], v[172:175], v[44:47], v[60:63]\n\t" \
    "buffer_load_dwordx4 a[4:7], v8, s[8:11], s68 offen offset:2048 sc1\n\ts_add_u32 s68, s46, 27648\n\tv_mfma_f32_16x16x32_bf16 v[68:71], v[176:179], v[44:47], v[68:71]\n\ts_waitcnt vmcnt(18)\n\ts_add_u32 s73, s49, 4096\n\tbuffer_store_dwordx2 v[248:249], off, s[8:11], s73 sc1\n\ts_waitcnt vmcnt(16) lgkmcnt(8)\n\tv_mfma_f32_16x16x32_bf16 v[72:75], v[192:195], v[16:19], v[72:75]\n\t" \
    "ds_read_b64_tr_b16 v[36:37], v225 offset:12288\n\tds_read_b64_tr_b16 v[38:39], v225 offset:14336\n\tv_mfma_f32_16x16x32_bf16 v[80:83], v[196:199], v[16:19], v[80:83]\n\tbuffer_load_dwordx4 a[8:11], v8, s[8:11], s68 offen offset:0 sc1\n\tv_mfma_f32_16x16x32_bf16 v[88:91], v[200:203], v[16:19], v[88:91]\n\ts_waitcnt lgkmcnt(8)\n\tv_mfma_f32_16x16x32_bf16 v[76:79], v[192:195], v[20:23], v[76:79]\n\tds_read_b64_tr_b16 v[40:41], v226 offset:12288\n\t" \
    "ds_read_b64_tr_b16 v[42:43], v226 offset:14336\n\tv_mfma_f32_16x16x32_bf16 v[84:87], v[196:199], v[20:23], v[84:87]\n\tbuffer_load_dwordx4 a[12:15], v8, s[8:11], s68 offen offset:1024 sc1\n\tv_mfma_f32_16x16x32_bf16 v[92:95], v[200:203], v[20:23], v[92:95]\n\ts_waitcnt lgkmcnt(8)\n\tv_mfma_f32_16x16x32_bf16 v[96:99], v[192:195], v[24:27], v[96:99]\n\tds_read_b64_tr_b16 v[44:45], v227 offset:12288\n\tds_read_b64_tr_b16 v[46:47], v227 offset:14336\n\t" \
    "v_mfma_f32_16x16x32_bf16 v[104:107], v[196:199], v[24:27], v[104:107]\n\tbuffer_load_dwordx4 a[16:19], v8, s[8:11], s68 offen offset:2048 sc1\n\ts_add_u32 s68, s46, 30720\n\tv_mfma_f32_16x16x32_bf16 v[112:115], v[200:203], v[24:27], v[112:115]\n\ts_waitcnt lgkmcnt(8)\n\tv_mfma_f32_16x16x32_bf16 v[100:103], v[192:195], v[28:31], v[100:103]\n\tds_read_b64_tr_b16 v[16:17], v4 offset:16384\n\tds_read_b64_tr_b16 v[18:19], v4 offset:18432\n\t" \
    "v_mfma_f32_16x16x32_bf16 v[108:111], v[196:199], v[28:31], v[108:111]\n\tbuffer_load_dwordx4 v[168:171], v8, s[8:11], s68 offen offset:0 sc1\n\tv_mfma_f32_16x16x32_bf16 v[116:119], v[200:203], v[28:31], v[116:119]\n\ts_waitcnt lgkmcnt(8)\n\tv_mfma_f32_16x16x32_bf16 v[120:123], v[192:195], v[32:35], v[120:123]\n\tds_read_b64_tr_b16 v[20:21], v5 offset:16384\n\tds_read_b64_tr_b16 v[22:23], v5 offset:18432\n\tv_mfma_f32_16x16x32_bf16 v[128:131], v[196:199], v[32:35], v[128:131]\n\t" \
    "buffer_load_dwordx4 v[172:175], v8, s[8:11], s68 offen offset:1024 sc1\n\tv_mfma_f32_16x16x32_bf16 v[136:139], v[200:203], v[32:35], v[136:139]\n\ts_waitcnt lgkmcnt(8)\n\tv_mfma_f32_16x16x32_bf16 v[124:127], v[192:195], v[36:39], v[124:127]\n\tds_read_b64_tr_b16 v[24:25], v6 offset:16384\n\tds_read_b64_tr_b16 v[26:27], v6 offset:18432\n\tv_mfma_f32_16x16x32_bf16 v[132:135], v[196:199], v[36:39], v[132:135]\n\tbuffer_load_dwordx4 v[176:179], v8, s[8:11], s68 offen offset:2048 sc1\n\t" \
    "s_add_u32 s68, s46, 33792\n\tv_mfma_f32_16x16x32_bf16 v[140:143], v[200:203], v[36:39], v[140:143]\n\ts_waitcnt lgkmcnt(8)\n\tv_mfma_f32_16x16x32_bf16 v[48:51], v[192:195], v[40:43], v[48:51]\n\tds_read_b64_tr_b16 v[28:29], v7 offset:16384\n\tds_read_b64_tr_b16 v[30:31], v7 offset:18432\n\tv_mfma_f32_16x16x32_bf16 v[56:59], v[196:199], v[40:43], v[56:59]\n\tv_mfma_f32_16x16x32_bf16 v[64:67], v[200:203], v[40:43], v[64:67]\n\t" \
    "s_waitcnt lgkmcnt(8)\n\tv_mfma_f32_16x16x32_bf16 v[52:55], v[192:195], v[44:47], v[52:55]\n\tds_read_b64_tr_b16 v[32:33], v224 offset:16384\n\tds_read_b64_tr_b16 v[34:35], v224 offset:18432\n\tv_mfma_f32_16x16x32_bf16 v[60:63], v[196:199], v[44:47], v[60:63]\n\tbuffer_load_dwordx4 a[20:23], v8, s[8:11], s68 offen offset:0 sc1\n\tv_mfma_f32_16x16x32_bf16 v[68:71], v[200:203], v[44:47], v[68:71]\n\ts_barrier\n\t" \
    "s_waitcnt vmcnt(20) lgkmcnt(8)\n\tv_mfma_f32_16x16x32_bf16 v[72:75], v[216:219], v[16:19], v[72:75]\n\tds_read_b64_tr_b16 v[36:37], v225 offset:16384\n\tds_read_b64_tr_b16 v[38:39], v225 offset:18432\n\tv_mfma_f32_16x16x32_bf16 v[80:83], v[204:207], v[16:19], v[80:83]\n\tbuffer_load_dwordx4 a[24:27], v8, s[8:11], s68 offen offset:1024 sc1\n\tv_mfma_f32_16x16x32_bf16 v[88:91], v[208:211], v[16:19], v[88:91]\n\ts_waitcnt lgkmcnt(8)\n\t" \
    "v_mfma_f32_16x16x32_bf16 v[76:79], v[216:219], v[20:23], v[76:79]\n\tds_read_b64_tr_b16 v[40:41], v226 offset:16384\n\tds_read_b64_tr_b16 v[42:43], v226 offset:18432\n\tv_mfma_f32_16x16x32_bf16 v[84:87], v[204:207], v[20:23], v[84:87]\n\tbuffer_load_dwordx4 a[28:31], v8, s[8:11], s68 offen offset:2048 sc1\n\ts_mov_b32 s69, 3145728\n\ts_add_u32 m0, s54, 53248\n\tv_mfma_f32_16x16x32_bf16 v[92:95], v[208:211], v[20:23], v[92:95]\n\t" \
    "s_waitcnt lgkmcnt(8)\n\tv_mfma_f32_16x16x32_bf16 v[96:99], v[216:219], v[24:27], v[96:99]\n\tds_read_b64_tr_b16 v[44:45], v227 offset:16384\n\tds_read_b64_tr_b16 v[46:47], v227 offset:18432\n\tv_mfma_f32_16x16x32_bf16 v[104:107], v[204:207], v[24:27], v[104:107]\n\tbuffer_load_dwordx4 v12, s[16:19], s69 offen lds\n\ts_mov_b32 s69, 3145728\n\ts_add_u32 m0, s54, 118784\n\t" \
    "v_mfma_f32_16x16x32_bf16 v[112:115], v[208:211], v[24:27], v[112:115]\n\ts_waitcnt lgkmcnt(8)\n\tv_mfma_f32_16x16x32_bf16 v[100:103], v[216:219], v[28:31], v[100:103]\n\tds_read_b64_tr_b16 v[16:17], v4 offset:20480\n\tds_read_b64_tr_b16 v[18:19], v4 offset:22528\n\tv_mfma_f32_16x16x32_bf16 v[108:111], v[204:207], v[28:31], v[108:111]\n\tbuffer_load_dwordx4 v13, s[16:19], s69 offen lds\n\ts_mov_b32 s69, 3801088\n\t" \
    "s_add_u32 m0, s54, 57344\n\tv_mfma_f32_16x16x32_bf16 v[116:119], v[208:211], v[28:31], v[116:119]\n\ts_waitcnt lgkmcnt(8)\n\tv_mfma_f32_16x16x32_bf16 v[120:123], v[216:219], v[32:35], v[120:123]\n\tds_read_b64_tr_b16 v[20:21], v5 offset:20480\n\tds_read_b64_tr_b16 v[22:23], v5 offset:22528\n\tv_mfma_f32_16x16x32_bf16 v[128:131], v[204:207], v[32:35], v[128:131]\n\tv_mfma_f32_16x16x32_bf16 v[136:139], v[208:211], v[32:35], v[136:139]\n\t" \
    "s_waitcnt lgkmcnt(8)\n\tv_mfma_f32_16x16x32_bf16 v[124:127], v[216:219], v[36:39], v[124:127]\n\tds_read_b64_tr_b16 v[24:25], v6 offset:20480\n\tds_read_b64_tr_b16 v[26:27], v6 offset:22528\n\tv_mfma_f32_16x16x32_bf16 v[132:135], v[204:207], v[36:39], v[132:135]\n\tbuffer_load_dwordx4 v12, s[16:19], s69 offen lds\n\ts_mov_b32 s69, 3801088\n\ts_add_u32 m0, s54, 122880\n\t" \
    "v_mfma_f32_16x16x32_bf16 v[140:143], v[208:211], v[36:39], v[140:143]\n\ts_waitcnt lgkmcnt(8)\n\tv_mfma_f32_16x16x32_bf16 v[48:51], v[216:219], v[40:43], v[48:51]\n\tds_read_b64_tr_b16 v[28:29], v7 offset:20480\n\tds_read_b64_tr_b16 v[30:31], v7 offset:22528\n\tv_mfma_f32_16x16x32_bf16 v[56:59], v[204:207], v[40:43], v[56:59]\n\tbuffer_load_dwordx4 v13, s[16:19], s69 offen lds\n\ts_add_u32 s68, s46, 36864\n\t" \
    "v_mfma_f32_16x16x32_bf16 v[64:67], v[208:211], v[40:43], v[64:67]\n\ts_waitcnt lgkmcnt(8)\n\tv_mfma_f32_16x16x32_bf16 v[52:55], v[216:219], v[44:47], v[52:55]\n\tds_read_b64_tr_b16 v[32:33], v224 offset:20480\n\tds_read_b64_tr_b16 v[34:35], v224 offset:22528\n\tv_mfma_f32_16x16x32_bf16 v[60:63], v[204:207], v[44:47], v[60:63]\n\tv_mfma_f32_16x16x32_bf16 v[68:71], v[208:211], v[44:47], v[68:71]\n\ts_waitcnt vmcnt(23) lgkmcnt(8)\n\t" \
    "v_mfma_f32_16x16x32_bf16 v[72:75], v[212:215], v[16:19], v[72:75]\n\tds_read_b64_tr_b16 v[36:37], v225 offset:20480\n\tds_read_b64_tr_b16 v[38:39], v225 offset:22528\n\tv_mfma_f32_16x16x32_bf16 v[80:83], v[144:147], v[16:19], v[80:83]\n\tbuffer_load_dwordx4 v[192:195], v8, s[8:11], s68 offen offset:0 sc1\n\tv_mfma_f32_16x16x32_bf16 v[88:91], v[148:151], v[16:19], v[88:91]\n\ts_waitcnt lgkmcnt(8)\n\tv_mfma_f32_16x16x32_bf16 v[76:79], v[212:215], v[20:23], v[76:79]\n\t" \
    "ds_read_b64_tr_b16 v[40:41], v226 offset:20480\n\tds_read_b64_tr_b16 v[42:43], v226 offset:22528\n\tv_mfma_f32_16x16x32_bf16 v[84:87], v[144:147], v[20:23], v[84:87]\n\tbuffer_load_dwordx4 v[196:199], v8, s[8:11], s68 offen offset:1024 sc1\n\tv_mfma_f32_16x16x32_bf16 v[92:95], v[148:151], v[20:23], v[92:95]\n\ts_waitcnt lgkmcnt(8)\n\tv_mfma_f32_16x16x32_bf16 v[96:99], v[212:215], v[24:27], v[96:99]\n\tds_read_b64_tr_b16 v[44:45], v227 offset:20480\n\t" \
    "ds_read_b64_tr_b16 v[46:47], v227 offset:22528\n\tv_mfma_f32_16x16x32_bf16 v[104:107], v[144:147], v[24:27], v[104:107]\n\tbuffer_load_dwordx4 v[200:203], v8, s[8:11], s68 offen offset:2048 sc1\n\ts_add_u32 s68, s46, 39936\n\tv_mfma_f32_16x16x32_bf16 v[112:115], v[148:151], v[24:27], v[112:115]\n\ts_waitcnt lgkmcnt(8)\n\tv_mfma_f32_16x16x32_bf16 v[100:103], v[212:215], v[28:31], v[100:103]\n\tds_read_b64_tr_b16 v[16:17], v4 offset:24576\n\t" \
    "ds_read_b64_tr_b16 v[18:19], v4 offset:26624\n\tv_mfma_f32_16x16x32_bf16 v[108:111], v[144:147], v[28:31], v[108:111]\n\tv_mfma_f32_16x16x32_bf16 v[116:119], v[148:151], v[28:31], v[116:119]\n\ts_waitcnt lgkmcnt(8)\n\tv_mfma_f32_16x16x32_bf16 v[120:123], v[212:215], v[32:35], v[120:123]\n\tds_read_b64_tr_b16 v[20:21], v5 offset:24576\n\tds_read_b64_tr_b16 v[22:23], v5 offset:26624\n\tv_mfma_f32_16x16x32_bf16 v[128:131], v[144:147], v[32:35], v[128:131]\n\t" \
    "buffer_load_dwordx4 v[216:219], v8, s[8:11], s68 offen offset:0 sc1\n\tv_mfma_f32_16x16x32_bf16 v[136:139], v[148:151], v[32:35], v[136:139]\n\ts_waitcnt lgkmcnt(8)\n\tv_mfma_f32_16x16x32_bf16 v[124:127], v[212:215], v[36:39], v[124:127]\n\tds_read_b64_tr_b16 v[24:25], v6 offset:24576\n\tds_read_b64_tr_b16 v[26:27], v6 offset:26624\n\tv_mfma_f32_16x16x32_bf16 v[132:135], v[144:147], v[36:39], v[132:135]\n\tbuffer_load_dwordx4 v[204:207], v8, s[8:11], s68 offen offset:1024 sc1\n\t" \
    "v_mfma_f32_16x16x32_bf16 v[140:143], v[148:151], v[36:39], v[140:143]\n\ts_waitcnt lgkmcnt(8)\n\tv_mfma_f32_16x16x32_bf16 v[48:51], v[212:215], v[40:43], v[48:51]\n\tds_read_b64_tr_b16 v[28:29], v7 offset:24576\n\tds_read_b64_tr_b16 v[30:31], v7 offset:26624\n\tv_mfma_f32_16x16x32_bf16 v[56:59], v[144:147], v[40:43], v[56:59]\n\tbuffer_load_dwordx4 v[208:211], v8, s[8:11], s68 offen offset:2048 sc1\n\ts_mov_b32 s69, 3670016\n\t" \
    "s_add_u32 m0, s54, 61440\n\tv_mfma_f32_16x16x32_bf16 v[64:67], v[148:151], v[40:43], v[64:67]\n\ts_waitcnt lgkmcnt(8)\n\tv_mfma_f32_16x16x32_bf16 v[52:55], v[212:215], v[44:47], v[52:55]\n\tds_read_b64_tr_b16 v[32:33], v224 offset:24576\n\tds_read_b64_tr_b16 v[34:35], v224 offset:26624\n\tv_mfma_f32_16x16x32_bf16 v[60:63], v[144:147], v[44:47], v[60:63]\n\tv_mfma_f32_16x16x32_bf16 v[68:71], v[148:151], v[44:47], v[68:71]\n\t" \
    "s_barrier\n\ts_waitcnt vmcnt(26) lgkmcnt(8)\n\tv_mfma_f32_16x16x32_bf16 v[72:75], v[152:155], v[16:19], v[72:75]\n\tds_read_b64_tr_b16 v[36:37], v225 offset:24576\n\tds_read_b64_tr_b16 v[38:39], v225 offset:26624\n\tv_mfma_f32_16x16x32_bf16 v[80:83], v[180:183], v[16:19], v[80:83]\n\tbuffer_load_dwordx4 v12, s[16:19], s69 offen lds\n\ts_mov_b32 s69, 3670016\n\t" \
    "s_add_u32 m0, s54, 126976\n\tv_mfma_f32_16x16x32_bf16 v[88:91], v[184:187], v[16:19], v[88:91]\n\ts_waitcnt lgkmcnt(8)\n\tv_mfma_f32_16x16x32_bf16 v[76:79], v[152:155], v[20:23], v[76:79]\n\tds_read_b64_tr_b16 v[40:41], v226 offset:24576\n\tds_read_b64_tr_b16 v[42:43], v226 offset:26624\n\tv_mfma_f32_16x16x32_bf16 v[84:87], v[180:183], v[20:23], v[84:87]\n\tbuffer_load_dwordx4 v13, s[16:19], s69 offen lds\n\t" \
    "s_mov_b32 s69, 4325376\n\ts_add_u32 m0, s54, 0\n\tv_mfma_f32_16x16x32_bf16 v[92:95], v[184:187], v[20:23], v[92:95]\n\ts_waitcnt lgkmcnt(8)\n\tv_mfma_f32_16x16x32_bf16 v[96:99], v[152:155], v[24:27], v[96:99]\n\tds_read_b64_tr_b16 v[44:45], v227 offset:24576\n\tds_read_b64_tr_b16 v[46:47], v227 offset:26624\n\tv_mfma_f32_16x16x32_bf16 v[104:107], v[180:183], v[24:27], v[104:107]\n\t" \
    "buffer_load_dwordx4 v12, s[16:19], s69 offen lds\n\ts_mov_b32 s69, 4325376\n\ts_add_u32 m0, s54, 65536\n\tv_mfma_f32_16x16x32_bf16 v[112:115], v[184:187], v[24:27], v[112:115]\n\ts_waitcnt lgkmcnt(8)\n\tv_mfma_f32_16x16x32_bf16 v[100:103], v[152:155], v[28:31], v[100:103]\n\tds_read_b64_tr_b16 v[16:17], v4 offset:28672\n\tds_read_b64_tr_b16 v[18:19], v4 offset:30720\n\t" \
    "v_mfma_f32_16x16x32_bf16 v[108:111], v[180:183], v[28:31], v[108:111]\n\tbuffer_load_dwordx4 v13, s[16:19], s69 offen lds\n\ts_mov_b32 s69, 4194304\n\ts_add_u32 m0, s54, 4096\n\tv_mfma_f32_16x16x32_bf16 v[116:119], v[184:187], v[28:31], v[116:119]\n\ts_waitcnt lgkmcnt(8)\n\tv_mfma_f32_16x16x32_bf16 v[120:123], v[152:155], v[32:35], v[120:123]\n\tds_read_b64_tr_b16 v[20:21], v5 offset:28672\n\t" \
    "ds_read_b64_tr_b16 v[22:23], v5 offset:30720\n\tv_mfma_f32_16x16x32_bf16 v[128:131], v[180:183], v[32:35], v[128:131]\n\tbuffer_load_dwordx4 v12, s[16:19], s69 offen lds\n\ts_add_u32 s68, s46, 43008\n\tv_mfma_f32_16x16x32_bf16 v[136:139], v[184:187], v[32:35], v[136:139]\n\ts_waitcnt lgkmcnt(8)\n\tv_mfma_f32_16x16x32_bf16 v[124:127], v[152:155], v[36:39], v[124:127]\n\tds_read_b64_tr_b16 v[24:25], v6 offset:28672\n\t" \
    "ds_read_b64_tr_b16 v[26:27], v6 offset:30720\n\tv_mfma_f32_16x16x32_bf16 v[132:135], v[180:183], v[36:39], v[132:135]\n\tv_mfma_f32_16x16x32_bf16 v[140:143], v[184:187], v[36:39], v[140:143]\n\ts_waitcnt lgkmcnt(8)\n\tv_mfma_f32_16x16x32_bf16 v[48:51], v[152:155], v[40:43], v[48:51]\n\tds_read_b64_tr_b16 v[28:29], v7 offset:28672\n\tds_read_b64_tr_b16 v[30:31], v7 offset:30720\n\tv_mfma_f32_16x16x32_bf16 v[56:59], v[180:183], v[40:43], v[56:59]\n\t" \
    "buffer_load_dwordx4 v[212:215], v8, s[8:11], s68 offen offset:0 sc1\n\tv_mfma_f32_16x16x32_bf16 v[64:67], v[184:187], v[40:43], v[64:67]\n\ts_waitcnt lgkmcnt(8)\n\tv_mfma_f32_16x16x32_bf16 v[52:55], v[152:155], v[44:47], v[52:55]\n\tds_read_b64_tr_b16 v[32:33], v224 offset:28672\n\tds_read_b64_tr_b16 v[34:35], v224 offset:30720\n\tv_mfma_f32_16x16x32_bf16 v[60:63], v[180:183], v[44:47], v[60:63]\n\tbuffer_load_dwordx4 v[144:147], v8, s[8:11], s68 offen offset:1024 sc1\n\t" \
    "v_mfma_f32_16x16x32_bf16 v[68:71], v[184:187], v[44:47], v[68:71]\n\ts_waitcnt vmcnt(30) lgkmcnt(8)\n\tv_mfma_f32_16x16x32_bf16 v[72:75], v[156:159], v[16:19], v[72:75]\n\tds_read_b64_tr_b16 v[36:37], v225 offset:28672\n\tds_read_b64_tr_b16 v[38:39], v225 offset:30720\n\tv_mfma_f32_16x16x32_bf16 v[80:83], v[160:163], v[16:19], v[80:83]\n\tv_mfma_f32_16x16x32_bf16 v[88:91], v[164:167], v[16:19], v[88:91]\n\ts_waitcnt lgkmcnt(8)\n\t" \
    "v_mfma_f32_16x16x32_bf16 v[76:79], v[156:159], v[20:23], v[76:79]\n\tds_read_b64_tr_b16 v[40:41], v226 offset:28672\n\tds_read_b64_tr_b16 v[42:43], v226 offset:30720\n\tv_mfma_f32_16x16x32_bf16 v[84:87], v[160:163], v[20:23], v[84:87]\n\tbuffer_load_dwordx4 v[148:151], v8, s[8:11], s68 offen offset:2048 sc1\n\ts_add_u32 s68, s46, 46080\n\tv_mfma_f32_16x16x32_bf16 v[92:95], v[164:167], v[20:23], v[92:95]\n\ts_add_u32 s50, s50, 4096\n\t" \
    "buffer_load_dwordx2 v[250:251], v241, s[8:11], s50 offen sc1\n\ts_waitcnt lgkmcnt(8)\n\tv_mfma_f32_16x16x32_bf16 v[96:99], v[156:159], v[24:27], v[96:99]\n\tds_read_b64_tr_b16 v[44:45], v227 offset:28672\n\tds_read_b64_tr_b16 v[46:47], v227 offset:30720\n\tv_mfma_f32_16x16x32_bf16 v[104:107], v[160:163], v[24:27], v[104:107]\n\tv_mfma_f32_16x16x32_bf16 v[112:115], v[164:167], v[24:27], v[112:115]\n\ts_waitcnt lgkmcnt(8)\n\t" \
    "v_mfma_f32_16x16x32_bf16 v[100:103], v[156:159], v[28:31], v[100:103]\n\tds_read_b64_tr_b16 v[16:17], v4 offset:32768\n\tds_read_b64_tr_b16 v[18:19], v4 offset:34816\n\tv_mfma_f32_16x16x32_bf16 v[108:111], v[160:163], v[28:31], v[108:111]\n\tbuffer_load_dwordx4 v[152:155], v8, s[8:11], s68 offen offset:0 sc1\n\tv_mfma_f32_16x16x32_bf16 v[116:119], v[164:167], v[28:31], v[116:119]\n\ts_waitcnt lgkmcnt(8)\n\tv_mfma_f32_16x16x32_bf16 v[120:123], v[156:159], v[32:35], v[120:123]\n\t" \
    "ds_read_b64_tr_b16 v[20:21], v5 offset:32768\n\tds_read_b64_tr_b16 v[22:23], v5 offset:34816\n\tv_mfma_f32_16x16x32_bf16 v[128:131], v[160:163], v[32:35], v[128:131]\n\tv_mfma_f32_16x16x32_bf16 v[136:139], v[164:167], v[32:35], v[136:139]\n\ts_waitcnt lgkmcnt(8)\n\tv_mfma_f32_16x16x32_bf16 v[124:127], v[156:159], v[36:39], v[124:127]\n\tds_read_b64_tr_b16 v[24:25], v6 offset:32768\n\tds_read_b64_tr_b16 v[26:27], v6 offset:34816\n\t" \
    "v_mfma_f32_16x16x32_bf16 v[132:135], v[160:163], v[36:39], v[132:135]\n\tbuffer_load_dwordx4 v[180:183], v8, s[8:11], s68 offen offset:1024 sc1\n\tv_mfma_f32_16x16x32_bf16 v[140:143], v[164:167], v[36:39], v[140:143]\n\ts_waitcnt lgkmcnt(8)\n\tv_mfma_f32_16x16x32_bf16 v[48:51], v[156:159], v[40:43], v[48:51]\n\tds_read_b64_tr_b16 v[28:29], v7 offset:32768\n\tds_read_b64_tr_b16 v[30:31], v7 offset:34816\n\tv_mfma_f32_16x16x32_bf16 v[56:59], v[160:163], v[40:43], v[56:59]\n\t" \
    "buffer_load_dwordx4 v[184:187], v8, s[8:11], s68 offen offset:2048 sc1\n\ts_add_u32 s68, s46, 49152\n\tv_mfma_f32_16x16x32_bf16 v[64:67], v[164:167], v[40:43], v[64:67]\n\ts_waitcnt lgkmcnt(8)\n\tv_mfma_f32_16x16x32_bf16 v[52:55], v[156:159], v[44:47], v[52:55]\n\tds_read_b64_tr_b16 v[32:33], v224 offset:32768\n\tds_read_b64_tr_b16 v[34:35], v224 offset:34816\n\tv_mfma_f32_16x16x32_bf16 v[60:63], v[160:163], v[44:47], v[60:63]\n\t" \
    "v_mfma_f32_16x16x32_bf16 v[68:71], v[164:167], v[44:47], v[68:71]\n\ts_barrier\n\ts_waitcnt vmcnt(32) lgkmcnt(8)\n\tv_mfma_f32_16x16x32_bf16 v[72:75], v[188:191], v[16:19], v[72:75]\n\tds_read_b64_tr_b16 v[36:37], v225 offset:32768\n\tds_read_b64_tr_b16 v[38:39], v225 offset:34816\n\tv_mfma_f32_16x16x32_bf16 v[80:83], a[0:3], v[16:19], v[80:83]\n\tbuffer_load_dwordx4 a[32:35], v8, s[8:11], s68 offen offset:0 sc1\n\t" \
    "v_mfma_f32_16x16x32_bf16 v[88:91], a[4:7], v[16:19], v[88:91]\n\ts_waitcnt lgkmcnt(8)\n\tv_mfma_f32_16x16x32_bf16 v[76:79], v[188:191], v[20:23], v[76:79]\n\tds_read_b64_tr_b16 v[40:41], v226 offset:32768\n\tds_read_b64_tr_b16 v[42:43], v226 offset:34816\n\tv_mfma_f32_16x16x32_bf16 v[84:87], a[0:3], v[20:23], v[84:87]\n\tbuffer_load_dwordx4 a[36:39], v8, s[8:11], s68 offen offset:1024 sc1\n\tv_mfma_f32_16x16x32_bf16 v[92:95], a[4:7], v[20:23], v[92:95]\n\t" \
    "s_waitcnt lgkmcnt(8)\n\tv_mfma_f32_16x16x32_bf16 v[96:99], v[188:191], v[24:27], v[96:99]\n\tds_read_b64_tr_b16 v[44:45], v227 offset:32768\n\tds_read_b64_tr_b16 v[46:47], v227 offset:34816\n\tv_mfma_f32_16x16x32_bf16 v[104:107], a[0:3], v[24:27], v[104:107]\n\tbuffer_load_dwordx4 a[40:43], v8, s[8:11], s68 offen offset:2048 sc1\n\ts_mov_b32 s69, 4194304\n\ts_add_u32 m0, s54, 69632\n\t" \
    "v_mfma_f32_16x16x32_bf16 v[112:115], a[4:7], v[24:27], v[112:115]\n\ts_waitcnt lgkmcnt(8)\n\tv_mfma_f32_16x16x32_bf16 v[100:103], v[188:191], v[28:31], v[100:103]\n\tds_read_b64_tr_b16 v[16:17], v4 offset:36864\n\tds_read_b64_tr_b16 v[18:19], v4 offset:38912\n\tv_mfma_f32_16x16x32_bf16 v[108:111], a[0:3], v[28:31], v[108:111]\n\tv_mfma_f32_16x16x32_bf16 v[116:119], a[4:7], v[28:31], v[116:119]\n\ts_waitcnt lgkmcnt(8)\n\t" \
    "v_mfma_f32_16x16x32_bf16 v[120:123], v[188:191], v[32:35], v[120:123]\n\tds_read_b64_tr_b16 v[20:21], v5 offset:36864\n\tds_read_b64_tr_b16 v[22:23], v5 offset:38912\n\tv_mfma_f32_16x16x32_bf16 v[128:131], a[0:3], v[32:35], v[128:131]\n\tbuffer_load_dwordx4 v13, s[16:19], s69 offen lds\n\ts_mov_b32 s69, 393216\n\ts_add_u32 m0, s54, 8192\n\tv_mfma_f32_16x16x32_bf16 v[136:139], a[4:7], v[32:35], v[136:139]\n\t" \
    "s_waitcnt lgkmcnt(8)\n\tv_mfma_f32_16x16x32_bf16 v[124:127], v[188:191], v[36:39], v[124:127]\n\tds_read_b64_tr_b16 v[24:25], v6 offset:36864\n\tds_read_b64_tr_b16 v[26:27], v6 offset:38912\n\tv_mfma_f32_16x16x32_bf16 v[132:135], a[0:3], v[36:39], v[132:135]\n\tbuffer_load_dwordx4 v12, s[16:19], s69 offen lds\n\ts_mov_b32 s69, 393216\n\ts_add_u32 m0, s54, 73728\n\t" \
    "v_mfma_f32_16x16x32_bf16 v[140:143], a[4:7], v[36:39], v[140:143]\n\ts_waitcnt lgkmcnt(8)\n\tv_mfma_f32_16x16x32_bf16 v[48:51], v[188:191], v[40:43], v[48:51]\n\tds_read_b64_tr_b16 v[28:29], v7 offset:36864\n\tds_read_b64_tr_b16 v[30:31], v7 offset:38912\n\tv_mfma_f32_16x16x32_bf16 v[56:59], a[0:3], v[40:43], v[56:59]\n\tv_mfma_f32_16x16x32_bf16 v[64:67], a[4:7], v[40:43], v[64:67]\n\ts_waitcnt lgkmcnt(8)\n\t" \
    "v_mfma_f32_16x16x32_bf16 v[52:55], v[188:191], v[44:47], v[52:55]\n\tds_read_b64_tr_b16 v[32:33], v224 offset:36864\n\tds_read_b64_tr_b16 v[34:35], v224 offset:38912\n\tv_mfma_f32_16x16x32_bf16 v[60:63], a[0:3], v[44:47], v[60:63]\n\tbuffer_load_dwordx4 v13, s[16:19], s69 offen lds\n\ts_mov_b32 s69, 262144\n\ts_add_u32 m0, s54, 12288\n\tv_mfma_f32_16x16x32_bf16 v[68:71], a[4:7], v[44:47], v[68:71]\n\t" \
    "s_waitcnt vmcnt(9)\n\tv_cmp_ne_u64 vcc, s[36:37], v[250:251]\n\ts_cbranch_vccz 111f\n\ts_mov_b32 s71, 1048576\n\t110:\n\ts_sleep 1\n\tbuffer_load_dwordx2 v[250:251], v241, s[8:11], s50 offen sc1\n\ts_waitcnt vmcnt(0)\n\t" \
    "v_cmp_ne_u64 vcc, s[36:37], v[250:251]\n\ts_cbranch_vccz 111f\n\ts_sub_u32 s71, s71, 1\n\ts_cbranch_scc0 110b\n\t111:\n\ts_waitcnt lgkmcnt(8)\n\tv_mfma_f32_16x16x32_bf16 v[72:75], a[8:11], v[16:19], v[72:75]\n\tds_read_b64_tr_b16 v[36:37], v225 offset:36864\n\t" \
    "ds_read_b64_tr_b16 v[38:39], v225 offset:38912\n\tv_mfma_f32_16x16x32_bf16 v[80:83], a[12:15], v[16:19], v[80:83]\n\tv_mfma_f32_16x16x32_bf16 v[88:91], a[16:19], v[16:19], v[88:91]\n\ts_waitcnt lgkmcnt(8)\n\tv_mfma_f32_16x16x32_bf16 v[76:79], a[8:11], v[20:23], v[76:79]\n\tds_read_b64_tr_b16 v[40:41], v226 offset:36864\n\tds_read_b64_tr_b16 v[42:43], v226 offset:38912\n\tv_mfma_f32_16x16x32_bf16 v[84:87], a[12:15], v[20:23], v[84:87]\n\t" \
    "buffer_load_dwordx4 v12, s[16:19], s69 offen lds\n\ts_mov_b32 s69, 262144\n\ts_add_u32 m0, s54, 77824\n\tv_mfma_f32_16x16x32_bf16 v[92:95], a[16:19], v[20:23], v[92:95]\n\ts_waitcnt lgkmcnt(8)\n\tv_mfma_f32_16x16x32_bf16 v[96:99], a[8:11], v[24:27], v[96:99]\n\tds_read_b64_tr_b16 v[44:45], v227 offset:36864\n\tds_read_b64_tr_b16 v[46:47], v227 offset:38912\n\t" \
    "v_mfma_f32_16x16x32_bf16 v[104:107], a[12:15], v[24:27], v[104:107]\n\tbuffer_load_dwordx4 v13, s[16:19], s69 offen lds\n\ts_add_u32 s68, s46, 52224\n\tv_mfma_f32_16x16x32_bf16 v[112:115], a[16:19], v[24:27], v[112:115]\n\ts_waitcnt lgkmcnt(8)\n\tv_mfma_f32_16x16x32_bf16 v[100:103], a[8:11], v[28:31], v[100:103]\n\tds_read_b64_tr_b16 v[16:17], v4 offset:40960\n\tds_read_b64_tr_b16 v[18:19], v4 offset:43008\n\t" \
    "v_mfma_f32_16x16x32_bf16 v[108:111], a[12:15], v[28:31], v[108:111]\n\tbuffer_load_dwordx4 v[156:159], v8, s[8:11], s68 offen offset:0 sc1\n\tv_mfma_f32_16x16x32_bf16 v[116:119], a[16:19], v[28:31], v[116:119]\n\ts_waitcnt lgkmcnt(8)\n\tv_mfma_f32_16x16x32_bf16 v[120:123], a[8:11], v[32:35], v[120:123]\n\tds_read_b64_tr_b16 v[20:21], v5 offset:40960\n\tds_read_b64_tr_b16 v[22:23], v5 offset:43008\n\tv_mfma_f32_16x16x32_bf16 v[128:131], a[12:15], v[32:35], v[128:131]\n\t" \
    "buffer_load_dwordx4 v[160:163], v8, s[8:11], s68 offen offset:1024 sc1\n\tv_mfma_f32_16x16x32_bf16 v[136:139], a[16:19], v[32:35], v[136:139]\n\ts_waitcnt lgkmcnt(8)\n\tv_mfma_f32_16x16x32_bf16 v[124:127], a[8:11], v[36:39], v[124:127]\n\tds_read_b64_tr_b16 v[24:25], v6 offset:40960\n\tds_read_b64_tr_b16 v[26:27], v6 offset:43008\n\tv_mfma_f32_16x16x32_bf16 v[132:135], a[12:15], v[36:39], v[132:135]\n\tbuffer_load_dwordx4 v[164:167], v8, s[8:11], s68 offen offset:2048 sc1\n\t" \
    "s_add_u32 s68, s46, 55296\n\tv_mfma_f32_16x16x32_bf16 v[140:143], a[16:19], v[36:39], v[140:143]\n\ts_waitcnt lgkmcnt(8)\n\tv_mfma_f32_16x16x32_bf16 v[48:51], a[8:11], v[40:43], v[48:51]\n\tds_read_b64_tr_b16 v[28:29], v7 offset:40960\n\tds_read_b64_tr_b16 v[30:31], v7 offset:43008\n\tv_mfma_f32_16x16x32_bf16 v[56:59], a[12:15], v[40:43], v[56:59]\n\tbuffer_load_dwordx4 v[188:191], v8, s[8:11], s68 offen offset:0 sc1\n\t" \
    "v_mfma_f32_16x16x32_bf16 v[64:67], a[16:19], v[40:43], v[64:67]\n\ts_waitcnt lgkmcnt(8)\n\tv_mfma_f32_16x16x32_bf16 v[52:55], a[8:11], v[44:47], v[52:55]\n\tds_read_b64_tr_b16 v[32:33], v224 offset:40960\n\tds_read_b64_tr_b16 v[34:35], v224 offset:43008\n\tv_mfma_f32_16x16x32_bf16 v[60:63], a[12:15], v[44:47], v[60:63]\n\tv_mfma_f32_16x16x32_bf16 v[68:71], a[16:19], v[44:47], v[68:71]\n\ts_barrier\n\t" \
    "s_waitcnt lgkmcnt(8)\n\tv_mfma_f32_16x16x32_bf16 v[72:75], v[168:171], v[16:19], v[72:75]\n\tds_read_b64_tr_b16 v[36:37], v225 offset:40960\n\tds_read_b64_tr_b16 v[38:39], v225 offset:43008\n\tv_mfma_f32_16x16x32_bf16 v[80:83], v[172:175], v[16:19], v[80:83]\n\tbuffer_load_dwordx4 a[0:3], v8, s[8:11], s68 offen offset:1024 sc1\n\tv_mfma_f32_16x16x32_bf16 v[88:91], v[176:179], v[16:19], v[88:91]\n\ts_waitcnt lgkmcnt(8)\n\t" \
    "v_mfma_f32_16x16x32_bf16 v[76:79], v[168:171], v[20:23], v[76:79]\n\tds_read_b64_tr_b16 v[40:41], v226 offset:40960\n\tds_read_b64_tr_b16 v[42:43], v226 offset:43008\n\tv_mfma_f32_16x16x32_bf16 v[84:87], v[172:175], v[20:23], v[84:87]\n\tbuffer_load_dwordx4 a[4:7], v8, s[8:11], s68 offen offset:2048 sc1\n\ts_mov_b32 s69, 917504\n\ts_add_u32 m0, s54, 16384\n\tv_mfma_f32_16x16x32_bf16 v[92:95], v[176:179], v[20:23], v[92:95]\n\t" \
    "s_waitcnt lgkmcnt(8)\n\tv_mfma_f32_16x16x32_bf16 v[96:99], v[168:171], v[24:27], v[96:99]\n\tds_read_b64_tr_b16 v[44:45], v227 offset:40960\n\tds_read_b64_tr_b16 v[46:47], v227 offset:43008\n\tv_mfma_f32_16x16x32_bf16 v[104:107], v[172:175], v[24:27], v[104:107]\n\tbuffer_load_dwordx4 v12, s[16:19], s69 offen lds\n\ts_mov_b32 s69, 917504\n\ts_add_u32 m0, s54, 81920\n\t" \
    "v_mfma_f32_16x16x32_bf16 v[112:115], v[176:179], v[24:27], v[112:115]\n\ts_waitcnt lgkmcnt(8)\n\tv_mfma_f32_16x16x32_bf16 v[100:103], v[168:171], v[28:31], v[100:103]\n\tds_read_b64_tr_b16 v[16:17], v4 offset:45056\n\tds_read_b64_tr_b16 v[18:19], v4 offset:47104\n\tv_mfma_f32_16x16x32_bf16 v[108:111], v[172:175], v[28:31], v[108:111]\n\tbuffer_load_dwordx4 v13, s[16:19], s69 offen lds\n\ts_mov_b32 s69, 786432\n\t" \
    "s_add_u32 m0, s54, 20480\n\tv_mfma_f32_16x16x32_bf16 v[116:119], v[176:179], v[28:31], v[116:119]\n\ts_waitcnt lgkmcnt(8)\n\tv_mfma_f32_16x16x32_bf16 v[120:123], v[168:171], v[32:35], v[120:123]\n\tds_read_b64_tr_b16 v[20:21], v5 offset:45056\n\tds_read_b64_tr_b16 v[22:23], v5 offset:47104\n\tv_mfma_f32_16x16x32_bf16 v[128:131], v[172:175], v[32:35], v[128:131]\n\tbuffer_load_dwordx4 v12, s[16:19], s69 offen lds\n\t" \
    "s_mov_b32 s69, 786432\n\ts_add_u32 m0, s54, 86016\n\tv_mfma_f32_16x16x32_bf16 v[136:139], v[176:179], v[32:35], v[136:139]\n\ts_waitcnt lgkmcnt(8)\n\tv_mfma_f32_16x16x32_bf16 v[124:127], v[168:171], v[36:39], v[124:127]\n\tds_read_b64_tr_b16 v[24:25], v6 offset:45056\n\tds_read_b64_tr_b16 v[26:27], v6 offset:47104\n\tv_mfma_f32_16x16x32_bf16 v[132:135], v[172:175], v[36:39], v[132:135]\n\t" \
    "v_mfma_f32_16x16x32_bf16 v[140:143], v[176:179], v[36:39], v[140:143]\n\ts_waitcnt lgkmcnt(8)\n\tv_mfma_f32_16x16x32_bf16 v[48:51], v[168:171], v[40:43], v[48:51]\n\tds_read_b64_tr_b16 v[28:29], v7 offset:45056\n\tds_read_b64_tr_b16 v[30:31], v7 offset:47104\n\tv_mfma_f32_16x16x32_bf16 v[56:59], v[172:175], v[40:43], v[56:59]\n\tbuffer_load_dwordx4 v13, s[16:19], s69 offen lds\n\ts_mov_b32 s69, 1441792\n\t" \
    "s_add_u32 m0, s54, 24576\n\tv_mfma_f32_16x16x32_bf16 v[64:67], v[176:179], v[40:43], v[64:67]\n\ts_waitcnt lgkmcnt(8)\n\tv_mfma_f32_16x16x32_bf16 v[52:55], v[168:171], v[44:47], v[52:55]\n\tds_read_b64_tr_b16 v[32:33], v224 offset:45056\n\tds_read_b64_tr_b16 v[34:35], v224 offset:47104\n\tv_mfma_f32_16x16x32_bf16 v[60:63], v[172:175], v[44:47], v[60:63]\n\tbuffer_load_dwordx4 v12, s[16:19], s69 offen lds\n\t" \
    "s_add_u32 s68, s46, 58368\n\tv_mfma_f32_16x16x32_bf16 v[68:71], v[176:179], v[44:47], v[68:71]\n\ts_waitcnt lgkmcnt(8)\n\tv_mfma_f32_16x16x32_bf16 v[72:75], a[20:23], v[16:19], v[72:75]\n\tds_read_b64_tr_b16 v[36:37], v225 offset:45056\n\tds_read_b64_tr_b16 v[38:39], v225 offset:47104\n\tv_mfma_f32_16x16x32_bf16 v[80:83], a[24:27], v[16:19], v[80:83]\n\tbuffer_load_dwordx4 a[8:11], v8, s[8:11], s68 offen offset:0 sc1\n\t" \
    "v_mfma_f32_16x16x32_bf16 v[88:91], a[28:31], v[16:19], v[88:91]\n\ts_waitcnt lgkmcnt(8)\n\tv_mfma_f32_16x16x32_bf16 v[76:79], a[20:23], v[20:23], v[76:79]\n\tds_read_b64_tr_b16 v[40:41], v226 offset:45056\n\tds_read_b64_tr_b16 v[42:43], v226 offset:47104\n\tv_mfma_f32_16x16x32_bf16 v[84:87], a[24:27], v[20:23], v[84:87]\n\tv_mfma_f32_16x16x32_bf16 v[92:95], a[28:31], v[20:23], v[92:95]\n\ts_waitcnt lgkmcnt(8)\n\t" \
    "v_mfma_f32_16x16x32_bf16 v[96:99], a[20:23], v[24:27], v[96:99]\n\tds_read_b64_tr_b16 v[44:45], v227 offset:45056\n\tds_read_b64_tr_b16 v[46:47], v227 offset:47104\n\tv_mfma_f32_16x16x32_bf16 v[104:107], a[24:27], v[24:27], v[104:107]\n\tbuffer_load_dwordx4 a[12:15], v8, s[8:11], s68 offen offset:1024 sc1\n\tv_mfma_f32_16x16x32_bf16 v[112:115], a[28:31], v[24:27], v[112:115]\n\ts_waitcnt lgkmcnt(8)\n\tv_mfma_f32_16x16x32_bf16 v[100:103], a[20:23], v[28:31], v[100:103]\n\t" \
    "ds_read_b64_tr_b16 v[16:17], v4 offset:49152\n\tds_read_b64_tr_b16 v[18:19], v4 offset:51200\n\tv_mfma_f32_16x16x32_bf16 v[108:111], a[24:27], v[28:31], v[108:111]\n\tbuffer_load_dwordx4 a[16:19], v8, s[8:11], s68 offen offset:2048 sc1\n\ts_add_u32 s68, s46, 61440\n\tv_mfma_f32_16x16x32_bf16 v[116:119], a[28:31], v[28:31], v[116:119]\n\ts_waitcnt lgkmcnt(8)\n\tv_mfma_f32_16x16x32_bf16 v[120:123], a[20:23], v[32:35], v[120:123]\n\t" \
    "ds_read_b64_tr_b16 v[20:21], v5 offset:49152\n\tds_read_b64_tr_b16 v[22:23], v5 offset:51200\n\tv_mfma_f32_16x16x32_bf16 v[128:131], a[24:27], v[32:35], v[128:131]\n\tv_mfma_f32_16x16x32_bf16 v[136:139], a[28:31], v[32:35], v[136:139]\n\ts_waitcnt lgkmcnt(8)\n\tv_mfma_f32_16x16x32_bf16 v[124:127], a[20:23], v[36:39], v[124:127]\n\tds_read_b64_tr_b16 v[24:25], v6 offset:49152\n\tds_read_b64_tr_b16 v[26:27], v6 offset:51200\n\t" \
    "v_mfma_f32_16x16x32_bf16 v[132:135], a[24:27], v[36:39], v[132:135]\n\tbuffer_load_dwordx4 a[44:47], v8, s[8:11], s68 offen offset:0 sc1\n\tv_mfma_f32_16x16x32_bf16 v[140:143], a[28:31], v[36:39], v[140:143]\n\ts_waitcnt lgkmcnt(8)\n\tv_mfma_f32_16x16x32_bf16 v[48:51], a[20:23], v[40:43], v[48:51]\n\tds_read_b64_tr_b16 v[28:29], v7 offset:49152\n\tds_read_b64_tr_b16 v[30:31], v7 offset:51200\n\tv_mfma_f32_16x16x32_bf16 v[56:59], a[24:27], v[40:43], v[56:59]\n\t" \
    "buffer_load_dwordx4 a[48:51], v8, s[8:11], s68 offen offset:1024 sc1\n\tv_mfma_f32_16x16x32_bf16 v[64:67], a[28:31], v[40:43], v[64:67]\n\ts_waitcnt lgkmcnt(8)\n\tv_mfma_f32_16x16x32_bf16 v[52:55], a[20:23], v[44:47], v[52:55]\n\tds_read_b64_tr_b16 v[32:33], v224 offset:49152\n\tds_read_b64_tr_b16 v[34:35], v224 offset:51200\n\tv_mfma_f32_16x16x32_bf16 v[60:63], a[24:27], v[44:47], v[60:63]\n\tbuffer_load_dwordx4 a[52:55], v8, s[8:11], s68 offen offset:2048 sc1\n\t" \
    "s_add_u32 s68, s46, 64512\n\tv_mfma_f32_16x16x32_bf16 v[68:71], a[28:31], v[44:47], v[68:71]\n\ts_barrier\n\ts_waitcnt lgkmcnt(8)\n\tv_mfma_f32_16x16x32_bf16 v[72:75], v[192:195], v[16:19], v[72:75]\n\tds_read_b64_tr_b16 v[36:37], v225 offset:49152\n\tds_read_b64_tr_b16 v[38:39], v225 offset:51200\n\tv_mfma_f32_16x16x32_bf16 v[80:83], v[196:199], v[16:19], v[80:83]\n\t" \
    "buffer_load_dwordx4 a[56:59], v8, s[8:11], s68 offen offset:0 sc1\n\tv_mfma_f32_16x16x32_bf16 v[88:91], v[200:203], v[16:19], v[88:91]\n\ts_waitcnt lgkmcnt(8)\n\tv_mfma_f32_16x16x32_bf16 v[76:79], v[192:195], v[20:23], v[76:79]\n\tds_read_b64_tr_b16 v[40:41], v226 offset:49152\n\tds_read_b64_tr_b16 v[42:43], v226 offset:51200\n\tv_mfma_f32_16x16x32_bf16 v[84:87], v[196:199], v[20:23], v[84:87]\n\tbuffer_load_dwordx4 a[60:63], v8, s[8:11], s68 offen offset:1024 sc1\n\t" \
    "v_mfma_f32_16x16x32_bf16 v[92:95], v[200:203], v[20:23], v[92:95]\n\ts_waitcnt lgkmcnt(8)\n\tv_mfma_f32_16x16x32_bf16 v[96:99], v[192:195], v[24:27], v[96:99]\n\tds_read_b64_tr_b16 v[44:45], v227 offset:49152\n\tds_read_b64_tr_b16 v[46:47], v227 offset:51200\n\tv_mfma_f32_16x16x32_bf16 v[104:107], v[196:199], v[24:27], v[104:107]\n\tv_mfma_f32_16x16x32_bf16 v[112:115], v[200:203], v[24:27], v[112:115]\n\ts_waitcnt lgkmcnt(8)\n\t" \
    "v_mfma_f32_16x16x32_bf16 v[100:103], v[192:195], v[28:31], v[100:103]\n\tds_read_b64_tr_b16 v[16:17], v4 offset:53248\n\tds_read_b64_tr_b16 v[18:19], v4 offset:55296\n\tv_mfma_f32_16x16x32_bf16 v[108:111], v[196:199], v[28:31], v[108:111]\n\tbuffer_load_dwordx4 a[64:67], v8, s[8:11], s68 offen offset:2048 sc1\n\ts_mov_b32 s69, 1441792\n\ts_add_u32 m0, s54, 90112\n\tv_mfma_f32_16x16x32_bf16 v[116:119], v[200:203], v[28:31], v[116:119]\n\t" \
    "s_waitcnt lgkmcnt(8)\n\tv_mfma_f32_16x16x32_bf16 v[120:123], v[192:195], v[32:35], v[120:123]\n\tds_read_b64_tr_b16 v[20:21], v5 offset:53248\n\tds_read_b64_tr_b16 v[22:23], v5 offset:55296\n\tv_mfma_f32_16x16x32_bf16 v[128:131], v[196:199], v[32:35], v[128:131]\n\tbuffer_load_dwordx4 v13, s[16:19], s69 offen lds\n\ts_mov_b32 s69, 1310720\n\ts_add_u32 m0, s54, 28672\n\t" \
    "v_mfma_f32_16x16x32_bf16 v[136:139], v[200:203], v[32:35], v[136:139]\n\ts_waitcnt lgkmcnt(8)\n\tv_mfma_f32_16x16x32_bf16 v[124:127], v[192:195], v[36:39], v[124:127]\n\tds_read_b64_tr_b16 v[24:25], v6 offset:53248\n\tds_read_b64_tr_b16 v[26:27], v6 offset:55296\n\tv_mfma_f32_16x16x32_bf16 v[132:135], v[196:199], v[36:39], v[132:135]\n\tbuffer_load_dwordx4 v12, s[16:19], s69 offen lds\n\ts_mov_b32 s69, 1310720\n\t" \
    "s_add_u32 m0, s54, 94208\n\tv_mfma_f32_16x16x32_bf16 v[140:143], v[200:203], v[36:39], v[140:143]\n\ts_waitcnt lgkmcnt(8)\n\tv_mfma_f32_16x16x32_bf16 v[48:51], v[192:195], v[40:43], v[48:51]\n\tds_read_b64_tr_b16 v[28:29], v7 offset:53248\n\tds_read_b64_tr_b16 v[30:31], v7 offset:55296\n\tv_mfma_f32_16x16x32_bf16 v[56:59], v[196:199], v[40:43], v[56:59]\n\tv_mfma_f32_16x16x32_bf16 v[64:67], v[200:203], v[40:43], v[64:67]\n\t" \
    "s_waitcnt lgkmcnt(8)\n\tv_mfma_f32_16x16x32_bf16 v[52:55], v[192:195], v[44:47], v[52:55]\n\tds_read_b64_tr_b16 v[32:33], v224 offset:53248\n\tds_read_b64_tr_b16 v[34:35], v224 offset:55296\n\tv_mfma_f32_16x16x32_bf16 v[60:63], v[196:199], v[44:47], v[60:63]\n\tbuffer_load_dwordx4 v13, s[16:19], s69 offen lds\n\ts_mov_b32 s69, 1966080\n\ts_add_u32 m0, s54, 32768\n\t" \
    "v_mfma_f32_16x16x32_bf16 v[68:71], v[200:203], v[44:47], v[68:71]\n\ts_waitcnt lgkmcnt(8)\n\tv_mfma_f32_16x16x32_bf16 v[72:75], v[216:219], v[16:19], v[72:75]\n\tds_read_b64_tr_b16 v[36:37], v225 offset:53248\n\tds_read_b64_tr_b16 v[38:39], v225 offset:55296\n\tv_mfma_f32_16x16x32_bf16 v[80:83], v[204:207], v[16:19], v[80:83]\n\tbuffer_load_dwordx4 v12, s[16:19], s69 offen lds\n\ts_mov_b32 s69, 1966080\n\t" \
    "s_add_u32 m0, s54, 98304\n\tv_mfma_f32_16x16x32_bf16 v[88:91], v[208:211], v[16:19], v[88:91]\n\ts_waitcnt lgkmcnt(8)\n\tv_mfma_f32_16x16x32_bf16 v[76:79], v[216:219], v[20:23], v[76:79]\n\tds_read_b64_tr_b16 v[40:41], v226 offset:53248\n\tds_read_b64_tr_b16 v[42:43], v226 offset:55296\n\tv_mfma_f32_16x16x32_bf16 v[84:87], v[204:207], v[20:23], v[84:87]\n\tbuffer_load_dwordx4 v13, s[16:19], s69 offen lds\n\t" \
    "s_mov_b32 s69, 1835008\n\ts_add_u32 m0, s54, 36864\n\tv_mfma_f32_16x16x32_bf16 v[92:95], v[208:211], v[20:23], v[92:95]\n\ts_waitcnt lgkmcnt(8)\n\tv_mfma_f32_16x16x32_bf16 v[96:99], v[216:219], v[24:27], v[96:99]\n\tds_read_b64_tr_b16 v[44:45], v227 offset:53248\n\tds_read_b64_tr_b16 v[46:47], v227 offset:55296\n\tv_mfma_f32_16x16x32_bf16 v[104:107], v[204:207], v[24:27], v[104:107]\n\t" \
    "v_mfma_f32_16x16x32_bf16 v[112:115], v[208:211], v[24:27], v[112:115]\n\ts_waitcnt lgkmcnt(8)\n\tv_mfma_f32_16x16x32_bf16 v[100:103], v[216:219], v[28:31], v[100:103]\n\tds_read_b64_tr_b16 v[16:17], v4 offset:57344\n\tds_read_b64_tr_b16 v[18:19], v4 offset:59392\n\tv_mfma_f32_16x16x32_bf16 v[108:111], v[204:207], v[28:31], v[108:111]\n\tbuffer_load_dwordx4 v12, s[16:19], s69 offen lds\n\ts_add_u32 s68, s46, 67584\n\t" \
    "v_mfma_f32_16x16x32_bf16 v[116:119], v[208:211], v[28:31], v[116:119]\n\ts_waitcnt lgkmcnt(8)\n\tv_mfma_f32_16x16x32_bf16 v[120:123], v[216:219], v[32:35], v[120:123]\n\tds_read_b64_tr_b16 v[20:21], v5 offset:57344\n\tds_read_b64_tr_b16 v[22:23], v5 offset:59392\n\tv_mfma_f32_16x16x32_bf16 v[128:131], v[204:207], v[32:35], v[128:131]\n\tbuffer_load_dwordx4 a[20:23], v8, s[8:11], s68 offen offset:0 sc1\n\tv_mfma_f32_16x16x32_bf16 v[136:139], v[208:211], v[32:35], v[136:139]\n\t" \
    "s_waitcnt lgkmcnt(8)\n\tv_mfma_f32_16x16x32_bf16 v[124:127], v[216:219], v[36:39], v[124:127]\n\tds_read_b64_tr_b16 v[24:25], v6 offset:57344\n\tds_read_b64_tr_b16 v[26:27], v6 offset:59392\n\tv_mfma_f32_16x16x32_bf16 v[132:135], v[204:207], v[36:39], v[132:135]\n\tv_mfma_f32_16x16x32_bf16 v[140:143], v[208:211], v[36:39], v[140:143]\n\ts_waitcnt lgkmcnt(8)\n\tv_mfma_f32_16x16x32_bf16 v[48:51], v[216:219], v[40:43], v[48:51]\n\t" \
    "ds_read_b64_tr_b16 v[28:29], v7 offset:57344\n\tds_read_b64_tr_b16 v[30:31], v7 offset:59392\n\tv_mfma_f32_16x16x32_bf16 v[56:59], v[204:207], v[40:43], v[56:59]\n\tbuffer_load_dwordx4 a[24:27], v8, s[8:11], s68 offen offset:1024 sc1\n\tv_mfma_f32_16x16x32_bf16 v[64:67], v[208:211], v[40:43], v[64:67]\n\ts_waitcnt lgkmcnt(8)\n\tv_mfma_f32_16x16x32_bf16 v[52:55], v[216:219], v[44:47], v[52:55]\n\tds_read_b64_tr_b16 v[32:33], v224 offset:57344\n\t" \
    "ds_read_b64_tr_b16 v[34:35], v224 offset:59392\n\tv_mfma_f32_16x16x32_bf16 v[60:63], v[204:207], v[44:47], v[60:63]\n\tbuffer_load_dwordx4 a[28:31], v8, s[8:11], s68 offen offset:2048 sc1\n\ts_add_u32 s68, s46, 70656\n\tv_mfma_f32_16x16x32_bf16 v[68:71], v[208:211], v[44:47], v[68:71]\n\ts_barrier\n\ts_waitcnt lgkmcnt(8)\n\tv_mfma_f32_16x16x32_bf16 v[72:75], v[212:215], v[16:19], v[72:75]\n\t" \
    "ds_read_b64_tr_b16 v[36:37], v225 offset:57344\n\tds_read_b64_tr_b16 v[38:39], v225 offset:59392\n\tv_mfma_f32_16x16x32_bf16 v[80:83], v[144:147], v[16:19], v[80:83]\n\tbuffer_load_dwordx4 a[68:71], v8, s[8:11], s68 offen offset:0 sc1\n\tv_mfma_f32_16x16x32_bf16 v[88:91], v[148:151], v[16:19], v[88:91]\n\ts_waitcnt lgkmcnt(8)\n\tv_mfma_f32_16x16x32_bf16 v[76:79], v[212:215], v[20:23], v[76:79]\n\tds_read_b64_tr_b16 v[40:41], v226 offset:57344\n\t" \
    "ds_read_b64_tr_b16 v[42:43], v226 offset:59392\n\tv_mfma_f32_16x16x32_bf16 v[84:87], v[144:147], v[20:23], v[84:87]\n\tbuffer_load_dwordx4 a[72:75], v8, s[8:11], s68 offen offset:1024 sc1\n\tv_mfma_f32_16x16x32_bf16 v[92:95], v[148:151], v[20:23], v[92:95]\n\ts_waitcnt lgkmcnt(8)\n\tv_mfma_f32_16x16x32_bf16 v[96:99], v[212:215], v[24:27], v[96:99]\n\tds_read_b64_tr_b16 v[44:45], v227 offset:57344\n\tds_read_b64_tr_b16 v[46:47], v227 offset:59392\n\t" \
    "v_mfma_f32_16x16x32_bf16 v[104:107], v[144:147], v[24:27], v[104:107]\n\tbuffer_load_dwordx4 a[76:79], v8, s[8:11], s68 offen offset:2048 sc1\n\ts_add_u32 s68, s46, 73728\n\tv_mfma_f32_16x16x32_bf16 v[112:115], v[148:151], v[24:27], v[112:115]\n\ts_waitcnt lgkmcnt(8)\n\tv_mfma_f32_16x16x32_bf16 v[100:103], v[212:215], v[28:31], v[100:103]\n\tds_read_b64_tr_b16 v[16:17], v4 offset:61440\n\tds_read_b64_tr_b16 v[18:19], v4 offset:63488\n\t" \
    "v_mfma_f32_16x16x32_bf16 v[108:111], v[144:147], v[28:31], v[108:111]\n\tv_mfma_f32_16x16x32_bf16 v[116:119], v[148:151], v[28:31], v[116:119]\n\ts_waitcnt lgkmcnt(8)\n\tv_mfma_f32_16x16x32_bf16 v[120:123], v[212:215], v[32:35], v[120:123]\n\tds_read_b64_tr_b16 v[20:21], v5 offset:61440\n\tds_read_b64_tr_b16 v[22:23], v5 offset:63488\n\tv_mfma_f32_16x16x32_bf16 v[128:131], v[144:147], v[32:35], v[128:131]\n\tbuffer_load_dwordx4 a[80:83], v8, s[8:11], s68 offen offset:0 sc1\n\t" \
    "v_mfma_f32_16x16x32_bf16 v[136:139], v[148:151], v[32:35], v[136:139]\n\ts_waitcnt lgkmcnt(8)\n\tv_mfma_f32_16x16x32_bf16 v[124:127], v[212:215], v[36:39], v[124:127]\n\tds_read_b64_tr_b16 v[24:25], v6 offset:61440\n\tds_read_b64_tr_b16 v[26:27], v6 offset:63488\n\tv_mfma_f32_16x16x32_bf16 v[132:135], v[144:147], v[36:39], v[132:135]\n\tbuffer_load_dwordx4 a[84:87], v8, s[8:11], s68 offen offset:1024 sc1\n\tv_mfma_f32_16x16x32_bf16 v[140:143], v[148:151], v[36:39], v[140:143]\n\t" \
    "s_waitcnt lgkmcnt(8)\n\tv_mfma_f32_16x16x32_bf16 v[48:51], v[212:215], v[40:43], v[48:51]\n\tds_read_b64_tr_b16 v[28:29], v7 offset:61440\n\tds_read_b64_tr_b16 v[30:31], v7 offset:63488\n\tv_mfma_f32_16x16x32_bf16 v[56:59], v[144:147], v[40:43], v[56:59]\n\tbuffer_load_dwordx4 a[88:91], v8, s[8:11], s68 offen offset:2048 sc1\n\ts_mov_b32 s69, 1835008\n\ts_add_u32 m0, s54, 102400\n\t" \
    "v_mfma_f32_16x16x32_bf16 v[64:67], v[148:151], v[40:43], v[64:67]\n\ts_waitcnt lgkmcnt(8)\n\tv_mfma_f32_16x16x32_bf16 v[52:55], v[212:215], v[44:47], v[52:55]\n\tds_read_b64_tr_b16 v[32:33], v224 offset:61440\n\tds_read_b64_tr_b16 v[34:35], v224 offset:63488\n\tv_mfma_f32_16x16x32_bf16 v[60:63], v[144:147], v[44:47], v[60:63]\n\tv_mfma_f32_16x16x32_bf16 v[68:71], v[148:151], v[44:47], v[68:71]\n\ts_waitcnt vmcnt(43) lgkmcnt(8)\n\t" \
    "v_mfma_f32_16x16x32_bf16 v[72:75], v[152:155], v[16:19], v[72:75]\n\tds_read_b64_tr_b16 v[36:37], v225 offset:61440\n\tds_read_b64_tr_b16 v[38:39], v225 offset:63488\n\tv_mfma_f32_16x16x32_bf16 v[80:83], v[180:183], v[16:19], v[80:83]\n\tbuffer_load_dwordx4 v13, s[16:19], s69 offen lds\n\ts_mov_b32 s69, 2490368\n\ts_add_u32 m0, s54, 40960\n\tv_mfma_f32_16x16x32_bf16 v[88:91], v[184:187], v[16:19], v[88:91]\n\t" \
    "s_waitcnt lgkmcnt(8)\n\tv_mfma_f32_16x16x32_bf16 v[76:79], v[152:155], v[20:23], v[76:79]\n\tds_read_b64_tr_b16 v[40:41], v4 offset:0\n\tds_read_b64_tr_b16 v[42:43], v4 offset:2048\n\tv_mfma_f32_16x16x32_bf16 v[84:87], v[180:183], v[20:23], v[84:87]\n\tbuffer_load_dwordx4 v12, s[16:19], s69 offen lds\n\ts_mov_b32 s69, 2490368\n\ts_add_u32 m0, s54, 106496\n\t" \
    "v_mfma_f32_16x16x32_bf16 v[92:95], v[184:187], v[20:23], v[92:95]\n\ts_waitcnt lgkmcnt(8)\n\tv_mfma_f32_16x16x32_bf16 v[96:99], v[152:155], v[24:27], v[96:99]\n\tds_read_b64_tr_b16 v[44:45], v5 offset:0\n\tds_read_b64_tr_b16 v[46:47], v5 offset:2048\n\tv_mfma_f32_16x16x32_bf16 v[104:107], v[180:183], v[24:27], v[104:107]\n\tv_mfma_f32_16x16x32_bf16 v[112:115], v[184:187], v[24:27], v[112:115]\n\ts_waitcnt lgkmcnt(8)\n\t" \
    "v_mfma_f32_16x16x32_bf16 v[100:103], v[152:155], v[28:31], v[100:103]\n\tds_read_b64_tr_b16 v[16:17], v6 offset:0\n\tds_read_b64_tr_b16 v[18:19], v6 offset:2048\n\tv_mfma_f32_16x16x32_bf16 v[108:111], v[180:183], v[28:31], v[108:111]\n\tbuffer_load_dwordx4 v13, s[16:19], s69 offen lds\n\ts_mov_b32 s69, 2359296\n\ts_add_u32 m0, s54, 45056\n\tv_mfma_f32_16x16x32_bf16 v[116:119], v[184:187], v[28:31], v[116:119]\n\t" \
    "s_waitcnt lgkmcnt(8)\n\tv_mfma_f32_16x16x32_bf16 v[120:123], v[152:155], v[32:35], v[120:123]\n\tds_read_b64_tr_b16 v[20:21], v7 offset:0\n\tds_read_b64_tr_b16 v[22:23], v7 offset:2048\n\tv_mfma_f32_16x16x32_bf16 v[128:131], v[180:183], v[32:35], v[128:131]\n\tbuffer_load_dwordx4 v12, s[16:19], s69 offen lds\n\ts_mov_b32 s69, 2359296\n\ts_add_u32 m0, s54, 110592\n\t" \
    "v_mfma_f32_16x16x32_bf16 v[136:139], v[184:187], v[32:35], v[136:139]\n\ts_waitcnt lgkmcnt(8)\n\tv_mfma_f32_16x16x32_bf16 v[124:127], v[152:155], v[36:39], v[124:127]\n\tds_read_b64_tr_b16 v[24:25], v224 offset:0\n\tds_read_b64_tr_b16 v[26:27], v224 offset:2048\n\tv_mfma_f32_16x16x32_bf16 v[132:135], v[180:183], v[36:39], v[132:135]\n\tbuffer_load_dwordx4 v13, s[16:19], s69 offen lds\n\ts_add_u32 s68, s46, 76800\n\t" \
    "v_mfma_f32_16x16x32_bf16 v[140:143], v[184:187], v[36:39], v[140:143]\n\ts_waitcnt vmcnt(45) lgkmcnt(8)\n\tv_mfma_f32_16x16x32_bf16 v[72:75], a[32:35], v[40:43], v[72:75]\n\tds_read_b64_tr_b16 v[28:29], v225 offset:0\n\tds_read_b64_tr_b16 v[30:31], v225 offset:2048\n\tv_mfma_f32_16x16x32_bf16 v[80:83], a[36:39], v[40:43], v[80:83]\n\tv_mfma_f32_16x16x32_bf16 v[88:91], a[40:43], v[40:43], v[88:91]\n\ts_waitcnt lgkmcnt(8)\n\t" \
    "v_mfma_f32_16x16x32_bf16 v[76:79], a[32:35], v[44:47], v[76:79]\n\tds_read_b64_tr_b16 v[32:33], v4 offset:4096\n\tds_read_b64_tr_b16 v[34:35], v4 offset:6144\n\tv_mfma_f32_16x16x32_bf16 v[84:87], a[36:39], v[44:47], v[84:87]\n\tbuffer_load_dwordx4 a[92:95], v8, s[8:11], s68 offen offset:0 sc1\n\tv_mfma_f32_16x16x32_bf16 v[92:95], a[40:43], v[44:47], v[92:95]\n\ts_waitcnt vmcnt(41)\n\ts_barrier\n\t" \
    "s_waitcnt lgkmcnt(8)\n\tv_mfma_f32_16x16x32_bf16 v[96:99], a[32:35], v[16:19], v[96:99]\n\tds_read_b64_tr_b16 v[36:37], v5 offset:4096\n\tds_read_b64_tr_b16 v[38:39], v5 offset:6144\n\tv_mfma_f32_16x16x32_bf16 v[104:107], a[36:39], v[16:19], v[104:107]\n\tbuffer_load_dwordx4 a[96:99], v8, s[8:11], s68 offen offset:1024 sc1\n\tv_mfma_f32_16x16x32_bf16 v[112:115], a[40:43], v[16:19], v[112:115]\n\ts_waitcnt lgkmcnt(8)\n\t" \
    "v_mfma_f32_16x16x32_bf16 v[100:103], a[32:35], v[20:23], v[100:103]\n\tds_read_b64_tr_b16 v[40:41], v6 offset:4096\n\tds_read_b64_tr_b16 v[42:43], v6 offset:6144\n\tv_mfma_f32_16x16x32_bf16 v[108:111], a[36:39], v[20:23], v[108:111]\n\tbuffer_load_dwordx4 a[100:103], v8, s[8:11], s68 offen offset:2048 sc1\n\ts_add_u32 s68, s46, 79872\n\tv_mfma_f32_16x16x32_bf16 v[116:119], a[40:43], v[20:23], v[116:119]\n\ts_waitcnt lgkmcnt(8)\n\t" \
    "v_mfma_f32_16x16x32_bf16 v[120:123], a[32:35], v[24:27], v[120:123]\n\tds_read_b64_tr_b16 v[44:45], v7 offset:4096\n\tds_read_b64_tr_b16 v[46:47], v7 offset:6144\n\tv_mfma_f32_16x16x32_bf16 v[128:131], a[36:39], v[24:27], v[128:131]\n\tbuffer_load_dwordx4 a[104:107], v8, s[8:11], s68 offen offset:0 sc1\n\tv_mfma_f32_16x16x32_bf16 v[136:139], a[40:43], v[24:27], v[136:139]\n\ts_waitcnt lgkmcnt(8)\n\tv_mfma_f32_16x16x32_bf16 v[124:127], a[32:35], v[28:31], v[124:127]\n\t" \
    "ds_read_b64_tr_b16 v[16:17], v224 offset:4096\n\tds_read_b64_tr_b16 v[18:19], v224 offset:6144\n\tv_mfma_f32_16x16x32_bf16 v[132:135], a[36:39], v[28:31], v[132:135]\n\tbuffer_load_dwordx4 a[108:111], v8, s[8:11], s68 offen offset:1024 sc1\n\tv_mfma_f32_16x16x32_bf16 v[140:143], a[40:43], v[28:31], v[140:143]\n\ts_waitcnt vmcnt(42) lgkmcnt(8)\n\tv_mfma_f32_16x16x32_bf16 v[72:75], v[156:159], v[32:35], v[72:75]\n\tds_read_b64_tr_b16 v[20:21], v225 offset:4096\n\t" \
    "ds_read_b64_tr_b16 v[22:23], v225 offset:6144\n\tv_mfma_f32_16x16x32_bf16 v[80:83], v[160:163], v[32:35], v[80:83]\n\tv_mfma_f32_16x16x32_bf16 v[88:91], v[164:167], v[32:35], v[88:91]\n\ts_waitcnt lgkmcnt(8)\n\tv_mfma_f32_16x16x32_bf16 v[76:79], v[156:159], v[36:39], v[76:79]\n\tds_read_b64_tr_b16 v[24:25], v4 offset:8192\n\tds_read_b64_tr_b16 v[26:27], v4 offset:10240\n\tv_mfma_f32_16x16x32_bf16 v[84:87], v[160:163], v[36:39], v[84:87]\n\t" \
    "buffer_load_dwordx4 a[112:115], v8, s[8:11], s68 offen offset:2048 sc1\n\ts_mov_b32 s69, 3014656\n\ts_add_u32 m0, s54, 49152\n\tv_mfma_f32_16x16x32_bf16 v[92:95], v[164:167], v[36:39], v[92:95]\n\ts_waitcnt lgkmcnt(8)\n\tv_mfma_f32_16x16x32_bf16 v[96:99], v[156:159], v[40:43], v[96:99]\n\tds_read_b64_tr_b16 v[28:29], v5 offset:8192\n\tds_read_b64_tr_b16 v[30:31], v5 offset:10240\n\t" \
    "v_mfma_f32_16x16x32_bf16 v[104:107], v[160:163], v[40:43], v[104:107]\n\tbuffer_load_dwordx4 v12, s[16:19], s69 offen lds\n\ts_mov_b32 s69, 2883584\n\ts_add_u32 m0, s54, 114688\n\tv_mfma_f32_16x16x32_bf16 v[112:115], v[164:167], v[40:43], v[112:115]\n\ts_waitcnt lgkmcnt(8)\n\tv_mfma_f32_16x16x32_bf16 v[100:103], v[156:159], v[44:47], v[100:103]\n\tds_read_b64_tr_b16 v[32:33], v6 offset:8192\n\t" \
    "ds_read_b64_tr_b16 v[34:35], v6 offset:10240\n\tv_mfma_f32_16x16x32_bf16 v[108:111], v[160:163], v[44:47], v[108:111]\n\tv_mfma_f32_16x16x32_bf16 v[116:119], v[164:167], v[44:47], v[116:119]\n\ts_waitcnt lgkmcnt(8)\n\tv_mfma_f32_16x16x32_bf16 v[120:123], v[156:159], v[16:19], v[120:123]\n\tds_read_b64_tr_b16 v[36:37], v7 offset:8192\n\tds_read_b64_tr_b16 v[38:39], v7 offset:10240\n\tv_mfma_f32_16x16x32_bf16 v[128:131], v[160:163], v[16:19], v[128:131]\n\t" \
    "buffer_load_dwordx4 v12, s[16:19], s69 offen lds\n\ts_mov_b32 s69, 3538944\n\ts_add_u32 m0, s54, 53248\n\tv_mfma_f32_16x16x32_bf16 v[136:139], v[164:167], v[16:19], v[136:139]\n\ts_waitcnt lgkmcnt(8)\n\tv_mfma_f32_16x16x32_bf16 v[124:127], v[156:159], v[20:23], v[124:127]\n\tds_read_b64_tr_b16 v[40:41], v224 offset:8192\n\tds_read_b64_tr_b16 v[42:43], v224 offset:10240\n\t" \
    "v_mfma_f32_16x16x32_bf16 v[132:135], v[160:163], v[20:23], v[132:135]\n\tbuffer_load_dwordx4 v12, s[16:19], s69 offen lds\n\ts_mov_b32 s69, 3407872\n\ts_add_u32 m0, s54, 118784\n\tv_mfma_f32_16x16x32_bf16 v[140:143], v[164:167], v[20:23], v[140:143]\n\ts_waitcnt vmcnt(43) lgkmcnt(8)\n\tv_mfma_f32_16x16x32_bf16 v[72:75], v[188:191], v[24:27], v[72:75]\n\tds_read_b64_tr_b16 v[44:45], v225 offset:8192\n\t" \
    "ds_read_b64_tr_b16 v[46:47], v225 offset:10240\n\tv_mfma_f32_16x16x32_bf16 v[80:83], a[0:3], v[24:27], v[80:83]\n\tv_mfma_f32_16x16x32_bf16 v[88:91], a[4:7], v[24:27], v[88:91]\n\ts_waitcnt lgkmcnt(8)\n\tv_mfma_f32_16x16x32_bf16 v[76:79], v[188:191], v[28:31], v[76:79]\n\tds_read_b64_tr_b16 v[16:17], v4 offset:12288\n\tds_read_b64_tr_b16 v[18:19], v4 offset:14336\n\tv_mfma_f32_16x16x32_bf16 v[84:87], a[0:3], v[28:31], v[84:87]\n\t" \
    "buffer_load_dwordx4 v12, s[16:19], s69 offen lds\n\ts_mov_b32 s69, 4063232\n\ts_add_u32 m0, s54, 57344\n\tv_mfma_f32_16x16x32_bf16 v[92:95], a[4:7], v[28:31], v[92:95]\n\ts_waitcnt lgkmcnt(8)\n\tv_mfma_f32_16x16x32_bf16 v[96:99], v[188:191], v[32:35], v[96:99]\n\tds_read_b64_tr_b16 v[20:21], v5 offset:12288\n\tds_read_b64_tr_b16 v[22:23], v5 offset:14336\n\t" \
    "v_mfma_f32_16x16x32_bf16 v[104:107], a[0:3], v[32:35], v[104:107]\n\tbuffer_load_dwordx4 v12, s[16:19], s69 offen lds\n\ts_mov_b32 s69, 3932160\n\ts_add_u32 m0, s54, 122880\n\tv_mfma_f32_16x16x32_bf16 v[112:115], a[4:7], v[32:35], v[112:115]\n\ts_waitcnt lgkmcnt(8)\n\tv_mfma_f32_16x16x32_bf16 v[100:103], v[188:191], v[36:39], v[100:103]\n\tds_read_b64_tr_b16 v[24:25], v6 offset:12288\n\t" \
    "ds_read_b64_tr_b16 v[26:27], v6 offset:14336\n\tv_mfma_f32_16x16x32_bf16 v[108:111], a[0:3], v[36:39], v[108:111]\n\tv_mfma_f32_16x16x32_bf16 v[116:119], a[4:7], v[36:39], v[116:119]\n\ts_waitcnt lgkmcnt(8)\n\tv_mfma_f32_16x16x32_bf16 v[120:123], v[188:191], v[40:43], v[120:123]\n\tds_read_b64_tr_b16 v[28:29], v7 offset:12288\n\tds_read_b64_tr_b16 v[30:31], v7 offset:14336\n\tv_mfma_f32_16x16x32_bf16 v[128:131], a[0:3], v[40:43], v[128:131]\n\t" \
    "buffer_load_dwordx4 v12, s[16:19], s69 offen lds\n\ts_mov_b32 s69, 4587520\n\ts_add_u32 m0, s54, 61440\n\tv_mfma_f32_16x16x32_bf16 v[136:139], a[4:7], v[40:43], v[136:139]\n\ts_waitcnt lgkmcnt(8)\n\tv_mfma_f32_16x16x32_bf16 v[124:127], v[188:191], v[44:47], v[124:127]\n\tds_read_b64_tr_b16 v[32:33], v224 offset:12288\n\tds_read_b64_tr_b16 v[34:35], v224 offset:14336\n\t" \
    "v_mfma_f32_16x16x32_bf16 v[132:135], a[0:3], v[44:47], v[132:135]\n\tbuffer_load_dwordx4 v12, s[16:19], s69 offen lds\n\ts_add_u32 s68, s46, 82944\n\tv_mfma_f32_16x16x32_bf16 v[140:143], a[4:7], v[44:47], v[140:143]\n\ts_waitcnt vmcnt(42)\n\ts_barrier\n\ts_waitcnt vmcnt(39) lgkmcnt(8)\n\tv_mfma_f32_16x16x32_bf16 v[72:75], a[8:11], v[16:19], v[72:75]\n\t" \
    "ds_read_b64_tr_b16 v[36:37], v225 offset:12288\n\tds_read_b64_tr_b16 v[38:39], v225 offset:14336\n\tv_mfma_f32_16x16x32_bf16 v[80:83], a[12:15], v[16:19], v[80:83]\n\tbuffer_load_dwordx4 a[116:119], v8, s[8:11], s68 offen offset:0 sc1\n\tv_mfma_f32_16x16x32_bf16 v[88:91], a[16:19], v[16:19], v[88:91]\n\ts_waitcnt lgkmcnt(8)\n\tv_mfma_f32_16x16x32_bf16 v[76:79], a[8:11], v[20:23], v[76:79]\n\tds_read_b64_tr_b16 v[40:41], v4 offset:16384\n\t" \
    "ds_read_b64_tr_b16 v[42:43], v4 offset:18432\n\tv_mfma_f32_16x16x32_bf16 v[84:87], a[12:15], v[20:23], v[84:87]\n\tv_mfma_f32_16x16x32_bf16 v[92:95], a[16:19], v[20:23], v[92:95]\n\ts_waitcnt lgkmcnt(8)\n\tv_mfma_f32_16x16x32_bf16 v[96:99], a[8:11], v[24:27], v[96:99]\n\tds_read_b64_tr_b16 v[44:45], v5 offset:16384\n\tds_read_b64_tr_b16 v[46:47], v5 offset:18432\n\tv_mfma_f32_16x16x32_bf16 v[104:107], a[12:15], v[24:27], v[104:107]\n\t" \
    "buffer_load_dwordx4 a[120:123], v8, s[8:11], s68 offen offset:1024 sc1\n\tv_mfma_f32_16x16x32_bf16 v[112:115], a[16:19], v[24:27], v[112:115]\n\ts_waitcnt lgkmcnt(8)\n\tv_mfma_f32_16x16x32_bf16 v[100:103], a[8:11], v[28:31], v[100:103]\n\tds_read_b64_tr_b16 v[16:17], v6 offset:16384\n\tds_read_b64_tr_b16 v[18:19], v6 offset:18432\n\tv_mfma_f32_16x16x32_bf16 v[108:111], a[12:15], v[28:31], v[108:111]\n\tbuffer_load_dwordx4 a[124:127], v8, s[8:11], s68 offen offset:2048 sc1\n\t" \
    "s_mov_b32 s69, 4456448\n\ts_add_u32 m0, s54, 0\n\tv_mfma_f32_16x16x32_bf16 v[116:119], a[16:19], v[28:31], v[116:119]\n\ts_waitcnt lgkmcnt(8)\n\tv_mfma_f32_16x16x32_bf16 v[120:123], a[8:11], v[32:35], v[120:123]\n\tds_read_b64_tr_b16 v[20:21], v7 offset:16384\n\tds_read_b64_tr_b16 v[22:23], v7 offset:18432\n\tv_mfma_f32_16x16x32_bf16 v[128:131], a[12:15], v[32:35], v[128:131]\n\t" \
    "v_mfma_f32_16x16x32_bf16 v[136:139], a[16:19], v[32:35], v[136:139]\n\ts_waitcnt lgkmcnt(8)\n\tv_mfma_f32_16x16x32_bf16 v[124:127], a[8:11], v[36:39], v[124:127]\n\tds_read_b64_tr_b16 v[24:25], v224 offset:16384\n\tds_read_b64_tr_b16 v[26:27], v224 offset:18432\n\tv_mfma_f32_16x16x32_bf16 v[132:135], a[12:15], v[36:39], v[132:135]\n\tbuffer_load_dwordx4 v12, s[16:19], s69 offen lds\n\ts_add_u32 s68, s46, 86016\n\t" \
    "v_mfma_f32_16x16x32_bf16 v[140:143], a[16:19], v[36:39], v[140:143]\n\ts_waitcnt vmcnt(40) lgkmcnt(8)\n\tv_mfma_f32_16x16x32_bf16 v[72:75], a[44:47], v[40:43], v[72:75]\n\tds_read_b64_tr_b16 v[28:29], v225 offset:16384\n\tds_read_b64_tr_b16 v[30:31], v225 offset:18432\n\tv_mfma_f32_16x16x32_bf16 v[80:83], a[48:51], v[40:43], v[80:83]\n\tbuffer_load_dwordx4 a[128:131], v8, s[8:11], s68 offen offset:0 sc1\n\tv_mfma_f32_16x16x32_bf16 v[88:91], a[52:55], v[40:43], v[88:91]\n\t" \
    "s_waitcnt lgkmcnt(8)\n\tv_mfma_f32_16x16x32_bf16 v[76:79], a[44:47], v[44:47], v[76:79]\n\tds_read_b64_tr_b16 v[32:33], v4 offset:20480\n\tds_read_b64_tr_b16 v[34:35], v4 offset:22528\n\tv_mfma_f32_16x16x32_bf16 v[84:87], a[48:51], v[44:47], v[84:87]\n\tv_mfma_f32_16x16x32_bf16 v[92:95], a[52:55], v[44:47], v[92:95]\n\ts_waitcnt lgkmcnt(8)\n\tv_mfma_f32_16x16x32_bf16 v[96:99], a[44:47], v[16:19], v[96:99]\n\t" \
    "ds_read_b64_tr_b16 v[36:37], v5 offset:20480\n\tds_read_b64_tr_b16 v[38:39], v5 offset:22528\n\tv_mfma_f32_16x16x32_bf16 v[104:107], a[48:51], v[16:19], v[104:107]\n\tbuffer_load_dwordx4 a[132:135], v8, s[8:11], s68 offen offset:1024 sc1\n\tv_mfma_f32_16x16x32_bf16 v[112:115], a[52:55], v[16:19], v[112:115]\n\ts_waitcnt lgkmcnt(8)\n\tv_mfma_f32_16x16x32_bf16 v[100:103], a[44:47], v[20:23], v[100:103]\n\tds_read_b64_tr_b16 v[40:41], v6 offset:20480\n\t" \
    "ds_read_b64_tr_b16 v[42:43], v6 offset:22528\n\tv_mfma_f32_16x16x32_bf16 v[108:111], a[48:51], v[20:23], v[108:111]\n\tbuffer_load_dwordx4 a[136:139], v8, s[8:11], s68 offen offset:2048 sc1\n\ts_add_u32 s68, s46, 89088\n\tv_mfma_f32_16x16x32_bf16 v[116:119], a[52:55], v[20:23], v[116:119]\n\ts_waitcnt lgkmcnt(8)\n\tv_mfma_f32_16x16x32_bf16 v[120:123], a[44:47], v[24:27], v[120:123]\n\tds_read_b64_tr_b16 v[44:45], v7 offset:20480\n\t" \
    "ds_read_b64_tr_b16 v[46:47], v7 offset:22528\n\tv_mfma_f32_16x16x32_bf16 v[128:131], a[48:51], v[24:27], v[128:131]\n\tv_mfma_f32_16x16x32_bf16 v[136:139], a[52:55], v[24:27], v[136:139]\n\ts_waitcnt lgkmcnt(8)\n\tv_mfma_f32_16x16x32_bf16 v[124:127], a[44:47], v[28:31], v[124:127]\n\tds_read_b64_tr_b16 v[16:17], v224 offset:20480\n\tds_read_b64_tr_b16 v[18:19], v224 offset:22528\n\tv_mfma_f32_16x16x32_bf16 v[132:135], a[48:51], v[28:31], v[132:135]\n\t" \
    "buffer_load_dwordx4 a[140:143], v8, s[8:11], s68 offen offset:0 sc1\n\tv_mfma_f32_16x16x32_bf16 v[140:143], a[52:55], v[28:31], v[140:143]\n\ts_waitcnt vmcnt(41) lgkmcnt(8)\n\tv_mfma_f32_16x16x32_bf16 v[72:75], a[56:59], v[32:35], v[72:75]\n\tds_read_b64_tr_b16 v[20:21], v225 offset:20480\n\tds_read_b64_tr_b16 v[22:23], v225 offset:22528\n\tv_mfma_f32_16x16x32_bf16 v[80:83], a[60:63], v[32:35], v[80:83]\n\tbuffer_load_dwordx4 a[144:147], v8, s[8:11], s68 offen offset:1024 sc1\n\t" \
    "v_mfma_f32_16x16x32_bf16 v[88:91], a[64:67], v[32:35], v[88:91]\n\ts_waitcnt lgkmcnt(8)\n\tv_mfma_f32_16x16x32_bf16 v[76:79], a[56:59], v[36:39], v[76:79]\n\tds_read_b64_tr_b16 v[24:25], v4 offset:24576\n\tds_read_b64_tr_b16 v[26:27], v4 offset:26624\n\tv_mfma_f32_16x16x32_bf16 v[84:87], a[60:63], v[36:39], v[84:87]\n\tv_mfma_f32_16x16x32_bf16 v[92:95], a[64:67], v[36:39], v[92:95]\n\ts_waitcnt lgkmcnt(8)\n\t" \
    "v_mfma_f32_16x16x32_bf16 v[96:99], a[56:59], v[40:43], v[96:99]\n\tds_read_b64_tr_b16 v[28:29], v5 offset:24576\n\tds_read_b64_tr_b16 v[30:31], v5 offset:26624\n\tv_mfma_f32_16x16x32_bf16 v[104:107], a[60:63], v[40:43], v[104:107]\n\tbuffer_load_dwordx4 a[148:151], v8, s[8:11], s68 offen offset:2048 sc1\n\ts_add_u32 s68, s46, 92160\n\tv_mfma_f32_16x16x32_bf16 v[112:115], a[64:67], v[40:43], v[112:115]\n\ts_waitcnt lgkmcnt(8)\n\t" \
    "v_mfma_f32_16x16x32_bf16 v[100:103], a[56:59], v[44:47], v[100:103]\n\tds_read_b64_tr_b16 v[32:33], v6 offset:24576\n\tds_read_b64_tr_b16 v[34:35], v6 offset:26624\n\tv_mfma_f32_16x16x32_bf16 v[108:111], a[60:63], v[44:47], v[108:111]\n\tbuffer_load_dwordx4 a[152:155], v8, s[8:11], s68 offen offset:0 sc1\n\tv_mfma_f32_16x16x32_bf16 v[116:119], a[64:67], v[44:47], v[116:119]\n\ts_waitcnt vmcnt(38)\n\ts_barrier\n\t" \
    "s_waitcnt lgkmcnt(8)\n\tv_mfma_f32_16x16x32_bf16 v[120:123], a[56:59], v[16:19], v[120:123]\n\tds_read_b64_tr_b16 v[36:37], v7 offset:24576\n\tds_read_b64_tr_b16 v[38:39], v7 offset:26624\n\tv_mfma_f32_16x16x32_bf16 v[128:131], a[60:63], v[16:19], v[128:131]\n\tbuffer_load_dwordx4 a[156:159], v8, s[8:11], s68 offen offset:1024 sc1\n\tv_mfma_f32_16x16x32_bf16 v[136:139], a[64:67], v[16:19], v[136:139]\n\ts_waitcnt lgkmcnt(8)\n\t" \
    "v_mfma_f32_16x16x32_bf16 v[124:127], a[56:59], v[20:23], v[124:127]\n\tds_read_b64_tr_b16 v[40:41], v224 offset:24576\n\tds_read_b64_tr_b16 v[42:43], v224 offset:26624\n\tv_mfma_f32_16x16x32_bf16 v[132:135], a[60:63], v[20:23], v[132:135]\n\tv_mfma_f32_16x16x32_bf16 v[140:143], a[64:67], v[20:23], v[140:143]\n\ts_waitcnt vmcnt(36) lgkmcnt(8)\n\tv_mfma_f32_16x16x32_bf16 v[72:75], a[20:23], v[24:27], v[72:75]\n\tds_read_b64_tr_b16 v[44:45], v225 offset:24576\n\t" \
    "ds_read_b64_tr_b16 v[46:47], v225 offset:26624\n\tv_mfma_f32_16x16x32_bf16 v[80:83], a[24:27], v[24:27], v[80:83]\n\tbuffer_load_dwordx4 a[160:163], v8, s[8:11], s68 offen offset:2048 sc1\n\ts_add_u32 s68, s46, 95232\n\tv_mfma_f32_16x16x32_bf16 v[88:91], a[28:31], v[24:27], v[88:91]\n\ts_waitcnt lgkmcnt(8)\n\tv_mfma_f32_16x16x32_bf16 v[76:79], a[20:23], v[28:31], v[76:79]\n\tds_read_b64_tr_b16 v[16:17], v4 offset:28672\n\t" \
    "ds_read_b64_tr_b16 v[18:19], v4 offset:30720\n\tv_mfma_f32_16x16x32_bf16 v[84:87], a[24:27], v[28:31], v[84:87]\n\tbuffer_load_dwordx4 a[164:167], v8, s[8:11], s68 offen offset:0 sc1\n\tv_mfma_f32_16x16x32_bf16 v[92:95], a[28:31], v[28:31], v[92:95]\n\ts_waitcnt lgkmcnt(8)\n\tv_mfma_f32_16x16x32_bf16 v[96:99], a[20:23], v[32:35], v[96:99]\n\tds_read_b64_tr_b16 v[20:21], v5 offset:28672\n\tds_read_b64_tr_b16 v[22:23], v5 offset:30720\n\t" \
    "v_mfma_f32_16x16x32_bf16 v[104:107], a[24:27], v[32:35], v[104:107]\n\tv_mfma_f32_16x16x32_bf16 v[112:115], a[28:31], v[32:35], v[112:115]\n\ts_waitcnt lgkmcnt(8)\n\tv_mfma_f32_16x16x32_bf16 v[100:103], a[20:23], v[36:39], v[100:103]\n\tds_read_b64_tr_b16 v[24:25], v6 offset:28672\n\tds_read_b64_tr_b16 v[26:27], v6 offset:30720\n\tv_mfma_f32_16x16x32_bf16 v[108:111], a[24:27], v[36:39], v[108:111]\n\tbuffer_load_dwordx4 a[168:171], v8, s[8:11], s68 offen offset:1024 sc1\n\t" \
    "v_mfma_f32_16x16x32_bf16 v[116:119], a[28:31], v[36:39], v[116:119]\n\ts_waitcnt lgkmcnt(8)\n\tv_mfma_f32_16x16x32_bf16 v[120:123], a[20:23], v[40:43], v[120:123]\n\tds_read_b64_tr_b16 v[28:29], v7 offset:28672\n\tds_read_b64_tr_b16 v[30:31], v7 offset:30720\n\tv_mfma_f32_16x16x32_bf16 v[128:131], a[24:27], v[40:43], v[128:131]\n\tbuffer_load_dwordx4 a[172:175], v8, s[8:11], s68 offen offset:2048 sc1\n\ts_add_u32 s68, s46, 98304\n\t" \
    "v_mfma_f32_16x16x32_bf16 v[136:139], a[28:31], v[40:43], v[136:139]\n\ts_waitcnt lgkmcnt(8)\n\tv_mfma_f32_16x16x32_bf16 v[124:127], a[20:23], v[44:47], v[124:127]\n\tds_read_b64_tr_b16 v[32:33], v224 offset:28672\n\tds_read_b64_tr_b16 v[34:35], v224 offset:30720\n\tv_mfma_f32_16x16x32_bf16 v[132:135], a[24:27], v[44:47], v[132:135]\n\tv_mfma_f32_16x16x32_bf16 v[140:143], a[28:31], v[44:47], v[140:143]\n\ts_waitcnt vmcnt(37) lgkmcnt(8)\n\t" \
    "v_mfma_f32_16x16x32_bf16 v[72:75], a[68:71], v[16:19], v[72:75]\n\tds_read_b64_tr_b16 v[36:37], v225 offset:28672\n\tds_read_b64_tr_b16 v[38:39], v225 offset:30720\n\tv_mfma_f32_16x16x32_bf16 v[80:83], a[72:75], v[16:19], v[80:83]\n\tbuffer_load_dwordx4 a[176:179], v8, s[8:11], s68 offen offset:0 sc1\n\tv_mfma_f32_16x16x32_bf16 v[88:91], a[76:79], v[16:19], v[88:91]\n\ts_waitcnt lgkmcnt(8)\n\tv_mfma_f32_16x16x32_bf16 v[76:79], a[68:71], v[20:23], v[76:79]\n\t" \
    "ds_read_b64_tr_b16 v[40:41], v4 offset:32768\n\tds_read_b64_tr_b16 v[42:43], v4 offset:34816\n\tv_mfma_f32_16x16x32_bf16 v[84:87], a[72:75], v[20:23], v[84:87]\n\tbuffer_load_dwordx4 a[180:183], v8, s[8:11], s68 offen offset:1024 sc1\n\tv_mfma_f32_16x16x32_bf16 v[92:95], a[76:79], v[20:23], v[92:95]\n\ts_waitcnt lgkmcnt(8)\n\tv_mfma_f32_16x16x32_bf16 v[96:99], a[68:71], v[24:27], v[96:99]\n\tds_read_b64_tr_b16 v[44:45], v5 offset:32768\n\t" \
    "ds_read_b64_tr_b16 v[46:47], v5 offset:34816\n\tv_mfma_f32_16x16x32_bf16 v[104:107], a[72:75], v[24:27], v[104:107]\n\tv_mfma_f32_16x16x32_bf16 v[112:115], a[76:79], v[24:27], v[112:115]\n\ts_waitcnt lgkmcnt(8)\n\tv_mfma_f32_16x16x32_bf16 v[100:103], a[68:71], v[28:31], v[100:103]\n\tds_read_b64_tr_b16 v[16:17], v6 offset:32768\n\tds_read_b64_tr_b16 v[18:19], v6 offset:34816\n\tv_mfma_f32_16x16x32_bf16 v[108:111], a[72:75], v[28:31], v[108:111]\n\t" \
    "buffer_load_dwordx4 a[184:187], v8, s[8:11], s68 offen offset:2048 sc1\n\ts_add_u32 s68, s46, 101376\n\tv_mfma_f32_16x16x32_bf16 v[116:119], a[76:79], v[28:31], v[116:119]\n\ts_waitcnt lgkmcnt(8)\n\tv_mfma_f32_16x16x32_bf16 v[120:123], a[68:71], v[32:35], v[120:123]\n\tds_read_b64_tr_b16 v[20:21], v7 offset:32768\n\tds_read_b64_tr_b16 v[22:23], v7 offset:34816\n\tv_mfma_f32_16x16x32_bf16 v[128:131], a[72:75], v[32:35], v[128:131]\n\t" \
    "buffer_load_dwordx4 a[188:191], v8, s[8:11], s68 offen offset:0 sc1\n\tv_mfma_f32_16x16x32_bf16 v[136:139], a[76:79], v[32:35], v[136:139]\n\ts_waitcnt lgkmcnt(8)\n\tv_mfma_f32_16x16x32_bf16 v[124:127], a[68:71], v[36:39], v[124:127]\n\tds_read_b64_tr_b16 v[24:25], v224 offset:32768\n\tds_read_b64_tr_b16 v[26:27], v224 offset:34816\n\tv_mfma_f32_16x16x32_bf16 v[132:135], a[72:75], v[36:39], v[132:135]\n\tv_mfma_f32_16x16x32_bf16 v[140:143], a[76:79], v[36:39], v[140:143]\n\t" \
    "s_waitcnt vmcnt(38) lgkmcnt(8)\n\tv_mfma_f32_16x16x32_bf16 v[72:75], a[80:83], v[40:43], v[72:75]\n\tds_read_b64_tr_b16 v[28:29], v225 offset:32768\n\tds_read_b64_tr_b16 v[30:31], v225 offset:34816\n\tv_mfma_f32_16x16x32_bf16 v[80:83], a[84:87], v[40:43], v[80:83]\n\tbuffer_load_dwordx4 a[192:195], v8, s[8:11], s68 offen offset:1024 sc1\n\tv_mfma_f32_16x16x32_bf16 v[88:91], a[88:91], v[40:43], v[88:91]\n\ts_waitcnt lgkmcnt(8)\n\t" \
    "v_mfma_f32_16x16x32_bf16 v[76:79], a[80:83], v[44:47], v[76:79]\n\tds_read_b64_tr_b16 v[32:33], v4 offset:36864\n\tds_read_b64_tr_b16 v[34:35], v4 offset:38912\n\tv_mfma_f32_16x16x32_bf16 v[84:87], a[84:87], v[44:47], v[84:87]\n\tv_mfma_f32_16x16x32_bf16 v[92:95], a[88:91], v[44:47], v[92:95]\n\ts_waitcnt vmcnt(34)\n\ts_barrier\n\ts_waitcnt lgkmcnt(8)\n\t" \
    "v_mfma_f32_16x16x32_bf16 v[96:99], a[80:83], v[16:19], v[96:99]\n\tds_read_b64_tr_b16 v[36:37], v5 offset:36864\n\tds_read_b64_tr_b16 v[38:39], v5 offset:38912\n\tv_mfma_f32_16x16x32_bf16 v[104:107], a[84:87], v[16:19], v[104:107]\n\tbuffer_load_dwordx4 a[196:199], v8, s[8:11], s68 offen offset:2048 sc1\n\ts_add_u32 s68, s46, 104448\n\tv_mfma_f32_16x16x32_bf16 v[112:115], a[88:91], v[16:19], v[112:115]\n\ts_waitcnt lgkmcnt(8)\n\t" \
    "v_mfma_f32_16x16x32_bf16 v[100:103], a[80:83], v[20:23], v[100:103]\n\tds_read_b64_tr_b16 v[40:41], v6 offset:36864\n\tds_read_b64_tr_b16 v[42:43], v6 offset:38912\n\tv_mfma_f32_16x16x32_bf16 v[108:111], a[84:87], v[20:23], v[108:111]\n\tbuffer_load_dwordx4 a[200:203], v8, s[8:11], s68 offen offset:0 sc1\n\tv_mfma_f32_16x16x32_bf16 v[116:119], a[88:91], v[20:23], v[116:119]\n\ts_waitcnt lgkmcnt(8)\n\tv_mfma_f32_16x16x32_bf16 v[120:123], a[80:83], v[24:27], v[120:123]\n\t" \
    "ds_read_b64_tr_b16 v[44:45], v7 offset:36864\n\tds_read_b64_tr_b16 v[46:47], v7 offset:38912\n\tv_mfma_f32_16x16x32_bf16 v[128:131], a[84:87], v[24:27], v[128:131]\n\tbuffer_load_dwordx4 a[204:207], v8, s[8:11], s68 offen offset:1024 sc1\n\tv_mfma_f32_16x16x32_bf16 v[136:139], a[88:91], v[24:27], v[136:139]\n\ts_waitcnt lgkmcnt(8)\n\tv_mfma_f32_16x16x32_bf16 v[124:127], a[80:83], v[28:31], v[124:127]\n\tds_read_b64_tr_b16 v[16:17], v224 offset:36864\n\t" \
    "ds_read_b64_tr_b16 v[18:19], v224 offset:38912\n\tv_mfma_f32_16x16x32_bf16 v[132:135], a[84:87], v[28:31], v[132:135]\n\tbuffer_load_dwordx4 a[208:211], v8, s[8:11], s68 offen offset:2048 sc1\n\ts_add_u32 s68, s46, 107520\n\tv_mfma_f32_16x16x32_bf16 v[140:143], a[88:91], v[28:31], v[140:143]\n\ts_waitcnt vmcnt(35) lgkmcnt(8)\n\tv_mfma_f32_16x16x32_bf16 v[72:75], a[92:95], v[32:35], v[72:75]\n\tds_read_b64_tr_b16 v[20:21], v225 offset:36864\n\t" \
    "ds_read_b64_tr_b16 v[22:23], v225 offset:38912\n\tv_mfma_f32_16x16x32_bf16 v[80:83], a[96:99], v[32:35], v[80:83]\n\tv_mfma_f32_16x16x32_bf16 v[88:91], a[100:103], v[32:35], v[88:91]\n\ts_waitcnt lgkmcnt(8)\n\tv_mfma_f32_16x16x32_bf16 v[76:79], a[92:95], v[36:39], v[76:79]\n\tds_read_b64_tr_b16 v[24:25], v4 offset:40960\n\tds_read_b64_tr_b16 v[26:27], v4 offset:43008\n\tv_mfma_f32_16x16x32_bf16 v[84:87], a[96:99], v[36:39], v[84:87]\n\t" \
    "buffer_load_dwordx4 a[212:215], v8, s[8:11], s68 offen offset:0 sc1\n\tv_mfma_f32_16x16x32_bf16 v[92:95], a[100:103], v[36:39], v[92:95]\n\ts_waitcnt lgkmcnt(8)\n\tv_mfma_f32_16x16x32_bf16 v[96:99], a[92:95], v[40:43], v[96:99]\n\tds_read_b64_tr_b16 v[28:29], v5 offset:40960\n\tds_read_b64_tr_b16 v[30:31], v5 offset:43008\n\tv_mfma_f32_16x16x32_bf16 v[104:107], a[96:99], v[40:43], v[104:107]\n\tbuffer_load_dwordx4 a[216:219], v8, s[8:11], s68 offen offset:1024 sc1\n\t" \
    "v_mfma_f32_16x16x32_bf16 v[112:115], a[100:103], v[40:43], v[112:115]\n\ts_waitcnt lgkmcnt(8)\n\tv_mfma_f32_16x16x32_bf16 v[100:103], a[92:95], v[44:47], v[100:103]\n\tds_read_b64_tr_b16 v[32:33], v6 offset:40960\n\tds_read_b64_tr_b16 v[34:35], v6 offset:43008\n\tv_mfma_f32_16x16x32_bf16 v[108:111], a[96:99], v[44:47], v[108:111]\n\tv_mfma_f32_16x16x32_bf16 v[116:119], a[100:103], v[44:47], v[116:119]\n\ts_waitcnt lgkmcnt(8)\n\t" \
    "v_mfma_f32_16x16x32_bf16 v[120:123], a[92:95], v[16:19], v[120:123]\n\tds_read_b64_tr_b16 v[36:37], v7 offset:40960\n\tds_read_b64_tr_b16 v[38:39], v7 offset:43008\n\tv_mfma_f32_16x16x32_bf16 v[128:131], a[96:99], v[16:19], v[128:131]\n\tbuffer_load_dwordx4 a[220:223], v8, s[8:11], s68 offen offset:2048 sc1\n\ts_mov_b32 s69, 3014656\n\ts_add_u32 m0, s54, 4096\n\tv_mfma_f32_16x16x32_bf16 v[136:139], a[100:103], v[16:19], v[136:139]\n\t" \
    "s_waitcnt lgkmcnt(8)\n\tv_mfma_f32_16x16x32_bf16 v[124:127], a[92:95], v[20:23], v[124:127]\n\tds_read_b64_tr_b16 v[40:41], v224 offset:40960\n\tds_read_b64_tr_b16 v[42:43], v224 offset:43008\n\tv_mfma_f32_16x16x32_bf16 v[132:135], a[96:99], v[20:23], v[132:135]\n\tv_mfma_f32_16x16x32_bf16 v[140:143], a[100:103], v[20:23], v[140:143]\n\ts_waitcnt vmcnt(35) lgkmcnt(8)\n\tv_mfma_f32_16x16x32_bf16 v[72:75], a[104:107], v[24:27], v[72:75]\n\t" \
    "ds_read_b64_tr_b16 v[44:45], v225 offset:40960\n\tds_read_b64_tr_b16 v[46:47], v225 offset:43008\n\tv_mfma_f32_16x16x32_bf16 v[80:83], a[108:111], v[24:27], v[80:83]\n\tbuffer_load_dwordx4 v13, s[16:19], s69 offen lds\n\ts_mov_b32 s69, 2883584\n\ts_add_u32 m0, s54, 8192\n\tv_mfma_f32_16x16x32_bf16 v[88:91], a[112:115], v[24:27], v[88:91]\n\ts_waitcnt lgkmcnt(8)\n\t" \
    "v_mfma_f32_16x16x32_bf16 v[76:79], a[104:107], v[28:31], v[76:79]\n\tds_read_b64_tr_b16 v[16:17], v4 offset:45056\n\tds_read_b64_tr_b16 v[18:19], v4 offset:47104\n\tv_mfma_f32_16x16x32_bf16 v[84:87], a[108:111], v[28:31], v[84:87]\n\tbuffer_load_dwordx4 v13, s[16:19], s69 offen lds\n\ts_mov_b32 s69, 3538944\n\ts_add_u32 m0, s54, 12288\n\tv_mfma_f32_16x16x32_bf16 v[92:95], a[112:115], v[28:31], v[92:95]\n\t" \
    "s_waitcnt lgkmcnt(8)\n\tv_mfma_f32_16x16x32_bf16 v[96:99], a[104:107], v[32:35], v[96:99]\n\tds_read_b64_tr_b16 v[20:21], v5 offset:45056\n\tds_read_b64_tr_b16 v[22:23], v5 offset:47104\n\tv_mfma_f32_16x16x32_bf16 v[104:107], a[108:111], v[32:35], v[104:107]\n\tv_mfma_f32_16x16x32_bf16 v[112:115], a[112:115], v[32:35], v[112:115]\n\ts_waitcnt lgkmcnt(8)\n\tv_mfma_f32_16x16x32_bf16 v[100:103], a[104:107], v[36:39], v[100:103]\n\t" \
    "ds_read_b64_tr_b16 v[24:25], v6 offset:45056\n\tds_read_b64_tr_b16 v[26:27], v6 offset:47104\n\tv_mfma_f32_16x16x32_bf16 v[108:111], a[108:111], v[36:39], v[108:111]\n\tbuffer_load_dwordx4 v13, s[16:19], s69 offen lds\n\ts_mov_b32 s69, 3407872\n\ts_add_u32 m0, s54, 16384\n\tv_mfma_f32_16x16x32_bf16 v[116:119], a[112:115], v[36:39], v[116:119]\n\ts_waitcnt lgkmcnt(8)\n\t" \
    "v_mfma_f32_16x16x32_bf16 v[120:123], a[104:107], v[40:43], v[120:123]\n\tds_read_b64_tr_b16 v[28:29], v7 offset:45056\n\tds_read_b64_tr_b16 v[30:31], v7 offset:47104\n\tv_mfma_f32_16x16x32_bf16 v[128:131], a[108:111], v[40:43], v[128:131]\n\tbuffer_load_dwordx4 v13, s[16:19], s69 offen lds\n\tv_mfma_f32_16x16x32_bf16 v[136:139], a[112:115], v[40:43], v[136:139]\n\ts_waitcnt lgkmcnt(8)\n\tv_mfma_f32_16x16x32_bf16 v[124:127], a[104:107], v[44:47], v[124:127]\n\t" \
    "ds_read_b64_tr_b16 v[32:33], v224 offset:45056\n\tds_read_b64_tr_b16 v[34:35], v224 offset:47104\n\tv_mfma_f32_16x16x32_bf16 v[132:135], a[108:111], v[44:47], v[132:135]\n\tv_mfma_f32_16x16x32_bf16 v[140:143], a[112:115], v[44:47], v[140:143]\n\ts_waitcnt vmcnt(28)\n\ts_barrier\n\ts_waitcnt lgkmcnt(8)\n\tv_mfma_f32_16x16x32_bf16 v[72:75], a[116:119], v[16:19], v[72:75]\n\t" \
    "ds_read_b64_tr_b16 v[36:37], v225 offset:45056\n\tds_read_b64_tr_b16 v[38:39], v225 offset:47104\n\tv_mfma_f32_16x16x32_bf16 v[80:83], a[120:123], v[16:19], v[80:83]\n\tbuffer_load_dwordx4 v[192:195], v243, s[20:23], 0 offen offset:0\n\tv_mfma_f32_16x16x32_bf16 v[88:91], a[124:127], v[16:19], v[88:91]\n\ts_waitcnt lgkmcnt(8)\n\tv_mfma_f32_16x16x32_bf16 v[76:79], a[116:119], v[20:23], v[76:79]\n\tds_read_b64_tr_b16 v[40:41], v4 offset:49152\n\t" \
    "ds_read_b64_tr_b16 v[42:43], v4 offset:51200\n\tv_mfma_f32_16x16x32_bf16 v[84:87], a[120:123], v[20:23], v[84:87]\n\tbuffer_load_dwordx4 v[196:199], v243, s[20:23], 0 offen offset:32\n\tv_mfma_f32_16x16x32_bf16 v[92:95], a[124:127], v[20:23], v[92:95]\n\ts_waitcnt lgkmcnt(8)\n\tv_mfma_f32_16x16x32_bf16 v[96:99], a[116:119], v[24:27], v[96:99]\n\tds_read_b64_tr_b16 v[44:45], v5 offset:49152\n\tds_read_b64_tr_b16 v[46:47], v5 offset:51200\n\t" \
    "v_mfma_f32_16x16x32_bf16 v[104:107], a[120:123], v[24:27], v[104:107]\n\tbuffer_load_dwordx4 v[200:203], v243, s[20:23], 0 offen offset:64\n\tv_mfma_f32_16x16x32_bf16 v[112:115], a[124:127], v[24:27], v[112:115]\n\ts_waitcnt lgkmcnt(8)\n\tv_mfma_f32_16x16x32_bf16 v[100:103], a[116:119], v[28:31], v[100:103]\n\tds_read_b64_tr_b16 v[16:17], v224 offset:49152\n\tds_read_b64_tr_b16 v[18:19], v224 offset:51200\n\tv_mfma_f32_16x16x32_bf16 v[108:111], a[120:123], v[28:31], v[108:111]\n\t" \
    "buffer_load_dwordx2 v[216:217], v242, s[8:11], s59 offen offset:0 sc1\n\tv_mfma_f32_16x16x32_bf16 v[116:119], a[124:127], v[28:31], v[116:119]\n\tv_min_u32 v241, 88, v239\n\tbuffer_load_dwordx2 v[250:251], v241, s[8:11], s51 offen sc1\n\ts_waitcnt lgkmcnt(8)\n\tv_mfma_f32_16x16x32_bf16 v[120:123], a[116:119], v[32:35], v[120:123]\n\tds_read_b64_tr_b16 v[20:21], v225 offset:49152\n\tds_read_b64_tr_b16 v[22:23], v225 offset:51200\n\t" \
    "v_mfma_f32_16x16x32_bf16 v[128:131], a[120:123], v[32:35], v[128:131]\n\tv_mfma_f32_16x16x32_bf16 v[136:139], a[124:127], v[32:35], v[136:139]\n\ts_waitcnt lgkmcnt(8)\n\tv_mfma_f32_16x16x32_bf16 v[124:127], a[116:119], v[36:39], v[124:127]\n\tds_read_b64_tr_b16 v[24:25], v4 offset:53248\n\tds_read_b64_tr_b16 v[26:27], v4 offset:55296\n\tv_mfma_f32_16x16x32_bf16 v[132:135], a[120:123], v[36:39], v[132:135]\n\tv_mfma_f32_16x16x32_bf16 v[140:143], a[124:127], v[36:39], v[140:143]\n\t" \
    "s_waitcnt vmcnt(30) lgkmcnt(8)\n\tv_mfma_f32_16x16x32_bf16 v[72:75], a[128:131], v[40:43], v[72:75]\n\tds_read_b64_tr_b16 v[28:29], v5 offset:53248\n\tds_read_b64_tr_b16 v[30:31], v5 offset:55296\n\tv_mfma_f32_16x16x32_bf16 v[80:83], a[132:135], v[40:43], v[80:83]\n\tv_mfma_f32_16x16x32_bf16 v[88:91], a[136:139], v[40:43], v[88:91]\n\ts_waitcnt lgkmcnt(8)\n\tv_mfma_f32_16x16x32_bf16 v[76:79], a[128:131], v[44:47], v[76:79]\n\t" \
    "ds_read_b64_tr_b16 v[32:33], v224 offset:53248\n\tds_read_b64_tr_b16 v[34:35], v224 offset:55296\n\tv_mfma_f32_16x16x32_bf16 v[84:87], a[132:135], v[44:47], v[84:87]\n\tbuffer_load_dwordx2 v[218:219], v242, s[8:11], s59 offen offset:128 sc1\n\ts_mov_b32 s69, 4063232\n\ts_add_u32 m0, s54, 20480\n\tv_mfma_f32_16x16x32_bf16 v[92:95], a[136:139], v[44:47], v[92:95]\n\ts_waitcnt vmcnt(28) lgkmcnt(8)\n\t" \
    "v_mfma_f32_16x16x32_bf16 v[72:75], a[140:143], v[16:19], v[72:75]\n\tds_read_b64_tr_b16 v[36:37], v225 offset:53248\n\tds_read_b64_tr_b16 v[38:39], v225 offset:55296\n\tv_mfma_f32_16x16x32_bf16 v[80:83], a[144:147], v[16:19], v[80:83]\n\tbuffer_load_dwordx4 v13, s[16:19], s69 offen lds\n\ts_mov_b32 s69, 3932160\n\ts_add_u32 m0, s54, 24576\n\tv_mfma_f32_16x16x32_bf16 v[88:91], a[148:151], v[16:19], v[88:91]\n\t" \
    "s_waitcnt lgkmcnt(8)\n\tv_mfma_f32_16x16x32_bf16 v[76:79], a[140:143], v[20:23], v[76:79]\n\tds_read_b64_tr_b16 v[40:41], v4 offset:57344\n\tds_read_b64_tr_b16 v[42:43], v4 offset:59392\n\tv_mfma_f32_16x16x32_bf16 v[84:87], a[144:147], v[20:23], v[84:87]\n\tv_mfma_f32_16x16x32_bf16 v[92:95], a[148:151], v[20:23], v[92:95]\n\ts_waitcnt vmcnt(26) lgkmcnt(8)\n\tv_mfma_f32_16x16x32_bf16 v[72:75], a[152:155], v[24:27], v[72:75]\n\t" \
    "ds_read_b64_tr_b16 v[44:45], v5 offset:57344\n\tds_read_b64_tr_b16 v[46:47], v5 offset:59392\n\tv_mfma_f32_16x16x32_bf16 v[80:83], a[156:159], v[24:27], v[80:83]\n\tbuffer_load_dwordx4 v13, s[16:19], s69 offen lds\n\ts_mov_b32 s69, 4587520\n\ts_add_u32 m0, s54, 28672\n\tv_mfma_f32_16x16x32_bf16 v[88:91], a[160:163], v[24:27], v[88:91]\n\ts_waitcnt lgkmcnt(8)\n\t" \
    "v_mfma_f32_16x16x32_bf16 v[76:79], a[152:155], v[28:31], v[76:79]\n\tds_read_b64_tr_b16 v[16:17], v224 offset:57344\n\tds_read_b64_tr_b16 v[18:19], v224 offset:59392\n\tv_mfma_f32_16x16x32_bf16 v[84:87], a[156:159], v[28:31], v[84:87]\n\tbuffer_load_dwordx4 v13, s[16:19], s69 offen lds\n\ts_mov_b32 s69, 4456448\n\ts_add_u32 m0, s54, 32768\n\tv_mfma_f32_16x16x32_bf16 v[92:95], a[160:163], v[28:31], v[92:95]\n\t" \
    "s_waitcnt vmcnt(25) lgkmcnt(8)\n\tv_mfma_f32_16x16x32_bf16 v[72:75], a[164:167], v[32:35], v[72:75]\n\tds_read_b64_tr_b16 v[20:21], v225 offset:57344\n\tds_read_b64_tr_b16 v[22:23], v225 offset:59392\n\tv_mfma_f32_16x16x32_bf16 v[80:83], a[168:171], v[32:35], v[80:83]\n\tv_mfma_f32_16x16x32_bf16 v[88:91], a[172:175], v[32:35], v[88:91]\n\ts_waitcnt lgkmcnt(8)\n\tv_mfma_f32_16x16x32_bf16 v[76:79], a[164:167], v[36:39], v[76:79]\n\t" \
    "ds_read_b64_tr_b16 v[24:25], v4 offset:61440\n\tds_read_b64_tr_b16 v[26:27], v4 offset:63488\n\tv_mfma_f32_16x16x32_bf16 v[84:87], a[168:171], v[36:39], v[84:87]\n\tbuffer_load_dwordx4 v13, s[16:19], s69 offen lds\n\tv_mfma_f32_16x16x32_bf16 v[92:95], a[172:175], v[36:39], v[92:95]\n\ts_waitcnt vmcnt(23) lgkmcnt(8)\n\tv_mfma_f32_16x16x32_bf16 v[72:75], a[176:179], v[40:43], v[72:75]\n\tds_read_b64_tr_b16 v[28:29], v5 offset:61440\n\t" \
    "ds_read_b64_tr_b16 v[30:31], v5 offset:63488\n\tv_mfma_f32_16x16x32_bf16 v[80:83], a[180:183], v[40:43], v[80:83]\n\tv_mfma_f32_16x16x32_bf16 v[88:91], a[184:187], v[40:43], v[88:91]\n\ts_waitcnt lgkmcnt(8)\n\tv_mfma_f32_16x16x32_bf16 v[76:79], a[176:179], v[44:47], v[76:79]\n\tds_read_b64_tr_b16 v[32:33], v4 offset:0\n\tds_read_b64_tr_b16 v[34:35], v4 offset:2048\n\tv_mfma_f32_16x16x32_bf16 v[84:87], a[180:183], v[44:47], v[84:87]\n\t" \
    "v_mfma_f32_16x16x32_bf16 v[92:95], a[184:187], v[44:47], v[92:95]\n\ts_waitcnt vmcnt(20) lgkmcnt(8)\n\tv_mfma_f32_16x16x32_bf16 v[72:75], a[188:191], v[16:19], v[72:75]\n\tds_read_b64_tr_b16 v[36:37], v5 offset:0\n\tds_read_b64_tr_b16 v[38:39], v5 offset:2048\n\tv_mfma_f32_16x16x32_bf16 v[80:83], a[192:195], v[16:19], v[80:83]\n\tv_mfma_f32_16x16x32_bf16 v[88:91], a[196:199], v[16:19], v[88:91]\n\ts_waitcnt lgkmcnt(8)\n\t" \
    "v_mfma_f32_16x16x32_bf16 v[76:79], a[188:191], v[20:23], v[76:79]\n\tds_read_b64_tr_b16 v[40:41], v6 offset:49152\n\tds_read_b64_tr_b16 v[42:43], v6 offset:51200\n\tv_mfma_f32_16x16x32_bf16 v[84:87], a[192:195], v[20:23], v[84:87]\n\tv_mfma_f32_16x16x32_bf16 v[92:95], a[196:199], v[20:23], v[92:95]\n\ts_waitcnt vmcnt(5)\n\tv_cmp_ne_u64 vcc, s[36:37], v[250:251]\n\ts_cmp_lg_u64 vcc, 0\n\t" \
    "s_cselect_b32 s70, 1, 0\n\ts_waitcnt lgkmcnt(8)\n\tv_mfma_f32_16x16x32_bf16 v[72:75], a[200:203], v[24:27], v[72:75]\n\tds_read_b64_tr_b16 v[44:45], v7 offset:49152\n\tds_read_b64_tr_b16 v[46:47], v7 offset:51200\n\tv_mfma_f32_16x16x32_bf16 v[80:83], a[204:207], v[24:27], v[80:83]\n\tbuffer_load_dwordx4 v[204:207], v8, s[8:11], s56 offen offset:0 sc1\n\tv_mfma_f32_16x16x32_bf16 v[88:91], a[208:211], v[24:27], v[88:91]\n\t" \
    "s_waitcnt lgkmcnt(8)\n\tv_mfma_f32_16x16x32_bf16 v[76:79], a[200:203], v[28:31], v[76:79]\n\tds_read_b64_tr_b16 v[16:17], v226 offset:49152\n\tds_read_b64_tr_b16 v[18:19], v226 offset:51200\n\tv_mfma_f32_16x16x32_bf16 v[84:87], a[204:207], v[28:31], v[84:87]\n\tbuffer_load_dwordx4 v[208:211], v8, s[8:11], s56 offen offset:1024 sc1\n\tv_mfma_f32_16x16x32_bf16 v[92:95], a[208:211], v[28:31], v[92:95]\n\ts_waitcnt lgkmcnt(8)\n\t" \
    "v_mfma_f32_16x16x32_bf16 v[72:75], a[212:215], v[32:35], v[72:75]\n\tds_read_b64_tr_b16 v[20:21], v227 offset:49152\n\tds_read_b64_tr_b16 v[22:23], v227 offset:51200\n\tv_mfma_f32_16x16x32_bf16 v[80:83], a[216:219], v[32:35], v[80:83]\n\tbuffer_load_dwordx4 v[212:215], v8, s[8:11], s56 offen offset:2048 sc1\n\tv_mfma_f32_16x16x32_bf16 v[88:91], a[220:223], v[32:35], v[88:91]\n\ts_waitcnt lgkmcnt(8)\n\tv_mfma_f32_16x16x32_bf16 v[76:79], a[212:215], v[36:39], v[76:79]\n\t" \
    "ds_read_b64_tr_b16 v[24:25], v6 offset:53248\n\tds_read_b64_tr_b16 v[26:27], v6 offset:55296\n\tv_mfma_f32_16x16x32_bf16 v[84:87], a[216:219], v[36:39], v[84:87]\n\tv_mfma_f32_16x16x32_bf16 v[92:95], a[220:223], v[36:39], v[92:95]\n\ts_waitcnt lgkmcnt(8)\n\tv_mfma_f32_16x16x32_bf16 v[96:99], a[128:131], v[40:43], v[96:99]\n\tds_read_b64_tr_b16 v[28:29], v7 offset:53248\n\tds_read_b64_tr_b16 v[30:31], v7 offset:55296\n\t" \
    "s_xor_b32 s68, s53, 12\n\tv_mfma_f32_16x16x32_bf16 v[104:107], a[132:135], v[40:43], v[104:107]\n\ts_mul_i32 s68, s68, 12288\n\ts_add_u32 s68, s68, 7208960\n\tv_cvt_pk_bf16_f32 v72, v72, v73\n\tv_mfma_f32_16x16x32_bf16 v[112:115], a[136:139], v[40:43], v[112:115]\n\tv_cvt_pk_bf16_f32 v73, v74, v75\n\tv_cvt_pk_bf16_f32 v74, v76, v77\n\t" \
    "s_waitcnt lgkmcnt(8)\n\tv_mfma_f32_16x16x32_bf16 v[100:103], a[128:131], v[44:47], v[100:103]\n\tds_read_b64_tr_b16 v[32:33], v226 offset:53248\n\tds_read_b64_tr_b16 v[34:35], v226 offset:55296\n\tv_mov_b32 v75, s36\n\tv_mfma_f32_16x16x32_bf16 v[108:111], a[132:135], v[44:47], v[108:111]\n\tbuffer_store_dwordx4 v[72:75], v8, s[8:11], s68 offen offset:0 sc1\n\tv_cvt_pk_bf16_f32 v76, v78, v79\n\t" \
    "v_cvt_pk_bf16_f32 v77, v80, v81\n\tv_mfma_f32_16x16x32_bf16 v[116:119], a[136:139], v[44:47], v[116:119]\n\tv_cvt_pk_bf16_f32 v78, v82, v83\n\tv_mov_b32 v79, s36\n\ts_waitcnt lgkmcnt(8)\n\tv_mfma_f32_16x16x32_bf16 v[96:99], a[140:143], v[16:19], v[96:99]\n\tds_read_b64_tr_b16 v[36:37], v227 offset:53248\n\tds_read_b64_tr_b16 v[38:39], v227 offset:55296\n\t" \
    "v_mfma_f32_16x16x32_bf16 v[104:107], a[144:147], v[16:19], v[104:107]\n\tbuffer_store_dwordx4 v[76:79], v8, s[8:11], s68 offen offset:1024 sc1\n\tv_cvt_pk_bf16_f32 v80, v84, v85\n\tv_cvt_pk_bf16_f32 v81, v86, v87\n\tv_mfma_f32_16x16x32_bf16 v[112:115], a[148:151], v[16:19], v[112:115]\n\tv_cvt_pk_bf16_f32 v82, v88, v89\n\tv_mov_b32 v83, s36\n\ts_waitcnt lgkmcnt(8)\n\t" \
    "v_mfma_f32_16x16x32_bf16 v[100:103], a[140:143], v[20:23], v[100:103]\n\tds_read_b64_tr_b16 v[40:41], v6 offset:57344\n\tds_read_b64_tr_b16 v[42:43], v6 offset:59392\n\tv_mfma_f32_16x16x32_bf16 v[108:111], a[144:147], v[20:23], v[108:111]\n\tbuffer_store_dwordx4 v[80:83], v8, s[8:11], s68 offen offset:2048 sc1\n\tv_cvt_pk_bf16_f32 v84, v90, v91\n\tv_cvt_pk_bf16_f32 v85, v92, v93\n\tv_mfma_f32_16x16x32_bf16 v[116:119], a[148:151], v[20:23], v[116:119]\n\t" \
    "v_cvt_pk_bf16_f32 v86, v94, v95\n\tv_mov_b32 v87, s36\n\ts_waitcnt lgkmcnt(8)\n\tv_mfma_f32_16x16x32_bf16 v[96:99], a[152:155], v[24:27], v[96:99]\n\tds_read_b64_tr_b16 v[44:45], v7 offset:57344\n\tds_read_b64_tr_b16 v[46:47], v7 offset:59392\n\tv_mfma_f32_16x16x32_bf16 v[104:107], a[156:159], v[24:27], v[104:107]\n\tbuffer_store_dwordx4 v[84:87], v8, s[8:11], s68 offen offset:3072 sc1\n\t" \
    "v_mfma_f32_16x16x32_bf16 v[112:115], a[160:163], v[24:27], v[112:115]\n\ts_waitcnt lgkmcnt(8)\n\tv_mfma_f32_16x16x32_bf16 v[100:103], a[152:155], v[28:31], v[100:103]\n\tds_read_b64_tr_b16 v[16:17], v226 offset:57344\n\tds_read_b64_tr_b16 v[18:19], v226 offset:59392\n\tv_mfma_f32_16x16x32_bf16 v[108:111], a[156:159], v[28:31], v[108:111]\n\tv_mfma_f32_16x16x32_bf16 v[116:119], a[160:163], v[28:31], v[116:119]\n\ts_waitcnt lgkmcnt(8)\n\t" \
    "v_mfma_f32_16x16x32_bf16 v[96:99], a[164:167], v[32:35], v[96:99]\n\tds_read_b64_tr_b16 v[20:21], v227 offset:57344\n\tds_read_b64_tr_b16 v[22:23], v227 offset:59392\n\tv_mfma_f32_16x16x32_bf16 v[104:107], a[168:171], v[32:35], v[104:107]\n\tv_mfma_f32_16x16x32_bf16 v[112:115], a[172:175], v[32:35], v[112:115]\n\ts_waitcnt lgkmcnt(8)\n\tv_mfma_f32_16x16x32_bf16 v[100:103], a[164:167], v[36:39], v[100:103]\n\tds_read_b64_tr_b16 v[24:25], v6 offset:61440\n\t" \
    "ds_read_b64_tr_b16 v[26:27], v6 offset:63488\n\tv_mfma_f32_16x16x32_bf16 v[108:111], a[168:171], v[36:39], v[108:111]\n\tv_mfma_f32_16x16x32_bf16 v[116:119], a[172:175], v[36:39], v[116:119]\n\ts_waitcnt lgkmcnt(8)\n\tv_mfma_f32_16x16x32_bf16 v[96:99], a[176:179], v[40:43], v[96:99]\n\tds_read_b64_tr_b16 v[28:29], v7 offset:61440\n\tds_read_b64_tr_b16 v[30:31], v7 offset:63488\n\tv_mfma_f32_16x16x32_bf16 v[104:107], a[180:183], v[40:43], v[104:107]\n\t" \
    "v_mfma_f32_16x16x32_bf16 v[112:115], a[184:187], v[40:43], v[112:115]\n\ts_waitcnt lgkmcnt(8)\n\tv_mfma_f32_16x16x32_bf16 v[100:103], a[176:179], v[44:47], v[100:103]\n\tds_read_b64_tr_b16 v[32:33], v6 offset:0\n\tds_read_b64_tr_b16 v[34:35], v6 offset:2048\n\tv_mfma_f32_16x16x32_bf16 v[108:111], a[180:183], v[44:47], v[108:111]\n\tv_mfma_f32_16x16x32_bf16 v[116:119], a[184:187], v[44:47], v[116:119]\n\ts_waitcnt vmcnt(7)\n\t" \
    "s_barrier\n\ts_waitcnt lgkmcnt(8)\n\tv_mfma_f32_16x16x32_bf16 v[96:99], a[188:191], v[16:19], v[96:99]\n\tds_read_b64_tr_b16 v[36:37], v7 offset:0\n\tds_read_b64_tr_b16 v[38:39], v7 offset:2048\n\tv_mfma_f32_16x16x32_bf16 v[104:107], a[192:195], v[16:19], v[104:107]\n\tv_mfma_f32_16x16x32_bf16 v[112:115], a[196:199], v[16:19], v[112:115]\n\ts_waitcnt lgkmcnt(8)\n\t" \
    "v_mfma_f32_16x16x32_bf16 v[100:103], a[188:191], v[20:23], v[100:103]\n\tds_read_b64_tr_b16 v[40:41], v4 offset:4096\n\tds_read_b64_tr_b16 v[42:43], v4 offset:6144\n\tv_mfma_f32_16x16x32_bf16 v[108:111], a[192:195], v[20:23], v[108:111]\n\tv_mfma_f32_16x16x32_bf16 v[116:119], a[196:199], v[20:23], v[116:119]\n\ts_waitcnt lgkmcnt(8)\n\tv_mfma_f32_16x16x32_bf16 v[96:99], a[200:203], v[24:27], v[96:99]\n\tds_read_b64_tr_b16 v[44:45], v5 offset:4096\n\t" \
    "ds_read_b64_tr_b16 v[46:47], v5 offset:6144\n\tv_mfma_f32_16x16x32_bf16 v[104:107], a[204:207], v[24:27], v[104:107]\n\tv_mfma_f32_16x16x32_bf16 v[112:115], a[208:211], v[24:27], v[112:115]\n\ts_waitcnt lgkmcnt(8)\n\tv_mfma_f32_16x16x32_bf16 v[100:103], a[200:203], v[28:31], v[100:103]\n\tds_read_b64_tr_b16 v[16:17], v4 offset:8192\n\tds_read_b64_tr_b16 v[18:19], v4 offset:10240\n\tv_mfma_f32_16x16x32_bf16 v[108:111], a[204:207], v[28:31], v[108:111]\n\t" \
    "v_mfma_f32_16x16x32_bf16 v[116:119], a[208:211], v[28:31], v[116:119]\n\ts_waitcnt lgkmcnt(8)\n\tv_mfma_f32_16x16x32_bf16 v[96:99], a[212:215], v[32:35], v[96:99]\n\tds_read_b64_tr_b16 v[20:21], v5 offset:8192\n\tds_read_b64_tr_b16 v[22:23], v5 offset:10240\n\tv_mfma_f32_16x16x32_bf16 v[104:107], a[216:219], v[32:35], v[104:107]\n\tv_mfma_f32_16x16x32_bf16 v[112:115], a[220:223], v[32:35], v[112:115]\n\ts_waitcnt lgkmcnt(8)\n\t" \
    "v_mfma_f32_16x16x32_bf16 v[100:103], a[212:215], v[36:39], v[100:103]\n\tds_read_b64_tr_b16 v[24:25], v4 offset:12288\n\tds_read_b64_tr_b16 v[26:27], v4 offset:14336\n\tv_mfma_f32_16x16x32_bf16 v[108:111], a[216:219], v[36:39], v[108:111]\n\tv_mfma_f32_16x16x32_bf16 v[116:119], a[220:223], v[36:39], v[116:119]\n\ts_waitcnt lgkmcnt(8)\n\tv_mfma_f32_16x16x32_bf16 v[120:123], a[128:131], v[40:43], v[120:123]\n\tds_read_b64_tr_b16 v[28:29], v5 offset:12288\n\t" \
    "ds_read_b64_tr_b16 v[30:31], v5 offset:14336\n\ts_xor_b32 s68, s53, 8\n\tv_mfma_f32_16x16x32_bf16 v[128:131], a[132:135], v[40:43], v[128:131]\n\ts_mul_i32 s68, s68, 12288\n\ts_add_u32 s68, s68, 7213056\n\tv_cvt_pk_bf16_f32 v96, v96, v97\n\tv_mfma_f32_16x16x32_bf16 v[136:139], a[136:139], v[40:43], v[136:139]\n\tv_cvt_pk_bf16_f32 v97, v98, v99\n\t" \
    "v_cvt_pk_bf16_f32 v98, v100, v101\n\ts_waitcnt lgkmcnt(8)\n\tv_mfma_f32_16x16x32_bf16 v[124:127], a[128:131], v[44:47], v[124:127]\n\tds_read_b64_tr_b16 v[32:33], v4 offset:16384\n\tds_read_b64_tr_b16 v[34:35], v4 offset:18432\n\tv_mov_b32 v99, s36\n\tv_mfma_f32_16x16x32_bf16 v[132:135], a[132:135], v[44:47], v[132:135]\n\tbuffer_store_dwordx4 v[96:99], v8, s[8:11], s68 offen offset:0 sc1\n\t" \
    "v_cvt_pk_bf16_f32 v100, v102, v103\n\tv_cvt_pk_bf16_f32 v101, v104, v105\n\tv_mfma_f32_16x16x32_bf16 v[140:143], a[136:139], v[44:47], v[140:143]\n\tv_cvt_pk_bf16_f32 v102, v106, v107\n\tv_mov_b32 v103, s36\n\ts_waitcnt lgkmcnt(8)\n\tv_mfma_f32_16x16x32_bf16 v[120:123], a[140:143], v[16:19], v[120:123]\n\tds_read_b64_tr_b16 v[36:37], v5 offset:16384\n\t" \
    "ds_read_b64_tr_b16 v[38:39], v5 offset:18432\n\tv_mfma_f32_16x16x32_bf16 v[128:131], a[144:147], v[16:19], v[128:131]\n\tbuffer_store_dwordx4 v[100:103], v8, s[8:11], s68 offen offset:1024 sc1\n\tv_cvt_pk_bf16_f32 v104, v108, v109\n\tv_cvt_pk_bf16_f32 v105, v110, v111\n\tv_mfma_f32_16x16x32_bf16 v[136:139], a[148:151], v[16:19], v[136:139]\n\tv_cvt_pk_bf16_f32 v106, v112, v113\n\tv_mov_b32 v107, s36\n\t" \
    "s_waitcnt lgkmcnt(8)\n\tv_mfma_f32_16x16x32_bf16 v[124:127], a[140:143], v[20:23], v[124:127]\n\tds_read_b64_tr_b16 v[40:41], v4 offset:20480\n\tds_read_b64_tr_b16 v[42:43], v4 offset:22528\n\tv_mfma_f32_16x16x32_bf16 v[132:135], a[144:147], v[20:23], v[132:135]\n\tbuffer_store_dwordx4 v[104:107], v8, s[8:11], s68 offen offset:2048 sc1\n\tv_cvt_pk_bf16_f32 v108, v114, v115\n\tv_cvt_pk_bf16_f32 v109, v116, v117\n\t" \
    "v_mfma_f32_16x16x32_bf16 v[140:143], a[148:151], v[20:23], v[140:143]\n\tv_cvt_pk_bf16_f32 v110, v118, v119\n\tv_mov_b32 v111, s36\n\ts_waitcnt lgkmcnt(8)\n\tv_mfma_f32_16x16x32_bf16 v[120:123], a[152:155], v[24:27], v[120:123]\n\tds_read_b64_tr_b16 v[44:45], v5 offset:20480\n\tds_read_b64_tr_b16 v[46:47], v5 offset:22528\n\tv_mfma_f32_16x16x32_bf16 v[128:131], a[156:159], v[24:27], v[128:131]\n\t" \
    "buffer_store_dwordx4 v[108:111], v8, s[8:11], s68 offen offset:3072 sc1\n\tv_mfma_f32_16x16x32_bf16 v[136:139], a[160:163], v[24:27], v[136:139]\n\ts_waitcnt lgkmcnt(8)\n\tv_mfma_f32_16x16x32_bf16 v[124:127], a[152:155], v[28:31], v[124:127]\n\tds_read_b64_tr_b16 v[16:17], v4 offset:24576\n\tds_read_b64_tr_b16 v[18:19], v4 offset:26624\n\tv_mfma_f32_16x16x32_bf16 v[132:135], a[156:159], v[28:31], v[132:135]\n\tv_mfma_f32_16x16x32_bf16 v[140:143], a[160:163], v[28:31], v[140:143]\n\t" \
    "s_waitcnt lgkmcnt(8)\n\tv_mfma_f32_16x16x32_bf16 v[120:123], a[164:167], v[32:35], v[120:123]\n\tds_read_b64_tr_b16 v[20:21], v5 offset:24576\n\tds_read_b64_tr_b16 v[22:23], v5 offset:26624\n\tv_mfma_f32_16x16x32_bf16 v[128:131], a[168:171], v[32:35], v[128:131]\n\tv_mfma_f32_16x16x32_bf16 v[136:139], a[172:175], v[32:35], v[136:139]\n\ts_waitcnt lgkmcnt(8)\n\tv_mfma_f32_16x16x32_bf16 v[124:127], a[164:167], v[36:39], v[124:127]\n\t" \
    "ds_read_b64_tr_b16 v[24:25], v4 offset:28672\n\tds_read_b64_tr_b16 v[26:27], v4 offset:30720\n\tv_mfma_f32_16x16x32_bf16 v[132:135], a[168:171], v[36:39], v[132:135]\n\tv_mfma_f32_16x16x32_bf16 v[140:143], a[172:175], v[36:39], v[140:143]\n\ts_waitcnt lgkmcnt(8)\n\tv_mfma_f32_16x16x32_bf16 v[120:123], a[176:179], v[40:43], v[120:123]\n\tds_read_b64_tr_b16 v[28:29], v5 offset:28672\n\tds_read_b64_tr_b16 v[30:31], v5 offset:30720\n\t" \
    "v_mfma_f32_16x16x32_bf16 v[128:131], a[180:183], v[40:43], v[128:131]\n\tv_mfma_f32_16x16x32_bf16 v[136:139], a[184:187], v[40:43], v[136:139]\n\ts_waitcnt lgkmcnt(8)\n\tv_mfma_f32_16x16x32_bf16 v[124:127], a[176:179], v[44:47], v[124:127]\n\tds_read_b64_tr_b16 v[32:33], v4 offset:32768\n\tds_read_b64_tr_b16 v[34:35], v4 offset:34816\n\tv_mfma_f32_16x16x32_bf16 v[132:135], a[180:183], v[44:47], v[132:135]\n\tv_mfma_f32_16x16x32_bf16 v[140:143], a[184:187], v[44:47], v[140:143]\n\t" \
    "s_waitcnt lgkmcnt(8)\n\tv_mfma_f32_16x16x32_bf16 v[120:123], a[188:191], v[16:19], v[120:123]\n\tds_read_b64_tr_b16 v[36:37], v5 offset:32768\n\tds_read_b64_tr_b16 v[38:39], v5 offset:34816\n\tv_mfma_f32_16x16x32_bf16 v[128:131], a[192:195], v[16:19], v[128:131]\n\tv_mfma_f32_16x16x32_bf16 v[136:139], a[196:199], v[16:19], v[136:139]\n\ts_waitcnt lgkmcnt(8)\n\tv_mfma_f32_16x16x32_bf16 v[124:127], a[188:191], v[20:23], v[124:127]\n\t" \
    "ds_read_b64_tr_b16 v[40:41], v226 offset:61440\n\tds_read_b64_tr_b16 v[42:43], v226 offset:63488\n\tv_mfma_f32_16x16x32_bf16 v[132:135], a[192:195], v[20:23], v[132:135]\n\tv_mfma_f32_16x16x32_bf16 v[140:143], a[196:199], v[20:23], v[140:143]\n\ts_waitcnt lgkmcnt(8)\n\tv_mfma_f32_16x16x32_bf16 v[120:123], a[200:203], v[24:27], v[120:123]\n\tds_read_b64_tr_b16 v[44:45], v227 offset:61440\n\tds_read_b64_tr_b16 v[46:47], v227 offset:63488\n\t" \
    "v_mfma_f32_16x16x32_bf16 v[128:131], a[204:207], v[24:27], v[128:131]\n\tv_mfma_f32_16x16x32_bf16 v[136:139], a[208:211], v[24:27], v[136:139]\n\ts_waitcnt lgkmcnt(8)\n\tv_mfma_f32_16x16x32_bf16 v[124:127], a[200:203], v[28:31], v[124:127]\n\tds_read_b64_tr_b16 v[16:17], v226 offset:0\n\tds_read_b64_tr_b16 v[18:19], v226 offset:2048\n\tv_mfma_f32_16x16x32_bf16 v[132:135], a[204:207], v[28:31], v[132:135]\n\tv_mfma_f32_16x16x32_bf16 v[140:143], a[208:211], v[28:31], v[140:143]\n\t" \
    "s_waitcnt lgkmcnt(8)\n\tv_mfma_f32_16x16x32_bf16 v[120:123], a[212:215], v[32:35], v[120:123]\n\tds_read_b64_tr_b16 v[20:21], v227 offset:0\n\tds_read_b64_tr_b16 v[22:23], v227 offset:2048\n\tv_mfma_f32_16x16x32_bf16 v[128:131], a[216:219], v[32:35], v[128:131]\n\tv_mfma_f32_16x16x32_bf16 v[136:139], a[220:223], v[32:35], v[136:139]\n\ts_waitcnt lgkmcnt(8)\n\tv_mfma_f32_16x16x32_bf16 v[124:127], a[212:215], v[36:39], v[124:127]\n\t" \
    "ds_read_b64_tr_b16 v[24:25], v226 offset:4096\n\tds_read_b64_tr_b16 v[26:27], v226 offset:6144\n\tv_mfma_f32_16x16x32_bf16 v[132:135], a[216:219], v[36:39], v[132:135]\n\tv_mfma_f32_16x16x32_bf16 v[140:143], a[220:223], v[36:39], v[140:143]\n\ts_waitcnt lgkmcnt(8)\n\tv_mfma_f32_16x16x32_bf16 v[48:51], v[152:155], v[40:43], v[48:51]\n\tds_read_b64_tr_b16 v[28:29], v227 offset:4096\n\tds_read_b64_tr_b16 v[30:31], v227 offset:6144\n\t" \
    "s_xor_b32 s68, s53, 4\n\tv_mfma_f32_16x16x32_bf16 v[56:59], v[180:183], v[40:43], v[56:59]\n\ts_mul_i32 s68, s68, 12288\n\ts_add_u32 s68, s68, 7217152\n\tv_cvt_pk_bf16_f32 v120, v120, v121\n\tv_mfma_f32_16x16x32_bf16 v[64:67], v[184:187], v[40:43], v[64:67]\n\tv_cvt_pk_bf16_f32 v121, v122, v123\n\tv_cvt_pk_bf16_f32 v122, v124, v125\n\t" \
    "s_waitcnt lgkmcnt(8)\n\tv_mfma_f32_16x16x32_bf16 v[52:55], v[152:155], v[44:47], v[52:55]\n\tds_read_b64_tr_b16 v[32:33], v226 offset:8192\n\tds_read_b64_tr_b16 v[34:35], v226 offset:10240\n\tv_mov_b32 v123, s36\n\tv_mfma_f32_16x16x32_bf16 v[60:63], v[180:183], v[44:47], v[60:63]\n\tbuffer_store_dwordx4 v[120:123], v8, s[8:11], s68 offen offset:0 sc1\n\tv_cvt_pk_bf16_f32 v124, v126, v127\n\t" \
    "v_cvt_pk_bf16_f32 v125, v128, v129\n\tv_mfma_f32_16x16x32_bf16 v[68:71], v[184:187], v[44:47], v[68:71]\n\tv_cvt_pk_bf16_f32 v126, v130, v131\n\tv_mov_b32 v127, s36\n\ts_waitcnt lgkmcnt(8)\n\tv_mfma_f32_16x16x32_bf16 v[48:51], a[32:35], v[16:19], v[48:51]\n\tds_read_b64_tr_b16 v[36:37], v227 offset:8192\n\tds_read_b64_tr_b16 v[38:39], v227 offset:10240\n\t" \
    "v_mfma_f32_16x16x32_bf16 v[56:59], a[36:39], v[16:19], v[56:59]\n\tbuffer_store_dwordx4 v[124:127], v8, s[8:11], s68 offen offset:1024 sc1\n\tv_cvt_pk_bf16_f32 v128, v132, v133\n\tv_cvt_pk_bf16_f32 v129, v134, v135\n\tv_mfma_f32_16x16x32_bf16 v[64:67], a[40:43], v[16:19], v[64:67]\n\tv_cvt_pk_bf16_f32 v130, v136, v137\n\tv_mov_b32 v131, s36\n\ts_waitcnt lgkmcnt(8)\n\t" \
    "v_mfma_f32_16x16x32_bf16 v[52:55], a[32:35], v[20:23], v[52:55]\n\tds_read_b64_tr_b16 v[40:41], v226 offset:12288\n\tds_read_b64_tr_b16 v[42:43], v226 offset:14336\n\tv_mfma_f32_16x16x32_bf16 v[60:63], a[36:39], v[20:23], v[60:63]\n\tbuffer_store_dwordx4 v[128:131], v8, s[8:11], s68 offen offset:2048 sc1\n\tv_cvt_pk_bf16_f32 v132, v138, v139\n\tv_cvt_pk_bf16_f32 v133, v140, v141\n\tv_mfma_f32_16x16x32_bf16 v[68:71], a[40:43], v[20:23], v[68:71]\n\t" \
    "v_cvt_pk_bf16_f32 v134, v142, v143\n\tv_mov_b32 v135, s36\n\ts_waitcnt lgkmcnt(8)\n\tv_mfma_f32_16x16x32_bf16 v[48:51], v[156:159], v[24:27], v[48:51]\n\tds_read_b64_tr_b16 v[44:45], v227 offset:12288\n\tds_read_b64_tr_b16 v[46:47], v227 offset:14336\n\tv_mfma_f32_16x16x32_bf16 v[56:59], v[160:163], v[24:27], v[56:59]\n\tbuffer_store_dwordx4 v[132:135], v8, s[8:11], s68 offen offset:3072 sc1\n\t" \
    "v_mfma_f32_16x16x32_bf16 v[64:67], v[164:167], v[24:27], v[64:67]\n\ts_waitcnt lgkmcnt(8)\n\tv_mfma_f32_16x16x32_bf16 v[52:55], v[156:159], v[28:31], v[52:55]\n\tds_read_b64_tr_b16 v[16:17], v226 offset:16384\n\tds_read_b64_tr_b16 v[18:19], v226 offset:18432\n\tv_mfma_f32_16x16x32_bf16 v[60:63], v[160:163], v[28:31], v[60:63]\n\tv_mfma_f32_16x16x32_bf16 v[68:71], v[164:167], v[28:31], v[68:71]\n\ts_waitcnt lgkmcnt(8)\n\t" \
    "v_mfma_f32_16x16x32_bf16 v[48:51], v[188:191], v[32:35], v[48:51]\n\tds_read_b64_tr_b16 v[20:21], v227 offset:16384\n\tds_read_b64_tr_b16 v[22:23], v227 offset:18432\n\tv_mfma_f32_16x16x32_bf16 v[56:59], a[0:3], v[32:35], v[56:59]\n\tv_mfma_f32_16x16x32_bf16 v[64:67], a[4:7], v[32:35], v[64:67]\n\ts_waitcnt lgkmcnt(8)\n\tv_mfma_f32_16x16x32_bf16 v[52:55], v[188:191], v[36:39], v[52:55]\n\tds_read_b64_tr_b16 v[24:25], v226 offset:20480\n\t" \
    "ds_read_b64_tr_b16 v[26:27], v226 offset:22528\n\tv_mfma_f32_16x16x32_bf16 v[60:63], a[0:3], v[36:39], v[60:63]\n\tv_mfma_f32_16x16x32_bf16 v[68:71], a[4:7], v[36:39], v[68:71]\n\ts_waitcnt vmcnt(12) lgkmcnt(8)\n\tv_mfma_f32_16x16x32_bf16 v[48:51], a[8:11], v[40:43], v[48:51]\n\tds_read_b64_tr_b16 v[28:29], v227 offset:20480\n\tds_read_b64_tr_b16 v[30:31], v227 offset:22528\n\tv_mfma_f32_16x16x32_bf16 v[56:59], a[12:15], v[40:43], v[56:59]\n\t" \
    "v_permlane16_swap_b32 v192, v194\n\tv_permlane16_swap_b32 v193, v195\n\tv_mfma_f32_16x16x32_bf16 v[64:67], a[16:19], v[40:43], v[64:67]\n\tv_permlane16_swap_b32 v196, v198\n\tv_permlane16_swap_b32 v197, v199\n\ts_waitcnt lgkmcnt(8)\n\tv_mfma_f32_16x16x32_bf16 v[52:55], a[8:11], v[44:47], v[52:55]\n\tds_read_b64_tr_b16 v[32:33], v226 offset:24576\n\t" \
    "ds_read_b64_tr_b16 v[34:35], v226 offset:26624\n\tv_mfma_f32_16x16x32_bf16 v[60:63], a[12:15], v[44:47], v[60:63]\n\tv_permlane16_swap_b32 v200, v202\n\tv_permlane16_swap_b32 v201, v203\n\tv_mfma_f32_16x16x32_bf16 v[68:71], a[16:19], v[44:47], v[68:71]\n\tv_lshlrev_b32 v144, 16, v192\n\tv_and_b32 v145, 0xffff0000, v192\n\ts_waitcnt lgkmcnt(8)\n\t" \
    "v_mfma_f32_16x16x32_bf16 v[48:51], a[44:47], v[16:19], v[48:51]\n\tds_read_b64_tr_b16 v[36:37], v227 offset:24576\n\tds_read_b64_tr_b16 v[38:39], v227 offset:26624\n\tv_mfma_f32_16x16x32_bf16 v[56:59], a[48:51], v[16:19], v[56:59]\n\tv_lshlrev_b32 v146, 16, v193\n\tv_and_b32 v147, 0xffff0000, v193\n\tv_mfma_f32_16x16x32_bf16 v[64:67], a[52:55], v[16:19], v[64:67]\n\tv_lshlrev_b32 v148, 16, v194\n\t" \
    "v_and_b32 v149, 0xffff0000, v194\n\ts_waitcnt lgkmcnt(8)\n\tv_mfma_f32_16x16x32_bf16 v[52:55], a[44:47], v[20:23], v[52:55]\n\tds_read_b64_tr_b16 v[40:41], v226 offset:28672\n\tds_read_b64_tr_b16 v[42:43], v226 offset:30720\n\tv_mfma_f32_16x16x32_bf16 v[60:63], a[48:51], v[20:23], v[60:63]\n\tv_lshlrev_b32 v150, 16, v195\n\tv_and_b32 v151, 0xffff0000, v195\n\t" \
    "v_mfma_f32_16x16x32_bf16 v[68:71], a[52:55], v[20:23], v[68:71]\n\tv_lshlrev_b32 v152, 16, v196\n\tv_and_b32 v153, 0xffff0000, v196\n\ts_waitcnt lgkmcnt(8)\n\tv_mfma_f32_16x16x32_bf16 v[48:51], a[56:59], v[24:27], v[48:51]\n\tds_read_b64_tr_b16 v[44:45], v227 offset:28672\n\tds_read_b64_tr_b16 v[46:47], v227 offset:30720\n\tv_mfma_f32_16x16x32_bf16 v[56:59], a[60:63], v[24:27], v[56:59]\n\t" \
    "v_lshlrev_b32 v154, 16, v197\n\tv_and_b32 v155, 0xffff0000, v197\n\tv_mfma_f32_16x16x32_bf16 v[64:67], a[64:67], v[24:27], v[64:67]\n\tv_lshlrev_b32 v156, 16, v198\n\tv_and_b32 v157, 0xffff0000, v198\n\ts_waitcnt lgkmcnt(8)\n\tv_mfma_f32_16x16x32_bf16 v[52:55], a[56:59], v[28:31], v[52:55]\n\tds_read_b64_tr_b16 v[16:17], v226 offset:32768\n\t" \
    "ds_read_b64_tr_b16 v[18:19], v226 offset:34816\n\tv_mfma_f32_16x16x32_bf16 v[60:63], a[60:63], v[28:31], v[60:63]\n\tv_lshlrev_b32 v158, 16, v199\n\tv_and_b32 v159, 0xffff0000, v199\n\tv_mfma_f32_16x16x32_bf16 v[68:71], a[64:67], v[28:31], v[68:71]\n\tv_lshlrev_b32 v160, 16, v200\n\tv_and_b32 v161, 0xffff0000, v200\n\ts_waitcnt lgkmcnt(8)\n\t" \
    "v_mfma_f32_16x16x32_bf16 v[48:51], a[20:23], v[32:35], v[48:51]\n\tds_read_b64_tr_b16 v[20:21], v227 offset:32768\n\tds_read_b64_tr_b16 v[22:23], v227 offset:34816\n\tv_mfma_f32_16x16x32_bf16 v[56:59], a[24:27], v[32:35], v[56:59]\n\tv_lshlrev_b32 v162, 16, v201\n\tv_and_b32 v163, 0xffff0000, v201\n\tv_mfma_f32_16x16x32_bf16 v[64:67], a[28:31], v[32:35], v[64:67]\n\tv_lshlrev_b32 v164, 16, v202\n\t" \
    "v_and_b32 v165, 0xffff0000, v202\n\ts_waitcnt lgkmcnt(8)\n\tv_mfma_f32_16x16x32_bf16 v[52:55], a[20:23], v[36:39], v[52:55]\n\tds_read_b64_tr_b16 v[24:25], v226 offset:36864\n\tds_read_b64_tr_b16 v[26:27], v226 offset:38912\n\tv_mfma_f32_16x16x32_bf16 v[60:63], a[24:27], v[36:39], v[60:63]\n\tv_lshlrev_b32 v166, 16, v203\n\tv_and_b32 v167, 0xffff0000, v203\n\t" \
    "v_mfma_f32_16x16x32_bf16 v[68:71], a[28:31], v[36:39], v[68:71]\n\tv_fma_f32 v168, v144, v216, v217\n\tv_fma_f32 v169, v145, v216, v217\n\ts_waitcnt lgkmcnt(8)\n\tv_mfma_f32_16x16x32_bf16 v[48:51], a[68:71], v[40:43], v[48:51]\n\tds_read_b64_tr_b16 v[28:29], v227 offset:36864\n\tds_read_b64_tr_b16 v[30:31], v227 offset:38912\n\tv_mfma_f32_16x16x32_bf16 v[56:59], a[72:75], v[40:43], v[56:59]\n\t" \
    "v_fma_f32 v170, v146, v216, v217\n\tv_fma_f32 v171, v147, v216, v217\n\tv_mfma_f32_16x16x32_bf16 v[64:67], a[76:79], v[40:43], v[64:67]\n\tv_fma_f32 v172, v148, v218, v219\n\tv_fma_f32 v173, v149, v218, v219\n\ts_waitcnt lgkmcnt(8)\n\tv_mfma_f32_16x16x32_bf16 v[52:55], a[68:71], v[44:47], v[52:55]\n\tds_read_b64_tr_b16 v[32:33], v226 offset:40960\n\t" \
    "ds_read_b64_tr_b16 v[34:35], v226 offset:43008\n\tv_mfma_f32_16x16x32_bf16 v[60:63], a[72:75], v[44:47], v[60:63]\n\tv_fma_f32 v174, v150, v218, v219\n\tv_fma_f32 v175, v151, v218, v219\n\tv_mfma_f32_16x16x32_bf16 v[68:71], a[76:79], v[44:47], v[68:71]\n\tv_fma_f32 v176, v152, v216, v217\n\tv_fma_f32 v177, v153, v216, v217\n\ts_waitcnt lgkmcnt(8)\n\t" \
    "v_mfma_f32_16x16x32_bf16 v[48:51], a[80:83], v[16:19], v[48:51]\n\tds_read_b64_tr_b16 v[36:37], v227 offset:40960\n\tds_read_b64_tr_b16 v[38:39], v227 offset:43008\n\tv_mfma_f32_16x16x32_bf16 v[56:59], a[84:87], v[16:19], v[56:59]\n\tv_fma_f32 v178, v154, v216, v217\n\tv_fma_f32 v179, v155, v216, v217\n\tv_mfma_f32_16x16x32_bf16 v[64:67], a[88:91], v[16:19], v[64:67]\n\tv_fma_f32 v180, v156, v218, v219\n\t" \
    "v_fma_f32 v181, v157, v218, v219\n\ts_waitcnt lgkmcnt(8)\n\tv_mfma_f32_16x16x32_bf16 v[52:55], a[80:83], v[20:23], v[52:55]\n\tds_read_b64_tr_b16 v[40:41], v226 offset:45056\n\tds_read_b64_tr_b16 v[42:43], v226 offset:47104\n\tv_mfma_f32_16x16x32_bf16 v[60:63], a[84:87], v[20:23], v[60:63]\n\tv_fma_f32 v182, v158, v218, v219\n\tv_fma_f32 v183, v159, v218, v219\n\t" \
    "v_mfma_f32_16x16x32_bf16 v[68:71], a[88:91], v[20:23], v[68:71]\n\tv_fma_f32 v184, v160, v216, v217\n\tv_fma_f32 v185, v161, v216, v217\n\ts_waitcnt lgkmcnt(8)\n\tv_mfma_f32_16x16x32_bf16 v[48:51], a[92:95], v[24:27], v[48:51]\n\tds_read_b64_tr_b16 v[44:45], v227 offset:45056\n\tds_read_b64_tr_b16 v[46:47], v227 offset:47104\n\tv_mfma_f32_16x16x32_bf16 v[56:59], a[96:99], v[24:27], v[56:59]\n\t" \
    "v_fma_f32 v186, v162, v216, v217\n\tv_fma_f32 v187, v163, v216, v217\n\tv_mfma_f32_16x16x32_bf16 v[64:67], a[100:103], v[24:27], v[64:67]\n\tv_fma_f32 v188, v164, v218, v219\n\tv_fma_f32 v189, v165, v218, v219\n\ts_waitcnt lgkmcnt(8)\n\tv_mfma_f32_16x16x32_bf16 v[52:55], a[92:95], v[28:31], v[52:55]\n\tds_read_b64_tr_b16 v[16:17], v6 offset:4096\n\t" \
    "ds_read_b64_tr_b16 v[18:19], v6 offset:6144\n\tv_mfma_f32_16x16x32_bf16 v[60:63], a[96:99], v[28:31], v[60:63]\n\tv_fma_f32 v190, v166, v218, v219\n\tv_fma_f32 v191, v167, v218, v219\n\tv_mfma_f32_16x16x32_bf16 v[68:71], a[100:103], v[28:31], v[68:71]\n\tv_lshlrev_b32 v220, 16, v204\n\tv_and_b32 v221, 0xffff0000, v204\n\ts_waitcnt lgkmcnt(8)\n\t" \
    "v_mfma_f32_16x16x32_bf16 v[48:51], a[104:107], v[32:35], v[48:51]\n\tds_read_b64_tr_b16 v[20:21], v7 offset:4096\n\tds_read_b64_tr_b16 v[22:23], v7 offset:6144\n\ts_add_u32 s68, s55, 0\n\tv_mfma_f32_16x16x32_bf16 v[56:59], a[108:111], v[32:35], v[56:59]\n\tbuffer_load_dwordx4 v[72:75], v8, s[8:11], s68 offen offset:0 sc1\n\tv_mul_f32 v168, v168, v220\n\tv_mfma_f32_16x16x32_bf16 v[64:67], a[112:115], v[32:35], v[64:67]\n\t" \
    "v_mul_f32 v169, v169, v221\n\tv_lshlrev_b32 v222, 16, v205\n\ts_waitcnt lgkmcnt(8)\n\tv_mfma_f32_16x16x32_bf16 v[52:55], a[104:107], v[36:39], v[52:55]\n\tds_read_b64_tr_b16 v[24:25], v6 offset:8192\n\tds_read_b64_tr_b16 v[26:27], v6 offset:10240\n\tv_mfma_f32_16x16x32_bf16 v[60:63], a[108:111], v[36:39], v[60:63]\n\tbuffer_load_dwordx4 v[76:79], v8, s[8:11], s68 offen offset:1024 sc1\n\t" \
    "v_and_b32 v223, 0xffff0000, v205\n\tv_mfma_f32_16x16x32_bf16 v[68:71], a[112:115], v[36:39], v[68:71]\n\tv_mul_f32 v170, v170, v222\n\tv_mul_f32 v171, v171, v223\n\ts_waitcnt lgkmcnt(8)\n\tv_mfma_f32_16x16x32_bf16 v[48:51], a[116:119], v[40:43], v[48:51]\n\tds_read_b64_tr_b16 v[28:29], v7 offset:8192\n\tds_read_b64_tr_b16 v[30:31], v7 offset:10240\n\t" \
    "v_mfma_f32_16x16x32_bf16 v[56:59], a[120:123], v[40:43], v[56:59]\n\tbuffer_load_dwordx4 v[80:83], v8, s[8:11], s68 offen offset:2048 sc1\n\tv_lshlrev_b32 v220, 16, v206\n\tv_mfma_f32_16x16x32_bf16 v[64:67], a[124:127], v[40:43], v[64:67]\n\tv_and_b32 v221, 0xffff0000, v206\n\tv_mul_f32 v172, v172, v220\n\ts_waitcnt lgkmcnt(8)\n\tv_mfma_f32_16x16x32_bf16 v[52:55], a[116:119], v[44:47], v[52:55]\n\t" \
    "ds_read_b64_tr_b16 v[32:33], v6 offset:12288\n\tds_read_b64_tr_b16 v[34:35], v6 offset:14336\n\tv_mfma_f32_16x16x32_bf16 v[60:63], a[120:123], v[44:47], v[60:63]\n\tbuffer_load_dwordx4 v[84:87], v8, s[8:11], s68 offen offset:3072 sc1\n\tv_mul_f32 v173, v173, v221\n\tv_mfma_f32_16x16x32_bf16 v[68:71], a[124:127], v[44:47], v[68:71]\n\tv_lshlrev_b32 v222, 16, v207\n\tv_and_b32 v223, 0xffff0000, v207\n\t" \
    "s_waitcnt lgkmcnt(8)\n\tv_mfma_f32_16x16x32_bf16 v[48:51], a[128:131], v[16:19], v[48:51]\n\tds_read_b64_tr_b16 v[36:37], v7 offset:12288\n\tds_read_b64_tr_b16 v[38:39], v7 offset:14336\n\ts_add_u32 s68, s55, 4096\n\tv_mfma_f32_16x16x32_bf16 v[56:59], a[132:135], v[16:19], v[56:59]\n\tbuffer_load_dwordx4 v[96:99], v8, s[8:11], s68 offen offset:0 sc1\n\tv_mul_f32 v174, v174, v222\n\t" \
    "v_mfma_f32_16x16x32_bf16 v[64:67], a[136:139], v[16:19], v[64:67]\n\tv_mul_f32 v175, v175, v223\n\tv_lshlrev_b32 v220, 16, v208\n\ts_waitcnt lgkmcnt(8)\n\tv_mfma_f32_16x16x32_bf16 v[52:55], a[128:131], v[20:23], v[52:55]\n\tds_read_b64_tr_b16 v[40:41], v6 offset:16384\n\tds_read_b64_tr_b16 v[42:43], v6 offset:18432\n\tv_mfma_f32_16x16x32_bf16 v[60:63], a[132:135], v[20:23], v[60:63]\n\t" \
    "buffer_load_dwordx4 v[100:103], v8, s[8:11], s68 offen offset:1024 sc1\n\tv_and_b32 v221, 0xffff0000, v208\n\tv_mfma_f32_16x16x32_bf16 v[68:71], a[136:139], v[20:23], v[68:71]\n\tv_mul_f32 v176, v176, v220\n\tv_mul_f32 v177, v177, v221\n\ts_waitcnt lgkmcnt(8)\n\tv_mfma_f32_16x16x32_bf16 v[48:51], a[140:143], v[24:27], v[48:51]\n\tds_read_b64_tr_b16 v[44:45], v7 offset:16384\n\t" \
    "ds_read_b64_tr_b16 v[46:47], v7 offset:18432\n\tv_mfma_f32_16x16x32_bf16 v[56:59], a[144:147], v[24:27], v[56:59]\n\tv_lshlrev_b32 v222, 16, v209\n\tv_and_b32 v223, 0xffff0000, v209\n\tv_mfma_f32_16x16x32_bf16 v[64:67], a[148:151], v[24:27], v[64:67]\n\tv_mul_f32 v178, v178, v222\n\tv_mul_f32 v179, v179, v223\n\ts_waitcnt lgkmcnt(8)\n\t" \
    "v_mfma_f32_16x16x32_bf16 v[52:55], a[140:143], v[28:31], v[52:55]\n\tds_read_b64_tr_b16 v[16:17], v6 offset:20480\n\tds_read_b64_tr_b16 v[18:19], v6 offset:22528\n\tv_mfma_f32_16x16x32_bf16 v[60:63], a[144:147], v[28:31], v[60:63]\n\tbuffer_load_dwordx4 v[104:107], v8, s[8:11], s68 offen offset:2048 sc1\n\tv_lshlrev_b32 v220, 16, v210\n\tv_mfma_f32_16x16x32_bf16 v[68:71], a[148:151], v[28:31], v[68:71]\n\tv_and_b32 v221, 0xffff0000, v210\n\t" \
    "v_mul_f32 v180, v180, v220\n\ts_waitcnt lgkmcnt(8)\n\tv_mfma_f32_16x16x32_bf16 v[48:51], a[152:155], v[32:35], v[48:51]\n\tds_read_b64_tr_b16 v[20:21], v7 offset:20480\n\tds_read_b64_tr_b16 v[22:23], v7 offset:22528\n\tv_mfma_f32_16x16x32_bf16 v[56:59], a[156:159], v[32:35], v[56:59]\n\tbuffer_load_dwordx4 v[108:111], v8, s[8:11], s68 offen offset:3072 sc1\n\tv_mul_f32 v181, v181, v221\n\t" \
    "s_add_u32 s68, s55, 8192\n\tv_mfma_f32_16x16x32_bf16 v[64:67], a[160:163], v[32:35], v[64:67]\n\tv_lshlrev_b32 v222, 16, v211\n\tv_and_b32 v223, 0xffff0000, v211\n\ts_waitcnt lgkmcnt(8)\n\tv_mfma_f32_16x16x32_bf16 v[52:55], a[152:155], v[36:39], v[52:55]\n\tds_read_b64_tr_b16 v[24:25], v6 offset:24576\n\tds_read_b64_tr_b16 v[26:27], v6 offset:26624\n\t" \
    "v_mfma_f32_16x16x32_bf16 v[60:63], a[156:159], v[36:39], v[60:63]\n\tbuffer_load_dwordx4 v[120:123], v8, s[8:11], s68 offen offset:0 sc1\n\tv_mul_f32 v182, v182, v222\n\tv_mfma_f32_16x16x32_bf16 v[68:71], a[160:163], v[36:39], v[68:71]\n\tv_mul_f32 v183, v183, v223\n\tv_lshlrev_b32 v220, 16, v212\n\ts_waitcnt lgkmcnt(8)\n\tv_mfma_f32_16x16x32_bf16 v[48:51], a[164:167], v[40:43], v[48:51]\n\t" \
    "ds_read_b64_tr_b16 v[28:29], v7 offset:24576\n\tds_read_b64_tr_b16 v[30:31], v7 offset:26624\n\tv_mfma_f32_16x16x32_bf16 v[56:59], a[168:171], v[40:43], v[56:59]\n\tv_and_b32 v221, 0xffff0000, v212\n\tv_mul_f32 v184, v184, v220\n\tv_mfma_f32_16x16x32_bf16 v[64:67], a[172:175], v[40:43], v[64:67]\n\tv_mul_f32 v185, v185, v221\n\tv_lshlrev_b32 v222, 16, v213\n\t" \
    "s_waitcnt lgkmcnt(8)\n\tv_mfma_f32_16x16x32_bf16 v[52:55], a[164:167], v[44:47], v[52:55]\n\tds_read_b64_tr_b16 v[32:33], v6 offset:28672\n\tds_read_b64_tr_b16 v[34:35], v6 offset:30720\n\tv_mfma_f32_16x16x32_bf16 v[60:63], a[168:171], v[44:47], v[60:63]\n\tbuffer_load_dwordx4 v[124:127], v8, s[8:11], s68 offen offset:1024 sc1\n\tv_and_b32 v223, 0xffff0000, v213\n\tv_mfma_f32_16x16x32_bf16 v[68:71], a[172:175], v[44:47], v[68:71]\n\t" \
    "v_mul_f32 v186, v186, v222\n\tv_mul_f32 v187, v187, v223\n\ts_waitcnt lgkmcnt(8)\n\tv_mfma_f32_16x16x32_bf16 v[48:51], a[176:179], v[16:19], v[48:51]\n\tds_read_b64_tr_b16 v[36:37], v7 offset:28672\n\tds_read_b64_tr_b16 v[38:39], v7 offset:30720\n\tv_mfma_f32_16x16x32_bf16 v[56:59], a[180:183], v[16:19], v[56:59]\n\tbuffer_load_dwordx4 v[128:131], v8, s[8:11], s68 offen offset:2048 sc1\n\t" \
    "v_lshlrev_b32 v220, 16, v214\n\tv_mfma_f32_16x16x32_bf16 v[64:67], a[184:187], v[16:19], v[64:67]\n\tv_and_b32 v221, 0xffff0000, v214\n\tv_mul_f32 v188, v188, v220\n\ts_waitcnt lgkmcnt(8)\n\tv_mfma_f32_16x16x32_bf16 v[52:55], a[176:179], v[20:23], v[52:55]\n\tds_read_b64_tr_b16 v[40:41], v6 offset:32768\n\tds_read_b64_tr_b16 v[42:43], v6 offset:34816\n\t" \
    "v_mfma_f32_16x16x32_bf16 v[60:63], a[180:183], v[20:23], v[60:63]\n\tv_mul_f32 v189, v189, v221\n\tv_lshlrev_b32 v222, 16, v215\n\tv_mfma_f32_16x16x32_bf16 v[68:71], a[184:187], v[20:23], v[68:71]\n\tv_and_b32 v223, 0xffff0000, v215\n\tv_mul_f32 v190, v190, v222\n\ts_waitcnt lgkmcnt(8)\n\tv_mfma_f32_16x16x32_bf16 v[48:51], a[188:191], v[24:27], v[48:51]\n\t" \
    "ds_read_b64_tr_b16 v[44:45], v7 offset:32768\n\tds_read_b64_tr_b16 v[46:47], v7 offset:34816\n\tv_mfma_f32_16x16x32_bf16 v[56:59], a[192:195], v[24:27], v[56:59]\n\tbuffer_load_dwordx4 v[132:135], v8, s[8:11], s68 offen offset:3072 sc1\n\tv_mul_f32 v191, v191, v223\n\tv_mfma_f32_16x16x32_bf16 v[64:67], a[196:199], v[24:27], v[64:67]\n\ts_waitcnt lgkmcnt(8)\n\tv_mfma_f32_16x16x32_bf16 v[52:55], a[188:191], v[28:31], v[52:55]\n\t" \
    "v_mfma_f32_16x16x32_bf16 v[60:63], a[192:195], v[28:31], v[60:63]\n\tv_mfma_f32_16x16x32_bf16 v[68:71], a[196:199], v[28:31], v[68:71]\n\ts_waitcnt lgkmcnt(6)\n\tv_mfma_f32_16x16x32_bf16 v[48:51], a[200:203], v[32:35], v[48:51]\n\tv_mfma_f32_16x16x32_bf16 v[56:59], a[204:207], v[32:35], v[56:59]\n\tv_mfma_f32_16x16x32_bf16 v[64:67], a[208:211], v[32:35], v[64:67]\n\ts_waitcnt lgkmcnt(4)\n\tv_mfma_f32_16x16x32_bf16 v[52:55], a[200:203], v[36:39], v[52:55]\n\t" \
    "v_mfma_f32_16x16x32_bf16 v[60:63], a[204:207], v[36:39], v[60:63]\n\tv_mfma_f32_16x16x32_bf16 v[68:71], a[208:211], v[36:39], v[68:71]\n\ts_waitcnt lgkmcnt(2)\n\tv_mfma_f32_16x16x32_bf16 v[48:51], a[212:215], v[40:43], v[48:51]\n\tv_mfma_f32_16x16x32_bf16 v[56:59], a[216:219], v[40:43], v[56:59]\n\tv_mfma_f32_16x16x32_bf16 v[64:67], a[220:223], v[40:43], v[64:67]\n\ts_waitcnt lgkmcnt(0)\n\tv_mfma_f32_16x16x32_bf16 v[52:55], a[212:215], v[44:47], v[52:55]\n\t" \
    "v_mfma_f32_16x16x32_bf16 v[60:63], a[216:219], v[44:47], v[60:63]\n\tv_mfma_f32_16x16x32_bf16 v[68:71], a[220:223], v[44:47], v[68:71]\n\ts_cmp_eq_u32 s70, 0\n\ts_cbranch_scc1 112f\n\tv_cmp_ne_u64 vcc, s[36:37], v[250:251]\n\ts_cbranch_vccz 114f\n\ts_mov_b32 s71, 1048576\n\t113:\n\t" \
    "s_sleep 1\n\tbuffer_load_dwordx2 v[250:251], v241, s[8:11], s51 offen sc1\n\ts_waitcnt vmcnt(0)\n\tv_cmp_ne_u64 vcc, s[36:37], v[250:251]\n\ts_cbranch_vccz 114f\n\ts_sub_u32 s71, s71, 1\n\ts_cbranch_scc0 113b\n\t114:\n\t" \
    "buffer_load_dwordx4 v[204:207], v8, s[8:11], s56 offen offset:0 sc1\n\tbuffer_load_dwordx4 v[208:211], v8, s[8:11], s56 offen offset:1024 sc1\n\tbuffer_load_dwordx4 v[212:215], v8, s[8:11], s56 offen offset:2048 sc1\n\tv_fma_f32 v168, v144, v216, v217\n\tv_fma_f32 v169, v145, v216, v217\n\tv_fma_f32 v170, v146, v216, v217\n\tv_fma_f32 v171, v147, v216, v217\n\tv_fma_f32 v172, v148, v218, v219\n\t" \
    "v_fma_f32 v173, v149, v218, v219\n\tv_fma_f32 v174, v150, v218, v219\n\tv_fma_f32 v175, v151, v218, v219\n\tv_fma_f32 v176, v152, v216, v217\n\tv_fma_f32 v177, v153, v216, v217\n\tv_fma_f32 v178, v154, v216, v217\n\tv_fma_f32 v179, v155, v216, v217\n\tv_fma_f32 v180, v156, v218, v219\n\t" \
    "v_fma_f32 v181, v157, v218, v219\n\tv_fma_f32 v182, v158, v218, v219\n\tv_fma_f32 v183, v159, v218, v219\n\tv_fma_f32 v184, v160, v216, v217\n\tv_fma_f32 v185, v161, v216, v217\n\tv_fma_f32 v186, v162, v216, v217\n\tv_fma_f32 v187, v163, v216, v217\n\tv_fma_f32 v188, v164, v218, v219\n\t" \
    "v_fma_f32 v189, v165, v218, v219\n\tv_fma_f32 v190, v166, v218, v219\n\tv_fma_f32 v191, v167, v218, v219\n\ts_waitcnt vmcnt(0)\n\tv_lshlrev_b32 v220, 16, v204\n\tv_and_b32 v221, 0xffff0000, v204\n\tv_mul_f32 v168, v168, v220\n\tv_mul_f32 v169, v169, v221\n\t" \
    "v_lshlrev_b32 v222, 16, v205\n\tv_and_b32 v223, 0xffff0000, v205\n\tv_mul_f32 v170, v170, v222\n\tv_mul_f32 v171, v171, v223\n\tv_lshlrev_b32 v220, 16, v206\n\tv_and_b32 v221, 0xffff0000, v206\n\tv_mul_f32 v172, v172, v220\n\tv_mul_f32 v173, v173, v221\n\t" \
    "v_lshlrev_b32 v222, 16, v207\n\tv_and_b32 v223, 0xffff0000, v207\n\tv_mul_f32 v174, v174, v222\n\tv_mul_f32 v175, v175, v223\n\tv_lshlrev_b32 v220, 16, v208\n\tv_and_b32 v221, 0xffff0000, v208\n\tv_mul_f32 v176, v176, v220\n\tv_mul_f32 v177, v177, v221\n\t" \
    "v_lshlrev_b32 v222, 16, v209\n\tv_and_b32 v223, 0xffff0000, v209\n\tv_mul_f32 v178, v178, v222\n\tv_mul_f32 v179, v179, v223\n\tv_lshlrev_b32 v220, 16, v210\n\tv_and_b32 v221, 0xffff0000, v210\n\tv_mul_f32 v180, v180, v220\n\tv_mul_f32 v181, v181, v221\n\t" \
    "v_lshlrev_b32 v222, 16, v211\n\tv_and_b32 v223, 0xffff0000, v211\n\tv_mul_f32 v182, v182, v222\n\tv_mul_f32 v183, v183, v223\n\tv_lshlrev_b32 v220, 16, v212\n\tv_and_b32 v221, 0xffff0000, v212\n\tv_mul_f32 v184, v184, v220\n\tv_mul_f32 v185, v185, v221\n\t" \
    "v_lshlrev_b32 v222, 16, v213\n\tv_and_b32 v223, 0xffff0000, v213\n\tv_mul_f32 v186, v186, v222\n\tv_mul_f32 v187, v187, v223\n\tv_lshlrev_b32 v220, 16, v214\n\tv_and_b32 v221, 0xffff0000, v214\n\tv_mul_f32 v188, v188, v220\n\tv_mul_f32 v189, v189, v221\n\t" \
    "v_lshlrev_b32 v222, 16, v215\n\tv_and_b32 v223, 0xffff0000, v215\n\tv_mul_f32 v190, v190, v222\n\tv_mul_f32 v191, v191, v223\n\t112:\n\ts_mov_b32 s71, 1048576\n\t115:\n\ts_waitcnt vmcnt(0)\n\t" \
    "v_xor_b32 v16, s36, v75\n\tv_xor_b32 v17, s36, v79\n\tv_xor_b32 v18, s36, v83\n\tv_xor_b32 v19, s36, v87\n\tv_xor_b32 v20, s36, v99\n\tv_xor_b32 v21, s36, v103\n\tv_xor_b32 v22, s36, v107\n\tv_xor_b32 v23, s36, v111\n\t" \
    "v_xor_b32 v24, s36, v123\n\tv_xor_b32 v25, s36, v127\n\tv_xor_b32 v26, s36, v131\n\tv_xor_b32 v27, s36, v135\n\tv_or3_b32 v16, v16, v20, v24\n\tv_or3_b32 v17, v17, v21, v25\n\tv_or3_b32 v18, v18, v22, v26\n\tv_or3_b32 v19, v19, v23, v27\n\t" \
    "v_or3_b32 v16, v16, v17, v18\n\tv_or_b32 v16, v16, v19\n\tv_cmp_ne_u32 vcc, 0, v16\n\ts_cbranch_vccz 116f\n\ts_sub_u32 s71, s71, 1\n\ts_cbranch_scc1 116f\n\ts_add_u32 s68, s55, 0\n\tbuffer_load_dwordx4 v[72:75], v8, s[8:11], s68 offen offset:0 sc1\n\t" \
    "buffer_load_dwordx4 v[76:79], v8, s[8:11], s68 offen offset:1024 sc1\n\tbuffer_load_dwordx4 v[80:83], v8, s[8:11], s68 offen offset:2048 sc1\n\tbuffer_load_dwordx4 v[84:87], v8, s[8:11], s68 offen offset:3072 sc1\n\ts_add_u32 s68, s55, 4096\n\tbuffer_load_dwordx4 v[96:99], v8, s[8:11], s68 offen offset:0 sc1\n\tbuffer_load_dwordx4 v[100:103], v8, s[8:11], s68 offen offset:1024 sc1\n\tbuffer_load_dwordx4 v[104:107], v8, s[8:11], s68 offen offset:2048 sc1\n\tbuffer_load_dwordx4 v[108:111], v8, s[8:11], s68 offen offset:3072 sc1\n\t" \
    "s_add_u32 s68, s55, 8192\n\tbuffer_load_dwordx4 v[120:123], v8, s[8:11], s68 offen offset:0 sc1\n\tbuffer_load_dwordx4 v[124:127], v8, s[8:11], s68 offen offset:1024 sc1\n\tbuffer_load_dwordx4 v[128:131], v8, s[8:11], s68 offen offset:2048 sc1\n\tbuffer_load_dwordx4 v[132:135], v8, s[8:11], s68 offen offset:3072 sc1\n\ts_branch 115b\n\t116:\n\ts_mov_b32 s68, 0xbfb8aa3b\n\t" \
    "s_mov_b32 s69, 0xbfb8aa3b\n\tv_lshlrev_b32 v16, 16, v72\n\tv_and_b32 v17, 0xffff0000, v72\n\tv_lshlrev_b32 v18, 16, v73\n\tv_and_b32 v19, 0xffff0000, v73\n\tv_lshlrev_b32 v20, 16, v74\n\tv_and_b32 v21, 0xffff0000, v74\n\tv_lshlrev_b32 v22, 16, v76\n\t" \
    "v_and_b32 v23, 0xffff0000, v76\n\tv_pk_add_f32 v[48:49], v[48:49], v[16:17]\n\tv_pk_add_f32 v[50:51], v[50:51], v[18:19]\n\tv_pk_add_f32 v[52:53], v[52:53], v[20:21]\n\tv_pk_add_f32 v[54:55], v[54:55], v[22:23]\n\tv_lshlrev_b32 v16, 16, v96\n\tv_and_b32 v17, 0xffff0000, v96\n\tv_lshlrev_b32 v18, 16, v97\n\t" \
    "v_and_b32 v19, 0xffff0000, v97\n\tv_lshlrev_b32 v20, 16, v98\n\tv_and_b32 v21, 0xffff0000, v98\n\tv_lshlrev_b32 v22, 16, v100\n\tv_and_b32 v23, 0xffff0000, v100\n\tv_pk_add_f32 v[48:49], v[48:49], v[16:17]\n\tv_pk_add_f32 v[50:51], v[50:51], v[18:19]\n\tv_pk_add_f32 v[52:53], v[52:53], v[20:21]\n\t" \
    "v_pk_add_f32 v[54:55], v[54:55], v[22:23]\n\tv_lshlrev_b32 v16, 16, v120\n\tv_and_b32 v17, 0xffff0000, v120\n\tv_lshlrev_b32 v18, 16, v121\n\tv_and_b32 v19, 0xffff0000, v121\n\tv_lshlrev_b32 v20, 16, v122\n\tv_and_b32 v21, 0xffff0000, v122\n\tv_lshlrev_b32 v22, 16, v124\n\t" \
    "v_and_b32 v23, 0xffff0000, v124\n\tv_pk_add_f32 v[48:49], v[48:49], v[16:17]\n\tv_pk_add_f32 v[50:51], v[50:51], v[18:19]\n\tv_pk_add_f32 v[52:53], v[52:53], v[20:21]\n\tv_pk_add_f32 v[54:55], v[54:55], v[22:23]\n\tv_pk_mul_f32 v[16:17], v[48:49], s[68:69]\n\tv_pk_mul_f32 v[18:19], v[50:51], s[68:69]\n\tv_pk_mul_f32 v[20:21], v[52:53], s[68:69]\n\t" \
    "v_pk_mul_f32 v[22:23], v[54:55], s[68:69]\n\tv_lshlrev_b32 v24, 16, v77\n\tv_exp_f32 v16, v16\n\tv_and_b32 v25, 0xffff0000, v77\n\tv_exp_f32 v17, v17\n\tv_lshlrev_b32 v26, 16, v78\n\tv_exp_f32 v18, v18\n\tv_and_b32 v27, 0xffff0000, v78\n\t" \
    "v_exp_f32 v19, v19\n\tv_lshlrev_b32 v28, 16, v80\n\tv_exp_f32 v20, v20\n\tv_and_b32 v29, 0xffff0000, v80\n\tv_exp_f32 v21, v21\n\tv_lshlrev_b32 v30, 16, v81\n\tv_exp_f32 v22, v22\n\tv_and_b32 v31, 0xffff0000, v81\n\t" \
    "v_exp_f32 v23, v23\n\tv_pk_add_f32 v[56:57], v[56:57], v[24:25]\n\tv_pk_add_f32 v[16:17], v[16:17], 1.0 op_sel_hi:[1,0]\n\tv_pk_add_f32 v[18:19], v[18:19], 1.0 op_sel_hi:[1,0]\n\tv_pk_add_f32 v[20:21], v[20:21], 1.0 op_sel_hi:[1,0]\n\tv_pk_add_f32 v[22:23], v[22:23], 1.0 op_sel_hi:[1,0]\n\tv_pk_add_f32 v[58:59], v[58:59], v[26:27]\n\tv_rcp_f32 v16, v16\n\t" \
    "v_pk_add_f32 v[60:61], v[60:61], v[28:29]\n\tv_rcp_f32 v17, v17\n\tv_pk_add_f32 v[62:63], v[62:63], v[30:31]\n\tv_rcp_f32 v18, v18\n\tv_lshlrev_b32 v24, 16, v101\n\tv_rcp_f32 v19, v19\n\tv_and_b32 v25, 0xffff0000, v101\n\tv_rcp_f32 v20, v20\n\t" \
    "v_lshlrev_b32 v26, 16, v102\n\tv_rcp_f32 v21, v21\n\tv_and_b32 v27, 0xffff0000, v102\n\tv_rcp_f32 v22, v22\n\tv_lshlrev_b32 v28, 16, v104\n\tv_rcp_f32 v23, v23\n\tv_and_b32 v29, 0xffff0000, v104\n\tv_pk_mul_f32 v[48:49], v[48:49], v[168:169]\n\t" \
    "v_pk_mul_f32 v[50:51], v[50:51], v[170:171]\n\tv_pk_mul_f32 v[52:53], v[52:53], v[172:173]\n\tv_pk_mul_f32 v[54:55], v[54:55], v[174:175]\n\tv_pk_fma_f32 v[16:17], v[16:17], v[48:49], v[144:145]\n\tv_pk_fma_f32 v[18:19], v[18:19], v[50:51], v[146:147]\n\tv_pk_fma_f32 v[20:21], v[20:21], v[52:53], v[148:149]\n\tv_pk_fma_f32 v[22:23], v[22:23], v[54:55], v[150:151]\n\tv_cvt_pk_bf16_f32 v48, v16, v17\n\t" \
    "v_cvt_pk_bf16_f32 v49, v18, v19\n\tv_cvt_pk_bf16_f32 v50, v20, v21\n\tv_cvt_pk_bf16_f32 v51, v22, v23\n\ts_nop 0\n\tv_permlane16_swap_b32 v48, v50\n\tv_permlane16_swap_b32 v49, v51\n\tbuffer_store_dwordx4 v[48:51], v243, s[24:27], 0 offen offset:0\n\tv_lshlrev_b32 v30, 16, v105\n\t" \
    "v_and_b32 v31, 0xffff0000, v105\n\tv_pk_add_f32 v[56:57], v[56:57], v[24:25]\n\tv_pk_add_f32 v[58:59], v[58:59], v[26:27]\n\tv_pk_add_f32 v[60:61], v[60:61], v[28:29]\n\tv_pk_add_f32 v[62:63], v[62:63], v[30:31]\n\tv_lshlrev_b32 v24, 16, v125\n\tv_and_b32 v25, 0xffff0000, v125\n\tv_lshlrev_b32 v26, 16, v126\n\t" \
    "v_and_b32 v27, 0xffff0000, v126\n\tv_lshlrev_b32 v28, 16, v128\n\tv_and_b32 v29, 0xffff0000, v128\n\tv_lshlrev_b32 v30, 16, v129\n\tv_and_b32 v31, 0xffff0000, v129\n\tv_pk_add_f32 v[56:57], v[56:57], v[24:25]\n\tv_pk_add_f32 v[58:59], v[58:59], v[26:27]\n\tv_pk_add_f32 v[60:61], v[60:61], v[28:29]\n\t" \
    "v_pk_add_f32 v[62:63], v[62:63], v[30:31]\n\tv_pk_mul_f32 v[24:25], v[56:57], s[68:69]\n\tv_pk_mul_f32 v[26:27], v[58:59], s[68:69]\n\tv_pk_mul_f32 v[28:29], v[60:61], s[68:69]\n\tv_pk_mul_f32 v[30:31], v[62:63], s[68:69]\n\tv_lshlrev_b32 v32, 16, v82\n\tv_exp_f32 v24, v24\n\tv_and_b32 v33, 0xffff0000, v82\n\t" \
    "v_exp_f32 v25, v25\n\tv_lshlrev_b32 v34, 16, v84\n\tv_exp_f32 v26, v26\n\tv_and_b32 v35, 0xffff0000, v84\n\tv_exp_f32 v27, v27\n\tv_lshlrev_b32 v36, 16, v85\n\tv_exp_f32 v28, v28\n\tv_and_b32 v37, 0xffff0000, v85\n\t" \
    "v_exp_f32 v29, v29\n\tv_lshlrev_b32 v38, 16, v86\n\tv_exp_f32 v30, v30\n\tv_and_b32 v39, 0xffff0000, v86\n\tv_exp_f32 v31, v31\n\tv_pk_add_f32 v[64:65], v[64:65], v[32:33]\n\tv_pk_add_f32 v[24:25], v[24:25], 1.0 op_sel_hi:[1,0]\n\tv_pk_add_f32 v[26:27], v[26:27], 1.0 op_sel_hi:[1,0]\n\t" \
    "v_pk_add_f32 v[28:29], v[28:29], 1.0 op_sel_hi:[1,0]\n\tv_pk_add_f32 v[30:31], v[30:31], 1.0 op_sel_hi:[1,0]\n\tv_pk_add_f32 v[66:67], v[66:67], v[34:35]\n\tv_rcp_f32 v24, v24\n\tv_pk_add_f32 v[68:69], v[68:69], v[36:37]\n\tv_rcp_f32 v25, v25\n\tv_pk_add_f32 v[70:71], v[70:71], v[38:39]\n\tv_rcp_f32 v26, v26\n\t" \
    "v_lshlrev_b32 v32, 16, v106\n\tv_rcp_f32 v27, v27\n\tv_and_b32 v33, 0xffff0000, v106\n\tv_rcp_f32 v28, v28\n\tv_lshlrev_b32 v34, 16, v108\n\tv_rcp_f32 v29, v29\n\tv_and_b32 v35, 0xffff0000, v108\n\tv_rcp_f32 v30, v30\n\t" \
    "v_lshlrev_b32 v36, 16, v109\n\tv_rcp_f32 v31, v31\n\tv_and_b32 v37, 0xffff0000, v109\n\tv_pk_mul_f32 v[56:57], v[56:57], v[176:177]\n\tv_pk_mul_f32 v[58:59], v[58:59], v[178:179]\n\tv_pk_mul_f32 v[60:61], v[60:61], v[180:181]\n\tv_pk_mul_f32 v[62:63], v[62:63], v[182:183]\n\tv_pk_fma_f32 v[24:25], v[24:25], v[56:57], v[152:153]\n\t" \
    "v_pk_fma_f32 v[26:27], v[26:27], v[58:59], v[154:155]\n\tv_pk_fma_f32 v[28:29], v[28:29], v[60:61], v[156:157]\n\tv_pk_fma_f32 v[30:31], v[30:31], v[62:63], v[158:159]\n\tv_cvt_pk_bf16_f32 v56, v24, v25\n\tv_cvt_pk_bf16_f32 v57, v26, v27\n\tv_cvt_pk_bf16_f32 v58, v28, v29\n\tv_cvt_pk_bf16_f32 v59, v30, v31\n\ts_nop 0\n\t" \
    "v_permlane16_swap_b32 v56, v58\n\tv_permlane16_swap_b32 v57, v59\n\tbuffer_store_dwordx4 v[56:59], v243, s[24:27], 0 offen offset:32\n\tv_lshlrev_b32 v38, 16, v110\n\tv_and_b32 v39, 0xffff0000, v110\n\tv_pk_add_f32 v[64:65], v[64:65], v[32:33]\n\tv_pk_add_f32 v[66:67], v[66:67], v[34:35]\n\tv_pk_add_f32 v[68:69], v[68:69], v[36:37]\n\t" \
    "v_pk_add_f32 v[70:71], v[70:71], v[38:39]\n\tv_lshlrev_b32 v32, 16, v130\n\tv_and_b32 v33, 0xffff0000, v130\n\tv_lshlrev_b32 v34, 16, v132\n\tv_and_b32 v35, 0xffff0000, v132\n\tv_lshlrev_b32 v36, 16, v133\n\tv_and_b32 v37, 0xffff0000, v133\n\tv_lshlrev_b32 v38, 16, v134\n\t" \
    "v_and_b32 v39, 0xffff0000, v134\n\tv_pk_add_f32 v[64:65], v[64:65], v[32:33]\n\tv_pk_add_f32 v[66:67], v[66:67], v[34:35]\n\tv_pk_add_f32 v[68:69], v[68:69], v[36:37]\n\tv_pk_add_f32 v[70:71], v[70:71], v[38:39]\n\tv_pk_mul_f32 v[32:33], v[64:65], s[68:69]\n\tv_pk_mul_f32 v[34:35], v[66:67], s[68:69]\n\tv_pk_mul_f32 v[36:37], v[68:69], s[68:69]\n\t" \
    "v_pk_mul_f32 v[38:39], v[70:71], s[68:69]\n\tv_exp_f32 v32, v32\n\tv_exp_f32 v33, v33\n\tv_exp_f32 v34, v34\n\tv_exp_f32 v35, v35\n\tv_exp_f32 v36, v36\n\tv_exp_f32 v37, v37\n\tv_exp_f32 v38, v38\n\t" \
    "v_exp_f32 v39, v39\n\tv_pk_add_f32 v[32:33], v[32:33], 1.0 op_sel_hi:[1,0]\n\tv_pk_add_f32 v[34:35], v[34:35], 1.0 op_sel_hi:[1,0]\n\tv_pk_add_f32 v[36:37], v[36:37], 1.0 op_sel_hi:[1,0]\n\tv_pk_add_f32 v[38:39], v[38:39], 1.0 op_sel_hi:[1,0]\n\tv_rcp_f32 v32, v32\n\tv_rcp_f32 v33, v33\n\tv_rcp_f32 v34, v34\n\t" \
    "v_rcp_f32 v35, v35\n\tv_rcp_f32 v36, v36\n\tv_rcp_f32 v37, v37\n\tv_rcp_f32 v38, v38\n\tv_rcp_f32 v39, v39\n\tv_pk_mul_f32 v[64:65], v[64:65], v[184:185]\n\tv_pk_mul_f32 v[66:67], v[66:67], v[186:187]\n\tv_pk_mul_f32 v[68:69], v[68:69], v[188:189]\n\t" \
    "v_pk_mul_f32 v[70:71], v[70:71], v[190:191]\n\tv_pk_fma_f32 v[32:33], v[32:33], v[64:65], v[160:161]\n\tv_pk_fma_f32 v[34:35], v[34:35], v[66:67], v[162:163]\n\tv_pk_fma_f32 v[36:37], v[36:37], v[68:69], v[164:165]\n\tv_pk_fma_f32 v[38:39], v[38:39], v[70:71], v[166:167]\n\tv_cvt_pk_bf16_f32 v64, v32, v33\n\tv_cvt_pk_bf16_f32 v65, v34, v35\n\tv_cvt_pk_bf16_f32 v66, v36, v37\n\t" \
    "v_cvt_pk_bf16_f32 v67, v38, v39\n\ts_nop 0\n\tv_permlane16_swap_b32 v64, v66\n\tv_permlane16_swap_b32 v65, v67\n\tbuffer_store_dwordx4 v[64:67], v243, s[24:27], 0 offen offset:64\n\ts_branch 103f\n\t101:\n\ts_sub_u32 s49, s48, 4096\n\t" \
    "102:\n\ts_add_u32 s49, s49, 4096\n\ts_waitcnt vmcnt(0) lgkmcnt(0)\n\tbuffer_store_dwordx2 v[248:249], off, s[8:11], s49 sc1\n\t103:\n\ts_nop 1\n\t" \
    ""
#define CONV_ASM \
    "ds_read_b64 v[64:65], %3 offset:4032\n\tds_read_b64 v[66:67], %3 offset:4040\n\tds_read_b128 v[96:99], %4 offset:448\n\tds_read_b64 v[68:69], %3 offset:3520\n\tds_read_b64 v[70:71], %3 offset:3528\n\tds_read_b128 v[100:103], %4 offset:960\n\tds_read_b64 v[72:73], %3 offset:3008\n\tds_read_b64 v[74:75], %3 offset:3016\n\t" \
    "ds_read_b128 v[104:107], %4 offset:1472\n\tds_read_b64 v[76:77], %3 offset:2496\n\tds_read_b64 v[78:79], %3 offset:2504\n\tds_read_b128 v[108:111], %4 offset:1984\n\tds_read_b64 v[80:81], %3 offset:1984\n\tds_read_b64 v[82:83], %3 offset:1992\n\tds_read_b128 v[112:115], %4 offset:2496\n\tds_read_b64 v[84:85], %3 offset:1472\n\t" \
    "ds_read_b64 v[86:87], %3 offset:1480\n\tds_read_b128 v[116:119], %4 offset:3008\n\tds_read_b64 v[88:89], %3 offset:960\n\tds_read_b64 v[90:91], %3 offset:968\n\tds_read_b128 v[120:123], %4 offset:3520\n\tds_read_b64 v[92:93], %3 offset:448\n\tds_read_b64 v[94:95], %3 offset:456\n\tds_read_b128 v[124:127], %4 offset:4032\n\t" \
    "s_waitcnt lgkmcnt(0)\n\tv_mfma_f32_16x16x32_bf16 v[32:35], v[64:67], v[96:99], 0\n\tbuffer_load_dword %2, %5, %6, %7 offen\n\tds_read_b64 v[128:129], %3 offset:3968\n\tv_mfma_f32_16x16x32_bf16 v[36:39], v[64:67], v[100:103], 0\n\tds_read_b64 v[130:131], %3 offset:3976\n\tv_mfma_f32_16x16x32_bf16 v[40:43], v[64:67], v[104:107], 0\n\tds_read_b128 v[160:163], %4 offset:384\n\t" \
    "v_mfma_f32_16x16x32_bf16 v[44:47], v[64:67], v[108:111], 0\n\tds_read_b64 v[132:133], %3 offset:3456\n\tv_mfma_f32_16x16x32_bf16 v[48:51], v[64:67], v[112:115], 0\n\tds_read_b64 v[134:135], %3 offset:3464\n\tv_mfma_f32_16x16x32_bf16 v[52:55], v[64:67], v[116:119], 0\n\tds_read_b128 v[164:167], %4 offset:896\n\tv_mfma_f32_16x16x32_bf16 v[56:59], v[64:67], v[120:123], 0\n\tds_read_b64 v[136:137], %3 offset:2944\n\t" \
    "v_mfma_f32_16x16x32_bf16 v[60:63], v[64:67], v[124:127], 0\n\tds_read_b64 v[138:139], %3 offset:2952\n\tv_mfma_f32_16x16x32_bf16 v[36:39], v[68:71], v[96:99], v[36:39]\n\tds_read_b128 v[168:171], %4 offset:1408\n\tv_mfma_f32_16x16x32_bf16 v[40:43], v[68:71], v[100:103], v[40:43]\n\tds_read_b64 v[140:141], %3 offset:2432\n\tv_mfma_f32_16x16x32_bf16 v[44:47], v[68:71], v[104:107], v[44:47]\n\tds_read_b64 v[142:143], %3 offset:2440\n\t" \
    "v_mfma_f32_16x16x32_bf16 v[48:51], v[68:71], v[108:111], v[48:51]\n\tds_read_b128 v[172:175], %4 offset:1920\n\tv_mfma_f32_16x16x32_bf16 v[52:55], v[68:71], v[112:115], v[52:55]\n\tds_read_b64 v[144:145], %3 offset:1920\n\tv_mfma_f32_16x16x32_bf16 v[56:59], v[68:71], v[116:119], v[56:59]\n\tds_read_b64 v[146:147], %3 offset:1928\n\tv_mfma_f32_16x16x32_bf16 v[60:63], v[68:71], v[120:123], v[60:63]\n\tds_read_b128 v[176:179], %4 offset:2432\n\t" \
    "v_mfma_f32_16x16x32_bf16 v[40:43], v[72:75], v[96:99], v[40:43]\n\tds_read_b64 v[148:149], %3 offset:1408\n\tv_mfma_f32_16x16x32_bf16 v[44:47], v[72:75], v[100:103], v[44:47]\n\tds_read_b64 v[150:151], %3 offset:1416\n\tv_mfma_f32_16x16x32_bf16 v[48:51], v[72:75], v[104:107], v[48:51]\n\tds_read_b128 v[180:183], %4 offset:2944\n\tv_mfma_f32_16x16x32_bf16 v[52:55], v[72:75], v[108:111], v[52:55]\n\tds_read_b64 v[152:153], %3 offset:896\n\t" \
    "v_mfma_f32_16x16x32_bf16 v[56:59], v[72:75], v[112:115], v[56:59]\n\tds_read_b64 v[154:155], %3 offset:904\n\tv_mfma_f32_16x16x32_bf16 v[60:63], v[72:75], v[116:119], v[60:63]\n\tds_read_b128 v[184:187], %4 offset:3456\n\tv_mfma_f32_16x16x32_bf16 v[44:47], v[76:79], v[96:99], v[44:47]\n\tds_read_b64 v[156:157], %3 offset:384\n\tv_mfma_f32_16x16x32_bf16 v[48:51], v[76:79], v[100:103], v[48:51]\n\tds_read_b64 v[158:159], %3 offset:392\n\t" \
    "v_mfma_f32_16x16x32_bf16 v[52:55], v[76:79], v[104:107], v[52:55]\n\tds_read_b128 v[188:191], %4 offset:3968\n\tv_mfma_f32_16x16x32_bf16 v[56:59], v[76:79], v[108:111], v[56:59]\n\tv_mfma_f32_16x16x32_bf16 v[60:63], v[76:79], v[112:115], v[60:63]\n\tv_mfma_f32_16x16x32_bf16 v[48:51], v[80:83], v[96:99], v[48:51]\n\tv_mfma_f32_16x16x32_bf16 v[52:55], v[80:83], v[100:103], v[52:55]\n\tv_mfma_f32_16x16x32_bf16 v[56:59], v[80:83], v[104:107], v[56:59]\n\tv_mfma_f32_16x16x32_bf16 v[60:63], v[80:83], v[108:111], v[60:63]\n\t" \
    "v_mfma_f32_16x16x32_bf16 v[52:55], v[84:87], v[96:99], v[52:55]\n\tv_mfma_f32_16x16x32_bf16 v[56:59], v[84:87], v[100:103], v[56:59]\n\tv_mfma_f32_16x16x32_bf16 v[60:63], v[84:87], v[104:107], v[60:63]\n\tv_mfma_f32_16x16x32_bf16 v[56:59], v[88:91], v[96:99], v[56:59]\n\tv_mfma_f32_16x16x32_bf16 v[60:63], v[88:91], v[100:103], v[60:63]\n\tv_mfma_f32_16x16x32_bf16 v[60:63], v[92:95], v[96:99], v[60:63]\n\ts_waitcnt lgkmcnt(0)\n\tv_mfma_f32_16x16x32_bf16 v[32:35], v[128:131], v[160:163], v[32:35]\n\t" \
    "ds_read_b64 v[64:65], %3 offset:3904\n\tv_mfma_f32_16x16x32_bf16 v[36:39], v[128:131], v[164:167], v[36:39]\n\tds_read_b64 v[66:67], %3 offset:3912\n\tv_mfma_f32_16x16x32_bf16 v[40:43], v[128:131], v[168:171], v[40:43]\n\tds_read_b128 v[96:99], %4 offset:320\n\tv_mfma_f32_16x16x32_bf16 v[44:47], v[128:131], v[172:175], v[44:47]\n\tds_read_b64 v[68:69], %3 offset:3392\n\tv_mfma_f32_16x16x32_bf16 v[48:51], v[128:131], v[176:179], v[48:51]\n\t" \
    "ds_read_b64 v[70:71], %3 offset:3400\n\tv_mfma_f32_16x16x32_bf16 v[52:55], v[128:131], v[180:183], v[52:55]\n\tds_read_b128 v[100:103], %4 offset:832\n\tv_mfma_f32_16x16x32_bf16 v[56:59], v[128:131], v[184:187], v[56:59]\n\tds_read_b64 v[72:73], %3 offset:2880\n\tv_mfma_f32_16x16x32_bf16 v[60:63], v[128:131], v[188:191], v[60:63]\n\tds_read_b64 v[74:75], %3 offset:2888\n\tv_mfma_f32_16x16x32_bf16 v[36:39], v[132:135], v[160:163], v[36:39]\n\t" \
    "ds_read_b128 v[104:107], %4 offset:1344\n\tv_mfma_f32_16x16x32_bf16 v[40:43], v[132:135], v[164:167], v[40:43]\n\tds_read_b64 v[76:77], %3 offset:2368\n\tv_mfma_f32_16x16x32_bf16 v[44:47], v[132:135], v[168:171], v[44:47]\n\tds_read_b64 v[78:79], %3 offset:2376\n\tv_mfma_f32_16x16x32_bf16 v[48:51], v[132:135], v[172:175], v[48:51]\n\tds_read_b128 v[108:111], %4 offset:1856\n\tv_mfma_f32_16x16x32_bf16 v[52:55], v[132:135], v[176:179], v[52:55]\n\t" \
    "buffer_load_dword %2, %8, %9, %10 offen\n\tds_read_b64 v[80:81], %3 offset:1856\n\tv_mfma_f32_16x16x32_bf16 v[56:59], v[132:135], v[180:183], v[56:59]\n\tds_read_b64 v[82:83], %3 offset:1864\n\tv_mfma_f32_16x16x32_bf16 v[60:63], v[132:135], v[184:187], v[60:63]\n\tds_read_b128 v[112:115], %4 offset:2368\n\tv_mfma_f32_16x16x32_bf16 v[40:43], v[136:139], v[160:163], v[40:43]\n\tds_read_b64 v[84:85], %3 offset:1344\n\t" \
    "v_mfma_f32_16x16x32_bf16 v[44:47], v[136:139], v[164:167], v[44:47]\n\tds_read_b64 v[86:87], %3 offset:1352\n\tv_mfma_f32_16x16x32_bf16 v[48:51], v[136:139], v[168:171], v[48:51]\n\tds_read_b128 v[116:119], %4 offset:2880\n\tv_mfma_f32_16x16x32_bf16 v[52:55], v[136:139], v[172:175], v[52:55]\n\tds_read_b64 v[88:89], %3 offset:832\n\tv_mfma_f32_16x16x32_bf16 v[56:59], v[136:139], v[176:179], v[56:59]\n\tds_read_b64 v[90:91], %3 offset:840\n\t" \
    "v_mfma_f32_16x16x32_bf16 v[60:63], v[136:139], v[180:183], v[60:63]\n\tds_read_b128 v[120:123], %4 offset:3392\n\tv_mfma_f32_16x16x32_bf16 v[44:47], v[140:143], v[160:163], v[44:47]\n\tds_read_b64 v[92:93], %3 offset:320\n\tv_mfma_f32_16x16x32_bf16 v[48:51], v[140:143], v[164:167], v[48:51]\n\tds_read_b64 v[94:95], %3 offset:328\n\tv_mfma_f32_16x16x32_bf16 v[52:55], v[140:143], v[168:171], v[52:55]\n\tds_read_b128 v[124:127], %4 offset:3904\n\t" \
    "v_mfma_f32_16x16x32_bf16 v[56:59], v[140:143], v[172:175], v[56:59]\n\tv_mfma_f32_16x16x32_bf16 v[60:63], v[140:143], v[176:179], v[60:63]\n\tv_mfma_f32_16x16x32_bf16 v[48:51], v[144:147], v[160:163], v[48:51]\n\tv_mfma_f32_16x16x32_bf16 v[52:55], v[144:147], v[164:167], v[52:55]\n\tv_mfma_f32_16x16x32_bf16 v[56:59], v[144:147], v[168:171], v[56:59]\n\tv_mfma_f32_16x16x32_bf16 v[60:63], v[144:147], v[172:175], v[60:63]\n\tv_mfma_f32_16x16x32_bf16 v[52:55], v[148:151], v[160:163], v[52:55]\n\tv_mfma_f32_16x16x32_bf16 v[56:59], v[148:151], v[164:167], v[56:59]\n\t" \
    "v_mfma_f32_16x16x32_bf16 v[60:63], v[148:151], v[168:171], v[60:63]\n\tv_mfma_f32_16x16x32_bf16 v[56:59], v[152:155], v[160:163], v[56:59]\n\tv_mfma_f32_16x16x32_bf16 v[60:63], v[152:155], v[164:167], v[60:63]\n\tv_mfma_f32_16x16x32_bf16 v[60:63], v[156:159], v[160:163], v[60:63]\n\ts_waitcnt lgkmcnt(0)\n\tv_mfma_f32_16x16x32_bf16 v[32:35], v[64:67], v[96:99], v[32:35]\n\tds_read_b64 v[128:129], %3 offset:3840\n\tv_mfma_f32_16x16x32_bf16 v[36:39], v[64:67], v[100:103], v[36:39]\n\t" \
    "ds_read_b64 v[130:131], %3 offset:3848\n\tv_mfma_f32_16x16x32_bf16 v[40:43], v[64:67], v[104:107], v[40:43]\n\tds_read_b128 v[160:163], %4 offset:256\n\tv_mfma_f32_16x16x32_bf16 v[44:47], v[64:67], v[108:111], v[44:47]\n\tds_read_b64 v[132:133], %3 offset:3328\n\tv_mfma_f32_16x16x32_bf16 v[48:51], v[64:67], v[112:115], v[48:51]\n\tds_read_b64 v[134:135], %3 offset:3336\n\tv_mfma_f32_16x16x32_bf16 v[52:55], v[64:67], v[116:119], v[52:55]\n\t" \
    "ds_read_b128 v[164:167], %4 offset:768\n\tv_mfma_f32_16x16x32_bf16 v[56:59], v[64:67], v[120:123], v[56:59]\n\tds_read_b64 v[136:137], %3 offset:2816\n\tv_mfma_f32_16x16x32_bf16 v[60:63], v[64:67], v[124:127], v[60:63]\n\tds_read_b64 v[138:139], %3 offset:2824\n\tv_mfma_f32_16x16x32_bf16 v[36:39], v[68:71], v[96:99], v[36:39]\n\tds_read_b128 v[168:171], %4 offset:1280\n\tv_mfma_f32_16x16x32_bf16 v[40:43], v[68:71], v[100:103], v[40:43]\n\t" \
    "ds_read_b64 v[140:141], %3 offset:2304\n\tv_mfma_f32_16x16x32_bf16 v[44:47], v[68:71], v[104:107], v[44:47]\n\tds_read_b64 v[142:143], %3 offset:2312\n\tv_mfma_f32_16x16x32_bf16 v[48:51], v[68:71], v[108:111], v[48:51]\n\tds_read_b128 v[172:175], %4 offset:1792\n\tv_mfma_f32_16x16x32_bf16 v[52:55], v[68:71], v[112:115], v[52:55]\n\tds_read_b64 v[144:145], %3 offset:1792\n\tv_mfma_f32_16x16x32_bf16 v[56:59], v[68:71], v[116:119], v[56:59]\n\t" \
    "ds_read_b64 v[146:147], %3 offset:1800\n\tv_mfma_f32_16x16x32_bf16 v[60:63], v[68:71], v[120:123], v[60:63]\n\tds_read_b128 v[176:179], %4 offset:2304\n\tv_mfma_f32_16x16x32_bf16 v[40:43], v[72:75], v[96:99], v[40:43]\n\tds_read_b64 v[148:149], %3 offset:1280\n\tv_mfma_f32_16x16x32_bf16 v[44:47], v[72:75], v[100:103], v[44:47]\n\tds_read_b64 v[150:151], %3 offset:1288\n\tv_mfma_f32_16x16x32_bf16 v[48:51], v[72:75], v[104:107], v[48:51]\n\t" \
    "ds_read_b128 v[180:183], %4 offset:2816\n\tv_mfma_f32_16x16x32_bf16 v[52:55], v[72:75], v[108:111], v[52:55]\n\tds_read_b64 v[152:153], %3 offset:768\n\tv_mfma_f32_16x16x32_bf16 v[56:59], v[72:75], v[112:115], v[56:59]\n\tds_read_b64 v[154:155], %3 offset:776\n\tv_mfma_f32_16x16x32_bf16 v[60:63], v[72:75], v[116:119], v[60:63]\n\tds_read_b128 v[184:187], %4 offset:3328\n\tv_mfma_f32_16x16x32_bf16 v[44:47], v[76:79], v[96:99], v[44:47]\n\t" \
    "ds_read_b64 v[156:157], %3 offset:256\n\tv_mfma_f32_16x16x32_bf16 v[48:51], v[76:79], v[100:103], v[48:51]\n\tds_read_b64 v[158:159], %3 offset:264\n\tv_mfma_f32_16x16x32_bf16 v[52:55], v[76:79], v[104:107], v[52:55]\n\tds_read_b128 v[188:191], %4 offset:3840\n\tv_mfma_f32_16x16x32_bf16 v[56:59], v[76:79], v[108:111], v[56:59]\n\tbuffer_load_dword %2, %11, %12, %13 offen\n\tv_mfma_f32_16x16x32_bf16 v[60:63], v[76:79], v[112:115], v[60:63]\n\t" \
    "v_mfma_f32_16x16x32_bf16 v[48:51], v[80:83], v[96:99], v[48:51]\n\tv_mfma_f32_16x16x32_bf16 v[52:55], v[80:83], v[100:103], v[52:55]\n\tv_mfma_f32_16x16x32_bf16 v[56:59], v[80:83], v[104:107], v[56:59]\n\tv_mfma_f32_16x16x32_bf16 v[60:63], v[80:83], v[108:111], v[60:63]\n\tv_mfma_f32_16x16x32_bf16 v[52:55], v[84:87], v[96:99], v[52:55]\n\tv_mfma_f32_16x16x32_bf16 v[56:59], v[84:87], v[100:103], v[56:59]\n\tv_mfma_f32_16x16x32_bf16 v[60:63], v[84:87], v[104:107], v[60:63]\n\tv_mfma_f32_16x16x32_bf16 v[56:59], v[88:91], v[96:99], v[56:59]\n\t" \
    "v_mfma_f32_16x16x32_bf16 v[60:63], v[88:91], v[100:103], v[60:63]\n\tv_mfma_f32_16x16x32_bf16 v[60:63], v[92:95], v[96:99], v[60:63]\n\ts_waitcnt lgkmcnt(0)\n\tv_mfma_f32_16x16x32_bf16 v[32:35], v[128:131], v[160:163], v[32:35]\n\tds_read_b64 v[64:65], %3 offset:3776\n\tv_mfma_f32_16x16x32_bf16 v[36:39], v[128:131], v[164:167], v[36:39]\n\tds_read_b64 v[66:67], %3 offset:3784\n\tv_mfma_f32_16x16x32_bf16 v[40:43], v[128:131], v[168:171], v[40:43]\n\t" \
    "ds_read_b128 v[96:99], %4 offset:192\n\tv_mfma_f32_16x16x32_bf16 v[44:47], v[128:131], v[172:175], v[44:47]\n\tds_read_b64 v[68:69], %3 offset:3264\n\tv_mfma_f32_16x16x32_bf16 v[48:51], v[128:131], v[176:179], v[48:51]\n\tds_read_b64 v[70:71], %3 offset:3272\n\tv_mfma_f32_16x16x32_bf16 v[52:55], v[128:131], v[180:183], v[52:55]\n\tds_read_b128 v[100:103], %4 offset:704\n\tv_mfma_f32_16x16x32_bf16 v[56:59], v[128:131], v[184:187], v[56:59]\n\t" \
    "ds_read_b64 v[72:73], %3 offset:2752\n\tv_mfma_f32_16x16x32_bf16 v[60:63], v[128:131], v[188:191], v[60:63]\n\tds_read_b64 v[74:75], %3 offset:2760\n\tv_mfma_f32_16x16x32_bf16 v[36:39], v[132:135], v[160:163], v[36:39]\n\tds_read_b128 v[104:107], %4 offset:1216\n\tv_mfma_f32_16x16x32_bf16 v[40:43], v[132:135], v[164:167], v[40:43]\n\tds_read_b64 v[76:77], %3 offset:2240\n\tv_mfma_f32_16x16x32_bf16 v[44:47], v[132:135], v[168:171], v[44:47]\n\t" \
    "ds_read_b64 v[78:79], %3 offset:2248\n\tv_mfma_f32_16x16x32_bf16 v[48:51], v[132:135], v[172:175], v[48:51]\n\tds_read_b128 v[108:111], %4 offset:1728\n\tv_mfma_f32_16x16x32_bf16 v[52:55], v[132:135], v[176:179], v[52:55]\n\tds_read_b64 v[80:81], %3 offset:1728\n\tv_mfma_f32_16x16x32_bf16 v[56:59], v[132:135], v[180:183], v[56:59]\n\tds_read_b64 v[82:83], %3 offset:1736\n\tv_mfma_f32_16x16x32_bf16 v[60:63], v[132:135], v[184:187], v[60:63]\n\t" \
    "ds_read_b128 v[112:115], %4 offset:2240\n\tv_mfma_f32_16x16x32_bf16 v[40:43], v[136:139], v[160:163], v[40:43]\n\tds_read_b64 v[84:85], %3 offset:1216\n\tv_mfma_f32_16x16x32_bf16 v[44:47], v[136:139], v[164:167], v[44:47]\n\tds_read_b64 v[86:87], %3 offset:1224\n\tv_mfma_f32_16x16x32_bf16 v[48:51], v[136:139], v[168:171], v[48:51]\n\tds_read_b128 v[116:119], %4 offset:2752\n\tv_mfma_f32_16x16x32_bf16 v[52:55], v[136:139], v[172:175], v[52:55]\n\t" \
    "ds_read_b64 v[88:89], %3 offset:704\n\tv_mfma_f32_16x16x32_bf16 v[56:59], v[136:139], v[176:179], v[56:59]\n\tds_read_b64 v[90:91], %3 offset:712\n\tv_mfma_f32_16x16x32_bf16 v[60:63], v[136:139], v[180:183], v[60:63]\n\tds_read_b128 v[120:123], %4 offset:3264\n\tv_mfma_f32_16x16x32_bf16 v[44:47], v[140:143], v[160:163], v[44:47]\n\tds_read_b64 v[92:93], %3 offset:192\n\tv_mfma_f32_16x16x32_bf16 v[48:51], v[140:143], v[164:167], v[48:51]\n\t" \
    "ds_read_b64 v[94:95], %3 offset:200\n\tv_mfma_f32_16x16x32_bf16 v[52:55], v[140:143], v[168:171], v[52:55]\n\tds_read_b128 v[124:127], %4 offset:3776\n\tv_mfma_f32_16x16x32_bf16 v[56:59], v[140:143], v[172:175], v[56:59]\n\tv_mfma_f32_16x16x32_bf16 v[60:63], v[140:143], v[176:179], v[60:63]\n\tv_mfma_f32_16x16x32_bf16 v[48:51], v[144:147], v[160:163], v[48:51]\n\tv_mfma_f32_16x16x32_bf16 v[52:55], v[144:147], v[164:167], v[52:55]\n\tv_mfma_f32_16x16x32_bf16 v[56:59], v[144:147], v[168:171], v[56:59]\n\t" \
    "v_mfma_f32_16x16x32_bf16 v[60:63], v[144:147], v[172:175], v[60:63]\n\tv_mfma_f32_16x16x32_bf16 v[52:55], v[148:151], v[160:163], v[52:55]\n\tv_mfma_f32_16x16x32_bf16 v[56:59], v[148:151], v[164:167], v[56:59]\n\tv_mfma_f32_16x16x32_bf16 v[60:63], v[148:151], v[168:171], v[60:63]\n\tv_mfma_f32_16x16x32_bf16 v[56:59], v[152:155], v[160:163], v[56:59]\n\tv_mfma_f32_16x16x32_bf16 v[60:63], v[152:155], v[164:167], v[60:63]\n\tv_mfma_f32_16x16x32_bf16 v[60:63], v[156:159], v[160:163], v[60:63]\n\ts_waitcnt lgkmcnt(0)\n\t" \
    "v_mfma_f32_16x16x32_bf16 v[32:35], v[64:67], v[96:99], v[32:35]\n\tbuffer_load_dword %2, %14, %15, %16 offen\n\tds_read_b64 v[128:129], %3 offset:3712\n\tv_mfma_f32_16x16x32_bf16 v[36:39], v[64:67], v[100:103], v[36:39]\n\tds_read_b64 v[130:131], %3 offset:3720\n\tv_mfma_f32_16x16x32_bf16 v[40:43], v[64:67], v[104:107], v[40:43]\n\tds_read_b128 v[160:163], %4 offset:128\n\tv_mfma_f32_16x16x32_bf16 v[44:47], v[64:67], v[108:111], v[44:47]\n\t" \
    "ds_read_b64 v[132:133], %3 offset:3200\n\tv_mfma_f32_16x16x32_bf16 v[48:51], v[64:67], v[112:115], v[48:51]\n\tds_read_b64 v[134:135], %3 offset:3208\n\tv_mfma_f32_16x16x32_bf16 v[52:55], v[64:67], v[116:119], v[52:55]\n\tds_read_b128 v[164:167], %4 offset:640\n\tv_mfma_f32_16x16x32_bf16 v[56:59], v[64:67], v[120:123], v[56:59]\n\tds_read_b64 v[136:137], %3 offset:2688\n\tv_mfma_f32_16x16x32_bf16 v[60:63], v[64:67], v[124:127], v[60:63]\n\t" \
    "ds_read_b64 v[138:139], %3 offset:2696\n\tv_mfma_f32_16x16x32_bf16 v[36:39], v[68:71], v[96:99], v[36:39]\n\tds_read_b128 v[168:171], %4 offset:1152\n\tv_mfma_f32_16x16x32_bf16 v[40:43], v[68:71], v[100:103], v[40:43]\n\tds_read_b64 v[140:141], %3 offset:2176\n\tv_mfma_f32_16x16x32_bf16 v[44:47], v[68:71], v[104:107], v[44:47]\n\tds_read_b64 v[142:143], %3 offset:2184\n\tv_mfma_f32_16x16x32_bf16 v[48:51], v[68:71], v[108:111], v[48:51]\n\t" \
    "ds_read_b128 v[172:175], %4 offset:1664\n\tv_mfma_f32_16x16x32_bf16 v[52:55], v[68:71], v[112:115], v[52:55]\n\tds_read_b64 v[144:145], %3 offset:1664\n\tv_mfma_f32_16x16x32_bf16 v[56:59], v[68:71], v[116:119], v[56:59]\n\tds_read_b64 v[146:147], %3 offset:1672\n\tv_mfma_f32_16x16x32_bf16 v[60:63], v[68:71], v[120:123], v[60:63]\n\tds_read_b128 v[176:179], %4 offset:2176\n\tv_mfma_f32_16x16x32_bf16 v[40:43], v[72:75], v[96:99], v[40:43]\n\t" \
    "ds_read_b64 v[148:149], %3 offset:1152\n\tv_mfma_f32_16x16x32_bf16 v[44:47], v[72:75], v[100:103], v[44:47]\n\tds_read_b64 v[150:151], %3 offset:1160\n\tv_mfma_f32_16x16x32_bf16 v[48:51], v[72:75], v[104:107], v[48:51]\n\tds_read_b128 v[180:183], %4 offset:2688\n\tv_mfma_f32_16x16x32_bf16 v[52:55], v[72:75], v[108:111], v[52:55]\n\tds_read_b64 v[152:153], %3 offset:640\n\tv_mfma_f32_16x16x32_bf16 v[56:59], v[72:75], v[112:115], v[56:59]\n\t" \
    "ds_read_b64 v[154:155], %3 offset:648\n\tv_mfma_f32_16x16x32_bf16 v[60:63], v[72:75], v[116:119], v[60:63]\n\tds_read_b128 v[184:187], %4 offset:3200\n\tv_mfma_f32_16x16x32_bf16 v[44:47], v[76:79], v[96:99], v[44:47]\n\tds_read_b64 v[156:157], %3 offset:128\n\tv_mfma_f32_16x16x32_bf16 v[48:51], v[76:79], v[100:103], v[48:51]\n\tds_read_b64 v[158:159], %3 offset:136\n\tv_mfma_f32_16x16x32_bf16 v[52:55], v[76:79], v[104:107], v[52:55]\n\t" \
    "ds_read_b128 v[188:191], %4 offset:3712\n\tv_mfma_f32_16x16x32_bf16 v[56:59], v[76:79], v[108:111], v[56:59]\n\tv_mfma_f32_16x16x32_bf16 v[60:63], v[76:79], v[112:115], v[60:63]\n\tv_mfma_f32_16x16x32_bf16 v[48:51], v[80:83], v[96:99], v[48:51]\n\tv_mfma_f32_16x16x32_bf16 v[52:55], v[80:83], v[100:103], v[52:55]\n\tv_mfma_f32_16x16x32_bf16 v[56:59], v[80:83], v[104:107], v[56:59]\n\tv_mfma_f32_16x16x32_bf16 v[60:63], v[80:83], v[108:111], v[60:63]\n\tv_mfma_f32_16x16x32_bf16 v[52:55], v[84:87], v[96:99], v[52:55]\n\t" \
    "v_mfma_f32_16x16x32_bf16 v[56:59], v[84:87], v[100:103], v[56:59]\n\tv_mfma_f32_16x16x32_bf16 v[60:63], v[84:87], v[104:107], v[60:63]\n\tv_mfma_f32_16x16x32_bf16 v[56:59], v[88:91], v[96:99], v[56:59]\n\tv_mfma_f32_16x16x32_bf16 v[60:63], v[88:91], v[100:103], v[60:63]\n\tv_mfma_f32_16x16x32_bf16 v[60:63], v[92:95], v[96:99], v[60:63]\n\ts_waitcnt lgkmcnt(0)\n\tv_mfma_f32_16x16x32_bf16 v[32:35], v[128:131], v[160:163], v[32:35]\n\tds_read_b64 v[64:65], %3 offset:3648\n\t" \
    "v_mfma_f32_16x16x32_bf16 v[36:39], v[128:131], v[164:167], v[36:39]\n\tds_read_b64 v[66:67], %3 offset:3656\n\tv_mfma_f32_16x16x32_bf16 v[40:43], v[128:131], v[168:171], v[40:43]\n\tds_read_b128 v[96:99], %4 offset:64\n\tv_mfma_f32_16x16x32_bf16 v[44:47], v[128:131], v[172:175], v[44:47]\n\tds_read_b64 v[68:69], %3 offset:3136\n\tv_mfma_f32_16x16x32_bf16 v[48:51], v[128:131], v[176:179], v[48:51]\n\tds_read_b64 v[70:71], %3 offset:3144\n\t" \
    "v_mfma_f32_16x16x32_bf16 v[52:55], v[128:131], v[180:183], v[52:55]\n\tds_read_b128 v[100:103], %4 offset:576\n\tv_mfma_f32_16x16x32_bf16 v[56:59], v[128:131], v[184:187], v[56:59]\n\tds_read_b64 v[72:73], %3 offset:2624\n\tv_mfma_f32_16x16x32_bf16 v[60:63], v[128:131], v[188:191], v[60:63]\n\tds_read_b64 v[74:75], %3 offset:2632\n\tv_mfma_f32_16x16x32_bf16 v[36:39], v[132:135], v[160:163], v[36:39]\n\tds_read_b128 v[104:107], %4 offset:1088\n\t" \
    "v_mfma_f32_16x16x32_bf16 v[40:43], v[132:135], v[164:167], v[40:43]\n\tds_read_b64 v[76:77], %3 offset:2112\n\tv_mfma_f32_16x16x32_bf16 v[44:47], v[132:135], v[168:171], v[44:47]\n\tds_read_b64 v[78:79], %3 offset:2120\n\tv_mfma_f32_16x16x32_bf16 v[48:51], v[132:135], v[172:175], v[48:51]\n\tds_read_b128 v[108:111], %4 offset:1600\n\tv_mfma_f32_16x16x32_bf16 v[52:55], v[132:135], v[176:179], v[52:55]\n\tbuffer_load_dword %2, %17, %18, %19 offen\n\t" \
    "ds_read_b64 v[80:81], %3 offset:1600\n\tv_mfma_f32_16x16x32_bf16 v[56:59], v[132:135], v[180:183], v[56:59]\n\tds_read_b64 v[82:83], %3 offset:1608\n\tv_mfma_f32_16x16x32_bf16 v[60:63], v[132:135], v[184:187], v[60:63]\n\tds_read_b128 v[112:115], %4 offset:2112\n\tv_mfma_f32_16x16x32_bf16 v[40:43], v[136:139], v[160:163], v[40:43]\n\tds_read_b64 v[84:85], %3 offset:1088\n\tv_mfma_f32_16x16x32_bf16 v[44:47], v[136:139], v[164:167], v[44:47]\n\t" \
    "ds_read_b64 v[86:87], %3 offset:1096\n\tv_mfma_f32_16x16x32_bf16 v[48:51], v[136:139], v[168:171], v[48:51]\n\tds_read_b128 v[116:119], %4 offset:2624\n\tv_mfma_f32_16x16x32_bf16 v[52:55], v[136:139], v[172:175], v[52:55]\n\tds_read_b64 v[88:89], %3 offset:576\n\tv_mfma_f32_16x16x32_bf16 v[56:59], v[136:139], v[176:179], v[56:59]\n\tds_read_b64 v[90:91], %3 offset:584\n\tv_mfma_f32_16x16x32_bf16 v[60:63], v[136:139], v[180:183], v[60:63]\n\t" \
    "ds_read_b128 v[120:123], %4 offset:3136\n\tv_mfma_f32_16x16x32_bf16 v[44:47], v[140:143], v[160:163], v[44:47]\n\tds_read_b64 v[92:93], %3 offset:64\n\tv_mfma_f32_16x16x32_bf16 v[48:51], v[140:143], v[164:167], v[48:51]\n\tds_read_b64 v[94:95], %3 offset:72\n\tv_mfma_f32_16x16x32_bf16 v[52:55], v[140:143], v[168:171], v[52:55]\n\tds_read_b128 v[124:127], %4 offset:3648\n\tv_mfma_f32_16x16x32_bf16 v[56:59], v[140:143], v[172:175], v[56:59]\n\t" \
    "v_mfma_f32_16x16x32_bf16 v[60:63], v[140:143], v[176:179], v[60:63]\n\tv_mfma_f32_16x16x32_bf16 v[48:51], v[144:147], v[160:163], v[48:51]\n\tv_mfma_f32_16x16x32_bf16 v[52:55], v[144:147], v[164:167], v[52:55]\n\tv_mfma_f32_16x16x32_bf16 v[56:59], v[144:147], v[168:171], v[56:59]\n\tv_mfma_f32_16x16x32_bf16 v[60:63], v[144:147], v[172:175], v[60:63]\n\tv_mfma_f32_16x16x32_bf16 v[52:55], v[148:151], v[160:163], v[52:55]\n\tv_mfma_f32_16x16x32_bf16 v[56:59], v[148:151], v[164:167], v[56:59]\n\tv_mfma_f32_16x16x32_bf16 v[60:63], v[148:151], v[168:171], v[60:63]\n\t" \
    "v_mfma_f32_16x16x32_bf16 v[56:59], v[152:155], v[160:163], v[56:59]\n\tv_mfma_f32_16x16x32_bf16 v[60:63], v[152:155], v[164:167], v[60:63]\n\tv_mfma_f32_16x16x32_bf16 v[60:63], v[156:159], v[160:163], v[60:63]\n\ts_waitcnt lgkmcnt(0)\n\tv_mfma_f32_16x16x32_bf16 v[32:35], v[64:67], v[96:99], v[32:35]\n\tds_read_b64 v[128:129], %3 offset:3584\n\tv_mfma_f32_16x16x32_bf16 v[36:39], v[64:67], v[100:103], v[36:39]\n\tds_read_b64 v[130:131], %3 offset:3592\n\t" \
    "v_mfma_f32_16x16x32_bf16 v[40:43], v[64:67], v[104:107], v[40:43]\n\tds_read_b128 v[160:163], %4 offset:0\n\tv_mfma_f32_16x16x32_bf16 v[44:47], v[64:67], v[108:111], v[44:47]\n\tds_read_b64 v[132:133], %3 offset:3072\n\tv_mfma_f32_16x16x32_bf16 v[48:51], v[64:67], v[112:115], v[48:51]\n\tds_read_b64 v[134:135], %3 offset:3080\n\tv_mfma_f32_16x16x32_bf16 v[52:55], v[64:67], v[116:119], v[52:55]\n\tds_read_b128 v[164:167], %4 offset:512\n\t" \
    "v_mfma_f32_16x16x32_bf16 v[56:59], v[64:67], v[120:123], v[56:59]\n\tds_read_b64 v[136:137], %3 offset:2560\n\tv_mfma_f32_16x16x32_bf16 v[60:63], v[64:67], v[124:127], v[60:63]\n\tds_read_b64 v[138:139], %3 offset:2568\n\tv_mfma_f32_16x16x32_bf16 v[36:39], v[68:71], v[96:99], v[36:39]\n\tds_read_b128 v[168:171], %4 offset:1024\n\tv_mfma_f32_16x16x32_bf16 v[40:43], v[68:71], v[100:103], v[40:43]\n\tds_read_b64 v[140:141], %3 offset:2048\n\t" \
    "v_mfma_f32_16x16x32_bf16 v[44:47], v[68:71], v[104:107], v[44:47]\n\tds_read_b64 v[142:143], %3 offset:2056\n\tv_mfma_f32_16x16x32_bf16 v[48:51], v[68:71], v[108:111], v[48:51]\n\tds_read_b128 v[172:175], %4 offset:1536\n\tv_mfma_f32_16x16x32_bf16 v[52:55], v[68:71], v[112:115], v[52:55]\n\tds_read_b64 v[144:145], %3 offset:1536\n\tv_mfma_f32_16x16x32_bf16 v[56:59], v[68:71], v[116:119], v[56:59]\n\tds_read_b64 v[146:147], %3 offset:1544\n\t" \
    "v_mfma_f32_16x16x32_bf16 v[60:63], v[68:71], v[120:123], v[60:63]\n\tds_read_b128 v[176:179], %4 offset:2048\n\tv_mfma_f32_16x16x32_bf16 v[40:43], v[72:75], v[96:99], v[40:43]\n\tds_read_b64 v[148:149], %3 offset:1024\n\tv_mfma_f32_16x16x32_bf16 v[44:47], v[72:75], v[100:103], v[44:47]\n\tds_read_b64 v[150:151], %3 offset:1032\n\tv_mfma_f32_16x16x32_bf16 v[48:51], v[72:75], v[104:107], v[48:51]\n\tds_read_b128 v[180:183], %4 offset:2560\n\t" \
    "v_mfma_f32_16x16x32_bf16 v[52:55], v[72:75], v[108:111], v[52:55]\n\tds_read_b64 v[152:153], %3 offset:512\n\tv_mfma_f32_16x16x32_bf16 v[56:59], v[72:75], v[112:115], v[56:59]\n\tds_read_b64 v[154:155], %3 offset:520\n\tv_mfma_f32_16x16x32_bf16 v[60:63], v[72:75], v[116:119], v[60:63]\n\tds_read_b128 v[184:187], %4 offset:3072\n\tv_mfma_f32_16x16x32_bf16 v[44:47], v[76:79], v[96:99], v[44:47]\n\tds_read_b64 v[156:157], %3 offset:0\n\t" \
    "v_mfma_f32_16x16x32_bf16 v[48:51], v[76:79], v[100:103], v[48:51]\n\tds_read_b64 v[158:159], %3 offset:8\n\tv_mfma_f32_16x16x32_bf16 v[52:55], v[76:79], v[104:107], v[52:55]\n\tds_read_b128 v[188:191], %4 offset:3584\n\tv_mfma_f32_16x16x32_bf16 v[56:59], v[76:79], v[108:111], v[56:59]\n\tbuffer_load_dword %2, %20, %21, %22 offen\n\tv_mfma_f32_16x16x32_bf16 v[60:63], v[76:79], v[112:115], v[60:63]\n\tv_mfma_f32_16x16x32_bf16 v[48:51], v[80:83], v[96:99], v[48:51]\n\t" \
    "v_mfma_f32_16x16x32_bf16 v[52:55], v[80:83], v[100:103], v[52:55]\n\tv_mfma_f32_16x16x32_bf16 v[56:59], v[80:83], v[104:107], v[56:59]\n\tv_mfma_f32_16x16x32_bf16 v[60:63], v[80:83], v[108:111], v[60:63]\n\tv_mfma_f32_16x16x32_bf16 v[52:55], v[84:87], v[96:99], v[52:55]\n\tv_mfma_f32_16x16x32_bf16 v[56:59], v[84:87], v[100:103], v[56:59]\n\tv_mfma_f32_16x16x32_bf16 v[60:63], v[84:87], v[104:107], v[60:63]\n\tv_mfma_f32_16x16x32_bf16 v[56:59], v[88:91], v[96:99], v[56:59]\n\tv_mfma_f32_16x16x32_bf16 v[60:63], v[88:91], v[100:103], v[60:63]\n\t" \
    "v_mfma_f32_16x16x32_bf16 v[60:63], v[92:95], v[96:99], v[60:63]\n\ts_waitcnt lgkmcnt(0)\n\tv_mfma_f32_16x16x32_bf16 v[32:35], v[128:131], v[160:163], v[32:35]\n\tv_mfma_f32_16x16x32_bf16 v[36:39], v[128:131], v[164:167], v[36:39]\n\tv_mfma_f32_16x16x32_bf16 v[40:43], v[128:131], v[168:171], v[40:43]\n\tv_mfma_f32_16x16x32_bf16 v[44:47], v[128:131], v[172:175], v[44:47]\n\tv_mfma_f32_16x16x32_bf16 v[48:51], v[128:131], v[176:179], v[48:51]\n\tv_mfma_f32_16x16x32_bf16 v[52:55], v[128:131], v[180:183], v[52:55]\n\t" \
    "v_mfma_f32_16x16x32_bf16 v[56:59], v[128:131], v[184:187], v[56:59]\n\tv_mfma_f32_16x16x32_bf16 v[60:63], v[128:131], v[188:191], v[60:63]\n\tv_mfma_f32_16x16x32_bf16 v[36:39], v[132:135], v[160:163], v[36:39]\n\tv_mfma_f32_16x16x32_bf16 v[40:43], v[132:135], v[164:167], v[40:43]\n\tv_mfma_f32_16x16x32_bf16 v[44:47], v[132:135], v[168:171], v[44:47]\n\tv_mfma_f32_16x16x32_bf16 v[48:51], v[132:135], v[172:175], v[48:51]\n\tv_mfma_f32_16x16x32_bf16 v[52:55], v[132:135], v[176:179], v[52:55]\n\tv_mfma_f32_16x16x32_bf16 v[56:59], v[132:135], v[180:183], v[56:59]\n\t" \
    "v_mfma_f32_16x16x32_bf16 v[60:63], v[132:135], v[184:187], v[60:63]\n\tv_mfma_f32_16x16x32_bf16 v[40:43], v[136:139], v[160:163], v[40:43]\n\tv_mfma_f32_16x16x32_bf16 v[44:47], v[136:139], v[164:167], v[44:47]\n\tv_mfma_f32_16x16x32_bf16 v[48:51], v[136:139], v[168:171], v[48:51]\n\tv_mfma_f32_16x16x32_bf16 v[52:55], v[136:139], v[172:175], v[52:55]\n\tv_mfma_f32_16x16x32_bf16 v[56:59], v[136:139], v[176:179], v[56:59]\n\tv_mfma_f32_16x16x32_bf16 v[60:63], v[136:139], v[180:183], v[60:63]\n\tv_mfma_f32_16x16x32_bf16 v[44:47], v[140:143], v[160:163], v[44:47]\n\t" \
    "v_mfma_f32_16x16x32_bf16 v[48:51], v[140:143], v[164:167], v[48:51]\n\tv_mfma_f32_16x16x32_bf16 v[52:55], v[140:143], v[168:171], v[52:55]\n\tv_mfma_f32_16x16x32_bf16 v[56:59], v[140:143], v[172:175], v[56:59]\n\tv_mfma_f32_16x16x32_bf16 v[60:63], v[140:143], v[176:179], v[60:63]\n\tv_mfma_f32_16x16x32_bf16 v[48:51], v[144:147], v[160:163], v[48:51]\n\tv_mfma_f32_16x16x32_bf16 v[52:55], v[144:147], v[164:167], v[52:55]\n\tv_mfma_f32_16x16x32_bf16 v[56:59], v[144:147], v[168:171], v[56:59]\n\tv_mfma_f32_16x16x32_bf16 v[60:63], v[144:147], v[172:175], v[60:63]\n\t" \
    "v_mfma_f32_16x16x32_bf16 v[52:55], v[148:151], v[160:163], v[52:55]\n\tv_mfma_f32_16x16x32_bf16 v[56:59], v[148:151], v[164:167], v[56:59]\n\tv_mfma_f32_16x16x32_bf16 v[60:63], v[148:151], v[168:171], v[60:63]\n\tv_mfma_f32_16x16x32_bf16 v[56:59], v[152:155], v[160:163], v[56:59]\n\tv_mfma_f32_16x16x32_bf16 v[60:63], v[152:155], v[164:167], v[60:63]\n\tv_mfma_f32_16x16x32_bf16 v[60:63], v[156:159], v[160:163], v[60:63]\n\ts_nop 7\n\ts_nop 3\n\t" \
    ""
#define NTOUCH 6
#define CONV_CLOB "v64", "v65", "v66", "v67", "v68", "v69", "v70", "v71", "v72", "v73", "v74", "v75", "v76", "v77", "v78", "v79", "v80", "v81", "v82", "v83", "v84", "v85", "v86", "v87", "v88", "v89", "v90", "v91", "v92", "v93", "v94", "v95", "v96", "v97", "v98", "v99", "v100", "v101", "v102", "v103", "v104", "v105", "v106", "v107", "v108", "v109", "v110", "v111", "v112", "v113", "v114", "v115", "v116", "v117", "v118", "v119", "v120", "v121", "v122", "v123", "v124", "v125", "v126", "v127", "v128", "v129", "v130", "v131", "v132", "v133", "v134", "v135", "v136", "v137", "v138", "v139", "v140", "v141", "v142", "v143", "v144", "v145", "v146", "v147", "v148", "v149", "v150", "v151", "v152", "v153", "v154", "v155", "v156", "v157", "v158", "v159", "v160", "v161", "v162", "v163", "v164", "v165", "v166", "v167", "v168", "v169", "v170", "v171", "v172", "v173", "v174", "v175", "v176", "v177", "v178", "v179", "v180", "v181", "v182", "v183", "v184", "v185", "v186", "v187", "v188", "v189", "v190", "v191"
#define MAIN_CLOB "s4", "s5", "s6", "s7", "s8", "s9", "s10", "s11", "s12", "s13", "s14", "s15", "s16", "s17", "s18", "s19", "s20", "s21", "s22", "s23", "s24", "s25", "s26", "s27", "s28", "s29", "s30", "s31", "s33", "s34", "s35", "s39", "s41", "s42", "s43", "s44", "s45", "s46", "s47", "s48", "s49", "s50", "s51", "s52", "s53", "s54", "s55", "s56", "s57", "s58", "s59", "s60", "s61", "s62", "s63", "s64", "s65", "s66", "s67", "s68", "s69", "s70", "s71", "s72", "s73", "s74", "s75", "s76", "s77", "s78", "s79", "s80", "s81", "s82", "s83", "s84", "s85", "s86", "s87", "v0", "v1", "v2", "v3", "v4", "v5", "v6", "v7", "v8", "v9", "v10", "v11", "v12", "v13", "v14", "v15", "v16", "v17", "v18", "v19", "v20", "v21", "v22", "v23", "v24", "v25", "v26", "v27", "v28", "v29", "v30", "v31", "v32", "v33", "v34", "v35", "v36", "v37", "v38", "v39", "v40", "v41", "v42", "v43", "v44", "v45", "v46", "v47", "v48", "v49", "v50", "v51", "v52", "v53", "v54", "v55", "v56", "v57", "v58", "v59", "v60", "v61", "v62", "v63", "v64", "v65", "v66", "v67", "v68", "v69", "v70", "v71", "v72", "v73", "v74", "v75", "v76", "v77", "v78", "v79", "v80", "v81", "v82", "v83", "v84", "v85", "v86", "v87", "v88", "v89", "v90", "v91", "v92", "v93", "v94", "v95", "v96", "v97", "v98", "v99", "v100", "v101", "v102", "v103", "v104", "v105", "v106", "v107", "v108", "v109", "v110", "v111", "v112", "v113", "v114", "v115", "v116", "v117", "v118", "v119", "v120", "v121", "v122", "v123", "v124", "v125", "v126", "v127", "v128", "v129", "v130", "v131", "v132", "v133", "v134", "v135", "v136", "v137", "v138", "v139", "v140", "v141", "v142", "v143", "v144", "v145", "v146", "v147", "v148", "v149", "v150", "v151", "v152", "v153", "v154", "v155", "v156", "v157", "v158", "v159", "v160", "v161", "v162", "v163", "v164", "v165", "v166", "v167", "v168", "v169", "v170", "v171", "v172", "v173", "v174", "v175", "v176", "v177", "v178", "v179", "v180", "v181", "v182", "v183", "v184", "v185", "v186", "v187", "v188", "v189", "v190", "v191", "v192", "v193", "v194", "v195", "v196", "v197", "v198", "v199", "v200", "v201", "v202", "v203", "v204", "v205", "v206", "v207", "v208", "v209", "v210", "v211", "v212", "v213", "v214", "v215", "v216", "v217", "v218", "v219", "v220", "v221", "v222", "v223", "v224", "v225", "v226", "v227", "v228", "v229", "v230", "v231", "v232", "v233", "v234", "v235", "v236", "v237", "v238", "v239", "v241", "v242", "v243", "v244", "v245", "v246", "v247", "v248", "v249", "v250", "v251", "v252", "v253", "a0", "a1", "a2", "a3", "a4", "a5", "a6", "a7", "a8", "a9", "a10", "a11", "a12", "a13", "a14", "a15", "a16", "a17", "a18", "a19", "a20", "a21", "a22", "a23", "a24", "a25", "a26", "a27", "a28", "a29", "a30", "a31", "a32", "a33", "a34", "a35", "a36", "a37", "a38", "a39", "a40", "a41", "a42", "a43", "a44", "a45", "a46", "a47", "a48", "a49", "a50", "a51", "a52", "a53", "a54", "a55", "a56", "a57", "a58", "a59", "a60", "a61", "a62", "a63", "a64", "a65", "a66", "a67", "a68", "a69", "a70", "a71", "a72", "a73", "a74", "a75", "a76", "a77", "a78", "a79", "a80", "a81", "a82", "a83", "a84", "a85", "a86", "a87", "a88", "a89", "a90", "a91", "a92", "a93", "a94", "a95", "a96", "a97", "a98", "a99", "a100", "a101", "a102", "a103", "a104", "a105", "a106", "a107", "a108", "a109", "a110", "a111", "a112", "a113", "a114", "a115", "a116", "a117", "a118", "a119", "a120", "a121", "a122", "a123", "a124", "a125", "a126", "a127", "a128", "a129", "a130", "a131", "a132", "a133", "a134", "a135", "a136", "a137", "a138", "a139", "a140", "a141", "a142", "a143", "a144", "a145", "a146", "a147", "a148", "a149", "a150", "a151", "a152", "a153", "a154", "a155", "a156", "a157", "a158", "a159", "a160", "a161", "a162", "a163", "a164", "a165", "a166", "a167", "a168", "a169", "a170", "a171", "a172", "a173", "a174", "a175", "a176", "a177", "a178", "a179", "a180", "a181", "a182", "a183", "a184", "a185", "a186", "a187", "a188", "a189", "a190", "a191", "a192", "a193", "a194", "a195", "a196", "a197", "a198", "a199", "a200", "a201", "a202", "a203", "a204", "a205", "a206", "a207", "a208", "a209", "a210", "a211", "a212", "a213", "a214", "a215", "a216", "a217", "a218", "a219", "a220", "a221", "a222", "a223", "a224", "a225", "a226", "a227", "a228", "a229", "a230", "a231", "a232", "a233", "a234", "a235", "a236", "a237", "a238", "a239", "a240", "a241", "a242", "a243", "a244", "a245", "a246", "a247", "a248", "a249", "a250", "a251", "a252", "a253", "a254", "a255"

#define CV_KVPAD 256
#define CV_KVB ((CV_KVPAD + S_LEN) * 2 + 16)
#define CV_KR (S_LEN + 64 + 8)
#define CV_KCB (4 * CV_KR * 2)
#define CV_KC0 (12 * CV_KVB)
struct ConvF { u32x4 f0[4]; u32x2 f1[4]; unsigned Dv; };
__device__ __forceinline__ void conv_fload(ConvF& in, int d, int lane, u32x4 sF, u32x4 sD) {
    const unsigned o = (unsigned)d * (S_LEN * 2) + lane * 16;
    asm volatile("buffer_load_dwordx4 %0, %9, %11, 0 offen\n\tbuffer_load_dwordx4 %1, %9, %11, 0 offen offset:1024\n\t"
                 "buffer_load_dwordx4 %2, %9, %11, 0 offen offset:2048\n\tbuffer_load_dwordx4 %3, %9, %11, 0 offen offset:3072\n\t"
                 "buffer_load_dwordx2 %4, %10, %11, 0 offen\n\tbuffer_load_dwordx2 %5, %9, %11, 0 offen offset:1016\n\t"
                 "buffer_load_dwordx2 %6, %9, %11, 0 offen offset:2040\n\tbuffer_load_dwordx2 %7, %9, %11, 0 offen offset:3064\n\t"
                 "buffer_load_ushort %8, off, %12, %13"
                 : "=&v"(in.f0[0]), "=&v"(in.f0[1]), "=&v"(in.f0[2]), "=&v"(in.f0[3]), "=&v"(in.f1[0]), "=&v"(in.f1[1]), "=&v"(in.f1[2]),
                   "=&v"(in.f1[3]), "=&v"(in.Dv)
                 : "v"(o), "v"(lane > 0 ? o - 8 : o), "s"(sF), "s"(sD), "s"(RFL(d * 2))
                 : "memory");
}
template <int N>
__device__ __forceinline__ void conv_fwait(ConvF& in) {
    asm volatile("s_waitcnt vmcnt(%9)"
                 : "+v"(in.f0[0]), "+v"(in.f0[1]), "+v"(in.f0[2]), "+v"(in.f0[3]), "+v"(in.f1[0]), "+v"(in.f1[1]), "+v"(in.f1[2]),
                   "+v"(in.f1[3]), "+v"(in.Dv)
                 : "n"(N) : "memory");
}
__device__ __forceinline__ void conv_fbuild(const ConvF& in, u16* kc, int lane) {
    if (lane < 8) *(u32x4*)(kc + (lane >> 1) * CV_KR + (258 + (lane & 1)) * 8) = u32x4{0, 0, 0, 0};
#pragma unroll
    for (int c = 0; c < 4; ++c) {
        const int a = c * 64 + lane;
        unsigned W[6];
#pragma unroll
        for (int i = 0; i < 4; ++i) W[i] = __builtin_amdgcn_alignbit(in.f0[c][3 - i], in.f0[c][3 - i], 16);
        W[4] = a > 0 ? __builtin_amdgcn_alignbit(in.f1[c][1], in.f1[c][1], 16) : 0u;
        W[5] = a > 0 ? __builtin_amdgcn_alignbit(in.f1[c][0], in.f1[c][0], 16) : 0u;
        u16* dst = kc + (257 - a) * 8;
        *(u32x4*)(dst) = u32x4{W[0], W[1], W[2], W[3]};
        *(u32x4*)(dst + CV_KR) = u32x4{__builtin_amdgcn_alignbit(W[1], W[0], 16), __builtin_amdgcn_alignbit(W[2], W[1], 16),
                                       __builtin_amdgcn_alignbit(W[3], W[2], 16), __builtin_amdgcn_alignbit(W[4], W[3], 16)};
        *(u32x4*)(dst + 2 * CV_KR) = u32x4{W[1], W[2], W[3], W[4]};
        *(u32x4*)(dst + 3 * CV_KR) = u32x4{__builtin_amdgcn_alignbit(W[2], W[1], 16), __builtin_amdgcn_alignbit(W[3], W[2], 16),
                                           __builtin_amdgcn_alignbit(W[4], W[3], 16), __builtin_amdgcn_alignbit(W[5], W[4], 16)};
    }
}
struct Touch { u32x4 s1, s2, sc; unsigned v1, v2, vc; };
#define W2SPLIT 240
__device__ __forceinline__ unsigned touch_soff(int pw, int n) {
    const int i = n >= 100 ? W2SPLIT + pw + 12 * (n - 100) : pw + 20 * (n - 8), t = i >> 3;
    return RFL(n < 8 ? (unsigned)(T1S + 4 * ((n + 1) % 3)) * (32u * F_DIM * 2u) + (n >= 5 ? 128u : 0u)
                     : (i < (n >= 100 ? 288 : W2SPLIT) ? (unsigned)((t < 18 ? (t >> 1) * 128 : ((t - 18) >> 1) * 128 + 64) + 32 * (1 - (t & 1)) + (i & 7) * 4) * (S_LEN * 2u) : OOB));
}
#define TOUCH_OPS(n) "v"((n) < 8 ? T.v1 : T.v2), "s"((n) < 8 ? T.s1 : T.s2), "s"(touch_soff(pw, (n)))
__device__ __forceinline__ void touch(unsigned& dm, const Touch& T, int pw, int n) {
    asm volatile("s_nop 4\n\tbuffer_load_dword %0, %1, %2, %3 offen" : "+&v"(dm) : TOUCH_OPS(n) : "memory");
}
__device__ __forceinline__ void touch_code(unsigned& dm, const Touch& T) {
    asm volatile("s_nop 4\n\tbuffer_load_dword %0, %1, %2, 0 offen" : "+&v"(dm) : "v"(T.vc), "s"(T.sc) : "memory");
}
template <typename MID>
__device__ __forceinline__ void conv_round(MID mid, char* kvb, u16* kc, float Dv, int d0, int w, int lane, u32x4 sWS, unsigned& dm,
                                           const Touch& T, int pw, int n0) {
    const int r = lane & 15, g = lane >> 4;
    u16* kv = (u16*)(kvb + w * CV_KVB);
    f32x4 acc[8];
    {
        const int i00 = 2047 - r + 8 * g;
        const unsigned kb0 = (unsigned)(size_t)(kc + (i00 & 3) * CV_KR + (i00 & ~3) - 32 * 63);
        const unsigned ab0 = (unsigned)(size_t)(kv + CV_KVPAD - 16 + 16 * r + 8 * g - 32 * 7);
        f32x16 oc[2];
        touch_code(dm, T);
        asm volatile("s_waitcnt lgkmcnt(0)\n\ts_nop 4\n\t" CONV_ASM
                     : "=&{v[32:47]}"(oc[0]), "=&{v[48:63]}"(oc[1]), "+&v"(dm)
                     : "v"(kb0), "v"(ab0), TOUCH_OPS(n0), TOUCH_OPS(n0 + 1), TOUCH_OPS(n0 + 2), TOUCH_OPS(n0 + 3), TOUCH_OPS(n0 + 4),
                       TOUCH_OPS(n0 + 5), TOUCH_OPS(n0 + 6), TOUCH_OPS(n0 + 7)
                     : "memory", CONV_CLOB);
#pragma unroll
        for (int i = 0; i < 8; ++i) acc[i] = f32x4{oc[i >> 2][(i & 3) * 4], oc[i >> 2][(i & 3) * 4 + 1], oc[i >> 2][(i & 3) * 4 + 2], oc[i >> 2][(i & 3) * 4 + 3]};
    }
#pragma unroll
    for (int R = 0; R < 8; ++R) {
        const int s = 256 * R + 16 * r + 4 * g;
        const u32x2 kk = *(const u32x2*)(kv + CV_KVPAD + s);
        u32x2 o;
#pragma unroll
        for (int h = 0; h < 2; ++h) o[h] = pk2(rbf2(f32x2{acc[R][2 * h], acc[R][2 * h + 1]}) + rbf2(unpk(kk[h]) * Dv));
        *(u32x2*)(kv + CV_KVPAD + s) = o;
    }
    asm volatile("s_waitcnt vmcnt(%0)" ::"n"(NTOUCH) : "memory");
    BARRIER();
    mid();
    const int mb = d0 / 192, mh = (d0 % 192) / 48, mt = (d0 % 48) >> 4, gg = (d0 & 15) >> 2;
#pragma unroll
    for (int q = 0; q < 2; ++q) {
        const int it = q * 256 + w * 64 + lane;
        const int i2 = (it & 7) * 2, sb = it >> 3;
        const int s = 32 * sb + i2;
        unsigned cv[2][4];
#pragma unroll
        for (int jj = 0; jj < 2; ++jj)
#pragma unroll
            for (int c = 0; c < 4; ++c) cv[jj][c] = *(const unsigned*)((const u16*)(kvb + c * CV_KVB) + CV_KVPAD + s + 16 * jj);
        const u32x4 lo = {__builtin_amdgcn_perm(cv[0][1], cv[0][0], 0x05040100u), __builtin_amdgcn_perm(cv[0][3], cv[0][2], 0x05040100u),
                          __builtin_amdgcn_perm(cv[1][1], cv[1][0], 0x05040100u), __builtin_amdgcn_perm(cv[1][3], cv[1][2], 0x05040100u)};
        const u32x4 hi = {__builtin_amdgcn_perm(cv[0][1], cv[0][0], 0x07060302u), __builtin_amdgcn_perm(cv[0][3], cv[0][2], 0x07060302u),
                          __builtin_amdgcn_perm(cv[1][1], cv[1][0], 0x07060302u), __builtin_amdgcn_perm(cv[1][3], cv[1][2], 0x07060302u)};
        const unsigned o0 = WS_SG + (unsigned)(((mb * 64 + sb) * 4 + mh) * 3072 + mt * 1024 + (gg * 16 + i2) * 16);
        ST16_SC1(lo, o0, sWS);
        ST16_SC1(hi, o0 + 16u, sWS);
    }
}
#define T1PRE 2
template <int NR, int N0>
__device__ __forceinline__ void conv_role(char* smem, int d0, int w, int pw, const u16* ssmk, const u16* ssmD, u32x4 sT1,
                                          u32x4 sT2, u64 pc, u32x4 sWS, u32x2 tag, unsigned* dbg) {
    const int lane = lane_id();
    constexpr int NCH = 4 * NR;
    u16* kc = (u16*)(smem + CV_KC0 + w * CV_KCB);
    const u32x4 sF = make_srd(ssmk, D_DIM * S_LEN * 2), sD = make_srd(ssmD, D_DIM * 2);
    ConvF f[NR];
    conv_fload(f[0], d0 + w, lane, sF, sD);
    Touch T;
    T.s1 = sT1; T.s2 = sT2; T.sc = make_srd((const void*)(pc & ~127ull), CODE_BYTES); T.vc = (unsigned)(pw * 64 + lane) * 128u;
    { const int li = (pw % 20) * 64 + lane, row = li / 5; T.v1 = (unsigned)((row >> 7) * 1024 + (row & 127)) * (F_DIM * 2) + (li % 5) * 256; }
    T.v2 = (unsigned)((lane >> 4) * 4096 + (lane & 15) * 128);
    unsigned dm = 0;
    if (NR > 1) {
#pragma unroll
        for (int i = 0; i < T1PRE; ++i) touch(dm, T, pw, 2 + i);
    }
#pragma unroll
    for (int rr = 0; rr < NR; ++rr)
        if (lane < 32) *(u32x4*)(smem + (4 * rr + w) * CV_KVB + lane * 16) = u32x4{0, 0, 0, 0};
    unsigned go[NCH], lo[NCH];
#pragma unroll
    for (int i = 0; i < NCH; ++i) {
        const unsigned n = i * 64 + lane, sl = n / NCH, ch = n - sl * NCH;
        go[i] = WS_KV + ((w * 64 + sl) * D_DIM + d0 + ch) * 16u;
        lo[i] = ch * CV_KVB + CV_KVPAD * 2 + (w * 64 + sl) * 16;
        asm volatile("" : "+v"(go[i]), "+v"(lo[i]));
    }
    if (NR > 1) {
        conv_fwait<T1PRE>(f[0]);
        conv_fbuild(f[0], kc, lane);
    }
    CSTAMP(0);
    {
        const int c0 = d0 >> 6, c1 = (d0 + NCH - 1) >> 6;
        const int w0 = c0 == 8 ? 1 : (c0 & 3), w1 = c1 == 8 ? 1 : (c1 & 3);
        wait_flags(WS_KVF + (unsigned)(w0 * 256 + w * 64 + lane) * 8u, sWS, tag);
        if (w1 != w0) wait_flags(WS_KVF + (unsigned)(w1 * 256 + w * 64 + lane) * 8u, sWS, tag);
        CSTAMP(1);
        u32x4 t[NCH];
#pragma unroll
        for (int i = 0; i < NCH; ++i) asm volatile("buffer_load_dwordx4 %0, %1, %2, 0 offen sc1" : "=v"(t[i]) : "v"(go[i]), "s"(sWS) : "memory");
        if (NR == 1) {
            conv_fwait<63>(f[0]);
            conv_fbuild(f[0], kc, lane);
        }
        DRAIN();
#pragma unroll
        for (int i = 0; i < NCH; ++i) asm volatile("" : "+v"(t[i]));
#pragma unroll
        for (int i = 0; i < NCH; ++i) *(u32x4*)(smem + lo[i]) = t[i];
        if (NR > 1) conv_fload(f[1], d0 + 4 + w, lane, sF, sD);
        BARRIER();
    }
    CSTAMP(2);
    auto pub = [&](int dd) { if (w == 0) ST8_SC1(tag, WS_CF + (unsigned)dd * 2u, sWS); };
#pragma unroll
    for (int rr = 0; rr < NR; ++rr) {
        if (rr > 0) { conv_fwait<63>(f[rr]); conv_fbuild(f[rr], kc, lane); }
        if (rr > 0 && rr + 1 < NR) conv_fload(f[rr + 1], d0 + 4 * rr + 4 + w, lane, sF, sD);
        conv_round([&]() { if (rr > 0) pub(d0 + 4 * rr - 4); }, smem + 4 * rr * CV_KVB, kc, lo_f(f[rr].Dv), d0 + 4 * rr, w, lane, sWS, dm, T,
                   pw, N0 + NTOUCH * rr);
        CSTAMP(3 + rr);
    }
    DRAIN();
    BARRIER();
    pub(d0 + 4 * NR - 4);
    touch_code(dm, T);
    asm volatile("s_waitcnt vmcnt(0)" : "+v"(dm)::"memory");
}

#define P0_K 131072u
#define P0_V (P0_K + 13u * 1024u)
#define P0_H (P0_V + 9u * 1024u)
#define P0_HLD 1168u
#define LDS_BYTES 163840

struct Arg { const void* p; unsigned n, f; };
#define ARG(i) const void* p##i, unsigned n##i, unsigned f##i
__global__ __launch_bounds__(256, 1) void fused_kernel(ARG(0), ARG(1), ARG(2), ARG(3), ARG(4), ARG(5), ARG(6), ARG(7), ARG(8), ARG(9), ARG(10),
                                                       ARG(11)) {
    extern __shared__ __attribute__((aligned(16))) char smem[];
    HSTAMP(0);
    HSTAMP(1);
    const int b = blockIdx.x;
    unsigned tid = threadIdx.x;
    u64 ka = (u64)__builtin_amdgcn_kernarg_segment_ptr(), pc, tag64;
    int w;
#define MAIN_OUT
    asm volatile(MAIN_ASM
                 : "=&{s[36:37]}"(tag64), "=&{s38}"(w), "=&{s[2:3]}"(pc), "+{s[0:1]}"(ka), "+{v240}"(tid) MAIN_OUT
                 : "{s40}"(b)
                 : "memory", "scc", "vcc", MAIN_CLOB);
    w &= 3;
    const int xcd = b & 7, q = b >> 3;
    const int role = q < 24 ? 2 : (q < 27 ? 1 : 0);
    const __attribute__((address_space(4))) Arg* args = (const __attribute__((address_space(4))) Arg*)ka;
    char* ws = (char*)args[0].p;
    const u16 *w1 = (const u16*)args[1].p, *w2 = (const u16*)args[2].p, *ssmk = (const u16*)args[10].p, *ssmD = (const u16*)args[11].p;
    const u32x4 sWS = make_srd(ws, WS_END);
    unsigned* dbg = nullptr;
    if (role == 2) return;
    const u32x2 tag = {(unsigned)tag64, (unsigned)(tag64 >> 32)};
    const unsigned to1 = (unsigned)(xcd * 27 / 6) * 256, to2 = (unsigned)((xcd >> 1) * 1152) * (S_LEN * 2) + (xcd & 1) * 2048;
    const u32x4 sT1 = make_srd((const char*)w1 + to1, S_LEN * F_DIM * 2 - to1), sT2 = make_srd((const char*)w2 + to2, S_LEN * F_DIM * 2 - to2);
    BARRIER();
    if (role == 0) conv_role<3, 2 + T1PRE>(smem, ((q - 27) * 8 + xcd) * 12, w, (q - 27) * 4 + w, ssmk, ssmD, sT1, sT2, pc, sWS, tag, dbg);
    else conv_role<1, 100>(smem, 480 + ((q - 24) * 8 + xcd) * 4, w, (q - 24) * 4 + w, ssmk, ssmD, sT1, sT2, pc, sWS, tag, dbg);
}

extern "C" void kernel_launch(void* const* d_in, const int* in_sizes, int n_in, void* d_out, int out_size,
                              void* d_ws, size_t ws_size, hipStream_t stream) {
    (void)hipFuncSetAttribute((const void*)fused_kernel, hipFuncAttributeMaxDynamicSharedMemorySize, LDS_BYTES);
    const unsigned F = 0x20000u, NX = S_LEN * ROWB, NW = S_LEN * F_DIM * 2;
    fused_kernel<<<256, 256, LDS_BYTES, stream>>>(d_ws, WS_END, F, d_in[7], NW, F, d_in[9], NW, F, d_in[0], NX, F, d_out, NX, F, d_in[8], F_DIM * 2, F,
                                                  d_in[10], S_LEN * 2, F, d_in[1], NX, F, d_in[2], NX, F, d_in[3], D_DIM * 8, F, d_in[4], NX, F,
                                                  d_in[5], D_DIM * 2, F);
}
```

```cpp
#include <hip/hip_runtime.h>

typedef unsigned long long u64;

#define S_LEN 2048
#define D_DIM 576
#define F_DIM 4608
#define LDS_BYTES 163840

#define WS_END 22347776u
#define MAIN_ASM \
    "s_load_dwordx16 s[8:23], s[0:1], 0\n\ts_load_dwordx8 s[76:83], s[0:1], 112\n\ts_load_dwordx4 s[84:87], s[0:1], 144\n\ts_load_dwordx8 s[24:31], s[0:1], 64\n\ts_load_dwordx4 s[32:35], s[0:1], 96\n\ts_getpc_b64 s[2:3]\n\t99:\n\tv_lshrrev_b32 v245, 6, v240\n\t" \
    "v_and_b32 v240, 63, v240\n\tv_readfirstlane_b32 s38, v245\n\ts_and_b32 s2, s2, 0xffffff80\n\ts_mov_b32 s4, s2\n\ts_mov_b32 s5, s3\n\ts_mov_b32 s6, 98f-99b\n\ts_mov_b32 s7, 0x20000\n\tv_lshl_add_u32 v16, v245, 16, v240\n\t" \
    "v_lshlrev_b32 v16, 7, v16\n\ts_lshr_b32 s65, s40, 3\n\ts_lshl_b32 s73, s65, 13\n\tbuffer_load_dword v57, v16, s[4:7], s73 offen\n\tv_lshlrev_b32 v8, 4, v240\n\tv_and_b32 v244, 0xf0, v8\n\tv_lshrrev_b32 v245, 4, v240\n\tv_lshrrev_b32 v20, 5, v240\n\t" \
    "v_mul_u32_u24 v20, 1152, v20\n\tv_and_b32 v21, 31, v240\n\tv_lshl_add_u32 v22, v21, 2, v20\n\tv_lshl_add_u32 v21, v21, 4, v20\n\ts_lshl_b32 s68, s40, 2\n\ts_add_u32 s68, s68, s38\n\ts_mul_i32 s68, s68, 2304\n\ts_lshr_b32 s57, s38, 1\n\t" \
    "s_and_b32 s58, s38, 1\n\ts_cmp_lt_u32 s65, 27\n\ts_cselect_b32 s39, 1, 0\n\ts_cmp_lt_u32 s65, 24\n\ts_cselect_b32 s39, 2, s39\n\ts_sub_u32 s66, s65, 27\n\ts_mul_i32 s66, s66, 6\n\ts_cmp_lt_u32 s65, 27\n\t" \
    "s_cselect_b32 s65, s65, s66\n\ts_and_b32 s66, s40, 7\n\ts_mul_i32 s66, s66, 27\n\ts_add_u32 s65, s65, s66\n\ts_mul_i32 s42, s65, 171\n\ts_lshr_b32 s42, s42, 10\n\ts_mul_i32 s66, s42, 6\n\ts_sub_u32 s41, s65, s66\n\t" \
    "s_mul_i32 s65, s42, 57\n\ts_lshr_b32 s65, s65, 9\n\ts_mul_i32 s65, s65, 9\n\ts_sub_u32 s65, s42, s65\n\ts_lshl_b32 s66, s41, 2\n\ts_lshr_b32 s66, 5444640, s66\n\ts_and_b32 s66, s66, 15\n\ts_cmp_eq_u32 s65, 4\n\t" \
    "s_cselect_b32 s43, s66, s41\n\ts_mul_i32 s43, s43, 516\n\ts_lshr_b32 s43, s43, 8\n\ts_and_b32 s65, s40, 7\n\ts_mul_i32 s52, s65, 0\n\ts_mul_i32 s65, s65, 0\n\ts_lshr_b32 s65, s65, 1\n\ts_add_u32 s43, s43, s65\n\t" \
    "s_cmp_ge_u32 s43, 12\n\ts_cselect_b32 s66, 12, 0\n\ts_sub_u32 s43, s43, s66\n\ts_cmp_eq_u32 s39, 0\n\ts_cselect_b32 s43, 0, s43\n\ts_cselect_b32 s67, 3538944, 0\n\ts_sub_u32 s70, s43, 12\n\ts_mul_i32 s65, s57, 9437184\n\t" \
    "s_add_u32 s67, s67, s65\n\ts_lshl_b32 s66, s42, 8\n\ts_add_u32 s67, s67, s66\n\tv_lshrrev_b32 v16, 3, v240\n\ts_lshl_b32 s73, s58, 4\n\tv_add_u32 v16, s73, v16\n\tv_mul_u32_u24 v16, 9216, v16\n\tv_and_b32 v17, 0x70, v8\n\t" \
    "v_and_b32 v18, 16, v240\n\tv_lshlrev_b32 v18, 1, v18\n\tv_xor_b32 v17, v17, v18\n\tv_add_u32 v10, v16, v17\n\tv_xor_b32 v11, 64, v10\n\tv_add_u32 v11, 73728, v11\n\ts_lshl_b32 s72, s57, 16\n\ts_cmp_eq_u32 s39, 0\n\t" \
    "s_cselect_b32 s72, 0, s72\n\ts_lshl_b32 s73, s58, 11\n\ts_add_u32 s72, s72, s73\n\ts_lshl_b32 s54, s38, 10\n\tv_add_u32 v9, 4096, v8\n\tv_lshlrev_b32 v239, 3, v240\n\ts_mul_i32 s73, s40, 9216\n\ts_add_u32 s73, s73, s54\n\t" \
    "v_add_u32 v23, s73, v8\n\tv_subrev_u32 v19, 3456, v23\n\tv_and_b32 v88, 31, v240\n\tv_lshrrev_b32 v89, 5, v240\n\ts_lshl_b32 s73, s38, 1\n\tv_add_u32 v89, s73, v89\n\tv_mul_u32_u24 v90, 1168, v89\n\tv_add_u32 v90, 153600, v90\n\t" \
    "v_lshl_add_u32 v91, v88, 2, v90\n\tv_lshl_add_u32 v90, v88, 4, v90\n\tv_cmp_eq_u32 vcc, 0, v88\n\tv_lshlrev_b32 v89, 3, v89\n\tv_mov_b32 v92, 0x7ff00000\n\tv_cndmask_b32 v89, v92, v89, vcc\n\tv_bfe_u32 v88, v240, 2, 2\n\tv_and_b32 v92, 3, v240\n\t" \
    "v_lshlrev_b32 v92, 3, v92\n\tv_lshl_add_u32 v92, v245, 5, v92\n\ts_lshl_b32 s73, s38, 7\n\tv_add_u32 v93, s73, v92\n\tv_mul_u32_u24 v94, 1168, v88\n\tv_add_u32 v94, 153600, v94\n\tv_add_u32 v95, v94, v92\n\tv_add_u32 v94, v94, v93\n\t" \
    "v_mul_u32_u24 v88, 1152, v88\n\tv_add_u32 v88, 131072, v88\n\tv_add_u32 v92, v88, v92\n\tv_add_u32 v93, v88, v93\n\tv_lshl_add_u32 v88, v245, 10, v244\n\ts_lshr_b32 s61, s40, 2\n\ts_mul_i32 s61, s61, 36864\n\ts_and_b32 s65, s40, 3\n\t" \
    "s_lshl_b32 s65, s65, 8\n\ts_add_u32 s61, s61, s65\n\ts_add_u32 s61, s61, 131072\n\ts_mul_i32 s62, s40, 9216\n\ts_add_u32 s62, s62, 2490368\n\ts_lshl_b32 s63, s40, 3\n\ts_lshl_b32 s48, s38, 11\n\ts_add_u32 s48, s48, s63\n\t" \
    "s_add_u32 s48, s48, 17408\n\ts_add_u32 s63, s63, 1024\n\ts_cmp_eq_u32 s38, 0\n\ts_cselect_b32 s63, s63, 2146435072\n\ts_lshl_b32 s64, s40, 6\n\ts_add_u32 s64, s64, 58368\n\ts_waitcnt lgkmcnt(0)\n\tbuffer_load_dwordx4 v[48:51], v21, s[20:23], s68 offen\n\t" \
    "buffer_load_dwordx4 v[52:55], v21, s[20:23], s68 offen offset:512\n\tbuffer_load_dword v56, v22, s[20:23], s68 offen offset:1024\n\tbuffer_load_dwordx2 v[58:59], off, s[8:11], 0 sc1\n\ts_add_u32 s12, s12, s67\n\ts_addc_u32 s13, s13, 0\n\ts_sub_u32 s14, s14, s67\n\ts_cmp_eq_u32 s39, 0\n\ts_cselect_b32 s14, 0, s14\n\t" \
    "s_add_u32 m0, s54, 131072\n\ts_nop 0\n\tbuffer_load_dwordx4 v19, s[76:79], 0 offen lds\n\ts_and_b32 s4, s40, 7\n\ts_lshl_b32 s47, s4, 11\n\ts_lshl_b32 s65, s57, 10\n\ts_add_u32 s47, s47, s65\n\tv_add_u32 v19, 4096, v19\n\t" \
    "s_add_u32 m0, s54, 135168\n\ts_nop 0\n\tbuffer_load_dwordx4 v19, s[76:79], 0 offen lds\n\ts_add_u32 s47, s47, 1024\n\ts_mul_i32 s44, s57, 1179648\n\ts_mul_i32 s65, s41, 6144\n\ts_add_u32 s44, s44, s65\n\tv_add_u32 v19, 4096, v19\n\t" \
    "s_add_u32 m0, s54, 139264\n\ts_nop 0\n\tbuffer_load_dwordx4 v19, s[76:79], 0 offen lds\n\ts_mul_i32 s65, s58, 3072\n\ts_add_u32 s44, s44, s65\n\ts_add_u32 s44, s44, 131072\n\ts_lshr_b32 s5, s4, 1\n\tv_add_u32 v19, 4096, v19\n\t" \
    "s_add_u32 m0, s54, 144384\n\ts_nop 0\n\tbuffer_load_dwordx4 v23, s[80:83], 0 offen lds\n\tv_bfe_u32 v16, v240, 2, 2\n\tv_and_b32 v17, 3, v240\n\tv_lshlrev_b32 v17, 3, v17\n\tv_lshl_add_u32 v16, v16, 7, v17\n\tv_add_u32 v23, 4096, v23\n\t" \
    "s_add_u32 m0, s54, 148480\n\ts_nop 0\n\tbuffer_load_dwordx4 v23, s[80:83], 0 offen lds\n\tv_bfe_u32 v17, v240, 3, 2\n\tv_lshl_add_u32 v16, v17, 5, v16\n\ts_mul_i32 s65, s57, 65600\n\tv_xor_b32 v17, s65, v16\n\tv_add_u32 v23, 4096, v23\n\t" \
    "v_subrev_u32 v19, s54, v19\n\tv_subrev_u32 v23, s54, v23\n\ts_cmp_eq_u32 s58, 1\n\ts_cselect_b64 vcc, -1, 0\n\ts_cselect_b64 s[76:77], s[80:81], s[76:77]\n\ts_mov_b32 m0, 143360\n\ts_cselect_b32 m0, 152576, m0\n\tv_cndmask_b32 v19, v19, v23, vcc\n\t" \
    "buffer_load_dwordx4 v19, s[76:79], 0 offen lds\n\tv_lshl_add_u32 v0, v245, 10, v17\n\ts_and_b32 s65, s5, 1\n\ts_xor_b32 s65, s65, 1\n\ts_lshl_b32 s65, s65, 6\n\ts_lshr_b32 s71, s54, 1\n\tbuffer_load_dwordx2 v[60:61], v239, s[84:87], s71 offen\n\tv_xor_b32 v17, s65, v16\n\t" \
    "v_lshl_add_u32 v4, v245, 9, v17\n\tv_xor_b32 v1, 32, v0\n\tv_xor_b32 v5, 32, v4\n\tbuffer_load_dwordx2 v[62:63], v239, s[84:87], s71 offen offset:2048\n\tv_xor_b32 v2, 64, v0\n\tv_xor_b32 v6, 64, v4\n\tv_xor_b32 v3, 96, v0\n\tv_xor_b32 v7, 96, v4\n\t" \
    "s_mov_b32 s71, 4096\n\tbuffer_load_dwordx2 v[64:65], v239, s[84:87], s71 offen\n\tv_add_u32 v224, 0x10000, v4\n\tv_add_u32 v225, 0x10000, v5\n\tv_add_u32 v226, 0x10000, v6\n\tv_add_u32 v227, 0x10000, v7\n\ts_cmp_ge_u32 s43, 12\n\ts_cselect_b32 s69, s70, s43\n\t" \
    "s_mul_i32 s69, s69, 294912\n\ts_add_u32 s69, s69, 0\n\ts_add_u32 m0, s72, 0\n\ts_nop 0\n\tbuffer_load_dwordx4 v10, s[12:15], s69 offen lds\n\ts_mul_i32 s65, s38, 6144\n\ts_add_u32 s65, s65, 131072\n\tv_add_u32 v237, s65, v8\n\t" \
    "s_xor_b32 s65, s38, 2\n\ts_add_u32 m0, s72, 1024\n\ts_nop 0\n\tbuffer_load_dwordx4 v11, s[12:15], s69 offen lds\n\ts_mul_i32 s65, s65, 6144\n\ts_add_u32 s65, s65, 131072\n\tv_add_u32 v238, s65, v8\n\tv_mov_b32 v246, 0xbdd2d3e8\n\t" \
    "v_mov_b32 v247, 0xc0135761\n\ts_lshr_b32 s65, s40, 3\n\ts_mul_i32 s66, s65, 43\n\ts_lshr_b32 s66, s66, 7\n\ts_mul_i32 s6, s66, 3\n\ts_sub_u32 s6, s65, s6\n\ts_and_b32 s66, s66, 7\n\ts_and_b32 s7, s4, 1\n\t" \
    "s_lshl_b32 s7, s7, 3\n\ts_add_u32 s7, s7, s66\n\ts_mul_i32 s65, s5, 4718592\n\ts_lshl_b32 s67, s7, 8\n\ts_add_u32 s65, s65, s67\n\ts_add_u32 s16, s16, s65\n\ts_addc_u32 s17, s17, 0\n\ts_sub_u32 s18, s18, s65\n\t" \
    "s_cmp_eq_u32 s39, 2\n\ts_cselect_b32 s18, s18, 0\n\tv_lshrrev_b32 v16, 3, v240\n\ts_lshl_b32 s65, s38, 3\n\tv_add_u32 v16, s65, v16\n\tv_and_b32 v17, 0x70, v8\n\tv_lshlrev_b32 v18, 5, v245\n\tv_xor_b32 v17, v17, v18\n\t" \
    "v_lshl_add_u32 v16, v16, 12, v17\n\ts_lshr_b32 s65, s5, 1\n\ts_lshl_b32 s65, s65, 7\n\tv_add_u32 v13, s65, v16\n\tv_xor_b32 v12, 128, v13\n\ts_lshl_b32 s65, s7, 7\n\ts_lshl_b32 s66, s5, 5\n\ts_add_u32 s65, s65, s66\n\t" \
    "s_lshl_b32 s59, s65, 3\n\ts_add_u32 s59, s59, 58368\n\ts_lshl_b32 s66, s65, 1\n\ts_add_u32 s32, s32, s66\n\ts_addc_u32 s33, s33, 0\n\ts_sub_u32 s34, s34, s66\n\ts_lshl_b32 s66, s6, 2\n\ts_add_u32 s66, s66, s38\n\t" \
    "s_mul_i32 s67, s66, 96\n\ts_mul_i32 s65, s65, 1152\n\ts_add_u32 s65, s65, s67\n\ts_add_u32 s20, s20, s65\n\ts_addc_u32 s21, s21, 0\n\ts_sub_u32 s22, s22, s65\n\ts_add_u32 s24, s24, s65\n\ts_addc_u32 s25, s25, 0\n\t" \
    "s_sub_u32 s26, s26, s65\n\ts_lshl_b32 s51, s67, 2\n\ts_add_u32 s51, s51, 50176\n\ts_lshl_b32 s66, s66, 2\n\ts_add_u32 s66, s66, s5\n\ts_mul_i32 s46, s66, 110592\n\ts_add_u32 s46, s46, 16646144\n\ts_cmp_eq_u32 s39, 2\n\t" \
    "s_cselect_b32 s46, s46, 2146435072\n\ts_lshl_b32 s50, s66, 13\n\ts_add_u32 s50, s50, 21954560\n\tv_and_b32 v16, 31, v240\n\tv_mul_u32_u24 v243, 1152, v16\n\tv_lshrrev_b32 v16, 5, v240\n\tv_lshl_add_u32 v243, v16, 4, v243\n\tv_lshrrev_b32 v242, 1, v244\n\t" \
    "s_mul_i32 s65, s42, 57\n\ts_lshr_b32 s65, s65, 9\n\ts_mul_i32 s66, s65, 9\n\ts_sub_u32 s66, s42, s66\n\ts_lshl_b32 s66, s66, 1\n\ts_add_u32 s66, s66, s57\n\ts_lshl_b32 s67, s41, 1\n\ts_add_u32 s67, s67, s58\n\t" \
    "s_lshl_b32 s67, s67, 2\n\ts_add_u32 s65, s65, s67\n\ts_lshl_b32 s49, s65, 13\n\ts_lshl_b32 s67, s66, 7\n\ts_add_u32 s49, s49, s67\n\ts_add_u32 s49, s49, 21954560\n\ts_mul_i32 s45, s65, 36\n\ts_add_u32 s45, s45, s66\n\t" \
    "s_mul_i32 s45, s45, 3072\n\ts_add_u32 s45, s45, 16646144\n\tv_lshrrev_b32 v14, 1, v240\n\tv_and_b32 v15, 1, v240\n\tv_lshlrev_b32 v14, 12, v14\n\tv_lshl_add_u32 v14, v15, 7, v14\n\tv_min_u32 v241, 17, v240\n\tv_lshlrev_b32 v241, 7, v241\n\t" \
    "s_lshl_b32 s65, s6, 4\n\ts_add_u32 s65, s65, s7\n\ts_lshl_b32 s65, s65, 2\n\ts_add_u32 s65, s65, s5\n\ts_lshl_b32 s65, s65, 2\n\ts_add_u32 s53, s65, s38\n\ts_mul_i32 s55, s53, 12288\n\ts_add_u32 s55, s55, 7208960\n\t" \
    "s_cmp_eq_u32 s39, 2\n\ts_cselect_b32 s55, s55, 2146435072\n\ts_mul_i32 s56, s53, 3072\n\ts_add_u32 s56, s56, 4849664\n\ts_or_b32 s65, s40, s38\n\ts_cselect_b32 s60, 2146435072, 0\n\ts_waitcnt vmcnt(12)\n\ts_load_dwordx8 s[76:83], s[0:1], 160\n\t" \
    "v_lshlrev_b32 v24, 16, v48\n\tv_and_b32 v25, 0xffff0000, v48\n\tv_lshlrev_b32 v26, 16, v49\n\tv_and_b32 v27, 0xffff0000, v49\n\tv_lshlrev_b32 v28, 16, v50\n\tv_and_b32 v29, 0xffff0000, v50\n\tv_lshlrev_b32 v30, 16, v51\n\tv_and_b32 v31, 0xffff0000, v51\n\t" \
    "v_lshlrev_b32 v32, 16, v52\n\tv_and_b32 v33, 0xffff0000, v52\n\tv_lshlrev_b32 v34, 16, v53\n\tv_and_b32 v35, 0xffff0000, v53\n\tv_lshlrev_b32 v36, 16, v54\n\tv_and_b32 v37, 0xffff0000, v54\n\tv_lshlrev_b32 v38, 16, v55\n\tv_and_b32 v39, 0xffff0000, v55\n\t" \
    "v_lshlrev_b32 v40, 16, v56\n\tv_and_b32 v41, 0xffff0000, v56\n\tv_pk_add_f32 v[70:71], v[24:25], v[26:27]\n\tv_pk_add_f32 v[72:73], v[28:29], v[30:31]\n\tv_pk_add_f32 v[74:75], v[32:33], v[34:35]\n\tv_pk_add_f32 v[76:77], v[36:37], v[38:39]\n\tv_pk_add_f32 v[70:71], v[70:71], v[72:73]\n\tv_pk_add_f32 v[74:75], v[74:75], v[76:77]\n\t" \
    "v_pk_add_f32 v[70:71], v[70:71], v[40:41]\n\tv_pk_add_f32 v[70:71], v[70:71], v[74:75]\n\tv_add_f32 v66, v70, v71\n\ts_nop 1\n\tv_add_f32_dpp v66, v66, v66 row_ror:8 row_mask:0xf bank_mask:0xf\n\ts_nop 1\n\tv_add_f32_dpp v66, v66, v66 row_ror:4 row_mask:0xf bank_mask:0xf\n\ts_nop 1\n\t" \
    "v_add_f32_dpp v66, v66, v66 row_ror:2 row_mask:0xf bank_mask:0xf\n\ts_nop 1\n\tv_add_f32_dpp v66, v66, v66 row_ror:1 row_mask:0xf bank_mask:0xf\n\tv_mov_b32 v67, v66\n\ts_nop 1\n\tv_permlane16_swap_b32 v66, v67\n\tv_add_f32 v66, v66, v67\n\tv_mul_f32 v66, 0x3ae38e39, v66\n\t" \
    "v_pk_add_f32 v[70:71], v[24:25], v[66:67] op_sel_hi:[1,0] neg_lo:[0,1] neg_hi:[0,1]\n\tv_pk_add_f32 v[72:73], v[26:27], v[66:67] op_sel_hi:[1,0] neg_lo:[0,1] neg_hi:[0,1]\n\tv_pk_add_f32 v[74:75], v[28:29], v[66:67] op_sel_hi:[1,0] neg_lo:[0,1] neg_hi:[0,1]\n\tv_pk_add_f32 v[76:77], v[30:31], v[66:67] op_sel_hi:[1,0] neg_lo:[0,1] neg_hi:[0,1]\n\tv_pk_add_f32 v[78:79], v[32:33], v[66:67] op_sel_hi:[1,0] neg_lo:[0,1] neg_hi:[0,1]\n\tv_pk_add_f32 v[80:81], v[34:35], v[66:67] op_sel_hi:[1,0] neg_lo:[0,1] neg_hi:[0,1]\n\tv_pk_add_f32 v[82:83], v[36:37], v[66:67] op_sel_hi:[1,0] neg_lo:[0,1] neg_hi:[0,1]\n\tv_pk_add_f32 v[84:85], v[38:39], v[66:67] op_sel_hi:[1,0] neg_lo:[0,1] neg_hi:[0,1]\n\t" \
    "v_pk_add_f32 v[86:87], v[40:41], v[66:67] op_sel_hi:[1,0] neg_lo:[0,1] neg_hi:[0,1]\n\tv_pk_mul_f32 v[44:45], v[70:71], v[70:71]\n\tv_pk_mul_f32 v[46:47], v[72:73], v[72:73]\n\tv_pk_fma_f32 v[44:45], v[74:75], v[74:75], v[44:45]\n\tv_pk_fma_f32 v[46:47], v[76:77], v[76:77], v[46:47]\n\tv_pk_fma_f32 v[44:45], v[78:79], v[78:79], v[44:45]\n\tv_pk_fma_f32 v[46:47], v[80:81], v[80:81], v[46:47]\n\tv_pk_fma_f32 v[44:45], v[82:83], v[82:83], v[44:45]\n\t" \
    "v_pk_fma_f32 v[46:47], v[84:85], v[84:85], v[46:47]\n\tv_pk_fma_f32 v[44:45], v[86:87], v[86:87], v[44:45]\n\tv_pk_add_f32 v[44:45], v[44:45], v[46:47]\n\tv_add_f32 v68, v44, v45\n\ts_nop 1\n\tv_add_f32_dpp v68, v68, v68 row_ror:8 row_mask:0xf bank_mask:0xf\n\ts_nop 1\n\tv_add_f32_dpp v68, v68, v68 row_ror:4 row_mask:0xf bank_mask:0xf\n\t" \
    "s_nop 1\n\tv_add_f32_dpp v68, v68, v68 row_ror:2 row_mask:0xf bank_mask:0xf\n\ts_nop 1\n\tv_add_f32_dpp v68, v68, v68 row_ror:1 row_mask:0xf bank_mask:0xf\n\tv_mov_b32 v69, v68\n\ts_nop 1\n\tv_permlane16_swap_b32 v68, v69\n\tv_add_f32 v68, v68, v69\n\t" \
    "v_mul_f32 v68, 0x3ae38e39, v68\n\tv_cvt_pk_bf16_f32 v68, v68, v68\n\tv_lshlrev_b32 v68, 16, v68\n\tv_add_f32 v68, 0x3727c5ac, v68\n\tv_cvt_pk_bf16_f32 v68, v68, v68\n\tv_lshlrev_b32 v68, 16, v68\n\tv_sqrt_f32 v68, v68\n\tv_cvt_pk_bf16_f32 v66, v66, v66\n\t" \
    "v_lshlrev_b32 v66, 16, v66\n\tv_cvt_pk_bf16_f32 v68, v68, v68\n\tv_lshlrev_b32 v68, 16, v68\n\tv_rcp_f32 v68, v68\n\ts_nop 0\n\tv_mul_f32 v69, v66, v68\n\tv_xor_b32 v69, 0x80000000, v69\n\tv_pk_fma_f32 v[24:25], v[24:25], v[68:69], v[68:69] op_sel:[0,0,1] op_sel_hi:[1,0,1]\n\t" \
    "v_pk_fma_f32 v[26:27], v[26:27], v[68:69], v[68:69] op_sel:[0,0,1] op_sel_hi:[1,0,1]\n\tv_pk_fma_f32 v[28:29], v[28:29], v[68:69], v[68:69] op_sel:[0,0,1] op_sel_hi:[1,0,1]\n\tv_pk_fma_f32 v[30:31], v[30:31], v[68:69], v[68:69] op_sel:[0,0,1] op_sel_hi:[1,0,1]\n\tv_pk_fma_f32 v[32:33], v[32:33], v[68:69], v[68:69] op_sel:[0,0,1] op_sel_hi:[1,0,1]\n\tv_pk_fma_f32 v[34:35], v[34:35], v[68:69], v[68:69] op_sel:[0,0,1] op_sel_hi:[1,0,1]\n\tv_pk_fma_f32 v[36:37], v[36:37], v[68:69], v[68:69] op_sel:[0,0,1] op_sel_hi:[1,0,1]\n\tv_pk_fma_f32 v[38:39], v[38:39], v[68:69], v[68:69] op_sel:[0,0,1] op_sel_hi:[1,0,1]\n\tv_pk_fma_f32 v[40:41], v[40:41], v[68:69], v[68:69] op_sel:[0,0,1] op_sel_hi:[1,0,1]\n\t" \
    "v_cvt_pk_bf16_f32 v48, v24, v25\n\tv_cvt_pk_bf16_f32 v49, v26, v27\n\tv_cvt_pk_bf16_f32 v50, v28, v29\n\tv_cvt_pk_bf16_f32 v51, v30, v31\n\tv_cvt_pk_bf16_f32 v52, v32, v33\n\tv_cvt_pk_bf16_f32 v53, v34, v35\n\tv_cvt_pk_bf16_f32 v54, v36, v37\n\tv_cvt_pk_bf16_f32 v55, v38, v39\n\t" \
    "v_cvt_pk_bf16_f32 v56, v40, v41\n\tv_lshlrev_b32 v24, 16, v48\n\tv_and_b32 v25, 0xffff0000, v48\n\tv_lshlrev_b32 v26, 16, v49\n\tv_and_b32 v27, 0xffff0000, v49\n\tv_lshlrev_b32 v28, 16, v50\n\tv_and_b32 v29, 0xffff0000, v50\n\tv_lshlrev_b32 v30, 16, v51\n\t" \
    "v_and_b32 v31, 0xffff0000, v51\n\tv_lshlrev_b32 v32, 16, v52\n\tv_and_b32 v33, 0xffff0000, v52\n\tv_lshlrev_b32 v34, 16, v53\n\tv_and_b32 v35, 0xffff0000, v53\n\tv_lshlrev_b32 v36, 16, v54\n\tv_and_b32 v37, 0xffff0000, v54\n\tv_lshlrev_b32 v38, 16, v55\n\t" \
    "v_and_b32 v39, 0xffff0000, v55\n\tv_lshlrev_b32 v40, 16, v56\n\tv_and_b32 v41, 0xffff0000, v56\n\tv_pk_add_f32 v[70:71], v[24:25], v[26:27]\n\tv_pk_add_f32 v[72:73], v[28:29], v[30:31]\n\tv_pk_add_f32 v[74:75], v[32:33], v[34:35]\n\tv_pk_add_f32 v[76:77], v[36:37], v[38:39]\n\tv_pk_mul_f32 v[44:45], v[24:25], v[24:25]\n\t" \
    "v_pk_mul_f32 v[46:47], v[26:27], v[26:27]\n\tv_pk_add_f32 v[70:71], v[70:71], v[72:73]\n\tv_pk_add_f32 v[74:75], v[74:75], v[76:77]\n\tv_pk_fma_f32 v[44:45], v[28:29], v[28:29], v[44:45]\n\tv_pk_fma_f32 v[46:47], v[30:31], v[30:31], v[46:47]\n\tv_pk_add_f32 v[70:71], v[70:71], v[40:41]\n\tv_pk_fma_f32 v[44:45], v[32:33], v[32:33], v[44:45]\n\tv_pk_fma_f32 v[46:47], v[34:35], v[34:35], v[46:47]\n\t" \
    "v_pk_add_f32 v[70:71], v[70:71], v[74:75]\n\tv_pk_fma_f32 v[44:45], v[36:37], v[36:37], v[44:45]\n\tv_pk_fma_f32 v[46:47], v[38:39], v[38:39], v[46:47]\n\tv_pk_fma_f32 v[44:45], v[40:41], v[40:41], v[44:45]\n\tv_pk_add_f32 v[44:45], v[44:45], v[46:47]\n\tv_add_f32 v66, v70, v71\n\tv_add_f32 v42, v44, v45\n\ts_nop 0\n\t" \
    "v_add_f32_dpp v66, v66, v66 row_ror:8 row_mask:0xf bank_mask:0xf\n\tv_add_f32_dpp v42, v42, v42 row_ror:8 row_mask:0xf bank_mask:0xf\n\ts_nop 0\n\tv_add_f32_dpp v66, v66, v66 row_ror:4 row_mask:0xf bank_mask:0xf\n\tv_add_f32_dpp v42, v42, v42 row_ror:4 row_mask:0xf bank_mask:0xf\n\ts_nop 0\n\tv_add_f32_dpp v66, v66, v66 row_ror:2 row_mask:0xf bank_mask:0xf\n\tv_add_f32_dpp v42, v42, v42 row_ror:2 row_mask:0xf bank_mask:0xf\n\t" \
    "s_nop 0\n\tv_add_f32_dpp v66, v66, v66 row_ror:1 row_mask:0xf bank_mask:0xf\n\tv_add_f32_dpp v42, v42, v42 row_ror:1 row_mask:0xf bank_mask:0xf\n\tv_mov_b32 v67, v66\n\tv_mov_b32 v43, v42\n\ts_nop 0\n\tv_permlane16_swap_b32 v66, v67\n\tv_permlane16_swap_b32 v42, v43\n\t" \
    "v_add_f32 v66, v66, v67\n\tv_add_f32 v42, v42, v43\n\tv_mul_f32 v66, 0x3ae38e39, v66\n\tv_mul_f32 v42, 0x3ae38e39, v42\n\tv_fma_f32 v42, -v66, v66, v42\n\tv_max_f32 v42, 0, v42\n\tv_cvt_pk_bf16_f32 v42, v42, v42\n\tv_lshlrev_b32 v42, 16, v42\n\t" \
    "v_add_f32 v42, 0x3727c5ac, v42\n\tv_cvt_pk_bf16_f32 v42, v42, v42\n\tv_lshlrev_b32 v42, 16, v42\n\tv_sqrt_f32 v42, v42\n\tv_cvt_pk_bf16_f32 v66, v66, v66\n\tv_lshlrev_b32 v66, 16, v66\n\tv_cvt_pk_bf16_f32 v42, v42, v42\n\tv_lshlrev_b32 v42, 16, v42\n\t" \
    "v_rcp_f32 v42, v42\n\ts_nop 0\n\tv_mul_f32 v43, v66, v42\n\tv_xor_b32 v43, 0x80000000, v43\n\tv_pk_fma_f32 v[24:25], v[24:25], v[42:43], v[42:43] op_sel:[0,0,1] op_sel_hi:[1,0,1]\n\tv_pk_fma_f32 v[26:27], v[26:27], v[42:43], v[42:43] op_sel:[0,0,1] op_sel_hi:[1,0,1]\n\tv_pk_fma_f32 v[28:29], v[28:29], v[42:43], v[42:43] op_sel:[0,0,1] op_sel_hi:[1,0,1]\n\tv_pk_fma_f32 v[30:31], v[30:31], v[42:43], v[42:43] op_sel:[0,0,1] op_sel_hi:[1,0,1]\n\t" \
    "v_pk_fma_f32 v[32:33], v[32:33], v[42:43], v[42:43] op_sel:[0,0,1] op_sel_hi:[1,0,1]\n\tv_pk_fma_f32 v[34:35], v[34:35], v[42:43], v[42:43] op_sel:[0,0,1] op_sel_hi:[1,0,1]\n\tv_pk_fma_f32 v[36:37], v[36:37], v[42:43], v[42:43] op_sel:[0,0,1] op_sel_hi:[1,0,1]\n\tv_pk_fma_f32 v[38:39], v[38:39], v[42:43], v[42:43] op_sel:[0,0,1] op_sel_hi:[1,0,1]\n\tv_pk_fma_f32 v[40:41], v[40:41], v[42:43], v[42:43] op_sel:[0,0,1] op_sel_hi:[1,0,1]\n\tv_cvt_pk_bf16_f32 v48, v24, v25\n\tv_cvt_pk_bf16_f32 v49, v26, v27\n\tv_cvt_pk_bf16_f32 v50, v28, v29\n\t" \
    "v_cvt_pk_bf16_f32 v51, v30, v31\n\tv_cvt_pk_bf16_f32 v52, v32, v33\n\tv_cvt_pk_bf16_f32 v53, v34, v35\n\tv_cvt_pk_bf16_f32 v54, v36, v37\n\tv_cvt_pk_bf16_f32 v55, v38, v39\n\tv_cvt_pk_bf16_f32 v56, v40, v41\n\tds_write_b128 v90, v[48:51]\n\tds_write_b128 v90, v[52:55] offset:512\n\t" \
    "ds_write_b32 v91, v56 offset:1024\n\ts_waitcnt vmcnt(11) lgkmcnt(0)\n\ts_barrier\n\tds_read_b64_tr_b16 v[24:25], v94 offset:0\n\tds_read_b64_tr_b16 v[26:27], v94 offset:4672\n\tds_read_b64_tr_b16 v[28:29], v94 offset:512\n\tds_read_b64_tr_b16 v[30:31], v94 offset:5184\n\tds_read_b64_tr_b16 v[32:33], v95 offset:1024\n\t" \
    "ds_read_b64_tr_b16 v[34:35], v95 offset:5696\n\tbuffer_store_dwordx2 v[68:69], v89, s[8:11], s64 offen sc1\n\tv_readfirstlane_b32 s36, v58\n\tv_readfirstlane_b32 s37, v59\n\ts_lshl_b32 s73, s38, 12\n\ts_add_u32 s68, s61, s73\n\ts_add_u32 s36, s36, 0x7f4a7c15\n\ts_addc_u32 s37, s37, 0x9e3779b9\n\t" \
    "s_waitcnt lgkmcnt(4)\n\tbuffer_store_dwordx4 v[24:27], v88, s[8:11], s68 offen sc1\n\ts_add_u32 s68, s68, 16384\n\ts_waitcnt lgkmcnt(2)\n\tbuffer_store_dwordx4 v[28:31], v88, s[8:11], s68 offen sc1\n\ts_add_u32 s68, s61, 32768\n\ts_cmp_eq_u32 s38, 0\n\ts_cselect_b32 s68, s68, 0x7ff00000\n\t" \
    "s_waitcnt lgkmcnt(0)\n\tbuffer_store_dwordx4 v[32:35], v88, s[8:11], s68 offen sc1\n\tv_mov_b64 v[248:249], s[36:37]\n\tv_and_b32 v89, 7, v240\n\tv_lshlrev_b32 v89, 11, v89\n\ts_cmp_ge_u32 s43, 12\n\ts_cselect_b32 s69, s70, s43\n\ts_mul_i32 s69, s69, 294912\n\t" \
    "s_add_u32 s69, s69, 128\n\ts_add_u32 m0, s72, 4096\n\ts_nop 0\n\tbuffer_load_dwordx4 v10, s[12:15], s69 offen lds\n\ts_add_u32 m0, s72, 5120\n\ts_nop 0\n\tbuffer_load_dwordx4 v11, s[12:15], s69 offen lds\n\ts_cmp_ge_u32 s43, 11\n\t" \
    "s_cselect_b32 s69, s70, s43\n\ts_mul_i32 s69, s69, 294912\n\ts_add_u32 s69, s69, 294912\n\ts_add_u32 m0, s72, 8192\n\ts_nop 0\n\tbuffer_load_dwordx4 v10, s[12:15], s69 offen lds\n\ts_add_u32 m0, s72, 9216\n\ts_nop 0\n\t" \
    "buffer_load_dwordx4 v11, s[12:15], s69 offen lds\n\ts_waitcnt vmcnt(10)\n\ts_barrier\n\tds_read_b64_tr_b16 v[36:37], v93 offset:0\n\tds_read_b64_tr_b16 v[38:39], v93 offset:4608\n\tds_read_b64_tr_b16 v[40:41], v93 offset:9216\n\tds_read_b64_tr_b16 v[42:43], v93 offset:13312\n\tds_read_b64_tr_b16 v[44:45], v93 offset:17920\n\t" \
    "ds_read_b64_tr_b16 v[46:47], v93 offset:512\n\tds_read_b64_tr_b16 v[48:49], v93 offset:5120\n\tds_read_b64_tr_b16 v[50:51], v93 offset:9728\n\tds_read_b64_tr_b16 v[52:53], v93 offset:13824\n\tds_read_b64_tr_b16 v[54:55], v93 offset:18432\n\tds_read_b64_tr_b16 v[120:121], v92 offset:1024\n\tds_read_b64_tr_b16 v[122:123], v92 offset:5632\n\tds_read_b64_tr_b16 v[124:125], v92 offset:10240\n\t" \
    "ds_read_b64_tr_b16 v[126:127], v92 offset:14336\n\tds_read_b64_tr_b16 v[128:129], v92 offset:18944\n\tv_lshlrev_b32 v78, 16, v60\n\ts_cmp_ge_u32 s43, 11\n\ts_cselect_b32 s69, s70, s43\n\ts_mul_i32 s69, s69, 294912\n\ts_add_u32 s69, s69, 295040\n\ts_add_u32 m0, s72, 12288\n\t" \
    "v_and_b32 v79, 0xffff0000, v60\n\tv_lshlrev_b32 v80, 16, v61\n\tv_and_b32 v81, 0xffff0000, v61\n\ts_waitcnt lgkmcnt(10)\n\tv_lshlrev_b32 v66, 16, v36\n\tv_and_b32 v67, 0xffff0000, v36\n\tv_lshlrev_b32 v68, 16, v37\n\tbuffer_load_dwordx4 v10, s[12:15], s69 offen lds\n\t" \
    "s_add_u32 m0, s72, 13312\n\tv_and_b32 v69, 0xffff0000, v37\n\tv_lshlrev_b32 v70, 16, v38\n\tv_and_b32 v71, 0xffff0000, v38\n\tv_lshlrev_b32 v72, 16, v39\n\tv_and_b32 v73, 0xffff0000, v39\n\tv_lshlrev_b32 v74, 16, v40\n\tv_and_b32 v75, 0xffff0000, v40\n\t" \
    "v_lshlrev_b32 v76, 16, v41\n\tv_mul_f32 v16, v66, v81\n\tv_mul_f32 v17, v67, v81\n\tv_mul_f32 v18, v68, v81\n\tv_mul_f32 v19, v69, v81\n\tv_mul_f32 v20, v70, v81\n\tv_mul_f32 v21, v71, v81\n\tv_mul_f32 v22, v72, v81\n\t" \
    "v_mul_f32 v23, v73, v81\n\tv_fmac_f32 v16, v67, v80\n\tv_fmac_f32 v17, v68, v80\n\tv_fmac_f32 v18, v69, v80\n\tv_fmac_f32 v19, v70, v80\n\tv_fmac_f32 v20, v71, v80\n\tv_fmac_f32 v21, v72, v80\n\tv_fmac_f32 v22, v73, v80\n\t" \
    "v_fmac_f32 v23, v74, v80\n\tv_fmac_f32 v16, v68, v79\n\tv_fmac_f32 v17, v69, v79\n\tv_fmac_f32 v18, v70, v79\n\tv_fmac_f32 v19, v71, v79\n\tv_fmac_f32 v20, v72, v79\n\tbuffer_load_dwordx4 v11, s[12:15], s69 offen lds\n\ts_cmp_ge_u32 s43, 10\n\t" \
    "s_cselect_b32 s69, s70, s43\n\ts_mul_i32 s69, s69, 294912\n\ts_add_u32 s69, s69, 589824\n\ts_add_u32 m0, s72, 16384\n\tv_fmac_f32 v21, v73, v79\n\tv_fmac_f32 v22, v74, v79\n\tv_fmac_f32 v23, v75, v79\n\tv_fmac_f32 v16, v69, v78\n\t" \
    "v_fmac_f32 v17, v70, v78\n\tv_fmac_f32 v18, v71, v78\n\tv_fmac_f32 v19, v72, v78\n\tv_fmac_f32 v20, v73, v78\n\tv_fmac_f32 v21, v74, v78\n\tv_fmac_f32 v22, v75, v78\n\tv_fmac_f32 v23, v76, v78\n\tv_cvt_pk_bf16_f32 v66, v16, v17\n\t" \
    "v_cvt_pk_bf16_f32 v67, v18, v19\n\tv_cvt_pk_bf16_f32 v68, v20, v21\n\tv_cvt_pk_bf16_f32 v69, v22, v23\n\tv_lshlrev_b32 v16, 16, v66\n\tv_and_b32 v17, 0xffff0000, v66\n\tv_lshlrev_b32 v18, 16, v67\n\tv_and_b32 v19, 0xffff0000, v67\n\tv_lshlrev_b32 v20, 16, v68\n\t" \
    "v_and_b32 v21, 0xffff0000, v68\n\tv_lshlrev_b32 v22, 16, v69\n\tv_and_b32 v23, 0xffff0000, v69\n\tv_lshlrev_b32 v66, 16, v42\n\tv_and_b32 v67, 0xffff0000, v42\n\tbuffer_load_dwordx4 v10, s[12:15], s69 offen lds\n\ts_add_u32 m0, s72, 17408\n\tv_lshlrev_b32 v68, 16, v43\n\t" \
    "v_and_b32 v69, 0xffff0000, v43\n\tv_lshlrev_b32 v70, 16, v44\n\tv_and_b32 v71, 0xffff0000, v44\n\tv_lshlrev_b32 v72, 16, v45\n\tv_and_b32 v73, 0xffff0000, v45\n\tv_pk_mul_f32 v[16:17], v[16:17], v[66:67]\n\tv_pk_mul_f32 v[18:19], v[18:19], v[68:69]\n\tv_pk_mul_f32 v[20:21], v[20:21], v[70:71]\n\t" \
    "v_pk_mul_f32 v[22:23], v[22:23], v[72:73]\n\tv_cvt_pk_bf16_f32 v24, v16, v17\n\tv_cvt_pk_bf16_f32 v25, v18, v19\n\tv_cvt_pk_bf16_f32 v26, v20, v21\n\tv_cvt_pk_bf16_f32 v27, v22, v23\n\ts_add_u32 s68, s62, s54\n\tbuffer_store_dwordx4 v[24:27], v8, s[8:11], s68 offen sc1\n\tv_lshlrev_b32 v78, 16, v62\n\t" \
    "v_and_b32 v79, 0xffff0000, v62\n\tv_lshlrev_b32 v80, 16, v63\n\tv_and_b32 v81, 0xffff0000, v63\n\ts_waitcnt lgkmcnt(5)\n\tv_lshlrev_b32 v66, 16, v46\n\tv_and_b32 v67, 0xffff0000, v46\n\tv_lshlrev_b32 v68, 16, v47\n\tv_and_b32 v69, 0xffff0000, v47\n\t" \
    "v_lshlrev_b32 v70, 16, v48\n\tv_and_b32 v71, 0xffff0000, v48\n\tv_lshlrev_b32 v72, 16, v49\n\tv_and_b32 v73, 0xffff0000, v49\n\tv_lshlrev_b32 v74, 16, v50\n\tv_and_b32 v75, 0xffff0000, v50\n\tv_lshlrev_b32 v76, 16, v51\n\tv_mul_f32 v16, v66, v81\n\t" \
    "v_mul_f32 v17, v67, v81\n\tv_mul_f32 v18, v68, v81\n\tv_mul_f32 v19, v69, v81\n\tv_mul_f32 v20, v70, v81\n\tv_mul_f32 v21, v71, v81\n\tv_mul_f32 v22, v72, v81\n\tv_mul_f32 v23, v73, v81\n\tv_fmac_f32 v16, v67, v80\n\t" \
    "v_fmac_f32 v17, v68, v80\n\tv_fmac_f32 v18, v69, v80\n\tv_fmac_f32 v19, v70, v80\n\tv_fmac_f32 v20, v71, v80\n\tv_fmac_f32 v21, v72, v80\n\tbuffer_load_dwordx4 v11, s[12:15], s69 offen lds\n\ts_cmp_ge_u32 s43, 10\n\ts_cselect_b32 s69, s70, s43\n\t" \
    "s_mul_i32 s69, s69, 294912\n\ts_add_u32 s69, s69, 589952\n\ts_add_u32 m0, s72, 20480\n\tv_fmac_f32 v22, v73, v80\n\tv_fmac_f32 v23, v74, v80\n\tv_fmac_f32 v16, v68, v79\n\tv_fmac_f32 v17, v69, v79\n\tv_fmac_f32 v18, v70, v79\n\t" \
    "v_fmac_f32 v19, v71, v79\n\tv_fmac_f32 v20, v72, v79\n\tv_fmac_f32 v21, v73, v79\n\tv_fmac_f32 v22, v74, v79\n\tv_fmac_f32 v23, v75, v79\n\tv_fmac_f32 v16, v69, v78\n\tv_fmac_f32 v17, v70, v78\n\tv_fmac_f32 v18, v71, v78\n\t" \
    "v_fmac_f32 v19, v72, v78\n\tv_fmac_f32 v20, v73, v78\n\tv_fmac_f32 v21, v74, v78\n\tv_fmac_f32 v22, v75, v78\n\tv_fmac_f32 v23, v76, v78\n\tv_cvt_pk_bf16_f32 v66, v16, v17\n\tv_cvt_pk_bf16_f32 v67, v18, v19\n\tv_cvt_pk_bf16_f32 v68, v20, v21\n\t" \
    "v_cvt_pk_bf16_f32 v69, v22, v23\n\tv_lshlrev_b32 v16, 16, v66\n\tv_and_b32 v17, 0xffff0000, v66\n\tv_lshlrev_b32 v18, 16, v67\n\tbuffer_load_dwordx4 v10, s[12:15], s69 offen lds\n\ts_add_u32 m0, s72, 21504\n\tv_and_b32 v19, 0xffff0000, v67\n\tv_lshlrev_b32 v20, 16, v68\n\t" \
    "v_and_b32 v21, 0xffff0000, v68\n\tv_lshlrev_b32 v22, 16, v69\n\tv_and_b32 v23, 0xffff0000, v69\n\tv_lshlrev_b32 v66, 16, v52\n\tv_and_b32 v67, 0xffff0000, v52\n\tv_lshlrev_b32 v68, 16, v53\n\tv_and_b32 v69, 0xffff0000, v53\n\tv_lshlrev_b32 v70, 16, v54\n\t" \
    "v_and_b32 v71, 0xffff0000, v54\n\tv_lshlrev_b32 v72, 16, v55\n\tv_and_b32 v73, 0xffff0000, v55\n\tv_pk_mul_f32 v[16:17], v[16:17], v[66:67]\n\tv_pk_mul_f32 v[18:19], v[18:19], v[68:69]\n\tv_pk_mul_f32 v[20:21], v[20:21], v[70:71]\n\tv_pk_mul_f32 v[22:23], v[22:23], v[72:73]\n\tv_cvt_pk_bf16_f32 v28, v16, v17\n\t" \
    "v_cvt_pk_bf16_f32 v29, v18, v19\n\tv_cvt_pk_bf16_f32 v30, v20, v21\n\tv_cvt_pk_bf16_f32 v31, v22, v23\n\tbuffer_store_dwordx4 v[28:31], v9, s[8:11], s68 offen sc1\n\ts_waitcnt vmcnt(11) lgkmcnt(0)\n\ts_barrier\n\tbuffer_store_dwordx2 v[248:249], v89, s[8:11], s63 offen sc1\n\ts_cmp_lg_u32 s39, 0\n\t" \
    "s_cbranch_scc1 104f\n\ts_sub_u32 s73, s40, 216\n\ts_lshl_b32 s71, s73, 4\n\ts_mul_i32 s68, s73, 12\n\ts_add_u32 s68, s68, 96\n\ts_cmp_lt_u32 s73, 24\n\ts_cselect_b32 s71, s71, s68\n\ts_cselect_b32 s73, 0, 2146435072\n\t" \
    "v_cmp_eq_u32 vcc, 0, v240\n\tv_subrev_u32 v190, 8, v8\n\tv_mov_b32 v191, 2146435072\n\tv_cndmask_b32 v190, v190, v191, vcc\n\ts_add_u32 s68, s71, s38\n\ts_add_u32 s68, s68, 0\n\ts_lshl_b32 s73, s68, 1\n\ts_lshl_b32 s68, s68, 12\n\t" \
    "buffer_load_dwordx4 v[164:167], v8, s[76:79], s68 offen offset:0\n\tbuffer_load_dwordx4 v[168:171], v8, s[76:79], s68 offen offset:1024\n\tbuffer_load_dwordx4 v[172:175], v8, s[76:79], s68 offen offset:2048\n\tbuffer_load_dwordx4 v[176:179], v8, s[76:79], s68 offen offset:3072\n\tbuffer_load_dwordx2 v[180:181], v190, s[76:79], s68 offen\n\tbuffer_load_dwordx2 v[182:183], v8, s[76:79], s68 offen offset:1016\n\tbuffer_load_dwordx2 v[184:185], v8, s[76:79], s68 offen offset:2040\n\tbuffer_load_dwordx2 v[186:187], v8, s[76:79], s68 offen offset:3064\n\t" \
    "buffer_load_ushort v188, off, s[80:83], s73\n\t104:\n\tv_lshlrev_b32 v78, 16, v64\n\tds_read_b64_tr_b16 a[48:49], v0 offset:0\n\tv_and_b32 v79, 0xffff0000, v64\n\tds_read_b64_tr_b16 a[50:51], v0 offset:512\n\tv_lshlrev_b32 v80, 16, v65\n\tds_read_b64_tr_b16 a[52:53], v1 offset:0\n\t" \
    "v_and_b32 v81, 0xffff0000, v65\n\tds_read_b64_tr_b16 a[54:55], v1 offset:512\n\tv_lshlrev_b32 v66, 16, v120\n\tds_read_b64_tr_b16 a[56:57], v2 offset:0\n\tv_and_b32 v67, 0xffff0000, v120\n\tds_read_b64_tr_b16 a[58:59], v2 offset:512\n\tv_lshlrev_b32 v68, 16, v121\n\tds_read_b64_tr_b16 a[60:61], v3 offset:0\n\t" \
    "v_and_b32 v69, 0xffff0000, v121\n\tds_read_b64_tr_b16 a[62:63], v3 offset:512\n\tv_lshlrev_b32 v70, 16, v122\n\tv_and_b32 v71, 0xffff0000, v122\n\tv_lshlrev_b32 v72, 16, v123\n\tv_and_b32 v73, 0xffff0000, v123\n\tv_lshlrev_b32 v74, 16, v124\n\tv_and_b32 v75, 0xffff0000, v124\n\t" \
    "v_lshlrev_b32 v76, 16, v125\n\tv_mul_f32 v16, v66, v81\n\tv_mul_f32 v17, v67, v81\n\tv_mul_f32 v18, v68, v81\n\tv_mul_f32 v19, v69, v81\n\tv_mul_f32 v20, v70, v81\n\tbuffer_load_dwordx4 v11, s[12:15], s69 offen lds\n\ts_cmp_ge_u32 s43, 9\n\t" \
    "s_cselect_b32 s69, s70, s43\n\ts_mul_i32 s69, s69, 294912\n\ts_add_u32 s69, s69, 884736\n\ts_add_u32 m0, s72, 24576\n\tv_mul_f32 v21, v71, v81\n\tv_mul_f32 v22, v72, v81\n\tv_mul_f32 v23, v73, v81\n\tv_fmac_f32 v16, v67, v80\n\t" \
    "v_fmac_f32 v17, v68, v80\n\tv_fmac_f32 v18, v69, v80\n\tv_fmac_f32 v19, v70, v80\n\tv_fmac_f32 v20, v71, v80\n\tv_fmac_f32 v21, v72, v80\n\tv_fmac_f32 v22, v73, v80\n\tv_fmac_f32 v23, v74, v80\n\tv_fmac_f32 v16, v68, v79\n\t" \
    "v_fmac_f32 v17, v69, v79\n\tv_fmac_f32 v18, v70, v79\n\tv_fmac_f32 v19, v71, v79\n\tv_fmac_f32 v20, v72, v79\n\tv_fmac_f32 v21, v73, v79\n\tv_fmac_f32 v22, v74, v79\n\tv_fmac_f32 v23, v75, v79\n\tv_fmac_f32 v16, v69, v78\n\t" \
    "v_fmac_f32 v17, v70, v78\n\tv_fmac_f32 v18, v71, v78\n\tv_fmac_f32 v19, v72, v78\n\tv_fmac_f32 v20, v73, v78\n\tv_fmac_f32 v21, v74, v78\n\tbuffer_load_dwordx4 v10, s[12:15], s69 offen lds\n\ts_add_u32 m0, s72, 25600\n\tv_fmac_f32 v22, v75, v78\n\t" \
    "v_fmac_f32 v23, v76, v78\n\tv_cvt_pk_bf16_f32 v66, v16, v17\n\tv_cvt_pk_bf16_f32 v67, v18, v19\n\tv_cvt_pk_bf16_f32 v68, v20, v21\n\tv_cvt_pk_bf16_f32 v69, v22, v23\n\tv_lshlrev_b32 v16, 16, v66\n\tv_and_b32 v17, 0xffff0000, v66\n\tv_lshlrev_b32 v18, 16, v67\n\t" \
    "v_and_b32 v19, 0xffff0000, v67\n\tv_lshlrev_b32 v20, 16, v68\n\tv_and_b32 v21, 0xffff0000, v68\n\tv_lshlrev_b32 v22, 16, v69\n\tv_and_b32 v23, 0xffff0000, v69\n\tv_lshlrev_b32 v66, 16, v126\n\tv_and_b32 v67, 0xffff0000, v126\n\tv_lshlrev_b32 v68, 16, v127\n\t" \
    "v_and_b32 v69, 0xffff0000, v127\n\tv_lshlrev_b32 v70, 16, v128\n\tv_and_b32 v71, 0xffff0000, v128\n\tv_lshlrev_b32 v72, 16, v129\n\tv_and_b32 v73, 0xffff0000, v129\n\tv_pk_mul_f32 v[16:17], v[16:17], v[66:67]\n\tv_pk_mul_f32 v[18:19], v[18:19], v[68:69]\n\tv_pk_mul_f32 v[20:21], v[20:21], v[70:71]\n\t" \
    "v_pk_mul_f32 v[22:23], v[22:23], v[72:73]\n\tv_cvt_pk_bf16_f32 v32, v16, v17\n\tv_cvt_pk_bf16_f32 v33, v18, v19\n\tv_cvt_pk_bf16_f32 v34, v20, v21\n\tbuffer_load_dwordx4 v11, s[12:15], s69 offen lds\n\ts_xor_b32 s71, s57, 1\n\ts_lshl_b32 s71, s71, 6\n\ts_lshl_b32 s69, s42, 8\n\t" \
    "s_add_u32 s71, s71, s69\n\tv_lshlrev_b32 v96, 3, v245\n\tv_cvt_pk_bf16_f32 v35, v22, v23\n\ts_add_u32 s68, s62, 8192\n\ts_cmp_eq_u32 s38, 1\n\ts_cselect_b32 s68, s68, 0x7ff00000\n\tbuffer_store_dwordx4 v[32:35], v8, s[8:11], s68 offen sc1\n\ts_cmp_eq_u32 s39, 0\n\t" \
    "s_cbranch_scc1 101f\n\tbuffer_load_dwordx2 v[112:113], v96, s[28:31], s71 offen offset:0\n\tbuffer_load_dwordx2 v[114:115], v96, s[28:31], s71 offen offset:32\n\tbuffer_load_dwordx2 v[116:117], v96, s[28:31], s71 offen offset:128\n\tbuffer_load_dwordx2 v[118:119], v96, s[28:31], s71 offen offset:160\n\tv_lshrrev_b32 v229, 3, v244\n\tbuffer_load_ushort v228, v229, s[32:35], 0 offen offset:0\n\tbuffer_load_ushort v232, v229, s[32:35], 0 offen offset:32\n\t" \
    "s_cmp_ge_u32 s43, 9\n\ts_cselect_b32 s69, s70, s43\n\ts_mul_i32 s69, s69, 294912\n\ts_add_u32 s69, s69, 884864\n\ts_add_u32 m0, s72, 28672\n\ts_nop 0\n\tbuffer_load_dwordx4 v10, s[12:15], s69 offen lds\n\ts_add_u32 m0, s72, 29696\n\t" \
    "s_nop 0\n\tbuffer_load_dwordx4 v11, s[12:15], s69 offen lds\n\ts_cmp_ge_u32 s43, 8\n\ts_cselect_b32 s69, s70, s43\n\ts_mul_i32 s69, s69, 294912\n\ts_add_u32 s69, s69, 1179648\n\ts_add_u32 m0, s72, 32768\n\ts_nop 0\n\t" \
    "buffer_load_dwordx4 v10, s[12:15], s69 offen lds\n\ts_add_u32 m0, s72, 33792\n\ts_nop 0\n\tbuffer_load_dwordx4 v11, s[12:15], s69 offen lds\n\ts_waitcnt vmcnt(10)\n\tbuffer_store_dwordx2 v[248:249], off, s[8:11], s48 sc1\n\ts_cmp_ge_u32 s43, 8\n\ts_cselect_b32 s69, s70, s43\n\t" \
    "s_mul_i32 s69, s69, 294912\n\ts_add_u32 s69, s69, 1179776\n\ts_add_u32 m0, s72, 36864\n\ts_nop 0\n\tbuffer_load_dwordx4 v10, s[12:15], s69 offen lds\n\ts_add_u32 m0, s72, 37888\n\ts_nop 0\n\tbuffer_load_dwordx4 v11, s[12:15], s69 offen lds\n\t" \
    "s_cmp_ge_u32 s43, 7\n\ts_cselect_b32 s69, s70, s43\n\ts_mul_i32 s69, s69, 294912\n\ts_add_u32 s69, s69, 1474560\n\ts_add_u32 m0, s72, 40960\n\ts_nop 0\n\tbuffer_load_dwordx4 v10, s[12:15], s69 offen lds\n\ts_add_u32 m0, s72, 41984\n\t" \
    "s_nop 0\n\tbuffer_load_dwordx4 v11, s[12:15], s69 offen lds\n\ts_cmp_ge_u32 s43, 7\n\ts_cselect_b32 s69, s70, s43\n\ts_mul_i32 s69, s69, 294912\n\ts_add_u32 s69, s69, 1474688\n\ts_add_u32 m0, s72, 45056\n\ts_nop 0\n\t" \
    "buffer_load_dwordx4 v10, s[12:15], s69 offen lds\n\ts_add_u32 m0, s72, 46080\n\ts_nop 0\n\tbuffer_load_dwordx4 v11, s[12:15], s69 offen lds\n\ts_cmp_ge_u32 s43, 6\n\ts_cselect_b32 s69, s70, s43\n\ts_mul_i32 s69, s69, 294912\n\ts_add_u32 s69, s69, 1769472\n\t" \
    "s_add_u32 m0, s72, 49152\n\ts_nop 0\n\tbuffer_load_dwordx4 v10, s[12:15], s69 offen lds\n\ts_add_u32 m0, s72, 50176\n\ts_nop 0\n\tbuffer_load_dwordx4 v11, s[12:15], s69 offen lds\n\ts_cmp_ge_u32 s43, 6\n\ts_cselect_b32 s69, s70, s43\n\t" \
    "s_mul_i32 s69, s69, 294912\n\ts_add_u32 s69, s69, 1769600\n\ts_add_u32 m0, s72, 53248\n\ts_nop 0\n\tbuffer_load_dwordx4 v10, s[12:15], s69 offen lds\n\ts_add_u32 m0, s72, 54272\n\ts_nop 0\n\tbuffer_load_dwordx4 v11, s[12:15], s69 offen lds\n\t" \
    "s_cmp_ge_u32 s43, 5\n\ts_cselect_b32 s69, s70, s43\n\ts_mul_i32 s69, s69, 294912\n\ts_add_u32 s69, s69, 2064384\n\ts_add_u32 m0, s72, 57344\n\ts_nop 0\n\tbuffer_load_dwordx4 v10, s[12:15], s69 offen lds\n\ts_add_u32 m0, s72, 58368\n\t" \
    "s_nop 0\n\tbuffer_load_dwordx4 v11, s[12:15], s69 offen lds\n\ts_cmp_ge_u32 s43, 5\n\ts_cselect_b32 s69, s70, s43\n\ts_mul_i32 s69, s69, 294912\n\ts_add_u32 s69, s69, 2064512\n\ts_add_u32 m0, s72, 61440\n\ts_nop 0\n\t" \
    "buffer_load_dwordx4 v10, s[12:15], s69 offen lds\n\ts_add_u32 m0, s72, 62464\n\ts_nop 0\n\tbuffer_load_dwordx4 v11, s[12:15], s69 offen lds\n\ts_waitcnt vmcnt(17) lgkmcnt(0)\n\ts_barrier\n\tds_read_b64_tr_b16 a[64:65], v0 offset:4096\n\tds_read_b64_tr_b16 a[66:67], v0 offset:4608\n\t" \
    "ds_read_b64_tr_b16 a[68:69], v1 offset:4096\n\tds_read_b64_tr_b16 a[70:71], v1 offset:4608\n\tds_read_b64_tr_b16 a[72:73], v2 offset:4096\n\tds_read_b64_tr_b16 a[74:75], v2 offset:4608\n\tds_read_b64_tr_b16 a[76:77], v3 offset:4096\n\tds_read_b64_tr_b16 a[78:79], v3 offset:4608\n\tds_read_b64_tr_b16 a[80:81], v0 offset:8192\n\tds_read_b64_tr_b16 a[82:83], v0 offset:8704\n\t" \
    "ds_read_b64_tr_b16 a[84:85], v1 offset:8192\n\tds_read_b64_tr_b16 a[86:87], v1 offset:8704\n\tds_read_b64_tr_b16 a[88:89], v2 offset:8192\n\tds_read_b64_tr_b16 a[90:91], v2 offset:8704\n\tds_read_b64_tr_b16 a[92:93], v3 offset:8192\n\tds_read_b64_tr_b16 a[94:95], v3 offset:8704\n\tds_read_b64_tr_b16 a[96:97], v0 offset:12288\n\tds_read_b64_tr_b16 a[98:99], v0 offset:12800\n\t" \
    "ds_read_b64_tr_b16 a[100:101], v1 offset:12288\n\tds_read_b64_tr_b16 a[102:103], v1 offset:12800\n\tds_read_b64_tr_b16 a[104:105], v2 offset:12288\n\tds_read_b64_tr_b16 a[106:107], v2 offset:12800\n\tds_read_b64_tr_b16 a[108:109], v3 offset:12288\n\tds_read_b64_tr_b16 a[110:111], v3 offset:12800\n\tds_read_b64_tr_b16 a[112:113], v0 offset:16384\n\tds_read_b64_tr_b16 a[114:115], v0 offset:16896\n\t" \
    "ds_read_b64_tr_b16 a[116:117], v1 offset:16384\n\tds_read_b64_tr_b16 a[118:119], v1 offset:16896\n\tds_read_b64_tr_b16 a[120:121], v2 offset:16384\n\tds_read_b64_tr_b16 a[122:123], v2 offset:16896\n\tds_read_b64_tr_b16 a[124:125], v3 offset:16384\n\tds_read_b64_tr_b16 a[126:127], v3 offset:16896\n\tds_read_b64_tr_b16 a[128:129], v0 offset:20480\n\tds_read_b64_tr_b16 a[130:131], v0 offset:20992\n\t" \
    "ds_read_b64_tr_b16 a[132:133], v1 offset:20480\n\tds_read_b64_tr_b16 a[134:135], v1 offset:20992\n\tds_read_b64_tr_b16 a[136:137], v2 offset:20480\n\tds_read_b64_tr_b16 a[138:139], v2 offset:20992\n\tds_read_b64_tr_b16 a[140:141], v3 offset:20480\n\tds_read_b64_tr_b16 a[142:143], v3 offset:20992\n\tds_read_b64_tr_b16 a[144:145], v0 offset:24576\n\tds_read_b64_tr_b16 a[146:147], v0 offset:25088\n\t" \
    "ds_read_b64_tr_b16 a[148:149], v1 offset:24576\n\tds_read_b64_tr_b16 a[150:151], v1 offset:25088\n\tds_read_b64_tr_b16 a[152:153], v2 offset:24576\n\tds_read_b64_tr_b16 a[154:155], v2 offset:25088\n\tds_read_b64_tr_b16 a[156:157], v3 offset:24576\n\tds_read_b64_tr_b16 a[158:159], v3 offset:25088\n\tds_read_b64_tr_b16 a[160:161], v0 offset:28672\n\tds_read_b64_tr_b16 a[162:163], v0 offset:29184\n\t" \
    "ds_read_b64_tr_b16 a[164:165], v1 offset:28672\n\tds_read_b64_tr_b16 a[166:167], v1 offset:29184\n\tds_read_b64_tr_b16 a[168:169], v2 offset:28672\n\tds_read_b64_tr_b16 a[170:171], v2 offset:29184\n\tds_read_b64_tr_b16 a[172:173], v3 offset:28672\n\tds_read_b64_tr_b16 a[174:175], v3 offset:29184\n\ts_cmp_ge_u32 s43, 4\n\ts_cselect_b32 s69, s70, s43\n\t" \
    "s_mul_i32 s69, s69, 294912\n\ts_add_u32 s69, s69, 2359296\n\ts_add_u32 m0, s72, 0\n\ts_nop 0\n\tbuffer_load_dwordx4 v10, s[12:15], s69 offen lds\n\ts_add_u32 m0, s72, 1024\n\ts_nop 0\n\tbuffer_load_dwordx4 v11, s[12:15], s69 offen lds\n\t" \
    "s_waitcnt lgkmcnt(0)\n\ts_barrier\n\tbuffer_load_dwordx4 v[250:253], v8, s[8:11], s47 offen sc1\n\ts_cmp_ge_u32 s43, 4\n\ts_cselect_b32 s69, s70, s43\n\ts_mul_i32 s69, s69, 294912\n\ts_add_u32 s69, s69, 2359424\n\ts_add_u32 m0, s72, 4096\n\t" \
    "s_nop 0\n\tbuffer_load_dwordx4 v10, s[12:15], s69 offen lds\n\ts_add_u32 m0, s72, 5120\n\ts_nop 0\n\tbuffer_load_dwordx4 v11, s[12:15], s69 offen lds\n\ts_cmp_ge_u32 s43, 3\n\ts_cselect_b32 s69, s70, s43\n\ts_mul_i32 s69, s69, 294912\n\t" \
    "s_add_u32 s69, s69, 2654208\n\ts_add_u32 m0, s72, 8192\n\ts_nop 0\n\tbuffer_load_dwordx4 v10, s[12:15], s69 offen lds\n\ts_add_u32 m0, s72, 9216\n\ts_nop 0\n\tbuffer_load_dwordx4 v11, s[12:15], s69 offen lds\n\ts_cmp_ge_u32 s43, 3\n\t" \
    "s_cselect_b32 s69, s70, s43\n\ts_mul_i32 s69, s69, 294912\n\ts_add_u32 s69, s69, 2654336\n\ts_add_u32 m0, s72, 12288\n\ts_nop 0\n\tbuffer_load_dwordx4 v10, s[12:15], s69 offen lds\n\ts_add_u32 m0, s72, 13312\n\ts_nop 0\n\t" \
    "buffer_load_dwordx4 v11, s[12:15], s69 offen lds\n\ts_cmp_ge_u32 s43, 2\n\ts_cselect_b32 s69, s70, s43\n\ts_mul_i32 s69, s69, 294912\n\ts_add_u32 s69, s69, 2949120\n\ts_add_u32 m0, s72, 16384\n\ts_nop 0\n\tbuffer_load_dwordx4 v10, s[12:15], s69 offen lds\n\t" \
    "s_add_u32 m0, s72, 17408\n\ts_nop 0\n\tbuffer_load_dwordx4 v11, s[12:15], s69 offen lds\n\ts_cmp_ge_u32 s43, 2\n\ts_cselect_b32 s69, s70, s43\n\ts_mul_i32 s69, s69, 294912\n\ts_add_u32 s69, s69, 2949248\n\ts_add_u32 m0, s72, 20480\n\t" \
    "s_nop 0\n\tbuffer_load_dwordx4 v10, s[12:15], s69 offen lds\n\ts_add_u32 m0, s72, 21504\n\ts_nop 0\n\tbuffer_load_dwordx4 v11, s[12:15], s69 offen lds\n\ts_cmp_ge_u32 s43, 1\n\ts_cselect_b32 s69, s70, s43\n\ts_mul_i32 s69, s69, 294912\n\t" \
    "s_add_u32 s69, s69, 3244032\n\ts_add_u32 m0, s72, 24576\n\ts_nop 0\n\tbuffer_load_dwordx4 v10, s[12:15], s69 offen lds\n\ts_add_u32 m0, s72, 25600\n\ts_nop 0\n\tbuffer_load_dwordx4 v11, s[12:15], s69 offen lds\n\ts_cmp_ge_u32 s43, 1\n\t" \
    "s_cselect_b32 s69, s70, s43\n\ts_mul_i32 s69, s69, 294912\n\ts_add_u32 s69, s69, 3244160\n\ts_add_u32 m0, s72, 28672\n\ts_nop 0\n\tbuffer_load_dwordx4 v10, s[12:15], s69 offen lds\n\ts_add_u32 m0, s72, 29696\n\ts_nop 0\n\t" \
    "buffer_load_dwordx4 v11, s[12:15], s69 offen lds\n\ts_waitcnt vmcnt(14)\n\tv_cmp_ne_u64 vcc, s[36:37], v[250:251]\n\tv_cmp_ne_u64 s[74:75], s[36:37], v[252:253]\n\ts_or_b64 s[74:75], s[74:75], vcc\n\ts_cbranch_scc0 106f\n\ts_mov_b32 s71, 1048576\n\t105:\n\t" \
    "s_sleep 1\n\tbuffer_load_dwordx4 v[250:253], v8, s[8:11], s47 offen sc1\n\ts_waitcnt vmcnt(0)\n\tv_cmp_ne_u64 vcc, s[36:37], v[250:251]\n\tv_cmp_ne_u64 s[74:75], s[36:37], v[252:253]\n\ts_or_b64 s[74:75], s[74:75], vcc\n\ts_cbranch_scc0 106f\n\ts_sub_u32 s71, s71, 1\n\t" \
    "s_cbranch_scc0 105b\n\t106:\n\ts_cmp_ge_u32 s43, 12\n\ts_cselect_b32 s68, s70, s43\n\ts_mul_i32 s68, s68, 36864\n\ts_add_u32 s68, s68, s44\n\ts_add_u32 s68, s68, 0\n\tbuffer_load_dwordx4 v[120:123], v8, s[8:11], s68 offen offset:0 sc1\n\t" \
    "buffer_load_dwordx4 v[124:127], v8, s[8:11], s68 offen offset:1024 sc1\n\tbuffer_load_dwordx4 v[128:131], v8, s[8:11], s68 offen offset:2048 sc1\n\ts_cmp_ge_u32 s43, 11\n\ts_cselect_b32 s68, s70, s43\n\ts_mul_i32 s68, s68, 36864\n\ts_add_u32 s68, s68, s44\n\ts_add_u32 s68, s68, 36864\n\tbuffer_load_dwordx4 v[132:135], v8, s[8:11], s68 offen offset:0 sc1\n\t" \
    "buffer_load_dwordx4 v[136:139], v8, s[8:11], s68 offen offset:1024 sc1\n\tbuffer_load_dwordx4 v[140:143], v8, s[8:11], s68 offen offset:2048 sc1\n\ts_cmp_ge_u32 s43, 10\n\ts_cselect_b32 s68, s70, s43\n\ts_mul_i32 s68, s68, 36864\n\ts_add_u32 s68, s68, s44\n\ts_add_u32 s68, s68, 73728\n\tbuffer_load_dwordx4 v[144:147], v8, s[8:11], s68 offen offset:0 sc1\n\t" \
    "buffer_load_dwordx4 v[148:151], v8, s[8:11], s68 offen offset:1024 sc1\n\tbuffer_load_dwordx4 v[152:155], v8, s[8:11], s68 offen offset:2048 sc1\n\ts_cmp_ge_u32 s43, 9\n\ts_cselect_b32 s68, s70, s43\n\ts_mul_i32 s68, s68, 36864\n\ts_add_u32 s68, s68, s44\n\ts_add_u32 s68, s68, 110592\n\tbuffer_load_dwordx4 v[156:159], v8, s[8:11], s68 offen offset:0 sc1\n\t" \
    "buffer_load_dwordx4 v[160:163], v8, s[8:11], s68 offen offset:1024 sc1\n\tbuffer_load_dwordx4 v[164:167], v8, s[8:11], s68 offen offset:2048 sc1\n\tv_lshlrev_b32 v96, 16, v112\n\tv_and_b32 v97, 0xffff0000, v112\n\tv_lshlrev_b32 v98, 16, v113\n\tv_and_b32 v99, 0xffff0000, v113\n\tv_lshlrev_b32 v100, 16, v114\n\tv_and_b32 v101, 0xffff0000, v114\n\t" \
    "v_lshlrev_b32 v102, 16, v115\n\tv_and_b32 v103, 0xffff0000, v115\n\tv_lshlrev_b32 v104, 16, v116\n\tv_and_b32 v105, 0xffff0000, v116\n\tv_lshlrev_b32 v106, 16, v117\n\tv_and_b32 v107, 0xffff0000, v117\n\tv_lshlrev_b32 v108, 16, v118\n\tv_and_b32 v109, 0xffff0000, v118\n\t" \
    "v_lshlrev_b32 v110, 16, v119\n\tv_and_b32 v111, 0xffff0000, v119\n\tv_lshlrev_b32 v228, 16, v228\n\tv_mov_b32 v229, v228\n\tv_mov_b32 v230, v228\n\tv_mov_b32 v231, v228\n\tv_lshlrev_b32 v232, 16, v232\n\tv_mov_b32 v233, v232\n\t" \
    "v_mov_b32 v234, v232\n\tv_mov_b32 v235, v232\n\ts_barrier\n\ts_waitcnt vmcnt(9)\n\tv_mfma_f32_16x16x32_bf16 a[0:3], a[48:51], v[120:123], 0\n\ts_cmp_ge_u32 s43, 8\n\ts_cselect_b32 s68, s70, s43\n\ts_mul_i32 s68, s68, 36864\n\t" \
    "s_add_u32 s68, s68, s44\n\tv_mfma_f32_16x16x32_bf16 a[8:11], a[48:51], v[124:127], 0\n\ts_add_u32 s68, s68, 147456\n\tv_mfma_f32_16x16x32_bf16 a[16:19], a[48:51], v[128:131], 0\n\tv_mfma_f32_16x16x32_bf16 a[4:7], a[52:55], v[120:123], 0\n\tv_mfma_f32_16x16x32_bf16 a[12:15], a[52:55], v[124:127], 0\n\tbuffer_load_dwordx4 v[168:171], v8, s[8:11], s68 offen offset:0 sc1\n\tv_mfma_f32_16x16x32_bf16 a[20:23], a[52:55], v[128:131], 0\n\t" \
    "v_mfma_f32_16x16x32_bf16 v[48:51], a[56:59], v[120:123], v[96:99]\n\tv_mfma_f32_16x16x32_bf16 v[56:59], a[56:59], v[124:127], v[96:99]\n\tbuffer_load_dwordx4 v[172:175], v8, s[8:11], s68 offen offset:1024 sc1\n\tv_mfma_f32_16x16x32_bf16 v[64:67], a[56:59], v[128:131], v[96:99]\n\tv_mfma_f32_16x16x32_bf16 v[52:55], a[60:63], v[120:123], v[100:103]\n\tv_mfma_f32_16x16x32_bf16 v[60:63], a[60:63], v[124:127], v[100:103]\n\tbuffer_load_dwordx4 v[176:179], v8, s[8:11], s68 offen offset:2048 sc1\n\ts_cmp_ge_u32 s43, 7\n\t" \
    "s_cselect_b32 s68, s70, s43\n\ts_mul_i32 s68, s68, 36864\n\tv_mfma_f32_16x16x32_bf16 v[68:71], a[60:63], v[128:131], v[100:103]\n\ts_add_u32 s68, s68, s44\n\ts_add_u32 s68, s68, 184320\n\tv_mfma_f32_16x16x32_bf16 a[24:27], a[64:67], v[120:123], 0\n\tv_mfma_f32_16x16x32_bf16 a[32:35], a[64:67], v[124:127], 0\n\tbuffer_load_dwordx4 v[180:183], v8, s[8:11], s68 offen offset:0 sc1\n\t" \
    "v_mfma_f32_16x16x32_bf16 a[40:43], a[64:67], v[128:131], 0\n\tv_mfma_f32_16x16x32_bf16 a[28:31], a[68:71], v[120:123], 0\n\tv_mfma_f32_16x16x32_bf16 a[36:39], a[68:71], v[124:127], 0\n\tbuffer_load_dwordx4 v[184:187], v8, s[8:11], s68 offen offset:1024 sc1\n\tv_mfma_f32_16x16x32_bf16 a[44:47], a[68:71], v[128:131], 0\n\tv_mfma_f32_16x16x32_bf16 v[72:75], a[72:75], v[120:123], v[104:107]\n\tv_mfma_f32_16x16x32_bf16 v[80:83], a[72:75], v[124:127], v[104:107]\n\tbuffer_load_dwordx4 v[188:191], v8, s[8:11], s68 offen offset:2048 sc1\n\t" \
    "s_cmp_ge_u32 s43, 6\n\ts_cselect_b32 s68, s70, s43\n\ts_mul_i32 s68, s68, 36864\n\tv_mfma_f32_16x16x32_bf16 v[88:91], a[72:75], v[128:131], v[104:107]\n\ts_add_u32 s68, s68, s44\n\ts_add_u32 s68, s68, 221184\n\tv_mfma_f32_16x16x32_bf16 v[76:79], a[76:79], v[120:123], v[108:111]\n\tv_mfma_f32_16x16x32_bf16 v[84:87], a[76:79], v[124:127], v[108:111]\n\t" \
    "buffer_load_dwordx4 v[192:195], v8, s[8:11], s68 offen offset:0 sc1\n\tv_mfma_f32_16x16x32_bf16 v[92:95], a[76:79], v[128:131], v[108:111]\n\ts_barrier\n\ts_waitcnt vmcnt(13)\n\tv_mfma_f32_16x16x32_bf16 a[0:3], a[80:83], v[132:135], a[0:3]\n\tv_mfma_f32_16x16x32_bf16 a[8:11], a[80:83], v[136:139], a[8:11]\n\tbuffer_load_dwordx4 v[196:199], v8, s[8:11], s68 offen offset:1024 sc1\n\tv_mfma_f32_16x16x32_bf16 a[16:19], a[80:83], v[140:143], a[16:19]\n\t" \
    "v_mfma_f32_16x16x32_bf16 a[4:7], a[84:87], v[132:135], a[4:7]\n\tv_mfma_f32_16x16x32_bf16 a[12:15], a[84:87], v[136:139], a[12:15]\n\tbuffer_load_dwordx4 v[200:203], v8, s[8:11], s68 offen offset:2048 sc1\n\ts_cmp_ge_u32 s43, 5\n\ts_cselect_b32 s68, s70, s43\n\ts_mul_i32 s68, s68, 36864\n\tv_mfma_f32_16x16x32_bf16 a[20:23], a[84:87], v[140:143], a[20:23]\n\ts_add_u32 s68, s68, s44\n\t" \
    "s_add_u32 s68, s68, 258048\n\tv_mfma_f32_16x16x32_bf16 v[48:51], a[88:91], v[132:135], v[48:51]\n\tv_mfma_f32_16x16x32_bf16 v[56:59], a[88:91], v[136:139], v[56:59]\n\tv_mfma_f32_16x16x32_bf16 v[64:67], a[88:91], v[140:143], v[64:67]\n\tv_mfma_f32_16x16x32_bf16 v[52:55], a[92:95], v[132:135], v[52:55]\n\tv_mfma_f32_16x16x32_bf16 v[60:63], a[92:95], v[136:139], v[60:63]\n\tbuffer_load_dwordx4 v[120:123], v8, s[8:11], s68 offen offset:0 sc1\n\tv_mfma_f32_16x16x32_bf16 v[68:71], a[92:95], v[140:143], v[68:71]\n\t" \
    "v_mfma_f32_16x16x32_bf16 a[24:27], a[96:99], v[132:135], a[24:27]\n\tv_mfma_f32_16x16x32_bf16 a[32:35], a[96:99], v[136:139], a[32:35]\n\tbuffer_load_dwordx4 v[124:127], v8, s[8:11], s68 offen offset:1024 sc1\n\tv_mfma_f32_16x16x32_bf16 a[40:43], a[96:99], v[140:143], a[40:43]\n\tv_mfma_f32_16x16x32_bf16 a[28:31], a[100:103], v[132:135], a[28:31]\n\tv_mfma_f32_16x16x32_bf16 a[36:39], a[100:103], v[136:139], a[36:39]\n\tbuffer_load_dwordx4 v[128:131], v8, s[8:11], s68 offen offset:2048 sc1\n\ts_cmp_ge_u32 s43, 4\n\t" \
    "s_cselect_b32 s68, s70, s43\n\ts_mul_i32 s68, s68, 36864\n\tv_mfma_f32_16x16x32_bf16 a[44:47], a[100:103], v[140:143], a[44:47]\n\ts_add_u32 s68, s68, s44\n\ts_add_u32 s68, s68, 294912\n\tv_mfma_f32_16x16x32_bf16 v[72:75], a[104:107], v[132:135], v[72:75]\n\tv_mfma_f32_16x16x32_bf16 v[80:83], a[104:107], v[136:139], v[80:83]\n\tv_mfma_f32_16x16x32_bf16 v[88:91], a[104:107], v[140:143], v[88:91]\n\t" \
    "v_mfma_f32_16x16x32_bf16 v[76:79], a[108:111], v[132:135], v[76:79]\n\tv_mfma_f32_16x16x32_bf16 v[84:87], a[108:111], v[136:139], v[84:87]\n\tbuffer_load_dwordx4 v[204:207], v8, s[8:11], s68 offen offset:0 sc1\n\tv_mfma_f32_16x16x32_bf16 v[92:95], a[108:111], v[140:143], v[92:95]\n\ts_barrier\n\ts_waitcnt vmcnt(16)\n\tv_mfma_f32_16x16x32_bf16 a[0:3], a[112:115], v[144:147], a[0:3]\n\tv_mfma_f32_16x16x32_bf16 a[8:11], a[112:115], v[148:151], a[8:11]\n\t" \
    "buffer_load_dwordx4 v[208:211], v8, s[8:11], s68 offen offset:1024 sc1\n\tv_mfma_f32_16x16x32_bf16 a[16:19], a[112:115], v[152:155], a[16:19]\n\tv_mfma_f32_16x16x32_bf16 a[4:7], a[116:119], v[144:147], a[4:7]\n\tv_mfma_f32_16x16x32_bf16 a[12:15], a[116:119], v[148:151], a[12:15]\n\tbuffer_load_dwordx4 v[212:215], v8, s[8:11], s68 offen offset:2048 sc1\n\ts_cmp_ge_u32 s43, 3\n\ts_cselect_b32 s68, s70, s43\n\ts_mul_i32 s68, s68, 36864\n\t" \
    "v_mfma_f32_16x16x32_bf16 a[20:23], a[116:119], v[152:155], a[20:23]\n\ts_add_u32 s68, s68, s44\n\ts_add_u32 s68, s68, 331776\n\tv_mfma_f32_16x16x32_bf16 v[48:51], a[120:123], v[144:147], v[48:51]\n\tv_mfma_f32_16x16x32_bf16 v[56:59], a[120:123], v[148:151], v[56:59]\n\tbuffer_load_dwordx4 v[132:135], v8, s[8:11], s68 offen offset:0 sc1\n\tv_mfma_f32_16x16x32_bf16 v[64:67], a[120:123], v[152:155], v[64:67]\n\tv_mfma_f32_16x16x32_bf16 v[52:55], a[124:127], v[144:147], v[52:55]\n\t" \
    "v_mfma_f32_16x16x32_bf16 v[60:63], a[124:127], v[148:151], v[60:63]\n\tv_mfma_f32_16x16x32_bf16 v[68:71], a[124:127], v[152:155], v[68:71]\n\tv_mfma_f32_16x16x32_bf16 a[24:27], a[128:131], v[144:147], a[24:27]\n\tv_mfma_f32_16x16x32_bf16 a[32:35], a[128:131], v[148:151], a[32:35]\n\tbuffer_load_dwordx4 v[136:139], v8, s[8:11], s68 offen offset:1024 sc1\n\tv_mfma_f32_16x16x32_bf16 a[40:43], a[128:131], v[152:155], a[40:43]\n\tv_mfma_f32_16x16x32_bf16 a[28:31], a[132:135], v[144:147], a[28:31]\n\tv_mfma_f32_16x16x32_bf16 a[36:39], a[132:135], v[148:151], a[36:39]\n\t" \
    "buffer_load_dwordx4 v[140:143], v8, s[8:11], s68 offen offset:2048 sc1\n\ts_cmp_ge_u32 s43, 2\n\ts_cselect_b32 s68, s70, s43\n\ts_mul_i32 s68, s68, 36864\n\tv_mfma_f32_16x16x32_bf16 a[44:47], a[132:135], v[152:155], a[44:47]\n\ts_add_u32 s68, s68, s44\n\ts_add_u32 s68, s68, 368640\n\tv_mfma_f32_16x16x32_bf16 v[72:75], a[136:139], v[144:147], v[72:75]\n\t" \
    "v_mfma_f32_16x16x32_bf16 v[80:83], a[136:139], v[148:151], v[80:83]\n\tbuffer_load_dwordx4 v[216:219], v8, s[8:11], s68 offen offset:0 sc1\n\tv_mfma_f32_16x16x32_bf16 v[88:91], a[136:139], v[152:155], v[88:91]\n\tv_mfma_f32_16x16x32_bf16 v[76:79], a[140:143], v[144:147], v[76:79]\n\tv_mfma_f32_16x16x32_bf16 v[84:87], a[140:143], v[148:151], v[84:87]\n\tv_mfma_f32_16x16x32_bf16 v[92:95], a[140:143], v[152:155], v[92:95]\n\ts_barrier\n\ts_waitcnt vmcnt(19)\n\t" \
    "v_mfma_f32_16x16x32_bf16 a[0:3], a[144:147], v[156:159], a[0:3]\n\tv_mfma_f32_16x16x32_bf16 a[8:11], a[144:147], v[160:163], a[8:11]\n\tbuffer_load_dwordx4 a[48:51], v8, s[8:11], s68 offen offset:1024 sc1\n\tv_mfma_f32_16x16x32_bf16 a[16:19], a[144:147], v[164:167], a[16:19]\n\tv_mfma_f32_16x16x32_bf16 a[4:7], a[148:151], v[156:159], a[4:7]\n\tv_mfma_f32_16x16x32_bf16 a[12:15], a[148:151], v[160:163], a[12:15]\n\tbuffer_load_dwordx4 a[52:55], v8, s[8:11], s68 offen offset:2048 sc1\n\ts_cmp_ge_u32 s43, 1\n\t" \
    "s_cselect_b32 s68, s70, s43\n\ts_mul_i32 s68, s68, 36864\n\tv_mfma_f32_16x16x32_bf16 a[20:23], a[148:151], v[164:167], a[20:23]\n\ts_add_u32 s68, s68, s44\n\ts_add_u32 s68, s68, 405504\n\tv_mfma_f32_16x16x32_bf16 v[48:51], a[152:155], v[156:159], v[48:51]\n\tv_mfma_f32_16x16x32_bf16 v[56:59], a[152:155], v[160:163], v[56:59]\n\tbuffer_load_dwordx4 v[144:147], v8, s[8:11], s68 offen offset:0 sc1\n\t" \
    "v_mfma_f32_16x16x32_bf16 v[64:67], a[152:155], v[164:167], v[64:67]\n\tv_mfma_f32_16x16x32_bf16 v[52:55], a[156:159], v[156:159], v[52:55]\n\tds_read_b64_tr_b16 v[16:17], v0 offset:32768\n\tds_read_b64_tr_b16 v[18:19], v0 offset:33280\n\tv_mfma_f32_16x16x32_bf16 v[60:63], a[156:159], v[160:163], v[60:63]\n\tbuffer_load_dwordx4 v[148:151], v8, s[8:11], s68 offen offset:1024 sc1\n\tv_mfma_f32_16x16x32_bf16 v[68:71], a[156:159], v[164:167], v[68:71]\n\tv_mfma_f32_16x16x32_bf16 a[24:27], a[160:163], v[156:159], a[24:27]\n\t" \
    "ds_read_b64_tr_b16 v[20:21], v1 offset:32768\n\tds_read_b64_tr_b16 v[22:23], v1 offset:33280\n\tv_mfma_f32_16x16x32_bf16 a[32:35], a[160:163], v[160:163], a[32:35]\n\tv_mfma_f32_16x16x32_bf16 a[40:43], a[160:163], v[164:167], a[40:43]\n\tv_mfma_f32_16x16x32_bf16 a[28:31], a[164:167], v[156:159], a[28:31]\n\tds_read_b64_tr_b16 v[24:25], v2 offset:32768\n\tds_read_b64_tr_b16 v[26:27], v2 offset:33280\n\tv_mfma_f32_16x16x32_bf16 a[36:39], a[164:167], v[160:163], a[36:39]\n\t" \
    "buffer_load_dwordx4 v[152:155], v8, s[8:11], s68 offen offset:2048 sc1\n\ts_add_u32 s68, s44, 442368\n\tv_mfma_f32_16x16x32_bf16 a[44:47], a[164:167], v[164:167], a[44:47]\n\tv_mfma_f32_16x16x32_bf16 v[72:75], a[168:171], v[156:159], v[72:75]\n\tds_read_b64_tr_b16 v[28:29], v3 offset:32768\n\tds_read_b64_tr_b16 v[30:31], v3 offset:33280\n\tv_mfma_f32_16x16x32_bf16 v[80:83], a[168:171], v[160:163], v[80:83]\n\tbuffer_load_dwordx4 a[56:59], v8, s[8:11], s68 offen offset:0 sc1\n\t" \
    "v_mfma_f32_16x16x32_bf16 v[88:91], a[168:171], v[164:167], v[88:91]\n\tv_mfma_f32_16x16x32_bf16 v[76:79], a[172:175], v[156:159], v[76:79]\n\tds_read_b64_tr_b16 v[32:33], v0 offset:36864\n\tds_read_b64_tr_b16 v[34:35], v0 offset:37376\n\tv_mfma_f32_16x16x32_bf16 v[84:87], a[172:175], v[160:163], v[84:87]\n\tbuffer_load_dwordx4 a[60:63], v8, s[8:11], s68 offen offset:1024 sc1\n\tv_mfma_f32_16x16x32_bf16 v[92:95], a[172:175], v[164:167], v[92:95]\n\ts_barrier\n\t" \
    "s_waitcnt vmcnt(23) lgkmcnt(8)\n\tv_mfma_f32_16x16x32_bf16 a[0:3], v[16:19], v[168:171], a[0:3]\n\tds_read_b64_tr_b16 v[36:37], v1 offset:36864\n\tds_read_b64_tr_b16 v[38:39], v1 offset:37376\n\tv_mfma_f32_16x16x32_bf16 a[8:11], v[16:19], v[172:175], a[8:11]\n\tbuffer_load_dwordx4 a[64:67], v8, s[8:11], s68 offen offset:2048 sc1\n\ts_add_u32 s68, s44, 479232\n\tv_mfma_f32_16x16x32_bf16 a[16:19], v[16:19], v[176:179], a[16:19]\n\t" \
    "s_waitcnt lgkmcnt(8)\n\tv_mfma_f32_16x16x32_bf16 a[4:7], v[20:23], v[168:171], a[4:7]\n\tds_read_b64_tr_b16 v[40:41], v2 offset:36864\n\tds_read_b64_tr_b16 v[42:43], v2 offset:37376\n\tv_mfma_f32_16x16x32_bf16 a[12:15], v[20:23], v[172:175], a[12:15]\n\tbuffer_load_dwordx4 v[156:159], v8, s[8:11], s68 offen offset:0 sc1\n\tv_mfma_f32_16x16x32_bf16 a[20:23], v[20:23], v[176:179], a[20:23]\n\ts_waitcnt lgkmcnt(8)\n\t" \
    "v_mfma_f32_16x16x32_bf16 v[48:51], v[24:27], v[168:171], v[48:51]\n\tds_read_b64_tr_b16 v[44:45], v3 offset:36864\n\tds_read_b64_tr_b16 v[46:47], v3 offset:37376\n\tv_mfma_f32_16x16x32_bf16 v[56:59], v[24:27], v[172:175], v[56:59]\n\tv_mfma_f32_16x16x32_bf16 v[64:67], v[24:27], v[176:179], v[64:67]\n\ts_waitcnt lgkmcnt(8)\n\tv_mfma_f32_16x16x32_bf16 v[52:55], v[28:31], v[168:171], v[52:55]\n\tds_read_b64_tr_b16 v[16:17], v0 offset:40960\n\t" \
    "ds_read_b64_tr_b16 v[18:19], v0 offset:41472\n\tv_mfma_f32_16x16x32_bf16 v[60:63], v[28:31], v[172:175], v[60:63]\n\tbuffer_load_dwordx4 v[160:163], v8, s[8:11], s68 offen offset:1024 sc1\n\tv_mfma_f32_16x16x32_bf16 v[68:71], v[28:31], v[176:179], v[68:71]\n\ts_waitcnt lgkmcnt(8)\n\tv_mfma_f32_16x16x32_bf16 a[24:27], v[32:35], v[168:171], a[24:27]\n\tds_read_b64_tr_b16 v[20:21], v1 offset:40960\n\tds_read_b64_tr_b16 v[22:23], v1 offset:41472\n\t" \
    "v_mfma_f32_16x16x32_bf16 a[32:35], v[32:35], v[172:175], a[32:35]\n\tbuffer_load_dwordx4 v[164:167], v8, s[8:11], s68 offen offset:2048 sc1\n\ts_add_u32 s68, s44, 516096\n\tv_mfma_f32_16x16x32_bf16 a[40:43], v[32:35], v[176:179], a[40:43]\n\ts_waitcnt lgkmcnt(8)\n\tv_mfma_f32_16x16x32_bf16 a[28:31], v[36:39], v[168:171], a[28:31]\n\tds_read_b64_tr_b16 v[24:25], v2 offset:40960\n\tds_read_b64_tr_b16 v[26:27], v2 offset:41472\n\t" \
    "v_mfma_f32_16x16x32_bf16 a[36:39], v[36:39], v[172:175], a[36:39]\n\tv_mfma_f32_16x16x32_bf16 a[44:47], v[36:39], v[176:179], a[44:47]\n\ts_waitcnt lgkmcnt(8)\n\tv_mfma_f32_16x16x32_bf16 v[72:75], v[40:43], v[168:171], v[72:75]\n\tds_read_b64_tr_b16 v[28:29], v3 offset:40960\n\tds_read_b64_tr_b16 v[30:31], v3 offset:41472\n\tv_mfma_f32_16x16x32_bf16 v[80:83], v[40:43], v[172:175], v[80:83]\n\tbuffer_load_dwordx4 a[68:71], v8, s[8:11], s68 offen offset:0 sc1\n\t" \
    "v_mfma_f32_16x16x32_bf16 v[88:91], v[40:43], v[176:179], v[88:91]\n\ts_waitcnt lgkmcnt(8)\n\tv_mfma_f32_16x16x32_bf16 v[76:79], v[44:47], v[168:171], v[76:79]\n\tds_read_b64_tr_b16 v[32:33], v0 offset:45056\n\tds_read_b64_tr_b16 v[34:35], v0 offset:45568\n\tv_mfma_f32_16x16x32_bf16 v[84:87], v[44:47], v[172:175], v[84:87]\n\tbuffer_load_dwordx4 a[72:75], v8, s[8:11], s68 offen offset:1024 sc1\n\tv_mfma_f32_16x16x32_bf16 v[92:95], v[44:47], v[176:179], v[92:95]\n\t" \
    "s_barrier\n\ts_waitcnt vmcnt(26) lgkmcnt(8)\n\tv_mfma_f32_16x16x32_bf16 a[0:3], v[16:19], v[180:183], a[0:3]\n\tds_read_b64_tr_b16 v[36:37], v1 offset:45056\n\tds_read_b64_tr_b16 v[38:39], v1 offset:45568\n\tv_mfma_f32_16x16x32_bf16 a[8:11], v[16:19], v[184:187], a[8:11]\n\tbuffer_load_dwordx4 a[76:79], v8, s[8:11], s68 offen offset:2048 sc1\n\ts_mov_b32 s69, 3538944\n\t" \
    "s_add_u32 m0, s72, 32768\n\tv_mfma_f32_16x16x32_bf16 a[16:19], v[16:19], v[188:191], a[16:19]\n\ts_waitcnt lgkmcnt(8)\n\tv_mfma_f32_16x16x32_bf16 a[4:7], v[20:23], v[180:183], a[4:7]\n\tds_read_b64_tr_b16 v[40:41], v2 offset:45056\n\tds_read_b64_tr_b16 v[42:43], v2 offset:45568\n\tv_mfma_f32_16x16x32_bf16 a[12:15], v[20:23], v[184:187], a[12:15]\n\tbuffer_load_dwordx4 v10, s[12:15], s69 offen lds\n\t" \
    "s_add_u32 m0, s72, 33792\n\tv_mfma_f32_16x16x32_bf16 a[20:23], v[20:23], v[188:191], a[20:23]\n\ts_waitcnt lgkmcnt(8)\n\tv_mfma_f32_16x16x32_bf16 v[48:51], v[24:27], v[180:183], v[48:51]\n\tds_read_b64_tr_b16 v[44:45], v3 offset:45056\n\tds_read_b64_tr_b16 v[46:47], v3 offset:45568\n\tv_mfma_f32_16x16x32_bf16 v[56:59], v[24:27], v[184:187], v[56:59]\n\tbuffer_load_dwordx4 v11, s[12:15], s69 offen lds\n\t" \
    "s_mov_b32 s69, 3539072\n\ts_add_u32 m0, s72, 36864\n\tv_mfma_f32_16x16x32_bf16 v[64:67], v[24:27], v[188:191], v[64:67]\n\ts_waitcnt lgkmcnt(8)\n\tv_mfma_f32_16x16x32_bf16 v[52:55], v[28:31], v[180:183], v[52:55]\n\tds_read_b64_tr_b16 v[16:17], v0 offset:49152\n\tds_read_b64_tr_b16 v[18:19], v0 offset:49664\n\tv_mfma_f32_16x16x32_bf16 v[60:63], v[28:31], v[184:187], v[60:63]\n\t" \
    "v_mfma_f32_16x16x32_bf16 v[68:71], v[28:31], v[188:191], v[68:71]\n\ts_waitcnt lgkmcnt(8)\n\tv_mfma_f32_16x16x32_bf16 a[24:27], v[32:35], v[180:183], a[24:27]\n\tds_read_b64_tr_b16 v[20:21], v1 offset:49152\n\tds_read_b64_tr_b16 v[22:23], v1 offset:49664\n\tv_mfma_f32_16x16x32_bf16 a[32:35], v[32:35], v[184:187], a[32:35]\n\tbuffer_load_dwordx4 v10, s[12:15], s69 offen lds\n\ts_add_u32 m0, s72, 37888\n\t" \
    "v_mfma_f32_16x16x32_bf16 a[40:43], v[32:35], v[188:191], a[40:43]\n\ts_waitcnt lgkmcnt(8)\n\tv_mfma_f32_16x16x32_bf16 a[28:31], v[36:39], v[180:183], a[28:31]\n\tds_read_b64_tr_b16 v[24:25], v2 offset:49152\n\tds_read_b64_tr_b16 v[26:27], v2 offset:49664\n\tv_mfma_f32_16x16x32_bf16 a[36:39], v[36:39], v[184:187], a[36:39]\n\tbuffer_load_dwordx4 v11, s[12:15], s69 offen lds\n\ts_add_u32 s68, s44, 552960\n\t" \
    "v_mfma_f32_16x16x32_bf16 a[44:47], v[36:39], v[188:191], a[44:47]\n\ts_waitcnt lgkmcnt(8)\n\tv_mfma_f32_16x16x32_bf16 v[72:75], v[40:43], v[180:183], v[72:75]\n\tds_read_b64_tr_b16 v[28:29], v3 offset:49152\n\tds_read_b64_tr_b16 v[30:31], v3 offset:49664\n\tv_mfma_f32_16x16x32_bf16 v[80:83], v[40:43], v[184:187], v[80:83]\n\tv_mfma_f32_16x16x32_bf16 v[88:91], v[40:43], v[188:191], v[88:91]\n\ts_waitcnt lgkmcnt(8)\n\t" \
    "v_mfma_f32_16x16x32_bf16 v[76:79], v[44:47], v[180:183], v[76:79]\n\tds_read_b64_tr_b16 v[32:33], v0 offset:53248\n\tds_read_b64_tr_b16 v[34:35], v0 offset:53760\n\tv_mfma_f32_16x16x32_bf16 v[84:87], v[44:47], v[184:187], v[84:87]\n\tbuffer_load_dwordx4 v[168:171], v8, s[8:11], s68 offen offset:0 sc1\n\tv_mfma_f32_16x16x32_bf16 v[92:95], v[44:47], v[188:191], v[92:95]\n\ts_barrier\n\ts_waitcnt vmcnt(29) lgkmcnt(8)\n\t" \
    "v_mfma_f32_16x16x32_bf16 a[0:3], v[16:19], v[192:195], a[0:3]\n\tds_read_b64_tr_b16 v[36:37], v1 offset:53248\n\tds_read_b64_tr_b16 v[38:39], v1 offset:53760\n\tv_mfma_f32_16x16x32_bf16 a[8:11], v[16:19], v[196:199], a[8:11]\n\tbuffer_load_dwordx4 v[172:175], v8, s[8:11], s68 offen offset:1024 sc1\n\tv_mfma_f32_16x16x32_bf16 a[16:19], v[16:19], v[200:203], a[16:19]\n\ts_waitcnt lgkmcnt(8)\n\tv_mfma_f32_16x16x32_bf16 a[4:7], v[20:23], v[192:195], a[4:7]\n\t" \
    "ds_read_b64_tr_b16 v[40:41], v2 offset:53248\n\tds_read_b64_tr_b16 v[42:43], v2 offset:53760\n\tv_mfma_f32_16x16x32_bf16 a[12:15], v[20:23], v[196:199], a[12:15]\n\tbuffer_load_dwordx4 v[176:179], v8, s[8:11], s68 offen offset:2048 sc1\n\ts_mov_b32 s69, 3833856\n\ts_add_u32 m0, s72, 40960\n\tv_mfma_f32_16x16x32_bf16 a[20:23], v[20:23], v[200:203], a[20:23]\n\ts_waitcnt lgkmcnt(8)\n\t" \
    "v_mfma_f32_16x16x32_bf16 v[48:51], v[24:27], v[192:195], v[48:51]\n\tds_read_b64_tr_b16 v[44:45], v3 offset:53248\n\tds_read_b64_tr_b16 v[46:47], v3 offset:53760\n\tv_mfma_f32_16x16x32_bf16 v[56:59], v[24:27], v[196:199], v[56:59]\n\tbuffer_load_dwordx4 v10, s[12:15], s69 offen lds\n\ts_add_u32 m0, s72, 41984\n\tv_mfma_f32_16x16x32_bf16 v[64:67], v[24:27], v[200:203], v[64:67]\n\ts_waitcnt lgkmcnt(8)\n\t" \
    "v_mfma_f32_16x16x32_bf16 v[52:55], v[28:31], v[192:195], v[52:55]\n\tds_read_b64_tr_b16 v[16:17], v0 offset:57344\n\tds_read_b64_tr_b16 v[18:19], v0 offset:57856\n\tv_mfma_f32_16x16x32_bf16 v[60:63], v[28:31], v[196:199], v[60:63]\n\tv_mfma_f32_16x16x32_bf16 v[68:71], v[28:31], v[200:203], v[68:71]\n\ts_waitcnt lgkmcnt(8)\n\tv_mfma_f32_16x16x32_bf16 a[24:27], v[32:35], v[192:195], a[24:27]\n\tds_read_b64_tr_b16 v[20:21], v1 offset:57344\n\t" \
    "ds_read_b64_tr_b16 v[22:23], v1 offset:57856\n\tv_mfma_f32_16x16x32_bf16 a[32:35], v[32:35], v[196:199], a[32:35]\n\tbuffer_load_dwordx4 v11, s[12:15], s69 offen lds\n\ts_mov_b32 s69, 3833984\n\ts_add_u32 m0, s72, 45056\n\tv_mfma_f32_16x16x32_bf16 a[40:43], v[32:35], v[200:203], a[40:43]\n\ts_waitcnt lgkmcnt(8)\n\tv_mfma_f32_16x16x32_bf16 a[28:31], v[36:39], v[192:195], a[28:31]\n\t" \
    "ds_read_b64_tr_b16 v[24:25], v2 offset:57344\n\tds_read_b64_tr_b16 v[26:27], v2 offset:57856\n\tv_mfma_f32_16x16x32_bf16 a[36:39], v[36:39], v[196:199], a[36:39]\n\tbuffer_load_dwordx4 v10, s[12:15], s69 offen lds\n\ts_add_u32 m0, s72, 46080\n\tv_mfma_f32_16x16x32_bf16 a[44:47], v[36:39], v[200:203], a[44:47]\n\ts_waitcnt lgkmcnt(8)\n\tv_mfma_f32_16x16x32_bf16 v[72:75], v[40:43], v[192:195], v[72:75]\n\t" \
    "ds_read_b64_tr_b16 v[28:29], v3 offset:57344\n\tds_read_b64_tr_b16 v[30:31], v3 offset:57856\n\tv_mfma_f32_16x16x32_bf16 v[80:83], v[40:43], v[196:199], v[80:83]\n\tbuffer_load_dwordx4 v11, s[12:15], s69 offen lds\n\ts_add_u32 s68, s44, 589824\n\tv_mfma_f32_16x16x32_bf16 v[88:91], v[40:43], v[200:203], v[88:91]\n\ts_waitcnt lgkmcnt(8)\n\tv_mfma_f32_16x16x32_bf16 v[76:79], v[44:47], v[192:195], v[76:79]\n\t" \
    "ds_read_b64_tr_b16 v[32:33], v0 offset:61440\n\tds_read_b64_tr_b16 v[34:35], v0 offset:61952\n\tv_mfma_f32_16x16x32_bf16 v[84:87], v[44:47], v[196:199], v[84:87]\n\tv_mfma_f32_16x16x32_bf16 v[92:95], v[44:47], v[200:203], v[92:95]\n\ts_barrier\n\ts_waitcnt vmcnt(32) lgkmcnt(8)\n\tv_mfma_f32_16x16x32_bf16 a[0:3], v[16:19], v[120:123], a[0:3]\n\tds_read_b64_tr_b16 v[36:37], v1 offset:61440\n\t" \
    "ds_read_b64_tr_b16 v[38:39], v1 offset:61952\n\tv_mfma_f32_16x16x32_bf16 a[8:11], v[16:19], v[124:127], a[8:11]\n\tbuffer_load_dwordx4 v[180:183], v8, s[8:11], s68 offen offset:0 sc1\n\tv_mfma_f32_16x16x32_bf16 a[16:19], v[16:19], v[128:131], a[16:19]\n\ts_waitcnt lgkmcnt(8)\n\tv_mfma_f32_16x16x32_bf16 a[4:7], v[20:23], v[120:123], a[4:7]\n\tds_read_b64_tr_b16 v[40:41], v2 offset:61440\n\tds_read_b64_tr_b16 v[42:43], v2 offset:61952\n\t" \
    "v_mfma_f32_16x16x32_bf16 a[12:15], v[20:23], v[124:127], a[12:15]\n\tbuffer_load_dwordx4 v[184:187], v8, s[8:11], s68 offen offset:1024 sc1\n\tv_mfma_f32_16x16x32_bf16 a[20:23], v[20:23], v[128:131], a[20:23]\n\ts_waitcnt lgkmcnt(8)\n\tv_mfma_f32_16x16x32_bf16 v[48:51], v[24:27], v[120:123], v[48:51]\n\tds_read_b64_tr_b16 v[44:45], v3 offset:61440\n\tds_read_b64_tr_b16 v[46:47], v3 offset:61952\n\tv_mfma_f32_16x16x32_bf16 v[56:59], v[24:27], v[124:127], v[56:59]\n\t" \
    "buffer_load_dwordx4 v[188:191], v8, s[8:11], s68 offen offset:2048 sc1\n\ts_mov_b32 s69, 4128768\n\ts_add_u32 m0, s72, 49152\n\tv_mfma_f32_16x16x32_bf16 v[64:67], v[24:27], v[128:131], v[64:67]\n\ts_waitcnt lgkmcnt(8)\n\tv_mfma_f32_16x16x32_bf16 v[52:55], v[28:31], v[120:123], v[52:55]\n\tds_read_b64_tr_b16 v[16:17], v0 offset:0\n\tds_read_b64_tr_b16 v[18:19], v0 offset:512\n\t" \
    "v_mfma_f32_16x16x32_bf16 v[60:63], v[28:31], v[124:127], v[60:63]\n\tbuffer_load_dwordx4 v10, s[12:15], s69 offen lds\n\ts_add_u32 m0, s72, 50176\n\tv_mfma_f32_16x16x32_bf16 v[68:71], v[28:31], v[128:131], v[68:71]\n\ts_waitcnt lgkmcnt(8)\n\tv_mfma_f32_16x16x32_bf16 a[24:27], v[32:35], v[120:123], a[24:27]\n\tds_read_b64_tr_b16 v[20:21], v1 offset:0\n\tds_read_b64_tr_b16 v[22:23], v1 offset:512\n\t" \
    "v_mfma_f32_16x16x32_bf16 a[32:35], v[32:35], v[124:127], a[32:35]\n\tv_mfma_f32_16x16x32_bf16 a[40:43], v[32:35], v[128:131], a[40:43]\n\ts_waitcnt lgkmcnt(8)\n\tv_mfma_f32_16x16x32_bf16 a[28:31], v[36:39], v[120:123], a[28:31]\n\tds_read_b64_tr_b16 v[24:25], v2 offset:0\n\tds_read_b64_tr_b16 v[26:27], v2 offset:512\n\tv_mfma_f32_16x16x32_bf16 a[36:39], v[36:39], v[124:127], a[36:39]\n\tbuffer_load_dwordx4 v11, s[12:15], s69 offen lds\n\t" \
    "s_mov_b32 s69, 4128896\n\ts_add_u32 m0, s72, 53248\n\tv_mfma_f32_16x16x32_bf16 a[44:47], v[36:39], v[128:131], a[44:47]\n\ts_waitcnt lgkmcnt(8)\n\tv_mfma_f32_16x16x32_bf16 v[72:75], v[40:43], v[120:123], v[72:75]\n\tds_read_b64_tr_b16 v[28:29], v3 offset:0\n\tds_read_b64_tr_b16 v[30:31], v3 offset:512\n\tv_mfma_f32_16x16x32_bf16 v[80:83], v[40:43], v[124:127], v[80:83]\n\t" \
    "buffer_load_dwordx4 v10, s[12:15], s69 offen lds\n\ts_add_u32 m0, s72, 54272\n\tv_mfma_f32_16x16x32_bf16 v[88:91], v[40:43], v[128:131], v[88:91]\n\ts_waitcnt lgkmcnt(8)\n\tv_mfma_f32_16x16x32_bf16 v[76:79], v[44:47], v[120:123], v[76:79]\n\tds_read_b64_tr_b16 v[32:33], v0 offset:4096\n\tds_read_b64_tr_b16 v[34:35], v0 offset:4608\n\tv_mfma_f32_16x16x32_bf16 v[84:87], v[44:47], v[124:127], v[84:87]\n\t" \
    "buffer_load_dwordx4 v11, s[12:15], s69 offen lds\n\ts_add_u32 s68, s44, 626688\n\tv_mfma_f32_16x16x32_bf16 v[92:95], v[44:47], v[128:131], v[92:95]\n\ts_barrier\n\ts_waitcnt vmcnt(36) lgkmcnt(8)\n\tv_mfma_f32_16x16x32_bf16 a[0:3], v[16:19], v[204:207], a[0:3]\n\tds_read_b64_tr_b16 v[36:37], v1 offset:4096\n\tds_read_b64_tr_b16 v[38:39], v1 offset:4608\n\t" \
    "v_mfma_f32_16x16x32_bf16 a[8:11], v[16:19], v[208:211], a[8:11]\n\tbuffer_load_dwordx4 v[192:195], v8, s[8:11], s68 offen offset:0 sc1\n\tv_mfma_f32_16x16x32_bf16 a[16:19], v[16:19], v[212:215], a[16:19]\n\ts_waitcnt lgkmcnt(8)\n\tv_mfma_f32_16x16x32_bf16 a[4:7], v[20:23], v[204:207], a[4:7]\n\tds_read_b64_tr_b16 v[40:41], v2 offset:4096\n\tds_read_b64_tr_b16 v[42:43], v2 offset:4608\n\tv_mfma_f32_16x16x32_bf16 a[12:15], v[20:23], v[208:211], a[12:15]\n\t" \
    "buffer_load_dwordx4 v[196:199], v8, s[8:11], s68 offen offset:1024 sc1\n\tv_mfma_f32_16x16x32_bf16 a[20:23], v[20:23], v[212:215], a[20:23]\n\ts_waitcnt lgkmcnt(8)\n\tv_mfma_f32_16x16x32_bf16 v[48:51], v[24:27], v[204:207], v[48:51]\n\tds_read_b64_tr_b16 v[44:45], v3 offset:4096\n\tds_read_b64_tr_b16 v[46:47], v3 offset:4608\n\tv_mfma_f32_16x16x32_bf16 v[56:59], v[24:27], v[208:211], v[56:59]\n\tv_mfma_f32_16x16x32_bf16 v[64:67], v[24:27], v[212:215], v[64:67]\n\t" \
    "s_waitcnt lgkmcnt(8)\n\tv_mfma_f32_16x16x32_bf16 v[52:55], v[28:31], v[204:207], v[52:55]\n\tds_read_b64_tr_b16 v[16:17], v0 offset:8192\n\tds_read_b64_tr_b16 v[18:19], v0 offset:8704\n\tv_mfma_f32_16x16x32_bf16 v[60:63], v[28:31], v[208:211], v[60:63]\n\tbuffer_load_dwordx4 v[200:203], v8, s[8:11], s68 offen offset:2048 sc1\n\ts_mov_b32 s69, 4423680\n\ts_add_u32 m0, s72, 57344\n\t" \
    "v_mfma_f32_16x16x32_bf16 v[68:71], v[28:31], v[212:215], v[68:71]\n\ts_waitcnt lgkmcnt(8)\n\tv_mfma_f32_16x16x32_bf16 a[24:27], v[32:35], v[204:207], a[24:27]\n\tds_read_b64_tr_b16 v[20:21], v1 offset:8192\n\tds_read_b64_tr_b16 v[22:23], v1 offset:8704\n\tv_mfma_f32_16x16x32_bf16 a[32:35], v[32:35], v[208:211], a[32:35]\n\tbuffer_load_dwordx4 v10, s[12:15], s69 offen lds\n\ts_add_u32 m0, s72, 58368\n\t" \
    "v_mfma_f32_16x16x32_bf16 a[40:43], v[32:35], v[212:215], a[40:43]\n\ts_waitcnt lgkmcnt(8)\n\tv_mfma_f32_16x16x32_bf16 a[28:31], v[36:39], v[204:207], a[28:31]\n\tds_read_b64_tr_b16 v[24:25], v2 offset:8192\n\tds_read_b64_tr_b16 v[26:27], v2 offset:8704\n\tv_mfma_f32_16x16x32_bf16 a[36:39], v[36:39], v[208:211], a[36:39]\n\tv_mfma_f32_16x16x32_bf16 a[44:47], v[36:39], v[212:215], a[44:47]\n\ts_waitcnt lgkmcnt(8)\n\t" \
    "v_mfma_f32_16x16x32_bf16 v[72:75], v[40:43], v[204:207], v[72:75]\n\tds_read_b64_tr_b16 v[28:29], v3 offset:8192\n\tds_read_b64_tr_b16 v[30:31], v3 offset:8704\n\tv_mfma_f32_16x16x32_bf16 v[80:83], v[40:43], v[208:211], v[80:83]\n\tbuffer_load_dwordx4 v11, s[12:15], s69 offen lds\n\ts_mov_b32 s69, 4423808\n\ts_add_u32 m0, s72, 61440\n\tv_mfma_f32_16x16x32_bf16 v[88:91], v[40:43], v[212:215], v[88:91]\n\t" \
    "s_waitcnt lgkmcnt(8)\n\tv_mfma_f32_16x16x32_bf16 v[76:79], v[44:47], v[204:207], v[76:79]\n\tds_read_b64_tr_b16 v[32:33], v0 offset:12288\n\tds_read_b64_tr_b16 v[34:35], v0 offset:12800\n\tv_mfma_f32_16x16x32_bf16 v[84:87], v[44:47], v[208:211], v[84:87]\n\tbuffer_load_dwordx4 v10, s[12:15], s69 offen lds\n\ts_add_u32 m0, s72, 62464\n\tv_mfma_f32_16x16x32_bf16 v[92:95], v[44:47], v[212:215], v[92:95]\n\t" \
    "s_barrier\n\ts_waitcnt vmcnt(39) lgkmcnt(8)\n\tv_mfma_f32_16x16x32_bf16 a[0:3], v[16:19], v[132:135], a[0:3]\n\tds_read_b64_tr_b16 v[36:37], v1 offset:12288\n\tds_read_b64_tr_b16 v[38:39], v1 offset:12800\n\tv_mfma_f32_16x16x32_bf16 a[8:11], v[16:19], v[136:139], a[8:11]\n\tbuffer_load_dwordx4 v11, s[12:15], s69 offen lds\n\ts_mov_b32 s69, 4718592\n\t" \
    "s_add_u32 m0, s72, 0\n\tv_mfma_f32_16x16x32_bf16 a[16:19], v[16:19], v[140:143], a[16:19]\n\ts_waitcnt lgkmcnt(8)\n\tv_mfma_f32_16x16x32_bf16 a[4:7], v[20:23], v[132:135], a[4:7]\n\tds_read_b64_tr_b16 v[40:41], v2 offset:12288\n\tds_read_b64_tr_b16 v[42:43], v2 offset:12800\n\tv_mfma_f32_16x16x32_bf16 a[12:15], v[20:23], v[136:139], a[12:15]\n\tbuffer_load_dwordx4 v10, s[12:15], s69 offen lds\n\t" \
    "s_add_u32 m0, s72, 1024\n\tv_mfma_f32_16x16x32_bf16 a[20:23], v[20:23], v[140:143], a[20:23]\n\ts_waitcnt lgkmcnt(8)\n\tv_mfma_f32_16x16x32_bf16 v[48:51], v[24:27], v[132:135], v[48:51]\n\tds_read_b64_tr_b16 v[44:45], v3 offset:12288\n\tds_read_b64_tr_b16 v[46:47], v3 offset:12800\n\tv_mfma_f32_16x16x32_bf16 v[56:59], v[24:27], v[136:139], v[56:59]\n\tv_mfma_f32_16x16x32_bf16 v[64:67], v[24:27], v[140:143], v[64:67]\n\t" \
    "s_waitcnt lgkmcnt(8)\n\tv_mfma_f32_16x16x32_bf16 v[52:55], v[28:31], v[132:135], v[52:55]\n\tds_read_b64_tr_b16 v[16:17], v0 offset:16384\n\tds_read_b64_tr_b16 v[18:19], v0 offset:16896\n\tv_mfma_f32_16x16x32_bf16 v[60:63], v[28:31], v[136:139], v[60:63]\n\tbuffer_load_dwordx4 v11, s[12:15], s69 offen lds\n\ts_mov_b32 s69, 5013504\n\ts_add_u32 m0, s72, 4096\n\t" \
    "v_mfma_f32_16x16x32_bf16 v[68:71], v[28:31], v[140:143], v[68:71]\n\ts_waitcnt lgkmcnt(8)\n\tv_mfma_f32_16x16x32_bf16 a[24:27], v[32:35], v[132:135], a[24:27]\n\tds_read_b64_tr_b16 v[20:21], v1 offset:16384\n\tds_read_b64_tr_b16 v[22:23], v1 offset:16896\n\tv_mfma_f32_16x16x32_bf16 a[32:35], v[32:35], v[136:139], a[32:35]\n\tbuffer_load_dwordx4 v10, s[12:15], s69 offen lds\n\ts_add_u32 m0, s72, 5120\n\t" \
    "v_mfma_f32_16x16x32_bf16 a[40:43], v[32:35], v[140:143], a[40:43]\n\ts_waitcnt lgkmcnt(8)\n\tv_mfma_f32_16x16x32_bf16 a[28:31], v[36:39], v[132:135], a[28:31]\n\tds_read_b64_tr_b16 v[24:25], v2 offset:16384\n\tds_read_b64_tr_b16 v[26:27], v2 offset:16896\n\tv_mfma_f32_16x16x32_bf16 a[36:39], v[36:39], v[136:139], a[36:39]\n\tbuffer_load_dwordx4 v11, s[12:15], s69 offen lds\n\ts_add_u32 s68, s44, 663552\n\t" \
    "v_mfma_f32_16x16x32_bf16 a[44:47], v[36:39], v[140:143], a[44:47]\n\ts_waitcnt lgkmcnt(8)\n\tv_mfma_f32_16x16x32_bf16 v[72:75], v[40:43], v[132:135], v[72:75]\n\tds_read_b64_tr_b16 v[28:29], v3 offset:16384\n\tds_read_b64_tr_b16 v[30:31], v3 offset:16896\n\tv_mfma_f32_16x16x32_bf16 v[80:83], v[40:43], v[136:139], v[80:83]\n\tv_mfma_f32_16x16x32_bf16 v[88:91], v[40:43], v[140:143], v[88:91]\n\ts_waitcnt lgkmcnt(8)\n\t" \
    "v_mfma_f32_16x16x32_bf16 v[76:79], v[44:47], v[132:135], v[76:79]\n\tds_read_b64_tr_b16 v[32:33], v0 offset:20480\n\tds_read_b64_tr_b16 v[34:35], v0 offset:20992\n\tv_mfma_f32_16x16x32_bf16 v[84:87], v[44:47], v[136:139], v[84:87]\n\tbuffer_load_dwordx4 v[120:123], v8, s[8:11], s68 offen offset:0 sc1\n\tv_mfma_f32_16x16x32_bf16 v[92:95], v[44:47], v[140:143], v[92:95]\n\ts_barrier\n\ts_waitcnt vmcnt(42) lgkmcnt(8)\n\t" \
    "v_mfma_f32_16x16x32_bf16 a[0:3], v[16:19], v[216:219], a[0:3]\n\tds_read_b64_tr_b16 v[36:37], v1 offset:20480\n\tds_read_b64_tr_b16 v[38:39], v1 offset:20992\n\tv_mfma_f32_16x16x32_bf16 a[8:11], v[16:19], a[48:51], a[8:11]\n\tbuffer_load_dwordx4 v[124:127], v8, s[8:11], s68 offen offset:1024 sc1\n\tv_mfma_f32_16x16x32_bf16 a[16:19], v[16:19], a[52:55], a[16:19]\n\ts_waitcnt lgkmcnt(8)\n\tv_mfma_f32_16x16x32_bf16 a[4:7], v[20:23], v[216:219], a[4:7]\n\t" \
    "ds_read_b64_tr_b16 v[40:41], v2 offset:20480\n\tds_read_b64_tr_b16 v[42:43], v2 offset:20992\n\tv_mfma_f32_16x16x32_bf16 a[12:15], v[20:23], a[48:51], a[12:15]\n\tbuffer_load_dwordx4 v[128:131], v8, s[8:11], s68 offen offset:2048 sc1\n\ts_mov_b32 s69, 5308416\n\ts_add_u32 m0, s72, 8192\n\tv_mfma_f32_16x16x32_bf16 a[20:23], v[20:23], a[52:55], a[20:23]\n\ts_waitcnt lgkmcnt(8)\n\t" \
    "v_mfma_f32_16x16x32_bf16 v[48:51], v[24:27], v[216:219], v[48:51]\n\tds_read_b64_tr_b16 v[44:45], v3 offset:20480\n\tds_read_b64_tr_b16 v[46:47], v3 offset:20992\n\tv_mfma_f32_16x16x32_bf16 v[56:59], v[24:27], a[48:51], v[56:59]\n\tbuffer_load_dwordx4 v10, s[12:15], s69 offen lds\n\ts_add_u32 m0, s72, 9216\n\tv_mfma_f32_16x16x32_bf16 v[64:67], v[24:27], a[52:55], v[64:67]\n\ts_waitcnt lgkmcnt(8)\n\t" \
    "v_mfma_f32_16x16x32_bf16 v[52:55], v[28:31], v[216:219], v[52:55]\n\tds_read_b64_tr_b16 v[16:17], v0 offset:24576\n\tds_read_b64_tr_b16 v[18:19], v0 offset:25088\n\tv_mfma_f32_16x16x32_bf16 v[60:63], v[28:31], a[48:51], v[60:63]\n\tv_mfma_f32_16x16x32_bf16 v[68:71], v[28:31], a[52:55], v[68:71]\n\ts_waitcnt lgkmcnt(8)\n\tv_mfma_f32_16x16x32_bf16 a[24:27], v[32:35], v[216:219], a[24:27]\n\tds_read_b64_tr_b16 v[20:21], v1 offset:24576\n\t" \
    "ds_read_b64_tr_b16 v[22:23], v1 offset:25088\n\tv_mfma_f32_16x16x32_bf16 a[32:35], v[32:35], a[48:51], a[32:35]\n\tbuffer_load_dwordx4 v11, s[12:15], s69 offen lds\n\ts_mov_b32 s69, 5603328\n\ts_add_u32 m0, s72, 12288\n\tv_mfma_f32_16x16x32_bf16 a[40:43], v[32:35], a[52:55], a[40:43]\n\ts_waitcnt lgkmcnt(8)\n\tv_mfma_f32_16x16x32_bf16 a[28:31], v[36:39], v[216:219], a[28:31]\n\t" \
    "ds_read_b64_tr_b16 v[24:25], v2 offset:24576\n\tds_read_b64_tr_b16 v[26:27], v2 offset:25088\n\tv_mfma_f32_16x16x32_bf16 a[36:39], v[36:39], a[48:51], a[36:39]\n\tbuffer_load_dwordx4 v10, s[12:15], s69 offen lds\n\ts_add_u32 m0, s72, 13312\n\tv_mfma_f32_16x16x32_bf16 a[44:47], v[36:39], a[52:55], a[44:47]\n\ts_waitcnt lgkmcnt(8)\n\tv_mfma_f32_16x16x32_bf16 v[72:75], v[40:43], v[216:219], v[72:75]\n\t" \
    "ds_read_b64_tr_b16 v[28:29], v3 offset:24576\n\tds_read_b64_tr_b16 v[30:31], v3 offset:25088\n\tv_mfma_f32_16x16x32_bf16 v[80:83], v[40:43], a[48:51], v[80:83]\n\tbuffer_load_dwordx4 v11, s[12:15], s69 offen lds\n\ts_add_u32 s68, s44, 700416\n\tv_mfma_f32_16x16x32_bf16 v[88:91], v[40:43], a[52:55], v[88:91]\n\ts_waitcnt lgkmcnt(8)\n\tv_mfma_f32_16x16x32_bf16 v[76:79], v[44:47], v[216:219], v[76:79]\n\t" \
    "ds_read_b64_tr_b16 v[32:33], v0 offset:28672\n\tds_read_b64_tr_b16 v[34:35], v0 offset:29184\n\tv_mfma_f32_16x16x32_bf16 v[84:87], v[44:47], a[48:51], v[84:87]\n\tv_mfma_f32_16x16x32_bf16 v[92:95], v[44:47], a[52:55], v[92:95]\n\ts_waitcnt vmcnt(32)\n\ts_barrier\n\ts_waitcnt lgkmcnt(8)\n\tv_mfma_f32_16x16x32_bf16 a[0:3], v[16:19], v[144:147], a[0:3]\n\t" \
    "ds_read_b64_tr_b16 v[36:37], v1 offset:28672\n\tds_read_b64_tr_b16 v[38:39], v1 offset:29184\n\tv_mfma_f32_16x16x32_bf16 a[8:11], v[16:19], v[148:151], a[8:11]\n\tbuffer_load_dwordx4 v[132:135], v8, s[8:11], s68 offen offset:0 sc1\n\tv_mfma_f32_16x16x32_bf16 a[16:19], v[16:19], v[152:155], a[16:19]\n\ts_waitcnt lgkmcnt(8)\n\tv_mfma_f32_16x16x32_bf16 a[4:7], v[20:23], v[144:147], a[4:7]\n\tds_read_b64_tr_b16 v[40:41], v2 offset:28672\n\t" \
    "ds_read_b64_tr_b16 v[42:43], v2 offset:29184\n\tv_mfma_f32_16x16x32_bf16 a[12:15], v[20:23], v[148:151], a[12:15]\n\tbuffer_load_dwordx4 v[136:139], v8, s[8:11], s68 offen offset:1024 sc1\n\tv_mfma_f32_16x16x32_bf16 a[20:23], v[20:23], v[152:155], a[20:23]\n\ts_waitcnt lgkmcnt(8)\n\tv_mfma_f32_16x16x32_bf16 v[48:51], v[24:27], v[144:147], v[48:51]\n\tds_read_b64_tr_b16 v[44:45], v3 offset:28672\n\tds_read_b64_tr_b16 v[46:47], v3 offset:29184\n\t" \
    "v_mfma_f32_16x16x32_bf16 v[56:59], v[24:27], v[148:151], v[56:59]\n\tbuffer_load_dwordx4 v[140:143], v8, s[8:11], s68 offen offset:2048 sc1\n\ts_mov_b32 s69, 5898240\n\ts_add_u32 m0, s72, 16384\n\tv_mfma_f32_16x16x32_bf16 v[64:67], v[24:27], v[152:155], v[64:67]\n\ts_waitcnt lgkmcnt(8)\n\tv_mfma_f32_16x16x32_bf16 v[52:55], v[28:31], v[144:147], v[52:55]\n\tds_read_b64_tr_b16 v[16:17], v0 offset:32768\n\t" \
    "ds_read_b64_tr_b16 v[18:19], v0 offset:33280\n\tv_mfma_f32_16x16x32_bf16 v[60:63], v[28:31], v[148:151], v[60:63]\n\tbuffer_load_dwordx4 v10, s[12:15], s69 offen lds\n\ts_add_u32 m0, s72, 17408\n\tv_mfma_f32_16x16x32_bf16 v[68:71], v[28:31], v[152:155], v[68:71]\n\ts_waitcnt lgkmcnt(8)\n\tv_mfma_f32_16x16x32_bf16 a[24:27], v[32:35], v[144:147], a[24:27]\n\tds_read_b64_tr_b16 v[20:21], v1 offset:32768\n\t" \
    "ds_read_b64_tr_b16 v[22:23], v1 offset:33280\n\tv_mfma_f32_16x16x32_bf16 a[32:35], v[32:35], v[148:151], a[32:35]\n\tv_mfma_f32_16x16x32_bf16 a[40:43], v[32:35], v[152:155], a[40:43]\n\ts_waitcnt lgkmcnt(8)\n\tv_mfma_f32_16x16x32_bf16 a[28:31], v[36:39], v[144:147], a[28:31]\n\tds_read_b64_tr_b16 v[24:25], v2 offset:32768\n\tds_read_b64_tr_b16 v[26:27], v2 offset:33280\n\tv_mfma_f32_16x16x32_bf16 a[36:39], v[36:39], v[148:151], a[36:39]\n\t" \
    "buffer_load_dwordx4 v11, s[12:15], s69 offen lds\n\ts_mov_b32 s69, 6193152\n\ts_add_u32 m0, s72, 20480\n\tv_mfma_f32_16x16x32_bf16 a[44:47], v[36:39], v[152:155], a[44:47]\n\ts_waitcnt lgkmcnt(8)\n\tv_mfma_f32_16x16x32_bf16 v[72:75], v[40:43], v[144:147], v[72:75]\n\tds_read_b64_tr_b16 v[28:29], v3 offset:32768\n\tds_read_b64_tr_b16 v[30:31], v3 offset:33280\n\t" \
    "v_mfma_f32_16x16x32_bf16 v[80:83], v[40:43], v[148:151], v[80:83]\n\tbuffer_load_dwordx4 v10, s[12:15], s69 offen lds\n\ts_add_u32 m0, s72, 21504\n\tv_mfma_f32_16x16x32_bf16 v[88:91], v[40:43], v[152:155], v[88:91]\n\ts_waitcnt lgkmcnt(8)\n\tv_mfma_f32_16x16x32_bf16 v[76:79], v[44:47], v[144:147], v[76:79]\n\tds_read_b64_tr_b16 v[32:33], v0 offset:36864\n\tds_read_b64_tr_b16 v[34:35], v0 offset:37376\n\t" \
    "v_mfma_f32_16x16x32_bf16 v[84:87], v[44:47], v[148:151], v[84:87]\n\tv_mfma_f32_16x16x32_bf16 v[92:95], v[44:47], v[152:155], v[92:95]\n\ts_waitcnt vmcnt(31)\n\ts_barrier\n\ts_waitcnt lgkmcnt(8)\n\tv_mfma_f32_16x16x32_bf16 a[0:3], v[16:19], a[56:59], a[0:3]\n\tds_read_b64_tr_b16 v[36:37], v1 offset:36864\n\tds_read_b64_tr_b16 v[38:39], v1 offset:37376\n\t" \
    "v_mfma_f32_16x16x32_bf16 a[8:11], v[16:19], a[60:63], a[8:11]\n\tbuffer_load_dwordx4 v11, s[12:15], s69 offen lds\n\ts_mov_b32 s69, 6488064\n\ts_add_u32 m0, s72, 24576\n\tv_mfma_f32_16x16x32_bf16 a[16:19], v[16:19], a[64:67], a[16:19]\n\ts_waitcnt lgkmcnt(8)\n\tv_mfma_f32_16x16x32_bf16 a[4:7], v[20:23], a[56:59], a[4:7]\n\tds_read_b64_tr_b16 v[40:41], v2 offset:36864\n\t" \
    "ds_read_b64_tr_b16 v[42:43], v2 offset:37376\n\tv_mfma_f32_16x16x32_bf16 a[12:15], v[20:23], a[60:63], a[12:15]\n\tbuffer_load_dwordx4 v10, s[12:15], s69 offen lds\n\ts_add_u32 m0, s72, 25600\n\tv_mfma_f32_16x16x32_bf16 a[20:23], v[20:23], a[64:67], a[20:23]\n\ts_waitcnt lgkmcnt(8)\n\tv_mfma_f32_16x16x32_bf16 v[48:51], v[24:27], a[56:59], v[48:51]\n\tds_read_b64_tr_b16 v[44:45], v3 offset:36864\n\t" \
    "ds_read_b64_tr_b16 v[46:47], v3 offset:37376\n\tv_mfma_f32_16x16x32_bf16 v[56:59], v[24:27], a[60:63], v[56:59]\n\tbuffer_load_dwordx4 v11, s[12:15], s69 offen lds\n\ts_mov_b32 s69, 6782976\n\ts_add_u32 m0, s72, 28672\n\tv_mfma_f32_16x16x32_bf16 v[64:67], v[24:27], a[64:67], v[64:67]\n\ts_waitcnt lgkmcnt(8)\n\tv_mfma_f32_16x16x32_bf16 v[52:55], v[28:31], a[56:59], v[52:55]\n\t" \
    "ds_read_b64_tr_b16 v[16:17], v0 offset:40960\n\tds_read_b64_tr_b16 v[18:19], v0 offset:41472\n\tv_mfma_f32_16x16x32_bf16 v[60:63], v[28:31], a[60:63], v[60:63]\n\tbuffer_load_dwordx4 v10, s[12:15], s69 offen lds\n\ts_add_u32 m0, s72, 29696\n\tv_mfma_f32_16x16x32_bf16 v[68:71], v[28:31], a[64:67], v[68:71]\n\ts_waitcnt lgkmcnt(8)\n\tv_mfma_f32_16x16x32_bf16 a[24:27], v[32:35], a[56:59], a[24:27]\n\t" \
    "ds_read_b64_tr_b16 v[20:21], v1 offset:40960\n\tds_read_b64_tr_b16 v[22:23], v1 offset:41472\n\tv_mfma_f32_16x16x32_bf16 a[32:35], v[32:35], a[60:63], a[32:35]\n\tbuffer_load_dwordx4 v11, s[12:15], s69 offen lds\n\ts_add_u32 s68, s44, 737280\n\tv_mfma_f32_16x16x32_bf16 a[40:43], v[32:35], a[64:67], a[40:43]\n\ts_waitcnt lgkmcnt(8)\n\tv_mfma_f32_16x16x32_bf16 a[28:31], v[36:39], a[56:59], a[28:31]\n\t" \
    "ds_read_b64_tr_b16 v[24:25], v2 offset:40960\n\tds_read_b64_tr_b16 v[26:27], v2 offset:41472\n\tv_mfma_f32_16x16x32_bf16 a[36:39], v[36:39], a[60:63], a[36:39]\n\tv_mfma_f32_16x16x32_bf16 a[44:47], v[36:39], a[64:67], a[44:47]\n\ts_waitcnt lgkmcnt(8)\n\tv_mfma_f32_16x16x32_bf16 v[72:75], v[40:43], a[56:59], v[72:75]\n\tds_read_b64_tr_b16 v[28:29], v3 offset:40960\n\tds_read_b64_tr_b16 v[30:31], v3 offset:41472\n\t" \
    "v_mfma_f32_16x16x32_bf16 v[80:83], v[40:43], a[60:63], v[80:83]\n\tbuffer_load_dwordx4 v[144:147], v8, s[8:11], s68 offen offset:0 sc1\n\tv_mfma_f32_16x16x32_bf16 v[88:91], v[40:43], a[64:67], v[88:91]\n\ts_waitcnt lgkmcnt(8)\n\tv_mfma_f32_16x16x32_bf16 v[76:79], v[44:47], a[56:59], v[76:79]\n\tds_read_b64_tr_b16 v[32:33], v0 offset:45056\n\tds_read_b64_tr_b16 v[34:35], v0 offset:45568\n\tv_mfma_f32_16x16x32_bf16 v[84:87], v[44:47], a[60:63], v[84:87]\n\t" \
    "buffer_load_dwordx4 v[148:151], v8, s[8:11], s68 offen offset:1024 sc1\n\tv_mfma_f32_16x16x32_bf16 v[92:95], v[44:47], a[64:67], v[92:95]\n\ts_waitcnt vmcnt(31)\n\ts_barrier\n\ts_waitcnt lgkmcnt(8)\n\tv_mfma_f32_16x16x32_bf16 a[0:3], v[16:19], v[156:159], a[0:3]\n\tds_read_b64_tr_b16 v[36:37], v1 offset:45056\n\tds_read_b64_tr_b16 v[38:39], v1 offset:45568\n\t" \
    "v_mfma_f32_16x16x32_bf16 a[8:11], v[16:19], v[160:163], a[8:11]\n\tbuffer_load_dwordx4 v[152:155], v8, s[8:11], s68 offen offset:2048 sc1\n\ts_add_u32 s68, s44, 774144\n\tv_mfma_f32_16x16x32_bf16 a[16:19], v[16:19], v[164:167], a[16:19]\n\ts_waitcnt lgkmcnt(8)\n\tv_mfma_f32_16x16x32_bf16 a[4:7], v[20:23], v[156:159], a[4:7]\n\tds_read_b64_tr_b16 v[40:41], v2 offset:45056\n\tds_read_b64_tr_b16 v[42:43], v2 offset:45568\n\t" \
    "v_mfma_f32_16x16x32_bf16 a[12:15], v[20:23], v[160:163], a[12:15]\n\tbuffer_load_dwordx4 v[204:207], v8, s[8:11], s68 offen offset:0 sc1\n\tv_mfma_f32_16x16x32_bf16 a[20:23], v[20:23], v[164:167], a[20:23]\n\ts_waitcnt lgkmcnt(8)\n\tv_mfma_f32_16x16x32_bf16 v[48:51], v[24:27], v[156:159], v[48:51]\n\tds_read_b64_tr_b16 v[44:45], v3 offset:45056\n\tds_read_b64_tr_b16 v[46:47], v3 offset:45568\n\tv_mfma_f32_16x16x32_bf16 v[56:59], v[24:27], v[160:163], v[56:59]\n\t" \
    "v_mfma_f32_16x16x32_bf16 v[64:67], v[24:27], v[164:167], v[64:67]\n\ts_waitcnt lgkmcnt(8)\n\tv_mfma_f32_16x16x32_bf16 v[52:55], v[28:31], v[156:159], v[52:55]\n\tds_read_b64_tr_b16 v[16:17], v0 offset:49152\n\tds_read_b64_tr_b16 v[18:19], v0 offset:49664\n\tv_mfma_f32_16x16x32_bf16 v[60:63], v[28:31], v[160:163], v[60:63]\n\tbuffer_load_dwordx4 v[208:211], v8, s[8:11], s68 offen offset:1024 sc1\n\tv_mfma_f32_16x16x32_bf16 v[68:71], v[28:31], v[164:167], v[68:71]\n\t" \
    "s_waitcnt lgkmcnt(8)\n\tv_mfma_f32_16x16x32_bf16 a[24:27], v[32:35], v[156:159], a[24:27]\n\tds_read_b64_tr_b16 v[20:21], v1 offset:49152\n\tds_read_b64_tr_b16 v[22:23], v1 offset:49664\n\tv_mfma_f32_16x16x32_bf16 a[32:35], v[32:35], v[160:163], a[32:35]\n\tbuffer_load_dwordx4 v[212:215], v8, s[8:11], s68 offen offset:2048 sc1\n\ts_mov_b32 s69, 7077888\n\ts_add_u32 m0, s72, 32768\n\t" \
    "v_mfma_f32_16x16x32_bf16 a[40:43], v[32:35], v[164:167], a[40:43]\n\ts_waitcnt lgkmcnt(8)\n\tv_mfma_f32_16x16x32_bf16 a[28:31], v[36:39], v[156:159], a[28:31]\n\tds_read_b64_tr_b16 v[24:25], v2 offset:49152\n\tds_read_b64_tr_b16 v[26:27], v2 offset:49664\n\tv_mfma_f32_16x16x32_bf16 a[36:39], v[36:39], v[160:163], a[36:39]\n\tbuffer_load_dwordx4 v10, s[12:15], s69 offen lds\n\ts_add_u32 m0, s72, 33792\n\t" \
    "v_mfma_f32_16x16x32_bf16 a[44:47], v[36:39], v[164:167], a[44:47]\n\ts_waitcnt lgkmcnt(8)\n\tv_mfma_f32_16x16x32_bf16 v[72:75], v[40:43], v[156:159], v[72:75]\n\tds_read_b64_tr_b16 v[28:29], v3 offset:49152\n\tds_read_b64_tr_b16 v[30:31], v3 offset:49664\n\tv_mfma_f32_16x16x32_bf16 v[80:83], v[40:43], v[160:163], v[80:83]\n\tv_mfma_f32_16x16x32_bf16 v[88:91], v[40:43], v[164:167], v[88:91]\n\ts_waitcnt lgkmcnt(8)\n\t" \
    "v_mfma_f32_16x16x32_bf16 v[76:79], v[44:47], v[156:159], v[76:79]\n\tds_read_b64_tr_b16 v[32:33], v0 offset:53248\n\tds_read_b64_tr_b16 v[34:35], v0 offset:53760\n\tv_mfma_f32_16x16x32_bf16 v[84:87], v[44:47], v[160:163], v[84:87]\n\tbuffer_load_dwordx4 v11, s[12:15], s69 offen lds\n\ts_mov_b32 s69, 7372800\n\ts_add_u32 m0, s72, 36864\n\tv_mfma_f32_16x16x32_bf16 v[92:95], v[44:47], v[164:167], v[92:95]\n\t" \
    "s_waitcnt vmcnt(30)\n\ts_barrier\n\ts_waitcnt lgkmcnt(8)\n\tv_mfma_f32_16x16x32_bf16 a[0:3], v[16:19], a[68:71], a[0:3]\n\tds_read_b64_tr_b16 v[36:37], v1 offset:53248\n\tds_read_b64_tr_b16 v[38:39], v1 offset:53760\n\tv_mfma_f32_16x16x32_bf16 a[8:11], v[16:19], a[72:75], a[8:11]\n\tbuffer_load_dwordx4 v10, s[12:15], s69 offen lds\n\t" \
    "s_add_u32 m0, s72, 37888\n\tv_mfma_f32_16x16x32_bf16 a[16:19], v[16:19], a[76:79], a[16:19]\n\ts_waitcnt lgkmcnt(8)\n\tv_mfma_f32_16x16x32_bf16 a[4:7], v[20:23], a[68:71], a[4:7]\n\tds_read_b64_tr_b16 v[40:41], v2 offset:53248\n\tds_read_b64_tr_b16 v[42:43], v2 offset:53760\n\tv_mfma_f32_16x16x32_bf16 a[12:15], v[20:23], a[72:75], a[12:15]\n\tbuffer_load_dwordx4 v11, s[12:15], s69 offen lds\n\t" \
    "s_add_u32 s68, s44, 811008\n\tv_mfma_f32_16x16x32_bf16 a[20:23], v[20:23], a[76:79], a[20:23]\n\ts_waitcnt lgkmcnt(8)\n\tv_mfma_f32_16x16x32_bf16 v[48:51], v[24:27], a[68:71], v[48:51]\n\tds_read_b64_tr_b16 v[44:45], v3 offset:53248\n\tds_read_b64_tr_b16 v[46:47], v3 offset:53760\n\tv_mfma_f32_16x16x32_bf16 v[56:59], v[24:27], a[72:75], v[56:59]\n\tbuffer_load_dwordx4 v[156:159], v8, s[8:11], s68 offen offset:0 sc1\n\t" \
    "v_mfma_f32_16x16x32_bf16 v[64:67], v[24:27], a[76:79], v[64:67]\n\ts_waitcnt lgkmcnt(8)\n\tv_mfma_f32_16x16x32_bf16 v[52:55], v[28:31], a[68:71], v[52:55]\n\tds_read_b64_tr_b16 v[16:17], v0 offset:57344\n\tds_read_b64_tr_b16 v[18:19], v0 offset:57856\n\tv_mfma_f32_16x16x32_bf16 v[60:63], v[28:31], a[72:75], v[60:63]\n\tv_mfma_f32_16x16x32_bf16 v[68:71], v[28:31], a[76:79], v[68:71]\n\ts_waitcnt lgkmcnt(8)\n\t" \
    "v_mfma_f32_16x16x32_bf16 a[24:27], v[32:35], a[68:71], a[24:27]\n\tds_read_b64_tr_b16 v[20:21], v1 offset:57344\n\tds_read_b64_tr_b16 v[22:23], v1 offset:57856\n\tv_mfma_f32_16x16x32_bf16 a[32:35], v[32:35], a[72:75], a[32:35]\n\tbuffer_load_dwordx4 v[160:163], v8, s[8:11], s68 offen offset:1024 sc1\n\tv_mfma_f32_16x16x32_bf16 a[40:43], v[32:35], a[76:79], a[40:43]\n\ts_waitcnt lgkmcnt(8)\n\tv_mfma_f32_16x16x32_bf16 a[28:31], v[36:39], a[68:71], a[28:31]\n\t" \
    "ds_read_b64_tr_b16 v[24:25], v2 offset:57344\n\tds_read_b64_tr_b16 v[26:27], v2 offset:57856\n\tv_mfma_f32_16x16x32_bf16 a[36:39], v[36:39], a[72:75], a[36:39]\n\tbuffer_load_dwordx4 v[164:167], v8, s[8:11], s68 offen offset:2048 sc1\n\ts_add_u32 s68, s44, 847872\n\tv_mfma_f32_16x16x32_bf16 a[44:47], v[36:39], a[76:79], a[44:47]\n\ts_waitcnt lgkmcnt(8)\n\tv_mfma_f32_16x16x32_bf16 v[72:75], v[40:43], a[68:71], v[72:75]\n\t" \
    "ds_read_b64_tr_b16 v[28:29], v3 offset:57344\n\tds_read_b64_tr_b16 v[30:31], v3 offset:57856\n\tv_mfma_f32_16x16x32_bf16 v[80:83], v[40:43], a[72:75], v[80:83]\n\tv_mfma_f32_16x16x32_bf16 v[88:91], v[40:43], a[76:79], v[88:91]\n\ts_waitcnt lgkmcnt(8)\n\tv_mfma_f32_16x16x32_bf16 v[76:79], v[44:47], a[68:71], v[76:79]\n\tds_read_b64_tr_b16 v[32:33], v0 offset:61440\n\tds_read_b64_tr_b16 v[34:35], v0 offset:61952\n\t" \
    "v_mfma_f32_16x16x32_bf16 v[84:87], v[44:47], a[72:75], v[84:87]\n\tbuffer_load_dwordx4 v[216:219], v8, s[8:11], s68 offen offset:0 sc1\n\tv_mfma_f32_16x16x32_bf16 v[92:95], v[44:47], a[76:79], v[92:95]\n\ts_waitcnt vmcnt(32)\n\ts_barrier\n\ts_waitcnt lgkmcnt(8)\n\tv_mfma_f32_16x16x32_bf16 a[0:3], v[16:19], v[168:171], a[0:3]\n\tds_read_b64_tr_b16 v[36:37], v1 offset:61440\n\t" \
    "ds_read_b64_tr_b16 v[38:39], v1 offset:61952\n\tv_mfma_f32_16x16x32_bf16 a[8:11], v[16:19], v[172:175], a[8:11]\n\tbuffer_load_dwordx4 a[48:51], v8, s[8:11], s68 offen offset:1024 sc1\n\tv_mfma_f32_16x16x32_bf16 a[16:19], v[16:19], v[176:179], a[16:19]\n\ts_waitcnt lgkmcnt(8)\n\tv_mfma_f32_16x16x32_bf16 a[4:7], v[20:23], v[168:171], a[4:7]\n\tds_read_b64_tr_b16 v[40:41], v2 offset:61440\n\tds_read_b64_tr_b16 v[42:43], v2 offset:61952\n\t" \
    "v_mfma_f32_16x16x32_bf16 a[12:15], v[20:23], v[172:175], a[12:15]\n\tbuffer_load_dwordx4 a[52:55], v8, s[8:11], s68 offen offset:2048 sc1\n\ts_mov_b32 s69, 7667712\n\ts_add_u32 m0, s72, 40960\n\tv_mfma_f32_16x16x32_bf16 a[20:23], v[20:23], v[176:179], a[20:23]\n\ts_waitcnt lgkmcnt(8)\n\tv_mfma_f32_16x16x32_bf16 v[48:51], v[24:27], v[168:171], v[48:51]\n\tds_read_b64_tr_b16 v[44:45], v3 offset:61440\n\t" \
    "ds_read_b64_tr_b16 v[46:47], v3 offset:61952\n\tv_mfma_f32_16x16x32_bf16 v[56:59], v[24:27], v[172:175], v[56:59]\n\tbuffer_load_dwordx4 v10, s[12:15], s69 offen lds\n\ts_add_u32 m0, s72, 41984\n\tv_mfma_f32_16x16x32_bf16 v[64:67], v[24:27], v[176:179], v[64:67]\n\ts_waitcnt lgkmcnt(8)\n\tv_mfma_f32_16x16x32_bf16 v[52:55], v[28:31], v[168:171], v[52:55]\n\tds_read_b64_tr_b16 v[16:17], v0 offset:0\n\t" \
    "ds_read_b64_tr_b16 v[18:19], v0 offset:512\n\tv_mfma_f32_16x16x32_bf16 v[60:63], v[28:31], v[172:175], v[60:63]\n\tbuffer_load_dwordx4 v11, s[12:15], s69 offen lds\n\ts_mov_b32 s69, 7962624\n\ts_add_u32 m0, s72, 45056\n\tv_mfma_f32_16x16x32_bf16 v[68:71], v[28:31], v[176:179], v[68:71]\n\ts_waitcnt lgkmcnt(8)\n\tv_mfma_f32_16x16x32_bf16 a[24:27], v[32:35], v[168:171], a[24:27]\n\t" \
    "ds_read_b64_tr_b16 v[20:21], v1 offset:0\n\tds_read_b64_tr_b16 v[22:23], v1 offset:512\n\tv_mfma_f32_16x16x32_bf16 a[32:35], v[32:35], v[172:175], a[32:35]\n\tv_mfma_f32_16x16x32_bf16 a[40:43], v[32:35], v[176:179], a[40:43]\n\ts_waitcnt lgkmcnt(8)\n\tv_mfma_f32_16x16x32_bf16 a[28:31], v[36:39], v[168:171], a[28:31]\n\tds_read_b64_tr_b16 v[24:25], v2 offset:0\n\tds_read_b64_tr_b16 v[26:27], v2 offset:512\n\t" \
    "v_mfma_f32_16x16x32_bf16 a[36:39], v[36:39], v[172:175], a[36:39]\n\tbuffer_load_dwordx4 v10, s[12:15], s69 offen lds\n\ts_add_u32 m0, s72, 46080\n\tv_mfma_f32_16x16x32_bf16 a[44:47], v[36:39], v[176:179], a[44:47]\n\ts_waitcnt lgkmcnt(8)\n\tv_mfma_f32_16x16x32_bf16 v[72:75], v[40:43], v[168:171], v[72:75]\n\tds_read_b64_tr_b16 v[28:29], v3 offset:0\n\tds_read_b64_tr_b16 v[30:31], v3 offset:512\n\t" \
    "v_mfma_f32_16x16x32_bf16 v[80:83], v[40:43], v[172:175], v[80:83]\n\tbuffer_load_dwordx4 v11, s[12:15], s69 offen lds\n\ts_add_u32 s68, s44, 884736\n\tv_mfma_f32_16x16x32_bf16 v[88:91], v[40:43], v[176:179], v[88:91]\n\ts_waitcnt lgkmcnt(8)\n\tv_mfma_f32_16x16x32_bf16 v[76:79], v[44:47], v[168:171], v[76:79]\n\tds_read_b64_tr_b16 v[32:33], v0 offset:4096\n\tds_read_b64_tr_b16 v[34:35], v0 offset:4608\n\t" \
    "v_mfma_f32_16x16x32_bf16 v[84:87], v[44:47], v[172:175], v[84:87]\n\tv_mfma_f32_16x16x32_bf16 v[92:95], v[44:47], v[176:179], v[92:95]\n\ts_waitcnt vmcnt(31)\n\ts_barrier\n\ts_waitcnt lgkmcnt(8)\n\tv_mfma_f32_16x16x32_bf16 a[0:3], v[16:19], v[180:183], a[0:3]\n\tds_read_b64_tr_b16 v[36:37], v1 offset:4096\n\tds_read_b64_tr_b16 v[38:39], v1 offset:4608\n\t" \
    "v_mfma_f32_16x16x32_bf16 a[8:11], v[16:19], v[184:187], a[8:11]\n\tbuffer_load_dwordx4 a[56:59], v8, s[8:11], s68 offen offset:0 sc1\n\tv_mfma_f32_16x16x32_bf16 a[16:19], v[16:19], v[188:191], a[16:19]\n\ts_waitcnt lgkmcnt(8)\n\tv_mfma_f32_16x16x32_bf16 a[4:7], v[20:23], v[180:183], a[4:7]\n\tds_read_b64_tr_b16 v[40:41], v2 offset:4096\n\tds_read_b64_tr_b16 v[42:43], v2 offset:4608\n\tv_mfma_f32_16x16x32_bf16 a[12:15], v[20:23], v[184:187], a[12:15]\n\t" \
    "buffer_load_dwordx4 a[60:63], v8, s[8:11], s68 offen offset:1024 sc1\n\tv_mfma_f32_16x16x32_bf16 a[20:23], v[20:23], v[188:191], a[20:23]\n\ts_waitcnt lgkmcnt(8)\n\tv_mfma_f32_16x16x32_bf16 v[48:51], v[24:27], v[180:183], v[48:51]\n\tds_read_b64_tr_b16 v[44:45], v3 offset:4096\n\tds_read_b64_tr_b16 v[46:47], v3 offset:4608\n\tv_mfma_f32_16x16x32_bf16 v[56:59], v[24:27], v[184:187], v[56:59]\n\tbuffer_load_dwordx4 a[64:67], v8, s[8:11], s68 offen offset:2048 sc1\n\t" \
    "s_add_u32 s68, s44, 921600\n\tv_mfma_f32_16x16x32_bf16 v[64:67], v[24:27], v[188:191], v[64:67]\n\ts_waitcnt lgkmcnt(8)\n\tv_mfma_f32_16x16x32_bf16 v[52:55], v[28:31], v[180:183], v[52:55]\n\tds_read_b64_tr_b16 v[16:17], v0 offset:8192\n\tds_read_b64_tr_b16 v[18:19], v0 offset:8704\n\tv_mfma_f32_16x16x32_bf16 v[60:63], v[28:31], v[184:187], v[60:63]\n\tbuffer_load_dwordx4 v[168:171], v8, s[8:11], s68 offen offset:0 sc1\n\t" \
    "v_mfma_f32_16x16x32_bf16 v[68:71], v[28:31], v[188:191], v[68:71]\n\ts_waitcnt lgkmcnt(8)\n\tv_mfma_f32_16x16x32_bf16 a[0:3], v[32:35], v[192:195], a[0:3]\n\tds_read_b64_tr_b16 v[20:21], v1 offset:8192\n\tds_read_b64_tr_b16 v[22:23], v1 offset:8704\n\tv_mfma_f32_16x16x32_bf16 a[8:11], v[32:35], v[196:199], a[8:11]\n\tbuffer_load_dwordx4 v[172:175], v8, s[8:11], s68 offen offset:1024 sc1\n\tv_mfma_f32_16x16x32_bf16 a[16:19], v[32:35], v[200:203], a[16:19]\n\t" \
    "s_waitcnt lgkmcnt(8)\n\tv_mfma_f32_16x16x32_bf16 a[4:7], v[36:39], v[192:195], a[4:7]\n\tds_read_b64_tr_b16 v[24:25], v2 offset:8192\n\tds_read_b64_tr_b16 v[26:27], v2 offset:8704\n\tv_mfma_f32_16x16x32_bf16 a[12:15], v[36:39], v[196:199], a[12:15]\n\tv_mfma_f32_16x16x32_bf16 a[20:23], v[36:39], v[200:203], a[20:23]\n\ts_waitcnt lgkmcnt(8)\n\tv_mfma_f32_16x16x32_bf16 v[48:51], v[40:43], v[192:195], v[48:51]\n\t" \
    "ds_read_b64_tr_b16 v[28:29], v3 offset:8192\n\tds_read_b64_tr_b16 v[30:31], v3 offset:8704\n\tv_mfma_f32_16x16x32_bf16 v[56:59], v[40:43], v[196:199], v[56:59]\n\tbuffer_load_dwordx4 v[176:179], v8, s[8:11], s68 offen offset:2048 sc1\n\ts_mov_b32 s69, 8257536\n\ts_add_u32 m0, s72, 49152\n\tv_mfma_f32_16x16x32_bf16 v[64:67], v[40:43], v[200:203], v[64:67]\n\ts_waitcnt lgkmcnt(8)\n\t" \
    "v_mfma_f32_16x16x32_bf16 v[52:55], v[44:47], v[192:195], v[52:55]\n\tds_read_b64_tr_b16 v[32:33], v0 offset:12288\n\tds_read_b64_tr_b16 v[34:35], v0 offset:12800\n\tv_mfma_f32_16x16x32_bf16 v[60:63], v[44:47], v[196:199], v[60:63]\n\tbuffer_load_dwordx4 v10, s[12:15], s69 offen lds\n\ts_add_u32 m0, s72, 50176\n\tv_mfma_f32_16x16x32_bf16 v[68:71], v[44:47], v[200:203], v[68:71]\n\ts_waitcnt vmcnt(31)\n\t" \
    "s_barrier\n\ts_waitcnt lgkmcnt(8)\n\tv_mfma_f32_16x16x32_bf16 a[0:3], v[16:19], v[120:123], a[0:3]\n\tds_read_b64_tr_b16 v[36:37], v1 offset:12288\n\tds_read_b64_tr_b16 v[38:39], v1 offset:12800\n\tv_mfma_f32_16x16x32_bf16 a[8:11], v[16:19], v[124:127], a[8:11]\n\tbuffer_load_dwordx4 v11, s[12:15], s69 offen lds\n\ts_mov_b32 s69, 8552448\n\t" \
    "s_add_u32 m0, s72, 53248\n\tv_mfma_f32_16x16x32_bf16 a[16:19], v[16:19], v[128:131], a[16:19]\n\ts_waitcnt lgkmcnt(8)\n\tv_mfma_f32_16x16x32_bf16 a[4:7], v[20:23], v[120:123], a[4:7]\n\tds_read_b64_tr_b16 v[40:41], v2 offset:12288\n\tds_read_b64_tr_b16 v[42:43], v2 offset:12800\n\tv_mfma_f32_16x16x32_bf16 a[12:15], v[20:23], v[124:127], a[12:15]\n\tbuffer_load_dwordx4 v10, s[12:15], s69 offen lds\n\t" \
    "s_add_u32 m0, s72, 54272\n\tv_mfma_f32_16x16x32_bf16 a[20:23], v[20:23], v[128:131], a[20:23]\n\ts_waitcnt lgkmcnt(8)\n\tv_mfma_f32_16x16x32_bf16 v[48:51], v[24:27], v[120:123], v[48:51]\n\tds_read_b64_tr_b16 v[44:45], v3 offset:12288\n\tds_read_b64_tr_b16 v[46:47], v3 offset:12800\n\tv_mfma_f32_16x16x32_bf16 v[56:59], v[24:27], v[124:127], v[56:59]\n\tv_mfma_f32_16x16x32_bf16 v[64:67], v[24:27], v[128:131], v[64:67]\n\t" \
    "s_waitcnt lgkmcnt(8)\n\tv_mfma_f32_16x16x32_bf16 v[52:55], v[28:31], v[120:123], v[52:55]\n\tds_read_b64_tr_b16 v[16:17], v0 offset:16384\n\tds_read_b64_tr_b16 v[18:19], v0 offset:16896\n\tv_mfma_f32_16x16x32_bf16 v[60:63], v[28:31], v[124:127], v[60:63]\n\tbuffer_load_dwordx4 v11, s[12:15], s69 offen lds\n\ts_mov_b32 s69, 8847360\n\ts_add_u32 m0, s72, 57344\n\t" \
    "v_mfma_f32_16x16x32_bf16 v[68:71], v[28:31], v[128:131], v[68:71]\n\ts_mul_i32 s43, s6, 0\n\ts_sub_u32 s70, s43, 0\n\ts_waitcnt lgkmcnt(8)\n\tv_mfma_f32_16x16x32_bf16 a[0:3], v[32:35], v[132:135], a[0:3]\n\tds_read_b64_tr_b16 v[20:21], v1 offset:16384\n\tds_read_b64_tr_b16 v[22:23], v1 offset:16896\n\tv_mfma_f32_16x16x32_bf16 a[8:11], v[32:35], v[136:139], a[8:11]\n\t" \
    "buffer_load_dwordx4 v10, s[12:15], s69 offen lds\n\ts_add_u32 m0, s72, 58368\n\tv_mfma_f32_16x16x32_bf16 a[16:19], v[32:35], v[140:143], a[16:19]\n\ts_waitcnt lgkmcnt(8)\n\tv_mfma_f32_16x16x32_bf16 a[4:7], v[36:39], v[132:135], a[4:7]\n\tds_read_b64_tr_b16 v[24:25], v2 offset:16384\n\tds_read_b64_tr_b16 v[26:27], v2 offset:16896\n\tv_mfma_f32_16x16x32_bf16 a[12:15], v[36:39], v[136:139], a[12:15]\n\t" \
    "v_mfma_f32_16x16x32_bf16 a[20:23], v[36:39], v[140:143], a[20:23]\n\ts_waitcnt lgkmcnt(8)\n\tv_mfma_f32_16x16x32_bf16 v[48:51], v[40:43], v[132:135], v[48:51]\n\tds_read_b64_tr_b16 v[28:29], v3 offset:16384\n\tds_read_b64_tr_b16 v[30:31], v3 offset:16896\n\tv_mfma_f32_16x16x32_bf16 v[56:59], v[40:43], v[136:139], v[56:59]\n\tbuffer_load_dwordx4 v11, s[12:15], s69 offen lds\n\ts_add_u32 s68, s44, 958464\n\t" \
    "v_mfma_f32_16x16x32_bf16 v[64:67], v[40:43], v[140:143], v[64:67]\n\ts_waitcnt lgkmcnt(8)\n\tv_mfma_f32_16x16x32_bf16 v[52:55], v[44:47], v[132:135], v[52:55]\n\tds_read_b64_tr_b16 v[32:33], v0 offset:20480\n\tds_read_b64_tr_b16 v[34:35], v0 offset:20992\n\tv_mfma_f32_16x16x32_bf16 v[60:63], v[44:47], v[136:139], v[60:63]\n\tbuffer_load_dwordx4 a[68:71], v8, s[8:11], s68 offen offset:0 sc1\n\tv_mfma_f32_16x16x32_bf16 v[68:71], v[44:47], v[140:143], v[68:71]\n\t" \
    "s_waitcnt vmcnt(33)\n\ts_barrier\n\ts_waitcnt vmcnt(30) lgkmcnt(8)\n\tv_mfma_f32_16x16x32_bf16 a[0:3], v[16:19], v[144:147], a[0:3]\n\tds_read_b64_tr_b16 v[36:37], v1 offset:20480\n\tds_read_b64_tr_b16 v[38:39], v1 offset:20992\n\tv_mfma_f32_16x16x32_bf16 a[8:11], v[16:19], v[148:151], a[8:11]\n\tbuffer_load_dwordx4 a[72:75], v8, s[8:11], s68 offen offset:1024 sc1\n\t" \
    "v_mfma_f32_16x16x32_bf16 a[16:19], v[16:19], v[152:155], a[16:19]\n\ts_waitcnt lgkmcnt(8)\n\tv_mfma_f32_16x16x32_bf16 a[4:7], v[20:23], v[144:147], a[4:7]\n\tds_read_b64_tr_b16 v[40:41], v2 offset:20480\n\tds_read_b64_tr_b16 v[42:43], v2 offset:20992\n\tv_mfma_f32_16x16x32_bf16 a[12:15], v[20:23], v[148:151], a[12:15]\n\tbuffer_load_dwordx4 a[76:79], v8, s[8:11], s68 offen offset:2048 sc1\n\ts_add_u32 s68, s44, 995328\n\t" \
    "v_mfma_f32_16x16x32_bf16 a[20:23], v[20:23], v[152:155], a[20:23]\n\ts_waitcnt lgkmcnt(8)\n\tv_mfma_f32_16x16x32_bf16 v[48:51], v[24:27], v[144:147], v[48:51]\n\tds_read_b64_tr_b16 v[44:45], v3 offset:20480\n\tds_read_b64_tr_b16 v[46:47], v3 offset:20992\n\tv_mfma_f32_16x16x32_bf16 v[56:59], v[24:27], v[148:151], v[56:59]\n\tbuffer_load_dwordx4 a[80:83], v8, s[8:11], s68 offen offset:0 sc1\n\tv_mfma_f32_16x16x32_bf16 v[64:67], v[24:27], v[152:155], v[64:67]\n\t" \
    "s_waitcnt lgkmcnt(8)\n\tv_mfma_f32_16x16x32_bf16 v[52:55], v[28:31], v[144:147], v[52:55]\n\tds_read_b64_tr_b16 v[16:17], v0 offset:24576\n\tds_read_b64_tr_b16 v[18:19], v0 offset:25088\n\tv_mfma_f32_16x16x32_bf16 v[60:63], v[28:31], v[148:151], v[60:63]\n\tv_mfma_f32_16x16x32_bf16 v[68:71], v[28:31], v[152:155], v[68:71]\n\ts_waitcnt vmcnt(30) lgkmcnt(8)\n\tv_mfma_f32_16x16x32_bf16 a[0:3], v[32:35], v[204:207], a[0:3]\n\t" \
    "ds_read_b64_tr_b16 v[20:21], v1 offset:24576\n\tds_read_b64_tr_b16 v[22:23], v1 offset:25088\n\tv_mfma_f32_16x16x32_bf16 a[8:11], v[32:35], v[208:211], a[8:11]\n\tbuffer_load_dwordx4 a[84:87], v8, s[8:11], s68 offen offset:1024 sc1\n\tv_mfma_f32_16x16x32_bf16 a[16:19], v[32:35], v[212:215], a[16:19]\n\ts_waitcnt lgkmcnt(8)\n\tv_mfma_f32_16x16x32_bf16 a[4:7], v[36:39], v[204:207], a[4:7]\n\tds_read_b64_tr_b16 v[24:25], v2 offset:24576\n\t" \
    "ds_read_b64_tr_b16 v[26:27], v2 offset:25088\n\tv_mfma_f32_16x16x32_bf16 a[12:15], v[36:39], v[208:211], a[12:15]\n\tbuffer_load_dwordx4 a[88:91], v8, s[8:11], s68 offen offset:2048 sc1\n\ts_mov_b32 s69, 9142272\n\ts_add_u32 m0, s72, 61440\n\tv_mfma_f32_16x16x32_bf16 a[20:23], v[36:39], v[212:215], a[20:23]\n\ts_waitcnt lgkmcnt(8)\n\tv_mfma_f32_16x16x32_bf16 v[48:51], v[40:43], v[204:207], v[48:51]\n\t" \
    "ds_read_b64_tr_b16 v[28:29], v3 offset:24576\n\tds_read_b64_tr_b16 v[30:31], v3 offset:25088\n\tv_mfma_f32_16x16x32_bf16 v[56:59], v[40:43], v[208:211], v[56:59]\n\tv_mfma_f32_16x16x32_bf16 v[64:67], v[40:43], v[212:215], v[64:67]\n\ts_waitcnt lgkmcnt(8)\n\tv_mfma_f32_16x16x32_bf16 v[52:55], v[44:47], v[204:207], v[52:55]\n\tds_read_b64_tr_b16 v[32:33], v0 offset:28672\n\tds_read_b64_tr_b16 v[34:35], v0 offset:29184\n\t" \
    "v_mfma_f32_16x16x32_bf16 v[60:63], v[44:47], v[208:211], v[60:63]\n\tbuffer_load_dwordx4 v10, s[12:15], s69 offen lds\n\ts_add_u32 m0, s72, 62464\n\tv_mfma_f32_16x16x32_bf16 v[68:71], v[44:47], v[212:215], v[68:71]\n\ts_waitcnt vmcnt(29)\n\ts_barrier\n\ts_waitcnt vmcnt(26) lgkmcnt(8)\n\tv_mfma_f32_16x16x32_bf16 a[0:3], v[16:19], v[156:159], a[0:3]\n\t" \
    "ds_read_b64_tr_b16 v[36:37], v1 offset:28672\n\tds_read_b64_tr_b16 v[38:39], v1 offset:29184\n\tv_mfma_f32_16x16x32_bf16 a[8:11], v[16:19], v[160:163], a[8:11]\n\tbuffer_load_dwordx4 v11, s[12:15], s69 offen lds\n\ts_add_u32 s68, s44, 1032192\n\tv_mfma_f32_16x16x32_bf16 a[16:19], v[16:19], v[164:167], a[16:19]\n\ts_waitcnt lgkmcnt(8)\n\tv_mfma_f32_16x16x32_bf16 a[4:7], v[20:23], v[156:159], a[4:7]\n\t" \
    "ds_read_b64_tr_b16 v[40:41], v2 offset:28672\n\tds_read_b64_tr_b16 v[42:43], v2 offset:29184\n\tv_mfma_f32_16x16x32_bf16 a[12:15], v[20:23], v[160:163], a[12:15]\n\tbuffer_load_dwordx4 a[92:95], v8, s[8:11], s68 offen offset:0 sc1\n\tv_mfma_f32_16x16x32_bf16 a[20:23], v[20:23], v[164:167], a[20:23]\n\ts_waitcnt lgkmcnt(8)\n\tv_mfma_f32_16x16x32_bf16 v[48:51], v[24:27], v[156:159], v[48:51]\n\tds_read_b64_tr_b16 v[44:45], v3 offset:28672\n\t" \
    "ds_read_b64_tr_b16 v[46:47], v3 offset:29184\n\tv_mfma_f32_16x16x32_bf16 v[56:59], v[24:27], v[160:163], v[56:59]\n\tbuffer_load_dwordx4 a[96:99], v8, s[8:11], s68 offen offset:1024 sc1\n\tv_mfma_f32_16x16x32_bf16 v[64:67], v[24:27], v[164:167], v[64:67]\n\ts_waitcnt lgkmcnt(8)\n\tv_mfma_f32_16x16x32_bf16 v[52:55], v[28:31], v[156:159], v[52:55]\n\tds_read_b64_tr_b16 v[16:17], v0 offset:32768\n\tds_read_b64_tr_b16 v[18:19], v0 offset:33280\n\t" \
    "v_mfma_f32_16x16x32_bf16 v[60:63], v[28:31], v[160:163], v[60:63]\n\tbuffer_load_dwordx4 a[100:103], v8, s[8:11], s68 offen offset:2048 sc1\n\ts_add_u32 s68, s44, 1069056\n\tv_mfma_f32_16x16x32_bf16 v[68:71], v[28:31], v[164:167], v[68:71]\n\ts_waitcnt vmcnt(27) lgkmcnt(8)\n\tv_mfma_f32_16x16x32_bf16 a[0:3], v[32:35], v[216:219], a[0:3]\n\tds_read_b64_tr_b16 v[20:21], v1 offset:32768\n\tds_read_b64_tr_b16 v[22:23], v1 offset:33280\n\t" \
    "v_mfma_f32_16x16x32_bf16 a[8:11], v[32:35], a[48:51], a[8:11]\n\tv_mfma_f32_16x16x32_bf16 a[16:19], v[32:35], a[52:55], a[16:19]\n\ts_waitcnt lgkmcnt(8)\n\tv_mfma_f32_16x16x32_bf16 a[4:7], v[36:39], v[216:219], a[4:7]\n\tds_read_b64_tr_b16 v[24:25], v2 offset:32768\n\tds_read_b64_tr_b16 v[26:27], v2 offset:33280\n\tv_mfma_f32_16x16x32_bf16 a[12:15], v[36:39], a[48:51], a[12:15]\n\tbuffer_load_dwordx4 a[104:107], v8, s[8:11], s68 offen offset:0 sc1\n\t" \
    "v_mfma_f32_16x16x32_bf16 a[20:23], v[36:39], a[52:55], a[20:23]\n\ts_waitcnt lgkmcnt(8)\n\tv_mfma_f32_16x16x32_bf16 v[48:51], v[40:43], v[216:219], v[48:51]\n\tds_read_b64_tr_b16 v[28:29], v3 offset:32768\n\tds_read_b64_tr_b16 v[30:31], v3 offset:33280\n\tv_mfma_f32_16x16x32_bf16 v[56:59], v[40:43], a[48:51], v[56:59]\n\tbuffer_load_dwordx4 a[108:111], v8, s[8:11], s68 offen offset:1024 sc1\n\tv_mfma_f32_16x16x32_bf16 v[64:67], v[40:43], a[52:55], v[64:67]\n\t" \
    "s_waitcnt lgkmcnt(8)\n\tv_mfma_f32_16x16x32_bf16 v[52:55], v[44:47], v[216:219], v[52:55]\n\tds_read_b64_tr_b16 v[32:33], v0 offset:36864\n\tds_read_b64_tr_b16 v[34:35], v0 offset:37376\n\tv_mfma_f32_16x16x32_bf16 v[60:63], v[44:47], a[48:51], v[60:63]\n\tv_mfma_f32_16x16x32_bf16 v[68:71], v[44:47], a[52:55], v[68:71]\n\ts_waitcnt vmcnt(25)\n\ts_barrier\n\t" \
    "s_waitcnt vmcnt(22) lgkmcnt(8)\n\tv_mfma_f32_16x16x32_bf16 a[0:3], v[16:19], a[56:59], a[0:3]\n\tds_read_b64_tr_b16 v[36:37], v1 offset:36864\n\tds_read_b64_tr_b16 v[38:39], v1 offset:37376\n\tv_mfma_f32_16x16x32_bf16 a[8:11], v[16:19], a[60:63], a[8:11]\n\tbuffer_load_dwordx4 a[112:115], v8, s[8:11], s68 offen offset:2048 sc1\n\ts_add_u32 s68, s44, 1105920\n\tv_mfma_f32_16x16x32_bf16 a[16:19], v[16:19], a[64:67], a[16:19]\n\t" \
    "s_waitcnt lgkmcnt(8)\n\tv_mfma_f32_16x16x32_bf16 a[4:7], v[20:23], a[56:59], a[4:7]\n\tds_read_b64_tr_b16 v[40:41], v2 offset:36864\n\tds_read_b64_tr_b16 v[42:43], v2 offset:37376\n\tv_mfma_f32_16x16x32_bf16 a[12:15], v[20:23], a[60:63], a[12:15]\n\tbuffer_load_dwordx4 a[116:119], v8, s[8:11], s68 offen offset:0 sc1\n\tv_mfma_f32_16x16x32_bf16 a[20:23], v[20:23], a[64:67], a[20:23]\n\ts_waitcnt lgkmcnt(8)\n\t" \
    "v_mfma_f32_16x16x32_bf16 v[48:51], v[24:27], a[56:59], v[48:51]\n\tds_read_b64_tr_b16 v[44:45], v3 offset:36864\n\tds_read_b64_tr_b16 v[46:47], v3 offset:37376\n\tv_mfma_f32_16x16x32_bf16 v[56:59], v[24:27], a[60:63], v[56:59]\n\tbuffer_load_dwordx4 a[120:123], v8, s[8:11], s68 offen offset:1024 sc1\n\tv_mfma_f32_16x16x32_bf16 v[64:67], v[24:27], a[64:67], v[64:67]\n\ts_waitcnt lgkmcnt(8)\n\tv_mfma_f32_16x16x32_bf16 v[52:55], v[28:31], a[56:59], v[52:55]\n\t" \
    "ds_read_b64_tr_b16 v[16:17], v0 offset:40960\n\tds_read_b64_tr_b16 v[18:19], v0 offset:41472\n\tv_mfma_f32_16x16x32_bf16 v[60:63], v[28:31], a[60:63], v[60:63]\n\tbuffer_load_dwordx4 a[124:127], v8, s[8:11], s68 offen offset:2048 sc1\n\ts_add_u32 s68, s44, 1142784\n\tv_mfma_f32_16x16x32_bf16 v[68:71], v[28:31], a[64:67], v[68:71]\n\ts_waitcnt vmcnt(23) lgkmcnt(8)\n\tv_mfma_f32_16x16x32_bf16 a[0:3], v[32:35], v[168:171], a[0:3]\n\t" \
    "ds_read_b64_tr_b16 v[20:21], v1 offset:40960\n\tds_read_b64_tr_b16 v[22:23], v1 offset:41472\n\tv_mfma_f32_16x16x32_bf16 a[8:11], v[32:35], v[172:175], a[8:11]\n\tv_mfma_f32_16x16x32_bf16 a[16:19], v[32:35], v[176:179], a[16:19]\n\ts_waitcnt lgkmcnt(8)\n\tv_mfma_f32_16x16x32_bf16 a[4:7], v[36:39], v[168:171], a[4:7]\n\tds_read_b64_tr_b16 v[24:25], v2 offset:40960\n\tds_read_b64_tr_b16 v[26:27], v2 offset:41472\n\t" \
    "v_mfma_f32_16x16x32_bf16 a[12:15], v[36:39], v[172:175], a[12:15]\n\tbuffer_load_dwordx4 a[128:131], v8, s[8:11], s68 offen offset:0 sc1\n\tv_mfma_f32_16x16x32_bf16 a[20:23], v[36:39], v[176:179], a[20:23]\n\ts_waitcnt lgkmcnt(8)\n\tv_mfma_f32_16x16x32_bf16 v[48:51], v[40:43], v[168:171], v[48:51]\n\tds_read_b64_tr_b16 v[28:29], v3 offset:40960\n\tds_read_b64_tr_b16 v[30:31], v3 offset:41472\n\tv_mfma_f32_16x16x32_bf16 v[56:59], v[40:43], v[172:175], v[56:59]\n\t" \
    "buffer_load_dwordx4 a[132:135], v8, s[8:11], s68 offen offset:1024 sc1\n\tv_mfma_f32_16x16x32_bf16 v[64:67], v[40:43], v[176:179], v[64:67]\n\ts_waitcnt lgkmcnt(8)\n\tv_mfma_f32_16x16x32_bf16 v[52:55], v[44:47], v[168:171], v[52:55]\n\tds_read_b64_tr_b16 v[32:33], v0 offset:45056\n\tds_read_b64_tr_b16 v[34:35], v0 offset:45568\n\tv_mfma_f32_16x16x32_bf16 v[60:63], v[44:47], v[172:175], v[60:63]\n\tbuffer_load_dwordx4 a[136:139], v8, s[8:11], s68 offen offset:2048 sc1\n\t" \
    "s_mov_b32 s69, 4718720\n\ts_add_u32 m0, s72, 0\n\tv_mfma_f32_16x16x32_bf16 v[68:71], v[44:47], v[176:179], v[68:71]\n\ts_waitcnt vmcnt(20)\n\ts_barrier\n\ts_waitcnt vmcnt(17) lgkmcnt(8)\n\tv_mfma_f32_16x16x32_bf16 a[0:3], v[16:19], a[68:71], a[0:3]\n\tds_read_b64_tr_b16 v[36:37], v1 offset:45056\n\t" \
    "ds_read_b64_tr_b16 v[38:39], v1 offset:45568\n\tv_mfma_f32_16x16x32_bf16 a[8:11], v[16:19], a[72:75], a[8:11]\n\tbuffer_load_dwordx4 v10, s[12:15], s69 offen lds\n\ts_add_u32 m0, s72, 1024\n\tv_mfma_f32_16x16x32_bf16 a[16:19], v[16:19], a[76:79], a[16:19]\n\ts_waitcnt lgkmcnt(8)\n\tv_mfma_f32_16x16x32_bf16 a[4:7], v[20:23], a[68:71], a[4:7]\n\tds_read_b64_tr_b16 v[40:41], v2 offset:45056\n\t" \
    "ds_read_b64_tr_b16 v[42:43], v2 offset:45568\n\tv_mfma_f32_16x16x32_bf16 a[12:15], v[20:23], a[72:75], a[12:15]\n\tbuffer_load_dwordx4 v11, s[12:15], s69 offen lds\n\ts_mov_b32 s69, 5013632\n\ts_add_u32 m0, s72, 4096\n\tv_mfma_f32_16x16x32_bf16 a[20:23], v[20:23], a[76:79], a[20:23]\n\ts_waitcnt lgkmcnt(8)\n\tv_mfma_f32_16x16x32_bf16 v[48:51], v[24:27], a[68:71], v[48:51]\n\t" \
    "ds_read_b64_tr_b16 v[44:45], v3 offset:45056\n\tds_read_b64_tr_b16 v[46:47], v3 offset:45568\n\tv_mfma_f32_16x16x32_bf16 v[56:59], v[24:27], a[72:75], v[56:59]\n\tv_mfma_f32_16x16x32_bf16 v[64:67], v[24:27], a[76:79], v[64:67]\n\ts_waitcnt lgkmcnt(8)\n\tv_mfma_f32_16x16x32_bf16 v[52:55], v[28:31], a[68:71], v[52:55]\n\tds_read_b64_tr_b16 v[16:17], v0 offset:49152\n\tds_read_b64_tr_b16 v[18:19], v0 offset:49664\n\t" \
    "v_mfma_f32_16x16x32_bf16 v[60:63], v[28:31], a[72:75], v[60:63]\n\tbuffer_load_dwordx4 v10, s[12:15], s69 offen lds\n\ts_add_u32 m0, s72, 5120\n\tv_mfma_f32_16x16x32_bf16 v[68:71], v[28:31], a[76:79], v[68:71]\n\ts_waitcnt vmcnt(17) lgkmcnt(8)\n\tv_mfma_f32_16x16x32_bf16 a[0:3], v[32:35], a[80:83], a[0:3]\n\tds_read_b64_tr_b16 v[20:21], v1 offset:49152\n\tds_read_b64_tr_b16 v[22:23], v1 offset:49664\n\t" \
    "v_mfma_f32_16x16x32_bf16 a[8:11], v[32:35], a[84:87], a[8:11]\n\tbuffer_load_dwordx4 v11, s[12:15], s69 offen lds\n\ts_mov_b32 s69, 5308544\n\ts_add_u32 m0, s72, 8192\n\tv_mfma_f32_16x16x32_bf16 a[16:19], v[32:35], a[88:91], a[16:19]\n\ts_waitcnt lgkmcnt(8)\n\tv_mfma_f32_16x16x32_bf16 a[4:7], v[36:39], a[80:83], a[4:7]\n\tds_read_b64_tr_b16 v[24:25], v0 offset:53248\n\t" \
    "ds_read_b64_tr_b16 v[26:27], v0 offset:53760\n\tv_mfma_f32_16x16x32_bf16 a[12:15], v[36:39], a[84:87], a[12:15]\n\tv_mfma_f32_16x16x32_bf16 a[20:23], v[36:39], a[88:91], a[20:23]\n\ts_waitcnt lgkmcnt(8)\n\tv_mfma_f32_16x16x32_bf16 v[48:51], v[40:43], a[80:83], v[48:51]\n\tds_read_b64_tr_b16 v[28:29], v1 offset:53248\n\tds_read_b64_tr_b16 v[30:31], v1 offset:53760\n\tv_mfma_f32_16x16x32_bf16 v[56:59], v[40:43], a[84:87], v[56:59]\n\t" \
    "buffer_load_dwordx4 v10, s[12:15], s69 offen lds\n\ts_add_u32 m0, s72, 9216\n\tv_mfma_f32_16x16x32_bf16 v[64:67], v[40:43], a[88:91], v[64:67]\n\ts_waitcnt lgkmcnt(8)\n\tv_mfma_f32_16x16x32_bf16 v[52:55], v[44:47], a[80:83], v[52:55]\n\tds_read_b64_tr_b16 v[32:33], v0 offset:57344\n\tds_read_b64_tr_b16 v[34:35], v0 offset:57856\n\tv_mfma_f32_16x16x32_bf16 v[60:63], v[44:47], a[84:87], v[60:63]\n\t" \
    "buffer_load_dwordx4 v11, s[12:15], s69 offen lds\n\ts_mov_b32 s69, 5603456\n\ts_add_u32 m0, s72, 12288\n\tv_mfma_f32_16x16x32_bf16 v[68:71], v[44:47], a[88:91], v[68:71]\n\ts_waitcnt vmcnt(18)\n\ts_barrier\n\ts_waitcnt vmcnt(15) lgkmcnt(8)\n\tv_mfma_f32_16x16x32_bf16 a[0:3], v[16:19], a[92:95], a[0:3]\n\t" \
    "ds_read_b64_tr_b16 v[36:37], v1 offset:57344\n\tds_read_b64_tr_b16 v[38:39], v1 offset:57856\n\tv_mfma_f32_16x16x32_bf16 a[8:11], v[16:19], a[96:99], a[8:11]\n\tbuffer_load_dwordx4 v10, s[12:15], s69 offen lds\n\ts_add_u32 m0, s72, 13312\n\tv_mfma_f32_16x16x32_bf16 a[16:19], v[16:19], a[100:103], a[16:19]\n\ts_waitcnt lgkmcnt(8)\n\tv_mfma_f32_16x16x32_bf16 a[4:7], v[20:23], a[92:95], a[4:7]\n\t" \
    "ds_read_b64_tr_b16 v[40:41], v0 offset:61440\n\tds_read_b64_tr_b16 v[42:43], v0 offset:61952\n\tv_mfma_f32_16x16x32_bf16 a[12:15], v[20:23], a[96:99], a[12:15]\n\tbuffer_load_dwordx4 v11, s[12:15], s69 offen lds\n\ts_mov_b32 s69, 5898368\n\ts_add_u32 m0, s72, 16384\n\tv_mfma_f32_16x16x32_bf16 a[20:23], v[20:23], a[100:103], a[20:23]\n\ts_waitcnt vmcnt(14) lgkmcnt(8)\n\t" \
    "v_mfma_f32_16x16x32_bf16 a[0:3], v[24:27], a[104:107], a[0:3]\n\tds_read_b64_tr_b16 v[44:45], v1 offset:61440\n\tds_read_b64_tr_b16 v[46:47], v1 offset:61952\n\tv_mfma_f32_16x16x32_bf16 a[8:11], v[24:27], a[108:111], a[8:11]\n\tbuffer_load_dwordx4 v10, s[12:15], s69 offen lds\n\ts_add_u32 m0, s72, 17408\n\tv_mfma_f32_16x16x32_bf16 a[16:19], v[24:27], a[112:115], a[16:19]\n\ts_waitcnt lgkmcnt(8)\n\t" \
    "v_mfma_f32_16x16x32_bf16 a[4:7], v[28:31], a[104:107], a[4:7]\n\tds_read_b64_tr_b16 v[16:17], v2 offset:49152\n\tds_read_b64_tr_b16 v[18:19], v2 offset:49664\n\tv_mfma_f32_16x16x32_bf16 a[12:15], v[28:31], a[108:111], a[12:15]\n\tv_mfma_f32_16x16x32_bf16 a[20:23], v[28:31], a[112:115], a[20:23]\n\ts_waitcnt vmcnt(12) lgkmcnt(8)\n\tv_mfma_f32_16x16x32_bf16 a[0:3], v[32:35], a[116:119], a[0:3]\n\tds_read_b64_tr_b16 v[20:21], v3 offset:49152\n\t" \
    "ds_read_b64_tr_b16 v[22:23], v3 offset:49664\n\tv_mfma_f32_16x16x32_bf16 a[8:11], v[32:35], a[120:123], a[8:11]\n\tbuffer_load_dwordx4 v11, s[12:15], s69 offen lds\n\ts_mov_b32 s69, 6193280\n\ts_add_u32 m0, s72, 20480\n\tv_mfma_f32_16x16x32_bf16 a[16:19], v[32:35], a[124:127], a[16:19]\n\ts_waitcnt lgkmcnt(8)\n\tv_mfma_f32_16x16x32_bf16 a[4:7], v[36:39], a[116:119], a[4:7]\n\t" \
    "ds_read_b64_tr_b16 v[24:25], v2 offset:53248\n\tds_read_b64_tr_b16 v[26:27], v2 offset:53760\n\tv_mfma_f32_16x16x32_bf16 a[12:15], v[36:39], a[120:123], a[12:15]\n\tbuffer_load_dwordx4 v10, s[12:15], s69 offen lds\n\ts_add_u32 m0, s72, 21504\n\tv_mfma_f32_16x16x32_bf16 a[20:23], v[36:39], a[124:127], a[20:23]\n\ts_waitcnt vmcnt(11) lgkmcnt(8)\n\tv_mfma_f32_16x16x32_bf16 a[0:3], v[40:43], a[128:131], a[0:3]\n\t" \
    "ds_read_b64_tr_b16 v[28:29], v3 offset:53248\n\tds_read_b64_tr_b16 v[30:31], v3 offset:53760\n\tv_mfma_f32_16x16x32_bf16 a[8:11], v[40:43], a[132:135], a[8:11]\n\tbuffer_load_dwordx4 v11, s[12:15], s69 offen lds\n\ts_mov_b32 s69, 6488192\n\ts_add_u32 m0, s72, 24576\n\tv_mfma_f32_16x16x32_bf16 a[16:19], v[40:43], a[136:139], a[16:19]\n\ts_waitcnt lgkmcnt(8)\n\t" \
    "v_mfma_f32_16x16x32_bf16 a[4:7], v[44:47], a[128:131], a[4:7]\n\tds_read_b64_tr_b16 v[32:33], v2 offset:57344\n\tds_read_b64_tr_b16 v[34:35], v2 offset:57856\n\tv_mfma_f32_16x16x32_bf16 a[12:15], v[44:47], a[132:135], a[12:15]\n\tbuffer_load_dwordx4 v10, s[12:15], s69 offen lds\n\ts_add_u32 m0, s72, 25600\n\tv_mfma_f32_16x16x32_bf16 a[20:23], v[44:47], a[136:139], a[20:23]\n\ts_waitcnt vmcnt(11)\n\t" \
    "s_barrier\n\ts_waitcnt lgkmcnt(8)\n\tv_mfma_f32_16x16x32_bf16 v[48:51], v[16:19], a[92:95], v[48:51]\n\tds_read_b64_tr_b16 v[36:37], v3 offset:57344\n\tds_read_b64_tr_b16 v[38:39], v3 offset:57856\n\tv_mfma_f32_16x16x32_bf16 v[56:59], v[16:19], a[96:99], v[56:59]\n\tbuffer_load_dwordx4 v11, s[12:15], s69 offen lds\n\ts_mov_b32 s69, 6783104\n\t" \
    "s_add_u32 m0, s72, 28672\n\tv_mfma_f32_16x16x32_bf16 v[64:67], v[16:19], a[100:103], v[64:67]\n\tds_write_b128 v237, a[0:3] offset:0\n\ts_waitcnt lgkmcnt(9)\n\tv_mfma_f32_16x16x32_bf16 v[52:55], v[20:23], a[92:95], v[52:55]\n\tds_read_b64_tr_b16 v[40:41], v2 offset:61440\n\tds_read_b64_tr_b16 v[42:43], v2 offset:61952\n\tv_mfma_f32_16x16x32_bf16 v[60:63], v[20:23], a[96:99], v[60:63]\n\t" \
    "buffer_load_dwordx4 v10, s[12:15], s69 offen lds\n\ts_add_u32 m0, s72, 29696\n\tv_mfma_f32_16x16x32_bf16 v[68:71], v[20:23], a[100:103], v[68:71]\n\tds_write_b128 v237, a[8:11] offset:2048\n\ts_waitcnt lgkmcnt(10)\n\tv_mfma_f32_16x16x32_bf16 v[48:51], v[24:27], a[104:107], v[48:51]\n\tds_read_b64_tr_b16 v[44:45], v3 offset:61440\n\tds_read_b64_tr_b16 v[46:47], v3 offset:61952\n\t" \
    "v_mfma_f32_16x16x32_bf16 v[56:59], v[24:27], a[108:111], v[56:59]\n\tbuffer_load_dwordx4 v11, s[12:15], s69 offen lds\n\ts_mov_b32 s69, 7078016\n\ts_add_u32 m0, s72, 32768\n\tv_mfma_f32_16x16x32_bf16 v[64:67], v[24:27], a[112:115], v[64:67]\n\tds_write_b128 v237, a[16:19] offset:4096\n\ts_waitcnt lgkmcnt(11)\n\tv_mfma_f32_16x16x32_bf16 v[52:55], v[28:31], a[104:107], v[52:55]\n\t" \
    "ds_read_b64_tr_b16 v[16:17], v0 offset:0\n\tds_read_b64_tr_b16 v[18:19], v0 offset:512\n\tv_mfma_f32_16x16x32_bf16 v[60:63], v[28:31], a[108:111], v[60:63]\n\tbuffer_load_dwordx4 v10, s[12:15], s69 offen lds\n\ts_add_u32 m0, s72, 33792\n\tv_mfma_f32_16x16x32_bf16 v[68:71], v[28:31], a[112:115], v[68:71]\n\tds_write_b128 v237, a[4:7] offset:1024\n\tds_write_b128 v237, a[12:15] offset:3072\n\t" \
    "ds_write_b128 v237, a[20:23] offset:5120\n\ts_waitcnt vmcnt(13) lgkmcnt(0)\n\ts_barrier\n\tv_mfma_f32_16x16x32_bf16 v[48:51], v[32:35], a[116:119], v[48:51]\n\tds_read_b64_tr_b16 v[20:21], v1 offset:0\n\tds_read_b64_tr_b16 v[22:23], v1 offset:512\n\tv_mfma_f32_16x16x32_bf16 v[56:59], v[32:35], a[120:123], v[56:59]\n\tbuffer_load_dwordx4 v11, s[12:15], s69 offen lds\n\t" \
    "ds_read_b128 v[96:99], v238 offset:0\n\ts_mov_b32 s69, 7372928\n\tv_mfma_f32_16x16x32_bf16 v[64:67], v[32:35], a[124:127], v[64:67]\n\tds_read_b128 v[100:103], v238 offset:1024\n\tds_read_b128 v[104:107], v238 offset:2048\n\tv_mfma_f32_16x16x32_bf16 v[52:55], v[36:39], a[116:119], v[52:55]\n\tds_read_b64_tr_b16 v[24:25], v2 offset:0\n\tds_read_b64_tr_b16 v[26:27], v2 offset:512\n\t" \
    "s_add_u32 m0, s72, 36864\n\tv_mfma_f32_16x16x32_bf16 v[60:63], v[36:39], a[120:123], v[60:63]\n\tbuffer_load_dwordx4 v10, s[12:15], s69 offen lds\n\tds_read_b128 v[108:111], v238 offset:3072\n\ts_add_u32 m0, s72, 37888\n\tv_mfma_f32_16x16x32_bf16 v[68:71], v[36:39], a[124:127], v[68:71]\n\tds_read_b128 v[112:115], v238 offset:4096\n\tds_read_b128 v[116:119], v238 offset:5120\n\t" \
    "v_mfma_f32_16x16x32_bf16 v[48:51], v[40:43], a[128:131], v[48:51]\n\tds_read_b64_tr_b16 v[28:29], v3 offset:0\n\tds_read_b64_tr_b16 v[30:31], v3 offset:512\n\tv_mfma_f32_16x16x32_bf16 v[56:59], v[40:43], a[132:135], v[56:59]\n\tbuffer_load_dwordx4 v11, s[12:15], s69 offen lds\n\ts_mov_b32 s69, 7667840\n\ts_add_u32 m0, s72, 40960\n\tv_mfma_f32_16x16x32_bf16 v[64:67], v[40:43], a[136:139], v[64:67]\n\t" \
    "v_mfma_f32_16x16x32_bf16 v[52:55], v[44:47], a[128:131], v[52:55]\n\tds_read_b64_tr_b16 v[32:33], v0 offset:4096\n\tds_read_b64_tr_b16 v[34:35], v0 offset:4608\n\tv_mfma_f32_16x16x32_bf16 v[60:63], v[44:47], a[132:135], v[60:63]\n\tbuffer_load_dwordx4 v10, s[12:15], s69 offen lds\n\ts_add_u32 m0, s72, 41984\n\tv_mfma_f32_16x16x32_bf16 v[68:71], v[44:47], a[136:139], v[68:71]\n\ts_waitcnt lgkmcnt(10)\n\t" \
    "v_pk_add_f32 v[48:49], v[48:49], v[96:97]\n\tv_pk_add_f32 v[50:51], v[50:51], v[98:99]\n\tv_pk_add_f32 v[52:53], v[52:53], v[100:101]\n\tbuffer_load_dwordx4 v11, s[12:15], s69 offen lds\n\tv_pk_add_f32 v[54:55], v[54:55], v[102:103]\n\ts_mov_b32 s69, 7962752\n\tv_pk_mul_f32 v[96:97], v[48:49], v[48:49]\n\ts_add_u32 m0, s72, 45056\n\t" \
    "v_pk_mul_f32 v[98:99], v[50:51], v[50:51]\n\tv_pk_mul_f32 v[100:101], v[52:53], v[52:53]\n\tv_pk_mul_f32 v[102:103], v[54:55], v[54:55]\n\tv_pk_fma_f32 v[96:97], v[96:97], v[246:247], v[246:247] op_sel:[0,0,1] op_sel_hi:[1,0,1]\n\tv_pk_fma_f32 v[98:99], v[98:99], v[246:247], v[246:247] op_sel:[0,0,1] op_sel_hi:[1,0,1]\n\tv_pk_fma_f32 v[100:101], v[100:101], v[246:247], v[246:247] op_sel:[0,0,1] op_sel_hi:[1,0,1]\n\tv_pk_fma_f32 v[102:103], v[102:103], v[246:247], v[246:247] op_sel:[0,0,1] op_sel_hi:[1,0,1]\n\tv_pk_mul_f32 v[96:97], v[96:97], v[48:49]\n\t" \
    "v_pk_mul_f32 v[98:99], v[98:99], v[50:51]\n\tbuffer_load_dwordx4 v10, s[12:15], s69 offen lds\n\tv_pk_mul_f32 v[100:101], v[100:101], v[52:53]\n\ts_add_u32 m0, s72, 46080\n\tv_pk_mul_f32 v[102:103], v[102:103], v[54:55]\n\ts_waitcnt lgkmcnt(6)\n\tv_exp_f32 v96, v96\n\tv_pk_add_f32 v[56:57], v[56:57], v[104:105]\n\t" \
    "v_exp_f32 v97, v97\n\tv_pk_add_f32 v[58:59], v[58:59], v[106:107]\n\tv_exp_f32 v98, v98\n\tv_pk_add_f32 v[60:61], v[60:61], v[108:109]\n\tv_exp_f32 v99, v99\n\tbuffer_load_dwordx4 v11, s[12:15], s69 offen lds\n\tv_pk_add_f32 v[62:63], v[62:63], v[110:111]\n\ts_mov_b32 s69, 8257664\n\t" \
    "v_exp_f32 v100, v100\n\ts_add_u32 m0, s72, 49152\n\tv_pk_mul_f32 v[104:105], v[56:57], v[56:57]\n\tv_exp_f32 v101, v101\n\tv_pk_mul_f32 v[106:107], v[58:59], v[58:59]\n\tv_exp_f32 v102, v102\n\tv_pk_mul_f32 v[108:109], v[60:61], v[60:61]\n\tv_exp_f32 v103, v103\n\t" \
    "v_pk_mul_f32 v[110:111], v[62:63], v[62:63]\n\tv_pk_add_f32 v[96:97], v[96:97], 1.0 op_sel_hi:[1,0]\n\tbuffer_load_dwordx4 v10, s[12:15], s69 offen lds\n\tv_pk_add_f32 v[98:99], v[98:99], 1.0 op_sel_hi:[1,0]\n\ts_add_u32 m0, s72, 50176\n\tv_pk_add_f32 v[100:101], v[100:101], 1.0 op_sel_hi:[1,0]\n\tv_pk_add_f32 v[102:103], v[102:103], 1.0 op_sel_hi:[1,0]\n\tv_pk_fma_f32 v[104:105], v[104:105], v[246:247], v[246:247] op_sel:[0,0,1] op_sel_hi:[1,0,1]\n\t" \
    "v_rcp_f32 v96, v96\n\tv_pk_fma_f32 v[106:107], v[106:107], v[246:247], v[246:247] op_sel:[0,0,1] op_sel_hi:[1,0,1]\n\tv_rcp_f32 v97, v97\n\tv_pk_fma_f32 v[108:109], v[108:109], v[246:247], v[246:247] op_sel:[0,0,1] op_sel_hi:[1,0,1]\n\tv_rcp_f32 v98, v98\n\tv_pk_fma_f32 v[110:111], v[110:111], v[246:247], v[246:247] op_sel:[0,0,1] op_sel_hi:[1,0,1]\n\tv_rcp_f32 v99, v99\n\tbuffer_load_dwordx4 v11, s[12:15], s69 offen lds\n\t" \
    "v_pk_mul_f32 v[104:105], v[104:105], v[56:57]\n\ts_mov_b32 s69, 8552576\n\tv_rcp_f32 v100, v100\n\ts_add_u32 m0, s72, 53248\n\tv_pk_mul_f32 v[106:107], v[106:107], v[58:59]\n\tv_rcp_f32 v101, v101\n\tv_pk_mul_f32 v[108:109], v[108:109], v[60:61]\n\tv_rcp_f32 v102, v102\n\t" \
    "v_pk_mul_f32 v[110:111], v[110:111], v[62:63]\n\tv_rcp_f32 v103, v103\n\ts_waitcnt lgkmcnt(4)\n\tv_exp_f32 v104, v104\n\tbuffer_load_dwordx4 v10, s[12:15], s69 offen lds\n\tv_pk_mul_f32 v[96:97], v[96:97], v[48:49]\n\ts_add_u32 m0, s72, 54272\n\tv_exp_f32 v105, v105\n\t" \
    "v_pk_mul_f32 v[98:99], v[98:99], v[50:51]\n\tv_exp_f32 v106, v106\n\tv_pk_mul_f32 v[100:101], v[100:101], v[52:53]\n\tv_exp_f32 v107, v107\n\tv_pk_mul_f32 v[102:103], v[102:103], v[54:55]\n\tv_exp_f32 v108, v108\n\tbuffer_load_dwordx4 v11, s[12:15], s69 offen lds\n\tv_cvt_pk_bf16_f32 v48, v96, v97\n\t" \
    "v_exp_f32 v109, v109\n\tv_cvt_pk_bf16_f32 v49, v98, v99\n\tv_exp_f32 v110, v110\n\tv_cvt_pk_bf16_f32 v50, v100, v101\n\tv_exp_f32 v111, v111\n\tv_cvt_pk_bf16_f32 v51, v102, v103\n\tbuffer_store_dwordx4 v[48:51], v8, s[8:11], s45 offen offset:0 sc1\n\tv_pk_add_f32 v[104:105], v[104:105], 1.0 op_sel_hi:[1,0]\n\t" \
    "v_pk_add_f32 v[106:107], v[106:107], 1.0 op_sel_hi:[1,0]\n\tv_pk_add_f32 v[108:109], v[108:109], 1.0 op_sel_hi:[1,0]\n\tv_pk_add_f32 v[110:111], v[110:111], 1.0 op_sel_hi:[1,0]\n\tv_pk_add_f32 v[64:65], v[64:65], v[112:113]\n\tv_rcp_f32 v104, v104\n\tv_pk_add_f32 v[66:67], v[66:67], v[114:115]\n\tv_rcp_f32 v105, v105\n\tv_pk_add_f32 v[68:69], v[68:69], v[116:117]\n\t" \
    "v_rcp_f32 v106, v106\n\tv_pk_add_f32 v[70:71], v[70:71], v[118:119]\n\tv_rcp_f32 v107, v107\n\tv_pk_mul_f32 v[112:113], v[64:65], v[64:65]\n\tv_rcp_f32 v108, v108\n\tv_pk_mul_f32 v[114:115], v[66:67], v[66:67]\n\tv_rcp_f32 v109, v109\n\tv_pk_mul_f32 v[116:117], v[68:69], v[68:69]\n\t" \
    "buffer_store_dwordx2 v[248:249], off, s[8:11], s60 sc1\n\tv_rcp_f32 v110, v110\n\tv_pk_mul_f32 v[118:119], v[70:71], v[70:71]\n\tv_rcp_f32 v111, v111\n\tv_pk_fma_f32 v[112:113], v[112:113], v[246:247], v[246:247] op_sel:[0,0,1] op_sel_hi:[1,0,1]\n\tv_pk_mul_f32 v[104:105], v[104:105], v[56:57]\n\tv_pk_mul_f32 v[106:107], v[106:107], v[58:59]\n\tv_pk_mul_f32 v[108:109], v[108:109], v[60:61]\n\t" \
    "v_pk_mul_f32 v[110:111], v[110:111], v[62:63]\n\tv_cvt_pk_bf16_f32 v56, v104, v105\n\tv_cvt_pk_bf16_f32 v57, v106, v107\n\tv_cvt_pk_bf16_f32 v58, v108, v109\n\tv_cvt_pk_bf16_f32 v59, v110, v111\n\tbuffer_store_dwordx4 v[56:59], v8, s[8:11], s45 offen offset:1024 sc1\n\tv_pk_fma_f32 v[114:115], v[114:115], v[246:247], v[246:247] op_sel:[0,0,1] op_sel_hi:[1,0,1]\n\tv_pk_fma_f32 v[116:117], v[116:117], v[246:247], v[246:247] op_sel:[0,0,1] op_sel_hi:[1,0,1]\n\t" \
    "v_pk_fma_f32 v[118:119], v[118:119], v[246:247], v[246:247] op_sel:[0,0,1] op_sel_hi:[1,0,1]\n\tv_pk_mul_f32 v[112:113], v[112:113], v[64:65]\n\tv_pk_mul_f32 v[114:115], v[114:115], v[66:67]\n\tv_pk_mul_f32 v[116:117], v[116:117], v[68:69]\n\tv_pk_mul_f32 v[118:119], v[118:119], v[70:71]\n\tv_exp_f32 v112, v112\n\tv_exp_f32 v113, v113\n\tv_exp_f32 v114, v114\n\t" \
    "v_exp_f32 v115, v115\n\tv_exp_f32 v116, v116\n\tv_exp_f32 v117, v117\n\tv_exp_f32 v118, v118\n\tv_exp_f32 v119, v119\n\tv_pk_add_f32 v[112:113], v[112:113], 1.0 op_sel_hi:[1,0]\n\tv_pk_add_f32 v[114:115], v[114:115], 1.0 op_sel_hi:[1,0]\n\tv_pk_add_f32 v[116:117], v[116:117], 1.0 op_sel_hi:[1,0]\n\t" \
    "v_pk_add_f32 v[118:119], v[118:119], 1.0 op_sel_hi:[1,0]\n\tv_rcp_f32 v112, v112\n\tv_rcp_f32 v113, v113\n\tv_rcp_f32 v114, v114\n\tv_rcp_f32 v115, v115\n\tv_rcp_f32 v116, v116\n\tv_rcp_f32 v117, v117\n\tv_rcp_f32 v118, v118\n\t" \
    "v_rcp_f32 v119, v119\n\tv_pk_mul_f32 v[112:113], v[112:113], v[64:65]\n\tv_pk_mul_f32 v[114:115], v[114:115], v[66:67]\n\tv_pk_mul_f32 v[116:117], v[116:117], v[68:69]\n\tv_pk_mul_f32 v[118:119], v[118:119], v[70:71]\n\tv_cvt_pk_bf16_f32 v64, v112, v113\n\tv_cvt_pk_bf16_f32 v65, v114, v115\n\tv_cvt_pk_bf16_f32 v66, v116, v117\n\t" \
    "v_cvt_pk_bf16_f32 v67, v118, v119\n\tbuffer_store_dwordx4 v[64:67], v8, s[8:11], s45 offen offset:2048 sc1\n\ts_lshl_b32 s65, s6, 2\n\ts_add_u32 s65, s65, s38\n\ts_add_u32 s65, s65, 0\n\ts_cmp_ge_u32 s65, 18\n\ts_cselect_b32 s66, 1088, 0\n\ts_xor_b32 s65, s65, 1\n\t" \
    "s_and_b32 s67, s65, 1\n\ts_lshl_b32 s67, s67, 5\n\ts_lshr_b32 s65, s65, 1\n\ts_lshl_b32 s65, s65, 7\n\ts_add_u32 s65, s65, s67\n\ts_sub_u32 s65, s65, s66\n\ts_lshl_b32 s65, s65, 12\n\tbuffer_load_dword v15, v14, s[16:19], s65 offen\n\t" \
    "s_waitcnt vmcnt(25)\n\ts_barrier\n\tv_mfma_f32_16x16x32_bf16 a[24:27], v[16:19], v[180:183], a[24:27]\n\tds_read_b64_tr_b16 v[36:37], v1 offset:4096\n\tds_read_b64_tr_b16 v[38:39], v1 offset:4608\n\ts_mov_b32 s69, 8847488\n\tv_mfma_f32_16x16x32_bf16 a[32:35], v[16:19], v[184:187], a[32:35]\n\ts_add_u32 m0, s72, 57344\n\t" \
    "v_mfma_f32_16x16x32_bf16 a[40:43], v[16:19], v[188:191], a[40:43]\n\tv_mfma_f32_16x16x32_bf16 a[28:31], v[20:23], v[180:183], a[28:31]\n\tds_read_b64_tr_b16 v[40:41], v2 offset:4096\n\tds_read_b64_tr_b16 v[42:43], v2 offset:4608\n\tv_mfma_f32_16x16x32_bf16 a[36:39], v[20:23], v[184:187], a[36:39]\n\tv_mfma_f32_16x16x32_bf16 a[44:47], v[20:23], v[188:191], a[44:47]\n\tv_mfma_f32_16x16x32_bf16 v[72:75], v[24:27], v[180:183], v[72:75]\n\tds_read_b64_tr_b16 v[44:45], v3 offset:4096\n\t" \
    "ds_read_b64_tr_b16 v[46:47], v3 offset:4608\n\tv_mfma_f32_16x16x32_bf16 v[80:83], v[24:27], v[184:187], v[80:83]\n\tv_mfma_f32_16x16x32_bf16 v[88:91], v[24:27], v[188:191], v[88:91]\n\ts_waitcnt lgkmcnt(8)\n\tv_mfma_f32_16x16x32_bf16 v[76:79], v[28:31], v[180:183], v[76:79]\n\tds_read_b64_tr_b16 v[16:17], v0 offset:8192\n\tds_read_b64_tr_b16 v[18:19], v0 offset:8704\n\tv_mfma_f32_16x16x32_bf16 v[84:87], v[28:31], v[184:187], v[84:87]\n\t" \
    "v_mfma_f32_16x16x32_bf16 v[92:95], v[28:31], v[188:191], v[92:95]\n\ts_waitcnt lgkmcnt(8)\n\tv_mfma_f32_16x16x32_bf16 a[24:27], v[32:35], v[192:195], a[24:27]\n\tds_read_b64_tr_b16 v[20:21], v1 offset:8192\n\tds_read_b64_tr_b16 v[22:23], v1 offset:8704\n\tv_mfma_f32_16x16x32_bf16 a[32:35], v[32:35], v[196:199], a[32:35]\n\tv_mfma_f32_16x16x32_bf16 a[40:43], v[32:35], v[200:203], a[40:43]\n\ts_waitcnt lgkmcnt(8)\n\t" \
    "v_mfma_f32_16x16x32_bf16 a[28:31], v[36:39], v[192:195], a[28:31]\n\tds_read_b64_tr_b16 v[24:25], v2 offset:8192\n\tds_read_b64_tr_b16 v[26:27], v2 offset:8704\n\tv_mfma_f32_16x16x32_bf16 a[36:39], v[36:39], v[196:199], a[36:39]\n\tv_mfma_f32_16x16x32_bf16 a[44:47], v[36:39], v[200:203], a[44:47]\n\ts_waitcnt lgkmcnt(8)\n\tv_mfma_f32_16x16x32_bf16 v[72:75], v[40:43], v[192:195], v[72:75]\n\tds_read_b64_tr_b16 v[28:29], v3 offset:8192\n\t" \
    "ds_read_b64_tr_b16 v[30:31], v3 offset:8704\n\tv_mfma_f32_16x16x32_bf16 v[80:83], v[40:43], v[196:199], v[80:83]\n\tv_mfma_f32_16x16x32_bf16 v[88:91], v[40:43], v[200:203], v[88:91]\n\ts_waitcnt lgkmcnt(8)\n\tv_mfma_f32_16x16x32_bf16 v[76:79], v[44:47], v[192:195], v[76:79]\n\tds_read_b64_tr_b16 v[32:33], v0 offset:12288\n\tds_read_b64_tr_b16 v[34:35], v0 offset:12800\n\tv_mfma_f32_16x16x32_bf16 v[84:87], v[44:47], v[196:199], v[84:87]\n\t" \
    "v_mfma_f32_16x16x32_bf16 v[92:95], v[44:47], v[200:203], v[92:95]\n\ts_waitcnt vmcnt(21)\n\ts_barrier\n\ts_waitcnt lgkmcnt(8)\n\tv_mfma_f32_16x16x32_bf16 a[24:27], v[16:19], v[120:123], a[24:27]\n\tds_read_b64_tr_b16 v[36:37], v1 offset:12288\n\tds_read_b64_tr_b16 v[38:39], v1 offset:12800\n\tv_mfma_f32_16x16x32_bf16 a[32:35], v[16:19], v[124:127], a[32:35]\n\t" \
    "buffer_load_dwordx4 v10, s[12:15], s69 offen lds\n\ts_add_u32 m0, s72, 58368\n\tv_mfma_f32_16x16x32_bf16 a[40:43], v[16:19], v[128:131], a[40:43]\n\ts_waitcnt lgkmcnt(8)\n\tv_mfma_f32_16x16x32_bf16 a[28:31], v[20:23], v[120:123], a[28:31]\n\tds_read_b64_tr_b16 v[40:41], v2 offset:12288\n\tds_read_b64_tr_b16 v[42:43], v2 offset:12800\n\tv_mfma_f32_16x16x32_bf16 a[36:39], v[20:23], v[124:127], a[36:39]\n\t" \
    "buffer_load_dwordx4 v11, s[12:15], s69 offen lds\n\ts_mov_b32 s69, 9142400\n\ts_add_u32 m0, s72, 61440\n\tv_mfma_f32_16x16x32_bf16 a[44:47], v[20:23], v[128:131], a[44:47]\n\ts_waitcnt lgkmcnt(8)\n\tv_mfma_f32_16x16x32_bf16 v[72:75], v[24:27], v[120:123], v[72:75]\n\tds_read_b64_tr_b16 v[44:45], v3 offset:12288\n\tds_read_b64_tr_b16 v[46:47], v3 offset:12800\n\t" \
    "v_mfma_f32_16x16x32_bf16 v[80:83], v[24:27], v[124:127], v[80:83]\n\tbuffer_load_dwordx4 v10, s[12:15], s69 offen lds\n\ts_add_u32 m0, s72, 62464\n\tv_mfma_f32_16x16x32_bf16 v[88:91], v[24:27], v[128:131], v[88:91]\n\ts_waitcnt lgkmcnt(8)\n\tv_mfma_f32_16x16x32_bf16 v[76:79], v[28:31], v[120:123], v[76:79]\n\tds_read_b64_tr_b16 v[16:17], v0 offset:16384\n\tds_read_b64_tr_b16 v[18:19], v0 offset:16896\n\t" \
    "v_mfma_f32_16x16x32_bf16 v[84:87], v[28:31], v[124:127], v[84:87]\n\tv_mfma_f32_16x16x32_bf16 v[92:95], v[28:31], v[128:131], v[92:95]\n\ts_waitcnt lgkmcnt(8)\n\tv_mfma_f32_16x16x32_bf16 a[24:27], v[32:35], v[132:135], a[24:27]\n\tds_read_b64_tr_b16 v[20:21], v1 offset:16384\n\tds_read_b64_tr_b16 v[22:23], v1 offset:16896\n\tv_mfma_f32_16x16x32_bf16 a[32:35], v[32:35], v[136:139], a[32:35]\n\tbuffer_load_dwordx4 v11, s[12:15], s69 offen lds\n\t" \
    "s_mov_b32 s69, 131072\n\ts_add_u32 m0, s54, 0\n\tv_mfma_f32_16x16x32_bf16 a[40:43], v[32:35], v[140:143], a[40:43]\n\ts_waitcnt lgkmcnt(8)\n\tv_mfma_f32_16x16x32_bf16 a[28:31], v[36:39], v[132:135], a[28:31]\n\tds_read_b64_tr_b16 v[24:25], v2 offset:16384\n\tds_read_b64_tr_b16 v[26:27], v2 offset:16896\n\tv_mfma_f32_16x16x32_bf16 a[36:39], v[36:39], v[136:139], a[36:39]\n\t" \
    "v_mfma_f32_16x16x32_bf16 a[44:47], v[36:39], v[140:143], a[44:47]\n\ts_waitcnt lgkmcnt(8)\n\tv_mfma_f32_16x16x32_bf16 v[72:75], v[40:43], v[132:135], v[72:75]\n\tds_read_b64_tr_b16 v[28:29], v3 offset:16384\n\tds_read_b64_tr_b16 v[30:31], v3 offset:16896\n\tv_mfma_f32_16x16x32_bf16 v[80:83], v[40:43], v[136:139], v[80:83]\n\tv_mfma_f32_16x16x32_bf16 v[88:91], v[40:43], v[140:143], v[88:91]\n\ts_waitcnt lgkmcnt(8)\n\t" \
    "v_mfma_f32_16x16x32_bf16 v[76:79], v[44:47], v[132:135], v[76:79]\n\tds_read_b64_tr_b16 v[32:33], v0 offset:20480\n\tds_read_b64_tr_b16 v[34:35], v0 offset:20992\n\tv_mfma_f32_16x16x32_bf16 v[84:87], v[44:47], v[136:139], v[84:87]\n\tv_mfma_f32_16x16x32_bf16 v[92:95], v[44:47], v[140:143], v[92:95]\n\ts_waitcnt vmcnt(21)\n\ts_barrier\n\ts_waitcnt lgkmcnt(8)\n\t" \
    "v_mfma_f32_16x16x32_bf16 a[24:27], v[16:19], v[144:147], a[24:27]\n\tds_read_b64_tr_b16 v[36:37], v1 offset:20480\n\tds_read_b64_tr_b16 v[38:39], v1 offset:20992\n\tv_mfma_f32_16x16x32_bf16 a[32:35], v[16:19], v[148:151], a[32:35]\n\tv_mfma_f32_16x16x32_bf16 a[40:43], v[16:19], v[152:155], a[40:43]\n\ts_waitcnt lgkmcnt(8)\n\tv_mfma_f32_16x16x32_bf16 a[28:31], v[20:23], v[144:147], a[28:31]\n\tds_read_b64_tr_b16 v[40:41], v2 offset:20480\n\t" \
    "ds_read_b64_tr_b16 v[42:43], v2 offset:20992\n\tv_mfma_f32_16x16x32_bf16 a[36:39], v[20:23], v[148:151], a[36:39]\n\tv_mfma_f32_16x16x32_bf16 a[44:47], v[20:23], v[152:155], a[44:47]\n\ts_waitcnt lgkmcnt(8)\n\tv_mfma_f32_16x16x32_bf16 v[72:75], v[24:27], v[144:147], v[72:75]\n\tds_read_b64_tr_b16 v[44:45], v3 offset:20480\n\tds_read_b64_tr_b16 v[46:47], v3 offset:20992\n\tv_mfma_f32_16x16x32_bf16 v[80:83], v[24:27], v[148:151], v[80:83]\n\t" \
    "v_mfma_f32_16x16x32_bf16 v[88:91], v[24:27], v[152:155], v[88:91]\n\ts_waitcnt lgkmcnt(8)\n\tv_mfma_f32_16x16x32_bf16 v[76:79], v[28:31], v[144:147], v[76:79]\n\tds_read_b64_tr_b16 v[16:17], v0 offset:24576\n\tds_read_b64_tr_b16 v[18:19], v0 offset:25088\n\tv_mfma_f32_16x16x32_bf16 v[84:87], v[28:31], v[148:151], v[84:87]\n\tv_mfma_f32_16x16x32_bf16 v[92:95], v[28:31], v[152:155], v[92:95]\n\ts_waitcnt lgkmcnt(8)\n\t" \
    "v_mfma_f32_16x16x32_bf16 a[24:27], v[32:35], v[204:207], a[24:27]\n\tds_read_b64_tr_b16 v[20:21], v1 offset:24576\n\tds_read_b64_tr_b16 v[22:23], v1 offset:25088\n\tv_mfma_f32_16x16x32_bf16 a[32:35], v[32:35], v[208:211], a[32:35]\n\tv_mfma_f32_16x16x32_bf16 a[40:43], v[32:35], v[212:215], a[40:43]\n\ts_waitcnt lgkmcnt(8)\n\tv_mfma_f32_16x16x32_bf16 a[28:31], v[36:39], v[204:207], a[28:31]\n\tds_read_b64_tr_b16 v[24:25], v2 offset:24576\n\t" \
    "ds_read_b64_tr_b16 v[26:27], v2 offset:25088\n\tv_mfma_f32_16x16x32_bf16 a[36:39], v[36:39], v[208:211], a[36:39]\n\tv_mfma_f32_16x16x32_bf16 a[44:47], v[36:39], v[212:215], a[44:47]\n\ts_waitcnt lgkmcnt(8)\n\tv_mfma_f32_16x16x32_bf16 v[72:75], v[40:43], v[204:207], v[72:75]\n\tds_read_b64_tr_b16 v[28:29], v3 offset:24576\n\tds_read_b64_tr_b16 v[30:31], v3 offset:25088\n\tv_mfma_f32_16x16x32_bf16 v[80:83], v[40:43], v[208:211], v[80:83]\n\t" \
    "v_mfma_f32_16x16x32_bf16 v[88:91], v[40:43], v[212:215], v[88:91]\n\ts_waitcnt lgkmcnt(8)\n\tv_mfma_f32_16x16x32_bf16 v[76:79], v[44:47], v[204:207], v[76:79]\n\tds_read_b64_tr_b16 v[32:33], v0 offset:28672\n\tds_read_b64_tr_b16 v[34:35], v0 offset:29184\n\tv_mfma_f32_16x16x32_bf16 v[84:87], v[44:47], v[208:211], v[84:87]\n\tv_mfma_f32_16x16x32_bf16 v[92:95], v[44:47], v[212:215], v[92:95]\n\ts_waitcnt vmcnt(5)\n\t" \
    "buffer_store_dwordx2 v[248:249], off, s[8:11], s49 sc1\n\ts_barrier\n\ts_waitcnt lgkmcnt(8)\n\tv_mfma_f32_16x16x32_bf16 a[24:27], v[16:19], v[156:159], a[24:27]\n\tds_read_b64_tr_b16 v[36:37], v1 offset:28672\n\tds_read_b64_tr_b16 v[38:39], v1 offset:29184\n\tv_mfma_f32_16x16x32_bf16 a[32:35], v[16:19], v[160:163], a[32:35]\n\tv_mfma_f32_16x16x32_bf16 a[40:43], v[16:19], v[164:167], a[40:43]\n\t" \
    "s_waitcnt lgkmcnt(8)\n\tv_mfma_f32_16x16x32_bf16 a[28:31], v[20:23], v[156:159], a[28:31]\n\tds_read_b64_tr_b16 v[40:41], v2 offset:28672\n\tds_read_b64_tr_b16 v[42:43], v2 offset:29184\n\tv_mfma_f32_16x16x32_bf16 a[36:39], v[20:23], v[160:163], a[36:39]\n\tv_mfma_f32_16x16x32_bf16 a[44:47], v[20:23], v[164:167], a[44:47]\n\ts_waitcnt lgkmcnt(8)\n\tv_mfma_f32_16x16x32_bf16 v[72:75], v[24:27], v[156:159], v[72:75]\n\t" \
    "ds_read_b64_tr_b16 v[44:45], v3 offset:28672\n\tds_read_b64_tr_b16 v[46:47], v3 offset:29184\n\tv_mfma_f32_16x16x32_bf16 v[80:83], v[24:27], v[160:163], v[80:83]\n\tv_mfma_f32_16x16x32_bf16 v[88:91], v[24:27], v[164:167], v[88:91]\n\ts_waitcnt lgkmcnt(8)\n\tv_mfma_f32_16x16x32_bf16 v[76:79], v[28:31], v[156:159], v[76:79]\n\tds_read_b64_tr_b16 v[16:17], v0 offset:32768\n\tds_read_b64_tr_b16 v[18:19], v0 offset:33280\n\t" \
    "v_mfma_f32_16x16x32_bf16 v[84:87], v[28:31], v[160:163], v[84:87]\n\tv_mfma_f32_16x16x32_bf16 v[92:95], v[28:31], v[164:167], v[92:95]\n\ts_waitcnt lgkmcnt(8)\n\tv_mfma_f32_16x16x32_bf16 a[24:27], v[32:35], v[216:219], a[24:27]\n\tds_read_b64_tr_b16 v[20:21], v1 offset:32768\n\tds_read_b64_tr_b16 v[22:23], v1 offset:33280\n\tv_mfma_f32_16x16x32_bf16 a[32:35], v[32:35], a[48:51], a[32:35]\n\tv_mfma_f32_16x16x32_bf16 a[40:43], v[32:35], a[52:55], a[40:43]\n\t" \
    "s_waitcnt lgkmcnt(8)\n\tv_mfma_f32_16x16x32_bf16 a[28:31], v[36:39], v[216:219], a[28:31]\n\tds_read_b64_tr_b16 v[24:25], v2 offset:32768\n\tds_read_b64_tr_b16 v[26:27], v2 offset:33280\n\tv_mfma_f32_16x16x32_bf16 a[36:39], v[36:39], a[48:51], a[36:39]\n\tv_mfma_f32_16x16x32_bf16 a[44:47], v[36:39], a[52:55], a[44:47]\n\ts_waitcnt lgkmcnt(8)\n\tv_mfma_f32_16x16x32_bf16 v[72:75], v[40:43], v[216:219], v[72:75]\n\t" \
    "ds_read_b64_tr_b16 v[28:29], v3 offset:32768\n\tds_read_b64_tr_b16 v[30:31], v3 offset:33280\n\tv_mfma_f32_16x16x32_bf16 v[80:83], v[40:43], a[48:51], v[80:83]\n\tv_mfma_f32_16x16x32_bf16 v[88:91], v[40:43], a[52:55], v[88:91]\n\ts_waitcnt lgkmcnt(8)\n\tv_mfma_f32_16x16x32_bf16 v[76:79], v[44:47], v[216:219], v[76:79]\n\tds_read_b64_tr_b16 v[32:33], v0 offset:36864\n\tds_read_b64_tr_b16 v[34:35], v0 offset:37376\n\t" \
    "v_mfma_f32_16x16x32_bf16 v[84:87], v[44:47], a[48:51], v[84:87]\n\tv_mfma_f32_16x16x32_bf16 v[92:95], v[44:47], a[52:55], v[92:95]\n\ts_barrier\n\ts_waitcnt lgkmcnt(8)\n\tv_mfma_f32_16x16x32_bf16 a[24:27], v[16:19], a[56:59], a[24:27]\n\tds_read_b64_tr_b16 v[36:37], v1 offset:36864\n\tds_read_b64_tr_b16 v[38:39], v1 offset:37376\n\tv_mfma_f32_16x16x32_bf16 a[32:35], v[16:19], a[60:63], a[32:35]\n\t" \
    "v_mfma_f32_16x16x32_bf16 a[40:43], v[16:19], a[64:67], a[40:43]\n\ts_waitcnt lgkmcnt(8)\n\tv_mfma_f32_16x16x32_bf16 a[28:31], v[20:23], a[56:59], a[28:31]\n\tds_read_b64_tr_b16 v[40:41], v2 offset:36864\n\tds_read_b64_tr_b16 v[42:43], v2 offset:37376\n\tv_mfma_f32_16x16x32_bf16 a[36:39], v[20:23], a[60:63], a[36:39]\n\tv_mfma_f32_16x16x32_bf16 a[44:47], v[20:23], a[64:67], a[44:47]\n\ts_waitcnt lgkmcnt(8)\n\t" \
    "v_mfma_f32_16x16x32_bf16 v[72:75], v[24:27], a[56:59], v[72:75]\n\tds_read_b64_tr_b16 v[44:45], v3 offset:36864\n\tds_read_b64_tr_b16 v[46:47], v3 offset:37376\n\tv_mfma_f32_16x16x32_bf16 v[80:83], v[24:27], a[60:63], v[80:83]\n\tv_mfma_f32_16x16x32_bf16 v[88:91], v[24:27], a[64:67], v[88:91]\n\ts_waitcnt lgkmcnt(8)\n\tv_mfma_f32_16x16x32_bf16 v[76:79], v[28:31], a[56:59], v[76:79]\n\tds_read_b64_tr_b16 v[16:17], v0 offset:40960\n\t" \
    "ds_read_b64_tr_b16 v[18:19], v0 offset:41472\n\tv_mfma_f32_16x16x32_bf16 v[84:87], v[28:31], a[60:63], v[84:87]\n\tv_mfma_f32_16x16x32_bf16 v[92:95], v[28:31], a[64:67], v[92:95]\n\ts_waitcnt lgkmcnt(8)\n\tv_mfma_f32_16x16x32_bf16 a[24:27], v[32:35], v[168:171], a[24:27]\n\tds_read_b64_tr_b16 v[20:21], v1 offset:40960\n\tds_read_b64_tr_b16 v[22:23], v1 offset:41472\n\tv_mfma_f32_16x16x32_bf16 a[32:35], v[32:35], v[172:175], a[32:35]\n\t" \
    "v_mfma_f32_16x16x32_bf16 a[40:43], v[32:35], v[176:179], a[40:43]\n\ts_waitcnt lgkmcnt(8)\n\tv_mfma_f32_16x16x32_bf16 a[28:31], v[36:39], v[168:171], a[28:31]\n\tds_read_b64_tr_b16 v[24:25], v2 offset:40960\n\tds_read_b64_tr_b16 v[26:27], v2 offset:41472\n\tv_mfma_f32_16x16x32_bf16 a[36:39], v[36:39], v[172:175], a[36:39]\n\tv_mfma_f32_16x16x32_bf16 a[44:47], v[36:39], v[176:179], a[44:47]\n\ts_waitcnt lgkmcnt(8)\n\t" \
    "v_mfma_f32_16x16x32_bf16 v[72:75], v[40:43], v[168:171], v[72:75]\n\tds_read_b64_tr_b16 v[28:29], v3 offset:40960\n\tds_read_b64_tr_b16 v[30:31], v3 offset:41472\n\tv_mfma_f32_16x16x32_bf16 v[80:83], v[40:43], v[172:175], v[80:83]\n\tv_mfma_f32_16x16x32_bf16 v[88:91], v[40:43], v[176:179], v[88:91]\n\ts_waitcnt lgkmcnt(8)\n\tv_mfma_f32_16x16x32_bf16 v[76:79], v[44:47], v[168:171], v[76:79]\n\tds_read_b64_tr_b16 v[32:33], v0 offset:45056\n\t" \
    "ds_read_b64_tr_b16 v[34:35], v0 offset:45568\n\tv_mfma_f32_16x16x32_bf16 v[84:87], v[44:47], v[172:175], v[84:87]\n\tv_mfma_f32_16x16x32_bf16 v[92:95], v[44:47], v[176:179], v[92:95]\n\tbuffer_load_dwordx4 v12, s[16:19], s69 offen lds\n\ts_mov_b32 s69, 131072\n\ts_add_u32 m0, s54, 65536\n\ts_nop 0\n\tbuffer_load_dwordx4 v13, s[16:19], s69 offen lds\n\t" \
    "s_mov_b32 s69, 0\n\ts_add_u32 m0, s54, 4096\n\ts_nop 0\n\tbuffer_load_dwordx4 v12, s[16:19], s69 offen lds\n\ts_waitcnt vmcnt(6)\n\ts_barrier\n\ts_waitcnt lgkmcnt(8)\n\tv_mfma_f32_16x16x32_bf16 a[24:27], v[16:19], a[68:71], a[24:27]\n\t" \
    "ds_read_b64_tr_b16 v[36:37], v1 offset:45056\n\tds_read_b64_tr_b16 v[38:39], v1 offset:45568\n\ts_mov_b32 s69, 0\n\tv_mfma_f32_16x16x32_bf16 a[32:35], v[16:19], a[72:75], a[32:35]\n\ts_add_u32 m0, s54, 69632\n\tv_mfma_f32_16x16x32_bf16 a[40:43], v[16:19], a[76:79], a[40:43]\n\ts_waitcnt lgkmcnt(8)\n\tv_mfma_f32_16x16x32_bf16 a[28:31], v[20:23], a[68:71], a[28:31]\n\t" \
    "ds_read_b64_tr_b16 v[40:41], v2 offset:45056\n\tds_read_b64_tr_b16 v[42:43], v2 offset:45568\n\tv_mfma_f32_16x16x32_bf16 a[36:39], v[20:23], a[72:75], a[36:39]\n\tbuffer_load_dwordx4 v13, s[16:19], s69 offen lds\n\ts_mov_b32 s69, 655360\n\ts_add_u32 m0, s54, 8192\n\tv_mfma_f32_16x16x32_bf16 a[44:47], v[20:23], a[76:79], a[44:47]\n\ts_waitcnt lgkmcnt(8)\n\t" \
    "v_mfma_f32_16x16x32_bf16 v[72:75], v[24:27], a[68:71], v[72:75]\n\tds_read_b64_tr_b16 v[44:45], v3 offset:45056\n\tds_read_b64_tr_b16 v[46:47], v3 offset:45568\n\tv_mfma_f32_16x16x32_bf16 v[80:83], v[24:27], a[72:75], v[80:83]\n\tbuffer_load_dwordx4 v12, s[16:19], s69 offen lds\n\ts_mov_b32 s69, 655360\n\ts_add_u32 m0, s54, 73728\n\tv_mfma_f32_16x16x32_bf16 v[88:91], v[24:27], a[76:79], v[88:91]\n\t" \
    "s_waitcnt lgkmcnt(8)\n\tv_mfma_f32_16x16x32_bf16 v[76:79], v[28:31], a[68:71], v[76:79]\n\tds_read_b64_tr_b16 v[16:17], v0 offset:49152\n\tds_read_b64_tr_b16 v[18:19], v0 offset:49664\n\tv_mfma_f32_16x16x32_bf16 v[84:87], v[28:31], a[72:75], v[84:87]\n\tbuffer_load_dwordx4 v13, s[16:19], s69 offen lds\n\ts_mov_b32 s69, 524288\n\ts_add_u32 m0, s54, 12288\n\t" \
    "v_mfma_f32_16x16x32_bf16 v[92:95], v[28:31], a[76:79], v[92:95]\n\ts_waitcnt lgkmcnt(8)\n\tv_mfma_f32_16x16x32_bf16 a[24:27], v[32:35], a[80:83], a[24:27]\n\tds_read_b64_tr_b16 v[20:21], v1 offset:49152\n\tds_read_b64_tr_b16 v[22:23], v1 offset:49664\n\tv_mfma_f32_16x16x32_bf16 a[32:35], v[32:35], a[84:87], a[32:35]\n\tbuffer_load_dwordx4 v12, s[16:19], s69 offen lds\n\ts_mov_b32 s69, 524288\n\t" \
    "s_add_u32 m0, s54, 77824\n\tv_mfma_f32_16x16x32_bf16 a[40:43], v[32:35], a[88:91], a[40:43]\n\ts_waitcnt lgkmcnt(8)\n\tv_mfma_f32_16x16x32_bf16 a[28:31], v[36:39], a[80:83], a[28:31]\n\tds_read_b64_tr_b16 v[24:25], v0 offset:53248\n\tds_read_b64_tr_b16 v[26:27], v0 offset:53760\n\tv_mfma_f32_16x16x32_bf16 a[36:39], v[36:39], a[84:87], a[36:39]\n\tbuffer_load_dwordx4 v13, s[16:19], s69 offen lds\n\t" \
    "s_mov_b32 s69, 1179648\n\ts_add_u32 m0, s54, 16384\n\tv_mfma_f32_16x16x32_bf16 a[44:47], v[36:39], a[88:91], a[44:47]\n\ts_waitcnt lgkmcnt(8)\n\tv_mfma_f32_16x16x32_bf16 v[72:75], v[40:43], a[80:83], v[72:75]\n\tds_read_b64_tr_b16 v[28:29], v1 offset:53248\n\tds_read_b64_tr_b16 v[30:31], v1 offset:53760\n\tv_mfma_f32_16x16x32_bf16 v[80:83], v[40:43], a[84:87], v[80:83]\n\t" \
    "buffer_load_dwordx4 v12, s[16:19], s69 offen lds\n\ts_mov_b32 s69, 1179648\n\ts_add_u32 m0, s54, 81920\n\tv_mfma_f32_16x16x32_bf16 v[88:91], v[40:43], a[88:91], v[88:91]\n\ts_waitcnt lgkmcnt(8)\n\tv_mfma_f32_16x16x32_bf16 v[76:79], v[44:47], a[80:83], v[76:79]\n\tds_read_b64_tr_b16 v[32:33], v0 offset:57344\n\tds_read_b64_tr_b16 v[34:35], v0 offset:57856\n\t" \
    "v_mfma_f32_16x16x32_bf16 v[84:87], v[44:47], a[84:87], v[84:87]\n\tv_mfma_f32_16x16x32_bf16 v[92:95], v[44:47], a[88:91], v[92:95]\n\ts_waitcnt vmcnt(10)\n\ts_barrier\n\ts_waitcnt lgkmcnt(8)\n\tv_mfma_f32_16x16x32_bf16 a[24:27], v[16:19], a[92:95], a[24:27]\n\tds_read_b64_tr_b16 v[36:37], v1 offset:57344\n\tds_read_b64_tr_b16 v[38:39], v1 offset:57856\n\t" \
    "v_mfma_f32_16x16x32_bf16 a[32:35], v[16:19], a[96:99], a[32:35]\n\tbuffer_load_dwordx4 v13, s[16:19], s69 offen lds\n\ts_mov_b32 s69, 1048576\n\ts_add_u32 m0, s54, 20480\n\tv_mfma_f32_16x16x32_bf16 a[40:43], v[16:19], a[100:103], a[40:43]\n\ts_waitcnt lgkmcnt(8)\n\tv_mfma_f32_16x16x32_bf16 a[28:31], v[20:23], a[92:95], a[28:31]\n\tds_read_b64_tr_b16 v[40:41], v0 offset:61440\n\t" \
    "ds_read_b64_tr_b16 v[42:43], v0 offset:61952\n\tv_mfma_f32_16x16x32_bf16 a[36:39], v[20:23], a[96:99], a[36:39]\n\tbuffer_load_dwordx4 v12, s[16:19], s69 offen lds\n\ts_mov_b32 s69, 1048576\n\ts_add_u32 m0, s54, 86016\n\tv_mfma_f32_16x16x32_bf16 a[44:47], v[20:23], a[100:103], a[44:47]\n\ts_waitcnt lgkmcnt(8)\n\tv_mfma_f32_16x16x32_bf16 a[24:27], v[24:27], a[104:107], a[24:27]\n\t" \
    "ds_read_b64_tr_b16 v[44:45], v1 offset:61440\n\tds_read_b64_tr_b16 v[46:47], v1 offset:61952\n\tv_mfma_f32_16x16x32_bf16 a[32:35], v[24:27], a[108:111], a[32:35]\n\tbuffer_load_dwordx4 v13, s[16:19], s69 offen lds\n\ts_mov_b32 s69, 1703936\n\ts_add_u32 m0, s54, 24576\n\tv_mfma_f32_16x16x32_bf16 a[40:43], v[24:27], a[112:115], a[40:43]\n\ts_waitcnt lgkmcnt(8)\n\t" \
    "v_mfma_f32_16x16x32_bf16 a[28:31], v[28:31], a[104:107], a[28:31]\n\tds_read_b64_tr_b16 v[16:17], v2 offset:49152\n\tds_read_b64_tr_b16 v[18:19], v2 offset:49664\n\tv_mfma_f32_16x16x32_bf16 a[36:39], v[28:31], a[108:111], a[36:39]\n\tbuffer_load_dwordx4 v12, s[16:19], s69 offen lds\n\ts_mov_b32 s69, 1703936\n\ts_add_u32 m0, s54, 90112\n\tv_mfma_f32_16x16x32_bf16 a[44:47], v[28:31], a[112:115], a[44:47]\n\t" \
    "buffer_load_dwordx2 v[250:251], v241, s[8:11], s50 offen sc1\n\ts_waitcnt lgkmcnt(8)\n\tv_mfma_f32_16x16x32_bf16 a[24:27], v[32:35], a[116:119], a[24:27]\n\tds_read_b64_tr_b16 v[20:21], v3 offset:49152\n\tds_read_b64_tr_b16 v[22:23], v3 offset:49664\n\tv_mfma_f32_16x16x32_bf16 a[32:35], v[32:35], a[120:123], a[32:35]\n\tv_mfma_f32_16x16x32_bf16 a[40:43], v[32:35], a[124:127], a[40:43]\n\ts_waitcnt lgkmcnt(8)\n\t" \
    "v_mfma_f32_16x16x32_bf16 a[28:31], v[36:39], a[116:119], a[28:31]\n\tds_read_b64_tr_b16 v[24:25], v2 offset:53248\n\tds_read_b64_tr_b16 v[26:27], v2 offset:53760\n\tv_mfma_f32_16x16x32_bf16 a[36:39], v[36:39], a[120:123], a[36:39]\n\tbuffer_load_dwordx4 v13, s[16:19], s69 offen lds\n\ts_mov_b32 s69, 1572864\n\ts_add_u32 m0, s54, 28672\n\tv_mfma_f32_16x16x32_bf16 a[44:47], v[36:39], a[124:127], a[44:47]\n\t" \
    "s_waitcnt lgkmcnt(8)\n\tv_mfma_f32_16x16x32_bf16 a[24:27], v[40:43], a[128:131], a[24:27]\n\tds_read_b64_tr_b16 v[28:29], v3 offset:53248\n\tds_read_b64_tr_b16 v[30:31], v3 offset:53760\n\tv_mfma_f32_16x16x32_bf16 a[32:35], v[40:43], a[132:135], a[32:35]\n\tv_mfma_f32_16x16x32_bf16 a[40:43], v[40:43], a[136:139], a[40:43]\n\ts_waitcnt lgkmcnt(8)\n\tv_mfma_f32_16x16x32_bf16 a[28:31], v[44:47], a[128:131], a[28:31]\n\t" \
    "ds_read_b64_tr_b16 v[32:33], v2 offset:57344\n\tds_read_b64_tr_b16 v[34:35], v2 offset:57856\n\tv_mfma_f32_16x16x32_bf16 a[36:39], v[44:47], a[132:135], a[36:39]\n\tbuffer_load_dwordx4 v12, s[16:19], s69 offen lds\n\ts_mov_b32 s69, 1572864\n\ts_add_u32 m0, s54, 94208\n\tv_mfma_f32_16x16x32_bf16 a[44:47], v[44:47], a[136:139], a[44:47]\n\ts_waitcnt vmcnt(15)\n\t" \
    "s_barrier\n\ts_waitcnt lgkmcnt(8)\n\tv_mfma_f32_16x16x32_bf16 v[72:75], v[16:19], a[92:95], v[72:75]\n\tds_read_b64_tr_b16 v[36:37], v3 offset:57344\n\tds_read_b64_tr_b16 v[38:39], v3 offset:57856\n\tv_mfma_f32_16x16x32_bf16 v[80:83], v[16:19], a[96:99], v[80:83]\n\tbuffer_load_dwordx4 v13, s[16:19], s69 offen lds\n\ts_mov_b32 s69, 2228224\n\t" \
    "s_add_u32 m0, s54, 32768\n\tv_mfma_f32_16x16x32_bf16 v[88:91], v[16:19], a[100:103], v[88:91]\n\tds_write_b128 v237, a[24:27] offset:0\n\ts_waitcnt lgkmcnt(9)\n\tv_mfma_f32_16x16x32_bf16 v[76:79], v[20:23], a[92:95], v[76:79]\n\tds_read_b64_tr_b16 v[40:41], v2 offset:61440\n\tds_read_b64_tr_b16 v[42:43], v2 offset:61952\n\tv_mfma_f32_16x16x32_bf16 v[84:87], v[20:23], a[96:99], v[84:87]\n\t" \
    "buffer_load_dwordx4 v12, s[16:19], s69 offen lds\n\ts_mov_b32 s69, 2228224\n\ts_add_u32 m0, s54, 98304\n\tv_mfma_f32_16x16x32_bf16 v[92:95], v[20:23], a[100:103], v[92:95]\n\tds_write_b128 v237, a[32:35] offset:2048\n\ts_waitcnt lgkmcnt(10)\n\tv_mfma_f32_16x16x32_bf16 v[72:75], v[24:27], a[104:107], v[72:75]\n\tds_read_b64_tr_b16 v[44:45], v3 offset:61440\n\t" \
    "ds_read_b64_tr_b16 v[46:47], v3 offset:61952\n\tv_mfma_f32_16x16x32_bf16 v[80:83], v[24:27], a[108:111], v[80:83]\n\tbuffer_load_dwordx4 v13, s[16:19], s69 offen lds\n\ts_mov_b32 s69, 2097152\n\ts_add_u32 m0, s54, 36864\n\tv_mfma_f32_16x16x32_bf16 v[88:91], v[24:27], a[112:115], v[88:91]\n\tds_write_b128 v237, a[40:43] offset:4096\n\ts_waitcnt lgkmcnt(11)\n\t" \
    "v_mfma_f32_16x16x32_bf16 v[76:79], v[28:31], a[104:107], v[76:79]\n\tds_read_b64_tr_b16 v[16:17], v4 offset:0\n\tds_read_b64_tr_b16 v[18:19], v4 offset:2048\n\tv_mfma_f32_16x16x32_bf16 v[84:87], v[28:31], a[108:111], v[84:87]\n\tbuffer_load_dwordx4 v12, s[16:19], s69 offen lds\n\ts_mov_b32 s69, 2097152\n\ts_add_u32 m0, s54, 102400\n\tv_mfma_f32_16x16x32_bf16 v[92:95], v[28:31], a[112:115], v[92:95]\n\t" \
    "ds_write_b128 v237, a[28:31] offset:1024\n\tds_write_b128 v237, a[36:39] offset:3072\n\tds_write_b128 v237, a[44:47] offset:5120\n\ts_waitcnt vmcnt(18) lgkmcnt(0)\n\ts_barrier\n\tv_mfma_f32_16x16x32_bf16 v[72:75], v[32:35], a[116:119], v[72:75]\n\tds_read_b64_tr_b16 v[20:21], v5 offset:0\n\tds_read_b64_tr_b16 v[22:23], v5 offset:2048\n\t" \
    "v_mfma_f32_16x16x32_bf16 v[80:83], v[32:35], a[120:123], v[80:83]\n\tbuffer_load_dwordx4 v13, s[16:19], s69 offen lds\n\tds_read_b128 v[96:99], v238 offset:0\n\ts_mov_b32 s69, 2752512\n\tv_mfma_f32_16x16x32_bf16 v[88:91], v[32:35], a[124:127], v[88:91]\n\tds_read_b128 v[100:103], v238 offset:1024\n\tds_read_b128 v[104:107], v238 offset:2048\n\tv_mfma_f32_16x16x32_bf16 v[76:79], v[36:39], a[116:119], v[76:79]\n\t" \
    "ds_read_b64_tr_b16 v[24:25], v6 offset:0\n\tds_read_b64_tr_b16 v[26:27], v6 offset:2048\n\ts_add_u32 m0, s54, 40960\n\tv_mfma_f32_16x16x32_bf16 v[84:87], v[36:39], a[120:123], v[84:87]\n\tbuffer_load_dwordx4 v12, s[16:19], s69 offen lds\n\tds_read_b128 v[108:111], v238 offset:3072\n\ts_mov_b32 s69, 2752512\n\tv_mfma_f32_16x16x32_bf16 v[92:95], v[36:39], a[124:127], v[92:95]\n\t" \
    "ds_read_b128 v[112:115], v238 offset:4096\n\tds_read_b128 v[116:119], v238 offset:5120\n\tv_mfma_f32_16x16x32_bf16 v[72:75], v[40:43], a[128:131], v[72:75]\n\tds_read_b64_tr_b16 v[28:29], v7 offset:0\n\tds_read_b64_tr_b16 v[30:31], v7 offset:2048\n\ts_add_u32 m0, s54, 106496\n\tv_mfma_f32_16x16x32_bf16 v[80:83], v[40:43], a[132:135], v[80:83]\n\tbuffer_load_dwordx4 v13, s[16:19], s69 offen lds\n\t" \
    "s_mov_b32 s69, 2621440\n\ts_add_u32 m0, s54, 45056\n\tv_mfma_f32_16x16x32_bf16 v[88:91], v[40:43], a[136:139], v[88:91]\n\tv_mfma_f32_16x16x32_bf16 v[76:79], v[44:47], a[128:131], v[76:79]\n\tds_read_b64_tr_b16 v[32:33], v224 offset:0\n\tds_read_b64_tr_b16 v[34:35], v224 offset:2048\n\tv_mfma_f32_16x16x32_bf16 v[84:87], v[44:47], a[132:135], v[84:87]\n\tbuffer_load_dwordx4 v12, s[16:19], s69 offen lds\n\t" \
    "s_mov_b32 s69, 2621440\n\ts_add_u32 m0, s54, 110592\n\tv_mfma_f32_16x16x32_bf16 v[92:95], v[44:47], a[136:139], v[92:95]\n\tbuffer_load_dwordx4 v13, s[16:19], s69 offen lds\n\ts_cmp_lg_u32 s39, 2\n\ts_cbranch_scc1 107f\n\ts_waitcnt vmcnt(11)\n\tv_cmp_ne_u64 vcc, s[36:37], v[250:251]\n\t" \
    "s_cbranch_vccz 107f\n\ts_add_u32 s73, s45, 55296\n\ts_waitcnt lgkmcnt(10)\n\tv_pk_add_f32 v[72:73], v[72:73], v[96:97]\n\tv_pk_add_f32 v[74:75], v[74:75], v[98:99]\n\tv_pk_add_f32 v[76:77], v[76:77], v[100:101]\n\tv_pk_add_f32 v[78:79], v[78:79], v[102:103]\n\tv_pk_mul_f32 v[96:97], v[72:73], v[72:73]\n\t" \
    "v_pk_mul_f32 v[98:99], v[74:75], v[74:75]\n\tv_pk_mul_f32 v[100:101], v[76:77], v[76:77]\n\tv_pk_mul_f32 v[102:103], v[78:79], v[78:79]\n\tv_pk_fma_f32 v[96:97], v[96:97], v[246:247], v[246:247] op_sel:[0,0,1] op_sel_hi:[1,0,1]\n\tv_pk_fma_f32 v[98:99], v[98:99], v[246:247], v[246:247] op_sel:[0,0,1] op_sel_hi:[1,0,1]\n\tv_pk_fma_f32 v[100:101], v[100:101], v[246:247], v[246:247] op_sel:[0,0,1] op_sel_hi:[1,0,1]\n\tv_pk_fma_f32 v[102:103], v[102:103], v[246:247], v[246:247] op_sel:[0,0,1] op_sel_hi:[1,0,1]\n\tv_pk_mul_f32 v[96:97], v[96:97], v[72:73]\n\t" \
    "v_pk_mul_f32 v[98:99], v[98:99], v[74:75]\n\tv_pk_mul_f32 v[100:101], v[100:101], v[76:77]\n\tv_pk_mul_f32 v[102:103], v[102:103], v[78:79]\n\ts_waitcnt lgkmcnt(6)\n\tv_exp_f32 v96, v96\n\tv_pk_add_f32 v[80:81], v[80:81], v[104:105]\n\tv_exp_f32 v97, v97\n\tv_pk_add_f32 v[82:83], v[82:83], v[106:107]\n\t" \
    "v_exp_f32 v98, v98\n\tv_pk_add_f32 v[84:85], v[84:85], v[108:109]\n\tv_exp_f32 v99, v99\n\tv_pk_add_f32 v[86:87], v[86:87], v[110:111]\n\tv_exp_f32 v100, v100\n\tv_pk_mul_f32 v[104:105], v[80:81], v[80:81]\n\tv_exp_f32 v101, v101\n\tv_pk_mul_f32 v[106:107], v[82:83], v[82:83]\n\t" \
    "v_exp_f32 v102, v102\n\tv_pk_mul_f32 v[108:109], v[84:85], v[84:85]\n\tv_exp_f32 v103, v103\n\tv_pk_mul_f32 v[110:111], v[86:87], v[86:87]\n\tv_pk_add_f32 v[96:97], v[96:97], 1.0 op_sel_hi:[1,0]\n\tv_pk_add_f32 v[98:99], v[98:99], 1.0 op_sel_hi:[1,0]\n\tv_pk_add_f32 v[100:101], v[100:101], 1.0 op_sel_hi:[1,0]\n\tv_pk_add_f32 v[102:103], v[102:103], 1.0 op_sel_hi:[1,0]\n\t" \
    "v_pk_fma_f32 v[104:105], v[104:105], v[246:247], v[246:247] op_sel:[0,0,1] op_sel_hi:[1,0,1]\n\tv_rcp_f32 v96, v96\n\tv_pk_fma_f32 v[106:107], v[106:107], v[246:247], v[246:247] op_sel:[0,0,1] op_sel_hi:[1,0,1]\n\tv_rcp_f32 v97, v97\n\tv_pk_fma_f32 v[108:109], v[108:109], v[246:247], v[246:247] op_sel:[0,0,1] op_sel_hi:[1,0,1]\n\tv_rcp_f32 v98, v98\n\tv_pk_fma_f32 v[110:111], v[110:111], v[246:247], v[246:247] op_sel:[0,0,1] op_sel_hi:[1,0,1]\n\tv_rcp_f32 v99, v99\n\t" \
    "v_pk_mul_f32 v[104:105], v[104:105], v[80:81]\n\tv_rcp_f32 v100, v100\n\tv_pk_mul_f32 v[106:107], v[106:107], v[82:83]\n\tv_rcp_f32 v101, v101\n\tv_pk_mul_f32 v[108:109], v[108:109], v[84:85]\n\tv_rcp_f32 v102, v102\n\tv_pk_mul_f32 v[110:111], v[110:111], v[86:87]\n\tv_rcp_f32 v103, v103\n\t" \
    "s_waitcnt lgkmcnt(4)\n\tv_exp_f32 v104, v104\n\tv_pk_mul_f32 v[96:97], v[96:97], v[72:73]\n\tv_exp_f32 v105, v105\n\tv_pk_mul_f32 v[98:99], v[98:99], v[74:75]\n\tv_exp_f32 v106, v106\n\tv_pk_mul_f32 v[100:101], v[100:101], v[76:77]\n\tv_exp_f32 v107, v107\n\t" \
    "v_pk_mul_f32 v[102:103], v[102:103], v[78:79]\n\tv_exp_f32 v108, v108\n\tv_cvt_pk_bf16_f32 v72, v96, v97\n\tv_exp_f32 v109, v109\n\tv_cvt_pk_bf16_f32 v73, v98, v99\n\tv_exp_f32 v110, v110\n\tv_cvt_pk_bf16_f32 v74, v100, v101\n\tv_exp_f32 v111, v111\n\t" \
    "v_cvt_pk_bf16_f32 v75, v102, v103\n\tv_pk_add_f32 v[104:105], v[104:105], 1.0 op_sel_hi:[1,0]\n\tv_pk_add_f32 v[106:107], v[106:107], 1.0 op_sel_hi:[1,0]\n\tv_pk_add_f32 v[108:109], v[108:109], 1.0 op_sel_hi:[1,0]\n\tv_pk_add_f32 v[110:111], v[110:111], 1.0 op_sel_hi:[1,0]\n\tv_pk_add_f32 v[88:89], v[88:89], v[112:113]\n\tv_rcp_f32 v104, v104\n\tv_pk_add_f32 v[90:91], v[90:91], v[114:115]\n\t" \
    "v_rcp_f32 v105, v105\n\tv_pk_add_f32 v[92:93], v[92:93], v[116:117]\n\tv_rcp_f32 v106, v106\n\tv_pk_add_f32 v[94:95], v[94:95], v[118:119]\n\tv_rcp_f32 v107, v107\n\tv_pk_mul_f32 v[112:113], v[88:89], v[88:89]\n\tv_rcp_f32 v108, v108\n\tv_pk_mul_f32 v[114:115], v[90:91], v[90:91]\n\t" \
    "v_rcp_f32 v109, v109\n\tv_pk_mul_f32 v[116:117], v[92:93], v[92:93]\n\tv_rcp_f32 v110, v110\n\tv_pk_mul_f32 v[118:119], v[94:95], v[94:95]\n\tv_rcp_f32 v111, v111\n\tv_pk_fma_f32 v[112:113], v[112:113], v[246:247], v[246:247] op_sel:[0,0,1] op_sel_hi:[1,0,1]\n\tv_pk_mul_f32 v[104:105], v[104:105], v[80:81]\n\tv_pk_mul_f32 v[106:107], v[106:107], v[82:83]\n\t" \
    "v_pk_mul_f32 v[108:109], v[108:109], v[84:85]\n\tv_pk_mul_f32 v[110:111], v[110:111], v[86:87]\n\tv_cvt_pk_bf16_f32 v80, v104, v105\n\tv_cvt_pk_bf16_f32 v81, v106, v107\n\tv_cvt_pk_bf16_f32 v82, v108, v109\n\tv_cvt_pk_bf16_f32 v83, v110, v111\n\tbuffer_load_dwordx2 v[250:251], v241, s[8:11], s50 offen sc1\n\tv_pk_fma_f32 v[114:115], v[114:115], v[246:247], v[246:247] op_sel:[0,0,1] op_sel_hi:[1,0,1]\n\t" \
    "v_pk_fma_f32 v[116:117], v[116:117], v[246:247], v[246:247] op_sel:[0,0,1] op_sel_hi:[1,0,1]\n\tv_pk_fma_f32 v[118:119], v[118:119], v[246:247], v[246:247] op_sel:[0,0,1] op_sel_hi:[1,0,1]\n\tv_pk_mul_f32 v[112:113], v[112:113], v[88:89]\n\tv_pk_mul_f32 v[114:115], v[114:115], v[90:91]\n\tv_pk_mul_f32 v[116:117], v[116:117], v[92:93]\n\tv_pk_mul_f32 v[118:119], v[118:119], v[94:95]\n\tv_exp_f32 v112, v112\n\tv_exp_f32 v113, v113\n\t" \
    "v_exp_f32 v114, v114\n\tv_exp_f32 v115, v115\n\tv_exp_f32 v116, v116\n\tv_exp_f32 v117, v117\n\tv_exp_f32 v118, v118\n\tv_exp_f32 v119, v119\n\tv_pk_add_f32 v[112:113], v[112:113], 1.0 op_sel_hi:[1,0]\n\tv_pk_add_f32 v[114:115], v[114:115], 1.0 op_sel_hi:[1,0]\n\t" \
    "v_pk_add_f32 v[116:117], v[116:117], 1.0 op_sel_hi:[1,0]\n\tv_pk_add_f32 v[118:119], v[118:119], 1.0 op_sel_hi:[1,0]\n\tv_rcp_f32 v112, v112\n\tv_rcp_f32 v113, v113\n\tv_rcp_f32 v114, v114\n\tv_rcp_f32 v115, v115\n\tv_rcp_f32 v116, v116\n\tv_rcp_f32 v117, v117\n\t" \
    "v_rcp_f32 v118, v118\n\tv_rcp_f32 v119, v119\n\tv_pk_mul_f32 v[112:113], v[112:113], v[88:89]\n\tv_pk_mul_f32 v[114:115], v[114:115], v[90:91]\n\tv_pk_mul_f32 v[116:117], v[116:117], v[92:93]\n\tv_pk_mul_f32 v[118:119], v[118:119], v[94:95]\n\tv_cvt_pk_bf16_f32 v88, v112, v113\n\tv_cvt_pk_bf16_f32 v89, v114, v115\n\t" \
    "v_cvt_pk_bf16_f32 v90, v116, v117\n\tv_cvt_pk_bf16_f32 v91, v118, v119\n\ts_waitcnt vmcnt(0)\n\tv_cmp_ne_u64 vcc, s[36:37], v[250:251]\n\ts_cbranch_vccz 110f\n\ts_mov_b32 s71, 1048576\n\t109:\n\ts_sleep 1\n\t" \
    "buffer_load_dwordx2 v[250:251], v241, s[8:11], s50 offen sc1\n\ts_waitcnt vmcnt(0)\n\tv_cmp_ne_u64 vcc, s[36:37], v[250:251]\n\ts_cbranch_vccz 110f\n\ts_sub_u32 s71, s71, 1\n\ts_cbranch_scc0 109b\n\t110:\n\ts_add_u32 s68, s46, 0\n\t" \
    "buffer_load_dwordx4 v[144:147], v8, s[8:11], s68 offen offset:0 sc1\n\tbuffer_load_dwordx4 v[148:151], v8, s[8:11], s68 offen offset:1024 sc1\n\tbuffer_load_dwordx4 v[152:155], v8, s[8:11], s68 offen offset:2048 sc1\n\ts_add_u32 s68, s46, 3072\n\tbuffer_load_dwordx4 v[156:159], v8, s[8:11], s68 offen offset:0 sc1\n\tbuffer_load_dwordx4 v[160:163], v8, s[8:11], s68 offen offset:1024 sc1\n\tbuffer_load_dwordx4 v[164:167], v8, s[8:11], s68 offen offset:2048 sc1\n\ts_add_u32 s68, s46, 6144\n\t" \
    "buffer_load_dwordx4 v[168:171], v8, s[8:11], s68 offen offset:0 sc1\n\tbuffer_load_dwordx4 v[172:175], v8, s[8:11], s68 offen offset:1024 sc1\n\tbuffer_load_dwordx4 v[176:179], v8, s[8:11], s68 offen offset:2048 sc1\n\tbuffer_store_dwordx4 v[72:75], v8, s[8:11], s73 offen offset:0 sc1\n\tbuffer_store_dwordx4 v[80:83], v8, s[8:11], s73 offen offset:1024 sc1\n\tbuffer_store_dwordx4 v[88:91], v8, s[8:11], s73 offen offset:2048 sc1\n\ts_branch 108f\n\t107:\n\t" \
    "s_add_u32 s68, s46, 0\n\tbuffer_load_dwordx4 v[144:147], v8, s[8:11], s68 offen offset:0 sc1\n\tbuffer_load_dwordx4 v[148:151], v8, s[8:11], s68 offen offset:1024 sc1\n\tbuffer_load_dwordx4 v[152:155], v8, s[8:11], s68 offen offset:2048 sc1\n\ts_add_u32 s73, s45, 55296\n\ts_waitcnt lgkmcnt(10)\n\tv_pk_add_f32 v[72:73], v[72:73], v[96:97]\n\ts_add_u32 s68, s46, 3072\n\t" \
    "v_pk_add_f32 v[74:75], v[74:75], v[98:99]\n\tv_pk_add_f32 v[76:77], v[76:77], v[100:101]\n\tv_pk_add_f32 v[78:79], v[78:79], v[102:103]\n\tv_pk_mul_f32 v[96:97], v[72:73], v[72:73]\n\tv_pk_mul_f32 v[98:99], v[74:75], v[74:75]\n\tv_pk_mul_f32 v[100:101], v[76:77], v[76:77]\n\tv_pk_mul_f32 v[102:103], v[78:79], v[78:79]\n\tv_pk_fma_f32 v[96:97], v[96:97], v[246:247], v[246:247] op_sel:[0,0,1] op_sel_hi:[1,0,1]\n\t" \
    "v_pk_fma_f32 v[98:99], v[98:99], v[246:247], v[246:247] op_sel:[0,0,1] op_sel_hi:[1,0,1]\n\tv_pk_fma_f32 v[100:101], v[100:101], v[246:247], v[246:247] op_sel:[0,0,1] op_sel_hi:[1,0,1]\n\tv_pk_fma_f32 v[102:103], v[102:103], v[246:247], v[246:247] op_sel:[0,0,1] op_sel_hi:[1,0,1]\n\tv_pk_mul_f32 v[96:97], v[96:97], v[72:73]\n\tv_pk_mul_f32 v[98:99], v[98:99], v[74:75]\n\tv_pk_mul_f32 v[100:101], v[100:101], v[76:77]\n\tv_pk_mul_f32 v[102:103], v[102:103], v[78:79]\n\ts_waitcnt lgkmcnt(6)\n\t" \
    "v_exp_f32 v96, v96\n\tv_pk_add_f32 v[80:81], v[80:81], v[104:105]\n\tv_exp_f32 v97, v97\n\tv_pk_add_f32 v[82:83], v[82:83], v[106:107]\n\tv_exp_f32 v98, v98\n\tv_pk_add_f32 v[84:85], v[84:85], v[108:109]\n\tv_exp_f32 v99, v99\n\tbuffer_load_dwordx4 v[156:159], v8, s[8:11], s68 offen offset:0 sc1\n\t" \
    "v_pk_add_f32 v[86:87], v[86:87], v[110:111]\n\tv_exp_f32 v100, v100\n\tv_pk_mul_f32 v[104:105], v[80:81], v[80:81]\n\tv_exp_f32 v101, v101\n\tv_pk_mul_f32 v[106:107], v[82:83], v[82:83]\n\tv_exp_f32 v102, v102\n\tv_pk_mul_f32 v[108:109], v[84:85], v[84:85]\n\tv_exp_f32 v103, v103\n\t" \
    "v_pk_mul_f32 v[110:111], v[86:87], v[86:87]\n\tv_pk_add_f32 v[96:97], v[96:97], 1.0 op_sel_hi:[1,0]\n\tv_pk_add_f32 v[98:99], v[98:99], 1.0 op_sel_hi:[1,0]\n\tv_pk_add_f32 v[100:101], v[100:101], 1.0 op_sel_hi:[1,0]\n\tv_pk_add_f32 v[102:103], v[102:103], 1.0 op_sel_hi:[1,0]\n\tv_pk_fma_f32 v[104:105], v[104:105], v[246:247], v[246:247] op_sel:[0,0,1] op_sel_hi:[1,0,1]\n\tv_rcp_f32 v96, v96\n\tv_pk_fma_f32 v[106:107], v[106:107], v[246:247], v[246:247] op_sel:[0,0,1] op_sel_hi:[1,0,1]\n\t" \
    "v_rcp_f32 v97, v97\n\tv_pk_fma_f32 v[108:109], v[108:109], v[246:247], v[246:247] op_sel:[0,0,1] op_sel_hi:[1,0,1]\n\tv_rcp_f32 v98, v98\n\tv_pk_fma_f32 v[110:111], v[110:111], v[246:247], v[246:247] op_sel:[0,0,1] op_sel_hi:[1,0,1]\n\tv_rcp_f32 v99, v99\n\tv_pk_mul_f32 v[104:105], v[104:105], v[80:81]\n\tbuffer_load_dwordx4 v[160:163], v8, s[8:11], s68 offen offset:1024 sc1\n\tv_rcp_f32 v100, v100\n\t" \
    "v_pk_mul_f32 v[106:107], v[106:107], v[82:83]\n\tv_rcp_f32 v101, v101\n\tv_pk_mul_f32 v[108:109], v[108:109], v[84:85]\n\tv_rcp_f32 v102, v102\n\tv_pk_mul_f32 v[110:111], v[110:111], v[86:87]\n\tv_rcp_f32 v103, v103\n\ts_waitcnt lgkmcnt(4)\n\tv_exp_f32 v104, v104\n\t" \
    "v_pk_mul_f32 v[96:97], v[96:97], v[72:73]\n\tv_exp_f32 v105, v105\n\tv_pk_mul_f32 v[98:99], v[98:99], v[74:75]\n\tv_exp_f32 v106, v106\n\tv_pk_mul_f32 v[100:101], v[100:101], v[76:77]\n\tv_exp_f32 v107, v107\n\tv_pk_mul_f32 v[102:103], v[102:103], v[78:79]\n\tv_exp_f32 v108, v108\n\t" \
    "v_cvt_pk_bf16_f32 v72, v96, v97\n\tv_exp_f32 v109, v109\n\tv_cvt_pk_bf16_f32 v73, v98, v99\n\tbuffer_load_dwordx4 v[164:167], v8, s[8:11], s68 offen offset:2048 sc1\n\ts_add_u32 s68, s46, 6144\n\tv_exp_f32 v110, v110\n\tv_cvt_pk_bf16_f32 v74, v100, v101\n\tv_exp_f32 v111, v111\n\t" \
    "v_cvt_pk_bf16_f32 v75, v102, v103\n\tv_pk_add_f32 v[104:105], v[104:105], 1.0 op_sel_hi:[1,0]\n\tv_pk_add_f32 v[106:107], v[106:107], 1.0 op_sel_hi:[1,0]\n\tv_pk_add_f32 v[108:109], v[108:109], 1.0 op_sel_hi:[1,0]\n\tv_pk_add_f32 v[110:111], v[110:111], 1.0 op_sel_hi:[1,0]\n\tv_pk_add_f32 v[88:89], v[88:89], v[112:113]\n\tv_rcp_f32 v104, v104\n\tv_pk_add_f32 v[90:91], v[90:91], v[114:115]\n\t" \
    "v_rcp_f32 v105, v105\n\tv_pk_add_f32 v[92:93], v[92:93], v[116:117]\n\tv_rcp_f32 v106, v106\n\tv_pk_add_f32 v[94:95], v[94:95], v[118:119]\n\tv_rcp_f32 v107, v107\n\tv_pk_mul_f32 v[112:113], v[88:89], v[88:89]\n\tv_rcp_f32 v108, v108\n\tv_pk_mul_f32 v[114:115], v[90:91], v[90:91]\n\t" \
    "v_rcp_f32 v109, v109\n\tv_pk_mul_f32 v[116:117], v[92:93], v[92:93]\n\tbuffer_load_dwordx4 v[168:171], v8, s[8:11], s68 offen offset:0 sc1\n\tv_rcp_f32 v110, v110\n\tv_pk_mul_f32 v[118:119], v[94:95], v[94:95]\n\tv_rcp_f32 v111, v111\n\tv_pk_fma_f32 v[112:113], v[112:113], v[246:247], v[246:247] op_sel:[0,0,1] op_sel_hi:[1,0,1]\n\tv_pk_mul_f32 v[104:105], v[104:105], v[80:81]\n\t" \
    "v_pk_mul_f32 v[106:107], v[106:107], v[82:83]\n\tv_pk_mul_f32 v[108:109], v[108:109], v[84:85]\n\tv_pk_mul_f32 v[110:111], v[110:111], v[86:87]\n\tv_cvt_pk_bf16_f32 v80, v104, v105\n\tv_cvt_pk_bf16_f32 v81, v106, v107\n\tv_cvt_pk_bf16_f32 v82, v108, v109\n\tv_cvt_pk_bf16_f32 v83, v110, v111\n\tv_pk_fma_f32 v[114:115], v[114:115], v[246:247], v[246:247] op_sel:[0,0,1] op_sel_hi:[1,0,1]\n\t" \
    "v_pk_fma_f32 v[116:117], v[116:117], v[246:247], v[246:247] op_sel:[0,0,1] op_sel_hi:[1,0,1]\n\tv_pk_fma_f32 v[118:119], v[118:119], v[246:247], v[246:247] op_sel:[0,0,1] op_sel_hi:[1,0,1]\n\tv_pk_mul_f32 v[112:113], v[112:113], v[88:89]\n\tv_pk_mul_f32 v[114:115], v[114:115], v[90:91]\n\tv_pk_mul_f32 v[116:117], v[116:117], v[92:93]\n\tv_pk_mul_f32 v[118:119], v[118:119], v[94:95]\n\tv_exp_f32 v112, v112\n\tv_exp_f32 v113, v113\n\t" \
    "v_exp_f32 v114, v114\n\tv_exp_f32 v115, v115\n\tv_exp_f32 v116, v116\n\tbuffer_load_dwordx4 v[172:175], v8, s[8:11], s68 offen offset:1024 sc1\n\tv_exp_f32 v117, v117\n\tv_exp_f32 v118, v118\n\tv_exp_f32 v119, v119\n\tv_pk_add_f32 v[112:113], v[112:113], 1.0 op_sel_hi:[1,0]\n\t" \
    "v_pk_add_f32 v[114:115], v[114:115], 1.0 op_sel_hi:[1,0]\n\tv_pk_add_f32 v[116:117], v[116:117], 1.0 op_sel_hi:[1,0]\n\tv_pk_add_f32 v[118:119], v[118:119], 1.0 op_sel_hi:[1,0]\n\tv_rcp_f32 v112, v112\n\tv_rcp_f32 v113, v113\n\tv_rcp_f32 v114, v114\n\tv_rcp_f32 v115, v115\n\tv_rcp_f32 v116, v116\n\t" \
    "v_rcp_f32 v117, v117\n\tv_rcp_f32 v118, v118\n\tv_rcp_f32 v119, v119\n\tv_pk_mul_f32 v[112:113], v[112:113], v[88:89]\n\tv_pk_mul_f32 v[114:115], v[114:115], v[90:91]\n\tv_pk_mul_f32 v[116:117], v[116:117], v[92:93]\n\tv_pk_mul_f32 v[118:119], v[118:119], v[94:95]\n\tbuffer_load_dwordx4 v[176:179], v8, s[8:11], s68 offen offset:2048 sc1\n\t" \
    "v_cvt_pk_bf16_f32 v88, v112, v113\n\tv_cvt_pk_bf16_f32 v89, v114, v115\n\tv_cvt_pk_bf16_f32 v90, v116, v117\n\tv_cvt_pk_bf16_f32 v91, v118, v119\n\tbuffer_store_dwordx4 v[72:75], v8, s[8:11], s73 offen offset:0 sc1\n\tbuffer_store_dwordx4 v[80:83], v8, s[8:11], s73 offen offset:1024 sc1\n\tbuffer_store_dwordx4 v[88:91], v8, s[8:11], s73 offen offset:2048 sc1\n\ts_cmp_lg_u32 s39, 2\n\t" \
    "s_cbranch_scc1 102f\n\t108:\n\ts_waitcnt vmcnt(9)\n\ts_barrier\n\tv_mfma_f32_16x16x32_bf16 v[72:75], v[144:147], v[16:19], 0\n\tds_read_b64_tr_b16 v[36:37], v225 offset:0\n\tds_read_b64_tr_b16 v[38:39], v225 offset:2048\n\ts_add_u32 s68, s46, 9216\n\t" \
    "v_mfma_f32_16x16x32_bf16 v[80:83], v[148:151], v[16:19], 0\n\tbuffer_load_dwordx4 v[192:195], v8, s[8:11], s68 offen offset:0 sc1\n\tv_mfma_f32_16x16x32_bf16 v[88:91], v[152:155], v[16:19], 0\n\tv_mfma_f32_16x16x32_bf16 v[76:79], v[144:147], v[20:23], 0\n\tds_read_b64_tr_b16 v[40:41], v226 offset:0\n\tds_read_b64_tr_b16 v[42:43], v226 offset:2048\n\tv_mfma_f32_16x16x32_bf16 v[84:87], v[148:151], v[20:23], 0\n\tbuffer_load_dwordx4 v[196:199], v8, s[8:11], s68 offen offset:1024 sc1\n\t" \
    "v_mfma_f32_16x16x32_bf16 v[92:95], v[152:155], v[20:23], 0\n\tv_mfma_f32_16x16x32_bf16 v[96:99], v[144:147], v[24:27], 0\n\tds_read_b64_tr_b16 v[44:45], v227 offset:0\n\tds_read_b64_tr_b16 v[46:47], v227 offset:2048\n\tv_mfma_f32_16x16x32_bf16 v[104:107], v[148:151], v[24:27], 0\n\tbuffer_load_dwordx4 v[200:203], v8, s[8:11], s68 offen offset:2048 sc1\n\ts_add_u32 s68, s46, 12288\n\tv_mfma_f32_16x16x32_bf16 v[112:115], v[152:155], v[24:27], 0\n\t" \
    "s_waitcnt lgkmcnt(8)\n\tv_mfma_f32_16x16x32_bf16 v[100:103], v[144:147], v[28:31], 0\n\tds_read_b64_tr_b16 v[16:17], v4 offset:4096\n\tds_read_b64_tr_b16 v[18:19], v4 offset:6144\n\tv_mfma_f32_16x16x32_bf16 v[108:111], v[148:151], v[28:31], 0\n\tbuffer_load_dwordx4 v[216:219], v8, s[8:11], s68 offen offset:0 sc1\n\tv_mfma_f32_16x16x32_bf16 v[116:119], v[152:155], v[28:31], 0\n\ts_waitcnt lgkmcnt(8)\n\t" \
    "v_mfma_f32_16x16x32_bf16 v[120:123], v[144:147], v[32:35], 0\n\tds_read_b64_tr_b16 v[20:21], v5 offset:4096\n\tds_read_b64_tr_b16 v[22:23], v5 offset:6144\n\tv_mfma_f32_16x16x32_bf16 v[128:131], v[148:151], v[32:35], 0\n\tbuffer_load_dwordx4 v[204:207], v8, s[8:11], s68 offen offset:1024 sc1\n\tv_mfma_f32_16x16x32_bf16 v[136:139], v[152:155], v[32:35], 0\n\ts_waitcnt lgkmcnt(8)\n\tv_mfma_f32_16x16x32_bf16 v[124:127], v[144:147], v[36:39], 0\n\t" \
    "ds_read_b64_tr_b16 v[24:25], v6 offset:4096\n\tds_read_b64_tr_b16 v[26:27], v6 offset:6144\n\tv_mfma_f32_16x16x32_bf16 v[132:135], v[148:151], v[36:39], 0\n\tbuffer_load_dwordx4 v[208:211], v8, s[8:11], s68 offen offset:2048 sc1\n\ts_add_u32 s68, s46, 15360\n\tv_mfma_f32_16x16x32_bf16 v[140:143], v[152:155], v[36:39], 0\n\ts_waitcnt lgkmcnt(8)\n\tv_mfma_f32_16x16x32_bf16 v[48:51], v[144:147], v[40:43], v[228:231]\n\t" \
    "ds_read_b64_tr_b16 v[28:29], v7 offset:4096\n\tds_read_b64_tr_b16 v[30:31], v7 offset:6144\n\tv_mfma_f32_16x16x32_bf16 v[56:59], v[148:151], v[40:43], v[228:231]\n\tv_mfma_f32_16x16x32_bf16 v[64:67], v[152:155], v[40:43], v[228:231]\n\ts_waitcnt lgkmcnt(8)\n\tv_mfma_f32_16x16x32_bf16 v[52:55], v[144:147], v[44:47], v[232:235]\n\tds_read_b64_tr_b16 v[32:33], v224 offset:4096\n\tds_read_b64_tr_b16 v[34:35], v224 offset:6144\n\t" \
    "v_mfma_f32_16x16x32_bf16 v[60:63], v[148:151], v[44:47], v[232:235]\n\tbuffer_load_dwordx4 v[212:215], v8, s[8:11], s68 offen offset:0 sc1\n\tv_mfma_f32_16x16x32_bf16 v[68:71], v[152:155], v[44:47], v[232:235]\n\ts_waitcnt vmcnt(13) lgkmcnt(8)\n\tv_mfma_f32_16x16x32_bf16 v[72:75], v[156:159], v[16:19], v[72:75]\n\tds_read_b64_tr_b16 v[36:37], v225 offset:4096\n\tds_read_b64_tr_b16 v[38:39], v225 offset:6144\n\tv_mfma_f32_16x16x32_bf16 v[80:83], v[160:163], v[16:19], v[80:83]\n\t" \
    "buffer_load_dwordx4 v[180:183], v8, s[8:11], s68 offen offset:1024 sc1\n\tv_mfma_f32_16x16x32_bf16 v[88:91], v[164:167], v[16:19], v[88:91]\n\ts_waitcnt lgkmcnt(8)\n\tv_mfma_f32_16x16x32_bf16 v[76:79], v[156:159], v[20:23], v[76:79]\n\tds_read_b64_tr_b16 v[40:41], v226 offset:4096\n\tds_read_b64_tr_b16 v[42:43], v226 offset:6144\n\tv_mfma_f32_16x16x32_bf16 v[84:87], v[160:163], v[20:23], v[84:87]\n\tbuffer_load_dwordx4 v[184:187], v8, s[8:11], s68 offen offset:2048 sc1\n\t" \
    "s_add_u32 s68, s46, 18432\n\tv_mfma_f32_16x16x32_bf16 v[92:95], v[164:167], v[20:23], v[92:95]\n\ts_waitcnt lgkmcnt(8)\n\tv_mfma_f32_16x16x32_bf16 v[96:99], v[156:159], v[24:27], v[96:99]\n\tds_read_b64_tr_b16 v[44:45], v227 offset:4096\n\tds_read_b64_tr_b16 v[46:47], v227 offset:6144\n\tv_mfma_f32_16x16x32_bf16 v[104:107], v[160:163], v[24:27], v[104:107]\n\tv_mfma_f32_16x16x32_bf16 v[112:115], v[164:167], v[24:27], v[112:115]\n\t" \
    "s_waitcnt lgkmcnt(8)\n\tv_mfma_f32_16x16x32_bf16 v[100:103], v[156:159], v[28:31], v[100:103]\n\tds_read_b64_tr_b16 v[16:17], v4 offset:8192\n\tds_read_b64_tr_b16 v[18:19], v4 offset:10240\n\tv_mfma_f32_16x16x32_bf16 v[108:111], v[160:163], v[28:31], v[108:111]\n\tbuffer_load_dwordx4 v[144:147], v8, s[8:11], s68 offen offset:0 sc1\n\tv_mfma_f32_16x16x32_bf16 v[116:119], v[164:167], v[28:31], v[116:119]\n\ts_waitcnt lgkmcnt(8)\n\t" \
    "v_mfma_f32_16x16x32_bf16 v[120:123], v[156:159], v[32:35], v[120:123]\n\tds_read_b64_tr_b16 v[20:21], v5 offset:8192\n\tds_read_b64_tr_b16 v[22:23], v5 offset:10240\n\tv_mfma_f32_16x16x32_bf16 v[128:131], v[160:163], v[32:35], v[128:131]\n\tbuffer_load_dwordx4 v[148:151], v8, s[8:11], s68 offen offset:1024 sc1\n\tv_mfma_f32_16x16x32_bf16 v[136:139], v[164:167], v[32:35], v[136:139]\n\ts_waitcnt lgkmcnt(8)\n\tv_mfma_f32_16x16x32_bf16 v[124:127], v[156:159], v[36:39], v[124:127]\n\t" \
    "ds_read_b64_tr_b16 v[24:25], v6 offset:8192\n\tds_read_b64_tr_b16 v[26:27], v6 offset:10240\n\tv_mfma_f32_16x16x32_bf16 v[132:135], v[160:163], v[36:39], v[132:135]\n\tv_mfma_f32_16x16x32_bf16 v[140:143], v[164:167], v[36:39], v[140:143]\n\ts_waitcnt lgkmcnt(8)\n\tv_mfma_f32_16x16x32_bf16 v[48:51], v[156:159], v[40:43], v[48:51]\n\tds_read_b64_tr_b16 v[28:29], v7 offset:8192\n\tds_read_b64_tr_b16 v[30:31], v7 offset:10240\n\t" \
    "v_mfma_f32_16x16x32_bf16 v[56:59], v[160:163], v[40:43], v[56:59]\n\tbuffer_load_dwordx4 v[152:155], v8, s[8:11], s68 offen offset:2048 sc1\n\ts_add_u32 s68, s46, 21504\n\tv_mfma_f32_16x16x32_bf16 v[64:67], v[164:167], v[40:43], v[64:67]\n\ts_waitcnt lgkmcnt(8)\n\tv_mfma_f32_16x16x32_bf16 v[52:55], v[156:159], v[44:47], v[52:55]\n\tds_read_b64_tr_b16 v[32:33], v224 offset:8192\n\tds_read_b64_tr_b16 v[34:35], v224 offset:10240\n\t" \
    "v_mfma_f32_16x16x32_bf16 v[60:63], v[160:163], v[44:47], v[60:63]\n\tbuffer_load_dwordx4 v[188:191], v8, s[8:11], s68 offen offset:0 sc1\n\tv_mfma_f32_16x16x32_bf16 v[68:71], v[164:167], v[44:47], v[68:71]\n\ts_barrier\n\ts_waitcnt vmcnt(16) lgkmcnt(8)\n\tv_mfma_f32_16x16x32_bf16 v[72:75], v[168:171], v[16:19], v[72:75]\n\tds_read_b64_tr_b16 v[36:37], v225 offset:8192\n\tds_read_b64_tr_b16 v[38:39], v225 offset:10240\n\t" \
    "v_mfma_f32_16x16x32_bf16 v[80:83], v[172:175], v[16:19], v[80:83]\n\tbuffer_load_dwordx4 a[0:3], v8, s[8:11], s68 offen offset:1024 sc1\n\tv_mfma_f32_16x16x32_bf16 v[88:91], v[176:179], v[16:19], v[88:91]\n\ts_waitcnt lgkmcnt(8)\n\tv_mfma_f32_16x16x32_bf16 v[76:79], v[168:171], v[20:23], v[76:79]\n\tds_read_b64_tr_b16 v[40:41], v226 offset:8192\n\tds_read_b64_tr_b16 v[42:43], v226 offset:10240\n\tv_mfma_f32_16x16x32_bf16 v[84:87], v[172:175], v[20:23], v[84:87]\n\t" \
    "buffer_load_dwordx4 a[4:7], v8, s[8:11], s68 offen offset:2048 sc1\n\ts_add_u32 s68, s46, 24576\n\tv_mfma_f32_16x16x32_bf16 v[92:95], v[176:179], v[20:23], v[92:95]\n\ts_waitcnt lgkmcnt(8)\n\tv_mfma_f32_16x16x32_bf16 v[96:99], v[168:171], v[24:27], v[96:99]\n\tds_read_b64_tr_b16 v[44:45], v227 offset:8192\n\tds_read_b64_tr_b16 v[46:47], v227 offset:10240\n\tv_mfma_f32_16x16x32_bf16 v[104:107], v[172:175], v[24:27], v[104:107]\n\t" \
    "buffer_load_dwordx4 v[156:159], v8, s[8:11], s68 offen offset:0 sc1\n\tv_mfma_f32_16x16x32_bf16 v[112:115], v[176:179], v[24:27], v[112:115]\n\ts_waitcnt lgkmcnt(8)\n\tv_mfma_f32_16x16x32_bf16 v[100:103], v[168:171], v[28:31], v[100:103]\n\tds_read_b64_tr_b16 v[16:17], v4 offset:12288\n\tds_read_b64_tr_b16 v[18:19], v4 offset:14336\n\tv_mfma_f32_16x16x32_bf16 v[108:111], v[172:175], v[28:31], v[108:111]\n\tv_mfma_f32_16x16x32_bf16 v[116:119], v[176:179], v[28:31], v[116:119]\n\t" \
    "s_waitcnt lgkmcnt(8)\n\tv_mfma_f32_16x16x32_bf16 v[120:123], v[168:171], v[32:35], v[120:123]\n\tds_read_b64_tr_b16 v[20:21], v5 offset:12288\n\tds_read_b64_tr_b16 v[22:23], v5 offset:14336\n\tv_mfma_f32_16x16x32_bf16 v[128:131], v[172:175], v[32:35], v[128:131]\n\tbuffer_load_dwordx4 v[160:163], v8, s[8:11], s68 offen offset:1024 sc1\n\tv_mfma_f32_16x16x32_bf16 v[136:139], v[176:179], v[32:35], v[136:139]\n\ts_waitcnt lgkmcnt(8)\n\t" \
    "v_mfma_f32_16x16x32_bf16 v[124:127], v[168:171], v[36:39], v[124:127]\n\tds_read_b64_tr_b16 v[24:25], v6 offset:12288\n\tds_read_b64_tr_b16 v[26:27], v6 offset:14336\n\tv_mfma_f32_16x16x32_bf16 v[132:135], v[172:175], v[36:39], v[132:135]\n\tbuffer_load_dwordx4 v[164:167], v8, s[8:11], s68 offen offset:2048 sc1\n\ts_add_u32 s68, s46, 27648\n\tv_mfma_f32_16x16x32_bf16 v[140:143], v[176:179], v[36:39], v[140:143]\n\ts_waitcnt lgkmcnt(8)\n\t" \
    "v_mfma_f32_16x16x32_bf16 v[48:51], v[168:171], v[40:43], v[48:51]\n\tds_read_b64_tr_b16 v[28:29], v7 offset:12288\n\tds_read_b64_tr_b16 v[30:31], v7 offset:14336\n\tv_mfma_f32_16x16x32_bf16 v[56:59], v[172:175], v[40:43], v[56:59]\n\tbuffer_load_dwordx4 a[8:11], v8, s[8:11], s68 offen offset:0 sc1\n\tv_mfma_f32_16x16x32_bf16 v[64:67], v[176:179], v[40:43], v[64:67]\n\ts_waitcnt lgkmcnt(8)\n\tv_mfma_f32_16x16x32_bf16 v[52:55], v[168:171], v[44:47], v[52:55]\n\t" \
    "ds_read_b64_tr_b16 v[32:33], v224 offset:12288\n\tds_read_b64_tr_b16 v[34:35], v224 offset:14336\n\tv_mfma_f32_16x16x32_bf16 v[60:63], v[172:175], v[44:47], v[60:63]\n\tv_mfma_f32_16x16x32_bf16 v[68:71], v[176:179], v[44:47], v[68:71]\n\ts_waitcnt vmcnt(19)\n\ts_add_u32 s73, s49, 4096\n\tbuffer_store_dwordx2 v[248:249], off, s[8:11], s73 sc1\n\ts_waitcnt vmcnt(17) lgkmcnt(8)\n\t" \
    "v_mfma_f32_16x16x32_bf16 v[72:75], v[192:195], v[16:19], v[72:75]\n\tds_read_b64_tr_b16 v[36:37], v225 offset:12288\n\tds_read_b64_tr_b16 v[38:39], v225 offset:14336\n\tv_mfma_f32_16x16x32_bf16 v[80:83], v[196:199], v[16:19], v[80:83]\n\tbuffer_load_dwordx4 a[12:15], v8, s[8:11], s68 offen offset:1024 sc1\n\tv_mfma_f32_16x16x32_bf16 v[88:91], v[200:203], v[16:19], v[88:91]\n\ts_waitcnt lgkmcnt(8)\n\tv_mfma_f32_16x16x32_bf16 v[76:79], v[192:195], v[20:23], v[76:79]\n\t" \
    "ds_read_b64_tr_b16 v[40:41], v226 offset:12288\n\tds_read_b64_tr_b16 v[42:43], v226 offset:14336\n\tv_mfma_f32_16x16x32_bf16 v[84:87], v[196:199], v[20:23], v[84:87]\n\tbuffer_load_dwordx4 a[16:19], v8, s[8:11], s68 offen offset:2048 sc1\n\ts_mov_b32 s69, 3276800\n\ts_add_u32 m0, s54, 49152\n\tv_mfma_f32_16x16x32_bf16 v[92:95], v[200:203], v[20:23], v[92:95]\n\ts_waitcnt lgkmcnt(8)\n\t" \
    "v_mfma_f32_16x16x32_bf16 v[96:99], v[192:195], v[24:27], v[96:99]\n\tds_read_b64_tr_b16 v[44:45], v227 offset:12288\n\tds_read_b64_tr_b16 v[46:47], v227 offset:14336\n\tv_mfma_f32_16x16x32_bf16 v[104:107], v[196:199], v[24:27], v[104:107]\n\tv_mfma_f32_16x16x32_bf16 v[112:115], v[200:203], v[24:27], v[112:115]\n\ts_waitcnt lgkmcnt(8)\n\tv_mfma_f32_16x16x32_bf16 v[100:103], v[192:195], v[28:31], v[100:103]\n\tds_read_b64_tr_b16 v[16:17], v4 offset:16384\n\t" \
    "ds_read_b64_tr_b16 v[18:19], v4 offset:18432\n\tv_mfma_f32_16x16x32_bf16 v[108:111], v[196:199], v[28:31], v[108:111]\n\tbuffer_load_dwordx4 v12, s[16:19], s69 offen lds\n\ts_mov_b32 s69, 3276800\n\ts_add_u32 m0, s54, 114688\n\tv_mfma_f32_16x16x32_bf16 v[116:119], v[200:203], v[28:31], v[116:119]\n\ts_waitcnt lgkmcnt(8)\n\tv_mfma_f32_16x16x32_bf16 v[120:123], v[192:195], v[32:35], v[120:123]\n\t" \
    "ds_read_b64_tr_b16 v[20:21], v5 offset:16384\n\tds_read_b64_tr_b16 v[22:23], v5 offset:18432\n\tv_mfma_f32_16x16x32_bf16 v[128:131], v[196:199], v[32:35], v[128:131]\n\tbuffer_load_dwordx4 v13, s[16:19], s69 offen lds\n\ts_add_u32 s68, s46, 30720\n\tv_mfma_f32_16x16x32_bf16 v[136:139], v[200:203], v[32:35], v[136:139]\n\ts_waitcnt lgkmcnt(8)\n\tv_mfma_f32_16x16x32_bf16 v[124:127], v[192:195], v[36:39], v[124:127]\n\t" \
    "ds_read_b64_tr_b16 v[24:25], v6 offset:16384\n\tds_read_b64_tr_b16 v[26:27], v6 offset:18432\n\tv_mfma_f32_16x16x32_bf16 v[132:135], v[196:199], v[36:39], v[132:135]\n\tbuffer_load_dwordx4 v[168:171], v8, s[8:11], s68 offen offset:0 sc1\n\tv_mfma_f32_16x16x32_bf16 v[140:143], v[200:203], v[36:39], v[140:143]\n\ts_waitcnt lgkmcnt(8)\n\tv_mfma_f32_16x16x32_bf16 v[48:51], v[192:195], v[40:43], v[48:51]\n\tds_read_b64_tr_b16 v[28:29], v7 offset:16384\n\t" \
    "ds_read_b64_tr_b16 v[30:31], v7 offset:18432\n\tv_mfma_f32_16x16x32_bf16 v[56:59], v[196:199], v[40:43], v[56:59]\n\tv_mfma_f32_16x16x32_bf16 v[64:67], v[200:203], v[40:43], v[64:67]\n\ts_waitcnt lgkmcnt(8)\n\tv_mfma_f32_16x16x32_bf16 v[52:55], v[192:195], v[44:47], v[52:55]\n\tds_read_b64_tr_b16 v[32:33], v224 offset:16384\n\tds_read_b64_tr_b16 v[34:35], v224 offset:18432\n\tv_mfma_f32_16x16x32_bf16 v[60:63], v[196:199], v[44:47], v[60:63]\n\t" \
    "buffer_load_dwordx4 v[172:175], v8, s[8:11], s68 offen offset:1024 sc1\n\tv_mfma_f32_16x16x32_bf16 v[68:71], v[200:203], v[44:47], v[68:71]\n\ts_barrier\n\ts_waitcnt vmcnt(20) lgkmcnt(8)\n\tv_mfma_f32_16x16x32_bf16 v[72:75], v[216:219], v[16:19], v[72:75]\n\tds_read_b64_tr_b16 v[36:37], v225 offset:16384\n\tds_read_b64_tr_b16 v[38:39], v225 offset:18432\n\tv_mfma_f32_16x16x32_bf16 v[80:83], v[204:207], v[16:19], v[80:83]\n\t" \
    "buffer_load_dwordx4 v[176:179], v8, s[8:11], s68 offen offset:2048 sc1\n\ts_add_u32 s68, s46, 33792\n\tv_mfma_f32_16x16x32_bf16 v[88:91], v[208:211], v[16:19], v[88:91]\n\ts_waitcnt lgkmcnt(8)\n\tv_mfma_f32_16x16x32_bf16 v[76:79], v[216:219], v[20:23], v[76:79]\n\tds_read_b64_tr_b16 v[40:41], v226 offset:16384\n\tds_read_b64_tr_b16 v[42:43], v226 offset:18432\n\tv_mfma_f32_16x16x32_bf16 v[84:87], v[204:207], v[20:23], v[84:87]\n\t" \
    "buffer_load_dwordx4 v[192:195], v8, s[8:11], s68 offen offset:0 sc1\n\tv_mfma_f32_16x16x32_bf16 v[92:95], v[208:211], v[20:23], v[92:95]\n\ts_waitcnt lgkmcnt(8)\n\tv_mfma_f32_16x16x32_bf16 v[96:99], v[216:219], v[24:27], v[96:99]\n\tds_read_b64_tr_b16 v[44:45], v227 offset:16384\n\tds_read_b64_tr_b16 v[46:47], v227 offset:18432\n\tv_mfma_f32_16x16x32_bf16 v[104:107], v[204:207], v[24:27], v[104:107]\n\tbuffer_load_dwordx4 v[196:199], v8, s[8:11], s68 offen offset:1024 sc1\n\t" \
    "v_mfma_f32_16x16x32_bf16 v[112:115], v[208:211], v[24:27], v[112:115]\n\ts_waitcnt lgkmcnt(8)\n\tv_mfma_f32_16x16x32_bf16 v[100:103], v[216:219], v[28:31], v[100:103]\n\tds_read_b64_tr_b16 v[16:17], v4 offset:20480\n\tds_read_b64_tr_b16 v[18:19], v4 offset:22528\n\tv_mfma_f32_16x16x32_bf16 v[108:111], v[204:207], v[28:31], v[108:111]\n\tbuffer_load_dwordx4 v[200:203], v8, s[8:11], s68 offen offset:2048 sc1\n\ts_mov_b32 s69, 3145728\n\t" \
    "s_add_u32 m0, s54, 53248\n\tv_mfma_f32_16x16x32_bf16 v[116:119], v[208:211], v[28:31], v[116:119]\n\ts_waitcnt lgkmcnt(8)\n\tv_mfma_f32_16x16x32_bf16 v[120:123], v[216:219], v[32:35], v[120:123]\n\tds_read_b64_tr_b16 v[20:21], v5 offset:20480\n\tds_read_b64_tr_b16 v[22:23], v5 offset:22528\n\tv_mfma_f32_16x16x32_bf16 v[128:131], v[204:207], v[32:35], v[128:131]\n\tv_mfma_f32_16x16x32_bf16 v[136:139], v[208:211], v[32:35], v[136:139]\n\t" \
    "s_waitcnt lgkmcnt(8)\n\tv_mfma_f32_16x16x32_bf16 v[124:127], v[216:219], v[36:39], v[124:127]\n\tds_read_b64_tr_b16 v[24:25], v6 offset:20480\n\tds_read_b64_tr_b16 v[26:27], v6 offset:22528\n\tv_mfma_f32_16x16x32_bf16 v[132:135], v[204:207], v[36:39], v[132:135]\n\tbuffer_load_dwordx4 v12, s[16:19], s69 offen lds\n\ts_mov_b32 s69, 3145728\n\ts_add_u32 m0, s54, 118784\n\t" \
    "v_mfma_f32_16x16x32_bf16 v[140:143], v[208:211], v[36:39], v[140:143]\n\ts_waitcnt lgkmcnt(8)\n\tv_mfma_f32_16x16x32_bf16 v[48:51], v[216:219], v[40:43], v[48:51]\n\tds_read_b64_tr_b16 v[28:29], v7 offset:20480\n\tds_read_b64_tr_b16 v[30:31], v7 offset:22528\n\tv_mfma_f32_16x16x32_bf16 v[56:59], v[204:207], v[40:43], v[56:59]\n\tbuffer_load_dwordx4 v13, s[16:19], s69 offen lds\n\ts_mov_b32 s69, 3801088\n\t" \
    "s_add_u32 m0, s54, 57344\n\tv_mfma_f32_16x16x32_bf16 v[64:67], v[208:211], v[40:43], v[64:67]\n\ts_waitcnt lgkmcnt(8)\n\tv_mfma_f32_16x16x32_bf16 v[52:55], v[216:219], v[44:47], v[52:55]\n\tds_read_b64_tr_b16 v[32:33], v224 offset:20480\n\tds_read_b64_tr_b16 v[34:35], v224 offset:22528\n\tv_mfma_f32_16x16x32_bf16 v[60:63], v[204:207], v[44:47], v[60:63]\n\tv_mfma_f32_16x16x32_bf16 v[68:71], v[208:211], v[44:47], v[68:71]\n\t" \
    "s_waitcnt vmcnt(23) lgkmcnt(8)\n\tv_mfma_f32_16x16x32_bf16 v[72:75], v[212:215], v[16:19], v[72:75]\n\tds_read_b64_tr_b16 v[36:37], v225 offset:20480\n\tds_read_b64_tr_b16 v[38:39], v225 offset:22528\n\tv_mfma_f32_16x16x32_bf16 v[80:83], v[180:183], v[16:19], v[80:83]\n\tbuffer_load_dwordx4 v12, s[16:19], s69 offen lds\n\ts_mov_b32 s69, 3801088\n\ts_add_u32 m0, s54, 122880\n\t" \
    "v_mfma_f32_16x16x32_bf16 v[88:91], v[184:187], v[16:19], v[88:91]\n\ts_waitcnt lgkmcnt(8)\n\tv_mfma_f32_16x16x32_bf16 v[76:79], v[212:215], v[20:23], v[76:79]\n\tds_read_b64_tr_b16 v[40:41], v226 offset:20480\n\tds_read_b64_tr_b16 v[42:43], v226 offset:22528\n\tv_mfma_f32_16x16x32_bf16 v[84:87], v[180:183], v[20:23], v[84:87]\n\tbuffer_load_dwordx4 v13, s[16:19], s69 offen lds\n\ts_add_u32 s68, s46, 36864\n\t" \
    "v_mfma_f32_16x16x32_bf16 v[92:95], v[184:187], v[20:23], v[92:95]\n\ts_waitcnt lgkmcnt(8)\n\tv_mfma_f32_16x16x32_bf16 v[96:99], v[212:215], v[24:27], v[96:99]\n\tds_read_b64_tr_b16 v[44:45], v227 offset:20480\n\tds_read_b64_tr_b16 v[46:47], v227 offset:22528\n\tv_mfma_f32_16x16x32_bf16 v[104:107], v[180:183], v[24:27], v[104:107]\n\tbuffer_load_dwordx4 v[216:219], v8, s[8:11], s68 offen offset:0 sc1\n\tv_mfma_f32_16x16x32_bf16 v[112:115], v[184:187], v[24:27], v[112:115]\n\t" \
    "s_waitcnt lgkmcnt(8)\n\tv_mfma_f32_16x16x32_bf16 v[100:103], v[212:215], v[28:31], v[100:103]\n\tds_read_b64_tr_b16 v[16:17], v4 offset:24576\n\tds_read_b64_tr_b16 v[18:19], v4 offset:26624\n\tv_mfma_f32_16x16x32_bf16 v[108:111], v[180:183], v[28:31], v[108:111]\n\tv_mfma_f32_16x16x32_bf16 v[116:119], v[184:187], v[28:31], v[116:119]\n\ts_waitcnt lgkmcnt(8)\n\tv_mfma_f32_16x16x32_bf16 v[120:123], v[212:215], v[32:35], v[120:123]\n\t" \
    "ds_read_b64_tr_b16 v[20:21], v5 offset:24576\n\tds_read_b64_tr_b16 v[22:23], v5 offset:26624\n\tv_mfma_f32_16x16x32_bf16 v[128:131], v[180:183], v[32:35], v[128:131]\n\tbuffer_load_dwordx4 v[204:207], v8, s[8:11], s68 offen offset:1024 sc1\n\tv_mfma_f32_16x16x32_bf16 v[136:139], v[184:187], v[32:35], v[136:139]\n\ts_waitcnt lgkmcnt(8)\n\tv_mfma_f32_16x16x32_bf16 v[124:127], v[212:215], v[36:39], v[124:127]\n\tds_read_b64_tr_b16 v[24:25], v6 offset:24576\n\t" \
    "ds_read_b64_tr_b16 v[26:27], v6 offset:26624\n\tv_mfma_f32_16x16x32_bf16 v[132:135], v[180:183], v[36:39], v[132:135]\n\tbuffer_load_dwordx4 v[208:211], v8, s[8:11], s68 offen offset:2048 sc1\n\ts_add_u32 s68, s46, 39936\n\tv_mfma_f32_16x16x32_bf16 v[140:143], v[184:187], v[36:39], v[140:143]\n\ts_waitcnt lgkmcnt(8)\n\tv_mfma_f32_16x16x32_bf16 v[48:51], v[212:215], v[40:43], v[48:51]\n\tds_read_b64_tr_b16 v[28:29], v7 offset:24576\n\t" \
    "ds_read_b64_tr_b16 v[30:31], v7 offset:26624\n\tv_mfma_f32_16x16x32_bf16 v[56:59], v[180:183], v[40:43], v[56:59]\n\tbuffer_load_dwordx4 a[20:23], v8, s[8:11], s68 offen offset:0 sc1\n\tv_mfma_f32_16x16x32_bf16 v[64:67], v[184:187], v[40:43], v[64:67]\n\ts_waitcnt lgkmcnt(8)\n\tv_mfma_f32_16x16x32_bf16 v[52:55], v[212:215], v[44:47], v[52:55]\n\tds_read_b64_tr_b16 v[32:33], v224 offset:24576\n\tds_read_b64_tr_b16 v[34:35], v224 offset:26624\n\t" \
    "v_mfma_f32_16x16x32_bf16 v[60:63], v[180:183], v[44:47], v[60:63]\n\tv_mfma_f32_16x16x32_bf16 v[68:71], v[184:187], v[44:47], v[68:71]\n\ts_barrier\n\ts_waitcnt vmcnt(26) lgkmcnt(8)\n\tv_mfma_f32_16x16x32_bf16 v[72:75], v[144:147], v[16:19], v[72:75]\n\tds_read_b64_tr_b16 v[36:37], v225 offset:24576\n\tds_read_b64_tr_b16 v[38:39], v225 offset:26624\n\tv_mfma_f32_16x16x32_bf16 v[80:83], v[148:151], v[16:19], v[80:83]\n\t" \
    "buffer_load_dwordx4 a[24:27], v8, s[8:11], s68 offen offset:1024 sc1\n\tv_mfma_f32_16x16x32_bf16 v[88:91], v[152:155], v[16:19], v[88:91]\n\ts_waitcnt lgkmcnt(8)\n\tv_mfma_f32_16x16x32_bf16 v[76:79], v[144:147], v[20:23], v[76:79]\n\tds_read_b64_tr_b16 v[40:41], v226 offset:24576\n\tds_read_b64_tr_b16 v[42:43], v226 offset:26624\n\tv_mfma_f32_16x16x32_bf16 v[84:87], v[148:151], v[20:23], v[84:87]\n\tbuffer_load_dwordx4 a[28:31], v8, s[8:11], s68 offen offset:2048 sc1\n\t" \
    "s_mov_b32 s69, 3670016\n\ts_add_u32 m0, s54, 61440\n\tv_mfma_f32_16x16x32_bf16 v[92:95], v[152:155], v[20:23], v[92:95]\n\ts_waitcnt lgkmcnt(8)\n\tv_mfma_f32_16x16x32_bf16 v[96:99], v[144:147], v[24:27], v[96:99]\n\tds_read_b64_tr_b16 v[44:45], v227 offset:24576\n\tds_read_b64_tr_b16 v[46:47], v227 offset:26624\n\tv_mfma_f32_16x16x32_bf16 v[104:107], v[148:151], v[24:27], v[104:107]\n\t" \
    "buffer_load_dwordx4 v12, s[16:19], s69 offen lds\n\ts_mov_b32 s69, 3670016\n\ts_add_u32 m0, s54, 126976\n\tv_mfma_f32_16x16x32_bf16 v[112:115], v[152:155], v[24:27], v[112:115]\n\ts_waitcnt lgkmcnt(8)\n\tv_mfma_f32_16x16x32_bf16 v[100:103], v[144:147], v[28:31], v[100:103]\n\tds_read_b64_tr_b16 v[16:17], v4 offset:28672\n\tds_read_b64_tr_b16 v[18:19], v4 offset:30720\n\t" \
    "v_mfma_f32_16x16x32_bf16 v[108:111], v[148:151], v[28:31], v[108:111]\n\tbuffer_load_dwordx4 v13, s[16:19], s69 offen lds\n\ts_mov_b32 s69, 4325376\n\ts_add_u32 m0, s54, 0\n\tv_mfma_f32_16x16x32_bf16 v[116:119], v[152:155], v[28:31], v[116:119]\n\ts_waitcnt lgkmcnt(8)\n\tv_mfma_f32_16x16x32_bf16 v[120:123], v[144:147], v[32:35], v[120:123]\n\tds_read_b64_tr_b16 v[20:21], v5 offset:28672\n\t" \
    "ds_read_b64_tr_b16 v[22:23], v5 offset:30720\n\tv_mfma_f32_16x16x32_bf16 v[128:131], v[148:151], v[32:35], v[128:131]\n\tv_mfma_f32_16x16x32_bf16 v[136:139], v[152:155], v[32:35], v[136:139]\n\ts_waitcnt lgkmcnt(8)\n\tv_mfma_f32_16x16x32_bf16 v[124:127], v[144:147], v[36:39], v[124:127]\n\tds_read_b64_tr_b16 v[24:25], v6 offset:28672\n\tds_read_b64_tr_b16 v[26:27], v6 offset:30720\n\tv_mfma_f32_16x16x32_bf16 v[132:135], v[148:151], v[36:39], v[132:135]\n\t" \
    "buffer_load_dwordx4 v12, s[16:19], s69 offen lds\n\ts_mov_b32 s69, 4325376\n\ts_add_u32 m0, s54, 65536\n\tv_mfma_f32_16x16x32_bf16 v[140:143], v[152:155], v[36:39], v[140:143]\n\ts_waitcnt lgkmcnt(8)\n\tv_mfma_f32_16x16x32_bf16 v[48:51], v[144:147], v[40:43], v[48:51]\n\tds_read_b64_tr_b16 v[28:29], v7 offset:28672\n\tds_read_b64_tr_b16 v[30:31], v7 offset:30720\n\t" \
    "v_mfma_f32_16x16x32_bf16 v[56:59], v[148:151], v[40:43], v[56:59]\n\tbuffer_load_dwordx4 v13, s[16:19], s69 offen lds\n\ts_mov_b32 s69, 4194304\n\ts_add_u32 m0, s54, 4096\n\tv_mfma_f32_16x16x32_bf16 v[64:67], v[152:155], v[40:43], v[64:67]\n\ts_waitcnt lgkmcnt(8)\n\tv_mfma_f32_16x16x32_bf16 v[52:55], v[144:147], v[44:47], v[52:55]\n\tds_read_b64_tr_b16 v[32:33], v224 offset:28672\n\t" \
    "ds_read_b64_tr_b16 v[34:35], v224 offset:30720\n\tv_mfma_f32_16x16x32_bf16 v[60:63], v[148:151], v[44:47], v[60:63]\n\tbuffer_load_dwordx4 v12, s[16:19], s69 offen lds\n\ts_add_u32 s68, s46, 43008\n\tv_mfma_f32_16x16x32_bf16 v[68:71], v[152:155], v[44:47], v[68:71]\n\ts_waitcnt vmcnt(30) lgkmcnt(8)\n\tv_mfma_f32_16x16x32_bf16 v[72:75], v[188:191], v[16:19], v[72:75]\n\tds_read_b64_tr_b16 v[36:37], v225 offset:28672\n\t" \
    "ds_read_b64_tr_b16 v[38:39], v225 offset:30720\n\tv_mfma_f32_16x16x32_bf16 v[80:83], a[0:3], v[16:19], v[80:83]\n\tv_mfma_f32_16x16x32_bf16 v[88:91], a[4:7], v[16:19], v[88:91]\n\ts_waitcnt lgkmcnt(8)\n\tv_mfma_f32_16x16x32_bf16 v[76:79], v[188:191], v[20:23], v[76:79]\n\tds_read_b64_tr_b16 v[40:41], v226 offset:28672\n\tds_read_b64_tr_b16 v[42:43], v226 offset:30720\n\tv_mfma_f32_16x16x32_bf16 v[84:87], a[0:3], v[20:23], v[84:87]\n\t" \
    "buffer_load_dwordx4 v[212:215], v8, s[8:11], s68 offen offset:0 sc1\n\tv_mfma_f32_16x16x32_bf16 v[92:95], a[4:7], v[20:23], v[92:95]\n\ts_add_u32 s50, s50, 4096\n\tbuffer_load_dwordx2 v[250:251], v241, s[8:11], s50 offen sc1\n\ts_waitcnt lgkmcnt(8)\n\tv_mfma_f32_16x16x32_bf16 v[96:99], v[188:191], v[24:27], v[96:99]\n\tds_read_b64_tr_b16 v[44:45], v227 offset:28672\n\tds_read_b64_tr_b16 v[46:47], v227 offset:30720\n\t" \
    "v_mfma_f32_16x16x32_bf16 v[104:107], a[0:3], v[24:27], v[104:107]\n\tv_mfma_f32_16x16x32_bf16 v[112:115], a[4:7], v[24:27], v[112:115]\n\ts_waitcnt lgkmcnt(8)\n\tv_mfma_f32_16x16x32_bf16 v[100:103], v[188:191], v[28:31], v[100:103]\n\tds_read_b64_tr_b16 v[16:17], v4 offset:32768\n\tds_read_b64_tr_b16 v[18:19], v4 offset:34816\n\tv_mfma_f32_16x16x32_bf16 v[108:111], a[0:3], v[28:31], v[108:111]\n\tbuffer_load_dwordx4 v[180:183], v8, s[8:11], s68 offen offset:1024 sc1\n\t" \
    "v_mfma_f32_16x16x32_bf16 v[116:119], a[4:7], v[28:31], v[116:119]\n\ts_waitcnt lgkmcnt(8)\n\tv_mfma_f32_16x16x32_bf16 v[120:123], v[188:191], v[32:35], v[120:123]\n\tds_read_b64_tr_b16 v[20:21], v5 offset:32768\n\tds_read_b64_tr_b16 v[22:23], v5 offset:34816\n\tv_mfma_f32_16x16x32_bf16 v[128:131], a[0:3], v[32:35], v[128:131]\n\tv_mfma_f32_16x16x32_bf16 v[136:139], a[4:7], v[32:35], v[136:139]\n\ts_waitcnt lgkmcnt(8)\n\t" \
    "v_mfma_f32_16x16x32_bf16 v[124:127], v[188:191], v[36:39], v[124:127]\n\tds_read_b64_tr_b16 v[24:25], v6 offset:32768\n\tds_read_b64_tr_b16 v[26:27], v6 offset:34816\n\tv_mfma_f32_16x16x32_bf16 v[132:135], a[0:3], v[36:39], v[132:135]\n\tbuffer_load_dwordx4 v[184:187], v8, s[8:11], s68 offen offset:2048 sc1\n\ts_add_u32 s68, s46, 46080\n\tv_mfma_f32_16x16x32_bf16 v[140:143], a[4:7], v[36:39], v[140:143]\n\ts_waitcnt lgkmcnt(8)\n\t" \
    "v_mfma_f32_16x16x32_bf16 v[48:51], v[188:191], v[40:43], v[48:51]\n\tds_read_b64_tr_b16 v[28:29], v7 offset:32768\n\tds_read_b64_tr_b16 v[30:31], v7 offset:34816\n\tv_mfma_f32_16x16x32_bf16 v[56:59], a[0:3], v[40:43], v[56:59]\n\tbuffer_load_dwordx4 v[144:147], v8, s[8:11], s68 offen offset:0 sc1\n\tv_mfma_f32_16x16x32_bf16 v[64:67], a[4:7], v[40:43], v[64:67]\n\ts_waitcnt lgkmcnt(8)\n\tv_mfma_f32_16x16x32_bf16 v[52:55], v[188:191], v[44:47], v[52:55]\n\t" \
    "ds_read_b64_tr_b16 v[32:33], v224 offset:32768\n\tds_read_b64_tr_b16 v[34:35], v224 offset:34816\n\tv_mfma_f32_16x16x32_bf16 v[60:63], a[0:3], v[44:47], v[60:63]\n\tv_mfma_f32_16x16x32_bf16 v[68:71], a[4:7], v[44:47], v[68:71]\n\ts_barrier\n\ts_waitcnt vmcnt(32) lgkmcnt(8)\n\tv_mfma_f32_16x16x32_bf16 v[72:75], v[156:159], v[16:19], v[72:75]\n\tds_read_b64_tr_b16 v[36:37], v225 offset:32768\n\t" \
    "ds_read_b64_tr_b16 v[38:39], v225 offset:34816\n\tv_mfma_f32_16x16x32_bf16 v[80:83], v[160:163], v[16:19], v[80:83]\n\tbuffer_load_dwordx4 v[148:151], v8, s[8:11], s68 offen offset:1024 sc1\n\tv_mfma_f32_16x16x32_bf16 v[88:91], v[164:167], v[16:19], v[88:91]\n\ts_waitcnt lgkmcnt(8)\n\tv_mfma_f32_16x16x32_bf16 v[76:79], v[156:159], v[20:23], v[76:79]\n\tds_read_b64_tr_b16 v[40:41], v226 offset:32768\n\tds_read_b64_tr_b16 v[42:43], v226 offset:34816\n\t" \
    "v_mfma_f32_16x16x32_bf16 v[84:87], v[160:163], v[20:23], v[84:87]\n\tbuffer_load_dwordx4 v[152:155], v8, s[8:11], s68 offen offset:2048 sc1\n\ts_add_u32 s68, s46, 49152\n\tv_mfma_f32_16x16x32_bf16 v[92:95], v[164:167], v[20:23], v[92:95]\n\ts_waitcnt lgkmcnt(8)\n\tv_mfma_f32_16x16x32_bf16 v[96:99], v[156:159], v[24:27], v[96:99]\n\tds_read_b64_tr_b16 v[44:45], v227 offset:32768\n\tds_read_b64_tr_b16 v[46:47], v227 offset:34816\n\t" \
    "v_mfma_f32_16x16x32_bf16 v[104:107], v[160:163], v[24:27], v[104:107]\n\tbuffer_load_dwordx4 v[188:191], v8, s[8:11], s68 offen offset:0 sc1\n\tv_mfma_f32_16x16x32_bf16 v[112:115], v[164:167], v[24:27], v[112:115]\n\ts_waitcnt lgkmcnt(8)\n\tv_mfma_f32_16x16x32_bf16 v[100:103], v[156:159], v[28:31], v[100:103]\n\tds_read_b64_tr_b16 v[16:17], v4 offset:36864\n\tds_read_b64_tr_b16 v[18:19], v4 offset:38912\n\tv_mfma_f32_16x16x32_bf16 v[108:111], v[160:163], v[28:31], v[108:111]\n\t" \
    "buffer_load_dwordx4 a[0:3], v8, s[8:11], s68 offen offset:1024 sc1\n\tv_mfma_f32_16x16x32_bf16 v[116:119], v[164:167], v[28:31], v[116:119]\n\ts_waitcnt lgkmcnt(8)\n\tv_mfma_f32_16x16x32_bf16 v[120:123], v[156:159], v[32:35], v[120:123]\n\tds_read_b64_tr_b16 v[20:21], v5 offset:36864\n\tds_read_b64_tr_b16 v[22:23], v5 offset:38912\n\tv_mfma_f32_16x16x32_bf16 v[128:131], v[160:163], v[32:35], v[128:131]\n\tv_mfma_f32_16x16x32_bf16 v[136:139], v[164:167], v[32:35], v[136:139]\n\t" \
    "s_waitcnt lgkmcnt(8)\n\tv_mfma_f32_16x16x32_bf16 v[124:127], v[156:159], v[36:39], v[124:127]\n\tds_read_b64_tr_b16 v[24:25], v6 offset:36864\n\tds_read_b64_tr_b16 v[26:27], v6 offset:38912\n\tv_mfma_f32_16x16x32_bf16 v[132:135], v[160:163], v[36:39], v[132:135]\n\tbuffer_load_dwordx4 a[4:7], v8, s[8:11], s68 offen offset:2048 sc1\n\ts_mov_b32 s69, 4194304\n\ts_add_u32 m0, s54, 69632\n\t" \
    "v_mfma_f32_16x16x32_bf16 v[140:143], v[164:167], v[36:39], v[140:143]\n\ts_waitcnt lgkmcnt(8)\n\tv_mfma_f32_16x16x32_bf16 v[48:51], v[156:159], v[40:43], v[48:51]\n\tds_read_b64_tr_b16 v[28:29], v7 offset:36864\n\tds_read_b64_tr_b16 v[30:31], v7 offset:38912\n\tv_mfma_f32_16x16x32_bf16 v[56:59], v[160:163], v[40:43], v[56:59]\n\tv_mfma_f32_16x16x32_bf16 v[64:67], v[164:167], v[40:43], v[64:67]\n\ts_waitcnt lgkmcnt(8)\n\t" \
    "v_mfma_f32_16x16x32_bf16 v[52:55], v[156:159], v[44:47], v[52:55]\n\tds_read_b64_tr_b16 v[32:33], v224 offset:36864\n\tds_read_b64_tr_b16 v[34:35], v224 offset:38912\n\tv_mfma_f32_16x16x32_bf16 v[60:63], v[160:163], v[44:47], v[60:63]\n\tbuffer_load_dwordx4 v13, s[16:19], s69 offen lds\n\ts_mov_b32 s69, 393216\n\ts_add_u32 m0, s54, 8192\n\tv_mfma_f32_16x16x32_bf16 v[68:71], v[164:167], v[44:47], v[68:71]\n\t" \
    "s_waitcnt vmcnt(9)\n\tv_cmp_ne_u64 vcc, s[36:37], v[250:251]\n\ts_cbranch_vccz 112f\n\ts_mov_b32 s71, 1048576\n\t111:\n\ts_sleep 1\n\tbuffer_load_dwordx2 v[250:251], v241, s[8:11], s50 offen sc1\n\ts_waitcnt vmcnt(0)\n\t" \
    "v_cmp_ne_u64 vcc, s[36:37], v[250:251]\n\ts_cbranch_vccz 112f\n\ts_sub_u32 s71, s71, 1\n\ts_cbranch_scc0 111b\n\t112:\n\ts_waitcnt lgkmcnt(8)\n\tv_mfma_f32_16x16x32_bf16 v[72:75], a[8:11], v[16:19], v[72:75]\n\tds_read_b64_tr_b16 v[36:37], v225 offset:36864\n\t" \
    "ds_read_b64_tr_b16 v[38:39], v225 offset:38912\n\tv_mfma_f32_16x16x32_bf16 v[80:83], a[12:15], v[16:19], v[80:83]\n\tv_mfma_f32_16x16x32_bf16 v[88:91], a[16:19], v[16:19], v[88:91]\n\ts_waitcnt lgkmcnt(8)\n\tv_mfma_f32_16x16x32_bf16 v[76:79], a[8:11], v[20:23], v[76:79]\n\tds_read_b64_tr_b16 v[40:41], v226 offset:36864\n\tds_read_b64_tr_b16 v[42:43], v226 offset:38912\n\tv_mfma_f32_16x16x32_bf16 v[84:87], a[12:15], v[20:23], v[84:87]\n\t" \
    "buffer_load_dwordx4 v12, s[16:19], s69 offen lds\n\ts_mov_b32 s69, 393216\n\ts_add_u32 m0, s54, 73728\n\tv_mfma_f32_16x16x32_bf16 v[92:95], a[16:19], v[20:23], v[92:95]\n\ts_waitcnt lgkmcnt(8)\n\tv_mfma_f32_16x16x32_bf16 v[96:99], a[8:11], v[24:27], v[96:99]\n\tds_read_b64_tr_b16 v[44:45], v227 offset:36864\n\tds_read_b64_tr_b16 v[46:47], v227 offset:38912\n\t" \
    "v_mfma_f32_16x16x32_bf16 v[104:107], a[12:15], v[24:27], v[104:107]\n\tbuffer_load_dwordx4 v13, s[16:19], s69 offen lds\n\ts_mov_b32 s69, 262144\n\ts_add_u32 m0, s54, 12288\n\tv_mfma_f32_16x16x32_bf16 v[112:115], a[16:19], v[24:27], v[112:115]\n\ts_waitcnt lgkmcnt(8)\n\tv_mfma_f32_16x16x32_bf16 v[100:103], a[8:11], v[28:31], v[100:103]\n\tds_read_b64_tr_b16 v[16:17], v4 offset:40960\n\t" \
    "ds_read_b64_tr_b16 v[18:19], v4 offset:43008\n\tv_mfma_f32_16x16x32_bf16 v[108:111], a[12:15], v[28:31], v[108:111]\n\tbuffer_load_dwordx4 v12, s[16:19], s69 offen lds\n\ts_mov_b32 s69, 262144\n\ts_add_u32 m0, s54, 77824\n\tv_mfma_f32_16x16x32_bf16 v[116:119], a[16:19], v[28:31], v[116:119]\n\ts_waitcnt lgkmcnt(8)\n\tv_mfma_f32_16x16x32_bf16 v[120:123], a[8:11], v[32:35], v[120:123]\n\t" \
    "ds_read_b64_tr_b16 v[20:21], v5 offset:40960\n\tds_read_b64_tr_b16 v[22:23], v5 offset:43008\n\tv_mfma_f32_16x16x32_bf16 v[128:131], a[12:15], v[32:35], v[128:131]\n\tbuffer_load_dwordx4 v13, s[16:19], s69 offen lds\n\ts_add_u32 s68, s46, 52224\n\tv_mfma_f32_16x16x32_bf16 v[136:139], a[16:19], v[32:35], v[136:139]\n\ts_waitcnt lgkmcnt(8)\n\tv_mfma_f32_16x16x32_bf16 v[124:127], a[8:11], v[36:39], v[124:127]\n\t" \
    "ds_read_b64_tr_b16 v[24:25], v6 offset:40960\n\tds_read_b64_tr_b16 v[26:27], v6 offset:43008\n\tv_mfma_f32_16x16x32_bf16 v[132:135], a[12:15], v[36:39], v[132:135]\n\tbuffer_load_dwordx4 v[156:159], v8, s[8:11], s68 offen offset:0 sc1\n\tv_mfma_f32_16x16x32_bf16 v[140:143], a[16:19], v[36:39], v[140:143]\n\ts_waitcnt lgkmcnt(8)\n\tv_mfma_f32_16x16x32_bf16 v[48:51], a[8:11], v[40:43], v[48:51]\n\tds_read_b64_tr_b16 v[28:29], v7 offset:40960\n\t" \
    "ds_read_b64_tr_b16 v[30:31], v7 offset:43008\n\tv_mfma_f32_16x16x32_bf16 v[56:59], a[12:15], v[40:43], v[56:59]\n\tbuffer_load_dwordx4 v[160:163], v8, s[8:11], s68 offen offset:1024 sc1\n\tv_mfma_f32_16x16x32_bf16 v[64:67], a[16:19], v[40:43], v[64:67]\n\ts_waitcnt lgkmcnt(8)\n\tv_mfma_f32_16x16x32_bf16 v[52:55], a[8:11], v[44:47], v[52:55]\n\tds_read_b64_tr_b16 v[32:33], v224 offset:40960\n\tds_read_b64_tr_b16 v[34:35], v224 offset:43008\n\t" \
    "v_mfma_f32_16x16x32_bf16 v[60:63], a[12:15], v[44:47], v[60:63]\n\tv_mfma_f32_16x16x32_bf16 v[68:71], a[16:19], v[44:47], v[68:71]\n\ts_barrier\n\ts_waitcnt lgkmcnt(8)\n\tv_mfma_f32_16x16x32_bf16 v[72:75], v[168:171], v[16:19], v[72:75]\n\tds_read_b64_tr_b16 v[36:37], v225 offset:40960\n\tds_read_b64_tr_b16 v[38:39], v225 offset:43008\n\tv_mfma_f32_16x16x32_bf16 v[80:83], v[172:175], v[16:19], v[80:83]\n\t" \
    "buffer_load_dwordx4 v[164:167], v8, s[8:11], s68 offen offset:2048 sc1\n\ts_add_u32 s68, s46, 55296\n\tv_mfma_f32_16x16x32_bf16 v[88:91], v[176:179], v[16:19], v[88:91]\n\ts_waitcnt lgkmcnt(8)\n\tv_mfma_f32_16x16x32_bf16 v[76:79], v[168:171], v[20:23], v[76:79]\n\tds_read_b64_tr_b16 v[40:41], v226 offset:40960\n\tds_read_b64_tr_b16 v[42:43], v226 offset:43008\n\tv_mfma_f32_16x16x32_bf16 v[84:87], v[172:175], v[20:23], v[84:87]\n\t" \
    "buffer_load_dwordx4 a[8:11], v8, s[8:11], s68 offen offset:0 sc1\n\tv_mfma_f32_16x16x32_bf16 v[92:95], v[176:179], v[20:23], v[92:95]\n\ts_waitcnt lgkmcnt(8)\n\tv_mfma_f32_16x16x32_bf16 v[96:99], v[168:171], v[24:27], v[96:99]\n\tds_read_b64_tr_b16 v[44:45], v227 offset:40960\n\tds_read_b64_tr_b16 v[46:47], v227 offset:43008\n\tv_mfma_f32_16x16x32_bf16 v[104:107], v[172:175], v[24:27], v[104:107]\n\tbuffer_load_dwordx4 a[12:15], v8, s[8:11], s68 offen offset:1024 sc1\n\t" \
    "v_mfma_f32_16x16x32_bf16 v[112:115], v[176:179], v[24:27], v[112:115]\n\ts_waitcnt lgkmcnt(8)\n\tv_mfma_f32_16x16x32_bf16 v[100:103], v[168:171], v[28:31], v[100:103]\n\tds_read_b64_tr_b16 v[16:17], v4 offset:45056\n\tds_read_b64_tr_b16 v[18:19], v4 offset:47104\n\tv_mfma_f32_16x16x32_bf16 v[108:111], v[172:175], v[28:31], v[108:111]\n\tbuffer_load_dwordx4 a[16:19], v8, s[8:11], s68 offen offset:2048 sc1\n\ts_mov_b32 s69, 917504\n\t" \
    "s_add_u32 m0, s54, 16384\n\tv_mfma_f32_16x16x32_bf16 v[116:119], v[176:179], v[28:31], v[116:119]\n\ts_waitcnt lgkmcnt(8)\n\tv_mfma_f32_16x16x32_bf16 v[120:123], v[168:171], v[32:35], v[120:123]\n\tds_read_b64_tr_b16 v[20:21], v5 offset:45056\n\tds_read_b64_tr_b16 v[22:23], v5 offset:47104\n\tv_mfma_f32_16x16x32_bf16 v[128:131], v[172:175], v[32:35], v[128:131]\n\tbuffer_load_dwordx4 v12, s[16:19], s69 offen lds\n\t" \
    "s_mov_b32 s69, 917504\n\ts_add_u32 m0, s54, 81920\n\tv_mfma_f32_16x16x32_bf16 v[136:139], v[176:179], v[32:35], v[136:139]\n\ts_waitcnt lgkmcnt(8)\n\tv_mfma_f32_16x16x32_bf16 v[124:127], v[168:171], v[36:39], v[124:127]\n\tds_read_b64_tr_b16 v[24:25], v6 offset:45056\n\tds_read_b64_tr_b16 v[26:27], v6 offset:47104\n\tv_mfma_f32_16x16x32_bf16 v[132:135], v[172:175], v[36:39], v[132:135]\n\t" \
    "v_mfma_f32_16x16x32_bf16 v[140:143], v[176:179], v[36:39], v[140:143]\n\ts_waitcnt lgkmcnt(8)\n\tv_mfma_f32_16x16x32_bf16 v[48:51], v[168:171], v[40:43], v[48:51]\n\tds_read_b64_tr_b16 v[28:29], v7 offset:45056\n\tds_read_b64_tr_b16 v[30:31], v7 offset:47104\n\tv_mfma_f32_16x16x32_bf16 v[56:59], v[172:175], v[40:43], v[56:59]\n\tbuffer_load_dwordx4 v13, s[16:19], s69 offen lds\n\ts_mov_b32 s69, 786432\n\t" \
    "s_add_u32 m0, s54, 20480\n\tv_mfma_f32_16x16x32_bf16 v[64:67], v[176:179], v[40:43], v[64:67]\n\ts_waitcnt lgkmcnt(8)\n\tv_mfma_f32_16x16x32_bf16 v[52:55], v[168:171], v[44:47], v[52:55]\n\tds_read_b64_tr_b16 v[32:33], v224 offset:45056\n\tds_read_b64_tr_b16 v[34:35], v224 offset:47104\n\tv_mfma_f32_16x16x32_bf16 v[60:63], v[172:175], v[44:47], v[60:63]\n\tbuffer_load_dwordx4 v12, s[16:19], s69 offen lds\n\t" \
    "s_mov_b32 s69, 786432\n\ts_add_u32 m0, s54, 86016\n\tv_mfma_f32_16x16x32_bf16 v[68:71], v[176:179], v[44:47], v[68:71]\n\ts_waitcnt lgkmcnt(8)\n\tv_mfma_f32_16x16x32_bf16 v[72:75], v[192:195], v[16:19], v[72:75]\n\tds_read_b64_tr_b16 v[36:37], v225 offset:45056\n\tds_read_b64_tr_b16 v[38:39], v225 offset:47104\n\tv_mfma_f32_16x16x32_bf16 v[80:83], v[196:199], v[16:19], v[80:83]\n\t" \
    "buffer_load_dwordx4 v13, s[16:19], s69 offen lds\n\ts_mov_b32 s69, 1441792\n\ts_add_u32 m0, s54, 24576\n\tv_mfma_f32_16x16x32_bf16 v[88:91], v[200:203], v[16:19], v[88:91]\n\ts_waitcnt lgkmcnt(8)\n\tv_mfma_f32_16x16x32_bf16 v[76:79], v[192:195], v[20:23], v[76:79]\n\tds_read_b64_tr_b16 v[40:41], v226 offset:45056\n\tds_read_b64_tr_b16 v[42:43], v226 offset:47104\n\t" \
    "v_mfma_f32_16x16x32_bf16 v[84:87], v[196:199], v[20:23], v[84:87]\n\tv_mfma_f32_16x16x32_bf16 v[92:95], v[200:203], v[20:23], v[92:95]\n\ts_waitcnt lgkmcnt(8)\n\tv_mfma_f32_16x16x32_bf16 v[96:99], v[192:195], v[24:27], v[96:99]\n\tds_read_b64_tr_b16 v[44:45], v227 offset:45056\n\tds_read_b64_tr_b16 v[46:47], v227 offset:47104\n\tv_mfma_f32_16x16x32_bf16 v[104:107], v[196:199], v[24:27], v[104:107]\n\tbuffer_load_dwordx4 v12, s[16:19], s69 offen lds\n\t" \
    "s_add_u32 s68, s46, 58368\n\tv_mfma_f32_16x16x32_bf16 v[112:115], v[200:203], v[24:27], v[112:115]\n\ts_waitcnt lgkmcnt(8)\n\tv_mfma_f32_16x16x32_bf16 v[100:103], v[192:195], v[28:31], v[100:103]\n\tds_read_b64_tr_b16 v[16:17], v4 offset:49152\n\tds_read_b64_tr_b16 v[18:19], v4 offset:51200\n\tv_mfma_f32_16x16x32_bf16 v[108:111], v[196:199], v[28:31], v[108:111]\n\tbuffer_load_dwordx4 a[32:35], v8, s[8:11], s68 offen offset:0 sc1\n\t" \
    "v_mfma_f32_16x16x32_bf16 v[116:119], v[200:203], v[28:31], v[116:119]\n\ts_waitcnt lgkmcnt(8)\n\tv_mfma_f32_16x16x32_bf16 v[120:123], v[192:195], v[32:35], v[120:123]\n\tds_read_b64_tr_b16 v[20:21], v5 offset:49152\n\tds_read_b64_tr_b16 v[22:23], v5 offset:51200\n\tv_mfma_f32_16x16x32_bf16 v[128:131], v[196:199], v[32:35], v[128:131]\n\tv_mfma_f32_16x16x32_bf16 v[136:139], v[200:203], v[32:35], v[136:139]\n\ts_waitcnt lgkmcnt(8)\n\t" \
    "v_mfma_f32_16x16x32_bf16 v[124:127], v[192:195], v[36:39], v[124:127]\n\tds_read_b64_tr_b16 v[24:25], v6 offset:49152\n\tds_read_b64_tr_b16 v[26:27], v6 offset:51200\n\tv_mfma_f32_16x16x32_bf16 v[132:135], v[196:199], v[36:39], v[132:135]\n\tbuffer_load_dwordx4 a[36:39], v8, s[8:11], s68 offen offset:1024 sc1\n\tv_mfma_f32_16x16x32_bf16 v[140:143], v[200:203], v[36:39], v[140:143]\n\ts_waitcnt lgkmcnt(8)\n\tv_mfma_f32_16x16x32_bf16 v[48:51], v[192:195], v[40:43], v[48:51]\n\t" \
    "ds_read_b64_tr_b16 v[28:29], v7 offset:49152\n\tds_read_b64_tr_b16 v[30:31], v7 offset:51200\n\tv_mfma_f32_16x16x32_bf16 v[56:59], v[196:199], v[40:43], v[56:59]\n\tbuffer_load_dwordx4 a[40:43], v8, s[8:11], s68 offen offset:2048 sc1\n\ts_add_u32 s68, s46, 61440\n\tv_mfma_f32_16x16x32_bf16 v[64:67], v[200:203], v[40:43], v[64:67]\n\ts_waitcnt lgkmcnt(8)\n\tv_mfma_f32_16x16x32_bf16 v[52:55], v[192:195], v[44:47], v[52:55]\n\t" \
    "ds_read_b64_tr_b16 v[32:33], v224 offset:49152\n\tds_read_b64_tr_b16 v[34:35], v224 offset:51200\n\tv_mfma_f32_16x16x32_bf16 v[60:63], v[196:199], v[44:47], v[60:63]\n\tbuffer_load_dwordx4 a[44:47], v8, s[8:11], s68 offen offset:0 sc1\n\tv_mfma_f32_16x16x32_bf16 v[68:71], v[200:203], v[44:47], v[68:71]\n\ts_barrier\n\ts_waitcnt lgkmcnt(8)\n\tv_mfma_f32_16x16x32_bf16 v[72:75], v[216:219], v[16:19], v[72:75]\n\t" \
    "ds_read_b64_tr_b16 v[36:37], v225 offset:49152\n\tds_read_b64_tr_b16 v[38:39], v225 offset:51200\n\tv_mfma_f32_16x16x32_bf16 v[80:83], v[204:207], v[16:19], v[80:83]\n\tbuffer_load_dwordx4 a[48:51], v8, s[8:11], s68 offen offset:1024 sc1\n\tv_mfma_f32_16x16x32_bf16 v[88:91], v[208:211], v[16:19], v[88:91]\n\ts_waitcnt lgkmcnt(8)\n\tv_mfma_f32_16x16x32_bf16 v[76:79], v[216:219], v[20:23], v[76:79]\n\tds_read_b64_tr_b16 v[40:41], v226 offset:49152\n\t" \
    "ds_read_b64_tr_b16 v[42:43], v226 offset:51200\n\tv_mfma_f32_16x16x32_bf16 v[84:87], v[204:207], v[20:23], v[84:87]\n\tbuffer_load_dwordx4 a[52:55], v8, s[8:11], s68 offen offset:2048 sc1\n\ts_add_u32 s68, s46, 64512\n\tv_mfma_f32_16x16x32_bf16 v[92:95], v[208:211], v[20:23], v[92:95]\n\ts_waitcnt lgkmcnt(8)\n\tv_mfma_f32_16x16x32_bf16 v[96:99], v[216:219], v[24:27], v[96:99]\n\tds_read_b64_tr_b16 v[44:45], v227 offset:49152\n\t" \
    "ds_read_b64_tr_b16 v[46:47], v227 offset:51200\n\tv_mfma_f32_16x16x32_bf16 v[104:107], v[204:207], v[24:27], v[104:107]\n\tv_mfma_f32_16x16x32_bf16 v[112:115], v[208:211], v[24:27], v[112:115]\n\ts_waitcnt lgkmcnt(8)\n\tv_mfma_f32_16x16x32_bf16 v[100:103], v[216:219], v[28:31], v[100:103]\n\tds_read_b64_tr_b16 v[16:17], v4 offset:53248\n\tds_read_b64_tr_b16 v[18:19], v4 offset:55296\n\tv_mfma_f32_16x16x32_bf16 v[108:111], v[204:207], v[28:31], v[108:111]\n\t" \
    "buffer_load_dwordx4 a[56:59], v8, s[8:11], s68 offen offset:0 sc1\n\tv_mfma_f32_16x16x32_bf16 v[116:119], v[208:211], v[28:31], v[116:119]\n\ts_waitcnt lgkmcnt(8)\n\tv_mfma_f32_16x16x32_bf16 v[120:123], v[216:219], v[32:35], v[120:123]\n\tds_read_b64_tr_b16 v[20:21], v5 offset:53248\n\tds_read_b64_tr_b16 v[22:23], v5 offset:55296\n\tv_mfma_f32_16x16x32_bf16 v[128:131], v[204:207], v[32:35], v[128:131]\n\tbuffer_load_dwordx4 a[60:63], v8, s[8:11], s68 offen offset:1024 sc1\n\t" \
    "v_mfma_f32_16x16x32_bf16 v[136:139], v[208:211], v[32:35], v[136:139]\n\ts_waitcnt lgkmcnt(8)\n\tv_mfma_f32_16x16x32_bf16 v[124:127], v[216:219], v[36:39], v[124:127]\n\tds_read_b64_tr_b16 v[24:25], v6 offset:53248\n\tds_read_b64_tr_b16 v[26:27], v6 offset:55296\n\tv_mfma_f32_16x16x32_bf16 v[132:135], v[204:207], v[36:39], v[132:135]\n\tbuffer_load_dwordx4 a[64:67], v8, s[8:11], s68 offen offset:2048 sc1\n\ts_mov_b32 s69, 1441792\n\t" \
    "s_add_u32 m0, s54, 90112\n\tv_mfma_f32_16x16x32_bf16 v[140:143], v[208:211], v[36:39], v[140:143]\n\ts_waitcnt lgkmcnt(8)\n\tv_mfma_f32_16x16x32_bf16 v[48:51], v[216:219], v[40:43], v[48:51]\n\tds_read_b64_tr_b16 v[28:29], v7 offset:53248\n\tds_read_b64_tr_b16 v[30:31], v7 offset:55296\n\tv_mfma_f32_16x16x32_bf16 v[56:59], v[204:207], v[40:43], v[56:59]\n\tv_mfma_f32_16x16x32_bf16 v[64:67], v[208:211], v[40:43], v[64:67]\n\t" \
    "s_waitcnt lgkmcnt(8)\n\tv_mfma_f32_16x16x32_bf16 v[52:55], v[216:219], v[44:47], v[52:55]\n\tds_read_b64_tr_b16 v[32:33], v224 offset:53248\n\tds_read_b64_tr_b16 v[34:35], v224 offset:55296\n\tv_mfma_f32_16x16x32_bf16 v[60:63], v[204:207], v[44:47], v[60:63]\n\tbuffer_load_dwordx4 v13, s[16:19], s69 offen lds\n\ts_mov_b32 s69, 1310720\n\ts_add_u32 m0, s54, 28672\n\t" \
    "v_mfma_f32_16x16x32_bf16 v[68:71], v[208:211], v[44:47], v[68:71]\n\ts_waitcnt lgkmcnt(8)\n\tv_mfma_f32_16x16x32_bf16 v[72:75], a[20:23], v[16:19], v[72:75]\n\tds_read_b64_tr_b16 v[36:37], v225 offset:53248\n\tds_read_b64_tr_b16 v[38:39], v225 offset:55296\n\tv_mfma_f32_16x16x32_bf16 v[80:83], a[24:27], v[16:19], v[80:83]\n\tbuffer_load_dwordx4 v12, s[16:19], s69 offen lds\n\ts_mov_b32 s69, 1310720\n\t" \
    "s_add_u32 m0, s54, 94208\n\tv_mfma_f32_16x16x32_bf16 v[88:91], a[28:31], v[16:19], v[88:91]\n\ts_waitcnt lgkmcnt(8)\n\tv_mfma_f32_16x16x32_bf16 v[76:79], a[20:23], v[20:23], v[76:79]\n\tds_read_b64_tr_b16 v[40:41], v226 offset:53248\n\tds_read_b64_tr_b16 v[42:43], v226 offset:55296\n\tv_mfma_f32_16x16x32_bf16 v[84:87], a[24:27], v[20:23], v[84:87]\n\tbuffer_load_dwordx4 v13, s[16:19], s69 offen lds\n\t" \
    "s_mov_b32 s69, 1966080\n\ts_add_u32 m0, s54, 32768\n\tv_mfma_f32_16x16x32_bf16 v[92:95], a[28:31], v[20:23], v[92:95]\n\ts_waitcnt lgkmcnt(8)\n\tv_mfma_f32_16x16x32_bf16 v[96:99], a[20:23], v[24:27], v[96:99]\n\tds_read_b64_tr_b16 v[44:45], v227 offset:53248\n\tds_read_b64_tr_b16 v[46:47], v227 offset:55296\n\tv_mfma_f32_16x16x32_bf16 v[104:107], a[24:27], v[24:27], v[104:107]\n\t" \
    "v_mfma_f32_16x16x32_bf16 v[112:115], a[28:31], v[24:27], v[112:115]\n\ts_waitcnt lgkmcnt(8)\n\tv_mfma_f32_16x16x32_bf16 v[100:103], a[20:23], v[28:31], v[100:103]\n\tds_read_b64_tr_b16 v[16:17], v4 offset:57344\n\tds_read_b64_tr_b16 v[18:19], v4 offset:59392\n\tv_mfma_f32_16x16x32_bf16 v[108:111], a[24:27], v[28:31], v[108:111]\n\tbuffer_load_dwordx4 v12, s[16:19], s69 offen lds\n\ts_mov_b32 s69, 1966080\n\t" \
    "s_add_u32 m0, s54, 98304\n\tv_mfma_f32_16x16x32_bf16 v[116:119], a[28:31], v[28:31], v[116:119]\n\ts_waitcnt lgkmcnt(8)\n\tv_mfma_f32_16x16x32_bf16 v[120:123], a[20:23], v[32:35], v[120:123]\n\tds_read_b64_tr_b16 v[20:21], v5 offset:57344\n\tds_read_b64_tr_b16 v[22:23], v5 offset:59392\n\tv_mfma_f32_16x16x32_bf16 v[128:131], a[24:27], v[32:35], v[128:131]\n\tbuffer_load_dwordx4 v13, s[16:19], s69 offen lds\n\t" \
    "s_mov_b32 s69, 1835008\n\ts_add_u32 m0, s54, 36864\n\tv_mfma_f32_16x16x32_bf16 v[136:139], a[28:31], v[32:35], v[136:139]\n\ts_waitcnt lgkmcnt(8)\n\tv_mfma_f32_16x16x32_bf16 v[124:127], a[20:23], v[36:39], v[124:127]\n\tds_read_b64_tr_b16 v[24:25], v6 offset:57344\n\tds_read_b64_tr_b16 v[26:27], v6 offset:59392\n\tv_mfma_f32_16x16x32_bf16 v[132:135], a[24:27], v[36:39], v[132:135]\n\t" \
    "v_mfma_f32_16x16x32_bf16 v[140:143], a[28:31], v[36:39], v[140:143]\n\ts_waitcnt lgkmcnt(8)\n\tv_mfma_f32_16x16x32_bf16 v[48:51], a[20:23], v[40:43], v[48:51]\n\tds_read_b64_tr_b16 v[28:29], v7 offset:57344\n\tds_read_b64_tr_b16 v[30:31], v7 offset:59392\n\tv_mfma_f32_16x16x32_bf16 v[56:59], a[24:27], v[40:43], v[56:59]\n\tbuffer_load_dwordx4 v12, s[16:19], s69 offen lds\n\ts_add_u32 s68, s46, 67584\n\t" \
    "v_mfma_f32_16x16x32_bf16 v[64:67], a[28:31], v[40:43], v[64:67]\n\ts_waitcnt lgkmcnt(8)\n\tv_mfma_f32_16x16x32_bf16 v[52:55], a[20:23], v[44:47], v[52:55]\n\tds_read_b64_tr_b16 v[32:33], v224 offset:57344\n\tds_read_b64_tr_b16 v[34:35], v224 offset:59392\n\tv_mfma_f32_16x16x32_bf16 v[60:63], a[24:27], v[44:47], v[60:63]\n\tbuffer_load_dwordx4 a[68:71], v8, s[8:11], s68 offen offset:0 sc1\n\tv_mfma_f32_16x16x32_bf16 v[68:71], a[28:31], v[44:47], v[68:71]\n\t" \
    "s_barrier\n\ts_waitcnt vmcnt(38) lgkmcnt(8)\n\tv_mfma_f32_16x16x32_bf16 v[72:75], v[212:215], v[16:19], v[72:75]\n\tds_read_b64_tr_b16 v[36:37], v225 offset:57344\n\tds_read_b64_tr_b16 v[38:39], v225 offset:59392\n\tv_mfma_f32_16x16x32_bf16 v[80:83], v[180:183], v[16:19], v[80:83]\n\tbuffer_load_dwordx4 a[72:75], v8, s[8:11], s68 offen offset:1024 sc1\n\tv_mfma_f32_16x16x32_bf16 v[88:91], v[184:187], v[16:19], v[88:91]\n\t" \
    "s_waitcnt lgkmcnt(8)\n\tv_mfma_f32_16x16x32_bf16 v[76:79], v[212:215], v[20:23], v[76:79]\n\tds_read_b64_tr_b16 v[40:41], v226 offset:57344\n\tds_read_b64_tr_b16 v[42:43], v226 offset:59392\n\tv_mfma_f32_16x16x32_bf16 v[84:87], v[180:183], v[20:23], v[84:87]\n\tbuffer_load_dwordx4 a[76:79], v8, s[8:11], s68 offen offset:2048 sc1\n\ts_add_u32 s68, s46, 70656\n\tv_mfma_f32_16x16x32_bf16 v[92:95], v[184:187], v[20:23], v[92:95]\n\t" \
    "s_waitcnt lgkmcnt(8)\n\tv_mfma_f32_16x16x32_bf16 v[96:99], v[212:215], v[24:27], v[96:99]\n\tds_read_b64_tr_b16 v[44:45], v227 offset:57344\n\tds_read_b64_tr_b16 v[46:47], v227 offset:59392\n\tv_mfma_f32_16x16x32_bf16 v[104:107], v[180:183], v[24:27], v[104:107]\n\tbuffer_load_dwordx4 a[20:23], v8, s[8:11], s68 offen offset:0 sc1\n\tv_mfma_f32_16x16x32_bf16 v[112:115], v[184:187], v[24:27], v[112:115]\n\ts_waitcnt lgkmcnt(8)\n\t" \
    "v_mfma_f32_16x16x32_bf16 v[100:103], v[212:215], v[28:31], v[100:103]\n\tds_read_b64_tr_b16 v[16:17], v4 offset:61440\n\tds_read_b64_tr_b16 v[18:19], v4 offset:63488\n\tv_mfma_f32_16x16x32_bf16 v[108:111], v[180:183], v[28:31], v[108:111]\n\tv_mfma_f32_16x16x32_bf16 v[116:119], v[184:187], v[28:31], v[116:119]\n\ts_waitcnt lgkmcnt(8)\n\tv_mfma_f32_16x16x32_bf16 v[120:123], v[212:215], v[32:35], v[120:123]\n\tds_read_b64_tr_b16 v[20:21], v5 offset:61440\n\t" \
    "ds_read_b64_tr_b16 v[22:23], v5 offset:63488\n\tv_mfma_f32_16x16x32_bf16 v[128:131], v[180:183], v[32:35], v[128:131]\n\tbuffer_load_dwordx4 a[24:27], v8, s[8:11], s68 offen offset:1024 sc1\n\tv_mfma_f32_16x16x32_bf16 v[136:139], v[184:187], v[32:35], v[136:139]\n\ts_waitcnt lgkmcnt(8)\n\tv_mfma_f32_16x16x32_bf16 v[124:127], v[212:215], v[36:39], v[124:127]\n\tds_read_b64_tr_b16 v[24:25], v6 offset:61440\n\tds_read_b64_tr_b16 v[26:27], v6 offset:63488\n\t" \
    "v_mfma_f32_16x16x32_bf16 v[132:135], v[180:183], v[36:39], v[132:135]\n\tbuffer_load_dwordx4 a[28:31], v8, s[8:11], s68 offen offset:2048 sc1\n\ts_add_u32 s68, s46, 73728\n\tv_mfma_f32_16x16x32_bf16 v[140:143], v[184:187], v[36:39], v[140:143]\n\ts_waitcnt lgkmcnt(8)\n\tv_mfma_f32_16x16x32_bf16 v[48:51], v[212:215], v[40:43], v[48:51]\n\tds_read_b64_tr_b16 v[28:29], v7 offset:61440\n\tds_read_b64_tr_b16 v[30:31], v7 offset:63488\n\t" \
    "v_mfma_f32_16x16x32_bf16 v[56:59], v[180:183], v[40:43], v[56:59]\n\tbuffer_load_dwordx4 a[80:83], v8, s[8:11], s68 offen offset:0 sc1\n\tv_mfma_f32_16x16x32_bf16 v[64:67], v[184:187], v[40:43], v[64:67]\n\ts_waitcnt lgkmcnt(8)\n\tv_mfma_f32_16x16x32_bf16 v[52:55], v[212:215], v[44:47], v[52:55]\n\tds_read_b64_tr_b16 v[32:33], v224 offset:61440\n\tds_read_b64_tr_b16 v[34:35], v224 offset:63488\n\tv_mfma_f32_16x16x32_bf16 v[60:63], v[180:183], v[44:47], v[60:63]\n\t" \
    "v_mfma_f32_16x16x32_bf16 v[68:71], v[184:187], v[44:47], v[68:71]\n\ts_waitcnt vmcnt(41) lgkmcnt(8)\n\tv_mfma_f32_16x16x32_bf16 v[72:75], v[144:147], v[16:19], v[72:75]\n\tds_read_b64_tr_b16 v[36:37], v225 offset:61440\n\tds_read_b64_tr_b16 v[38:39], v225 offset:63488\n\tv_mfma_f32_16x16x32_bf16 v[80:83], v[148:151], v[16:19], v[80:83]\n\tbuffer_load_dwordx4 a[84:87], v8, s[8:11], s68 offen offset:1024 sc1\n\tv_mfma_f32_16x16x32_bf16 v[88:91], v[152:155], v[16:19], v[88:91]\n\t" \
    "s_waitcnt lgkmcnt(8)\n\tv_mfma_f32_16x16x32_bf16 v[76:79], v[144:147], v[20:23], v[76:79]\n\tds_read_b64_tr_b16 v[40:41], v4 offset:0\n\tds_read_b64_tr_b16 v[42:43], v4 offset:2048\n\tv_mfma_f32_16x16x32_bf16 v[84:87], v[148:151], v[20:23], v[84:87]\n\tbuffer_load_dwordx4 a[88:91], v8, s[8:11], s68 offen offset:2048 sc1\n\ts_mov_b32 s69, 1835008\n\ts_add_u32 m0, s54, 102400\n\t" \
    "v_mfma_f32_16x16x32_bf16 v[92:95], v[152:155], v[20:23], v[92:95]\n\ts_waitcnt lgkmcnt(8)\n\tv_mfma_f32_16x16x32_bf16 v[96:99], v[144:147], v[24:27], v[96:99]\n\tds_read_b64_tr_b16 v[44:45], v5 offset:0\n\tds_read_b64_tr_b16 v[46:47], v5 offset:2048\n\tv_mfma_f32_16x16x32_bf16 v[104:107], v[148:151], v[24:27], v[104:107]\n\tv_mfma_f32_16x16x32_bf16 v[112:115], v[152:155], v[24:27], v[112:115]\n\ts_waitcnt lgkmcnt(8)\n\t" \
    "v_mfma_f32_16x16x32_bf16 v[100:103], v[144:147], v[28:31], v[100:103]\n\tds_read_b64_tr_b16 v[16:17], v6 offset:0\n\tds_read_b64_tr_b16 v[18:19], v6 offset:2048\n\tv_mfma_f32_16x16x32_bf16 v[108:111], v[148:151], v[28:31], v[108:111]\n\tbuffer_load_dwordx4 v13, s[16:19], s69 offen lds\n\ts_mov_b32 s69, 2490368\n\ts_add_u32 m0, s54, 40960\n\tv_mfma_f32_16x16x32_bf16 v[116:119], v[152:155], v[28:31], v[116:119]\n\t" \
    "s_waitcnt lgkmcnt(8)\n\tv_mfma_f32_16x16x32_bf16 v[120:123], v[144:147], v[32:35], v[120:123]\n\tds_read_b64_tr_b16 v[20:21], v7 offset:0\n\tds_read_b64_tr_b16 v[22:23], v7 offset:2048\n\tv_mfma_f32_16x16x32_bf16 v[128:131], v[148:151], v[32:35], v[128:131]\n\tbuffer_load_dwordx4 v12, s[16:19], s69 offen lds\n\ts_mov_b32 s69, 2490368\n\ts_add_u32 m0, s54, 106496\n\t" \
    "v_mfma_f32_16x16x32_bf16 v[136:139], v[152:155], v[32:35], v[136:139]\n\ts_waitcnt lgkmcnt(8)\n\tv_mfma_f32_16x16x32_bf16 v[124:127], v[144:147], v[36:39], v[124:127]\n\tds_read_b64_tr_b16 v[24:25], v224 offset:0\n\tds_read_b64_tr_b16 v[26:27], v224 offset:2048\n\tv_mfma_f32_16x16x32_bf16 v[132:135], v[148:151], v[36:39], v[132:135]\n\tbuffer_load_dwordx4 v13, s[16:19], s69 offen lds\n\ts_mov_b32 s69, 2359296\n\t" \
    "s_add_u32 m0, s54, 45056\n\tv_mfma_f32_16x16x32_bf16 v[140:143], v[152:155], v[36:39], v[140:143]\n\ts_waitcnt vmcnt(43) lgkmcnt(8)\n\tv_mfma_f32_16x16x32_bf16 v[72:75], v[188:191], v[40:43], v[72:75]\n\tds_read_b64_tr_b16 v[28:29], v225 offset:0\n\tds_read_b64_tr_b16 v[30:31], v225 offset:2048\n\tv_mfma_f32_16x16x32_bf16 v[80:83], a[0:3], v[40:43], v[80:83]\n\tv_mfma_f32_16x16x32_bf16 v[88:91], a[4:7], v[40:43], v[88:91]\n\t" \
    "s_waitcnt lgkmcnt(8)\n\tv_mfma_f32_16x16x32_bf16 v[76:79], v[188:191], v[44:47], v[76:79]\n\tds_read_b64_tr_b16 v[32:33], v4 offset:4096\n\tds_read_b64_tr_b16 v[34:35], v4 offset:6144\n\tv_mfma_f32_16x16x32_bf16 v[84:87], a[0:3], v[44:47], v[84:87]\n\tbuffer_load_dwordx4 v12, s[16:19], s69 offen lds\n\ts_mov_b32 s69, 2359296\n\ts_add_u32 m0, s54, 110592\n\t" \
    "v_mfma_f32_16x16x32_bf16 v[92:95], a[4:7], v[44:47], v[92:95]\n\ts_waitcnt vmcnt(39)\n\ts_barrier\n\ts_waitcnt lgkmcnt(8)\n\tv_mfma_f32_16x16x32_bf16 v[96:99], v[188:191], v[16:19], v[96:99]\n\tds_read_b64_tr_b16 v[36:37], v5 offset:4096\n\tds_read_b64_tr_b16 v[38:39], v5 offset:6144\n\tv_mfma_f32_16x16x32_bf16 v[104:107], a[0:3], v[16:19], v[104:107]\n\t" \
    "buffer_load_dwordx4 v13, s[16:19], s69 offen lds\n\ts_add_u32 s68, s46, 76800\n\tv_mfma_f32_16x16x32_bf16 v[112:115], a[4:7], v[16:19], v[112:115]\n\ts_waitcnt lgkmcnt(8)\n\tv_mfma_f32_16x16x32_bf16 v[100:103], v[188:191], v[20:23], v[100:103]\n\tds_read_b64_tr_b16 v[40:41], v6 offset:4096\n\tds_read_b64_tr_b16 v[42:43], v6 offset:6144\n\tv_mfma_f32_16x16x32_bf16 v[108:111], a[0:3], v[20:23], v[108:111]\n\t" \
    "buffer_load_dwordx4 a[92:95], v8, s[8:11], s68 offen offset:0 sc1\n\tv_mfma_f32_16x16x32_bf16 v[116:119], a[4:7], v[20:23], v[116:119]\n\ts_waitcnt lgkmcnt(8)\n\tv_mfma_f32_16x16x32_bf16 v[120:123], v[188:191], v[24:27], v[120:123]\n\tds_read_b64_tr_b16 v[44:45], v7 offset:4096\n\tds_read_b64_tr_b16 v[46:47], v7 offset:6144\n\tv_mfma_f32_16x16x32_bf16 v[128:131], a[0:3], v[24:27], v[128:131]\n\tbuffer_load_dwordx4 a[96:99], v8, s[8:11], s68 offen offset:1024 sc1\n\t" \
    "v_mfma_f32_16x16x32_bf16 v[136:139], a[4:7], v[24:27], v[136:139]\n\ts_waitcnt lgkmcnt(8)\n\tv_mfma_f32_16x16x32_bf16 v[124:127], v[188:191], v[28:31], v[124:127]\n\tds_read_b64_tr_b16 v[16:17], v224 offset:4096\n\tds_read_b64_tr_b16 v[18:19], v224 offset:6144\n\tv_mfma_f32_16x16x32_bf16 v[132:135], a[0:3], v[28:31], v[132:135]\n\tbuffer_load_dwordx4 a[100:103], v8, s[8:11], s68 offen offset:2048 sc1\n\ts_add_u32 s68, s46, 79872\n\t" \
    "v_mfma_f32_16x16x32_bf16 v[140:143], a[4:7], v[28:31], v[140:143]\n\ts_waitcnt vmcnt(40) lgkmcnt(8)\n\tv_mfma_f32_16x16x32_bf16 v[72:75], v[156:159], v[32:35], v[72:75]\n\tds_read_b64_tr_b16 v[20:21], v225 offset:4096\n\tds_read_b64_tr_b16 v[22:23], v225 offset:6144\n\tv_mfma_f32_16x16x32_bf16 v[80:83], v[160:163], v[32:35], v[80:83]\n\tv_mfma_f32_16x16x32_bf16 v[88:91], v[164:167], v[32:35], v[88:91]\n\ts_waitcnt lgkmcnt(8)\n\t" \
    "v_mfma_f32_16x16x32_bf16 v[76:79], v[156:159], v[36:39], v[76:79]\n\tds_read_b64_tr_b16 v[24:25], v4 offset:8192\n\tds_read_b64_tr_b16 v[26:27], v4 offset:10240\n\tv_mfma_f32_16x16x32_bf16 v[84:87], v[160:163], v[36:39], v[84:87]\n\tbuffer_load_dwordx4 a[104:107], v8, s[8:11], s68 offen offset:0 sc1\n\tv_mfma_f32_16x16x32_bf16 v[92:95], v[164:167], v[36:39], v[92:95]\n\ts_waitcnt lgkmcnt(8)\n\tv_mfma_f32_16x16x32_bf16 v[96:99], v[156:159], v[40:43], v[96:99]\n\t" \
    "ds_read_b64_tr_b16 v[28:29], v5 offset:8192\n\tds_read_b64_tr_b16 v[30:31], v5 offset:10240\n\tv_mfma_f32_16x16x32_bf16 v[104:107], v[160:163], v[40:43], v[104:107]\n\tbuffer_load_dwordx4 a[108:111], v8, s[8:11], s68 offen offset:1024 sc1\n\tv_mfma_f32_16x16x32_bf16 v[112:115], v[164:167], v[40:43], v[112:115]\n\ts_waitcnt lgkmcnt(8)\n\tv_mfma_f32_16x16x32_bf16 v[100:103], v[156:159], v[44:47], v[100:103]\n\tds_read_b64_tr_b16 v[32:33], v6 offset:8192\n\t" \
    "ds_read_b64_tr_b16 v[34:35], v6 offset:10240\n\tv_mfma_f32_16x16x32_bf16 v[108:111], v[160:163], v[44:47], v[108:111]\n\tv_mfma_f32_16x16x32_bf16 v[116:119], v[164:167], v[44:47], v[116:119]\n\ts_waitcnt lgkmcnt(8)\n\tv_mfma_f32_16x16x32_bf16 v[120:123], v[156:159], v[16:19], v[120:123]\n\tds_read_b64_tr_b16 v[36:37], v7 offset:8192\n\tds_read_b64_tr_b16 v[38:39], v7 offset:10240\n\tv_mfma_f32_16x16x32_bf16 v[128:131], v[160:163], v[16:19], v[128:131]\n\t" \
    "buffer_load_dwordx4 a[112:115], v8, s[8:11], s68 offen offset:2048 sc1\n\ts_mov_b32 s69, 3014656\n\ts_add_u32 m0, s54, 49152\n\tv_mfma_f32_16x16x32_bf16 v[136:139], v[164:167], v[16:19], v[136:139]\n\ts_waitcnt lgkmcnt(8)\n\tv_mfma_f32_16x16x32_bf16 v[124:127], v[156:159], v[20:23], v[124:127]\n\tds_read_b64_tr_b16 v[40:41], v224 offset:8192\n\tds_read_b64_tr_b16 v[42:43], v224 offset:10240\n\t" \
    "v_mfma_f32_16x16x32_bf16 v[132:135], v[160:163], v[20:23], v[132:135]\n\tbuffer_load_dwordx4 v12, s[16:19], s69 offen lds\n\ts_mov_b32 s69, 2883584\n\ts_add_u32 m0, s54, 114688\n\tv_mfma_f32_16x16x32_bf16 v[140:143], v[164:167], v[20:23], v[140:143]\n\ts_waitcnt vmcnt(41) lgkmcnt(8)\n\tv_mfma_f32_16x16x32_bf16 v[72:75], a[8:11], v[24:27], v[72:75]\n\tds_read_b64_tr_b16 v[44:45], v225 offset:8192\n\t" \
    "ds_read_b64_tr_b16 v[46:47], v225 offset:10240\n\tv_mfma_f32_16x16x32_bf16 v[80:83], a[12:15], v[24:27], v[80:83]\n\tv_mfma_f32_16x16x32_bf16 v[88:91], a[16:19], v[24:27], v[88:91]\n\ts_waitcnt lgkmcnt(8)\n\tv_mfma_f32_16x16x32_bf16 v[76:79], a[8:11], v[28:31], v[76:79]\n\tds_read_b64_tr_b16 v[16:17], v4 offset:12288\n\tds_read_b64_tr_b16 v[18:19], v4 offset:14336\n\tv_mfma_f32_16x16x32_bf16 v[84:87], a[12:15], v[28:31], v[84:87]\n\t" \
    "buffer_load_dwordx4 v12, s[16:19], s69 offen lds\n\ts_mov_b32 s69, 3538944\n\ts_add_u32 m0, s54, 53248\n\tv_mfma_f32_16x16x32_bf16 v[92:95], a[16:19], v[28:31], v[92:95]\n\ts_waitcnt lgkmcnt(8)\n\tv_mfma_f32_16x16x32_bf16 v[96:99], a[8:11], v[32:35], v[96:99]\n\tds_read_b64_tr_b16 v[20:21], v5 offset:12288\n\tds_read_b64_tr_b16 v[22:23], v5 offset:14336\n\t" \
    "v_mfma_f32_16x16x32_bf16 v[104:107], a[12:15], v[32:35], v[104:107]\n\tbuffer_load_dwordx4 v12, s[16:19], s69 offen lds\n\ts_mov_b32 s69, 3407872\n\ts_add_u32 m0, s54, 118784\n\tv_mfma_f32_16x16x32_bf16 v[112:115], a[16:19], v[32:35], v[112:115]\n\ts_waitcnt lgkmcnt(8)\n\tv_mfma_f32_16x16x32_bf16 v[100:103], a[8:11], v[36:39], v[100:103]\n\tds_read_b64_tr_b16 v[24:25], v6 offset:12288\n\t" \
    "ds_read_b64_tr_b16 v[26:27], v6 offset:14336\n\tv_mfma_f32_16x16x32_bf16 v[108:111], a[12:15], v[36:39], v[108:111]\n\tv_mfma_f32_16x16x32_bf16 v[116:119], a[16:19], v[36:39], v[116:119]\n\ts_waitcnt lgkmcnt(8)\n\tv_mfma_f32_16x16x32_bf16 v[120:123], a[8:11], v[40:43], v[120:123]\n\tds_read_b64_tr_b16 v[28:29], v7 offset:12288\n\tds_read_b64_tr_b16 v[30:31], v7 offset:14336\n\tv_mfma_f32_16x16x32_bf16 v[128:131], a[12:15], v[40:43], v[128:131]\n\t" \
    "buffer_load_dwordx4 v12, s[16:19], s69 offen lds\n\ts_mov_b32 s69, 4063232\n\ts_add_u32 m0, s54, 57344\n\tv_mfma_f32_16x16x32_bf16 v[136:139], a[16:19], v[40:43], v[136:139]\n\ts_waitcnt lgkmcnt(8)\n\tv_mfma_f32_16x16x32_bf16 v[124:127], a[8:11], v[44:47], v[124:127]\n\tds_read_b64_tr_b16 v[32:33], v224 offset:12288\n\tds_read_b64_tr_b16 v[34:35], v224 offset:14336\n\t" \
    "v_mfma_f32_16x16x32_bf16 v[132:135], a[12:15], v[44:47], v[132:135]\n\tbuffer_load_dwordx4 v12, s[16:19], s69 offen lds\n\ts_mov_b32 s69, 3932160\n\ts_add_u32 m0, s54, 122880\n\tv_mfma_f32_16x16x32_bf16 v[140:143], a[16:19], v[44:47], v[140:143]\n\ts_waitcnt vmcnt(40)\n\ts_barrier\n\ts_waitcnt vmcnt(37) lgkmcnt(8)\n\t" \
    "v_mfma_f32_16x16x32_bf16 v[72:75], a[32:35], v[16:19], v[72:75]\n\tds_read_b64_tr_b16 v[36:37], v225 offset:12288\n\tds_read_b64_tr_b16 v[38:39], v225 offset:14336\n\tv_mfma_f32_16x16x32_bf16 v[80:83], a[36:39], v[16:19], v[80:83]\n\tbuffer_load_dwordx4 v12, s[16:19], s69 offen lds\n\ts_mov_b32 s69, 4587520\n\ts_add_u32 m0, s54, 61440\n\tv_mfma_f32_16x16x32_bf16 v[88:91], a[40:43], v[16:19], v[88:91]\n\t" \
    "s_waitcnt lgkmcnt(8)\n\tv_mfma_f32_16x16x32_bf16 v[76:79], a[32:35], v[20:23], v[76:79]\n\tds_read_b64_tr_b16 v[40:41], v4 offset:16384\n\tds_read_b64_tr_b16 v[42:43], v4 offset:18432\n\tv_mfma_f32_16x16x32_bf16 v[84:87], a[36:39], v[20:23], v[84:87]\n\tv_mfma_f32_16x16x32_bf16 v[92:95], a[40:43], v[20:23], v[92:95]\n\ts_waitcnt lgkmcnt(8)\n\tv_mfma_f32_16x16x32_bf16 v[96:99], a[32:35], v[24:27], v[96:99]\n\t" \
    "ds_read_b64_tr_b16 v[44:45], v5 offset:16384\n\tds_read_b64_tr_b16 v[46:47], v5 offset:18432\n\tv_mfma_f32_16x16x32_bf16 v[104:107], a[36:39], v[24:27], v[104:107]\n\tbuffer_load_dwordx4 v12, s[16:19], s69 offen lds\n\ts_mov_b32 s69, 4456448\n\ts_add_u32 m0, s54, 0\n\tv_mfma_f32_16x16x32_bf16 v[112:115], a[40:43], v[24:27], v[112:115]\n\ts_waitcnt lgkmcnt(8)\n\t" \
    "v_mfma_f32_16x16x32_bf16 v[100:103], a[32:35], v[28:31], v[100:103]\n\tds_read_b64_tr_b16 v[16:17], v6 offset:16384\n\tds_read_b64_tr_b16 v[18:19], v6 offset:18432\n\tv_mfma_f32_16x16x32_bf16 v[108:111], a[36:39], v[28:31], v[108:111]\n\tbuffer_load_dwordx4 v12, s[16:19], s69 offen lds\n\ts_add_u32 s68, s46, 82944\n\tv_mfma_f32_16x16x32_bf16 v[116:119], a[40:43], v[28:31], v[116:119]\n\ts_waitcnt lgkmcnt(8)\n\t" \
    "v_mfma_f32_16x16x32_bf16 v[120:123], a[32:35], v[32:35], v[120:123]\n\tds_read_b64_tr_b16 v[20:21], v7 offset:16384\n\tds_read_b64_tr_b16 v[22:23], v7 offset:18432\n\tv_mfma_f32_16x16x32_bf16 v[128:131], a[36:39], v[32:35], v[128:131]\n\tv_mfma_f32_16x16x32_bf16 v[136:139], a[40:43], v[32:35], v[136:139]\n\ts_waitcnt lgkmcnt(8)\n\tv_mfma_f32_16x16x32_bf16 v[124:127], a[32:35], v[36:39], v[124:127]\n\tds_read_b64_tr_b16 v[24:25], v224 offset:16384\n\t" \
    "ds_read_b64_tr_b16 v[26:27], v224 offset:18432\n\tv_mfma_f32_16x16x32_bf16 v[132:135], a[36:39], v[36:39], v[132:135]\n\tbuffer_load_dwordx4 a[116:119], v8, s[8:11], s68 offen offset:0 sc1\n\tv_mfma_f32_16x16x32_bf16 v[140:143], a[40:43], v[36:39], v[140:143]\n\ts_waitcnt vmcnt(38) lgkmcnt(8)\n\tv_mfma_f32_16x16x32_bf16 v[72:75], a[44:47], v[40:43], v[72:75]\n\tds_read_b64_tr_b16 v[28:29], v225 offset:16384\n\tds_read_b64_tr_b16 v[30:31], v225 offset:18432\n\t" \
    "v_mfma_f32_16x16x32_bf16 v[80:83], a[48:51], v[40:43], v[80:83]\n\tbuffer_load_dwordx4 a[120:123], v8, s[8:11], s68 offen offset:1024 sc1\n\tv_mfma_f32_16x16x32_bf16 v[88:91], a[52:55], v[40:43], v[88:91]\n\ts_waitcnt lgkmcnt(8)\n\tv_mfma_f32_16x16x32_bf16 v[76:79], a[44:47], v[44:47], v[76:79]\n\tds_read_b64_tr_b16 v[32:33], v4 offset:20480\n\tds_read_b64_tr_b16 v[34:35], v4 offset:22528\n\tv_mfma_f32_16x16x32_bf16 v[84:87], a[48:51], v[44:47], v[84:87]\n\t" \
    "v_mfma_f32_16x16x32_bf16 v[92:95], a[52:55], v[44:47], v[92:95]\n\ts_waitcnt lgkmcnt(8)\n\tv_mfma_f32_16x16x32_bf16 v[96:99], a[44:47], v[16:19], v[96:99]\n\tds_read_b64_tr_b16 v[36:37], v5 offset:20480\n\tds_read_b64_tr_b16 v[38:39], v5 offset:22528\n\tv_mfma_f32_16x16x32_bf16 v[104:107], a[48:51], v[16:19], v[104:107]\n\tbuffer_load_dwordx4 a[124:127], v8, s[8:11], s68 offen offset:2048 sc1\n\ts_add_u32 s68, s46, 86016\n\t" \
    "v_mfma_f32_16x16x32_bf16 v[112:115], a[52:55], v[16:19], v[112:115]\n\ts_waitcnt lgkmcnt(8)\n\tv_mfma_f32_16x16x32_bf16 v[100:103], a[44:47], v[20:23], v[100:103]\n\tds_read_b64_tr_b16 v[40:41], v6 offset:20480\n\tds_read_b64_tr_b16 v[42:43], v6 offset:22528\n\tv_mfma_f32_16x16x32_bf16 v[108:111], a[48:51], v[20:23], v[108:111]\n\tbuffer_load_dwordx4 a[128:131], v8, s[8:11], s68 offen offset:0 sc1\n\tv_mfma_f32_16x16x32_bf16 v[116:119], a[52:55], v[20:23], v[116:119]\n\t" \
    "s_waitcnt lgkmcnt(8)\n\tv_mfma_f32_16x16x32_bf16 v[120:123], a[44:47], v[24:27], v[120:123]\n\tds_read_b64_tr_b16 v[44:45], v7 offset:20480\n\tds_read_b64_tr_b16 v[46:47], v7 offset:22528\n\tv_mfma_f32_16x16x32_bf16 v[128:131], a[48:51], v[24:27], v[128:131]\n\tv_mfma_f32_16x16x32_bf16 v[136:139], a[52:55], v[24:27], v[136:139]\n\ts_waitcnt lgkmcnt(8)\n\tv_mfma_f32_16x16x32_bf16 v[124:127], a[44:47], v[28:31], v[124:127]\n\t" \
    "ds_read_b64_tr_b16 v[16:17], v224 offset:20480\n\tds_read_b64_tr_b16 v[18:19], v224 offset:22528\n\tv_mfma_f32_16x16x32_bf16 v[132:135], a[48:51], v[28:31], v[132:135]\n\tbuffer_load_dwordx4 a[132:135], v8, s[8:11], s68 offen offset:1024 sc1\n\tv_mfma_f32_16x16x32_bf16 v[140:143], a[52:55], v[28:31], v[140:143]\n\ts_waitcnt vmcnt(39) lgkmcnt(8)\n\tv_mfma_f32_16x16x32_bf16 v[72:75], a[56:59], v[32:35], v[72:75]\n\tds_read_b64_tr_b16 v[20:21], v225 offset:20480\n\t" \
    "ds_read_b64_tr_b16 v[22:23], v225 offset:22528\n\tv_mfma_f32_16x16x32_bf16 v[80:83], a[60:63], v[32:35], v[80:83]\n\tbuffer_load_dwordx4 a[136:139], v8, s[8:11], s68 offen offset:2048 sc1\n\ts_add_u32 s68, s46, 89088\n\tv_mfma_f32_16x16x32_bf16 v[88:91], a[64:67], v[32:35], v[88:91]\n\ts_waitcnt lgkmcnt(8)\n\tv_mfma_f32_16x16x32_bf16 v[76:79], a[56:59], v[36:39], v[76:79]\n\tds_read_b64_tr_b16 v[24:25], v4 offset:24576\n\t" \
    "ds_read_b64_tr_b16 v[26:27], v4 offset:26624\n\tv_mfma_f32_16x16x32_bf16 v[84:87], a[60:63], v[36:39], v[84:87]\n\tv_mfma_f32_16x16x32_bf16 v[92:95], a[64:67], v[36:39], v[92:95]\n\ts_waitcnt lgkmcnt(8)\n\tv_mfma_f32_16x16x32_bf16 v[96:99], a[56:59], v[40:43], v[96:99]\n\tds_read_b64_tr_b16 v[28:29], v5 offset:24576\n\tds_read_b64_tr_b16 v[30:31], v5 offset:26624\n\tv_mfma_f32_16x16x32_bf16 v[104:107], a[60:63], v[40:43], v[104:107]\n\t" \
    "buffer_load_dwordx4 a[140:143], v8, s[8:11], s68 offen offset:0 sc1\n\tv_mfma_f32_16x16x32_bf16 v[112:115], a[64:67], v[40:43], v[112:115]\n\ts_waitcnt lgkmcnt(8)\n\tv_mfma_f32_16x16x32_bf16 v[100:103], a[56:59], v[44:47], v[100:103]\n\tds_read_b64_tr_b16 v[32:33], v6 offset:24576\n\tds_read_b64_tr_b16 v[34:35], v6 offset:26624\n\tv_mfma_f32_16x16x32_bf16 v[108:111], a[60:63], v[44:47], v[108:111]\n\tbuffer_load_dwordx4 a[144:147], v8, s[8:11], s68 offen offset:1024 sc1\n\t" \
    "v_mfma_f32_16x16x32_bf16 v[116:119], a[64:67], v[44:47], v[116:119]\n\ts_waitcnt vmcnt(36)\n\ts_barrier\n\ts_waitcnt lgkmcnt(8)\n\tv_mfma_f32_16x16x32_bf16 v[120:123], a[56:59], v[16:19], v[120:123]\n\tds_read_b64_tr_b16 v[36:37], v7 offset:24576\n\tds_read_b64_tr_b16 v[38:39], v7 offset:26624\n\tv_mfma_f32_16x16x32_bf16 v[128:131], a[60:63], v[16:19], v[128:131]\n\t" \
    "buffer_load_dwordx4 a[148:151], v8, s[8:11], s68 offen offset:2048 sc1\n\ts_add_u32 s68, s46, 92160\n\tv_mfma_f32_16x16x32_bf16 v[136:139], a[64:67], v[16:19], v[136:139]\n\ts_waitcnt lgkmcnt(8)\n\tv_mfma_f32_16x16x32_bf16 v[124:127], a[56:59], v[20:23], v[124:127]\n\tds_read_b64_tr_b16 v[40:41], v224 offset:24576\n\tds_read_b64_tr_b16 v[42:43], v224 offset:26624\n\tv_mfma_f32_16x16x32_bf16 v[132:135], a[60:63], v[20:23], v[132:135]\n\t" \
    "v_mfma_f32_16x16x32_bf16 v[140:143], a[64:67], v[20:23], v[140:143]\n\ts_waitcnt vmcnt(34) lgkmcnt(8)\n\tv_mfma_f32_16x16x32_bf16 v[72:75], a[68:71], v[24:27], v[72:75]\n\tds_read_b64_tr_b16 v[44:45], v225 offset:24576\n\tds_read_b64_tr_b16 v[46:47], v225 offset:26624\n\tv_mfma_f32_16x16x32_bf16 v[80:83], a[72:75], v[24:27], v[80:83]\n\tbuffer_load_dwordx4 a[152:155], v8, s[8:11], s68 offen offset:0 sc1\n\tv_mfma_f32_16x16x32_bf16 v[88:91], a[76:79], v[24:27], v[88:91]\n\t" \
    "s_waitcnt lgkmcnt(8)\n\tv_mfma_f32_16x16x32_bf16 v[76:79], a[68:71], v[28:31], v[76:79]\n\tds_read_b64_tr_b16 v[16:17], v4 offset:28672\n\tds_read_b64_tr_b16 v[18:19], v4 offset:30720\n\tv_mfma_f32_16x16x32_bf16 v[84:87], a[72:75], v[28:31], v[84:87]\n\tbuffer_load_dwordx4 a[156:159], v8, s[8:11], s68 offen offset:1024 sc1\n\tv_mfma_f32_16x16x32_bf16 v[92:95], a[76:79], v[28:31], v[92:95]\n\ts_waitcnt lgkmcnt(8)\n\t" \
    "v_mfma_f32_16x16x32_bf16 v[96:99], a[68:71], v[32:35], v[96:99]\n\tds_read_b64_tr_b16 v[20:21], v5 offset:28672\n\tds_read_b64_tr_b16 v[22:23], v5 offset:30720\n\tv_mfma_f32_16x16x32_bf16 v[104:107], a[72:75], v[32:35], v[104:107]\n\tv_mfma_f32_16x16x32_bf16 v[112:115], a[76:79], v[32:35], v[112:115]\n\ts_waitcnt lgkmcnt(8)\n\tv_mfma_f32_16x16x32_bf16 v[100:103], a[68:71], v[36:39], v[100:103]\n\tds_read_b64_tr_b16 v[24:25], v6 offset:28672\n\t" \
    "ds_read_b64_tr_b16 v[26:27], v6 offset:30720\n\tv_mfma_f32_16x16x32_bf16 v[108:111], a[72:75], v[36:39], v[108:111]\n\tbuffer_load_dwordx4 a[160:163], v8, s[8:11], s68 offen offset:2048 sc1\n\ts_add_u32 s68, s46, 95232\n\tv_mfma_f32_16x16x32_bf16 v[116:119], a[76:79], v[36:39], v[116:119]\n\ts_waitcnt lgkmcnt(8)\n\tv_mfma_f32_16x16x32_bf16 v[120:123], a[68:71], v[40:43], v[120:123]\n\tds_read_b64_tr_b16 v[28:29], v7 offset:28672\n\t" \
    "ds_read_b64_tr_b16 v[30:31], v7 offset:30720\n\tv_mfma_f32_16x16x32_bf16 v[128:131], a[72:75], v[40:43], v[128:131]\n\tbuffer_load_dwordx4 a[164:167], v8, s[8:11], s68 offen offset:0 sc1\n\tv_mfma_f32_16x16x32_bf16 v[136:139], a[76:79], v[40:43], v[136:139]\n\ts_waitcnt lgkmcnt(8)\n\tv_mfma_f32_16x16x32_bf16 v[124:127], a[68:71], v[44:47], v[124:127]\n\tds_read_b64_tr_b16 v[32:33], v224 offset:28672\n\tds_read_b64_tr_b16 v[34:35], v224 offset:30720\n\t" \
    "v_mfma_f32_16x16x32_bf16 v[132:135], a[72:75], v[44:47], v[132:135]\n\tv_mfma_f32_16x16x32_bf16 v[140:143], a[76:79], v[44:47], v[140:143]\n\ts_waitcnt vmcnt(35) lgkmcnt(8)\n\tv_mfma_f32_16x16x32_bf16 v[72:75], a[20:23], v[16:19], v[72:75]\n\tds_read_b64_tr_b16 v[36:37], v225 offset:28672\n\tds_read_b64_tr_b16 v[38:39], v225 offset:30720\n\tv_mfma_f32_16x16x32_bf16 v[80:83], a[24:27], v[16:19], v[80:83]\n\tbuffer_load_dwordx4 a[168:171], v8, s[8:11], s68 offen offset:1024 sc1\n\t" \
    "v_mfma_f32_16x16x32_bf16 v[88:91], a[28:31], v[16:19], v[88:91]\n\ts_waitcnt lgkmcnt(8)\n\tv_mfma_f32_16x16x32_bf16 v[76:79], a[20:23], v[20:23], v[76:79]\n\tds_read_b64_tr_b16 v[40:41], v4 offset:32768\n\tds_read_b64_tr_b16 v[42:43], v4 offset:34816\n\tv_mfma_f32_16x16x32_bf16 v[84:87], a[24:27], v[20:23], v[84:87]\n\tbuffer_load_dwordx4 a[172:175], v8, s[8:11], s68 offen offset:2048 sc1\n\ts_add_u32 s68, s46, 98304\n\t" \
    "v_mfma_f32_16x16x32_bf16 v[92:95], a[28:31], v[20:23], v[92:95]\n\ts_waitcnt lgkmcnt(8)\n\tv_mfma_f32_16x16x32_bf16 v[96:99], a[20:23], v[24:27], v[96:99]\n\tds_read_b64_tr_b16 v[44:45], v5 offset:32768\n\tds_read_b64_tr_b16 v[46:47], v5 offset:34816\n\tv_mfma_f32_16x16x32_bf16 v[104:107], a[24:27], v[24:27], v[104:107]\n\tv_mfma_f32_16x16x32_bf16 v[112:115], a[28:31], v[24:27], v[112:115]\n\ts_waitcnt lgkmcnt(8)\n\t" \
    "v_mfma_f32_16x16x32_bf16 v[100:103], a[20:23], v[28:31], v[100:103]\n\tds_read_b64_tr_b16 v[16:17], v6 offset:32768\n\tds_read_b64_tr_b16 v[18:19], v6 offset:34816\n\tv_mfma_f32_16x16x32_bf16 v[108:111], a[24:27], v[28:31], v[108:111]\n\tbuffer_load_dwordx4 a[176:179], v8, s[8:11], s68 offen offset:0 sc1\n\tv_mfma_f32_16x16x32_bf16 v[116:119], a[28:31], v[28:31], v[116:119]\n\ts_waitcnt lgkmcnt(8)\n\tv_mfma_f32_16x16x32_bf16 v[120:123], a[20:23], v[32:35], v[120:123]\n\t" \
    "ds_read_b64_tr_b16 v[20:21], v7 offset:32768\n\tds_read_b64_tr_b16 v[22:23], v7 offset:34816\n\tv_mfma_f32_16x16x32_bf16 v[128:131], a[24:27], v[32:35], v[128:131]\n\tbuffer_load_dwordx4 a[180:183], v8, s[8:11], s68 offen offset:1024 sc1\n\tv_mfma_f32_16x16x32_bf16 v[136:139], a[28:31], v[32:35], v[136:139]\n\ts_waitcnt lgkmcnt(8)\n\tv_mfma_f32_16x16x32_bf16 v[124:127], a[20:23], v[36:39], v[124:127]\n\tds_read_b64_tr_b16 v[24:25], v224 offset:32768\n\t" \
    "ds_read_b64_tr_b16 v[26:27], v224 offset:34816\n\tv_mfma_f32_16x16x32_bf16 v[132:135], a[24:27], v[36:39], v[132:135]\n\tv_mfma_f32_16x16x32_bf16 v[140:143], a[28:31], v[36:39], v[140:143]\n\ts_waitcnt vmcnt(36) lgkmcnt(8)\n\tv_mfma_f32_16x16x32_bf16 v[72:75], a[80:83], v[40:43], v[72:75]\n\tds_read_b64_tr_b16 v[28:29], v225 offset:32768\n\tds_read_b64_tr_b16 v[30:31], v225 offset:34816\n\tv_mfma_f32_16x16x32_bf16 v[80:83], a[84:87], v[40:43], v[80:83]\n\t" \
    "buffer_load_dwordx4 a[184:187], v8, s[8:11], s68 offen offset:2048 sc1\n\ts_add_u32 s68, s46, 101376\n\tv_mfma_f32_16x16x32_bf16 v[88:91], a[88:91], v[40:43], v[88:91]\n\ts_waitcnt lgkmcnt(8)\n\tv_mfma_f32_16x16x32_bf16 v[76:79], a[80:83], v[44:47], v[76:79]\n\tds_read_b64_tr_b16 v[32:33], v4 offset:36864\n\tds_read_b64_tr_b16 v[34:35], v4 offset:38912\n\tv_mfma_f32_16x16x32_bf16 v[84:87], a[84:87], v[44:47], v[84:87]\n\t" \
    "v_mfma_f32_16x16x32_bf16 v[92:95], a[88:91], v[44:47], v[92:95]\n\ts_waitcnt vmcnt(32)\n\ts_barrier\n\ts_waitcnt lgkmcnt(8)\n\tv_mfma_f32_16x16x32_bf16 v[96:99], a[80:83], v[16:19], v[96:99]\n\tds_read_b64_tr_b16 v[36:37], v5 offset:36864\n\tds_read_b64_tr_b16 v[38:39], v5 offset:38912\n\tv_mfma_f32_16x16x32_bf16 v[104:107], a[84:87], v[16:19], v[104:107]\n\t" \
    "buffer_load_dwordx4 a[188:191], v8, s[8:11], s68 offen offset:0 sc1\n\tv_mfma_f32_16x16x32_bf16 v[112:115], a[88:91], v[16:19], v[112:115]\n\ts_waitcnt lgkmcnt(8)\n\tv_mfma_f32_16x16x32_bf16 v[100:103], a[80:83], v[20:23], v[100:103]\n\tds_read_b64_tr_b16 v[40:41], v6 offset:36864\n\tds_read_b64_tr_b16 v[42:43], v6 offset:38912\n\tv_mfma_f32_16x16x32_bf16 v[108:111], a[84:87], v[20:23], v[108:111]\n\tbuffer_load_dwordx4 a[192:195], v8, s[8:11], s68 offen offset:1024 sc1\n\t" \
    "v_mfma_f32_16x16x32_bf16 v[116:119], a[88:91], v[20:23], v[116:119]\n\ts_waitcnt lgkmcnt(8)\n\tv_mfma_f32_16x16x32_bf16 v[120:123], a[80:83], v[24:27], v[120:123]\n\tds_read_b64_tr_b16 v[44:45], v7 offset:36864\n\tds_read_b64_tr_b16 v[46:47], v7 offset:38912\n\tv_mfma_f32_16x16x32_bf16 v[128:131], a[84:87], v[24:27], v[128:131]\n\tbuffer_load_dwordx4 a[196:199], v8, s[8:11], s68 offen offset:2048 sc1\n\ts_add_u32 s68, s46, 104448\n\t" \
    "v_mfma_f32_16x16x32_bf16 v[136:139], a[88:91], v[24:27], v[136:139]\n\ts_waitcnt lgkmcnt(8)\n\tv_mfma_f32_16x16x32_bf16 v[124:127], a[80:83], v[28:31], v[124:127]\n\tds_read_b64_tr_b16 v[16:17], v224 offset:36864\n\tds_read_b64_tr_b16 v[18:19], v224 offset:38912\n\tv_mfma_f32_16x16x32_bf16 v[132:135], a[84:87], v[28:31], v[132:135]\n\tbuffer_load_dwordx4 a[200:203], v8, s[8:11], s68 offen offset:0 sc1\n\tv_mfma_f32_16x16x32_bf16 v[140:143], a[88:91], v[28:31], v[140:143]\n\t" \
    "s_waitcnt vmcnt(33) lgkmcnt(8)\n\tv_mfma_f32_16x16x32_bf16 v[72:75], a[92:95], v[32:35], v[72:75]\n\tds_read_b64_tr_b16 v[20:21], v225 offset:36864\n\tds_read_b64_tr_b16 v[22:23], v225 offset:38912\n\tv_mfma_f32_16x16x32_bf16 v[80:83], a[96:99], v[32:35], v[80:83]\n\tv_mfma_f32_16x16x32_bf16 v[88:91], a[100:103], v[32:35], v[88:91]\n\ts_waitcnt lgkmcnt(8)\n\tv_mfma_f32_16x16x32_bf16 v[76:79], a[92:95], v[36:39], v[76:79]\n\t" \
    "ds_read_b64_tr_b16 v[24:25], v4 offset:40960\n\tds_read_b64_tr_b16 v[26:27], v4 offset:43008\n\tv_mfma_f32_16x16x32_bf16 v[84:87], a[96:99], v[36:39], v[84:87]\n\tbuffer_load_dwordx4 a[204:207], v8, s[8:11], s68 offen offset:1024 sc1\n\tv_mfma_f32_16x16x32_bf16 v[92:95], a[100:103], v[36:39], v[92:95]\n\ts_waitcnt lgkmcnt(8)\n\tv_mfma_f32_16x16x32_bf16 v[96:99], a[92:95], v[40:43], v[96:99]\n\tds_read_b64_tr_b16 v[28:29], v5 offset:40960\n\t" \
    "ds_read_b64_tr_b16 v[30:31], v5 offset:43008\n\tv_mfma_f32_16x16x32_bf16 v[104:107], a[96:99], v[40:43], v[104:107]\n\tbuffer_load_dwordx4 a[208:211], v8, s[8:11], s68 offen offset:2048 sc1\n\ts_add_u32 s68, s46, 107520\n\tv_mfma_f32_16x16x32_bf16 v[112:115], a[100:103], v[40:43], v[112:115]\n\ts_waitcnt lgkmcnt(8)\n\tv_mfma_f32_16x16x32_bf16 v[100:103], a[92:95], v[44:47], v[100:103]\n\tds_read_b64_tr_b16 v[32:33], v6 offset:40960\n\t" \
    "ds_read_b64_tr_b16 v[34:35], v6 offset:43008\n\tv_mfma_f32_16x16x32_bf16 v[108:111], a[96:99], v[44:47], v[108:111]\n\tv_mfma_f32_16x16x32_bf16 v[116:119], a[100:103], v[44:47], v[116:119]\n\ts_waitcnt lgkmcnt(8)\n\tv_mfma_f32_16x16x32_bf16 v[120:123], a[92:95], v[16:19], v[120:123]\n\tds_read_b64_tr_b16 v[36:37], v7 offset:40960\n\tds_read_b64_tr_b16 v[38:39], v7 offset:43008\n\tv_mfma_f32_16x16x32_bf16 v[128:131], a[96:99], v[16:19], v[128:131]\n\t" \
    "buffer_load_dwordx4 a[212:215], v8, s[8:11], s68 offen offset:0 sc1\n\tv_mfma_f32_16x16x32_bf16 v[136:139], a[100:103], v[16:19], v[136:139]\n\ts_waitcnt lgkmcnt(8)\n\tv_mfma_f32_16x16x32_bf16 v[124:127], a[92:95], v[20:23], v[124:127]\n\tds_read_b64_tr_b16 v[40:41], v224 offset:40960\n\tds_read_b64_tr_b16 v[42:43], v224 offset:43008\n\tv_mfma_f32_16x16x32_bf16 v[132:135], a[96:99], v[20:23], v[132:135]\n\tv_mfma_f32_16x16x32_bf16 v[140:143], a[100:103], v[20:23], v[140:143]\n\t" \
    "s_waitcnt vmcnt(33) lgkmcnt(8)\n\tv_mfma_f32_16x16x32_bf16 v[72:75], a[104:107], v[24:27], v[72:75]\n\tds_read_b64_tr_b16 v[44:45], v225 offset:40960\n\tds_read_b64_tr_b16 v[46:47], v225 offset:43008\n\tv_mfma_f32_16x16x32_bf16 v[80:83], a[108:111], v[24:27], v[80:83]\n\tbuffer_load_dwordx4 a[216:219], v8, s[8:11], s68 offen offset:1024 sc1\n\tv_mfma_f32_16x16x32_bf16 v[88:91], a[112:115], v[24:27], v[88:91]\n\ts_waitcnt lgkmcnt(8)\n\t" \
    "v_mfma_f32_16x16x32_bf16 v[76:79], a[104:107], v[28:31], v[76:79]\n\tds_read_b64_tr_b16 v[16:17], v4 offset:45056\n\tds_read_b64_tr_b16 v[18:19], v4 offset:47104\n\tv_mfma_f32_16x16x32_bf16 v[84:87], a[108:111], v[28:31], v[84:87]\n\tbuffer_load_dwordx4 a[220:223], v8, s[8:11], s68 offen offset:2048 sc1\n\ts_mov_b32 s69, 3014656\n\ts_add_u32 m0, s54, 4096\n\tv_mfma_f32_16x16x32_bf16 v[92:95], a[112:115], v[28:31], v[92:95]\n\t" \
    "s_waitcnt lgkmcnt(8)\n\tv_mfma_f32_16x16x32_bf16 v[96:99], a[104:107], v[32:35], v[96:99]\n\tds_read_b64_tr_b16 v[20:21], v5 offset:45056\n\tds_read_b64_tr_b16 v[22:23], v5 offset:47104\n\tv_mfma_f32_16x16x32_bf16 v[104:107], a[108:111], v[32:35], v[104:107]\n\tv_mfma_f32_16x16x32_bf16 v[112:115], a[112:115], v[32:35], v[112:115]\n\ts_waitcnt lgkmcnt(8)\n\tv_mfma_f32_16x16x32_bf16 v[100:103], a[104:107], v[36:39], v[100:103]\n\t" \
    "ds_read_b64_tr_b16 v[24:25], v6 offset:45056\n\tds_read_b64_tr_b16 v[26:27], v6 offset:47104\n\tv_mfma_f32_16x16x32_bf16 v[108:111], a[108:111], v[36:39], v[108:111]\n\tbuffer_load_dwordx4 v13, s[16:19], s69 offen lds\n\ts_mov_b32 s69, 2883584\n\ts_add_u32 m0, s54, 8192\n\tv_mfma_f32_16x16x32_bf16 v[116:119], a[112:115], v[36:39], v[116:119]\n\ts_waitcnt lgkmcnt(8)\n\t" \
    "v_mfma_f32_16x16x32_bf16 v[120:123], a[104:107], v[40:43], v[120:123]\n\tds_read_b64_tr_b16 v[28:29], v7 offset:45056\n\tds_read_b64_tr_b16 v[30:31], v7 offset:47104\n\tv_mfma_f32_16x16x32_bf16 v[128:131], a[108:111], v[40:43], v[128:131]\n\tbuffer_load_dwordx4 v13, s[16:19], s69 offen lds\n\tv_mfma_f32_16x16x32_bf16 v[136:139], a[112:115], v[40:43], v[136:139]\n\ts_waitcnt lgkmcnt(8)\n\tv_mfma_f32_16x16x32_bf16 v[124:127], a[104:107], v[44:47], v[124:127]\n\t" \
    "ds_read_b64_tr_b16 v[32:33], v224 offset:45056\n\tds_read_b64_tr_b16 v[34:35], v224 offset:47104\n\tv_mfma_f32_16x16x32_bf16 v[132:135], a[108:111], v[44:47], v[132:135]\n\tv_mfma_f32_16x16x32_bf16 v[140:143], a[112:115], v[44:47], v[140:143]\n\ts_waitcnt vmcnt(29)\n\ts_barrier\n\ts_waitcnt vmcnt(26) lgkmcnt(8)\n\tv_mfma_f32_16x16x32_bf16 v[72:75], a[116:119], v[16:19], v[72:75]\n\t" \
    "ds_read_b64_tr_b16 v[36:37], v225 offset:45056\n\tds_read_b64_tr_b16 v[38:39], v225 offset:47104\n\tv_mfma_f32_16x16x32_bf16 v[80:83], a[120:123], v[16:19], v[80:83]\n\tbuffer_load_dwordx4 v[192:195], v243, s[20:23], 0 offen offset:0\n\tv_mfma_f32_16x16x32_bf16 v[88:91], a[124:127], v[16:19], v[88:91]\n\ts_waitcnt lgkmcnt(8)\n\tv_mfma_f32_16x16x32_bf16 v[76:79], a[116:119], v[20:23], v[76:79]\n\tds_read_b64_tr_b16 v[40:41], v4 offset:49152\n\t" \
    "ds_read_b64_tr_b16 v[42:43], v4 offset:51200\n\tv_mfma_f32_16x16x32_bf16 v[84:87], a[120:123], v[20:23], v[84:87]\n\tbuffer_load_dwordx4 v[196:199], v243, s[20:23], 0 offen offset:32\n\tv_mfma_f32_16x16x32_bf16 v[92:95], a[124:127], v[20:23], v[92:95]\n\ts_waitcnt lgkmcnt(8)\n\tv_mfma_f32_16x16x32_bf16 v[96:99], a[116:119], v[24:27], v[96:99]\n\tds_read_b64_tr_b16 v[44:45], v5 offset:49152\n\tds_read_b64_tr_b16 v[46:47], v5 offset:51200\n\t" \
    "v_mfma_f32_16x16x32_bf16 v[104:107], a[120:123], v[24:27], v[104:107]\n\tbuffer_load_dwordx4 v[200:203], v243, s[20:23], 0 offen offset:64\n\tv_mfma_f32_16x16x32_bf16 v[112:115], a[124:127], v[24:27], v[112:115]\n\ts_waitcnt lgkmcnt(8)\n\tv_mfma_f32_16x16x32_bf16 v[100:103], a[116:119], v[28:31], v[100:103]\n\tds_read_b64_tr_b16 v[16:17], v224 offset:49152\n\tds_read_b64_tr_b16 v[18:19], v224 offset:51200\n\tv_mfma_f32_16x16x32_bf16 v[108:111], a[120:123], v[28:31], v[108:111]\n\t" \
    "buffer_load_dwordx2 v[216:217], v242, s[8:11], s59 offen offset:0 sc1\n\tv_mfma_f32_16x16x32_bf16 v[116:119], a[124:127], v[28:31], v[116:119]\n\tv_min_u32 v241, 376, v239\n\tbuffer_load_dwordx2 v[250:251], v241, s[8:11], s51 offen sc1\n\ts_waitcnt lgkmcnt(8)\n\tv_mfma_f32_16x16x32_bf16 v[120:123], a[116:119], v[32:35], v[120:123]\n\tds_read_b64_tr_b16 v[20:21], v225 offset:49152\n\tds_read_b64_tr_b16 v[22:23], v225 offset:51200\n\t" \
    "v_mfma_f32_16x16x32_bf16 v[128:131], a[120:123], v[32:35], v[128:131]\n\tv_mfma_f32_16x16x32_bf16 v[136:139], a[124:127], v[32:35], v[136:139]\n\ts_waitcnt lgkmcnt(8)\n\tv_mfma_f32_16x16x32_bf16 v[124:127], a[116:119], v[36:39], v[124:127]\n\tds_read_b64_tr_b16 v[24:25], v4 offset:53248\n\tds_read_b64_tr_b16 v[26:27], v4 offset:55296\n\tv_mfma_f32_16x16x32_bf16 v[132:135], a[120:123], v[36:39], v[132:135]\n\tv_mfma_f32_16x16x32_bf16 v[140:143], a[124:127], v[36:39], v[140:143]\n\t" \
    "s_waitcnt vmcnt(28) lgkmcnt(8)\n\tv_mfma_f32_16x16x32_bf16 v[72:75], a[128:131], v[40:43], v[72:75]\n\tds_read_b64_tr_b16 v[28:29], v5 offset:53248\n\tds_read_b64_tr_b16 v[30:31], v5 offset:55296\n\tv_mfma_f32_16x16x32_bf16 v[80:83], a[132:135], v[40:43], v[80:83]\n\tv_mfma_f32_16x16x32_bf16 v[88:91], a[136:139], v[40:43], v[88:91]\n\ts_waitcnt lgkmcnt(8)\n\tv_mfma_f32_16x16x32_bf16 v[76:79], a[128:131], v[44:47], v[76:79]\n\t" \
    "ds_read_b64_tr_b16 v[32:33], v224 offset:53248\n\tds_read_b64_tr_b16 v[34:35], v224 offset:55296\n\tv_mfma_f32_16x16x32_bf16 v[84:87], a[132:135], v[44:47], v[84:87]\n\tbuffer_load_dwordx2 v[218:219], v242, s[8:11], s59 offen offset:128 sc1\n\ts_mov_b32 s69, 3538944\n\ts_add_u32 m0, s54, 12288\n\tv_mfma_f32_16x16x32_bf16 v[92:95], a[136:139], v[44:47], v[92:95]\n\ts_waitcnt vmcnt(26) lgkmcnt(8)\n\t" \
    "v_mfma_f32_16x16x32_bf16 v[72:75], a[140:143], v[16:19], v[72:75]\n\tds_read_b64_tr_b16 v[36:37], v225 offset:53248\n\tds_read_b64_tr_b16 v[38:39], v225 offset:55296\n\tv_mfma_f32_16x16x32_bf16 v[80:83], a[144:147], v[16:19], v[80:83]\n\tbuffer_load_dwordx4 v13, s[16:19], s69 offen lds\n\ts_mov_b32 s69, 3407872\n\ts_add_u32 m0, s54, 16384\n\tv_mfma_f32_16x16x32_bf16 v[88:91], a[148:151], v[16:19], v[88:91]\n\t" \
    "s_waitcnt lgkmcnt(8)\n\tv_mfma_f32_16x16x32_bf16 v[76:79], a[140:143], v[20:23], v[76:79]\n\tds_read_b64_tr_b16 v[40:41], v4 offset:57344\n\tds_read_b64_tr_b16 v[42:43], v4 offset:59392\n\tv_mfma_f32_16x16x32_bf16 v[84:87], a[144:147], v[20:23], v[84:87]\n\tv_mfma_f32_16x16x32_bf16 v[92:95], a[148:151], v[20:23], v[92:95]\n\ts_waitcnt vmcnt(24) lgkmcnt(8)\n\tv_mfma_f32_16x16x32_bf16 v[72:75], a[152:155], v[24:27], v[72:75]\n\t" \
    "ds_read_b64_tr_b16 v[44:45], v5 offset:57344\n\tds_read_b64_tr_b16 v[46:47], v5 offset:59392\n\tv_mfma_f32_16x16x32_bf16 v[80:83], a[156:159], v[24:27], v[80:83]\n\tbuffer_load_dwordx4 v13, s[16:19], s69 offen lds\n\ts_mov_b32 s69, 4063232\n\ts_add_u32 m0, s54, 20480\n\tv_mfma_f32_16x16x32_bf16 v[88:91], a[160:163], v[24:27], v[88:91]\n\ts_waitcnt lgkmcnt(8)\n\t" \
    "v_mfma_f32_16x16x32_bf16 v[76:79], a[152:155], v[28:31], v[76:79]\n\tds_read_b64_tr_b16 v[16:17], v224 offset:57344\n\tds_read_b64_tr_b16 v[18:19], v224 offset:59392\n\tv_mfma_f32_16x16x32_bf16 v[84:87], a[156:159], v[28:31], v[84:87]\n\tbuffer_load_dwordx4 v13, s[16:19], s69 offen lds\n\ts_mov_b32 s69, 3932160\n\ts_add_u32 m0, s54, 24576\n\tv_mfma_f32_16x16x32_bf16 v[92:95], a[160:163], v[28:31], v[92:95]\n\t" \
    "s_waitcnt vmcnt(23) lgkmcnt(8)\n\tv_mfma_f32_16x16x32_bf16 v[72:75], a[164:167], v[32:35], v[72:75]\n\tds_read_b64_tr_b16 v[20:21], v225 offset:57344\n\tds_read_b64_tr_b16 v[22:23], v225 offset:59392\n\tv_mfma_f32_16x16x32_bf16 v[80:83], a[168:171], v[32:35], v[80:83]\n\tv_mfma_f32_16x16x32_bf16 v[88:91], a[172:175], v[32:35], v[88:91]\n\ts_waitcnt lgkmcnt(8)\n\tv_mfma_f32_16x16x32_bf16 v[76:79], a[164:167], v[36:39], v[76:79]\n\t" \
    "ds_read_b64_tr_b16 v[24:25], v4 offset:61440\n\tds_read_b64_tr_b16 v[26:27], v4 offset:63488\n\tv_mfma_f32_16x16x32_bf16 v[84:87], a[168:171], v[36:39], v[84:87]\n\tbuffer_load_dwordx4 v13, s[16:19], s69 offen lds\n\ts_mov_b32 s69, 4587520\n\ts_add_u32 m0, s54, 28672\n\tv_mfma_f32_16x16x32_bf16 v[92:95], a[172:175], v[36:39], v[92:95]\n\ts_waitcnt vmcnt(21) lgkmcnt(8)\n\t" \
    "v_mfma_f32_16x16x32_bf16 v[72:75], a[176:179], v[40:43], v[72:75]\n\tds_read_b64_tr_b16 v[28:29], v5 offset:61440\n\tds_read_b64_tr_b16 v[30:31], v5 offset:63488\n\tv_mfma_f32_16x16x32_bf16 v[80:83], a[180:183], v[40:43], v[80:83]\n\tbuffer_load_dwordx4 v13, s[16:19], s69 offen lds\n\ts_mov_b32 s69, 4456448\n\ts_add_u32 m0, s54, 32768\n\tv_mfma_f32_16x16x32_bf16 v[88:91], a[184:187], v[40:43], v[88:91]\n\t" \
    "s_waitcnt lgkmcnt(8)\n\tv_mfma_f32_16x16x32_bf16 v[76:79], a[176:179], v[44:47], v[76:79]\n\tds_read_b64_tr_b16 v[32:33], v4 offset:0\n\tds_read_b64_tr_b16 v[34:35], v4 offset:2048\n\tv_mfma_f32_16x16x32_bf16 v[84:87], a[180:183], v[44:47], v[84:87]\n\tv_mfma_f32_16x16x32_bf16 v[92:95], a[184:187], v[44:47], v[92:95]\n\ts_waitcnt vmcnt(19) lgkmcnt(8)\n\tv_mfma_f32_16x16x32_bf16 v[72:75], a[188:191], v[16:19], v[72:75]\n\t" \
    "ds_read_b64_tr_b16 v[36:37], v5 offset:0\n\tds_read_b64_tr_b16 v[38:39], v5 offset:2048\n\tv_mfma_f32_16x16x32_bf16 v[80:83], a[192:195], v[16:19], v[80:83]\n\tbuffer_load_dwordx4 v13, s[16:19], s69 offen lds\n\tv_mfma_f32_16x16x32_bf16 v[88:91], a[196:199], v[16:19], v[88:91]\n\ts_waitcnt lgkmcnt(8)\n\tv_mfma_f32_16x16x32_bf16 v[76:79], a[188:191], v[20:23], v[76:79]\n\tds_read_b64_tr_b16 v[40:41], v6 offset:49152\n\t" \
    "ds_read_b64_tr_b16 v[42:43], v6 offset:51200\n\tv_mfma_f32_16x16x32_bf16 v[84:87], a[192:195], v[20:23], v[84:87]\n\tv_mfma_f32_16x16x32_bf16 v[92:95], a[196:199], v[20:23], v[92:95]\n\ts_waitcnt vmcnt(7)\n\tv_cmp_ne_u64 vcc, s[36:37], v[250:251]\n\ts_cmp_lg_u64 vcc, 0\n\ts_cselect_b32 s70, 1, 0\n\ts_waitcnt lgkmcnt(8)\n\t" \
    "v_mfma_f32_16x16x32_bf16 v[72:75], a[200:203], v[24:27], v[72:75]\n\tds_read_b64_tr_b16 v[44:45], v7 offset:49152\n\tds_read_b64_tr_b16 v[46:47], v7 offset:51200\n\tv_mfma_f32_16x16x32_bf16 v[80:83], a[204:207], v[24:27], v[80:83]\n\tbuffer_load_dwordx4 v[204:207], v8, s[8:11], s56 offen offset:0 sc1\n\tv_mfma_f32_16x16x32_bf16 v[88:91], a[208:211], v[24:27], v[88:91]\n\ts_waitcnt lgkmcnt(8)\n\tv_mfma_f32_16x16x32_bf16 v[76:79], a[200:203], v[28:31], v[76:79]\n\t" \
    "ds_read_b64_tr_b16 v[16:17], v226 offset:49152\n\tds_read_b64_tr_b16 v[18:19], v226 offset:51200\n\tv_mfma_f32_16x16x32_bf16 v[84:87], a[204:207], v[28:31], v[84:87]\n\tbuffer_load_dwordx4 v[208:211], v8, s[8:11], s56 offen offset:1024 sc1\n\tv_mfma_f32_16x16x32_bf16 v[92:95], a[208:211], v[28:31], v[92:95]\n\ts_waitcnt lgkmcnt(8)\n\tv_mfma_f32_16x16x32_bf16 v[72:75], a[212:215], v[32:35], v[72:75]\n\tds_read_b64_tr_b16 v[20:21], v227 offset:49152\n\t" \
    "ds_read_b64_tr_b16 v[22:23], v227 offset:51200\n\tv_mfma_f32_16x16x32_bf16 v[80:83], a[216:219], v[32:35], v[80:83]\n\tbuffer_load_dwordx4 v[212:215], v8, s[8:11], s56 offen offset:2048 sc1\n\tv_mfma_f32_16x16x32_bf16 v[88:91], a[220:223], v[32:35], v[88:91]\n\ts_waitcnt lgkmcnt(8)\n\tv_mfma_f32_16x16x32_bf16 v[76:79], a[212:215], v[36:39], v[76:79]\n\tds_read_b64_tr_b16 v[24:25], v6 offset:53248\n\tds_read_b64_tr_b16 v[26:27], v6 offset:55296\n\t" \
    "v_mfma_f32_16x16x32_bf16 v[84:87], a[216:219], v[36:39], v[84:87]\n\tv_mfma_f32_16x16x32_bf16 v[92:95], a[220:223], v[36:39], v[92:95]\n\ts_waitcnt lgkmcnt(8)\n\tv_mfma_f32_16x16x32_bf16 v[96:99], a[128:131], v[40:43], v[96:99]\n\tds_read_b64_tr_b16 v[28:29], v7 offset:53248\n\tds_read_b64_tr_b16 v[30:31], v7 offset:55296\n\ts_xor_b32 s68, s53, 12\n\tv_mfma_f32_16x16x32_bf16 v[104:107], a[132:135], v[40:43], v[104:107]\n\t" \
    "s_mul_i32 s68, s68, 12288\n\ts_add_u32 s68, s68, 7208960\n\tv_cvt_pk_bf16_f32 v72, v72, v73\n\tv_mfma_f32_16x16x32_bf16 v[112:115], a[136:139], v[40:43], v[112:115]\n\tv_cvt_pk_bf16_f32 v73, v74, v75\n\tv_cvt_pk_bf16_f32 v74, v76, v77\n\ts_waitcnt lgkmcnt(8)\n\tv_mfma_f32_16x16x32_bf16 v[100:103], a[128:131], v[44:47], v[100:103]\n\t" \
    "ds_read_b64_tr_b16 v[32:33], v226 offset:53248\n\tds_read_b64_tr_b16 v[34:35], v226 offset:55296\n\tv_mov_b32 v75, s36\n\tv_mfma_f32_16x16x32_bf16 v[108:111], a[132:135], v[44:47], v[108:111]\n\tbuffer_store_dwordx4 v[72:75], v8, s[8:11], s68 offen offset:0 sc1\n\tv_cvt_pk_bf16_f32 v76, v78, v79\n\tv_cvt_pk_bf16_f32 v77, v80, v81\n\tv_mfma_f32_16x16x32_bf16 v[116:119], a[136:139], v[44:47], v[116:119]\n\t" \
    "v_cvt_pk_bf16_f32 v78, v82, v83\n\tv_mov_b32 v79, s36\n\ts_waitcnt lgkmcnt(8)\n\tv_mfma_f32_16x16x32_bf16 v[96:99], a[140:143], v[16:19], v[96:99]\n\tds_read_b64_tr_b16 v[36:37], v227 offset:53248\n\tds_read_b64_tr_b16 v[38:39], v227 offset:55296\n\tv_mfma_f32_16x16x32_bf16 v[104:107], a[144:147], v[16:19], v[104:107]\n\tbuffer_store_dwordx4 v[76:79], v8, s[8:11], s68 offen offset:1024 sc1\n\t" \
    "v_cvt_pk_bf16_f32 v80, v84, v85\n\tv_cvt_pk_bf16_f32 v81, v86, v87\n\tv_mfma_f32_16x16x32_bf16 v[112:115], a[148:151], v[16:19], v[112:115]\n\tv_cvt_pk_bf16_f32 v82, v88, v89\n\tv_mov_b32 v83, s36\n\ts_waitcnt lgkmcnt(8)\n\tv_mfma_f32_16x16x32_bf16 v[100:103], a[140:143], v[20:23], v[100:103]\n\tds_read_b64_tr_b16 v[40:41], v6 offset:57344\n\t" \
    "ds_read_b64_tr_b16 v[42:43], v6 offset:59392\n\tv_mfma_f32_16x16x32_bf16 v[108:111], a[144:147], v[20:23], v[108:111]\n\tbuffer_store_dwordx4 v[80:83], v8, s[8:11], s68 offen offset:2048 sc1\n\tv_cvt_pk_bf16_f32 v84, v90, v91\n\tv_cvt_pk_bf16_f32 v85, v92, v93\n\tv_mfma_f32_16x16x32_bf16 v[116:119], a[148:151], v[20:23], v[116:119]\n\tv_cvt_pk_bf16_f32 v86, v94, v95\n\tv_mov_b32 v87, s36\n\t" \
    "s_waitcnt lgkmcnt(8)\n\tv_mfma_f32_16x16x32_bf16 v[96:99], a[152:155], v[24:27], v[96:99]\n\tds_read_b64_tr_b16 v[44:45], v7 offset:57344\n\tds_read_b64_tr_b16 v[46:47], v7 offset:59392\n\tv_mfma_f32_16x16x32_bf16 v[104:107], a[156:159], v[24:27], v[104:107]\n\tbuffer_store_dwordx4 v[84:87], v8, s[8:11], s68 offen offset:3072 sc1\n\tv_mfma_f32_16x16x32_bf16 v[112:115], a[160:163], v[24:27], v[112:115]\n\ts_waitcnt lgkmcnt(8)\n\t" \
    "v_mfma_f32_16x16x32_bf16 v[100:103], a[152:155], v[28:31], v[100:103]\n\tds_read_b64_tr_b16 v[16:17], v226 offset:57344\n\tds_read_b64_tr_b16 v[18:19], v226 offset:59392\n\tv_mfma_f32_16x16x32_bf16 v[108:111], a[156:159], v[28:31], v[108:111]\n\tv_mfma_f32_16x16x32_bf16 v[116:119], a[160:163], v[28:31], v[116:119]\n\ts_waitcnt lgkmcnt(8)\n\tv_mfma_f32_16x16x32_bf16 v[96:99], a[164:167], v[32:35], v[96:99]\n\tds_read_b64_tr_b16 v[20:21], v227 offset:57344\n\t" \
    "ds_read_b64_tr_b16 v[22:23], v227 offset:59392\n\tv_mfma_f32_16x16x32_bf16 v[104:107], a[168:171], v[32:35], v[104:107]\n\tv_mfma_f32_16x16x32_bf16 v[112:115], a[172:175], v[32:35], v[112:115]\n\ts_waitcnt lgkmcnt(8)\n\tv_mfma_f32_16x16x32_bf16 v[100:103], a[164:167], v[36:39], v[100:103]\n\tds_read_b64_tr_b16 v[24:25], v6 offset:61440\n\tds_read_b64_tr_b16 v[26:27], v6 offset:63488\n\tv_mfma_f32_16x16x32_bf16 v[108:111], a[168:171], v[36:39], v[108:111]\n\t" \
    "v_mfma_f32_16x16x32_bf16 v[116:119], a[172:175], v[36:39], v[116:119]\n\ts_waitcnt lgkmcnt(8)\n\tv_mfma_f32_16x16x32_bf16 v[96:99], a[176:179], v[40:43], v[96:99]\n\tds_read_b64_tr_b16 v[28:29], v7 offset:61440\n\tds_read_b64_tr_b16 v[30:31], v7 offset:63488\n\tv_mfma_f32_16x16x32_bf16 v[104:107], a[180:183], v[40:43], v[104:107]\n\tv_mfma_f32_16x16x32_bf16 v[112:115], a[184:187], v[40:43], v[112:115]\n\ts_waitcnt lgkmcnt(8)\n\t" \
    "v_mfma_f32_16x16x32_bf16 v[100:103], a[176:179], v[44:47], v[100:103]\n\tds_read_b64_tr_b16 v[32:33], v6 offset:0\n\tds_read_b64_tr_b16 v[34:35], v6 offset:2048\n\tv_mfma_f32_16x16x32_bf16 v[108:111], a[180:183], v[44:47], v[108:111]\n\tv_mfma_f32_16x16x32_bf16 v[116:119], a[184:187], v[44:47], v[116:119]\n\ts_waitcnt vmcnt(7)\n\ts_barrier\n\ts_waitcnt lgkmcnt(8)\n\t" \
    "v_mfma_f32_16x16x32_bf16 v[96:99], a[188:191], v[16:19], v[96:99]\n\tds_read_b64_tr_b16 v[36:37], v7 offset:0\n\tds_read_b64_tr_b16 v[38:39], v7 offset:2048\n\tv_mfma_f32_16x16x32_bf16 v[104:107], a[192:195], v[16:19], v[104:107]\n\tv_mfma_f32_16x16x32_bf16 v[112:115], a[196:199], v[16:19], v[112:115]\n\ts_waitcnt lgkmcnt(8)\n\tv_mfma_f32_16x16x32_bf16 v[100:103], a[188:191], v[20:23], v[100:103]\n\tds_read_b64_tr_b16 v[40:41], v4 offset:4096\n\t" \
    "ds_read_b64_tr_b16 v[42:43], v4 offset:6144\n\tv_mfma_f32_16x16x32_bf16 v[108:111], a[192:195], v[20:23], v[108:111]\n\tv_mfma_f32_16x16x32_bf16 v[116:119], a[196:199], v[20:23], v[116:119]\n\ts_waitcnt lgkmcnt(8)\n\tv_mfma_f32_16x16x32_bf16 v[96:99], a[200:203], v[24:27], v[96:99]\n\tds_read_b64_tr_b16 v[44:45], v5 offset:4096\n\tds_read_b64_tr_b16 v[46:47], v5 offset:6144\n\tv_mfma_f32_16x16x32_bf16 v[104:107], a[204:207], v[24:27], v[104:107]\n\t" \
    "v_mfma_f32_16x16x32_bf16 v[112:115], a[208:211], v[24:27], v[112:115]\n\ts_waitcnt lgkmcnt(8)\n\tv_mfma_f32_16x16x32_bf16 v[100:103], a[200:203], v[28:31], v[100:103]\n\tds_read_b64_tr_b16 v[16:17], v4 offset:8192\n\tds_read_b64_tr_b16 v[18:19], v4 offset:10240\n\tv_mfma_f32_16x16x32_bf16 v[108:111], a[204:207], v[28:31], v[108:111]\n\tv_mfma_f32_16x16x32_bf16 v[116:119], a[208:211], v[28:31], v[116:119]\n\ts_waitcnt lgkmcnt(8)\n\t" \
    "v_mfma_f32_16x16x32_bf16 v[96:99], a[212:215], v[32:35], v[96:99]\n\tds_read_b64_tr_b16 v[20:21], v5 offset:8192\n\tds_read_b64_tr_b16 v[22:23], v5 offset:10240\n\tv_mfma_f32_16x16x32_bf16 v[104:107], a[216:219], v[32:35], v[104:107]\n\tv_mfma_f32_16x16x32_bf16 v[112:115], a[220:223], v[32:35], v[112:115]\n\ts_waitcnt lgkmcnt(8)\n\tv_mfma_f32_16x16x32_bf16 v[100:103], a[212:215], v[36:39], v[100:103]\n\tds_read_b64_tr_b16 v[24:25], v4 offset:12288\n\t" \
    "ds_read_b64_tr_b16 v[26:27], v4 offset:14336\n\tv_mfma_f32_16x16x32_bf16 v[108:111], a[216:219], v[36:39], v[108:111]\n\tv_mfma_f32_16x16x32_bf16 v[116:119], a[220:223], v[36:39], v[116:119]\n\ts_waitcnt lgkmcnt(8)\n\tv_mfma_f32_16x16x32_bf16 v[120:123], a[128:131], v[40:43], v[120:123]\n\tds_read_b64_tr_b16 v[28:29], v5 offset:12288\n\tds_read_b64_tr_b16 v[30:31], v5 offset:14336\n\ts_xor_b32 s68, s53, 8\n\t" \
    "v_mfma_f32_16x16x32_bf16 v[128:131], a[132:135], v[40:43], v[128:131]\n\ts_mul_i32 s68, s68, 12288\n\ts_add_u32 s68, s68, 7213056\n\tv_cvt_pk_bf16_f32 v96, v96, v97\n\tv_mfma_f32_16x16x32_bf16 v[136:139], a[136:139], v[40:43], v[136:139]\n\tv_cvt_pk_bf16_f32 v97, v98, v99\n\tv_cvt_pk_bf16_f32 v98, v100, v101\n\ts_waitcnt lgkmcnt(8)\n\t" \
    "v_mfma_f32_16x16x32_bf16 v[124:127], a[128:131], v[44:47], v[124:127]\n\tds_read_b64_tr_b16 v[32:33], v4 offset:16384\n\tds_read_b64_tr_b16 v[34:35], v4 offset:18432\n\tv_mov_b32 v99, s36\n\tv_mfma_f32_16x16x32_bf16 v[132:135], a[132:135], v[44:47], v[132:135]\n\tbuffer_store_dwordx4 v[96:99], v8, s[8:11], s68 offen offset:0 sc1\n\tv_cvt_pk_bf16_f32 v100, v102, v103\n\tv_cvt_pk_bf16_f32 v101, v104, v105\n\t" \
    "v_mfma_f32_16x16x32_bf16 v[140:143], a[136:139], v[44:47], v[140:143]\n\tv_cvt_pk_bf16_f32 v102, v106, v107\n\tv_mov_b32 v103, s36\n\ts_waitcnt lgkmcnt(8)\n\tv_mfma_f32_16x16x32_bf16 v[120:123], a[140:143], v[16:19], v[120:123]\n\tds_read_b64_tr_b16 v[36:37], v5 offset:16384\n\tds_read_b64_tr_b16 v[38:39], v5 offset:18432\n\tv_mfma_f32_16x16x32_bf16 v[128:131], a[144:147], v[16:19], v[128:131]\n\t" \
    "buffer_store_dwordx4 v[100:103], v8, s[8:11], s68 offen offset:1024 sc1\n\tv_cvt_pk_bf16_f32 v104, v108, v109\n\tv_cvt_pk_bf16_f32 v105, v110, v111\n\tv_mfma_f32_16x16x32_bf16 v[136:139], a[148:151], v[16:19], v[136:139]\n\tv_cvt_pk_bf16_f32 v106, v112, v113\n\tv_mov_b32 v107, s36\n\ts_waitcnt lgkmcnt(8)\n\tv_mfma_f32_16x16x32_bf16 v[124:127], a[140:143], v[20:23], v[124:127]\n\t" \
    "ds_read_b64_tr_b16 v[40:41], v4 offset:20480\n\tds_read_b64_tr_b16 v[42:43], v4 offset:22528\n\tv_mfma_f32_16x16x32_bf16 v[132:135], a[144:147], v[20:23], v[132:135]\n\tbuffer_store_dwordx4 v[104:107], v8, s[8:11], s68 offen offset:2048 sc1\n\tv_cvt_pk_bf16_f32 v108, v114, v115\n\tv_cvt_pk_bf16_f32 v109, v116, v117\n\tv_mfma_f32_16x16x32_bf16 v[140:143], a[148:151], v[20:23], v[140:143]\n\tv_cvt_pk_bf16_f32 v110, v118, v119\n\t" \
    "v_mov_b32 v111, s36\n\ts_waitcnt lgkmcnt(8)\n\tv_mfma_f32_16x16x32_bf16 v[120:123], a[152:155], v[24:27], v[120:123]\n\tds_read_b64_tr_b16 v[44:45], v5 offset:20480\n\tds_read_b64_tr_b16 v[46:47], v5 offset:22528\n\tv_mfma_f32_16x16x32_bf16 v[128:131], a[156:159], v[24:27], v[128:131]\n\tbuffer_store_dwordx4 v[108:111], v8, s[8:11], s68 offen offset:3072 sc1\n\tv_mfma_f32_16x16x32_bf16 v[136:139], a[160:163], v[24:27], v[136:139]\n\t" \
    "s_waitcnt lgkmcnt(8)\n\tv_mfma_f32_16x16x32_bf16 v[124:127], a[152:155], v[28:31], v[124:127]\n\tds_read_b64_tr_b16 v[16:17], v4 offset:24576\n\tds_read_b64_tr_b16 v[18:19], v4 offset:26624\n\tv_mfma_f32_16x16x32_bf16 v[132:135], a[156:159], v[28:31], v[132:135]\n\tv_mfma_f32_16x16x32_bf16 v[140:143], a[160:163], v[28:31], v[140:143]\n\ts_waitcnt lgkmcnt(8)\n\tv_mfma_f32_16x16x32_bf16 v[120:123], a[164:167], v[32:35], v[120:123]\n\t" \
    "ds_read_b64_tr_b16 v[20:21], v5 offset:24576\n\tds_read_b64_tr_b16 v[22:23], v5 offset:26624\n\tv_mfma_f32_16x16x32_bf16 v[128:131], a[168:171], v[32:35], v[128:131]\n\tv_mfma_f32_16x16x32_bf16 v[136:139], a[172:175], v[32:35], v[136:139]\n\ts_waitcnt lgkmcnt(8)\n\tv_mfma_f32_16x16x32_bf16 v[124:127], a[164:167], v[36:39], v[124:127]\n\tds_read_b64_tr_b16 v[24:25], v4 offset:28672\n\tds_read_b64_tr_b16 v[26:27], v4 offset:30720\n\t" \
    "v_mfma_f32_16x16x32_bf16 v[132:135], a[168:171], v[36:39], v[132:135]\n\tv_mfma_f32_16x16x32_bf16 v[140:143], a[172:175], v[36:39], v[140:143]\n\ts_waitcnt lgkmcnt(8)\n\tv_mfma_f32_16x16x32_bf16 v[120:123], a[176:179], v[40:43], v[120:123]\n\tds_read_b64_tr_b16 v[28:29], v5 offset:28672\n\tds_read_b64_tr_b16 v[30:31], v5 offset:30720\n\tv_mfma_f32_16x16x32_bf16 v[128:131], a[180:183], v[40:43], v[128:131]\n\tv_mfma_f32_16x16x32_bf16 v[136:139], a[184:187], v[40:43], v[136:139]\n\t" \
    "s_waitcnt lgkmcnt(8)\n\tv_mfma_f32_16x16x32_bf16 v[124:127], a[176:179], v[44:47], v[124:127]\n\tds_read_b64_tr_b16 v[32:33], v4 offset:32768\n\tds_read_b64_tr_b16 v[34:35], v4 offset:34816\n\tv_mfma_f32_16x16x32_bf16 v[132:135], a[180:183], v[44:47], v[132:135]\n\tv_mfma_f32_16x16x32_bf16 v[140:143], a[184:187], v[44:47], v[140:143]\n\ts_waitcnt lgkmcnt(8)\n\tv_mfma_f32_16x16x32_bf16 v[120:123], a[188:191], v[16:19], v[120:123]\n\t" \
    "ds_read_b64_tr_b16 v[36:37], v5 offset:32768\n\tds_read_b64_tr_b16 v[38:39], v5 offset:34816\n\tv_mfma_f32_16x16x32_bf16 v[128:131], a[192:195], v[16:19], v[128:131]\n\tv_mfma_f32_16x16x32_bf16 v[136:139], a[196:199], v[16:19], v[136:139]\n\ts_waitcnt lgkmcnt(8)\n\tv_mfma_f32_16x16x32_bf16 v[124:127], a[188:191], v[20:23], v[124:127]\n\tds_read_b64_tr_b16 v[40:41], v226 offset:61440\n\tds_read_b64_tr_b16 v[42:43], v226 offset:63488\n\t" \
    "v_mfma_f32_16x16x32_bf16 v[132:135], a[192:195], v[20:23], v[132:135]\n\tv_mfma_f32_16x16x32_bf16 v[140:143], a[196:199], v[20:23], v[140:143]\n\ts_waitcnt lgkmcnt(8)\n\tv_mfma_f32_16x16x32_bf16 v[120:123], a[200:203], v[24:27], v[120:123]\n\tds_read_b64_tr_b16 v[44:45], v227 offset:61440\n\tds_read_b64_tr_b16 v[46:47], v227 offset:63488\n\tv_mfma_f32_16x16x32_bf16 v[128:131], a[204:207], v[24:27], v[128:131]\n\tv_mfma_f32_16x16x32_bf16 v[136:139], a[208:211], v[24:27], v[136:139]\n\t" \
    "s_waitcnt lgkmcnt(8)\n\tv_mfma_f32_16x16x32_bf16 v[124:127], a[200:203], v[28:31], v[124:127]\n\tds_read_b64_tr_b16 v[16:17], v226 offset:0\n\tds_read_b64_tr_b16 v[18:19], v226 offset:2048\n\tv_mfma_f32_16x16x32_bf16 v[132:135], a[204:207], v[28:31], v[132:135]\n\tv_mfma_f32_16x16x32_bf16 v[140:143], a[208:211], v[28:31], v[140:143]\n\ts_waitcnt lgkmcnt(8)\n\tv_mfma_f32_16x16x32_bf16 v[120:123], a[212:215], v[32:35], v[120:123]\n\t" \
    "ds_read_b64_tr_b16 v[20:21], v227 offset:0\n\tds_read_b64_tr_b16 v[22:23], v227 offset:2048\n\tv_mfma_f32_16x16x32_bf16 v[128:131], a[216:219], v[32:35], v[128:131]\n\tv_mfma_f32_16x16x32_bf16 v[136:139], a[220:223], v[32:35], v[136:139]\n\ts_waitcnt lgkmcnt(8)\n\tv_mfma_f32_16x16x32_bf16 v[124:127], a[212:215], v[36:39], v[124:127]\n\tds_read_b64_tr_b16 v[24:25], v226 offset:4096\n\tds_read_b64_tr_b16 v[26:27], v226 offset:6144\n\t" \
    "v_mfma_f32_16x16x32_bf16 v[132:135], a[216:219], v[36:39], v[132:135]\n\tv_mfma_f32_16x16x32_bf16 v[140:143], a[220:223], v[36:39], v[140:143]\n\ts_waitcnt lgkmcnt(8)\n\tv_mfma_f32_16x16x32_bf16 v[48:51], v[144:147], v[40:43], v[48:51]\n\tds_read_b64_tr_b16 v[28:29], v227 offset:4096\n\tds_read_b64_tr_b16 v[30:31], v227 offset:6144\n\ts_xor_b32 s68, s53, 4\n\tv_mfma_f32_16x16x32_bf16 v[56:59], v[148:151], v[40:43], v[56:59]\n\t" \
    "s_mul_i32 s68, s68, 12288\n\ts_add_u32 s68, s68, 7217152\n\tv_cvt_pk_bf16_f32 v120, v120, v121\n\tv_mfma_f32_16x16x32_bf16 v[64:67], v[152:155], v[40:43], v[64:67]\n\tv_cvt_pk_bf16_f32 v121, v122, v123\n\tv_cvt_pk_bf16_f32 v122, v124, v125\n\ts_waitcnt lgkmcnt(8)\n\tv_mfma_f32_16x16x32_bf16 v[52:55], v[144:147], v[44:47], v[52:55]\n\t" \
    "ds_read_b64_tr_b16 v[32:33], v226 offset:8192\n\tds_read_b64_tr_b16 v[34:35], v226 offset:10240\n\tv_mov_b32 v123, s36\n\tv_mfma_f32_16x16x32_bf16 v[60:63], v[148:151], v[44:47], v[60:63]\n\tbuffer_store_dwordx4 v[120:123], v8, s[8:11], s68 offen offset:0 sc1\n\tv_cvt_pk_bf16_f32 v124, v126, v127\n\tv_cvt_pk_bf16_f32 v125, v128, v129\n\tv_mfma_f32_16x16x32_bf16 v[68:71], v[152:155], v[44:47], v[68:71]\n\t" \
    "v_cvt_pk_bf16_f32 v126, v130, v131\n\tv_mov_b32 v127, s36\n\ts_waitcnt lgkmcnt(8)\n\tv_mfma_f32_16x16x32_bf16 v[48:51], v[188:191], v[16:19], v[48:51]\n\tds_read_b64_tr_b16 v[36:37], v227 offset:8192\n\tds_read_b64_tr_b16 v[38:39], v227 offset:10240\n\tv_mfma_f32_16x16x32_bf16 v[56:59], a[0:3], v[16:19], v[56:59]\n\tbuffer_store_dwordx4 v[124:127], v8, s[8:11], s68 offen offset:1024 sc1\n\t" \
    "v_cvt_pk_bf16_f32 v128, v132, v133\n\tv_cvt_pk_bf16_f32 v129, v134, v135\n\tv_mfma_f32_16x16x32_bf16 v[64:67], a[4:7], v[16:19], v[64:67]\n\tv_cvt_pk_bf16_f32 v130, v136, v137\n\tv_mov_b32 v131, s36\n\ts_waitcnt lgkmcnt(8)\n\tv_mfma_f32_16x16x32_bf16 v[52:55], v[188:191], v[20:23], v[52:55]\n\tds_read_b64_tr_b16 v[40:41], v226 offset:12288\n\t" \
    "ds_read_b64_tr_b16 v[42:43], v226 offset:14336\n\tv_mfma_f32_16x16x32_bf16 v[60:63], a[0:3], v[20:23], v[60:63]\n\tbuffer_store_dwordx4 v[128:131], v8, s[8:11], s68 offen offset:2048 sc1\n\tv_cvt_pk_bf16_f32 v132, v138, v139\n\tv_cvt_pk_bf16_f32 v133, v140, v141\n\tv_mfma_f32_16x16x32_bf16 v[68:71], a[4:7], v[20:23], v[68:71]\n\tv_cvt_pk_bf16_f32 v134, v142, v143\n\tv_mov_b32 v135, s36\n\t" \
    "s_waitcnt lgkmcnt(8)\n\tv_mfma_f32_16x16x32_bf16 v[48:51], v[156:159], v[24:27], v[48:51]\n\tds_read_b64_tr_b16 v[44:45], v227 offset:12288\n\tds_read_b64_tr_b16 v[46:47], v227 offset:14336\n\tv_mfma_f32_16x16x32_bf16 v[56:59], v[160:163], v[24:27], v[56:59]\n\tbuffer_store_dwordx4 v[132:135], v8, s[8:11], s68 offen offset:3072 sc1\n\tv_mfma_f32_16x16x32_bf16 v[64:67], v[164:167], v[24:27], v[64:67]\n\ts_waitcnt lgkmcnt(8)\n\t" \
    "v_mfma_f32_16x16x32_bf16 v[52:55], v[156:159], v[28:31], v[52:55]\n\tds_read_b64_tr_b16 v[16:17], v226 offset:16384\n\tds_read_b64_tr_b16 v[18:19], v226 offset:18432\n\tv_mfma_f32_16x16x32_bf16 v[60:63], v[160:163], v[28:31], v[60:63]\n\tv_mfma_f32_16x16x32_bf16 v[68:71], v[164:167], v[28:31], v[68:71]\n\ts_waitcnt lgkmcnt(8)\n\tv_mfma_f32_16x16x32_bf16 v[48:51], a[8:11], v[32:35], v[48:51]\n\tds_read_b64_tr_b16 v[20:21], v227 offset:16384\n\t" \
    "ds_read_b64_tr_b16 v[22:23], v227 offset:18432\n\tv_mfma_f32_16x16x32_bf16 v[56:59], a[12:15], v[32:35], v[56:59]\n\tv_mfma_f32_16x16x32_bf16 v[64:67], a[16:19], v[32:35], v[64:67]\n\ts_waitcnt lgkmcnt(8)\n\tv_mfma_f32_16x16x32_bf16 v[52:55], a[8:11], v[36:39], v[52:55]\n\tds_read_b64_tr_b16 v[24:25], v226 offset:20480\n\tds_read_b64_tr_b16 v[26:27], v226 offset:22528\n\tv_mfma_f32_16x16x32_bf16 v[60:63], a[12:15], v[36:39], v[60:63]\n\t" \
    "v_mfma_f32_16x16x32_bf16 v[68:71], a[16:19], v[36:39], v[68:71]\n\ts_waitcnt vmcnt(12) lgkmcnt(8)\n\tv_mfma_f32_16x16x32_bf16 v[48:51], a[32:35], v[40:43], v[48:51]\n\tds_read_b64_tr_b16 v[28:29], v227 offset:20480\n\tds_read_b64_tr_b16 v[30:31], v227 offset:22528\n\tv_mfma_f32_16x16x32_bf16 v[56:59], a[36:39], v[40:43], v[56:59]\n\tv_permlane16_swap_b32 v192, v194\n\tv_permlane16_swap_b32 v193, v195\n\t" \
    "v_mfma_f32_16x16x32_bf16 v[64:67], a[40:43], v[40:43], v[64:67]\n\tv_permlane16_swap_b32 v196, v198\n\tv_permlane16_swap_b32 v197, v199\n\ts_waitcnt lgkmcnt(8)\n\tv_mfma_f32_16x16x32_bf16 v[52:55], a[32:35], v[44:47], v[52:55]\n\tds_read_b64_tr_b16 v[32:33], v226 offset:24576\n\tds_read_b64_tr_b16 v[34:35], v226 offset:26624\n\tv_mfma_f32_16x16x32_bf16 v[60:63], a[36:39], v[44:47], v[60:63]\n\t" \
    "v_permlane16_swap_b32 v200, v202\n\tv_permlane16_swap_b32 v201, v203\n\tv_mfma_f32_16x16x32_bf16 v[68:71], a[40:43], v[44:47], v[68:71]\n\tv_lshlrev_b32 v144, 16, v192\n\tv_and_b32 v145, 0xffff0000, v192\n\ts_waitcnt lgkmcnt(8)\n\tv_mfma_f32_16x16x32_bf16 v[48:51], a[44:47], v[16:19], v[48:51]\n\tds_read_b64_tr_b16 v[36:37], v227 offset:24576\n\t" \
    "ds_read_b64_tr_b16 v[38:39], v227 offset:26624\n\tv_mfma_f32_16x16x32_bf16 v[56:59], a[48:51], v[16:19], v[56:59]\n\tv_lshlrev_b32 v146, 16, v193\n\tv_and_b32 v147, 0xffff0000, v193\n\tv_mfma_f32_16x16x32_bf16 v[64:67], a[52:55], v[16:19], v[64:67]\n\tv_lshlrev_b32 v148, 16, v194\n\tv_and_b32 v149, 0xffff0000, v194\n\ts_waitcnt lgkmcnt(8)\n\t" \
    "v_mfma_f32_16x16x32_bf16 v[52:55], a[44:47], v[20:23], v[52:55]\n\tds_read_b64_tr_b16 v[40:41], v226 offset:28672\n\tds_read_b64_tr_b16 v[42:43], v226 offset:30720\n\tv_mfma_f32_16x16x32_bf16 v[60:63], a[48:51], v[20:23], v[60:63]\n\tv_lshlrev_b32 v150, 16, v195\n\tv_and_b32 v151, 0xffff0000, v195\n\tv_mfma_f32_16x16x32_bf16 v[68:71], a[52:55], v[20:23], v[68:71]\n\tv_lshlrev_b32 v152, 16, v196\n\t" \
    "v_and_b32 v153, 0xffff0000, v196\n\ts_waitcnt lgkmcnt(8)\n\tv_mfma_f32_16x16x32_bf16 v[48:51], a[56:59], v[24:27], v[48:51]\n\tds_read_b64_tr_b16 v[44:45], v227 offset:28672\n\tds_read_b64_tr_b16 v[46:47], v227 offset:30720\n\tv_mfma_f32_16x16x32_bf16 v[56:59], a[60:63], v[24:27], v[56:59]\n\tv_lshlrev_b32 v154, 16, v197\n\tv_and_b32 v155, 0xffff0000, v197\n\t" \
    "v_mfma_f32_16x16x32_bf16 v[64:67], a[64:67], v[24:27], v[64:67]\n\tv_lshlrev_b32 v156, 16, v198\n\tv_and_b32 v157, 0xffff0000, v198\n\ts_waitcnt lgkmcnt(8)\n\tv_mfma_f32_16x16x32_bf16 v[52:55], a[56:59], v[28:31], v[52:55]\n\tds_read_b64_tr_b16 v[16:17], v226 offset:32768\n\tds_read_b64_tr_b16 v[18:19], v226 offset:34816\n\tv_mfma_f32_16x16x32_bf16 v[60:63], a[60:63], v[28:31], v[60:63]\n\t" \
    "v_lshlrev_b32 v158, 16, v199\n\tv_and_b32 v159, 0xffff0000, v199\n\tv_mfma_f32_16x16x32_bf16 v[68:71], a[64:67], v[28:31], v[68:71]\n\tv_lshlrev_b32 v160, 16, v200\n\tv_and_b32 v161, 0xffff0000, v200\n\ts_waitcnt lgkmcnt(8)\n\tv_mfma_f32_16x16x32_bf16 v[48:51], a[68:71], v[32:35], v[48:51]\n\tds_read_b64_tr_b16 v[20:21], v227 offset:32768\n\t" \
    "ds_read_b64_tr_b16 v[22:23], v227 offset:34816\n\tv_mfma_f32_16x16x32_bf16 v[56:59], a[72:75], v[32:35], v[56:59]\n\tv_lshlrev_b32 v162, 16, v201\n\tv_and_b32 v163, 0xffff0000, v201\n\tv_mfma_f32_16x16x32_bf16 v[64:67], a[76:79], v[32:35], v[64:67]\n\tv_lshlrev_b32 v164, 16, v202\n\tv_and_b32 v165, 0xffff0000, v202\n\ts_waitcnt lgkmcnt(8)\n\t" \
    "v_mfma_f32_16x16x32_bf16 v[52:55], a[68:71], v[36:39], v[52:55]\n\tds_read_b64_tr_b16 v[24:25], v226 offset:36864\n\tds_read_b64_tr_b16 v[26:27], v226 offset:38912\n\tv_mfma_f32_16x16x32_bf16 v[60:63], a[72:75], v[36:39], v[60:63]\n\tv_lshlrev_b32 v166, 16, v203\n\tv_and_b32 v167, 0xffff0000, v203\n\tv_mfma_f32_16x16x32_bf16 v[68:71], a[76:79], v[36:39], v[68:71]\n\tv_fma_f32 v168, v144, v216, v217\n\t" \
    "v_fma_f32 v169, v145, v216, v217\n\ts_waitcnt lgkmcnt(8)\n\tv_mfma_f32_16x16x32_bf16 v[48:51], a[20:23], v[40:43], v[48:51]\n\tds_read_b64_tr_b16 v[28:29], v227 offset:36864\n\tds_read_b64_tr_b16 v[30:31], v227 offset:38912\n\tv_mfma_f32_16x16x32_bf16 v[56:59], a[24:27], v[40:43], v[56:59]\n\tv_fma_f32 v170, v146, v216, v217\n\tv_fma_f32 v171, v147, v216, v217\n\t" \
    "v_mfma_f32_16x16x32_bf16 v[64:67], a[28:31], v[40:43], v[64:67]\n\tv_fma_f32 v172, v148, v218, v219\n\tv_fma_f32 v173, v149, v218, v219\n\ts_waitcnt lgkmcnt(8)\n\tv_mfma_f32_16x16x32_bf16 v[52:55], a[20:23], v[44:47], v[52:55]\n\tds_read_b64_tr_b16 v[32:33], v226 offset:40960\n\tds_read_b64_tr_b16 v[34:35], v226 offset:43008\n\tv_mfma_f32_16x16x32_bf16 v[60:63], a[24:27], v[44:47], v[60:63]\n\t" \
    "v_fma_f32 v174, v150, v218, v219\n\tv_fma_f32 v175, v151, v218, v219\n\tv_mfma_f32_16x16x32_bf16 v[68:71], a[28:31], v[44:47], v[68:71]\n\tv_fma_f32 v176, v152, v216, v217\n\tv_fma_f32 v177, v153, v216, v217\n\ts_waitcnt lgkmcnt(8)\n\tv_mfma_f32_16x16x32_bf16 v[48:51], a[80:83], v[16:19], v[48:51]\n\tds_read_b64_tr_b16 v[36:37], v227 offset:40960\n\t" \
    "ds_read_b64_tr_b16 v[38:39], v227 offset:43008\n\tv_mfma_f32_16x16x32_bf16 v[56:59], a[84:87], v[16:19], v[56:59]\n\tv_fma_f32 v178, v154, v216, v217\n\tv_fma_f32 v179, v155, v216, v217\n\tv_mfma_f32_16x16x32_bf16 v[64:67], a[88:91], v[16:19], v[64:67]\n\tv_fma_f32 v180, v156, v218, v219\n\tv_fma_f32 v181, v157, v218, v219\n\ts_waitcnt lgkmcnt(8)\n\t" \
    "v_mfma_f32_16x16x32_bf16 v[52:55], a[80:83], v[20:23], v[52:55]\n\tds_read_b64_tr_b16 v[40:41], v226 offset:45056\n\tds_read_b64_tr_b16 v[42:43], v226 offset:47104\n\tv_mfma_f32_16x16x32_bf16 v[60:63], a[84:87], v[20:23], v[60:63]\n\tv_fma_f32 v182, v158, v218, v219\n\tv_fma_f32 v183, v159, v218, v219\n\tv_mfma_f32_16x16x32_bf16 v[68:71], a[88:91], v[20:23], v[68:71]\n\tv_fma_f32 v184, v160, v216, v217\n\t" \
    "v_fma_f32 v185, v161, v216, v217\n\ts_waitcnt lgkmcnt(8)\n\tv_mfma_f32_16x16x32_bf16 v[48:51], a[92:95], v[24:27], v[48:51]\n\tds_read_b64_tr_b16 v[44:45], v227 offset:45056\n\tds_read_b64_tr_b16 v[46:47], v227 offset:47104\n\tv_mfma_f32_16x16x32_bf16 v[56:59], a[96:99], v[24:27], v[56:59]\n\tv_fma_f32 v186, v162, v216, v217\n\tv_fma_f32 v187, v163, v216, v217\n\t" \
    "v_mfma_f32_16x16x32_bf16 v[64:67], a[100:103], v[24:27], v[64:67]\n\tv_fma_f32 v188, v164, v218, v219\n\tv_fma_f32 v189, v165, v218, v219\n\ts_waitcnt lgkmcnt(8)\n\tv_mfma_f32_16x16x32_bf16 v[52:55], a[92:95], v[28:31], v[52:55]\n\tds_read_b64_tr_b16 v[16:17], v6 offset:4096\n\tds_read_b64_tr_b16 v[18:19], v6 offset:6144\n\tv_mfma_f32_16x16x32_bf16 v[60:63], a[96:99], v[28:31], v[60:63]\n\t" \
    "v_fma_f32 v190, v166, v218, v219\n\tv_fma_f32 v191, v167, v218, v219\n\tv_mfma_f32_16x16x32_bf16 v[68:71], a[100:103], v[28:31], v[68:71]\n\tv_lshlrev_b32 v220, 16, v204\n\tv_and_b32 v221, 0xffff0000, v204\n\ts_waitcnt lgkmcnt(8)\n\tv_mfma_f32_16x16x32_bf16 v[48:51], a[104:107], v[32:35], v[48:51]\n\tds_read_b64_tr_b16 v[20:21], v7 offset:4096\n\t" \
    "ds_read_b64_tr_b16 v[22:23], v7 offset:6144\n\ts_add_u32 s68, s55, 0\n\tv_mfma_f32_16x16x32_bf16 v[56:59], a[108:111], v[32:35], v[56:59]\n\tbuffer_load_dwordx4 v[72:75], v8, s[8:11], s68 offen offset:0 sc1\n\tv_mul_f32 v168, v168, v220\n\tv_mfma_f32_16x16x32_bf16 v[64:67], a[112:115], v[32:35], v[64:67]\n\tv_mul_f32 v169, v169, v221\n\tv_lshlrev_b32 v222, 16, v205\n\t" \
    "s_waitcnt lgkmcnt(8)\n\tv_mfma_f32_16x16x32_bf16 v[52:55], a[104:107], v[36:39], v[52:55]\n\tds_read_b64_tr_b16 v[24:25], v6 offset:8192\n\tds_read_b64_tr_b16 v[26:27], v6 offset:10240\n\tv_mfma_f32_16x16x32_bf16 v[60:63], a[108:111], v[36:39], v[60:63]\n\tbuffer_load_dwordx4 v[76:79], v8, s[8:11], s68 offen offset:1024 sc1\n\tv_and_b32 v223, 0xffff0000, v205\n\tv_mfma_f32_16x16x32_bf16 v[68:71], a[112:115], v[36:39], v[68:71]\n\t" \
    "v_mul_f32 v170, v170, v222\n\tv_mul_f32 v171, v171, v223\n\ts_waitcnt lgkmcnt(8)\n\tv_mfma_f32_16x16x32_bf16 v[48:51], a[116:119], v[40:43], v[48:51]\n\tds_read_b64_tr_b16 v[28:29], v7 offset:8192\n\tds_read_b64_tr_b16 v[30:31], v7 offset:10240\n\tv_mfma_f32_16x16x32_bf16 v[56:59], a[120:123], v[40:43], v[56:59]\n\tbuffer_load_dwordx4 v[80:83], v8, s[8:11], s68 offen offset:2048 sc1\n\t" \
    "v_lshlrev_b32 v220, 16, v206\n\tv_mfma_f32_16x16x32_bf16 v[64:67], a[124:127], v[40:43], v[64:67]\n\tv_and_b32 v221, 0xffff0000, v206\n\tv_mul_f32 v172, v172, v220\n\ts_waitcnt lgkmcnt(8)\n\tv_mfma_f32_16x16x32_bf16 v[52:55], a[116:119], v[44:47], v[52:55]\n\tds_read_b64_tr_b16 v[32:33], v6 offset:12288\n\tds_read_b64_tr_b16 v[34:35], v6 offset:14336\n\t" \
    "v_mfma_f32_16x16x32_bf16 v[60:63], a[120:123], v[44:47], v[60:63]\n\tbuffer_load_dwordx4 v[84:87], v8, s[8:11], s68 offen offset:3072 sc1\n\tv_mul_f32 v173, v173, v221\n\tv_mfma_f32_16x16x32_bf16 v[68:71], a[124:127], v[44:47], v[68:71]\n\tv_lshlrev_b32 v222, 16, v207\n\tv_and_b32 v223, 0xffff0000, v207\n\ts_waitcnt lgkmcnt(8)\n\tv_mfma_f32_16x16x32_bf16 v[48:51], a[128:131], v[16:19], v[48:51]\n\t" \
    "ds_read_b64_tr_b16 v[36:37], v7 offset:12288\n\tds_read_b64_tr_b16 v[38:39], v7 offset:14336\n\ts_add_u32 s68, s55, 4096\n\tv_mfma_f32_16x16x32_bf16 v[56:59], a[132:135], v[16:19], v[56:59]\n\tbuffer_load_dwordx4 v[96:99], v8, s[8:11], s68 offen offset:0 sc1\n\tv_mul_f32 v174, v174, v222\n\tv_mfma_f32_16x16x32_bf16 v[64:67], a[136:139], v[16:19], v[64:67]\n\tv_mul_f32 v175, v175, v223\n\t" \
    "v_lshlrev_b32 v220, 16, v208\n\ts_waitcnt lgkmcnt(8)\n\tv_mfma_f32_16x16x32_bf16 v[52:55], a[128:131], v[20:23], v[52:55]\n\tds_read_b64_tr_b16 v[40:41], v6 offset:16384\n\tds_read_b64_tr_b16 v[42:43], v6 offset:18432\n\tv_mfma_f32_16x16x32_bf16 v[60:63], a[132:135], v[20:23], v[60:63]\n\tbuffer_load_dwordx4 v[100:103], v8, s[8:11], s68 offen offset:1024 sc1\n\tv_and_b32 v221, 0xffff0000, v208\n\t" \
    "v_mfma_f32_16x16x32_bf16 v[68:71], a[136:139], v[20:23], v[68:71]\n\tv_mul_f32 v176, v176, v220\n\tv_mul_f32 v177, v177, v221\n\ts_waitcnt lgkmcnt(8)\n\tv_mfma_f32_16x16x32_bf16 v[48:51], a[140:143], v[24:27], v[48:51]\n\tds_read_b64_tr_b16 v[44:45], v7 offset:16384\n\tds_read_b64_tr_b16 v[46:47], v7 offset:18432\n\tv_mfma_f32_16x16x32_bf16 v[56:59], a[144:147], v[24:27], v[56:59]\n\t" \
    "v_lshlrev_b32 v222, 16, v209\n\tv_and_b32 v223, 0xffff0000, v209\n\tv_mfma_f32_16x16x32_bf16 v[64:67], a[148:151], v[24:27], v[64:67]\n\tv_mul_f32 v178, v178, v222\n\tv_mul_f32 v179, v179, v223\n\ts_waitcnt lgkmcnt(8)\n\tv_mfma_f32_16x16x32_bf16 v[52:55], a[140:143], v[28:31], v[52:55]\n\tds_read_b64_tr_b16 v[16:17], v6 offset:20480\n\t" \
    "ds_read_b64_tr_b16 v[18:19], v6 offset:22528\n\tv_mfma_f32_16x16x32_bf16 v[60:63], a[144:147], v[28:31], v[60:63]\n\tbuffer_load_dwordx4 v[104:107], v8, s[8:11], s68 offen offset:2048 sc1\n\tv_lshlrev_b32 v220, 16, v210\n\tv_mfma_f32_16x16x32_bf16 v[68:71], a[148:151], v[28:31], v[68:71]\n\tv_and_b32 v221, 0xffff0000, v210\n\tv_mul_f32 v180, v180, v220\n\ts_waitcnt lgkmcnt(8)\n\t" \
    "v_mfma_f32_16x16x32_bf16 v[48:51], a[152:155], v[32:35], v[48:51]\n\tds_read_b64_tr_b16 v[20:21], v7 offset:20480\n\tds_read_b64_tr_b16 v[22:23], v7 offset:22528\n\tv_mfma_f32_16x16x32_bf16 v[56:59], a[156:159], v[32:35], v[56:59]\n\tbuffer_load_dwordx4 v[108:111], v8, s[8:11], s68 offen offset:3072 sc1\n\tv_mul_f32 v181, v181, v221\n\ts_add_u32 s68, s55, 8192\n\tv_mfma_f32_16x16x32_bf16 v[64:67], a[160:163], v[32:35], v[64:67]\n\t" \
    "v_lshlrev_b32 v222, 16, v211\n\tv_and_b32 v223, 0xffff0000, v211\n\ts_waitcnt lgkmcnt(8)\n\tv_mfma_f32_16x16x32_bf16 v[52:55], a[152:155], v[36:39], v[52:55]\n\tds_read_b64_tr_b16 v[24:25], v6 offset:24576\n\tds_read_b64_tr_b16 v[26:27], v6 offset:26624\n\tv_mfma_f32_16x16x32_bf16 v[60:63], a[156:159], v[36:39], v[60:63]\n\tbuffer_load_dwordx4 v[120:123], v8, s[8:11], s68 offen offset:0 sc1\n\t" \
    "v_mul_f32 v182, v182, v222\n\tv_mfma_f32_16x16x32_bf16 v[68:71], a[160:163], v[36:39], v[68:71]\n\tv_mul_f32 v183, v183, v223\n\tv_lshlrev_b32 v220, 16, v212\n\ts_waitcnt lgkmcnt(8)\n\tv_mfma_f32_16x16x32_bf16 v[48:51], a[164:167], v[40:43], v[48:51]\n\tds_read_b64_tr_b16 v[28:29], v7 offset:24576\n\tds_read_b64_tr_b16 v[30:31], v7 offset:26624\n\t" \
    "v_mfma_f32_16x16x32_bf16 v[56:59], a[168:171], v[40:43], v[56:59]\n\tv_and_b32 v221, 0xffff0000, v212\n\tv_mul_f32 v184, v184, v220\n\tv_mfma_f32_16x16x32_bf16 v[64:67], a[172:175], v[40:43], v[64:67]\n\tv_mul_f32 v185, v185, v221\n\tv_lshlrev_b32 v222, 16, v213\n\ts_waitcnt lgkmcnt(8)\n\tv_mfma_f32_16x16x32_bf16 v[52:55], a[164:167], v[44:47], v[52:55]\n\t" \
    "ds_read_b64_tr_b16 v[32:33], v6 offset:28672\n\tds_read_b64_tr_b16 v[34:35], v6 offset:30720\n\tv_mfma_f32_16x16x32_bf16 v[60:63], a[168:171], v[44:47], v[60:63]\n\tbuffer_load_dwordx4 v[124:127], v8, s[8:11], s68 offen offset:1024 sc1\n\tv_and_b32 v223, 0xffff0000, v213\n\tv_mfma_f32_16x16x32_bf16 v[68:71], a[172:175], v[44:47], v[68:71]\n\tv_mul_f32 v186, v186, v222\n\tv_mul_f32 v187, v187, v223\n\t" \
    "s_waitcnt lgkmcnt(8)\n\tv_mfma_f32_16x16x32_bf16 v[48:51], a[176:179], v[16:19], v[48:51]\n\tds_read_b64_tr_b16 v[36:37], v7 offset:28672\n\tds_read_b64_tr_b16 v[38:39], v7 offset:30720\n\tv_mfma_f32_16x16x32_bf16 v[56:59], a[180:183], v[16:19], v[56:59]\n\tbuffer_load_dwordx4 v[128:131], v8, s[8:11], s68 offen offset:2048 sc1\n\tv_lshlrev_b32 v220, 16, v214\n\tv_mfma_f32_16x16x32_bf16 v[64:67], a[184:187], v[16:19], v[64:67]\n\t" \
    "v_and_b32 v221, 0xffff0000, v214\n\tv_mul_f32 v188, v188, v220\n\ts_waitcnt lgkmcnt(8)\n\tv_mfma_f32_16x16x32_bf16 v[52:55], a[176:179], v[20:23], v[52:55]\n\tds_read_b64_tr_b16 v[40:41], v6 offset:32768\n\tds_read_b64_tr_b16 v[42:43], v6 offset:34816\n\tv_mfma_f32_16x16x32_bf16 v[60:63], a[180:183], v[20:23], v[60:63]\n\tv_mul_f32 v189, v189, v221\n\t" \
    "v_lshlrev_b32 v222, 16, v215\n\tv_mfma_f32_16x16x32_bf16 v[68:71], a[184:187], v[20:23], v[68:71]\n\tv_and_b32 v223, 0xffff0000, v215\n\tv_mul_f32 v190, v190, v222\n\ts_waitcnt lgkmcnt(8)\n\tv_mfma_f32_16x16x32_bf16 v[48:51], a[188:191], v[24:27], v[48:51]\n\tds_read_b64_tr_b16 v[44:45], v7 offset:32768\n\tds_read_b64_tr_b16 v[46:47], v7 offset:34816\n\t" \
    "v_mfma_f32_16x16x32_bf16 v[56:59], a[192:195], v[24:27], v[56:59]\n\tbuffer_load_dwordx4 v[132:135], v8, s[8:11], s68 offen offset:3072 sc1\n\tv_mul_f32 v191, v191, v223\n\tv_mfma_f32_16x16x32_bf16 v[64:67], a[196:199], v[24:27], v[64:67]\n\ts_waitcnt lgkmcnt(8)\n\tv_mfma_f32_16x16x32_bf16 v[52:55], a[188:191], v[28:31], v[52:55]\n\tv_mfma_f32_16x16x32_bf16 v[60:63], a[192:195], v[28:31], v[60:63]\n\tv_mfma_f32_16x16x32_bf16 v[68:71], a[196:199], v[28:31], v[68:71]\n\t" \
    "s_waitcnt lgkmcnt(6)\n\tv_mfma_f32_16x16x32_bf16 v[48:51], a[200:203], v[32:35], v[48:51]\n\tv_mfma_f32_16x16x32_bf16 v[56:59], a[204:207], v[32:35], v[56:59]\n\tv_mfma_f32_16x16x32_bf16 v[64:67], a[208:211], v[32:35], v[64:67]\n\ts_waitcnt lgkmcnt(4)\n\tv_mfma_f32_16x16x32_bf16 v[52:55], a[200:203], v[36:39], v[52:55]\n\tv_mfma_f32_16x16x32_bf16 v[60:63], a[204:207], v[36:39], v[60:63]\n\tv_mfma_f32_16x16x32_bf16 v[68:71], a[208:211], v[36:39], v[68:71]\n\t" \
    "s_waitcnt lgkmcnt(2)\n\tv_mfma_f32_16x16x32_bf16 v[48:51], a[212:215], v[40:43], v[48:51]\n\tv_mfma_f32_16x16x32_bf16 v[56:59], a[216:219], v[40:43], v[56:59]\n\tv_mfma_f32_16x16x32_bf16 v[64:67], a[220:223], v[40:43], v[64:67]\n\ts_waitcnt lgkmcnt(0)\n\tv_mfma_f32_16x16x32_bf16 v[52:55], a[212:215], v[44:47], v[52:55]\n\tv_mfma_f32_16x16x32_bf16 v[60:63], a[216:219], v[44:47], v[60:63]\n\tv_mfma_f32_16x16x32_bf16 v[68:71], a[220:223], v[44:47], v[68:71]\n\t" \
    "s_cmp_eq_u32 s70, 0\n\ts_cbranch_scc1 113f\n\tv_cmp_ne_u64 vcc, s[36:37], v[250:251]\n\ts_cbranch_vccz 115f\n\ts_mov_b32 s71, 1048576\n\t114:\n\ts_sleep 1\n\tbuffer_load_dwordx2 v[250:251], v241, s[8:11], s51 offen sc1\n\t" \
    "s_waitcnt vmcnt(0)\n\tv_cmp_ne_u64 vcc, s[36:37], v[250:251]\n\ts_cbranch_vccz 115f\n\ts_sub_u32 s71, s71, 1\n\ts_cbranch_scc0 114b\n\t115:\n\tbuffer_load_dwordx4 v[204:207], v8, s[8:11], s56 offen offset:0 sc1\n\tbuffer_load_dwordx4 v[208:211], v8, s[8:11], s56 offen offset:1024 sc1\n\t" \
    "buffer_load_dwordx4 v[212:215], v8, s[8:11], s56 offen offset:2048 sc1\n\tv_fma_f32 v168, v144, v216, v217\n\tv_fma_f32 v169, v145, v216, v217\n\tv_fma_f32 v170, v146, v216, v217\n\tv_fma_f32 v171, v147, v216, v217\n\tv_fma_f32 v172, v148, v218, v219\n\tv_fma_f32 v173, v149, v218, v219\n\tv_fma_f32 v174, v150, v218, v219\n\t" \
    "v_fma_f32 v175, v151, v218, v219\n\tv_fma_f32 v176, v152, v216, v217\n\tv_fma_f32 v177, v153, v216, v217\n\tv_fma_f32 v178, v154, v216, v217\n\tv_fma_f32 v179, v155, v216, v217\n\tv_fma_f32 v180, v156, v218, v219\n\tv_fma_f32 v181, v157, v218, v219\n\tv_fma_f32 v182, v158, v218, v219\n\t" \
    "v_fma_f32 v183, v159, v218, v219\n\tv_fma_f32 v184, v160, v216, v217\n\tv_fma_f32 v185, v161, v216, v217\n\tv_fma_f32 v186, v162, v216, v217\n\tv_fma_f32 v187, v163, v216, v217\n\tv_fma_f32 v188, v164, v218, v219\n\tv_fma_f32 v189, v165, v218, v219\n\tv_fma_f32 v190, v166, v218, v219\n\t" \
    "v_fma_f32 v191, v167, v218, v219\n\ts_waitcnt vmcnt(0)\n\tv_lshlrev_b32 v220, 16, v204\n\tv_and_b32 v221, 0xffff0000, v204\n\tv_mul_f32 v168, v168, v220\n\tv_mul_f32 v169, v169, v221\n\tv_lshlrev_b32 v222, 16, v205\n\tv_and_b32 v223, 0xffff0000, v205\n\t" \
    "v_mul_f32 v170, v170, v222\n\tv_mul_f32 v171, v171, v223\n\tv_lshlrev_b32 v220, 16, v206\n\tv_and_b32 v221, 0xffff0000, v206\n\tv_mul_f32 v172, v172, v220\n\tv_mul_f32 v173, v173, v221\n\tv_lshlrev_b32 v222, 16, v207\n\tv_and_b32 v223, 0xffff0000, v207\n\t" \
    "v_mul_f32 v174, v174, v222\n\tv_mul_f32 v175, v175, v223\n\tv_lshlrev_b32 v220, 16, v208\n\tv_and_b32 v221, 0xffff0000, v208\n\tv_mul_f32 v176, v176, v220\n\tv_mul_f32 v177, v177, v221\n\tv_lshlrev_b32 v222, 16, v209\n\tv_and_b32 v223, 0xffff0000, v209\n\t" \
    "v_mul_f32 v178, v178, v222\n\tv_mul_f32 v179, v179, v223\n\tv_lshlrev_b32 v220, 16, v210\n\tv_and_b32 v221, 0xffff0000, v210\n\tv_mul_f32 v180, v180, v220\n\tv_mul_f32 v181, v181, v221\n\tv_lshlrev_b32 v222, 16, v211\n\tv_and_b32 v223, 0xffff0000, v211\n\t" \
    "v_mul_f32 v182, v182, v222\n\tv_mul_f32 v183, v183, v223\n\tv_lshlrev_b32 v220, 16, v212\n\tv_and_b32 v221, 0xffff0000, v212\n\tv_mul_f32 v184, v184, v220\n\tv_mul_f32 v185, v185, v221\n\tv_lshlrev_b32 v222, 16, v213\n\tv_and_b32 v223, 0xffff0000, v213\n\t" \
    "v_mul_f32 v186, v186, v222\n\tv_mul_f32 v187, v187, v223\n\tv_lshlrev_b32 v220, 16, v214\n\tv_and_b32 v221, 0xffff0000, v214\n\tv_mul_f32 v188, v188, v220\n\tv_mul_f32 v189, v189, v221\n\tv_lshlrev_b32 v222, 16, v215\n\tv_and_b32 v223, 0xffff0000, v215\n\t" \
    "v_mul_f32 v190, v190, v222\n\tv_mul_f32 v191, v191, v223\n\t113:\n\ts_mov_b32 s71, 1048576\n\t116:\n\ts_waitcnt vmcnt(0)\n\tv_xor_b32 v16, s36, v75\n\tv_xor_b32 v17, s36, v79\n\t" \
    "v_xor_b32 v18, s36, v83\n\tv_xor_b32 v19, s36, v87\n\tv_xor_b32 v20, s36, v99\n\tv_xor_b32 v21, s36, v103\n\tv_xor_b32 v22, s36, v107\n\tv_xor_b32 v23, s36, v111\n\tv_xor_b32 v24, s36, v123\n\tv_xor_b32 v25, s36, v127\n\t" \
    "v_xor_b32 v26, s36, v131\n\tv_xor_b32 v27, s36, v135\n\tv_or3_b32 v16, v16, v20, v24\n\tv_or3_b32 v17, v17, v21, v25\n\tv_or3_b32 v18, v18, v22, v26\n\tv_or3_b32 v19, v19, v23, v27\n\tv_or3_b32 v16, v16, v17, v18\n\tv_or_b32 v16, v16, v19\n\t" \
    "v_cmp_ne_u32 vcc, 0, v16\n\ts_cbranch_vccz 117f\n\ts_sub_u32 s71, s71, 1\n\ts_cbranch_scc1 117f\n\ts_add_u32 s68, s55, 0\n\tbuffer_load_dwordx4 v[72:75], v8, s[8:11], s68 offen offset:0 sc1\n\tbuffer_load_dwordx4 v[76:79], v8, s[8:11], s68 offen offset:1024 sc1\n\tbuffer_load_dwordx4 v[80:83], v8, s[8:11], s68 offen offset:2048 sc1\n\t" \
    "buffer_load_dwordx4 v[84:87], v8, s[8:11], s68 offen offset:3072 sc1\n\ts_add_u32 s68, s55, 4096\n\tbuffer_load_dwordx4 v[96:99], v8, s[8:11], s68 offen offset:0 sc1\n\tbuffer_load_dwordx4 v[100:103], v8, s[8:11], s68 offen offset:1024 sc1\n\tbuffer_load_dwordx4 v[104:107], v8, s[8:11], s68 offen offset:2048 sc1\n\tbuffer_load_dwordx4 v[108:111], v8, s[8:11], s68 offen offset:3072 sc1\n\ts_add_u32 s68, s55, 8192\n\tbuffer_load_dwordx4 v[120:123], v8, s[8:11], s68 offen offset:0 sc1\n\t" \
    "buffer_load_dwordx4 v[124:127], v8, s[8:11], s68 offen offset:1024 sc1\n\tbuffer_load_dwordx4 v[128:131], v8, s[8:11], s68 offen offset:2048 sc1\n\tbuffer_load_dwordx4 v[132:135], v8, s[8:11], s68 offen offset:3072 sc1\n\ts_branch 116b\n\t117:\n\ts_mov_b32 s68, 0xbfb8aa3b\n\ts_mov_b32 s69, 0xbfb8aa3b\n\tv_lshlrev_b32 v16, 16, v72\n\t" \
    "v_and_b32 v17, 0xffff0000, v72\n\tv_lshlrev_b32 v18, 16, v73\n\tv_and_b32 v19, 0xffff0000, v73\n\tv_lshlrev_b32 v20, 16, v74\n\tv_and_b32 v21, 0xffff0000, v74\n\tv_lshlrev_b32 v22, 16, v76\n\tv_and_b32 v23, 0xffff0000, v76\n\tv_pk_add_f32 v[48:49], v[48:49], v[16:17]\n\t" \
    "v_pk_add_f32 v[50:51], v[50:51], v[18:19]\n\tv_pk_add_f32 v[52:53], v[52:53], v[20:21]\n\tv_pk_add_f32 v[54:55], v[54:55], v[22:23]\n\tv_lshlrev_b32 v16, 16, v96\n\tv_and_b32 v17, 0xffff0000, v96\n\tv_lshlrev_b32 v18, 16, v97\n\tv_and_b32 v19, 0xffff0000, v97\n\tv_lshlrev_b32 v20, 16, v98\n\t" \
    "v_and_b32 v21, 0xffff0000, v98\n\tv_lshlrev_b32 v22, 16, v100\n\tv_and_b32 v23, 0xffff0000, v100\n\tv_pk_add_f32 v[48:49], v[48:49], v[16:17]\n\tv_pk_add_f32 v[50:51], v[50:51], v[18:19]\n\tv_pk_add_f32 v[52:53], v[52:53], v[20:21]\n\tv_pk_add_f32 v[54:55], v[54:55], v[22:23]\n\tv_lshlrev_b32 v16, 16, v120\n\t" \
    "v_and_b32 v17, 0xffff0000, v120\n\tv_lshlrev_b32 v18, 16, v121\n\tv_and_b32 v19, 0xffff0000, v121\n\tv_lshlrev_b32 v20, 16, v122\n\tv_and_b32 v21, 0xffff0000, v122\n\tv_lshlrev_b32 v22, 16, v124\n\tv_and_b32 v23, 0xffff0000, v124\n\tv_pk_add_f32 v[48:49], v[48:49], v[16:17]\n\t" \
    "v_pk_add_f32 v[50:51], v[50:51], v[18:19]\n\tv_pk_add_f32 v[52:53], v[52:53], v[20:21]\n\tv_pk_add_f32 v[54:55], v[54:55], v[22:23]\n\tv_pk_mul_f32 v[16:17], v[48:49], s[68:69]\n\tv_pk_mul_f32 v[18:19], v[50:51], s[68:69]\n\tv_pk_mul_f32 v[20:21], v[52:53], s[68:69]\n\tv_pk_mul_f32 v[22:23], v[54:55], s[68:69]\n\tv_lshlrev_b32 v24, 16, v77\n\t" \
    "v_exp_f32 v16, v16\n\tv_and_b32 v25, 0xffff0000, v77\n\tv_exp_f32 v17, v17\n\tv_lshlrev_b32 v26, 16, v78\n\tv_exp_f32 v18, v18\n\tv_and_b32 v27, 0xffff0000, v78\n\tv_exp_f32 v19, v19\n\tv_lshlrev_b32 v28, 16, v80\n\t" \
    "v_exp_f32 v20, v20\n\tv_and_b32 v29, 0xffff0000, v80\n\tv_exp_f32 v21, v21\n\tv_lshlrev_b32 v30, 16, v81\n\tv_exp_f32 v22, v22\n\tv_and_b32 v31, 0xffff0000, v81\n\tv_exp_f32 v23, v23\n\tv_pk_add_f32 v[56:57], v[56:57], v[24:25]\n\t" \
    "v_pk_add_f32 v[16:17], v[16:17], 1.0 op_sel_hi:[1,0]\n\tv_pk_add_f32 v[18:19], v[18:19], 1.0 op_sel_hi:[1,0]\n\tv_pk_add_f32 v[20:21], v[20:21], 1.0 op_sel_hi:[1,0]\n\tv_pk_add_f32 v[22:23], v[22:23], 1.0 op_sel_hi:[1,0]\n\tv_pk_add_f32 v[58:59], v[58:59], v[26:27]\n\tv_rcp_f32 v16, v16\n\tv_pk_add_f32 v[60:61], v[60:61], v[28:29]\n\tv_rcp_f32 v17, v17\n\t" \
    "v_pk_add_f32 v[62:63], v[62:63], v[30:31]\n\tv_rcp_f32 v18, v18\n\tv_lshlrev_b32 v24, 16, v101\n\tv_rcp_f32 v19, v19\n\tv_and_b32 v25, 0xffff0000, v101\n\tv_rcp_f32 v20, v20\n\tv_lshlrev_b32 v26, 16, v102\n\tv_rcp_f32 v21, v21\n\t" \
    "v_and_b32 v27, 0xffff0000, v102\n\tv_rcp_f32 v22, v22\n\tv_lshlrev_b32 v28, 16, v104\n\tv_rcp_f32 v23, v23\n\tv_and_b32 v29, 0xffff0000, v104\n\tv_pk_mul_f32 v[48:49], v[48:49], v[168:169]\n\tv_pk_mul_f32 v[50:51], v[50:51], v[170:171]\n\tv_pk_mul_f32 v[52:53], v[52:53], v[172:173]\n\t" \
    "v_pk_mul_f32 v[54:55], v[54:55], v[174:175]\n\tv_pk_fma_f32 v[16:17], v[16:17], v[48:49], v[144:145]\n\tv_pk_fma_f32 v[18:19], v[18:19], v[50:51], v[146:147]\n\tv_pk_fma_f32 v[20:21], v[20:21], v[52:53], v[148:149]\n\tv_pk_fma_f32 v[22:23], v[22:23], v[54:55], v[150:151]\n\tv_cvt_pk_bf16_f32 v48, v16, v17\n\tv_cvt_pk_bf16_f32 v49, v18, v19\n\tv_cvt_pk_bf16_f32 v50, v20, v21\n\t" \
    "v_cvt_pk_bf16_f32 v51, v22, v23\n\ts_nop 0\n\tv_permlane16_swap_b32 v48, v50\n\tv_permlane16_swap_b32 v49, v51\n\tbuffer_store_dwordx4 v[48:51], v243, s[24:27], 0 offen offset:0\n\tv_lshlrev_b32 v30, 16, v105\n\tv_and_b32 v31, 0xffff0000, v105\n\tv_pk_add_f32 v[56:57], v[56:57], v[24:25]\n\t" \
    "v_pk_add_f32 v[58:59], v[58:59], v[26:27]\n\tv_pk_add_f32 v[60:61], v[60:61], v[28:29]\n\tv_pk_add_f32 v[62:63], v[62:63], v[30:31]\n\tv_lshlrev_b32 v24, 16, v125\n\tv_and_b32 v25, 0xffff0000, v125\n\tv_lshlrev_b32 v26, 16, v126\n\tv_and_b32 v27, 0xffff0000, v126\n\tv_lshlrev_b32 v28, 16, v128\n\t" \
    "v_and_b32 v29, 0xffff0000, v128\n\tv_lshlrev_b32 v30, 16, v129\n\tv_and_b32 v31, 0xffff0000, v129\n\tv_pk_add_f32 v[56:57], v[56:57], v[24:25]\n\tv_pk_add_f32 v[58:59], v[58:59], v[26:27]\n\tv_pk_add_f32 v[60:61], v[60:61], v[28:29]\n\tv_pk_add_f32 v[62:63], v[62:63], v[30:31]\n\tv_pk_mul_f32 v[24:25], v[56:57], s[68:69]\n\t" \
    "v_pk_mul_f32 v[26:27], v[58:59], s[68:69]\n\tv_pk_mul_f32 v[28:29], v[60:61], s[68:69]\n\tv_pk_mul_f32 v[30:31], v[62:63], s[68:69]\n\tv_lshlrev_b32 v32, 16, v82\n\tv_exp_f32 v24, v24\n\tv_and_b32 v33, 0xffff0000, v82\n\tv_exp_f32 v25, v25\n\tv_lshlrev_b32 v34, 16, v84\n\t" \
    "v_exp_f32 v26, v26\n\tv_and_b32 v35, 0xffff0000, v84\n\tv_exp_f32 v27, v27\n\tv_lshlrev_b32 v36, 16, v85\n\tv_exp_f32 v28, v28\n\tv_and_b32 v37, 0xffff0000, v85\n\tv_exp_f32 v29, v29\n\tv_lshlrev_b32 v38, 16, v86\n\t" \
    "v_exp_f32 v30, v30\n\tv_and_b32 v39, 0xffff0000, v86\n\tv_exp_f32 v31, v31\n\tv_pk_add_f32 v[64:65], v[64:65], v[32:33]\n\tv_pk_add_f32 v[24:25], v[24:25], 1.0 op_sel_hi:[1,0]\n\tv_pk_add_f32 v[26:27], v[26:27], 1.0 op_sel_hi:[1,0]\n\tv_pk_add_f32 v[28:29], v[28:29], 1.0 op_sel_hi:[1,0]\n\tv_pk_add_f32 v[30:31], v[30:31], 1.0 op_sel_hi:[1,0]\n\t" \
    "v_pk_add_f32 v[66:67], v[66:67], v[34:35]\n\tv_rcp_f32 v24, v24\n\tv_pk_add_f32 v[68:69], v[68:69], v[36:37]\n\tv_rcp_f32 v25, v25\n\tv_pk_add_f32 v[70:71], v[70:71], v[38:39]\n\tv_rcp_f32 v26, v26\n\tv_lshlrev_b32 v32, 16, v106\n\tv_rcp_f32 v27, v27\n\t" \
    "v_and_b32 v33, 0xffff0000, v106\n\tv_rcp_f32 v28, v28\n\tv_lshlrev_b32 v34, 16, v108\n\tv_rcp_f32 v29, v29\n\tv_and_b32 v35, 0xffff0000, v108\n\tv_rcp_f32 v30, v30\n\tv_lshlrev_b32 v36, 16, v109\n\tv_rcp_f32 v31, v31\n\t" \
    "v_and_b32 v37, 0xffff0000, v109\n\tv_pk_mul_f32 v[56:57], v[56:57], v[176:177]\n\tv_pk_mul_f32 v[58:59], v[58:59], v[178:179]\n\tv_pk_mul_f32 v[60:61], v[60:61], v[180:181]\n\tv_pk_mul_f32 v[62:63], v[62:63], v[182:183]\n\tv_pk_fma_f32 v[24:25], v[24:25], v[56:57], v[152:153]\n\tv_pk_fma_f32 v[26:27], v[26:27], v[58:59], v[154:155]\n\tv_pk_fma_f32 v[28:29], v[28:29], v[60:61], v[156:157]\n\t" \
    "v_pk_fma_f32 v[30:31], v[30:31], v[62:63], v[158:159]\n\tv_cvt_pk_bf16_f32 v56, v24, v25\n\tv_cvt_pk_bf16_f32 v57, v26, v27\n\tv_cvt_pk_bf16_f32 v58, v28, v29\n\tv_cvt_pk_bf16_f32 v59, v30, v31\n\ts_nop 0\n\tv_permlane16_swap_b32 v56, v58\n\tv_permlane16_swap_b32 v57, v59\n\t" \
    "buffer_store_dwordx4 v[56:59], v243, s[24:27], 0 offen offset:32\n\tv_lshlrev_b32 v38, 16, v110\n\tv_and_b32 v39, 0xffff0000, v110\n\tv_pk_add_f32 v[64:65], v[64:65], v[32:33]\n\tv_pk_add_f32 v[66:67], v[66:67], v[34:35]\n\tv_pk_add_f32 v[68:69], v[68:69], v[36:37]\n\tv_pk_add_f32 v[70:71], v[70:71], v[38:39]\n\tv_lshlrev_b32 v32, 16, v130\n\t" \
    "v_and_b32 v33, 0xffff0000, v130\n\tv_lshlrev_b32 v34, 16, v132\n\tv_and_b32 v35, 0xffff0000, v132\n\tv_lshlrev_b32 v36, 16, v133\n\tv_and_b32 v37, 0xffff0000, v133\n\tv_lshlrev_b32 v38, 16, v134\n\tv_and_b32 v39, 0xffff0000, v134\n\tv_pk_add_f32 v[64:65], v[64:65], v[32:33]\n\t" \
    "v_pk_add_f32 v[66:67], v[66:67], v[34:35]\n\tv_pk_add_f32 v[68:69], v[68:69], v[36:37]\n\tv_pk_add_f32 v[70:71], v[70:71], v[38:39]\n\tv_pk_mul_f32 v[32:33], v[64:65], s[68:69]\n\tv_pk_mul_f32 v[34:35], v[66:67], s[68:69]\n\tv_pk_mul_f32 v[36:37], v[68:69], s[68:69]\n\tv_pk_mul_f32 v[38:39], v[70:71], s[68:69]\n\tv_exp_f32 v32, v32\n\t" \
    "v_exp_f32 v33, v33\n\tv_exp_f32 v34, v34\n\tv_exp_f32 v35, v35\n\tv_exp_f32 v36, v36\n\tv_exp_f32 v37, v37\n\tv_exp_f32 v38, v38\n\tv_exp_f32 v39, v39\n\tv_pk_add_f32 v[32:33], v[32:33], 1.0 op_sel_hi:[1,0]\n\t" \
    "v_pk_add_f32 v[34:35], v[34:35], 1.0 op_sel_hi:[1,0]\n\tv_pk_add_f32 v[36:37], v[36:37], 1.0 op_sel_hi:[1,0]\n\tv_pk_add_f32 v[38:39], v[38:39], 1.0 op_sel_hi:[1,0]\n\tv_rcp_f32 v32, v32\n\tv_rcp_f32 v33, v33\n\tv_rcp_f32 v34, v34\n\tv_rcp_f32 v35, v35\n\tv_rcp_f32 v36, v36\n\t" \
    "v_rcp_f32 v37, v37\n\tv_rcp_f32 v38, v38\n\tv_rcp_f32 v39, v39\n\tv_pk_mul_f32 v[64:65], v[64:65], v[184:185]\n\tv_pk_mul_f32 v[66:67], v[66:67], v[186:187]\n\tv_pk_mul_f32 v[68:69], v[68:69], v[188:189]\n\tv_pk_mul_f32 v[70:71], v[70:71], v[190:191]\n\tv_pk_fma_f32 v[32:33], v[32:33], v[64:65], v[160:161]\n\t" \
    "v_pk_fma_f32 v[34:35], v[34:35], v[66:67], v[162:163]\n\tv_pk_fma_f32 v[36:37], v[36:37], v[68:69], v[164:165]\n\tv_pk_fma_f32 v[38:39], v[38:39], v[70:71], v[166:167]\n\tv_cvt_pk_bf16_f32 v64, v32, v33\n\tv_cvt_pk_bf16_f32 v65, v34, v35\n\tv_cvt_pk_bf16_f32 v66, v36, v37\n\tv_cvt_pk_bf16_f32 v67, v38, v39\n\ts_nop 0\n\t" \
    "v_permlane16_swap_b32 v64, v66\n\tv_permlane16_swap_b32 v65, v67\n\tbuffer_store_dwordx4 v[64:67], v243, s[24:27], 0 offen offset:64\n\ts_branch 103f\n\t102:\n\ts_add_u32 s49, s49, 4096\n\ts_waitcnt vmcnt(0) lgkmcnt(0)\n\tbuffer_store_dwordx2 v[248:249], off, s[8:11], s49 sc1\n\t" \
    "s_load_dwordx8 s[12:19], s[0:1], 16\n\ts_lshr_b32 s67, s40, 3\n\ts_sub_u32 s67, s67, 24\n\ts_lshl_b32 s67, s67, 2\n\ts_add_u32 s67, s67, s38\n\ts_lshl_b32 s65, s67, 6\n\tv_add_u32 v6, s65, v240\n\tv_mul_u32_u24 v7, 52429, v6\n\t" \
    "v_lshrrev_b32 v7, 18, v7\n\tv_mul_u32_u24 v5, 5, v7\n\tv_sub_u32 v6, v6, v5\n\tv_lshrrev_b32 v5, 7, v7\n\tv_and_b32 v7, 127, v7\n\tv_lshl_add_u32 v7, v5, 10, v7\n\tv_mul_u32_u24 v7, 9216, v7\n\tv_lshl_add_u32 v6, v6, 8, v7\n\t" \
    "v_lshlrev_b32 v7, 3, v244\n\tv_lshl_add_u32 v7, v245, 12, v7\n\ts_and_b32 s65, s40, 7\n\ts_mul_i32 s66, s65, 9\n\ts_lshr_b32 s66, s66, 1\n\ts_lshl_b32 s66, s66, 8\n\ts_lshr_b32 s73, s65, 1\n\ts_mul_i32 s73, s73, 4718592\n\t" \
    "s_and_b32 s65, s65, 1\n\ts_lshl_b32 s65, s65, 11\n\ts_add_u32 s65, s65, s73\n\ts_waitcnt lgkmcnt(0)\n\ts_add_u32 s12, s12, s66\n\ts_addc_u32 s13, s13, 0\n\ts_sub_u32 s14, s14, s66\n\ts_add_u32 s16, s16, s65\n\t" \
    "s_addc_u32 s17, s17, 0\n\ts_sub_u32 s18, s18, s65\n\tv_mov_b32 v238, 2146435072\n\tv_mov_b32 v236, 2146435072\n\tv_mul_u32_u24 v237, 12, v240\n\ts_add_u32 s73, s67, 216\n\tv_add_u32 v237, s73, v237\n\tv_lshrrev_b32 v5, 4, v237\n\t" \
    "v_lshlrev_b32 v5, 7, v5\n\tv_cmp_le_u32 vcc, 144, v237\n\tv_mov_b32 v4, 1088\n\tv_xor_b32 v3, 8, v237\n\tv_cndmask_b32 v4, 0, v4, vcc\n\tv_and_b32 v3, 15, v3\n\tv_lshl_add_u32 v5, v3, 2, v5\n\tv_sub_u32 v5, v5, v4\n\t" \
    "v_subrev_u32 v237, 216, v237\n\tv_cmp_gt_u32 vcc, 72, v237\n\tv_lshlrev_b32 v5, 12, v5\n\ts_mov_b32 s43, 0\n\tv_cndmask_b32 v237, v238, v5, vcc\n\ts_nop 0\n\tv_readlane_b32 s41, v236, 0\n\tv_readlane_b32 s42, v237, 0\n\t" \
    "s_nop 4\n\tbuffer_load_dword v15, v7, s[16:19], s42 offen\n\ts_add_u32 s43, s43, 1\n\tv_readlane_b32 s42, v237, s43\n\ts_nop 4\n\tbuffer_load_dword v15, v7, s[16:19], s42 offen\n\ts_add_u32 s43, s43, 1\n\tv_readlane_b32 s42, v237, s43\n\t" \
    "s_nop 4\n\tbuffer_load_dword v15, v7, s[16:19], s42 offen\n\ts_add_u32 s43, s43, 1\n\tv_readlane_b32 s42, v237, s43\n\ts_nop 4\n\tbuffer_load_dword v15, v7, s[16:19], s42 offen\n\ts_add_u32 s43, s43, 1\n\tv_readlane_b32 s42, v237, s43\n\t" \
    "s_nop 4\n\tbuffer_load_dword v15, v7, s[16:19], s42 offen\n\ts_add_u32 s43, s43, 1\n\tv_readlane_b32 s42, v237, s43\n\ts_nop 4\n\tbuffer_load_dword v15, v7, s[16:19], s42 offen\n\ts_add_u32 s43, s43, 1\n\tv_readlane_b32 s42, v237, s43\n\t" \
    "s_nop 4\n\ts_waitcnt vmcnt(0)\n\ts_branch 103f\n\t101:\n\ts_load_dwordx8 s[12:19], s[0:1], 16\n\ts_lshr_b32 s67, s40, 3\n\ts_sub_u32 s67, s67, 27\n\ts_lshl_b32 s67, s67, 2\n\t" \
    "s_add_u32 s67, s67, s38\n\ts_lshl_b32 s65, s67, 6\n\tv_add_u32 v6, s65, v240\n\tv_mul_u32_u24 v7, 52429, v6\n\tv_lshrrev_b32 v7, 18, v7\n\tv_mul_u32_u24 v5, 5, v7\n\tv_sub_u32 v6, v6, v5\n\tv_lshrrev_b32 v5, 7, v7\n\t" \
    "v_and_b32 v7, 127, v7\n\tv_lshl_add_u32 v7, v5, 10, v7\n\tv_mul_u32_u24 v7, 9216, v7\n\tv_lshl_add_u32 v6, v6, 8, v7\n\tv_lshlrev_b32 v7, 3, v244\n\tv_lshl_add_u32 v7, v245, 12, v7\n\ts_and_b32 s65, s40, 7\n\ts_mul_i32 s66, s65, 9\n\t" \
    "s_lshr_b32 s66, s66, 1\n\ts_lshl_b32 s66, s66, 8\n\ts_lshr_b32 s73, s65, 1\n\ts_mul_i32 s73, s73, 4718592\n\ts_and_b32 s65, s65, 1\n\ts_lshl_b32 s65, s65, 11\n\ts_add_u32 s65, s65, s73\n\ts_waitcnt lgkmcnt(0)\n\t" \
    "s_add_u32 s12, s12, s66\n\ts_addc_u32 s13, s13, 0\n\ts_sub_u32 s14, s14, s66\n\ts_add_u32 s16, s16, s65\n\ts_addc_u32 s17, s17, 0\n\ts_sub_u32 s18, s18, s65\n\tv_mov_b32 v238, 2146435072\n\tv_mov_b32 v236, 2146435072\n\t" \
    "s_mov_b32 s73, 3538944\n\tv_writelane_b32 v236, s73, 0\n\ts_mov_b32 s73, 3539072\n\tv_writelane_b32 v236, s73, 1\n\ts_mov_b32 s73, 4718592\n\tv_writelane_b32 v236, s73, 2\n\ts_mov_b32 s73, 5898240\n\tv_writelane_b32 v236, s73, 3\n\t" \
    "s_mov_b32 s73, 7077888\n\tv_writelane_b32 v236, s73, 4\n\ts_mov_b32 s73, 8257536\n\tv_writelane_b32 v236, s73, 5\n\ts_mov_b32 s73, 4718720\n\tv_writelane_b32 v236, s73, 6\n\ts_mov_b32 s73, 5898368\n\tv_writelane_b32 v236, s73, 7\n\t" \
    "s_mov_b32 s73, 7078016\n\tv_writelane_b32 v236, s73, 8\n\ts_mov_b32 s73, 8257664\n\tv_writelane_b32 v236, s73, 9\n\tv_mul_u32_u24 v237, 20, v240\n\ts_add_u32 s73, s67, -104\n\tv_add_u32 v237, s73, v237\n\tv_lshrrev_b32 v5, 4, v237\n\t" \
    "v_lshlrev_b32 v5, 7, v5\n\tv_cmp_le_u32 vcc, 144, v237\n\tv_mov_b32 v4, 1088\n\tv_xor_b32 v3, 8, v237\n\tv_cndmask_b32 v4, 0, v4, vcc\n\tv_and_b32 v3, 15, v3\n\tv_lshl_add_u32 v5, v3, 2, v5\n\tv_sub_u32 v5, v5, v4\n\t" \
    "v_subrev_u32 v237, 96, v237\n\tv_cmp_gt_u32 vcc, 120, v237\n\tv_lshlrev_b32 v5, 12, v5\n\ts_mov_b32 s43, 0\n\tv_cndmask_b32 v237, v238, v5, vcc\n\ts_nop 0\n\tv_readlane_b32 s41, v236, 0\n\tv_readlane_b32 s42, v237, 0\n\t" \
    "s_nop 4\n\ts_sub_u32 s65, s40, 216\n\ts_lshl_b32 s44, s65, 4\n\ts_mul_i32 s66, s65, 12\n\ts_add_u32 s66, s66, 96\n\ts_cmp_lt_u32 s65, 24\n\ts_cselect_b32 s44, s44, s66\n\ts_cselect_b32 s45, 0, 2146435072\n\t" \
    "v_cmp_eq_u32 vcc, 0, v240\n\tv_subrev_u32 v13, 8, v8\n\tv_mov_b32 v12, 2146435072\n\tv_cndmask_b32 v13, v13, v12, vcc\n\ts_mov_b32 s4, s2\n\ts_mov_b32 s5, s3\n\ts_mov_b32 s6, 98f-99b\n\ts_mov_b32 s7, 0x20000\n\t" \
    "v_lshlrev_b32 v14, 7, v240\n\ts_mov_b32 s46, 0xffff\n\tv_and_b32 v204, 15, v240\n\tv_lshlrev_b32 v205, 3, v245\n\tv_sub_u32 v206, v205, v204\n\tv_add_u32 v206, 31, v206\n\tv_and_b32 v207, 3, v206\n\tv_mul_u32_u24 v207, 4160, v207\n\t" \
    "v_and_b32 v206, 0xfc, v206\n\tv_lshl_add_u32 v0, v206, 1, v207\n\ts_mul_i32 s65, s38, 16640\n\ts_add_u32 s65, s65, 73984\n\tv_add_u32 v0, s65, v0\n\ts_add_u32 s66, s65, 1040\n\tv_sub_u32 v2, s66, v8\n\tv_bfe_u32 v206, v240, 1, 2\n\t" \
    "v_mul_u32_u24 v206, 4160, v206\n\tv_and_b32 v207, 16, v8\n\tv_add3_u32 v9, v206, v207, s65\n\ts_mul_i32 s66, s38, 4624\n\tv_lshl_add_u32 v3, v204, 5, v205\n\tv_add_u32 v3, s66, v3\n\tv_add3_u32 v1, v3, v205, 32\n\tv_add_u32 v3, s66, v239\n\t" \
    "s_lshl_b32 s73, s38, 4\n\tv_add_u32 v206, s73, v245\n\tv_bfe_u32 v207, v240, 2, 2\n\tv_mul_u32_u24 v4, 4624, v207\n\tv_lshl_add_u32 v4, v245, 4, v4\n\tv_and_b32 v207, 3, v240\n\tv_lshl_add_u32 v4, v207, 7, v4\n\tv_add_u32 v4, s54, v4\n\t" \
    "v_mul_u32_u24 v5, 12288, v206\n\tv_add_u32 v5, v5, v244\n\tv_lshrrev_b32 v206, 5, v240\n\ts_lshl_b32 s73, s38, 2\n\tv_add_u32 v206, s73, v206\n\tv_mul_u32_u24 v206, 4624, v206\n\tv_and_b32 v207, 0x1f0, v8\n\tv_add_u32 v12, v206, v207\n\t" \
    "v_mul_u32_u24 v10, 9216, v245\n\tv_add_u32 v10, v10, v244\n\tv_mul_u32_u24 v11, 4624, v204\n\tv_lshl_add_u32 v11, v245, 4, v11\n\tv_add_u32 v11, s54, v11\n\ts_mul_i32 s62, s38, 589824\n\ts_lshl_b32 s73, s44, 4\n\ts_add_u32 s62, s62, s73\n\t" \
    "s_add_u32 s62, s62, 2490368\n\tv_lshrrev_b32 v206, 3, v204\n\tv_add_u32 v206, 2, v206\n\tv_cmp_eq_u32 vcc, v206, v245\n\tv_and_b32 v207, 1, v204\n\tv_lshlrev_b32 v207, 4, v207\n\tv_lshlrev_b32 v207, v207, s46\n\tv_cndmask_b32 v207, 0, v207, vcc\n\t" \
    "v_bfe_u32 v206, v204, 1, 2\n\tv_cmp_eq_u32 vcc, 0, v206\n\tv_mov_b32 v196, 0\n\ts_nop 0\n\tv_cndmask_b32 v232, 0, v207, vcc\n\tv_cmp_eq_u32 vcc, 1, v206\n\tv_mov_b32 v197, 0\n\ts_nop 0\n\t" \
    "v_cndmask_b32 v233, 0, v207, vcc\n\tv_cmp_eq_u32 vcc, 2, v206\n\tv_mov_b32 v198, 0\n\ts_nop 0\n\tv_cndmask_b32 v234, 0, v207, vcc\n\tv_cmp_eq_u32 vcc, 3, v206\n\tv_mov_b32 v199, 0\n\ts_nop 0\n\t" \
    "v_cndmask_b32 v235, 0, v207, vcc\n\ts_add_u32 s65, s44, 0\n\ts_lshr_b32 s65, s65, 6\n\ts_cmp_eq_u32 s65, 8\n\ts_cselect_b32 s65, 1, s65\n\ts_and_b32 s65, s65, 3\n\ts_lshl_b32 s47, s65, 11\n\ts_lshr_b32 s65, s54, 1\n\t" \
    "s_add_u32 s47, s47, s65\n\ts_add_u32 s47, s47, 17408\n\ts_add_u32 s65, s44, 11\n\ts_lshr_b32 s65, s65, 6\n\ts_cmp_eq_u32 s65, 8\n\ts_cselect_b32 s65, 1, s65\n\ts_and_b32 s65, s65, 3\n\ts_lshl_b32 s50, s65, 11\n\t" \
    "s_lshr_b32 s65, s54, 1\n\ts_add_u32 s50, s50, s65\n\ts_add_u32 s50, s50, 17408\n\tds_write_b128 v9, v[196:199] offset:4128\n\tbuffer_load_dword v15, v6, s[12:15], s41 offen\n\tbuffer_load_dword v15, v7, s[16:19], s42 offen\n\ts_add_u32 s43, s43, 1\n\tv_readlane_b32 s41, v236, s43\n\t" \
    "v_readlane_b32 s42, v237, s43\n\ts_nop 4\n\tbuffer_load_dword v15, v6, s[12:15], s41 offen\n\tbuffer_load_dword v15, v7, s[16:19], s42 offen\n\ts_add_u32 s43, s43, 1\n\tv_readlane_b32 s41, v236, s43\n\tv_readlane_b32 s42, v237, s43\n\ts_nop 4\n\t" \
    "s_waitcnt vmcnt(4)\n\tbuffer_store_dwordx2 v[248:249], off, s[8:11], s48 sc1\n\tv_alignbit_b32 v196, v167, v167, 16\n\tv_alignbit_b32 v197, v166, v166, 16\n\tv_alignbit_b32 v198, v165, v165, 16\n\tv_alignbit_b32 v199, v164, v164, 16\n\tds_write_b128 v2, v[196:199] offset:3072\n\tv_bfi_b32 v200, s46, v167, v166\n\t" \
    "v_bfi_b32 v201, s46, v166, v165\n\tv_bfi_b32 v202, s46, v165, v164\n\tv_bfi_b32 v203, s46, v164, v181\n\tds_write_b128 v2, v[200:203] offset:7232\n\tv_alignbit_b32 v196, v166, v166, 16\n\tv_alignbit_b32 v197, v165, v165, 16\n\tv_alignbit_b32 v198, v164, v164, 16\n\tv_alignbit_b32 v199, v181, v181, 16\n\t" \
    "ds_write_b128 v2, v[196:199] offset:11392\n\tv_bfi_b32 v200, s46, v166, v165\n\tv_bfi_b32 v201, s46, v165, v164\n\tv_bfi_b32 v202, s46, v164, v181\n\tv_bfi_b32 v203, s46, v181, v180\n\tds_write_b128 v2, v[200:203] offset:15552\n\tv_alignbit_b32 v196, v171, v171, 16\n\tv_alignbit_b32 v197, v170, v170, 16\n\t" \
    "v_alignbit_b32 v198, v169, v169, 16\n\tv_alignbit_b32 v199, v168, v168, 16\n\tds_write_b128 v2, v[196:199] offset:2048\n\tv_bfi_b32 v200, s46, v171, v170\n\tv_bfi_b32 v201, s46, v170, v169\n\tv_bfi_b32 v202, s46, v169, v168\n\tv_bfi_b32 v203, s46, v168, v183\n\tds_write_b128 v2, v[200:203] offset:6208\n\t" \
    "v_alignbit_b32 v196, v170, v170, 16\n\tv_alignbit_b32 v197, v169, v169, 16\n\tv_alignbit_b32 v198, v168, v168, 16\n\tv_alignbit_b32 v199, v183, v183, 16\n\tds_write_b128 v2, v[196:199] offset:10368\n\tv_bfi_b32 v200, s46, v170, v169\n\tv_bfi_b32 v201, s46, v169, v168\n\tv_bfi_b32 v202, s46, v168, v183\n\t" \
    "v_bfi_b32 v203, s46, v183, v182\n\tds_write_b128 v2, v[200:203] offset:14528\n\tv_alignbit_b32 v196, v175, v175, 16\n\tv_alignbit_b32 v197, v174, v174, 16\n\tv_alignbit_b32 v198, v173, v173, 16\n\tv_alignbit_b32 v199, v172, v172, 16\n\tds_write_b128 v2, v[196:199] offset:1024\n\tv_bfi_b32 v200, s46, v175, v174\n\t" \
    "v_bfi_b32 v201, s46, v174, v173\n\tv_bfi_b32 v202, s46, v173, v172\n\tv_bfi_b32 v203, s46, v172, v185\n\tds_write_b128 v2, v[200:203] offset:5184\n\tv_alignbit_b32 v196, v174, v174, 16\n\tv_alignbit_b32 v197, v173, v173, 16\n\tv_alignbit_b32 v198, v172, v172, 16\n\tv_alignbit_b32 v199, v185, v185, 16\n\t" \
    "ds_write_b128 v2, v[196:199] offset:9344\n\tv_bfi_b32 v200, s46, v174, v173\n\tv_bfi_b32 v201, s46, v173, v172\n\tv_bfi_b32 v202, s46, v172, v185\n\tv_bfi_b32 v203, s46, v185, v184\n\tds_write_b128 v2, v[200:203] offset:13504\n\tv_alignbit_b32 v196, v179, v179, 16\n\tv_alignbit_b32 v197, v178, v178, 16\n\t" \
    "v_alignbit_b32 v198, v177, v177, 16\n\tv_alignbit_b32 v199, v176, v176, 16\n\tds_write_b128 v2, v[196:199] offset:0\n\tv_bfi_b32 v200, s46, v179, v178\n\tv_bfi_b32 v201, s46, v178, v177\n\tv_bfi_b32 v202, s46, v177, v176\n\tv_bfi_b32 v203, s46, v176, v187\n\tds_write_b128 v2, v[200:203] offset:4160\n\t" \
    "v_alignbit_b32 v196, v178, v178, 16\n\tv_alignbit_b32 v197, v177, v177, 16\n\tv_alignbit_b32 v198, v176, v176, 16\n\tv_alignbit_b32 v199, v187, v187, 16\n\tds_write_b128 v2, v[196:199] offset:8320\n\tv_bfi_b32 v200, s46, v178, v177\n\tv_bfi_b32 v201, s46, v177, v176\n\tv_bfi_b32 v202, s46, v176, v187\n\t" \
    "v_bfi_b32 v203, s46, v187, v186\n\tds_write_b128 v2, v[200:203] offset:12480\n\tv_lshl_or_b32 v188, v188, 16, v188\n\tv_and_b32 v228, v188, v232\n\tv_and_b32 v229, v188, v233\n\tv_and_b32 v230, v188, v234\n\tv_and_b32 v231, v188, v235\n\tds_read_b64 a[0:1], v0 offset:4032\n\t" \
    "ds_read_b64 a[2:3], v0 offset:4040\n\tds_read_b64 a[4:5], v0 offset:3520\n\tds_read_b64 a[6:7], v0 offset:3528\n\tds_read_b64 a[8:9], v0 offset:3008\n\tds_read_b64 a[10:11], v0 offset:3016\n\tds_read_b64 a[12:13], v0 offset:2496\n\tds_read_b64 a[14:15], v0 offset:2504\n\tds_read_b64 a[16:17], v0 offset:1984\n\t" \
    "ds_read_b64 a[18:19], v0 offset:1992\n\tds_read_b64 a[20:21], v0 offset:1472\n\tds_read_b64 a[22:23], v0 offset:1480\n\tds_read_b64 a[24:25], v0 offset:960\n\tds_read_b64 a[26:27], v0 offset:968\n\tds_read_b64 a[28:29], v0 offset:448\n\tds_read_b64 a[30:31], v0 offset:456\n\ts_barrier\n\t" \
    "v_mov_b32 v196, 0\n\tv_mov_b32 v197, 0\n\tv_mov_b32 v198, 0\n\tv_mov_b32 v199, 0\n\tds_write_b128 v12, v[196:199]\n\tds_write_b128 v12, v[196:199] offset:9248\n\ts_add_u32 s68, s44, s38\n\ts_add_u32 s68, s68, 4\n\t" \
    "s_lshl_b32 s66, s68, 1\n\ts_lshl_b32 s68, s68, 12\n\tbuffer_load_dwordx4 v[42:45], v8, s[76:79], s68 offen offset:0\n\tbuffer_load_dwordx4 v[46:49], v8, s[76:79], s68 offen offset:1024\n\tbuffer_load_dwordx4 v[50:53], v8, s[76:79], s68 offen offset:2048\n\tbuffer_load_dwordx4 v[54:57], v8, s[76:79], s68 offen offset:3072\n\tbuffer_load_dwordx2 v[58:59], v13, s[76:79], s68 offen\n\tbuffer_load_dwordx2 v[60:61], v8, s[76:79], s68 offen offset:1016\n\t" \
    "buffer_load_dwordx2 v[62:63], v8, s[76:79], s68 offen offset:2040\n\tbuffer_load_dwordx2 v[64:65], v8, s[76:79], s68 offen offset:3064\n\tbuffer_load_ushort v66, off, s[80:83], s66\n\ts_mov_b32 s71, 1048576\n\t118:\n\tbuffer_load_dword v15, v6, s[12:15], s41 offen\n\tbuffer_load_dword v15, v7, s[16:19], s42 offen\n\ts_add_u32 s43, s43, 1\n\t" \
    "v_readlane_b32 s41, v236, s43\n\tv_readlane_b32 s42, v237, s43\n\ts_nop 4\n\ts_min_u32 s43, s43, 36\n\tbuffer_load_dwordx2 v[250:251], v239, s[8:11], s47 offen sc1\n\tbuffer_load_dwordx2 v[252:253], v239, s[8:11], s50 offen sc1\n\ts_waitcnt vmcnt(0)\n\tv_cmp_ne_u64 vcc, s[36:37], v[250:251]\n\t" \
    "v_cmp_ne_u64 s[74:75], s[36:37], v[252:253]\n\ts_or_b64 s[74:75], s[74:75], vcc\n\ts_cbranch_scc0 119f\n\ts_sleep 8\n\ts_sub_u32 s71, s71, 1\n\ts_cbranch_scc0 118b\n\t119:\n\tbuffer_load_dwordx4 v[68:71], v10, s[8:11], s62 offen sc1\n\t" \
    "s_add_u32 s62, s62, 36864\n\tbuffer_load_dwordx4 v[72:75], v10, s[8:11], s62 offen sc1\n\ts_add_u32 s62, s62, 36864\n\tbuffer_load_dwordx4 v[76:79], v10, s[8:11], s62 offen sc1\n\ts_add_u32 s62, s62, 36864\n\tbuffer_load_dwordx4 v[80:83], v10, s[8:11], s62 offen sc1\n\ts_add_u32 s62, s62, 36864\n\tbuffer_load_dwordx4 v[84:87], v10, s[8:11], s62 offen sc1\n\t" \
    "s_add_u32 s62, s62, 36864\n\tbuffer_load_dwordx4 v[88:91], v10, s[8:11], s62 offen sc1\n\ts_add_u32 s62, s62, 36864\n\tbuffer_load_dwordx4 v[92:95], v10, s[8:11], s62 offen sc1\n\ts_add_u32 s62, s62, 36864\n\tbuffer_load_dwordx4 v[96:99], v10, s[8:11], s62 offen sc1\n\ts_add_u32 s62, s62, 36864\n\tbuffer_load_dwordx4 v[100:103], v10, s[8:11], s62 offen sc1\n\t" \
    "s_add_u32 s62, s62, 36864\n\tbuffer_load_dwordx4 v[104:107], v10, s[8:11], s62 offen sc1\n\ts_add_u32 s62, s62, 36864\n\tbuffer_load_dwordx4 v[108:111], v10, s[8:11], s62 offen sc1\n\ts_add_u32 s62, s62, 36864\n\tbuffer_load_dwordx4 v[112:115], v10, s[8:11], s62 offen sc1\n\ts_add_u32 s62, s62, 36864\n\tbuffer_load_dwordx4 v[116:119], v10, s[8:11], s62 offen sc1\n\t" \
    "s_add_u32 s62, s62, 36864\n\tbuffer_load_dwordx4 v[120:123], v10, s[8:11], s62 offen sc1\n\ts_add_u32 s62, s62, 36864\n\tbuffer_load_dwordx4 v[124:127], v10, s[8:11], s62 offen sc1\n\ts_add_u32 s62, s62, 36864\n\tbuffer_load_dwordx4 v[128:131], v10, s[8:11], s62 offen sc1\n\ts_add_u32 s62, s62, 36864\n\ts_waitcnt vmcnt(15)\n\t" \
    "ds_write_b128 v11, v[68:71] offset:512\n\ts_waitcnt vmcnt(14)\n\tds_write_b128 v11, v[72:75] offset:576\n\ts_waitcnt vmcnt(13)\n\tds_write_b128 v11, v[76:79] offset:640\n\ts_waitcnt vmcnt(12)\n\tds_write_b128 v11, v[80:83] offset:704\n\ts_waitcnt vmcnt(11)\n\t" \
    "ds_write_b128 v11, v[84:87] offset:768\n\ts_waitcnt vmcnt(10)\n\tds_write_b128 v11, v[88:91] offset:832\n\ts_waitcnt vmcnt(9)\n\tds_write_b128 v11, v[92:95] offset:896\n\ts_waitcnt vmcnt(8)\n\tds_write_b128 v11, v[96:99] offset:960\n\ts_waitcnt vmcnt(7)\n\t" \
    "ds_write_b128 v11, v[100:103] offset:1024\n\ts_waitcnt vmcnt(6)\n\tds_write_b128 v11, v[104:107] offset:1088\n\ts_waitcnt vmcnt(5)\n\tds_write_b128 v11, v[108:111] offset:1152\n\ts_waitcnt vmcnt(4)\n\tds_write_b128 v11, v[112:115] offset:1216\n\ts_waitcnt vmcnt(3)\n\t" \
    "ds_write_b128 v11, v[116:119] offset:1280\n\ts_waitcnt vmcnt(2)\n\tds_write_b128 v11, v[120:123] offset:1344\n\ts_waitcnt vmcnt(1)\n\tds_write_b128 v11, v[124:127] offset:1408\n\ts_waitcnt vmcnt(0)\n\tds_write_b128 v11, v[128:131] offset:1472\n\ts_waitcnt lgkmcnt(0)\n\t" \
    "s_barrier\n\tds_read_b128 v[68:71], v1 offset:448\n\tds_read_b128 v[72:75], v1 offset:960\n\tds_read_b128 v[76:79], v1 offset:1472\n\tds_read_b128 v[80:83], v1 offset:1984\n\tds_read_b128 v[84:87], v1 offset:2496\n\tds_read_b128 v[88:91], v1 offset:3008\n\tds_read_b128 v[92:95], v1 offset:3520\n\t" \
    "ds_read_b128 v[96:99], v1 offset:4032\n\tbuffer_load_dword v15, v6, s[12:15], s41 offen\n\tbuffer_load_dword v15, v7, s[16:19], s42 offen\n\ts_add_u32 s43, s43, 1\n\tv_readlane_b32 s41, v236, s43\n\tv_readlane_b32 s42, v237, s43\n\ts_nop 4\n\tbuffer_load_dword v15, v6, s[12:15], s41 offen\n\t" \
    "buffer_load_dword v15, v7, s[16:19], s42 offen\n\ts_add_u32 s43, s43, 1\n\tv_readlane_b32 s41, v236, s43\n\tv_readlane_b32 s42, v237, s43\n\ts_nop 4\n\ts_waitcnt lgkmcnt(0)\n\tv_mfma_f32_16x16x32_bf16 v[132:135], v[228:231], v[68:71], 0\n\tds_read_b64 a[32:33], v0 offset:3968\n\t" \
    "ds_read_b64 a[34:35], v0 offset:3976\n\tv_mfma_f32_16x16x32_bf16 v[136:139], v[228:231], v[72:75], 0\n\tds_read_b64 a[36:37], v0 offset:3456\n\tds_read_b64 a[38:39], v0 offset:3464\n\tv_mfma_f32_16x16x32_bf16 v[140:143], v[228:231], v[76:79], 0\n\tds_read_b64 a[40:41], v0 offset:2944\n\tds_read_b64 a[42:43], v0 offset:2952\n\tv_mfma_f32_16x16x32_bf16 v[144:147], v[228:231], v[80:83], 0\n\t" \
    "ds_read_b64 a[44:45], v0 offset:2432\n\tds_read_b64 a[46:47], v0 offset:2440\n\tv_mfma_f32_16x16x32_bf16 v[148:151], v[228:231], v[84:87], 0\n\tds_read_b64 a[48:49], v0 offset:1920\n\tds_read_b64 a[50:51], v0 offset:1928\n\tv_mfma_f32_16x16x32_bf16 v[152:155], v[228:231], v[88:91], 0\n\tds_read_b64 a[52:53], v0 offset:1408\n\tv_mfma_f32_16x16x32_bf16 v[156:159], v[228:231], v[92:95], 0\n\t" \
    "ds_read_b64 a[54:55], v0 offset:1416\n\tv_mfma_f32_16x16x32_bf16 v[160:163], v[228:231], v[96:99], 0\n\tds_read_b64 a[56:57], v0 offset:896\n\tv_mfma_f32_16x16x32_bf16 v[132:135], a[0:3], v[68:71], v[132:135]\n\tds_read_b64 a[58:59], v0 offset:904\n\tv_mfma_f32_16x16x32_bf16 v[136:139], a[0:3], v[72:75], v[136:139]\n\tds_read_b64 a[60:61], v0 offset:384\n\tv_mfma_f32_16x16x32_bf16 v[140:143], a[0:3], v[76:79], v[140:143]\n\t" \
    "ds_read_b64 a[62:63], v0 offset:392\n\tv_mfma_f32_16x16x32_bf16 v[144:147], a[0:3], v[80:83], v[144:147]\n\tds_read_b128 v[100:103], v1 offset:384\n\tv_mfma_f32_16x16x32_bf16 v[148:151], a[0:3], v[84:87], v[148:151]\n\tds_read_b128 v[104:107], v1 offset:896\n\tv_mfma_f32_16x16x32_bf16 v[152:155], a[0:3], v[88:91], v[152:155]\n\tds_read_b128 v[108:111], v1 offset:1408\n\tv_mfma_f32_16x16x32_bf16 v[156:159], a[0:3], v[92:95], v[156:159]\n\t" \
    "ds_read_b128 v[112:115], v1 offset:1920\n\tv_mfma_f32_16x16x32_bf16 v[160:163], a[0:3], v[96:99], v[160:163]\n\tds_read_b128 v[116:119], v1 offset:2432\n\tv_mfma_f32_16x16x32_bf16 v[136:139], a[4:7], v[68:71], v[136:139]\n\tds_read_b128 v[120:123], v1 offset:2944\n\tv_mfma_f32_16x16x32_bf16 v[140:143], a[4:7], v[72:75], v[140:143]\n\tds_read_b128 v[124:127], v1 offset:3456\n\tv_mfma_f32_16x16x32_bf16 v[144:147], a[4:7], v[76:79], v[144:147]\n\t" \
    "ds_read_b128 v[128:131], v1 offset:3968\n\tv_mfma_f32_16x16x32_bf16 v[148:151], a[4:7], v[80:83], v[148:151]\n\tds_read_b64 a[64:65], v0 offset:3904\n\tv_mfma_f32_16x16x32_bf16 v[152:155], a[4:7], v[84:87], v[152:155]\n\tds_read_b64 a[66:67], v0 offset:3912\n\tv_mfma_f32_16x16x32_bf16 v[156:159], a[4:7], v[88:91], v[156:159]\n\tds_read_b64 a[68:69], v0 offset:3392\n\tv_mfma_f32_16x16x32_bf16 v[160:163], a[4:7], v[92:95], v[160:163]\n\t" \
    "buffer_load_dword v15, v6, s[12:15], s41 offen\n\tds_read_b64 a[70:71], v0 offset:3400\n\tv_mfma_f32_16x16x32_bf16 v[140:143], a[8:11], v[68:71], v[140:143]\n\tbuffer_load_dword v15, v7, s[16:19], s42 offen\n\tds_read_b64 a[72:73], v0 offset:2880\n\tv_mfma_f32_16x16x32_bf16 v[144:147], a[8:11], v[72:75], v[144:147]\n\ts_add_u32 s43, s43, 1\n\tds_read_b64 a[74:75], v0 offset:2888\n\t" \
    "v_mfma_f32_16x16x32_bf16 v[148:151], a[8:11], v[76:79], v[148:151]\n\tv_readlane_b32 s41, v236, s43\n\tv_readlane_b32 s42, v237, s43\n\ts_nop 4\n\tds_read_b64 a[76:77], v0 offset:2368\n\tv_mfma_f32_16x16x32_bf16 v[152:155], a[8:11], v[80:83], v[152:155]\n\tds_read_b64 a[78:79], v0 offset:2376\n\tv_mfma_f32_16x16x32_bf16 v[156:159], a[8:11], v[84:87], v[156:159]\n\t" \
    "ds_read_b64 a[80:81], v0 offset:1856\n\tv_mfma_f32_16x16x32_bf16 v[160:163], a[8:11], v[88:91], v[160:163]\n\tds_read_b64 a[82:83], v0 offset:1864\n\tv_mfma_f32_16x16x32_bf16 v[144:147], a[12:15], v[68:71], v[144:147]\n\tds_read_b64 a[84:85], v0 offset:1344\n\tv_mfma_f32_16x16x32_bf16 v[148:151], a[12:15], v[72:75], v[148:151]\n\tds_read_b64 a[86:87], v0 offset:1352\n\tv_mfma_f32_16x16x32_bf16 v[152:155], a[12:15], v[76:79], v[152:155]\n\t" \
    "ds_read_b64 a[88:89], v0 offset:832\n\tv_mfma_f32_16x16x32_bf16 v[156:159], a[12:15], v[80:83], v[156:159]\n\tds_read_b64 a[90:91], v0 offset:840\n\tv_mfma_f32_16x16x32_bf16 v[160:163], a[12:15], v[84:87], v[160:163]\n\tds_read_b64 a[92:93], v0 offset:320\n\tv_mfma_f32_16x16x32_bf16 v[148:151], a[16:19], v[68:71], v[148:151]\n\tds_read_b64 a[94:95], v0 offset:328\n\tv_mfma_f32_16x16x32_bf16 v[152:155], a[16:19], v[72:75], v[152:155]\n\t" \
    "ds_read_b64 a[96:97], v0 offset:3840\n\tv_mfma_f32_16x16x32_bf16 v[156:159], a[16:19], v[76:79], v[156:159]\n\tds_read_b64 a[98:99], v0 offset:3848\n\tv_mfma_f32_16x16x32_bf16 v[160:163], a[16:19], v[80:83], v[160:163]\n\tds_read_b64 a[100:101], v0 offset:3328\n\tv_mfma_f32_16x16x32_bf16 v[152:155], a[20:23], v[68:71], v[152:155]\n\tds_read_b64 a[102:103], v0 offset:3336\n\tv_mfma_f32_16x16x32_bf16 v[156:159], a[20:23], v[72:75], v[156:159]\n\t" \
    "v_mfma_f32_16x16x32_bf16 v[160:163], a[20:23], v[76:79], v[160:163]\n\tv_mfma_f32_16x16x32_bf16 v[156:159], a[24:27], v[68:71], v[156:159]\n\tv_mfma_f32_16x16x32_bf16 v[160:163], a[24:27], v[72:75], v[160:163]\n\tv_mfma_f32_16x16x32_bf16 v[160:163], a[28:31], v[68:71], v[160:163]\n\ts_waitcnt lgkmcnt(15)\n\tv_mfma_f32_16x16x32_bf16 v[132:135], a[32:35], v[100:103], v[132:135]\n\tds_read_b64 a[104:105], v0 offset:2816\n\tds_read_b64 a[106:107], v0 offset:2824\n\t" \
    "v_mfma_f32_16x16x32_bf16 v[136:139], a[32:35], v[104:107], v[136:139]\n\tds_read_b64 a[108:109], v0 offset:2304\n\tds_read_b64 a[110:111], v0 offset:2312\n\tv_mfma_f32_16x16x32_bf16 v[140:143], a[32:35], v[108:111], v[140:143]\n\tds_read_b64 a[112:113], v0 offset:1792\n\tds_read_b64 a[114:115], v0 offset:1800\n\tv_mfma_f32_16x16x32_bf16 v[144:147], a[32:35], v[112:115], v[144:147]\n\tds_read_b64 a[116:117], v0 offset:1280\n\t" \
    "ds_read_b64 a[118:119], v0 offset:1288\n\tv_mfma_f32_16x16x32_bf16 v[148:151], a[32:35], v[116:119], v[148:151]\n\tds_read_b64 a[120:121], v0 offset:768\n\tds_read_b64 a[122:123], v0 offset:776\n\tv_mfma_f32_16x16x32_bf16 v[152:155], a[32:35], v[120:123], v[152:155]\n\tds_read_b64 a[124:125], v0 offset:256\n\tv_mfma_f32_16x16x32_bf16 v[156:159], a[32:35], v[124:127], v[156:159]\n\tds_read_b64 a[126:127], v0 offset:264\n\t" \
    "v_mfma_f32_16x16x32_bf16 v[160:163], a[32:35], v[128:131], v[160:163]\n\tds_read_b64 a[128:129], v0 offset:3776\n\tv_mfma_f32_16x16x32_bf16 v[136:139], a[36:39], v[100:103], v[136:139]\n\tds_read_b64 a[130:131], v0 offset:3784\n\tv_mfma_f32_16x16x32_bf16 v[140:143], a[36:39], v[104:107], v[140:143]\n\tds_read_b64 a[132:133], v0 offset:3264\n\tv_mfma_f32_16x16x32_bf16 v[144:147], a[36:39], v[108:111], v[144:147]\n\tds_read_b64 a[134:135], v0 offset:3272\n\t" \
    "v_mfma_f32_16x16x32_bf16 v[148:151], a[36:39], v[112:115], v[148:151]\n\tds_read_b128 v[68:71], v1 offset:320\n\tv_mfma_f32_16x16x32_bf16 v[152:155], a[36:39], v[116:119], v[152:155]\n\tds_read_b128 v[72:75], v1 offset:832\n\tv_mfma_f32_16x16x32_bf16 v[156:159], a[36:39], v[120:123], v[156:159]\n\tds_read_b128 v[76:79], v1 offset:1344\n\tv_mfma_f32_16x16x32_bf16 v[160:163], a[36:39], v[124:127], v[160:163]\n\tds_read_b128 v[80:83], v1 offset:1856\n\t" \
    "v_mfma_f32_16x16x32_bf16 v[140:143], a[40:43], v[100:103], v[140:143]\n\tds_read_b128 v[84:87], v1 offset:2368\n\tv_mfma_f32_16x16x32_bf16 v[144:147], a[40:43], v[104:107], v[144:147]\n\tds_read_b128 v[88:91], v1 offset:2880\n\tv_mfma_f32_16x16x32_bf16 v[148:151], a[40:43], v[108:111], v[148:151]\n\tds_read_b128 v[92:95], v1 offset:3392\n\tv_mfma_f32_16x16x32_bf16 v[152:155], a[40:43], v[112:115], v[152:155]\n\tds_read_b128 v[96:99], v1 offset:3904\n\t" \
    "v_mfma_f32_16x16x32_bf16 v[156:159], a[40:43], v[116:119], v[156:159]\n\tds_read_b64 a[136:137], v0 offset:2752\n\tv_mfma_f32_16x16x32_bf16 v[160:163], a[40:43], v[120:123], v[160:163]\n\tds_read_b64 a[138:139], v0 offset:2760\n\tv_mfma_f32_16x16x32_bf16 v[144:147], a[44:47], v[100:103], v[144:147]\n\tds_read_b64 a[140:141], v0 offset:2240\n\tv_mfma_f32_16x16x32_bf16 v[148:151], a[44:47], v[104:107], v[148:151]\n\tds_read_b64 a[142:143], v0 offset:2248\n\t" \
    "v_mfma_f32_16x16x32_bf16 v[152:155], a[44:47], v[108:111], v[152:155]\n\tds_read_b64 a[144:145], v0 offset:1728\n\tv_mfma_f32_16x16x32_bf16 v[156:159], a[44:47], v[112:115], v[156:159]\n\tds_read_b64 a[146:147], v0 offset:1736\n\tv_mfma_f32_16x16x32_bf16 v[160:163], a[44:47], v[116:119], v[160:163]\n\tds_read_b64 a[148:149], v0 offset:1216\n\tv_mfma_f32_16x16x32_bf16 v[148:151], a[48:51], v[100:103], v[148:151]\n\tds_read_b64 a[150:151], v0 offset:1224\n\t" \
    "v_mfma_f32_16x16x32_bf16 v[152:155], a[48:51], v[104:107], v[152:155]\n\tds_read_b64 a[152:153], v0 offset:704\n\tv_mfma_f32_16x16x32_bf16 v[156:159], a[48:51], v[108:111], v[156:159]\n\tds_read_b64 a[154:155], v0 offset:712\n\tv_mfma_f32_16x16x32_bf16 v[160:163], a[48:51], v[112:115], v[160:163]\n\tds_read_b64 a[156:157], v0 offset:192\n\tv_mfma_f32_16x16x32_bf16 v[152:155], a[52:55], v[100:103], v[152:155]\n\tds_read_b64 a[158:159], v0 offset:200\n\t" \
    "v_mfma_f32_16x16x32_bf16 v[156:159], a[52:55], v[104:107], v[156:159]\n\tv_mfma_f32_16x16x32_bf16 v[160:163], a[52:55], v[108:111], v[160:163]\n\tv_mfma_f32_16x16x32_bf16 v[156:159], a[56:59], v[100:103], v[156:159]\n\tv_mfma_f32_16x16x32_bf16 v[160:163], a[56:59], v[104:107], v[160:163]\n\tv_mfma_f32_16x16x32_bf16 v[160:163], a[60:63], v[100:103], v[160:163]\n\ts_waitcnt lgkmcnt(12)\n\tv_mfma_f32_16x16x32_bf16 v[132:135], a[64:67], v[68:71], v[132:135]\n\tds_read_b64 a[160:161], v0 offset:3712\n\t" \
    "ds_read_b64 a[162:163], v0 offset:3720\n\tv_mfma_f32_16x16x32_bf16 v[136:139], a[64:67], v[72:75], v[136:139]\n\tds_read_b64 a[164:165], v0 offset:3200\n\tds_read_b64 a[166:167], v0 offset:3208\n\tv_mfma_f32_16x16x32_bf16 v[140:143], a[64:67], v[76:79], v[140:143]\n\tds_read_b64 a[168:169], v0 offset:2688\n\tds_read_b64 a[170:171], v0 offset:2696\n\tv_mfma_f32_16x16x32_bf16 v[144:147], a[64:67], v[80:83], v[144:147]\n\t" \
    "ds_read_b64 a[172:173], v0 offset:2176\n\tds_read_b64 a[174:175], v0 offset:2184\n\tv_mfma_f32_16x16x32_bf16 v[148:151], a[64:67], v[84:87], v[148:151]\n\tds_read_b64 a[176:177], v0 offset:1664\n\tds_read_b64 a[178:179], v0 offset:1672\n\tv_mfma_f32_16x16x32_bf16 v[152:155], a[64:67], v[88:91], v[152:155]\n\tds_read_b64 a[180:181], v0 offset:1152\n\tv_mfma_f32_16x16x32_bf16 v[156:159], a[64:67], v[92:95], v[156:159]\n\t" \
    "ds_read_b64 a[182:183], v0 offset:1160\n\tv_mfma_f32_16x16x32_bf16 v[160:163], a[64:67], v[96:99], v[160:163]\n\tds_read_b64 a[184:185], v0 offset:640\n\tv_mfma_f32_16x16x32_bf16 v[136:139], a[68:71], v[68:71], v[136:139]\n\tds_read_b64 a[186:187], v0 offset:648\n\tv_mfma_f32_16x16x32_bf16 v[140:143], a[68:71], v[72:75], v[140:143]\n\tds_read_b64 a[188:189], v0 offset:128\n\tv_mfma_f32_16x16x32_bf16 v[144:147], a[68:71], v[76:79], v[144:147]\n\t" \
    "ds_read_b64 a[190:191], v0 offset:136\n\tv_mfma_f32_16x16x32_bf16 v[148:151], a[68:71], v[80:83], v[148:151]\n\tds_read_b128 v[100:103], v1 offset:256\n\tv_mfma_f32_16x16x32_bf16 v[152:155], a[68:71], v[84:87], v[152:155]\n\tds_read_b128 v[104:107], v1 offset:768\n\tv_mfma_f32_16x16x32_bf16 v[156:159], a[68:71], v[88:91], v[156:159]\n\tds_read_b128 v[108:111], v1 offset:1280\n\tv_mfma_f32_16x16x32_bf16 v[160:163], a[68:71], v[92:95], v[160:163]\n\t" \
    "ds_read_b128 v[112:115], v1 offset:1792\n\tv_mfma_f32_16x16x32_bf16 v[140:143], a[72:75], v[68:71], v[140:143]\n\tds_read_b128 v[116:119], v1 offset:2304\n\tv_mfma_f32_16x16x32_bf16 v[144:147], a[72:75], v[72:75], v[144:147]\n\tbuffer_load_dword v15, v6, s[12:15], s41 offen\n\tds_read_b128 v[120:123], v1 offset:2816\n\tv_mfma_f32_16x16x32_bf16 v[148:151], a[72:75], v[76:79], v[148:151]\n\tbuffer_load_dword v15, v7, s[16:19], s42 offen\n\t" \
    "ds_read_b128 v[124:127], v1 offset:3328\n\tv_mfma_f32_16x16x32_bf16 v[152:155], a[72:75], v[80:83], v[152:155]\n\ts_add_u32 s43, s43, 1\n\tds_read_b128 v[128:131], v1 offset:3840\n\tv_mfma_f32_16x16x32_bf16 v[156:159], a[72:75], v[84:87], v[156:159]\n\tv_readlane_b32 s41, v236, s43\n\tv_readlane_b32 s42, v237, s43\n\ts_nop 4\n\t" \
    "ds_read_b64 a[192:193], v0 offset:3648\n\tv_mfma_f32_16x16x32_bf16 v[160:163], a[72:75], v[88:91], v[160:163]\n\tds_read_b64 a[194:195], v0 offset:3656\n\tv_mfma_f32_16x16x32_bf16 v[144:147], a[76:79], v[68:71], v[144:147]\n\tds_read_b64 a[196:197], v0 offset:3136\n\tv_mfma_f32_16x16x32_bf16 v[148:151], a[76:79], v[72:75], v[148:151]\n\tds_read_b64 a[198:199], v0 offset:3144\n\tv_mfma_f32_16x16x32_bf16 v[152:155], a[76:79], v[76:79], v[152:155]\n\t" \
    "ds_read_b64 a[200:201], v0 offset:2624\n\tv_mfma_f32_16x16x32_bf16 v[156:159], a[76:79], v[80:83], v[156:159]\n\tds_read_b64 a[202:203], v0 offset:2632\n\tv_mfma_f32_16x16x32_bf16 v[160:163], a[76:79], v[84:87], v[160:163]\n\tds_read_b64 a[204:205], v0 offset:2112\n\tv_mfma_f32_16x16x32_bf16 v[148:151], a[80:83], v[68:71], v[148:151]\n\tds_read_b64 a[206:207], v0 offset:2120\n\tv_mfma_f32_16x16x32_bf16 v[152:155], a[80:83], v[72:75], v[152:155]\n\t" \
    "ds_read_b64 a[208:209], v0 offset:1600\n\tv_mfma_f32_16x16x32_bf16 v[156:159], a[80:83], v[76:79], v[156:159]\n\tds_read_b64 a[210:211], v0 offset:1608\n\tv_mfma_f32_16x16x32_bf16 v[160:163], a[80:83], v[80:83], v[160:163]\n\tds_read_b64 a[212:213], v0 offset:1088\n\tv_mfma_f32_16x16x32_bf16 v[152:155], a[84:87], v[68:71], v[152:155]\n\tds_read_b64 a[214:215], v0 offset:1096\n\tv_mfma_f32_16x16x32_bf16 v[156:159], a[84:87], v[72:75], v[156:159]\n\t" \
    "v_mfma_f32_16x16x32_bf16 v[160:163], a[84:87], v[76:79], v[160:163]\n\tv_mfma_f32_16x16x32_bf16 v[156:159], a[88:91], v[68:71], v[156:159]\n\tv_mfma_f32_16x16x32_bf16 v[160:163], a[88:91], v[72:75], v[160:163]\n\tv_mfma_f32_16x16x32_bf16 v[160:163], a[92:95], v[68:71], v[160:163]\n\ts_waitcnt lgkmcnt(12)\n\tv_mfma_f32_16x16x32_bf16 v[132:135], a[96:99], v[100:103], v[132:135]\n\tds_read_b64 a[216:217], v0 offset:576\n\tv_mfma_f32_16x16x32_bf16 v[136:139], a[96:99], v[104:107], v[136:139]\n\t" \
    "ds_read_b64 a[218:219], v0 offset:584\n\tv_mfma_f32_16x16x32_bf16 v[140:143], a[96:99], v[108:111], v[140:143]\n\tds_read_b64 a[220:221], v0 offset:64\n\tv_mfma_f32_16x16x32_bf16 v[144:147], a[96:99], v[112:115], v[144:147]\n\tds_read_b64 a[222:223], v0 offset:72\n\tv_mfma_f32_16x16x32_bf16 v[148:151], a[96:99], v[116:119], v[148:151]\n\tds_read_b64 a[224:225], v0 offset:3584\n\tv_mfma_f32_16x16x32_bf16 v[152:155], a[96:99], v[120:123], v[152:155]\n\t" \
    "ds_read_b64 a[226:227], v0 offset:3592\n\tv_mfma_f32_16x16x32_bf16 v[156:159], a[96:99], v[124:127], v[156:159]\n\tds_read_b64 a[228:229], v0 offset:3072\n\tv_mfma_f32_16x16x32_bf16 v[160:163], a[96:99], v[128:131], v[160:163]\n\tds_read_b64 a[230:231], v0 offset:3080\n\tv_mfma_f32_16x16x32_bf16 v[136:139], a[100:103], v[100:103], v[136:139]\n\tds_read_b64 a[232:233], v0 offset:2560\n\tv_mfma_f32_16x16x32_bf16 v[140:143], a[100:103], v[104:107], v[140:143]\n\t" \
    "ds_read_b64 a[234:235], v0 offset:2568\n\tv_mfma_f32_16x16x32_bf16 v[144:147], a[100:103], v[108:111], v[144:147]\n\tds_read_b64 a[236:237], v0 offset:2048\n\tv_mfma_f32_16x16x32_bf16 v[148:151], a[100:103], v[112:115], v[148:151]\n\tds_read_b64 a[238:239], v0 offset:2056\n\tv_mfma_f32_16x16x32_bf16 v[152:155], a[100:103], v[116:119], v[152:155]\n\tds_read_b64 a[240:241], v0 offset:1536\n\tv_mfma_f32_16x16x32_bf16 v[156:159], a[100:103], v[120:123], v[156:159]\n\t" \
    "ds_read_b64 a[242:243], v0 offset:1544\n\tv_mfma_f32_16x16x32_bf16 v[160:163], a[100:103], v[124:127], v[160:163]\n\tds_read_b64 a[244:245], v0 offset:1024\n\tv_mfma_f32_16x16x32_bf16 v[140:143], a[104:107], v[100:103], v[140:143]\n\tds_read_b64 a[246:247], v0 offset:1032\n\tv_mfma_f32_16x16x32_bf16 v[144:147], a[104:107], v[104:107], v[144:147]\n\tds_read_b128 v[68:71], v1 offset:192\n\tv_mfma_f32_16x16x32_bf16 v[148:151], a[104:107], v[108:111], v[148:151]\n\t" \
    "ds_read_b128 v[72:75], v1 offset:704\n\tv_mfma_f32_16x16x32_bf16 v[152:155], a[104:107], v[112:115], v[152:155]\n\tds_read_b128 v[76:79], v1 offset:1216\n\tv_mfma_f32_16x16x32_bf16 v[156:159], a[104:107], v[116:119], v[156:159]\n\tds_read_b128 v[80:83], v1 offset:1728\n\tv_mfma_f32_16x16x32_bf16 v[160:163], a[104:107], v[120:123], v[160:163]\n\tds_read_b128 v[84:87], v1 offset:2240\n\tv_mfma_f32_16x16x32_bf16 v[144:147], a[108:111], v[100:103], v[144:147]\n\t" \
    "ds_read_b128 v[88:91], v1 offset:2752\n\tv_mfma_f32_16x16x32_bf16 v[148:151], a[108:111], v[104:107], v[148:151]\n\tds_read_b128 v[92:95], v1 offset:3264\n\tv_mfma_f32_16x16x32_bf16 v[152:155], a[108:111], v[108:111], v[152:155]\n\tds_read_b128 v[96:99], v1 offset:3776\n\tv_mfma_f32_16x16x32_bf16 v[156:159], a[108:111], v[112:115], v[156:159]\n\tds_read_b64 a[248:249], v0 offset:512\n\tv_mfma_f32_16x16x32_bf16 v[160:163], a[108:111], v[116:119], v[160:163]\n\t" \
    "ds_read_b64 a[250:251], v0 offset:520\n\tv_mfma_f32_16x16x32_bf16 v[148:151], a[112:115], v[100:103], v[148:151]\n\tds_read_b64 a[252:253], v0 offset:0\n\tv_mfma_f32_16x16x32_bf16 v[152:155], a[112:115], v[104:107], v[152:155]\n\tds_read_b64 a[254:255], v0 offset:8\n\tv_mfma_f32_16x16x32_bf16 v[156:159], a[112:115], v[108:111], v[156:159]\n\tv_mfma_f32_16x16x32_bf16 v[160:163], a[112:115], v[112:115], v[160:163]\n\tv_mfma_f32_16x16x32_bf16 v[152:155], a[116:119], v[100:103], v[152:155]\n\t" \
    "v_mfma_f32_16x16x32_bf16 v[156:159], a[116:119], v[104:107], v[156:159]\n\tv_mfma_f32_16x16x32_bf16 v[160:163], a[116:119], v[108:111], v[160:163]\n\tv_mfma_f32_16x16x32_bf16 v[156:159], a[120:123], v[100:103], v[156:159]\n\tv_mfma_f32_16x16x32_bf16 v[160:163], a[120:123], v[104:107], v[160:163]\n\tv_mfma_f32_16x16x32_bf16 v[160:163], a[124:127], v[100:103], v[160:163]\n\ts_waitcnt lgkmcnt(4)\n\tv_mfma_f32_16x16x32_bf16 v[132:135], a[128:131], v[68:71], v[132:135]\n\tds_read_b128 v[100:103], v1 offset:128\n\t" \
    "v_alignbit_b32 v196, v45, v45, 16\n\tv_mfma_f32_16x16x32_bf16 v[136:139], a[128:131], v[72:75], v[136:139]\n\tds_read_b128 v[104:107], v1 offset:640\n\tv_alignbit_b32 v197, v44, v44, 16\n\tv_mfma_f32_16x16x32_bf16 v[140:143], a[128:131], v[76:79], v[140:143]\n\tds_read_b128 v[108:111], v1 offset:1152\n\tv_alignbit_b32 v198, v43, v43, 16\n\tv_mfma_f32_16x16x32_bf16 v[144:147], a[128:131], v[80:83], v[144:147]\n\t" \
    "ds_read_b128 v[112:115], v1 offset:1664\n\tv_alignbit_b32 v199, v42, v42, 16\n\tv_mfma_f32_16x16x32_bf16 v[148:151], a[128:131], v[84:87], v[148:151]\n\tds_write_b128 v2, v[196:199] offset:3072\n\tv_mfma_f32_16x16x32_bf16 v[152:155], a[128:131], v[88:91], v[152:155]\n\tds_read_b128 v[116:119], v1 offset:2176\n\tv_bfi_b32 v200, s46, v45, v44\n\tv_mfma_f32_16x16x32_bf16 v[156:159], a[128:131], v[92:95], v[156:159]\n\t" \
    "ds_read_b128 v[120:123], v1 offset:2688\n\tv_bfi_b32 v201, s46, v44, v43\n\tv_mfma_f32_16x16x32_bf16 v[160:163], a[128:131], v[96:99], v[160:163]\n\tds_read_b128 v[124:127], v1 offset:3200\n\tv_bfi_b32 v202, s46, v43, v42\n\tv_mfma_f32_16x16x32_bf16 v[136:139], a[132:135], v[68:71], v[136:139]\n\tds_read_b128 v[128:131], v1 offset:3712\n\tv_bfi_b32 v203, s46, v42, v59\n\t" \
    "v_mfma_f32_16x16x32_bf16 v[140:143], a[132:135], v[72:75], v[140:143]\n\tds_write_b128 v2, v[200:203] offset:7232\n\tv_mfma_f32_16x16x32_bf16 v[144:147], a[132:135], v[76:79], v[144:147]\n\tv_alignbit_b32 v196, v44, v44, 16\n\tv_alignbit_b32 v197, v43, v43, 16\n\tv_mfma_f32_16x16x32_bf16 v[148:151], a[132:135], v[80:83], v[148:151]\n\tv_alignbit_b32 v198, v42, v42, 16\n\tv_alignbit_b32 v199, v59, v59, 16\n\t" \
    "v_mfma_f32_16x16x32_bf16 v[152:155], a[132:135], v[84:87], v[152:155]\n\tds_write_b128 v2, v[196:199] offset:11392\n\tv_mfma_f32_16x16x32_bf16 v[156:159], a[132:135], v[88:91], v[156:159]\n\tv_bfi_b32 v200, s46, v44, v43\n\tv_bfi_b32 v201, s46, v43, v42\n\tv_mfma_f32_16x16x32_bf16 v[160:163], a[132:135], v[92:95], v[160:163]\n\tv_bfi_b32 v202, s46, v42, v59\n\tv_bfi_b32 v203, s46, v59, v58\n\t" \
    "v_mfma_f32_16x16x32_bf16 v[140:143], a[136:139], v[68:71], v[140:143]\n\tds_write_b128 v2, v[200:203] offset:15552\n\tv_mfma_f32_16x16x32_bf16 v[144:147], a[136:139], v[72:75], v[144:147]\n\tv_alignbit_b32 v196, v49, v49, 16\n\tv_alignbit_b32 v197, v48, v48, 16\n\tv_mfma_f32_16x16x32_bf16 v[148:151], a[136:139], v[76:79], v[148:151]\n\tv_alignbit_b32 v198, v47, v47, 16\n\tv_alignbit_b32 v199, v46, v46, 16\n\t" \
    "v_mfma_f32_16x16x32_bf16 v[152:155], a[136:139], v[80:83], v[152:155]\n\tbuffer_load_dword v15, v6, s[12:15], s41 offen\n\tv_mfma_f32_16x16x32_bf16 v[156:159], a[136:139], v[84:87], v[156:159]\n\tbuffer_load_dword v15, v7, s[16:19], s42 offen\n\tv_mfma_f32_16x16x32_bf16 v[160:163], a[136:139], v[88:91], v[160:163]\n\ts_add_u32 s43, s43, 1\n\tds_write_b128 v2, v[196:199] offset:2048\n\tv_mfma_f32_16x16x32_bf16 v[144:147], a[140:143], v[68:71], v[144:147]\n\t" \
    "v_readlane_b32 s41, v236, s43\n\tv_readlane_b32 s42, v237, s43\n\ts_nop 4\n\tv_bfi_b32 v200, s46, v49, v48\n\tv_bfi_b32 v201, s46, v48, v47\n\tv_mfma_f32_16x16x32_bf16 v[148:151], a[140:143], v[72:75], v[148:151]\n\tv_bfi_b32 v202, s46, v47, v46\n\tv_bfi_b32 v203, s46, v46, v61\n\t" \
    "v_mfma_f32_16x16x32_bf16 v[152:155], a[140:143], v[76:79], v[152:155]\n\tds_write_b128 v2, v[200:203] offset:6208\n\tv_mfma_f32_16x16x32_bf16 v[156:159], a[140:143], v[80:83], v[156:159]\n\tv_alignbit_b32 v196, v48, v48, 16\n\tv_alignbit_b32 v197, v47, v47, 16\n\tv_mfma_f32_16x16x32_bf16 v[160:163], a[140:143], v[84:87], v[160:163]\n\tv_alignbit_b32 v198, v46, v46, 16\n\tv_alignbit_b32 v199, v61, v61, 16\n\t" \
    "v_mfma_f32_16x16x32_bf16 v[148:151], a[144:147], v[68:71], v[148:151]\n\tds_write_b128 v2, v[196:199] offset:10368\n\tv_mfma_f32_16x16x32_bf16 v[152:155], a[144:147], v[72:75], v[152:155]\n\tv_bfi_b32 v200, s46, v48, v47\n\tv_bfi_b32 v201, s46, v47, v46\n\tv_mfma_f32_16x16x32_bf16 v[156:159], a[144:147], v[76:79], v[156:159]\n\tv_bfi_b32 v202, s46, v46, v61\n\tv_bfi_b32 v203, s46, v61, v60\n\t" \
    "v_mfma_f32_16x16x32_bf16 v[160:163], a[144:147], v[80:83], v[160:163]\n\tds_write_b128 v2, v[200:203] offset:14528\n\tv_mfma_f32_16x16x32_bf16 v[152:155], a[148:151], v[68:71], v[152:155]\n\tv_alignbit_b32 v196, v53, v53, 16\n\tv_alignbit_b32 v197, v52, v52, 16\n\tv_mfma_f32_16x16x32_bf16 v[156:159], a[148:151], v[72:75], v[156:159]\n\tv_alignbit_b32 v198, v51, v51, 16\n\tv_alignbit_b32 v199, v50, v50, 16\n\t" \
    "v_mfma_f32_16x16x32_bf16 v[160:163], a[148:151], v[76:79], v[160:163]\n\tds_write_b128 v2, v[196:199] offset:1024\n\tv_mfma_f32_16x16x32_bf16 v[156:159], a[152:155], v[68:71], v[156:159]\n\tv_bfi_b32 v200, s46, v53, v52\n\tv_bfi_b32 v201, s46, v52, v51\n\tv_mfma_f32_16x16x32_bf16 v[160:163], a[152:155], v[72:75], v[160:163]\n\tv_bfi_b32 v202, s46, v51, v50\n\tv_bfi_b32 v203, s46, v50, v63\n\t" \
    "v_mfma_f32_16x16x32_bf16 v[160:163], a[156:159], v[68:71], v[160:163]\n\tds_write_b128 v2, v[200:203] offset:5184\n\ts_waitcnt lgkmcnt(9)\n\tv_mfma_f32_16x16x32_bf16 v[132:135], a[160:163], v[100:103], v[132:135]\n\tds_read_b128 v[68:71], v1 offset:64\n\tv_alignbit_b32 v196, v52, v52, 16\n\tv_mfma_f32_16x16x32_bf16 v[136:139], a[160:163], v[104:107], v[136:139]\n\tds_read_b128 v[72:75], v1 offset:576\n\t" \
    "v_alignbit_b32 v197, v51, v51, 16\n\tv_mfma_f32_16x16x32_bf16 v[140:143], a[160:163], v[108:111], v[140:143]\n\tds_read_b128 v[76:79], v1 offset:1088\n\tv_alignbit_b32 v198, v50, v50, 16\n\tv_mfma_f32_16x16x32_bf16 v[144:147], a[160:163], v[112:115], v[144:147]\n\tds_read_b128 v[80:83], v1 offset:1600\n\tv_alignbit_b32 v199, v63, v63, 16\n\tv_mfma_f32_16x16x32_bf16 v[148:151], a[160:163], v[116:119], v[148:151]\n\t" \
    "ds_write_b128 v2, v[196:199] offset:9344\n\tv_mfma_f32_16x16x32_bf16 v[152:155], a[160:163], v[120:123], v[152:155]\n\tds_read_b128 v[84:87], v1 offset:2112\n\tv_bfi_b32 v200, s46, v52, v51\n\tv_mfma_f32_16x16x32_bf16 v[156:159], a[160:163], v[124:127], v[156:159]\n\tds_read_b128 v[88:91], v1 offset:2624\n\tv_bfi_b32 v201, s46, v51, v50\n\tv_mfma_f32_16x16x32_bf16 v[160:163], a[160:163], v[128:131], v[160:163]\n\t" \
    "ds_read_b128 v[92:95], v1 offset:3136\n\tv_bfi_b32 v202, s46, v50, v63\n\tv_mfma_f32_16x16x32_bf16 v[136:139], a[164:167], v[100:103], v[136:139]\n\tds_read_b128 v[96:99], v1 offset:3648\n\tv_bfi_b32 v203, s46, v63, v62\n\tv_mfma_f32_16x16x32_bf16 v[140:143], a[164:167], v[104:107], v[140:143]\n\tds_write_b128 v2, v[200:203] offset:13504\n\tv_mfma_f32_16x16x32_bf16 v[144:147], a[164:167], v[108:111], v[144:147]\n\t" \
    "v_alignbit_b32 v196, v57, v57, 16\n\tv_alignbit_b32 v197, v56, v56, 16\n\tv_mfma_f32_16x16x32_bf16 v[148:151], a[164:167], v[112:115], v[148:151]\n\tv_alignbit_b32 v198, v55, v55, 16\n\tv_alignbit_b32 v199, v54, v54, 16\n\tv_mfma_f32_16x16x32_bf16 v[152:155], a[164:167], v[116:119], v[152:155]\n\tds_write_b128 v2, v[196:199] offset:0\n\tv_mfma_f32_16x16x32_bf16 v[156:159], a[164:167], v[120:123], v[156:159]\n\t" \
    "v_bfi_b32 v200, s46, v57, v56\n\tv_bfi_b32 v201, s46, v56, v55\n\tv_mfma_f32_16x16x32_bf16 v[160:163], a[164:167], v[124:127], v[160:163]\n\tv_bfi_b32 v202, s46, v55, v54\n\tv_bfi_b32 v203, s46, v54, v65\n\tv_mfma_f32_16x16x32_bf16 v[140:143], a[168:171], v[100:103], v[140:143]\n\tds_write_b128 v2, v[200:203] offset:4160\n\tv_mfma_f32_16x16x32_bf16 v[144:147], a[168:171], v[104:107], v[144:147]\n\t" \
    "v_alignbit_b32 v196, v56, v56, 16\n\tv_alignbit_b32 v197, v55, v55, 16\n\tv_mfma_f32_16x16x32_bf16 v[148:151], a[168:171], v[108:111], v[148:151]\n\tv_alignbit_b32 v198, v54, v54, 16\n\tv_alignbit_b32 v199, v65, v65, 16\n\tv_mfma_f32_16x16x32_bf16 v[152:155], a[168:171], v[112:115], v[152:155]\n\tds_write_b128 v2, v[196:199] offset:8320\n\tv_mfma_f32_16x16x32_bf16 v[156:159], a[168:171], v[116:119], v[156:159]\n\t" \
    "v_bfi_b32 v200, s46, v56, v55\n\tv_bfi_b32 v201, s46, v55, v54\n\tv_mfma_f32_16x16x32_bf16 v[160:163], a[168:171], v[120:123], v[160:163]\n\tv_bfi_b32 v202, s46, v54, v65\n\tv_bfi_b32 v203, s46, v65, v64\n\tv_mfma_f32_16x16x32_bf16 v[144:147], a[172:175], v[100:103], v[144:147]\n\tds_write_b128 v2, v[200:203] offset:12480\n\tv_mfma_f32_16x16x32_bf16 v[148:151], a[172:175], v[104:107], v[148:151]\n\t" \
    "v_lshl_or_b32 v66, v66, 16, v66\n\tv_and_b32 v228, v66, v232\n\tv_mfma_f32_16x16x32_bf16 v[152:155], a[172:175], v[108:111], v[152:155]\n\tv_and_b32 v229, v66, v233\n\tv_and_b32 v230, v66, v234\n\tv_mfma_f32_16x16x32_bf16 v[156:159], a[172:175], v[112:115], v[156:159]\n\tv_and_b32 v231, v66, v235\n\tv_mfma_f32_16x16x32_bf16 v[160:163], a[172:175], v[116:119], v[160:163]\n\t" \
    "v_mfma_f32_16x16x32_bf16 v[148:151], a[176:179], v[100:103], v[148:151]\n\tv_mfma_f32_16x16x32_bf16 v[152:155], a[176:179], v[104:107], v[152:155]\n\tv_mfma_f32_16x16x32_bf16 v[156:159], a[176:179], v[108:111], v[156:159]\n\tv_mfma_f32_16x16x32_bf16 v[160:163], a[176:179], v[112:115], v[160:163]\n\tv_mfma_f32_16x16x32_bf16 v[152:155], a[180:183], v[100:103], v[152:155]\n\tv_mfma_f32_16x16x32_bf16 v[156:159], a[180:183], v[104:107], v[156:159]\n\tv_mfma_f32_16x16x32_bf16 v[160:163], a[180:183], v[108:111], v[160:163]\n\tv_mfma_f32_16x16x32_bf16 v[156:159], a[184:187], v[100:103], v[156:159]\n\t" \
    "v_mfma_f32_16x16x32_bf16 v[160:163], a[184:187], v[104:107], v[160:163]\n\tv_mfma_f32_16x16x32_bf16 v[160:163], a[188:191], v[100:103], v[160:163]\n\ts_waitcnt lgkmcnt(5)\n\tv_mfma_f32_16x16x32_bf16 v[132:135], a[192:195], v[68:71], v[132:135]\n\ts_add_u32 s68, s44, s38\n\tds_read_b128 v[100:103], v1 offset:0\n\tv_mfma_f32_16x16x32_bf16 v[136:139], a[192:195], v[72:75], v[136:139]\n\ts_add_u32 s68, s68, 8\n\t" \
    "ds_read_b128 v[104:107], v1 offset:512\n\tv_mfma_f32_16x16x32_bf16 v[140:143], a[192:195], v[76:79], v[140:143]\n\ts_lshl_b32 s66, s68, 1\n\tds_read_b128 v[108:111], v1 offset:1024\n\tv_mfma_f32_16x16x32_bf16 v[144:147], a[192:195], v[80:83], v[144:147]\n\ts_lshl_b32 s68, s68, 12\n\tbuffer_load_dwordx4 v[16:19], v8, s[76:79], s68 offen offset:0\n\tds_read_b128 v[112:115], v1 offset:1536\n\t" \
    "v_mfma_f32_16x16x32_bf16 v[148:151], a[192:195], v[84:87], v[148:151]\n\tds_read_b128 v[116:119], v1 offset:2048\n\tv_mfma_f32_16x16x32_bf16 v[152:155], a[192:195], v[88:91], v[152:155]\n\tds_read_b128 v[120:123], v1 offset:2560\n\tv_mfma_f32_16x16x32_bf16 v[156:159], a[192:195], v[92:95], v[156:159]\n\tds_read_b128 v[124:127], v1 offset:3072\n\tv_mfma_f32_16x16x32_bf16 v[160:163], a[192:195], v[96:99], v[160:163]\n\tds_read_b128 v[128:131], v1 offset:3584\n\t" \
    "v_mfma_f32_16x16x32_bf16 v[136:139], a[196:199], v[68:71], v[136:139]\n\tv_mfma_f32_16x16x32_bf16 v[140:143], a[196:199], v[72:75], v[140:143]\n\tbuffer_load_dwordx4 v[20:23], v8, s[76:79], s68 offen offset:1024\n\tv_mfma_f32_16x16x32_bf16 v[144:147], a[196:199], v[76:79], v[144:147]\n\tv_mfma_f32_16x16x32_bf16 v[148:151], a[196:199], v[80:83], v[148:151]\n\tv_mfma_f32_16x16x32_bf16 v[152:155], a[196:199], v[84:87], v[152:155]\n\tv_mfma_f32_16x16x32_bf16 v[156:159], a[196:199], v[88:91], v[156:159]\n\tv_mfma_f32_16x16x32_bf16 v[160:163], a[196:199], v[92:95], v[160:163]\n\t" \
    "v_mfma_f32_16x16x32_bf16 v[140:143], a[200:203], v[68:71], v[140:143]\n\tbuffer_load_dwordx4 v[24:27], v8, s[76:79], s68 offen offset:2048\n\tv_mfma_f32_16x16x32_bf16 v[144:147], a[200:203], v[72:75], v[144:147]\n\tv_mfma_f32_16x16x32_bf16 v[148:151], a[200:203], v[76:79], v[148:151]\n\tv_mfma_f32_16x16x32_bf16 v[152:155], a[200:203], v[80:83], v[152:155]\n\tv_mfma_f32_16x16x32_bf16 v[156:159], a[200:203], v[84:87], v[156:159]\n\tv_mfma_f32_16x16x32_bf16 v[160:163], a[200:203], v[88:91], v[160:163]\n\tv_mfma_f32_16x16x32_bf16 v[144:147], a[204:207], v[68:71], v[144:147]\n\t" \
    "buffer_load_dwordx4 v[28:31], v8, s[76:79], s68 offen offset:3072\n\tv_mfma_f32_16x16x32_bf16 v[148:151], a[204:207], v[72:75], v[148:151]\n\tv_mfma_f32_16x16x32_bf16 v[152:155], a[204:207], v[76:79], v[152:155]\n\tv_mfma_f32_16x16x32_bf16 v[156:159], a[204:207], v[80:83], v[156:159]\n\tv_mfma_f32_16x16x32_bf16 v[160:163], a[204:207], v[84:87], v[160:163]\n\tv_mfma_f32_16x16x32_bf16 v[148:151], a[208:211], v[68:71], v[148:151]\n\tv_mfma_f32_16x16x32_bf16 v[152:155], a[208:211], v[72:75], v[152:155]\n\tbuffer_load_dwordx2 v[32:33], v13, s[76:79], s68 offen\n\t" \
    "v_mfma_f32_16x16x32_bf16 v[156:159], a[208:211], v[76:79], v[156:159]\n\tv_mfma_f32_16x16x32_bf16 v[160:163], a[208:211], v[80:83], v[160:163]\n\tv_mfma_f32_16x16x32_bf16 v[152:155], a[212:215], v[68:71], v[152:155]\n\tv_mfma_f32_16x16x32_bf16 v[156:159], a[212:215], v[72:75], v[156:159]\n\tv_mfma_f32_16x16x32_bf16 v[160:163], a[212:215], v[76:79], v[160:163]\n\tv_mfma_f32_16x16x32_bf16 v[156:159], a[216:219], v[68:71], v[156:159]\n\tbuffer_load_dwordx2 v[34:35], v8, s[76:79], s68 offen offset:1016\n\tv_mfma_f32_16x16x32_bf16 v[160:163], a[216:219], v[72:75], v[160:163]\n\t" \
    "v_mfma_f32_16x16x32_bf16 v[160:163], a[220:223], v[68:71], v[160:163]\n\ts_waitcnt lgkmcnt(0)\n\tv_mfma_f32_16x16x32_bf16 v[132:135], a[224:227], v[100:103], v[132:135]\n\tds_read_b64 a[0:1], v0 offset:4032\n\tv_mfma_f32_16x16x32_bf16 v[136:139], a[224:227], v[104:107], v[136:139]\n\tds_read_b64 a[2:3], v0 offset:4040\n\tv_mfma_f32_16x16x32_bf16 v[140:143], a[224:227], v[108:111], v[140:143]\n\tds_read_b64 a[4:5], v0 offset:3520\n\t" \
    "v_mfma_f32_16x16x32_bf16 v[144:147], a[224:227], v[112:115], v[144:147]\n\tbuffer_load_dwordx2 v[36:37], v8, s[76:79], s68 offen offset:2040\n\tds_read_b64 a[6:7], v0 offset:3528\n\tv_mfma_f32_16x16x32_bf16 v[148:151], a[224:227], v[116:119], v[148:151]\n\tds_read_b64 a[8:9], v0 offset:3008\n\tv_mfma_f32_16x16x32_bf16 v[152:155], a[224:227], v[120:123], v[152:155]\n\tds_read_b64 a[10:11], v0 offset:3016\n\tv_mfma_f32_16x16x32_bf16 v[156:159], a[224:227], v[124:127], v[156:159]\n\t" \
    "ds_read_b64 a[12:13], v0 offset:2496\n\tv_mfma_f32_16x16x32_bf16 v[160:163], a[224:227], v[128:131], v[160:163]\n\tds_read_b64 a[14:15], v0 offset:2504\n\tv_mfma_f32_16x16x32_bf16 v[136:139], a[228:231], v[100:103], v[136:139]\n\tds_read_b64 a[16:17], v0 offset:1984\n\tv_mfma_f32_16x16x32_bf16 v[140:143], a[228:231], v[104:107], v[140:143]\n\tbuffer_load_dwordx2 v[38:39], v8, s[76:79], s68 offen offset:3064\n\tds_read_b64 a[18:19], v0 offset:1992\n\t" \
    "v_mfma_f32_16x16x32_bf16 v[144:147], a[228:231], v[108:111], v[144:147]\n\tds_read_b64 a[20:21], v0 offset:1472\n\tv_mfma_f32_16x16x32_bf16 v[148:151], a[228:231], v[112:115], v[148:151]\n\tds_read_b64 a[22:23], v0 offset:1480\n\tv_mfma_f32_16x16x32_bf16 v[152:155], a[228:231], v[116:119], v[152:155]\n\tds_read_b64 a[24:25], v0 offset:960\n\tv_mfma_f32_16x16x32_bf16 v[156:159], a[228:231], v[120:123], v[156:159]\n\tds_read_b64 a[26:27], v0 offset:968\n\t" \
    "v_mfma_f32_16x16x32_bf16 v[160:163], a[228:231], v[124:127], v[160:163]\n\tds_read_b64 a[28:29], v0 offset:448\n\tv_mfma_f32_16x16x32_bf16 v[140:143], a[232:235], v[100:103], v[140:143]\n\tbuffer_load_ushort v40, off, s[80:83], s66\n\tds_read_b64 a[30:31], v0 offset:456\n\tv_mfma_f32_16x16x32_bf16 v[144:147], a[232:235], v[104:107], v[144:147]\n\tds_read_b128 v[68:71], v1 offset:18944\n\tv_mfma_f32_16x16x32_bf16 v[148:151], a[232:235], v[108:111], v[148:151]\n\t" \
    "ds_read_b128 v[72:75], v1 offset:19456\n\tv_mfma_f32_16x16x32_bf16 v[152:155], a[232:235], v[112:115], v[152:155]\n\tds_read_b128 v[76:79], v1 offset:19968\n\tv_mfma_f32_16x16x32_bf16 v[156:159], a[232:235], v[116:119], v[156:159]\n\tds_read_b128 v[80:83], v1 offset:20480\n\tv_mfma_f32_16x16x32_bf16 v[160:163], a[232:235], v[120:123], v[160:163]\n\tds_read_b128 v[84:87], v1 offset:20992\n\tv_mfma_f32_16x16x32_bf16 v[144:147], a[236:239], v[100:103], v[144:147]\n\t" \
    "buffer_load_dword v15, v6, s[12:15], s41 offen\n\tds_read_b128 v[88:91], v1 offset:21504\n\tv_mfma_f32_16x16x32_bf16 v[148:151], a[236:239], v[104:107], v[148:151]\n\tbuffer_load_dword v15, v7, s[16:19], s42 offen\n\tds_read_b128 v[92:95], v1 offset:22016\n\tv_mfma_f32_16x16x32_bf16 v[152:155], a[236:239], v[108:111], v[152:155]\n\ts_add_u32 s43, s43, 1\n\tds_read_b128 v[96:99], v1 offset:22528\n\t" \
    "v_mfma_f32_16x16x32_bf16 v[156:159], a[236:239], v[112:115], v[156:159]\n\tv_readlane_b32 s41, v236, s43\n\tv_readlane_b32 s42, v237, s43\n\ts_nop 4\n\tv_mfma_f32_16x16x32_bf16 v[160:163], a[236:239], v[116:119], v[160:163]\n\tv_mfma_f32_16x16x32_bf16 v[148:151], a[240:243], v[100:103], v[148:151]\n\tv_mfma_f32_16x16x32_bf16 v[152:155], a[240:243], v[104:107], v[152:155]\n\tv_mfma_f32_16x16x32_bf16 v[156:159], a[240:243], v[108:111], v[156:159]\n\t" \
    "v_mfma_f32_16x16x32_bf16 v[160:163], a[240:243], v[112:115], v[160:163]\n\tv_mfma_f32_16x16x32_bf16 v[152:155], a[244:247], v[100:103], v[152:155]\n\tv_mfma_f32_16x16x32_bf16 v[156:159], a[244:247], v[104:107], v[156:159]\n\tv_mfma_f32_16x16x32_bf16 v[160:163], a[244:247], v[108:111], v[160:163]\n\tv_mfma_f32_16x16x32_bf16 v[156:159], a[248:251], v[100:103], v[156:159]\n\tv_mfma_f32_16x16x32_bf16 v[160:163], a[248:251], v[104:107], v[160:163]\n\tv_mfma_f32_16x16x32_bf16 v[160:163], a[252:255], v[100:103], v[160:163]\n\ts_waitcnt lgkmcnt(0)\n\t" \
    "v_mfma_f32_16x16x32_bf16 v[164:167], v[228:231], v[68:71], 0\n\tds_read_b64 a[32:33], v0 offset:3968\n\tds_read_b64 a[34:35], v0 offset:3976\n\tv_mfma_f32_16x16x32_bf16 v[168:171], v[228:231], v[72:75], 0\n\tds_read_b64 a[36:37], v0 offset:3456\n\tds_read_b64 a[38:39], v0 offset:3464\n\tv_mfma_f32_16x16x32_bf16 v[172:175], v[228:231], v[76:79], 0\n\tds_read_b64 a[40:41], v0 offset:2944\n\t" \
    "ds_read_b64 a[42:43], v0 offset:2952\n\tv_mfma_f32_16x16x32_bf16 v[176:179], v[228:231], v[80:83], 0\n\tds_read_b64 a[44:45], v0 offset:2432\n\tds_read_b64 a[46:47], v0 offset:2440\n\tv_mfma_f32_16x16x32_bf16 v[180:183], v[228:231], v[84:87], 0\n\tds_read_b64 a[48:49], v0 offset:1920\n\tds_read_b64 a[50:51], v0 offset:1928\n\tv_mfma_f32_16x16x32_bf16 v[184:187], v[228:231], v[88:91], 0\n\t" \
    "ds_read_b64 a[52:53], v0 offset:1408\n\tv_cvt_pk_bf16_f32 v132, v132, v133\n\tv_mfma_f32_16x16x32_bf16 v[188:191], v[228:231], v[92:95], 0\n\tds_read_b64 a[54:55], v0 offset:1416\n\tv_cvt_pk_bf16_f32 v133, v134, v135\n\tv_mfma_f32_16x16x32_bf16 v[192:195], v[228:231], v[96:99], 0\n\tds_read_b64 a[56:57], v0 offset:896\n\tds_write_b64 v3, v[132:133] offset:0\n\t" \
    "v_mfma_f32_16x16x32_bf16 v[164:167], a[0:3], v[68:71], v[164:167]\n\tds_read_b64 a[58:59], v0 offset:904\n\tv_cvt_pk_bf16_f32 v136, v136, v137\n\tv_mfma_f32_16x16x32_bf16 v[168:171], a[0:3], v[72:75], v[168:171]\n\tds_read_b64 a[60:61], v0 offset:384\n\tv_cvt_pk_bf16_f32 v137, v138, v139\n\tv_mfma_f32_16x16x32_bf16 v[172:175], a[0:3], v[76:79], v[172:175]\n\tds_read_b64 a[62:63], v0 offset:392\n\t" \
    "ds_write_b64 v3, v[136:137] offset:512\n\tv_mfma_f32_16x16x32_bf16 v[176:179], a[0:3], v[80:83], v[176:179]\n\tds_read_b128 v[100:103], v1 offset:18880\n\tv_cvt_pk_bf16_f32 v140, v140, v141\n\tv_mfma_f32_16x16x32_bf16 v[180:183], a[0:3], v[84:87], v[180:183]\n\tds_read_b128 v[104:107], v1 offset:19392\n\tv_cvt_pk_bf16_f32 v141, v142, v143\n\tv_mfma_f32_16x16x32_bf16 v[184:187], a[0:3], v[88:91], v[184:187]\n\t" \
    "ds_read_b128 v[108:111], v1 offset:19904\n\tds_write_b64 v3, v[140:141] offset:1024\n\tv_mfma_f32_16x16x32_bf16 v[188:191], a[0:3], v[92:95], v[188:191]\n\tds_read_b128 v[112:115], v1 offset:20416\n\tv_cvt_pk_bf16_f32 v144, v144, v145\n\tv_mfma_f32_16x16x32_bf16 v[192:195], a[0:3], v[96:99], v[192:195]\n\tds_read_b128 v[116:119], v1 offset:20928\n\tv_cvt_pk_bf16_f32 v145, v146, v147\n\t" \
    "v_mfma_f32_16x16x32_bf16 v[168:171], a[4:7], v[68:71], v[168:171]\n\tds_read_b128 v[120:123], v1 offset:21440\n\tds_write_b64 v3, v[144:145] offset:1536\n\tv_mfma_f32_16x16x32_bf16 v[172:175], a[4:7], v[72:75], v[172:175]\n\tds_read_b128 v[124:127], v1 offset:21952\n\tv_cvt_pk_bf16_f32 v148, v148, v149\n\tv_mfma_f32_16x16x32_bf16 v[176:179], a[4:7], v[76:79], v[176:179]\n\tds_read_b128 v[128:131], v1 offset:22464\n\t" \
    "v_cvt_pk_bf16_f32 v149, v150, v151\n\tv_mfma_f32_16x16x32_bf16 v[180:183], a[4:7], v[80:83], v[180:183]\n\tds_read_b64 a[64:65], v0 offset:3904\n\tds_write_b64 v3, v[148:149] offset:2048\n\tv_mfma_f32_16x16x32_bf16 v[184:187], a[4:7], v[84:87], v[184:187]\n\tds_read_b64 a[66:67], v0 offset:3912\n\tv_cvt_pk_bf16_f32 v152, v152, v153\n\tv_mfma_f32_16x16x32_bf16 v[188:191], a[4:7], v[88:91], v[188:191]\n\t" \
    "ds_read_b64 a[68:69], v0 offset:3392\n\tv_cvt_pk_bf16_f32 v153, v154, v155\n\tv_mfma_f32_16x16x32_bf16 v[192:195], a[4:7], v[92:95], v[192:195]\n\tbuffer_load_dword v15, v6, s[12:15], s41 offen\n\tds_read_b64 a[70:71], v0 offset:3400\n\tv_mfma_f32_16x16x32_bf16 v[172:175], a[8:11], v[68:71], v[172:175]\n\tbuffer_load_dword v15, v7, s[16:19], s42 offen\n\tds_read_b64 a[72:73], v0 offset:2880\n\t" \
    "v_mfma_f32_16x16x32_bf16 v[176:179], a[8:11], v[72:75], v[176:179]\n\ts_add_u32 s43, s43, 1\n\tds_read_b64 a[74:75], v0 offset:2888\n\tds_write_b64 v3, v[152:153] offset:2560\n\tv_mfma_f32_16x16x32_bf16 v[180:183], a[8:11], v[76:79], v[180:183]\n\tv_readlane_b32 s41, v236, s43\n\tv_readlane_b32 s42, v237, s43\n\ts_nop 4\n\t" \
    "ds_read_b64 a[76:77], v0 offset:2368\n\tv_cvt_pk_bf16_f32 v156, v156, v157\n\tv_mfma_f32_16x16x32_bf16 v[184:187], a[8:11], v[80:83], v[184:187]\n\tds_read_b64 a[78:79], v0 offset:2376\n\tv_cvt_pk_bf16_f32 v157, v158, v159\n\tv_mfma_f32_16x16x32_bf16 v[188:191], a[8:11], v[84:87], v[188:191]\n\tds_read_b64 a[80:81], v0 offset:1856\n\tds_write_b64 v3, v[156:157] offset:3072\n\t" \
    "v_mfma_f32_16x16x32_bf16 v[192:195], a[8:11], v[88:91], v[192:195]\n\tds_read_b64 a[82:83], v0 offset:1864\n\tv_cvt_pk_bf16_f32 v160, v160, v161\n\tv_mfma_f32_16x16x32_bf16 v[176:179], a[12:15], v[68:71], v[176:179]\n\tds_read_b64 a[84:85], v0 offset:1344\n\tv_cvt_pk_bf16_f32 v161, v162, v163\n\tv_mfma_f32_16x16x32_bf16 v[180:183], a[12:15], v[72:75], v[180:183]\n\tds_read_b64 a[86:87], v0 offset:1352\n\t" \
    "ds_write_b64 v3, v[160:161] offset:3584\n\tv_mfma_f32_16x16x32_bf16 v[184:187], a[12:15], v[76:79], v[184:187]\n\tds_read_b64 a[88:89], v0 offset:832\n\tv_mfma_f32_16x16x32_bf16 v[188:191], a[12:15], v[80:83], v[188:191]\n\tds_read_b64 a[90:91], v0 offset:840\n\tv_mfma_f32_16x16x32_bf16 v[192:195], a[12:15], v[84:87], v[192:195]\n\tds_read_b64 a[92:93], v0 offset:320\n\tv_mfma_f32_16x16x32_bf16 v[180:183], a[16:19], v[68:71], v[180:183]\n\t" \
    "ds_read_b64 a[94:95], v0 offset:328\n\tv_mfma_f32_16x16x32_bf16 v[184:187], a[16:19], v[72:75], v[184:187]\n\tds_read_b64 a[96:97], v0 offset:3840\n\tv_mfma_f32_16x16x32_bf16 v[188:191], a[16:19], v[76:79], v[188:191]\n\tds_read_b64 a[98:99], v0 offset:3848\n\tv_mfma_f32_16x16x32_bf16 v[192:195], a[16:19], v[80:83], v[192:195]\n\tds_read_b64 a[100:101], v0 offset:3328\n\tv_mfma_f32_16x16x32_bf16 v[184:187], a[20:23], v[68:71], v[184:187]\n\t" \
    "ds_read_b64 a[102:103], v0 offset:3336\n\tv_mfma_f32_16x16x32_bf16 v[188:191], a[20:23], v[72:75], v[188:191]\n\tv_mfma_f32_16x16x32_bf16 v[192:195], a[20:23], v[76:79], v[192:195]\n\tv_mfma_f32_16x16x32_bf16 v[188:191], a[24:27], v[68:71], v[188:191]\n\tv_mfma_f32_16x16x32_bf16 v[192:195], a[24:27], v[72:75], v[192:195]\n\tv_mfma_f32_16x16x32_bf16 v[192:195], a[28:31], v[68:71], v[192:195]\n\ts_waitcnt lgkmcnt(8)\n\ts_barrier\n\t" \
    "v_mfma_f32_16x16x32_bf16 v[164:167], a[32:35], v[100:103], v[164:167]\n\tds_read_b64 a[104:105], v0 offset:2816\n\tds_read_b64 a[106:107], v0 offset:2824\n\tv_mfma_f32_16x16x32_bf16 v[168:171], a[32:35], v[104:107], v[168:171]\n\tds_read_b64 a[108:109], v0 offset:2304\n\tds_read_b64 a[110:111], v0 offset:2312\n\tv_mfma_f32_16x16x32_bf16 v[172:175], a[32:35], v[108:111], v[172:175]\n\tds_read_b64 a[112:113], v0 offset:1792\n\t" \
    "ds_read_b64 a[114:115], v0 offset:1800\n\tv_mfma_f32_16x16x32_bf16 v[176:179], a[32:35], v[112:115], v[176:179]\n\tds_read_b64 a[116:117], v0 offset:1280\n\tds_read_b64 a[118:119], v0 offset:1288\n\tv_mfma_f32_16x16x32_bf16 v[180:183], a[32:35], v[116:119], v[180:183]\n\tds_read_b64 a[120:121], v0 offset:768\n\tds_read_b64 a[122:123], v0 offset:776\n\tv_mfma_f32_16x16x32_bf16 v[184:187], a[32:35], v[120:123], v[184:187]\n\t" \
    "ds_read_b64 a[124:125], v0 offset:256\n\ts_add_u32 s51, s44, 0\n\tv_mfma_f32_16x16x32_bf16 v[188:191], a[32:35], v[124:127], v[188:191]\n\tds_read_b64 a[126:127], v0 offset:264\n\ts_mul_i32 s65, s51, 1366\n\tv_mfma_f32_16x16x32_bf16 v[192:195], a[32:35], v[128:131], v[192:195]\n\tds_read_b64 a[128:129], v0 offset:3776\n\ts_lshr_b32 s65, s65, 16\n\t" \
    "v_mfma_f32_16x16x32_bf16 v[168:171], a[36:39], v[100:103], v[168:171]\n\tds_read_b64 a[130:131], v0 offset:3784\n\ts_mul_i32 s56, s65, 48\n\tv_mfma_f32_16x16x32_bf16 v[172:175], a[36:39], v[104:107], v[172:175]\n\tds_read_b64 a[132:133], v0 offset:3264\n\ts_sub_u32 s56, s51, s56\n\tv_mfma_f32_16x16x32_bf16 v[176:179], a[36:39], v[108:111], v[176:179]\n\tds_read_b64 a[134:135], v0 offset:3272\n\t" \
    "s_lshl_b32 s56, s56, 6\n\tv_mfma_f32_16x16x32_bf16 v[180:183], a[36:39], v[112:115], v[180:183]\n\tds_read_b128 v[68:71], v1 offset:18816\n\ts_lshr_b32 s70, s65, 2\n\tv_mfma_f32_16x16x32_bf16 v[184:187], a[36:39], v[116:119], v[184:187]\n\tds_read_b128 v[72:75], v1 offset:19328\n\ts_mul_i32 s70, s70, 252\n\tv_mfma_f32_16x16x32_bf16 v[188:191], a[36:39], v[120:123], v[188:191]\n\t" \
    "ds_read_b128 v[76:79], v1 offset:19840\n\ts_add_u32 s65, s65, s70\n\tv_mfma_f32_16x16x32_bf16 v[192:195], a[36:39], v[124:127], v[192:195]\n\tds_read_b128 v[80:83], v1 offset:20352\n\ts_mul_i32 s65, s65, 3072\n\tv_mfma_f32_16x16x32_bf16 v[172:175], a[40:43], v[100:103], v[172:175]\n\tds_read_b128 v[84:87], v1 offset:20864\n\ts_add_u32 s56, s56, s65\n\t" \
    "v_mfma_f32_16x16x32_bf16 v[176:179], a[40:43], v[104:107], v[176:179]\n\tds_read_b128 v[88:91], v1 offset:21376\n\ts_add_u32 s56, s56, 4849664\n\tv_mfma_f32_16x16x32_bf16 v[180:183], a[40:43], v[108:111], v[180:183]\n\tds_read_b128 v[92:95], v1 offset:21888\n\ts_add_u32 s51, s51, s38\n\tv_mfma_f32_16x16x32_bf16 v[184:187], a[40:43], v[112:115], v[184:187]\n\tds_read_b128 v[96:99], v1 offset:22400\n\t" \
    "s_lshl_b32 s51, s51, 3\n\tv_mfma_f32_16x16x32_bf16 v[188:191], a[40:43], v[116:119], v[188:191]\n\tds_read_b64 a[136:137], v0 offset:2752\n\ts_add_u32 s51, s51, 50176\n\tds_read_b64_tr_b16 v[204:205], v4 offset:0\n\tv_mfma_f32_16x16x32_bf16 v[192:195], a[40:43], v[120:123], v[192:195]\n\tds_read_b64 a[138:139], v0 offset:2760\n\tds_read_b64_tr_b16 v[206:207], v4 offset:8\n\t" \
    "v_mfma_f32_16x16x32_bf16 v[176:179], a[44:47], v[100:103], v[176:179]\n\tds_read_b64 a[140:141], v0 offset:2240\n\tds_read_b64_tr_b16 v[208:209], v4 offset:64\n\tv_mfma_f32_16x16x32_bf16 v[180:183], a[44:47], v[104:107], v[180:183]\n\tds_read_b64 a[142:143], v0 offset:2248\n\tds_read_b64_tr_b16 v[210:211], v4 offset:72\n\tv_mfma_f32_16x16x32_bf16 v[184:187], a[44:47], v[108:111], v[184:187]\n\tds_read_b64 a[144:145], v0 offset:1728\n\t" \
    "ds_read_b64_tr_b16 v[212:213], v4 offset:512\n\tv_mfma_f32_16x16x32_bf16 v[188:191], a[44:47], v[112:115], v[188:191]\n\tds_read_b64 a[146:147], v0 offset:1736\n\tds_read_b64_tr_b16 v[214:215], v4 offset:520\n\tv_mfma_f32_16x16x32_bf16 v[192:195], a[44:47], v[116:119], v[192:195]\n\tds_read_b64 a[148:149], v0 offset:1216\n\tds_read_b64_tr_b16 v[216:217], v4 offset:576\n\tv_mfma_f32_16x16x32_bf16 v[180:183], a[48:51], v[100:103], v[180:183]\n\t" \
    "ds_read_b64 a[150:151], v0 offset:1224\n\tds_read_b64_tr_b16 v[218:219], v4 offset:584\n\tv_mfma_f32_16x16x32_bf16 v[184:187], a[48:51], v[104:107], v[184:187]\n\tds_read_b64 a[152:153], v0 offset:704\n\tv_mfma_f32_16x16x32_bf16 v[188:191], a[48:51], v[108:111], v[188:191]\n\tds_read_b64 a[154:155], v0 offset:712\n\tv_mfma_f32_16x16x32_bf16 v[192:195], a[48:51], v[112:115], v[192:195]\n\tds_read_b64 a[156:157], v0 offset:192\n\t" \
    "v_mfma_f32_16x16x32_bf16 v[184:187], a[52:55], v[100:103], v[184:187]\n\tds_read_b64 a[158:159], v0 offset:200\n\tv_mfma_f32_16x16x32_bf16 v[188:191], a[52:55], v[104:107], v[188:191]\n\tv_mfma_f32_16x16x32_bf16 v[192:195], a[52:55], v[108:111], v[192:195]\n\ts_waitcnt lgkmcnt(15)\n\tbuffer_store_dwordx4 v[204:207], v5, s[8:11], s56 offen sc1\n\ts_add_u32 s56, s56, 49152\n\tv_mfma_f32_16x16x32_bf16 v[188:191], a[56:59], v[100:103], v[188:191]\n\t" \
    "v_mfma_f32_16x16x32_bf16 v[192:195], a[56:59], v[104:107], v[192:195]\n\tv_mfma_f32_16x16x32_bf16 v[192:195], a[60:63], v[100:103], v[192:195]\n\tv_mfma_f32_16x16x32_bf16 v[164:167], a[64:67], v[68:71], v[164:167]\n\tds_read_b64 a[160:161], v0 offset:3712\n\tds_read_b64 a[162:163], v0 offset:3720\n\tv_mfma_f32_16x16x32_bf16 v[168:171], a[64:67], v[72:75], v[168:171]\n\tds_read_b64 a[164:165], v0 offset:3200\n\tds_read_b64 a[166:167], v0 offset:3208\n\t" \
    "v_mfma_f32_16x16x32_bf16 v[172:175], a[64:67], v[76:79], v[172:175]\n\tds_read_b64 a[168:169], v0 offset:2688\n\tds_read_b64 a[170:171], v0 offset:2696\n\tv_mfma_f32_16x16x32_bf16 v[176:179], a[64:67], v[80:83], v[176:179]\n\tds_read_b64 a[172:173], v0 offset:2176\n\tds_read_b64 a[174:175], v0 offset:2184\n\tv_mfma_f32_16x16x32_bf16 v[180:183], a[64:67], v[84:87], v[180:183]\n\tds_read_b64 a[176:177], v0 offset:1664\n\t" \
    "ds_read_b64 a[178:179], v0 offset:1672\n\tv_mfma_f32_16x16x32_bf16 v[184:187], a[64:67], v[88:91], v[184:187]\n\tds_read_b64 a[180:181], v0 offset:1152\n\tv_mfma_f32_16x16x32_bf16 v[188:191], a[64:67], v[92:95], v[188:191]\n\tds_read_b64 a[182:183], v0 offset:1160\n\tv_mfma_f32_16x16x32_bf16 v[192:195], a[64:67], v[96:99], v[192:195]\n\tds_read_b64 a[184:185], v0 offset:640\n\tv_mfma_f32_16x16x32_bf16 v[168:171], a[68:71], v[68:71], v[168:171]\n\t" \
    "ds_read_b64 a[186:187], v0 offset:648\n\tv_mfma_f32_16x16x32_bf16 v[172:175], a[68:71], v[72:75], v[172:175]\n\tds_read_b64 a[188:189], v0 offset:128\n\tv_mfma_f32_16x16x32_bf16 v[176:179], a[68:71], v[76:79], v[176:179]\n\tds_read_b64 a[190:191], v0 offset:136\n\tv_mfma_f32_16x16x32_bf16 v[180:183], a[68:71], v[80:83], v[180:183]\n\tds_read_b128 v[100:103], v1 offset:18752\n\tv_mfma_f32_16x16x32_bf16 v[184:187], a[68:71], v[84:87], v[184:187]\n\t" \
    "ds_read_b128 v[104:107], v1 offset:19264\n\tv_mfma_f32_16x16x32_bf16 v[188:191], a[68:71], v[88:91], v[188:191]\n\tds_read_b128 v[108:111], v1 offset:19776\n\tv_mfma_f32_16x16x32_bf16 v[192:195], a[68:71], v[92:95], v[192:195]\n\tds_read_b128 v[112:115], v1 offset:20288\n\ts_waitcnt lgkmcnt(15)\n\tbuffer_store_dwordx4 v[208:211], v5, s[8:11], s56 offen sc1\n\tv_mfma_f32_16x16x32_bf16 v[172:175], a[72:75], v[68:71], v[172:175]\n\t" \
    "ds_read_b128 v[116:119], v1 offset:20800\n\ts_add_u32 s56, s56, 49152\n\tv_mfma_f32_16x16x32_bf16 v[176:179], a[72:75], v[72:75], v[176:179]\n\ts_lshl_b32 s69, s67, 13\n\tbuffer_load_dword v15, v14, s[4:7], s69 offen\n\tds_read_b128 v[120:123], v1 offset:21312\n\tv_mfma_f32_16x16x32_bf16 v[180:183], a[72:75], v[76:79], v[180:183]\n\tds_read_b128 v[124:127], v1 offset:21824\n\t" \
    "v_mfma_f32_16x16x32_bf16 v[184:187], a[72:75], v[80:83], v[184:187]\n\tds_read_b128 v[128:131], v1 offset:22336\n\tv_mfma_f32_16x16x32_bf16 v[188:191], a[72:75], v[84:87], v[188:191]\n\tds_read_b64 a[192:193], v0 offset:3648\n\tv_mfma_f32_16x16x32_bf16 v[192:195], a[72:75], v[88:91], v[192:195]\n\tds_read_b64 a[194:195], v0 offset:3656\n\tv_mfma_f32_16x16x32_bf16 v[176:179], a[76:79], v[68:71], v[176:179]\n\tds_read_b64 a[196:197], v0 offset:3136\n\t" \
    "v_mfma_f32_16x16x32_bf16 v[180:183], a[76:79], v[72:75], v[180:183]\n\tds_read_b64 a[198:199], v0 offset:3144\n\tv_mfma_f32_16x16x32_bf16 v[184:187], a[76:79], v[76:79], v[184:187]\n\tds_read_b64 a[200:201], v0 offset:2624\n\tv_mfma_f32_16x16x32_bf16 v[188:191], a[76:79], v[80:83], v[188:191]\n\tds_read_b64 a[202:203], v0 offset:2632\n\tv_mfma_f32_16x16x32_bf16 v[192:195], a[76:79], v[84:87], v[192:195]\n\tds_read_b64 a[204:205], v0 offset:2112\n\t" \
    "v_mfma_f32_16x16x32_bf16 v[180:183], a[80:83], v[68:71], v[180:183]\n\tds_read_b64 a[206:207], v0 offset:2120\n\tv_mfma_f32_16x16x32_bf16 v[184:187], a[80:83], v[72:75], v[184:187]\n\tds_read_b64 a[208:209], v0 offset:1600\n\tv_mfma_f32_16x16x32_bf16 v[188:191], a[80:83], v[76:79], v[188:191]\n\tds_read_b64 a[210:211], v0 offset:1608\n\tv_mfma_f32_16x16x32_bf16 v[192:195], a[80:83], v[80:83], v[192:195]\n\tds_read_b64 a[212:213], v0 offset:1088\n\t" \
    "v_mfma_f32_16x16x32_bf16 v[184:187], a[84:87], v[68:71], v[184:187]\n\tds_read_b64 a[214:215], v0 offset:1096\n\tv_mfma_f32_16x16x32_bf16 v[188:191], a[84:87], v[72:75], v[188:191]\n\tbuffer_store_dwordx4 v[212:215], v5, s[8:11], s56 offen sc1\n\tv_mfma_f32_16x16x32_bf16 v[192:195], a[84:87], v[76:79], v[192:195]\n\ts_add_u32 s56, s56, 49152\n\tv_mfma_f32_16x16x32_bf16 v[188:191], a[88:91], v[68:71], v[188:191]\n\tv_mfma_f32_16x16x32_bf16 v[192:195], a[88:91], v[72:75], v[192:195]\n\t" \
    "v_mfma_f32_16x16x32_bf16 v[192:195], a[92:95], v[68:71], v[192:195]\n\ts_waitcnt lgkmcnt(12)\n\tv_mfma_f32_16x16x32_bf16 v[164:167], a[96:99], v[100:103], v[164:167]\n\tds_read_b64 a[216:217], v0 offset:576\n\tv_mfma_f32_16x16x32_bf16 v[168:171], a[96:99], v[104:107], v[168:171]\n\tds_read_b64 a[218:219], v0 offset:584\n\tv_mfma_f32_16x16x32_bf16 v[172:175], a[96:99], v[108:111], v[172:175]\n\tds_read_b64 a[220:221], v0 offset:64\n\t" \
    "v_mfma_f32_16x16x32_bf16 v[176:179], a[96:99], v[112:115], v[176:179]\n\tds_read_b64 a[222:223], v0 offset:72\n\tv_mfma_f32_16x16x32_bf16 v[180:183], a[96:99], v[116:119], v[180:183]\n\tds_read_b64 a[224:225], v0 offset:3584\n\tv_mfma_f32_16x16x32_bf16 v[184:187], a[96:99], v[120:123], v[184:187]\n\tds_read_b64 a[226:227], v0 offset:3592\n\tv_mfma_f32_16x16x32_bf16 v[188:191], a[96:99], v[124:127], v[188:191]\n\tds_read_b64 a[228:229], v0 offset:3072\n\t" \
    "v_mfma_f32_16x16x32_bf16 v[192:195], a[96:99], v[128:131], v[192:195]\n\tds_read_b64 a[230:231], v0 offset:3080\n\tv_mfma_f32_16x16x32_bf16 v[168:171], a[100:103], v[100:103], v[168:171]\n\tds_read_b64 a[232:233], v0 offset:2560\n\tbuffer_store_dwordx4 v[216:219], v5, s[8:11], s56 offen sc1\n\tv_mfma_f32_16x16x32_bf16 v[172:175], a[100:103], v[104:107], v[172:175]\n\tds_read_b64 a[234:235], v0 offset:2568\n\ts_add_u32 s56, s56, 49152\n\t" \
    "v_mfma_f32_16x16x32_bf16 v[176:179], a[100:103], v[108:111], v[176:179]\n\tds_read_b64 a[236:237], v0 offset:2048\n\tv_mfma_f32_16x16x32_bf16 v[180:183], a[100:103], v[112:115], v[180:183]\n\tds_read_b64 a[238:239], v0 offset:2056\n\tv_mfma_f32_16x16x32_bf16 v[184:187], a[100:103], v[116:119], v[184:187]\n\tds_read_b64 a[240:241], v0 offset:1536\n\tv_mfma_f32_16x16x32_bf16 v[188:191], a[100:103], v[120:123], v[188:191]\n\tds_read_b64 a[242:243], v0 offset:1544\n\t" \
    "v_mfma_f32_16x16x32_bf16 v[192:195], a[100:103], v[124:127], v[192:195]\n\tds_read_b64 a[244:245], v0 offset:1024\n\tv_mfma_f32_16x16x32_bf16 v[172:175], a[104:107], v[100:103], v[172:175]\n\tds_read_b64 a[246:247], v0 offset:1032\n\tv_mfma_f32_16x16x32_bf16 v[176:179], a[104:107], v[104:107], v[176:179]\n\tds_read_b128 v[68:71], v1 offset:18688\n\tv_mfma_f32_16x16x32_bf16 v[180:183], a[104:107], v[108:111], v[180:183]\n\tds_read_b128 v[72:75], v1 offset:19200\n\t" \
    "v_mfma_f32_16x16x32_bf16 v[184:187], a[104:107], v[112:115], v[184:187]\n\tds_read_b128 v[76:79], v1 offset:19712\n\tv_mfma_f32_16x16x32_bf16 v[188:191], a[104:107], v[116:119], v[188:191]\n\tds_read_b128 v[80:83], v1 offset:20224\n\tv_mfma_f32_16x16x32_bf16 v[192:195], a[104:107], v[120:123], v[192:195]\n\tds_read_b128 v[84:87], v1 offset:20736\n\tv_mfma_f32_16x16x32_bf16 v[176:179], a[108:111], v[100:103], v[176:179]\n\tds_read_b128 v[88:91], v1 offset:21248\n\t" \
    "v_mfma_f32_16x16x32_bf16 v[180:183], a[108:111], v[104:107], v[180:183]\n\tds_read_b128 v[92:95], v1 offset:21760\n\tv_mfma_f32_16x16x32_bf16 v[184:187], a[108:111], v[108:111], v[184:187]\n\tds_read_b128 v[96:99], v1 offset:22272\n\tv_mfma_f32_16x16x32_bf16 v[188:191], a[108:111], v[112:115], v[188:191]\n\tds_read_b64 a[248:249], v0 offset:512\n\tv_mfma_f32_16x16x32_bf16 v[192:195], a[108:111], v[116:119], v[192:195]\n\tds_read_b64 a[250:251], v0 offset:520\n\t" \
    "v_mfma_f32_16x16x32_bf16 v[180:183], a[112:115], v[100:103], v[180:183]\n\tds_read_b64 a[252:253], v0 offset:0\n\tv_mfma_f32_16x16x32_bf16 v[184:187], a[112:115], v[104:107], v[184:187]\n\tds_read_b64 a[254:255], v0 offset:8\n\tv_mfma_f32_16x16x32_bf16 v[188:191], a[112:115], v[108:111], v[188:191]\n\tv_mfma_f32_16x16x32_bf16 v[192:195], a[112:115], v[112:115], v[192:195]\n\tv_mfma_f32_16x16x32_bf16 v[184:187], a[116:119], v[100:103], v[184:187]\n\tv_mfma_f32_16x16x32_bf16 v[188:191], a[116:119], v[104:107], v[188:191]\n\t" \
    "v_mfma_f32_16x16x32_bf16 v[192:195], a[116:119], v[108:111], v[192:195]\n\tv_mfma_f32_16x16x32_bf16 v[188:191], a[120:123], v[100:103], v[188:191]\n\tv_mfma_f32_16x16x32_bf16 v[192:195], a[120:123], v[104:107], v[192:195]\n\tv_mfma_f32_16x16x32_bf16 v[192:195], a[124:127], v[100:103], v[192:195]\n\ts_waitcnt lgkmcnt(4)\n\tv_mfma_f32_16x16x32_bf16 v[164:167], a[128:131], v[68:71], v[164:167]\n\tds_read_b128 v[100:103], v1 offset:18624\n\ts_waitcnt vmcnt(9)\n\t" \
    "v_alignbit_b32 v196, v19, v19, 16\n\tv_mfma_f32_16x16x32_bf16 v[168:171], a[128:131], v[72:75], v[168:171]\n\tds_read_b128 v[104:107], v1 offset:19136\n\tv_alignbit_b32 v197, v18, v18, 16\n\tv_mfma_f32_16x16x32_bf16 v[172:175], a[128:131], v[76:79], v[172:175]\n\tds_read_b128 v[108:111], v1 offset:19648\n\tv_alignbit_b32 v198, v17, v17, 16\n\tv_mfma_f32_16x16x32_bf16 v[176:179], a[128:131], v[80:83], v[176:179]\n\t" \
    "ds_read_b128 v[112:115], v1 offset:20160\n\tv_alignbit_b32 v199, v16, v16, 16\n\tv_mfma_f32_16x16x32_bf16 v[180:183], a[128:131], v[84:87], v[180:183]\n\tds_write_b128 v2, v[196:199] offset:3072\n\tv_mfma_f32_16x16x32_bf16 v[184:187], a[128:131], v[88:91], v[184:187]\n\tds_read_b128 v[116:119], v1 offset:20672\n\tv_bfi_b32 v200, s46, v19, v18\n\tv_mfma_f32_16x16x32_bf16 v[188:191], a[128:131], v[92:95], v[188:191]\n\t" \
    "ds_read_b128 v[120:123], v1 offset:21184\n\tv_bfi_b32 v201, s46, v18, v17\n\tv_mfma_f32_16x16x32_bf16 v[192:195], a[128:131], v[96:99], v[192:195]\n\tds_read_b128 v[124:127], v1 offset:21696\n\tv_bfi_b32 v202, s46, v17, v16\n\tv_mfma_f32_16x16x32_bf16 v[168:171], a[132:135], v[68:71], v[168:171]\n\tds_read_b128 v[128:131], v1 offset:22208\n\tv_bfi_b32 v203, s46, v16, v33\n\t" \
    "v_mfma_f32_16x16x32_bf16 v[172:175], a[132:135], v[72:75], v[172:175]\n\tds_write_b128 v2, v[200:203] offset:7232\n\tv_mfma_f32_16x16x32_bf16 v[176:179], a[132:135], v[76:79], v[176:179]\n\tv_alignbit_b32 v196, v18, v18, 16\n\tv_alignbit_b32 v197, v17, v17, 16\n\tv_mfma_f32_16x16x32_bf16 v[180:183], a[132:135], v[80:83], v[180:183]\n\tv_alignbit_b32 v198, v16, v16, 16\n\tv_alignbit_b32 v199, v33, v33, 16\n\t" \
    "v_mfma_f32_16x16x32_bf16 v[184:187], a[132:135], v[84:87], v[184:187]\n\tds_write_b128 v2, v[196:199] offset:11392\n\tv_mfma_f32_16x16x32_bf16 v[188:191], a[132:135], v[88:91], v[188:191]\n\tv_bfi_b32 v200, s46, v18, v17\n\tv_bfi_b32 v201, s46, v17, v16\n\tv_mfma_f32_16x16x32_bf16 v[192:195], a[132:135], v[92:95], v[192:195]\n\tv_bfi_b32 v202, s46, v16, v33\n\tv_bfi_b32 v203, s46, v33, v32\n\t" \
    "v_mfma_f32_16x16x32_bf16 v[172:175], a[136:139], v[68:71], v[172:175]\n\tds_write_b128 v2, v[200:203] offset:15552\n\tv_mfma_f32_16x16x32_bf16 v[176:179], a[136:139], v[72:75], v[176:179]\n\tv_alignbit_b32 v196, v23, v23, 16\n\tv_alignbit_b32 v197, v22, v22, 16\n\tv_mfma_f32_16x16x32_bf16 v[180:183], a[136:139], v[76:79], v[180:183]\n\tv_alignbit_b32 v198, v21, v21, 16\n\tv_alignbit_b32 v199, v20, v20, 16\n\t" \
    "v_mfma_f32_16x16x32_bf16 v[184:187], a[136:139], v[80:83], v[184:187]\n\tbuffer_load_dword v15, v6, s[12:15], s41 offen\n\tv_mfma_f32_16x16x32_bf16 v[188:191], a[136:139], v[84:87], v[188:191]\n\tbuffer_load_dword v15, v7, s[16:19], s42 offen\n\tv_mfma_f32_16x16x32_bf16 v[192:195], a[136:139], v[88:91], v[192:195]\n\ts_add_u32 s43, s43, 1\n\tds_write_b128 v2, v[196:199] offset:2048\n\tv_mfma_f32_16x16x32_bf16 v[176:179], a[140:143], v[68:71], v[176:179]\n\t" \
    "v_readlane_b32 s41, v236, s43\n\tv_readlane_b32 s42, v237, s43\n\ts_nop 4\n\tv_bfi_b32 v200, s46, v23, v22\n\tv_bfi_b32 v201, s46, v22, v21\n\tv_mfma_f32_16x16x32_bf16 v[180:183], a[140:143], v[72:75], v[180:183]\n\tv_bfi_b32 v202, s46, v21, v20\n\tv_bfi_b32 v203, s46, v20, v35\n\t" \
    "v_mfma_f32_16x16x32_bf16 v[184:187], a[140:143], v[76:79], v[184:187]\n\tds_write_b128 v2, v[200:203] offset:6208\n\tv_mfma_f32_16x16x32_bf16 v[188:191], a[140:143], v[80:83], v[188:191]\n\tv_alignbit_b32 v196, v22, v22, 16\n\tv_alignbit_b32 v197, v21, v21, 16\n\tv_mfma_f32_16x16x32_bf16 v[192:195], a[140:143], v[84:87], v[192:195]\n\tv_alignbit_b32 v198, v20, v20, 16\n\tv_alignbit_b32 v199, v35, v35, 16\n\t" \
    "v_mfma_f32_16x16x32_bf16 v[180:183], a[144:147], v[68:71], v[180:183]\n\tds_write_b128 v2, v[196:199] offset:10368\n\tv_mfma_f32_16x16x32_bf16 v[184:187], a[144:147], v[72:75], v[184:187]\n\tv_bfi_b32 v200, s46, v22, v21\n\tv_bfi_b32 v201, s46, v21, v20\n\tv_mfma_f32_16x16x32_bf16 v[188:191], a[144:147], v[76:79], v[188:191]\n\tv_bfi_b32 v202, s46, v20, v35\n\tv_bfi_b32 v203, s46, v35, v34\n\t" \
    "v_mfma_f32_16x16x32_bf16 v[192:195], a[144:147], v[80:83], v[192:195]\n\tds_write_b128 v2, v[200:203] offset:14528\n\tv_mfma_f32_16x16x32_bf16 v[184:187], a[148:151], v[68:71], v[184:187]\n\tv_alignbit_b32 v196, v27, v27, 16\n\tv_alignbit_b32 v197, v26, v26, 16\n\tv_mfma_f32_16x16x32_bf16 v[188:191], a[148:151], v[72:75], v[188:191]\n\tv_alignbit_b32 v198, v25, v25, 16\n\tv_alignbit_b32 v199, v24, v24, 16\n\t" \
    "v_mfma_f32_16x16x32_bf16 v[192:195], a[148:151], v[76:79], v[192:195]\n\tds_write_b128 v2, v[196:199] offset:1024\n\tv_mfma_f32_16x16x32_bf16 v[188:191], a[152:155], v[68:71], v[188:191]\n\tv_bfi_b32 v200, s46, v27, v26\n\tv_bfi_b32 v201, s46, v26, v25\n\tv_mfma_f32_16x16x32_bf16 v[192:195], a[152:155], v[72:75], v[192:195]\n\tv_bfi_b32 v202, s46, v25, v24\n\tv_bfi_b32 v203, s46, v24, v37\n\t" \
    "v_mfma_f32_16x16x32_bf16 v[192:195], a[156:159], v[68:71], v[192:195]\n\tds_write_b128 v2, v[200:203] offset:5184\n\ts_waitcnt lgkmcnt(9)\n\tv_mfma_f32_16x16x32_bf16 v[164:167], a[160:163], v[100:103], v[164:167]\n\tds_read_b128 v[68:71], v1 offset:18560\n\tv_alignbit_b32 v196, v26, v26, 16\n\tv_mfma_f32_16x16x32_bf16 v[168:171], a[160:163], v[104:107], v[168:171]\n\tds_read_b128 v[72:75], v1 offset:19072\n\t" \
    "v_alignbit_b32 v197, v25, v25, 16\n\tv_mfma_f32_16x16x32_bf16 v[172:175], a[160:163], v[108:111], v[172:175]\n\tds_read_b128 v[76:79], v1 offset:19584\n\tv_alignbit_b32 v198, v24, v24, 16\n\tv_mfma_f32_16x16x32_bf16 v[176:179], a[160:163], v[112:115], v[176:179]\n\tds_read_b128 v[80:83], v1 offset:20096\n\tv_alignbit_b32 v199, v37, v37, 16\n\tv_mfma_f32_16x16x32_bf16 v[180:183], a[160:163], v[116:119], v[180:183]\n\t" \
    "ds_write_b128 v2, v[196:199] offset:9344\n\tv_mfma_f32_16x16x32_bf16 v[184:187], a[160:163], v[120:123], v[184:187]\n\tds_read_b128 v[84:87], v1 offset:20608\n\tv_bfi_b32 v200, s46, v26, v25\n\tv_mfma_f32_16x16x32_bf16 v[188:191], a[160:163], v[124:127], v[188:191]\n\tds_read_b128 v[88:91], v1 offset:21120\n\tv_bfi_b32 v201, s46, v25, v24\n\tv_mfma_f32_16x16x32_bf16 v[192:195], a[160:163], v[128:131], v[192:195]\n\t" \
    "ds_read_b128 v[92:95], v1 offset:21632\n\tv_bfi_b32 v202, s46, v24, v37\n\tv_mfma_f32_16x16x32_bf16 v[168:171], a[164:167], v[100:103], v[168:171]\n\tds_read_b128 v[96:99], v1 offset:22144\n\tv_bfi_b32 v203, s46, v37, v36\n\tv_mfma_f32_16x16x32_bf16 v[172:175], a[164:167], v[104:107], v[172:175]\n\tds_write_b128 v2, v[200:203] offset:13504\n\tv_mfma_f32_16x16x32_bf16 v[176:179], a[164:167], v[108:111], v[176:179]\n\t" \
    "v_alignbit_b32 v196, v31, v31, 16\n\tv_alignbit_b32 v197, v30, v30, 16\n\tv_mfma_f32_16x16x32_bf16 v[180:183], a[164:167], v[112:115], v[180:183]\n\tv_alignbit_b32 v198, v29, v29, 16\n\tv_alignbit_b32 v199, v28, v28, 16\n\tv_mfma_f32_16x16x32_bf16 v[184:187], a[164:167], v[116:119], v[184:187]\n\tds_write_b128 v2, v[196:199] offset:0\n\tv_mfma_f32_16x16x32_bf16 v[188:191], a[164:167], v[120:123], v[188:191]\n\t" \
    "v_bfi_b32 v200, s46, v31, v30\n\tv_bfi_b32 v201, s46, v30, v29\n\tv_mfma_f32_16x16x32_bf16 v[192:195], a[164:167], v[124:127], v[192:195]\n\tv_bfi_b32 v202, s46, v29, v28\n\tv_bfi_b32 v203, s46, v28, v39\n\tv_mfma_f32_16x16x32_bf16 v[172:175], a[168:171], v[100:103], v[172:175]\n\tds_write_b128 v2, v[200:203] offset:4160\n\tv_mfma_f32_16x16x32_bf16 v[176:179], a[168:171], v[104:107], v[176:179]\n\t" \
    "v_alignbit_b32 v196, v30, v30, 16\n\tv_alignbit_b32 v197, v29, v29, 16\n\tv_mfma_f32_16x16x32_bf16 v[180:183], a[168:171], v[108:111], v[180:183]\n\tv_alignbit_b32 v198, v28, v28, 16\n\tv_alignbit_b32 v199, v39, v39, 16\n\tv_mfma_f32_16x16x32_bf16 v[184:187], a[168:171], v[112:115], v[184:187]\n\tds_write_b128 v2, v[196:199] offset:8320\n\tv_mfma_f32_16x16x32_bf16 v[188:191], a[168:171], v[116:119], v[188:191]\n\t" \
    "v_bfi_b32 v200, s46, v30, v29\n\tv_bfi_b32 v201, s46, v29, v28\n\tv_mfma_f32_16x16x32_bf16 v[192:195], a[168:171], v[120:123], v[192:195]\n\tv_bfi_b32 v202, s46, v28, v39\n\tv_bfi_b32 v203, s46, v39, v38\n\tv_mfma_f32_16x16x32_bf16 v[176:179], a[172:175], v[100:103], v[176:179]\n\tds_write_b128 v2, v[200:203] offset:12480\n\tv_mfma_f32_16x16x32_bf16 v[180:183], a[172:175], v[104:107], v[180:183]\n\t" \
    "v_lshl_or_b32 v40, v40, 16, v40\n\tv_and_b32 v228, v40, v232\n\tv_mfma_f32_16x16x32_bf16 v[184:187], a[172:175], v[108:111], v[184:187]\n\tv_and_b32 v229, v40, v233\n\tv_and_b32 v230, v40, v234\n\tv_mfma_f32_16x16x32_bf16 v[188:191], a[172:175], v[112:115], v[188:191]\n\tv_and_b32 v231, v40, v235\n\tv_mfma_f32_16x16x32_bf16 v[192:195], a[172:175], v[116:119], v[192:195]\n\t" \
    "v_mfma_f32_16x16x32_bf16 v[180:183], a[176:179], v[100:103], v[180:183]\n\tv_mfma_f32_16x16x32_bf16 v[184:187], a[176:179], v[104:107], v[184:187]\n\tv_mfma_f32_16x16x32_bf16 v[188:191], a[176:179], v[108:111], v[188:191]\n\tv_mfma_f32_16x16x32_bf16 v[192:195], a[176:179], v[112:115], v[192:195]\n\tv_mfma_f32_16x16x32_bf16 v[184:187], a[180:183], v[100:103], v[184:187]\n\tv_mfma_f32_16x16x32_bf16 v[188:191], a[180:183], v[104:107], v[188:191]\n\tv_mfma_f32_16x16x32_bf16 v[192:195], a[180:183], v[108:111], v[192:195]\n\tv_mfma_f32_16x16x32_bf16 v[188:191], a[184:187], v[100:103], v[188:191]\n\t" \
    "v_mfma_f32_16x16x32_bf16 v[192:195], a[184:187], v[104:107], v[192:195]\n\tv_mfma_f32_16x16x32_bf16 v[192:195], a[188:191], v[100:103], v[192:195]\n\ts_waitcnt lgkmcnt(5)\n\tv_mfma_f32_16x16x32_bf16 v[164:167], a[192:195], v[68:71], v[164:167]\n\ts_add_u32 s68, s44, s38\n\tds_read_b128 v[100:103], v1 offset:18496\n\tv_mfma_f32_16x16x32_bf16 v[168:171], a[192:195], v[72:75], v[168:171]\n\ts_add_u32 s68, s68, 12\n\t" \
    "ds_read_b128 v[104:107], v1 offset:19008\n\tv_mfma_f32_16x16x32_bf16 v[172:175], a[192:195], v[76:79], v[172:175]\n\ts_lshl_b32 s66, s68, 1\n\tds_read_b128 v[108:111], v1 offset:19520\n\tv_mfma_f32_16x16x32_bf16 v[176:179], a[192:195], v[80:83], v[176:179]\n\ts_lshl_b32 s68, s68, 12\n\tds_read_b128 v[112:115], v1 offset:20032\n\tv_mfma_f32_16x16x32_bf16 v[180:183], a[192:195], v[84:87], v[180:183]\n\t" \
    "s_or_b32 s68, s68, s45\n\tds_read_b128 v[116:119], v1 offset:20544\n\tv_mfma_f32_16x16x32_bf16 v[184:187], a[192:195], v[88:91], v[184:187]\n\ts_or_b32 s66, s66, s45\n\tbuffer_load_dwordx4 v[42:45], v8, s[76:79], s68 offen offset:0\n\tds_read_b128 v[120:123], v1 offset:21056\n\tv_mfma_f32_16x16x32_bf16 v[188:191], a[192:195], v[92:95], v[188:191]\n\tds_read_b128 v[124:127], v1 offset:21568\n\t" \
    "v_mfma_f32_16x16x32_bf16 v[192:195], a[192:195], v[96:99], v[192:195]\n\tds_read_b128 v[128:131], v1 offset:22080\n\tv_mfma_f32_16x16x32_bf16 v[168:171], a[196:199], v[68:71], v[168:171]\n\tv_mfma_f32_16x16x32_bf16 v[172:175], a[196:199], v[72:75], v[172:175]\n\tv_mfma_f32_16x16x32_bf16 v[176:179], a[196:199], v[76:79], v[176:179]\n\tv_mfma_f32_16x16x32_bf16 v[180:183], a[196:199], v[80:83], v[180:183]\n\tbuffer_load_dwordx4 v[46:49], v8, s[76:79], s68 offen offset:1024\n\tv_mfma_f32_16x16x32_bf16 v[184:187], a[196:199], v[84:87], v[184:187]\n\t" \
    "v_mfma_f32_16x16x32_bf16 v[188:191], a[196:199], v[88:91], v[188:191]\n\tv_mfma_f32_16x16x32_bf16 v[192:195], a[196:199], v[92:95], v[192:195]\n\tv_mfma_f32_16x16x32_bf16 v[172:175], a[200:203], v[68:71], v[172:175]\n\tv_mfma_f32_16x16x32_bf16 v[176:179], a[200:203], v[72:75], v[176:179]\n\tv_mfma_f32_16x16x32_bf16 v[180:183], a[200:203], v[76:79], v[180:183]\n\tbuffer_load_dwordx4 v[50:53], v8, s[76:79], s68 offen offset:2048\n\tv_mfma_f32_16x16x32_bf16 v[184:187], a[200:203], v[80:83], v[184:187]\n\tv_mfma_f32_16x16x32_bf16 v[188:191], a[200:203], v[84:87], v[188:191]\n\t" \
    "v_mfma_f32_16x16x32_bf16 v[192:195], a[200:203], v[88:91], v[192:195]\n\tv_mfma_f32_16x16x32_bf16 v[176:179], a[204:207], v[68:71], v[176:179]\n\tv_mfma_f32_16x16x32_bf16 v[180:183], a[204:207], v[72:75], v[180:183]\n\tv_mfma_f32_16x16x32_bf16 v[184:187], a[204:207], v[76:79], v[184:187]\n\tbuffer_load_dwordx4 v[54:57], v8, s[76:79], s68 offen offset:3072\n\tv_mfma_f32_16x16x32_bf16 v[188:191], a[204:207], v[80:83], v[188:191]\n\tv_mfma_f32_16x16x32_bf16 v[192:195], a[204:207], v[84:87], v[192:195]\n\tv_mfma_f32_16x16x32_bf16 v[180:183], a[208:211], v[68:71], v[180:183]\n\t" \
    "v_mfma_f32_16x16x32_bf16 v[184:187], a[208:211], v[72:75], v[184:187]\n\tv_mfma_f32_16x16x32_bf16 v[188:191], a[208:211], v[76:79], v[188:191]\n\tv_mfma_f32_16x16x32_bf16 v[192:195], a[208:211], v[80:83], v[192:195]\n\tbuffer_load_dwordx2 v[58:59], v13, s[76:79], s68 offen\n\tv_mfma_f32_16x16x32_bf16 v[184:187], a[212:215], v[68:71], v[184:187]\n\tv_mfma_f32_16x16x32_bf16 v[188:191], a[212:215], v[72:75], v[188:191]\n\tv_mfma_f32_16x16x32_bf16 v[192:195], a[212:215], v[76:79], v[192:195]\n\tv_mfma_f32_16x16x32_bf16 v[188:191], a[216:219], v[68:71], v[188:191]\n\t" \
    "v_mfma_f32_16x16x32_bf16 v[192:195], a[216:219], v[72:75], v[192:195]\n\tv_mfma_f32_16x16x32_bf16 v[192:195], a[220:223], v[68:71], v[192:195]\n\tbuffer_load_dwordx2 v[60:61], v8, s[76:79], s68 offen offset:1016\n\ts_waitcnt lgkmcnt(0)\n\tv_mfma_f32_16x16x32_bf16 v[164:167], a[224:227], v[100:103], v[164:167]\n\tds_read_b64 a[0:1], v0 offset:4032\n\tv_mfma_f32_16x16x32_bf16 v[168:171], a[224:227], v[104:107], v[168:171]\n\tds_read_b64 a[2:3], v0 offset:4040\n\t" \
    "v_mfma_f32_16x16x32_bf16 v[172:175], a[224:227], v[108:111], v[172:175]\n\tds_read_b64 a[4:5], v0 offset:3520\n\tv_mfma_f32_16x16x32_bf16 v[176:179], a[224:227], v[112:115], v[176:179]\n\tds_read_b64 a[6:7], v0 offset:3528\n\tv_mfma_f32_16x16x32_bf16 v[180:183], a[224:227], v[116:119], v[180:183]\n\tds_read_b64 a[8:9], v0 offset:3008\n\tv_mfma_f32_16x16x32_bf16 v[184:187], a[224:227], v[120:123], v[184:187]\n\tbuffer_load_dwordx2 v[62:63], v8, s[76:79], s68 offen offset:2040\n\t" \
    "ds_read_b64 a[10:11], v0 offset:3016\n\tv_mfma_f32_16x16x32_bf16 v[188:191], a[224:227], v[124:127], v[188:191]\n\tds_read_b64 a[12:13], v0 offset:2496\n\tv_mfma_f32_16x16x32_bf16 v[192:195], a[224:227], v[128:131], v[192:195]\n\tds_read_b64 a[14:15], v0 offset:2504\n\tv_mfma_f32_16x16x32_bf16 v[168:171], a[228:231], v[100:103], v[168:171]\n\tds_read_b64 a[16:17], v0 offset:1984\n\tv_mfma_f32_16x16x32_bf16 v[172:175], a[228:231], v[104:107], v[172:175]\n\t" \
    "ds_read_b64 a[18:19], v0 offset:1992\n\tv_mfma_f32_16x16x32_bf16 v[176:179], a[228:231], v[108:111], v[176:179]\n\tds_read_b64 a[20:21], v0 offset:1472\n\tv_mfma_f32_16x16x32_bf16 v[180:183], a[228:231], v[112:115], v[180:183]\n\tbuffer_load_dwordx2 v[64:65], v8, s[76:79], s68 offen offset:3064\n\tds_read_b64 a[22:23], v0 offset:1480\n\tv_mfma_f32_16x16x32_bf16 v[184:187], a[228:231], v[116:119], v[184:187]\n\tds_read_b64 a[24:25], v0 offset:960\n\t" \
    "v_mfma_f32_16x16x32_bf16 v[188:191], a[228:231], v[120:123], v[188:191]\n\tds_read_b64 a[26:27], v0 offset:968\n\tv_mfma_f32_16x16x32_bf16 v[192:195], a[228:231], v[124:127], v[192:195]\n\tds_read_b64 a[28:29], v0 offset:448\n\tv_mfma_f32_16x16x32_bf16 v[172:175], a[232:235], v[100:103], v[172:175]\n\tds_read_b64 a[30:31], v0 offset:456\n\tv_mfma_f32_16x16x32_bf16 v[176:179], a[232:235], v[104:107], v[176:179]\n\tds_read_b128 v[68:71], v1 offset:37440\n\t" \
    "v_mfma_f32_16x16x32_bf16 v[180:183], a[232:235], v[108:111], v[180:183]\n\tbuffer_load_ushort v66, off, s[80:83], s66\n\tds_read_b128 v[72:75], v1 offset:37952\n\tv_mfma_f32_16x16x32_bf16 v[184:187], a[232:235], v[112:115], v[184:187]\n\tds_read_b128 v[76:79], v1 offset:38464\n\tv_mfma_f32_16x16x32_bf16 v[188:191], a[232:235], v[116:119], v[188:191]\n\tds_read_b128 v[80:83], v1 offset:38976\n\tv_mfma_f32_16x16x32_bf16 v[192:195], a[232:235], v[120:123], v[192:195]\n\t" \
    "ds_read_b128 v[84:87], v1 offset:39488\n\tv_mfma_f32_16x16x32_bf16 v[176:179], a[236:239], v[100:103], v[176:179]\n\tds_read_b128 v[88:91], v1 offset:40000\n\tv_mfma_f32_16x16x32_bf16 v[180:183], a[236:239], v[104:107], v[180:183]\n\tds_read_b128 v[92:95], v1 offset:40512\n\tv_mfma_f32_16x16x32_bf16 v[184:187], a[236:239], v[108:111], v[184:187]\n\tbuffer_load_dword v15, v7, s[16:19], s42 offen\n\tds_read_b128 v[96:99], v1 offset:41024\n\t" \
    "v_mfma_f32_16x16x32_bf16 v[188:191], a[236:239], v[112:115], v[188:191]\n\ts_add_u32 s43, s43, 1\n\tv_mfma_f32_16x16x32_bf16 v[192:195], a[236:239], v[116:119], v[192:195]\n\tv_readlane_b32 s42, v237, s43\n\ts_nop 4\n\ts_waitcnt vmcnt(12)\n\tbuffer_store_dwordx2 v[248:249], off, s[8:11], s51 sc1\n\tv_mfma_f32_16x16x32_bf16 v[180:183], a[240:243], v[100:103], v[180:183]\n\t" \
    "v_mfma_f32_16x16x32_bf16 v[184:187], a[240:243], v[104:107], v[184:187]\n\tv_mfma_f32_16x16x32_bf16 v[188:191], a[240:243], v[108:111], v[188:191]\n\tv_mfma_f32_16x16x32_bf16 v[192:195], a[240:243], v[112:115], v[192:195]\n\tv_mfma_f32_16x16x32_bf16 v[184:187], a[244:247], v[100:103], v[184:187]\n\tv_mfma_f32_16x16x32_bf16 v[188:191], a[244:247], v[104:107], v[188:191]\n\tv_mfma_f32_16x16x32_bf16 v[192:195], a[244:247], v[108:111], v[192:195]\n\tv_mfma_f32_16x16x32_bf16 v[188:191], a[248:251], v[100:103], v[188:191]\n\tv_mfma_f32_16x16x32_bf16 v[192:195], a[248:251], v[104:107], v[192:195]\n\t" \
    "v_mfma_f32_16x16x32_bf16 v[192:195], a[252:255], v[100:103], v[192:195]\n\ts_waitcnt lgkmcnt(0)\n\tv_mfma_f32_16x16x32_bf16 v[132:135], v[228:231], v[68:71], 0\n\tds_read_b64 a[32:33], v0 offset:3968\n\tds_read_b64 a[34:35], v0 offset:3976\n\tv_mfma_f32_16x16x32_bf16 v[136:139], v[228:231], v[72:75], 0\n\tds_read_b64 a[36:37], v0 offset:3456\n\tds_read_b64 a[38:39], v0 offset:3464\n\t" \
    "v_mfma_f32_16x16x32_bf16 v[140:143], v[228:231], v[76:79], 0\n\tds_read_b64 a[40:41], v0 offset:2944\n\tds_read_b64 a[42:43], v0 offset:2952\n\tv_mfma_f32_16x16x32_bf16 v[144:147], v[228:231], v[80:83], 0\n\tds_read_b64 a[44:45], v0 offset:2432\n\tds_read_b64 a[46:47], v0 offset:2440\n\tv_mfma_f32_16x16x32_bf16 v[148:151], v[228:231], v[84:87], 0\n\tds_read_b64 a[48:49], v0 offset:1920\n\t" \
    "ds_read_b64 a[50:51], v0 offset:1928\n\tv_mfma_f32_16x16x32_bf16 v[152:155], v[228:231], v[88:91], 0\n\tds_read_b64 a[52:53], v0 offset:1408\n\tv_cvt_pk_bf16_f32 v164, v164, v165\n\tv_mfma_f32_16x16x32_bf16 v[156:159], v[228:231], v[92:95], 0\n\tds_read_b64 a[54:55], v0 offset:1416\n\tv_cvt_pk_bf16_f32 v165, v166, v167\n\tv_mfma_f32_16x16x32_bf16 v[160:163], v[228:231], v[96:99], 0\n\t" \
    "ds_read_b64 a[56:57], v0 offset:896\n\tds_write_b64 v3, v[164:165] offset:18496\n\tv_mfma_f32_16x16x32_bf16 v[132:135], a[0:3], v[68:71], v[132:135]\n\tds_read_b64 a[58:59], v0 offset:904\n\tv_cvt_pk_bf16_f32 v168, v168, v169\n\tv_mfma_f32_16x16x32_bf16 v[136:139], a[0:3], v[72:75], v[136:139]\n\tds_read_b64 a[60:61], v0 offset:384\n\tv_cvt_pk_bf16_f32 v169, v170, v171\n\t" \
    "v_mfma_f32_16x16x32_bf16 v[140:143], a[0:3], v[76:79], v[140:143]\n\tds_read_b64 a[62:63], v0 offset:392\n\tds_write_b64 v3, v[168:169] offset:19008\n\tv_mfma_f32_16x16x32_bf16 v[144:147], a[0:3], v[80:83], v[144:147]\n\tds_read_b128 v[100:103], v1 offset:37376\n\tv_cvt_pk_bf16_f32 v172, v172, v173\n\tv_mfma_f32_16x16x32_bf16 v[148:151], a[0:3], v[84:87], v[148:151]\n\tds_read_b128 v[104:107], v1 offset:37888\n\t" \
    "v_cvt_pk_bf16_f32 v173, v174, v175\n\tv_mfma_f32_16x16x32_bf16 v[152:155], a[0:3], v[88:91], v[152:155]\n\tds_read_b128 v[108:111], v1 offset:38400\n\tds_write_b64 v3, v[172:173] offset:19520\n\tv_mfma_f32_16x16x32_bf16 v[156:159], a[0:3], v[92:95], v[156:159]\n\tds_read_b128 v[112:115], v1 offset:38912\n\tv_cvt_pk_bf16_f32 v176, v176, v177\n\tv_mfma_f32_16x16x32_bf16 v[160:163], a[0:3], v[96:99], v[160:163]\n\t" \
    "ds_read_b128 v[116:119], v1 offset:39424\n\tv_cvt_pk_bf16_f32 v177, v178, v179\n\tv_mfma_f32_16x16x32_bf16 v[136:139], a[4:7], v[68:71], v[136:139]\n\tds_read_b128 v[120:123], v1 offset:39936\n\tds_write_b64 v3, v[176:177] offset:20032\n\tv_mfma_f32_16x16x32_bf16 v[140:143], a[4:7], v[72:75], v[140:143]\n\tds_read_b128 v[124:127], v1 offset:40448\n\tv_cvt_pk_bf16_f32 v180, v180, v181\n\t" \
    "v_mfma_f32_16x16x32_bf16 v[144:147], a[4:7], v[76:79], v[144:147]\n\tds_read_b128 v[128:131], v1 offset:40960\n\tv_cvt_pk_bf16_f32 v181, v182, v183\n\tv_mfma_f32_16x16x32_bf16 v[148:151], a[4:7], v[80:83], v[148:151]\n\tds_read_b64 a[64:65], v0 offset:3904\n\tds_write_b64 v3, v[180:181] offset:20544\n\tv_mfma_f32_16x16x32_bf16 v[152:155], a[4:7], v[84:87], v[152:155]\n\tds_read_b64 a[66:67], v0 offset:3912\n\t" \
    "v_cvt_pk_bf16_f32 v184, v184, v185\n\tv_mfma_f32_16x16x32_bf16 v[156:159], a[4:7], v[88:91], v[156:159]\n\tds_read_b64 a[68:69], v0 offset:3392\n\tv_cvt_pk_bf16_f32 v185, v186, v187\n\tv_mfma_f32_16x16x32_bf16 v[160:163], a[4:7], v[92:95], v[160:163]\n\tbuffer_load_dword v15, v7, s[16:19], s42 offen\n\tds_read_b64 a[70:71], v0 offset:3400\n\tv_mfma_f32_16x16x32_bf16 v[140:143], a[8:11], v[68:71], v[140:143]\n\t" \
    "s_add_u32 s43, s43, 1\n\tds_read_b64 a[72:73], v0 offset:2880\n\tds_write_b64 v3, v[184:185] offset:21056\n\tv_mfma_f32_16x16x32_bf16 v[144:147], a[8:11], v[72:75], v[144:147]\n\tv_readlane_b32 s42, v237, s43\n\ts_nop 4\n\tds_read_b64 a[74:75], v0 offset:2888\n\tv_cvt_pk_bf16_f32 v188, v188, v189\n\t" \
    "v_mfma_f32_16x16x32_bf16 v[148:151], a[8:11], v[76:79], v[148:151]\n\tds_read_b64 a[76:77], v0 offset:2368\n\tv_cvt_pk_bf16_f32 v189, v190, v191\n\tv_mfma_f32_16x16x32_bf16 v[152:155], a[8:11], v[80:83], v[152:155]\n\tds_read_b64 a[78:79], v0 offset:2376\n\tds_write_b64 v3, v[188:189] offset:21568\n\tv_mfma_f32_16x16x32_bf16 v[156:159], a[8:11], v[84:87], v[156:159]\n\tds_read_b64 a[80:81], v0 offset:1856\n\t" \
    "v_cvt_pk_bf16_f32 v192, v192, v193\n\tv_mfma_f32_16x16x32_bf16 v[160:163], a[8:11], v[88:91], v[160:163]\n\tds_read_b64 a[82:83], v0 offset:1864\n\tv_cvt_pk_bf16_f32 v193, v194, v195\n\tv_mfma_f32_16x16x32_bf16 v[144:147], a[12:15], v[68:71], v[144:147]\n\tds_read_b64 a[84:85], v0 offset:1344\n\tds_write_b64 v3, v[192:193] offset:22080\n\tv_mfma_f32_16x16x32_bf16 v[148:151], a[12:15], v[72:75], v[148:151]\n\t" \
    "ds_read_b64 a[86:87], v0 offset:1352\n\tv_mfma_f32_16x16x32_bf16 v[152:155], a[12:15], v[76:79], v[152:155]\n\tds_read_b64 a[88:89], v0 offset:832\n\tv_mfma_f32_16x16x32_bf16 v[156:159], a[12:15], v[80:83], v[156:159]\n\tds_read_b64 a[90:91], v0 offset:840\n\tv_mfma_f32_16x16x32_bf16 v[160:163], a[12:15], v[84:87], v[160:163]\n\tds_read_b64 a[92:93], v0 offset:320\n\tv_mfma_f32_16x16x32_bf16 v[148:151], a[16:19], v[68:71], v[148:151]\n\t" \
    "ds_read_b64 a[94:95], v0 offset:328\n\tv_mfma_f32_16x16x32_bf16 v[152:155], a[16:19], v[72:75], v[152:155]\n\tds_read_b64 a[96:97], v0 offset:3840\n\tv_mfma_f32_16x16x32_bf16 v[156:159], a[16:19], v[76:79], v[156:159]\n\tds_read_b64 a[98:99], v0 offset:3848\n\tv_mfma_f32_16x16x32_bf16 v[160:163], a[16:19], v[80:83], v[160:163]\n\tds_read_b64 a[100:101], v0 offset:3328\n\tv_mfma_f32_16x16x32_bf16 v[152:155], a[20:23], v[68:71], v[152:155]\n\t" \
    "ds_read_b64 a[102:103], v0 offset:3336\n\tv_mfma_f32_16x16x32_bf16 v[156:159], a[20:23], v[72:75], v[156:159]\n\tv_mfma_f32_16x16x32_bf16 v[160:163], a[20:23], v[76:79], v[160:163]\n\tv_mfma_f32_16x16x32_bf16 v[156:159], a[24:27], v[68:71], v[156:159]\n\tv_mfma_f32_16x16x32_bf16 v[160:163], a[24:27], v[72:75], v[160:163]\n\tv_mfma_f32_16x16x32_bf16 v[160:163], a[28:31], v[68:71], v[160:163]\n\ts_waitcnt lgkmcnt(9)\n\ts_barrier\n\t" \
    "v_mfma_f32_16x16x32_bf16 v[132:135], a[32:35], v[100:103], v[132:135]\n\tds_read_b64 a[104:105], v0 offset:2816\n\tds_read_b64 a[106:107], v0 offset:2824\n\tv_mfma_f32_16x16x32_bf16 v[136:139], a[32:35], v[104:107], v[136:139]\n\tds_read_b64 a[108:109], v0 offset:2304\n\tds_read_b64 a[110:111], v0 offset:2312\n\tv_mfma_f32_16x16x32_bf16 v[140:143], a[32:35], v[108:111], v[140:143]\n\tds_read_b64 a[112:113], v0 offset:1792\n\t" \
    "ds_read_b64 a[114:115], v0 offset:1800\n\tv_mfma_f32_16x16x32_bf16 v[144:147], a[32:35], v[112:115], v[144:147]\n\tds_read_b64 a[116:117], v0 offset:1280\n\tds_read_b64 a[118:119], v0 offset:1288\n\tv_mfma_f32_16x16x32_bf16 v[148:151], a[32:35], v[116:119], v[148:151]\n\tds_read_b64 a[120:121], v0 offset:768\n\tds_read_b64 a[122:123], v0 offset:776\n\tv_mfma_f32_16x16x32_bf16 v[152:155], a[32:35], v[120:123], v[152:155]\n\t" \
    "ds_read_b64 a[124:125], v0 offset:256\n\ts_add_u32 s51, s44, 4\n\tv_mfma_f32_16x16x32_bf16 v[156:159], a[32:35], v[124:127], v[156:159]\n\tds_read_b64 a[126:127], v0 offset:264\n\ts_mul_i32 s65, s51, 1366\n\tv_mfma_f32_16x16x32_bf16 v[160:163], a[32:35], v[128:131], v[160:163]\n\tds_read_b64 a[128:129], v0 offset:3776\n\ts_lshr_b32 s65, s65, 16\n\t" \
    "v_mfma_f32_16x16x32_bf16 v[136:139], a[36:39], v[100:103], v[136:139]\n\tds_read_b64 a[130:131], v0 offset:3784\n\ts_mul_i32 s56, s65, 48\n\tv_mfma_f32_16x16x32_bf16 v[140:143], a[36:39], v[104:107], v[140:143]\n\tds_read_b64 a[132:133], v0 offset:3264\n\ts_sub_u32 s56, s51, s56\n\tv_mfma_f32_16x16x32_bf16 v[144:147], a[36:39], v[108:111], v[144:147]\n\tds_read_b64 a[134:135], v0 offset:3272\n\t" \
    "s_lshl_b32 s56, s56, 6\n\tv_mfma_f32_16x16x32_bf16 v[148:151], a[36:39], v[112:115], v[148:151]\n\tds_read_b128 v[68:71], v1 offset:37312\n\ts_lshr_b32 s70, s65, 2\n\tv_mfma_f32_16x16x32_bf16 v[152:155], a[36:39], v[116:119], v[152:155]\n\tds_read_b128 v[72:75], v1 offset:37824\n\ts_mul_i32 s70, s70, 252\n\tv_mfma_f32_16x16x32_bf16 v[156:159], a[36:39], v[120:123], v[156:159]\n\t" \
    "ds_read_b128 v[76:79], v1 offset:38336\n\ts_add_u32 s65, s65, s70\n\tv_mfma_f32_16x16x32_bf16 v[160:163], a[36:39], v[124:127], v[160:163]\n\tds_read_b128 v[80:83], v1 offset:38848\n\ts_mul_i32 s65, s65, 3072\n\tv_mfma_f32_16x16x32_bf16 v[140:143], a[40:43], v[100:103], v[140:143]\n\tds_read_b128 v[84:87], v1 offset:39360\n\ts_add_u32 s56, s56, s65\n\t" \
    "v_mfma_f32_16x16x32_bf16 v[144:147], a[40:43], v[104:107], v[144:147]\n\tds_read_b128 v[88:91], v1 offset:39872\n\ts_add_u32 s56, s56, 4849664\n\tv_mfma_f32_16x16x32_bf16 v[148:151], a[40:43], v[108:111], v[148:151]\n\tds_read_b128 v[92:95], v1 offset:40384\n\ts_add_u32 s51, s51, s38\n\tv_mfma_f32_16x16x32_bf16 v[152:155], a[40:43], v[112:115], v[152:155]\n\tds_read_b128 v[96:99], v1 offset:40896\n\t" \
    "s_lshl_b32 s51, s51, 3\n\tv_mfma_f32_16x16x32_bf16 v[156:159], a[40:43], v[116:119], v[156:159]\n\tds_read_b64 a[136:137], v0 offset:2752\n\ts_add_u32 s51, s51, 50176\n\tds_read_b64_tr_b16 v[204:205], v4 offset:18496\n\tv_mfma_f32_16x16x32_bf16 v[160:163], a[40:43], v[120:123], v[160:163]\n\tds_read_b64 a[138:139], v0 offset:2760\n\tds_read_b64_tr_b16 v[206:207], v4 offset:18504\n\t" \
    "v_mfma_f32_16x16x32_bf16 v[144:147], a[44:47], v[100:103], v[144:147]\n\tds_read_b64 a[140:141], v0 offset:2240\n\tds_read_b64_tr_b16 v[208:209], v4 offset:18560\n\tv_mfma_f32_16x16x32_bf16 v[148:151], a[44:47], v[104:107], v[148:151]\n\tds_read_b64 a[142:143], v0 offset:2248\n\tds_read_b64_tr_b16 v[210:211], v4 offset:18568\n\tv_mfma_f32_16x16x32_bf16 v[152:155], a[44:47], v[108:111], v[152:155]\n\tds_read_b64 a[144:145], v0 offset:1728\n\t" \
    "ds_read_b64_tr_b16 v[212:213], v4 offset:19008\n\tv_mfma_f32_16x16x32_bf16 v[156:159], a[44:47], v[112:115], v[156:159]\n\tds_read_b64 a[146:147], v0 offset:1736\n\tds_read_b64_tr_b16 v[214:215], v4 offset:19016\n\tv_mfma_f32_16x16x32_bf16 v[160:163], a[44:47], v[116:119], v[160:163]\n\tds_read_b64 a[148:149], v0 offset:1216\n\tds_read_b64_tr_b16 v[216:217], v4 offset:19072\n\tv_mfma_f32_16x16x32_bf16 v[148:151], a[48:51], v[100:103], v[148:151]\n\t" \
    "ds_read_b64 a[150:151], v0 offset:1224\n\tds_read_b64_tr_b16 v[218:219], v4 offset:19080\n\tv_mfma_f32_16x16x32_bf16 v[152:155], a[48:51], v[104:107], v[152:155]\n\tds_read_b64 a[152:153], v0 offset:704\n\tv_mfma_f32_16x16x32_bf16 v[156:159], a[48:51], v[108:111], v[156:159]\n\tds_read_b64 a[154:155], v0 offset:712\n\tv_mfma_f32_16x16x32_bf16 v[160:163], a[48:51], v[112:115], v[160:163]\n\tds_read_b64 a[156:157], v0 offset:192\n\t" \
    "v_mfma_f32_16x16x32_bf16 v[152:155], a[52:55], v[100:103], v[152:155]\n\tds_read_b64 a[158:159], v0 offset:200\n\tv_mfma_f32_16x16x32_bf16 v[156:159], a[52:55], v[104:107], v[156:159]\n\tv_mfma_f32_16x16x32_bf16 v[160:163], a[52:55], v[108:111], v[160:163]\n\ts_waitcnt lgkmcnt(15)\n\tbuffer_store_dwordx4 v[204:207], v5, s[8:11], s56 offen sc1\n\ts_add_u32 s56, s56, 49152\n\tv_mfma_f32_16x16x32_bf16 v[156:159], a[56:59], v[100:103], v[156:159]\n\t" \
    "v_mfma_f32_16x16x32_bf16 v[160:163], a[56:59], v[104:107], v[160:163]\n\tv_mfma_f32_16x16x32_bf16 v[160:163], a[60:63], v[100:103], v[160:163]\n\tv_mfma_f32_16x16x32_bf16 v[132:135], a[64:67], v[68:71], v[132:135]\n\tds_read_b64 a[160:161], v0 offset:3712\n\tds_read_b64 a[162:163], v0 offset:3720\n\tv_mfma_f32_16x16x32_bf16 v[136:139], a[64:67], v[72:75], v[136:139]\n\tds_read_b64 a[164:165], v0 offset:3200\n\tds_read_b64 a[166:167], v0 offset:3208\n\t" \
    "v_mfma_f32_16x16x32_bf16 v[140:143], a[64:67], v[76:79], v[140:143]\n\tds_read_b64 a[168:169], v0 offset:2688\n\tds_read_b64 a[170:171], v0 offset:2696\n\tv_mfma_f32_16x16x32_bf16 v[144:147], a[64:67], v[80:83], v[144:147]\n\tds_read_b64 a[172:173], v0 offset:2176\n\tds_read_b64 a[174:175], v0 offset:2184\n\tv_mfma_f32_16x16x32_bf16 v[148:151], a[64:67], v[84:87], v[148:151]\n\tds_read_b64 a[176:177], v0 offset:1664\n\t" \
    "ds_read_b64 a[178:179], v0 offset:1672\n\tv_mfma_f32_16x16x32_bf16 v[152:155], a[64:67], v[88:91], v[152:155]\n\tds_read_b64 a[180:181], v0 offset:1152\n\tv_mfma_f32_16x16x32_bf16 v[156:159], a[64:67], v[92:95], v[156:159]\n\tds_read_b64 a[182:183], v0 offset:1160\n\tv_mfma_f32_16x16x32_bf16 v[160:163], a[64:67], v[96:99], v[160:163]\n\tds_read_b64 a[184:185], v0 offset:640\n\tv_mfma_f32_16x16x32_bf16 v[136:139], a[68:71], v[68:71], v[136:139]\n\t" \
    "ds_read_b64 a[186:187], v0 offset:648\n\tv_mfma_f32_16x16x32_bf16 v[140:143], a[68:71], v[72:75], v[140:143]\n\tds_read_b64 a[188:189], v0 offset:128\n\tv_mfma_f32_16x16x32_bf16 v[144:147], a[68:71], v[76:79], v[144:147]\n\tds_read_b64 a[190:191], v0 offset:136\n\tv_mfma_f32_16x16x32_bf16 v[148:151], a[68:71], v[80:83], v[148:151]\n\tds_read_b128 v[100:103], v1 offset:37248\n\tv_mfma_f32_16x16x32_bf16 v[152:155], a[68:71], v[84:87], v[152:155]\n\t" \
    "ds_read_b128 v[104:107], v1 offset:37760\n\tv_mfma_f32_16x16x32_bf16 v[156:159], a[68:71], v[88:91], v[156:159]\n\tds_read_b128 v[108:111], v1 offset:38272\n\tv_mfma_f32_16x16x32_bf16 v[160:163], a[68:71], v[92:95], v[160:163]\n\tds_read_b128 v[112:115], v1 offset:38784\n\ts_waitcnt lgkmcnt(15)\n\tbuffer_store_dwordx4 v[208:211], v5, s[8:11], s56 offen sc1\n\tv_mfma_f32_16x16x32_bf16 v[140:143], a[72:75], v[68:71], v[140:143]\n\t" \
    "ds_read_b128 v[116:119], v1 offset:39296\n\ts_add_u32 s56, s56, 49152\n\tv_mfma_f32_16x16x32_bf16 v[144:147], a[72:75], v[72:75], v[144:147]\n\tbuffer_load_dword v15, v7, s[16:19], s42 offen\n\tds_read_b128 v[120:123], v1 offset:39808\n\tv_mfma_f32_16x16x32_bf16 v[148:151], a[72:75], v[76:79], v[148:151]\n\ts_add_u32 s43, s43, 1\n\tds_read_b128 v[124:127], v1 offset:40320\n\t" \
    "v_mfma_f32_16x16x32_bf16 v[152:155], a[72:75], v[80:83], v[152:155]\n\tv_readlane_b32 s42, v237, s43\n\ts_nop 4\n\tds_read_b128 v[128:131], v1 offset:40832\n\tv_mfma_f32_16x16x32_bf16 v[156:159], a[72:75], v[84:87], v[156:159]\n\tds_read_b64 a[192:193], v0 offset:3648\n\tv_mfma_f32_16x16x32_bf16 v[160:163], a[72:75], v[88:91], v[160:163]\n\tds_read_b64 a[194:195], v0 offset:3656\n\t" \
    "v_mfma_f32_16x16x32_bf16 v[144:147], a[76:79], v[68:71], v[144:147]\n\tds_read_b64 a[196:197], v0 offset:3136\n\tv_mfma_f32_16x16x32_bf16 v[148:151], a[76:79], v[72:75], v[148:151]\n\tds_read_b64 a[198:199], v0 offset:3144\n\tv_mfma_f32_16x16x32_bf16 v[152:155], a[76:79], v[76:79], v[152:155]\n\tds_read_b64 a[200:201], v0 offset:2624\n\tv_mfma_f32_16x16x32_bf16 v[156:159], a[76:79], v[80:83], v[156:159]\n\tds_read_b64 a[202:203], v0 offset:2632\n\t" \
    "v_mfma_f32_16x16x32_bf16 v[160:163], a[76:79], v[84:87], v[160:163]\n\tds_read_b64 a[204:205], v0 offset:2112\n\tv_mfma_f32_16x16x32_bf16 v[148:151], a[80:83], v[68:71], v[148:151]\n\tds_read_b64 a[206:207], v0 offset:2120\n\tv_mfma_f32_16x16x32_bf16 v[152:155], a[80:83], v[72:75], v[152:155]\n\tds_read_b64 a[208:209], v0 offset:1600\n\tv_mfma_f32_16x16x32_bf16 v[156:159], a[80:83], v[76:79], v[156:159]\n\tds_read_b64 a[210:211], v0 offset:1608\n\t" \
    "v_mfma_f32_16x16x32_bf16 v[160:163], a[80:83], v[80:83], v[160:163]\n\tds_read_b64 a[212:213], v0 offset:1088\n\tv_mfma_f32_16x16x32_bf16 v[152:155], a[84:87], v[68:71], v[152:155]\n\tds_read_b64 a[214:215], v0 offset:1096\n\tv_mfma_f32_16x16x32_bf16 v[156:159], a[84:87], v[72:75], v[156:159]\n\tbuffer_store_dwordx4 v[212:215], v5, s[8:11], s56 offen sc1\n\tv_mfma_f32_16x16x32_bf16 v[160:163], a[84:87], v[76:79], v[160:163]\n\ts_add_u32 s56, s56, 49152\n\t" \
    "v_mfma_f32_16x16x32_bf16 v[156:159], a[88:91], v[68:71], v[156:159]\n\tv_mfma_f32_16x16x32_bf16 v[160:163], a[88:91], v[72:75], v[160:163]\n\tv_mfma_f32_16x16x32_bf16 v[160:163], a[92:95], v[68:71], v[160:163]\n\ts_waitcnt lgkmcnt(12)\n\tv_mfma_f32_16x16x32_bf16 v[132:135], a[96:99], v[100:103], v[132:135]\n\tds_read_b64 a[216:217], v0 offset:576\n\tv_mfma_f32_16x16x32_bf16 v[136:139], a[96:99], v[104:107], v[136:139]\n\tds_read_b64 a[218:219], v0 offset:584\n\t" \
    "v_mfma_f32_16x16x32_bf16 v[140:143], a[96:99], v[108:111], v[140:143]\n\tds_read_b64 a[220:221], v0 offset:64\n\tv_mfma_f32_16x16x32_bf16 v[144:147], a[96:99], v[112:115], v[144:147]\n\tds_read_b64 a[222:223], v0 offset:72\n\tv_mfma_f32_16x16x32_bf16 v[148:151], a[96:99], v[116:119], v[148:151]\n\tds_read_b64 a[224:225], v0 offset:3584\n\tv_mfma_f32_16x16x32_bf16 v[152:155], a[96:99], v[120:123], v[152:155]\n\tds_read_b64 a[226:227], v0 offset:3592\n\t" \
    "v_mfma_f32_16x16x32_bf16 v[156:159], a[96:99], v[124:127], v[156:159]\n\tds_read_b64 a[228:229], v0 offset:3072\n\tv_mfma_f32_16x16x32_bf16 v[160:163], a[96:99], v[128:131], v[160:163]\n\tds_read_b64 a[230:231], v0 offset:3080\n\tv_mfma_f32_16x16x32_bf16 v[136:139], a[100:103], v[100:103], v[136:139]\n\tds_read_b64 a[232:233], v0 offset:2560\n\tbuffer_store_dwordx4 v[216:219], v5, s[8:11], s56 offen sc1\n\tv_mfma_f32_16x16x32_bf16 v[140:143], a[100:103], v[104:107], v[140:143]\n\t" \
    "ds_read_b64 a[234:235], v0 offset:2568\n\ts_add_u32 s56, s56, 49152\n\tv_mfma_f32_16x16x32_bf16 v[144:147], a[100:103], v[108:111], v[144:147]\n\tds_read_b64 a[236:237], v0 offset:2048\n\tv_mfma_f32_16x16x32_bf16 v[148:151], a[100:103], v[112:115], v[148:151]\n\tds_read_b64 a[238:239], v0 offset:2056\n\tv_mfma_f32_16x16x32_bf16 v[152:155], a[100:103], v[116:119], v[152:155]\n\tds_read_b64 a[240:241], v0 offset:1536\n\t" \
    "v_mfma_f32_16x16x32_bf16 v[156:159], a[100:103], v[120:123], v[156:159]\n\tds_read_b64 a[242:243], v0 offset:1544\n\tv_mfma_f32_16x16x32_bf16 v[160:163], a[100:103], v[124:127], v[160:163]\n\tds_read_b64 a[244:245], v0 offset:1024\n\tv_mfma_f32_16x16x32_bf16 v[140:143], a[104:107], v[100:103], v[140:143]\n\tds_read_b64 a[246:247], v0 offset:1032\n\tv_mfma_f32_16x16x32_bf16 v[144:147], a[104:107], v[104:107], v[144:147]\n\tds_read_b128 v[68:71], v1 offset:37184\n\t" \
    "v_mfma_f32_16x16x32_bf16 v[148:151], a[104:107], v[108:111], v[148:151]\n\tds_read_b128 v[72:75], v1 offset:37696\n\tv_mfma_f32_16x16x32_bf16 v[152:155], a[104:107], v[112:115], v[152:155]\n\tds_read_b128 v[76:79], v1 offset:38208\n\tv_mfma_f32_16x16x32_bf16 v[156:159], a[104:107], v[116:119], v[156:159]\n\tds_read_b128 v[80:83], v1 offset:38720\n\tv_mfma_f32_16x16x32_bf16 v[160:163], a[104:107], v[120:123], v[160:163]\n\tds_read_b128 v[84:87], v1 offset:39232\n\t" \
    "v_mfma_f32_16x16x32_bf16 v[144:147], a[108:111], v[100:103], v[144:147]\n\tds_read_b128 v[88:91], v1 offset:39744\n\tv_mfma_f32_16x16x32_bf16 v[148:151], a[108:111], v[104:107], v[148:151]\n\tds_read_b128 v[92:95], v1 offset:40256\n\tv_mfma_f32_16x16x32_bf16 v[152:155], a[108:111], v[108:111], v[152:155]\n\tds_read_b128 v[96:99], v1 offset:40768\n\tv_mfma_f32_16x16x32_bf16 v[156:159], a[108:111], v[112:115], v[156:159]\n\tds_read_b64 a[248:249], v0 offset:512\n\t" \
    "v_mfma_f32_16x16x32_bf16 v[160:163], a[108:111], v[116:119], v[160:163]\n\tds_read_b64 a[250:251], v0 offset:520\n\tv_mfma_f32_16x16x32_bf16 v[148:151], a[112:115], v[100:103], v[148:151]\n\tds_read_b64 a[252:253], v0 offset:0\n\tv_mfma_f32_16x16x32_bf16 v[152:155], a[112:115], v[104:107], v[152:155]\n\tds_read_b64 a[254:255], v0 offset:8\n\tv_mfma_f32_16x16x32_bf16 v[156:159], a[112:115], v[108:111], v[156:159]\n\tv_mfma_f32_16x16x32_bf16 v[160:163], a[112:115], v[112:115], v[160:163]\n\t" \
    "v_mfma_f32_16x16x32_bf16 v[152:155], a[116:119], v[100:103], v[152:155]\n\tv_mfma_f32_16x16x32_bf16 v[156:159], a[116:119], v[104:107], v[156:159]\n\tv_mfma_f32_16x16x32_bf16 v[160:163], a[116:119], v[108:111], v[160:163]\n\tv_mfma_f32_16x16x32_bf16 v[156:159], a[120:123], v[100:103], v[156:159]\n\tv_mfma_f32_16x16x32_bf16 v[160:163], a[120:123], v[104:107], v[160:163]\n\tv_mfma_f32_16x16x32_bf16 v[160:163], a[124:127], v[100:103], v[160:163]\n\ts_waitcnt lgkmcnt(4)\n\tv_mfma_f32_16x16x32_bf16 v[132:135], a[128:131], v[68:71], v[132:135]\n\t" \
    "ds_read_b128 v[100:103], v1 offset:37120\n\ts_waitcnt vmcnt(8)\n\tv_alignbit_b32 v196, v45, v45, 16\n\tv_mfma_f32_16x16x32_bf16 v[136:139], a[128:131], v[72:75], v[136:139]\n\tds_read_b128 v[104:107], v1 offset:37632\n\tv_alignbit_b32 v197, v44, v44, 16\n\tv_mfma_f32_16x16x32_bf16 v[140:143], a[128:131], v[76:79], v[140:143]\n\tds_read_b128 v[108:111], v1 offset:38144\n\t" \
    "v_alignbit_b32 v198, v43, v43, 16\n\tv_mfma_f32_16x16x32_bf16 v[144:147], a[128:131], v[80:83], v[144:147]\n\tds_read_b128 v[112:115], v1 offset:38656\n\tv_alignbit_b32 v199, v42, v42, 16\n\tv_mfma_f32_16x16x32_bf16 v[148:151], a[128:131], v[84:87], v[148:151]\n\tds_write_b128 v2, v[196:199] offset:3072\n\tv_mfma_f32_16x16x32_bf16 v[152:155], a[128:131], v[88:91], v[152:155]\n\tds_read_b128 v[116:119], v1 offset:39168\n\t" \
    "v_bfi_b32 v200, s46, v45, v44\n\tv_mfma_f32_16x16x32_bf16 v[156:159], a[128:131], v[92:95], v[156:159]\n\tds_read_b128 v[120:123], v1 offset:39680\n\tv_bfi_b32 v201, s46, v44, v43\n\tv_mfma_f32_16x16x32_bf16 v[160:163], a[128:131], v[96:99], v[160:163]\n\tds_read_b128 v[124:127], v1 offset:40192\n\tv_bfi_b32 v202, s46, v43, v42\n\tv_mfma_f32_16x16x32_bf16 v[136:139], a[132:135], v[68:71], v[136:139]\n\t" \
    "ds_read_b128 v[128:131], v1 offset:40704\n\tv_bfi_b32 v203, s46, v42, v59\n\tv_mfma_f32_16x16x32_bf16 v[140:143], a[132:135], v[72:75], v[140:143]\n\tds_write_b128 v2, v[200:203] offset:7232\n\tv_mfma_f32_16x16x32_bf16 v[144:147], a[132:135], v[76:79], v[144:147]\n\tv_alignbit_b32 v196, v44, v44, 16\n\tv_alignbit_b32 v197, v43, v43, 16\n\tv_mfma_f32_16x16x32_bf16 v[148:151], a[132:135], v[80:83], v[148:151]\n\t" \
    "v_alignbit_b32 v198, v42, v42, 16\n\tv_alignbit_b32 v199, v59, v59, 16\n\tv_mfma_f32_16x16x32_bf16 v[152:155], a[132:135], v[84:87], v[152:155]\n\tds_write_b128 v2, v[196:199] offset:11392\n\tv_mfma_f32_16x16x32_bf16 v[156:159], a[132:135], v[88:91], v[156:159]\n\tv_bfi_b32 v200, s46, v44, v43\n\tv_bfi_b32 v201, s46, v43, v42\n\tv_mfma_f32_16x16x32_bf16 v[160:163], a[132:135], v[92:95], v[160:163]\n\t" \
    "v_bfi_b32 v202, s46, v42, v59\n\tv_bfi_b32 v203, s46, v59, v58\n\tv_mfma_f32_16x16x32_bf16 v[140:143], a[136:139], v[68:71], v[140:143]\n\tds_write_b128 v2, v[200:203] offset:15552\n\tv_mfma_f32_16x16x32_bf16 v[144:147], a[136:139], v[72:75], v[144:147]\n\tv_alignbit_b32 v196, v49, v49, 16\n\tv_alignbit_b32 v197, v48, v48, 16\n\tv_mfma_f32_16x16x32_bf16 v[148:151], a[136:139], v[76:79], v[148:151]\n\t" \
    "v_alignbit_b32 v198, v47, v47, 16\n\tv_alignbit_b32 v199, v46, v46, 16\n\tv_mfma_f32_16x16x32_bf16 v[152:155], a[136:139], v[80:83], v[152:155]\n\tbuffer_load_dword v15, v7, s[16:19], s42 offen\n\tv_mfma_f32_16x16x32_bf16 v[156:159], a[136:139], v[84:87], v[156:159]\n\ts_add_u32 s43, s43, 1\n\tds_write_b128 v2, v[196:199] offset:2048\n\tv_mfma_f32_16x16x32_bf16 v[160:163], a[136:139], v[88:91], v[160:163]\n\t" \
    "v_readlane_b32 s42, v237, s43\n\ts_nop 4\n\tv_bfi_b32 v200, s46, v49, v48\n\tv_bfi_b32 v201, s46, v48, v47\n\tv_mfma_f32_16x16x32_bf16 v[144:147], a[140:143], v[68:71], v[144:147]\n\tv_bfi_b32 v202, s46, v47, v46\n\tv_bfi_b32 v203, s46, v46, v61\n\tv_mfma_f32_16x16x32_bf16 v[148:151], a[140:143], v[72:75], v[148:151]\n\t" \
    "ds_write_b128 v2, v[200:203] offset:6208\n\tv_mfma_f32_16x16x32_bf16 v[152:155], a[140:143], v[76:79], v[152:155]\n\tv_alignbit_b32 v196, v48, v48, 16\n\tv_alignbit_b32 v197, v47, v47, 16\n\tv_mfma_f32_16x16x32_bf16 v[156:159], a[140:143], v[80:83], v[156:159]\n\tv_alignbit_b32 v198, v46, v46, 16\n\tv_alignbit_b32 v199, v61, v61, 16\n\tv_mfma_f32_16x16x32_bf16 v[160:163], a[140:143], v[84:87], v[160:163]\n\t" \
    "ds_write_b128 v2, v[196:199] offset:10368\n\tv_mfma_f32_16x16x32_bf16 v[148:151], a[144:147], v[68:71], v[148:151]\n\tv_bfi_b32 v200, s46, v48, v47\n\tv_bfi_b32 v201, s46, v47, v46\n\tv_mfma_f32_16x16x32_bf16 v[152:155], a[144:147], v[72:75], v[152:155]\n\tv_bfi_b32 v202, s46, v46, v61\n\tv_bfi_b32 v203, s46, v61, v60\n\tv_mfma_f32_16x16x32_bf16 v[156:159], a[144:147], v[76:79], v[156:159]\n\t" \
    "ds_write_b128 v2, v[200:203] offset:14528\n\tv_mfma_f32_16x16x32_bf16 v[160:163], a[144:147], v[80:83], v[160:163]\n\tv_alignbit_b32 v196, v53, v53, 16\n\tv_alignbit_b32 v197, v52, v52, 16\n\tv_mfma_f32_16x16x32_bf16 v[152:155], a[148:151], v[68:71], v[152:155]\n\tv_alignbit_b32 v198, v51, v51, 16\n\tv_alignbit_b32 v199, v50, v50, 16\n\tv_mfma_f32_16x16x32_bf16 v[156:159], a[148:151], v[72:75], v[156:159]\n\t" \
    "ds_write_b128 v2, v[196:199] offset:1024\n\tv_mfma_f32_16x16x32_bf16 v[160:163], a[148:151], v[76:79], v[160:163]\n\tv_bfi_b32 v200, s46, v53, v52\n\tv_bfi_b32 v201, s46, v52, v51\n\tv_mfma_f32_16x16x32_bf16 v[156:159], a[152:155], v[68:71], v[156:159]\n\tv_bfi_b32 v202, s46, v51, v50\n\tv_bfi_b32 v203, s46, v50, v63\n\tv_mfma_f32_16x16x32_bf16 v[160:163], a[152:155], v[72:75], v[160:163]\n\t" \
    "ds_write_b128 v2, v[200:203] offset:5184\n\tv_mfma_f32_16x16x32_bf16 v[160:163], a[156:159], v[68:71], v[160:163]\n\tv_alignbit_b32 v196, v52, v52, 16\n\tv_alignbit_b32 v197, v51, v51, 16\n\ts_waitcnt lgkmcnt(9)\n\tv_mfma_f32_16x16x32_bf16 v[132:135], a[160:163], v[100:103], v[132:135]\n\tds_read_b128 v[68:71], v1 offset:37056\n\tv_alignbit_b32 v198, v50, v50, 16\n\t" \
    "v_mfma_f32_16x16x32_bf16 v[136:139], a[160:163], v[104:107], v[136:139]\n\tds_read_b128 v[72:75], v1 offset:37568\n\tv_alignbit_b32 v199, v63, v63, 16\n\tv_mfma_f32_16x16x32_bf16 v[140:143], a[160:163], v[108:111], v[140:143]\n\tds_write_b128 v2, v[196:199] offset:9344\n\tv_mfma_f32_16x16x32_bf16 v[144:147], a[160:163], v[112:115], v[144:147]\n\tds_read_b128 v[76:79], v1 offset:38080\n\tv_bfi_b32 v200, s46, v52, v51\n\t" \
    "v_mfma_f32_16x16x32_bf16 v[148:151], a[160:163], v[116:119], v[148:151]\n\tds_read_b128 v[80:83], v1 offset:38592\n\tv_bfi_b32 v201, s46, v51, v50\n\tv_mfma_f32_16x16x32_bf16 v[152:155], a[160:163], v[120:123], v[152:155]\n\tds_read_b128 v[84:87], v1 offset:39104\n\tv_bfi_b32 v202, s46, v50, v63\n\tv_mfma_f32_16x16x32_bf16 v[156:159], a[160:163], v[124:127], v[156:159]\n\tds_read_b128 v[88:91], v1 offset:39616\n\t" \
    "v_bfi_b32 v203, s46, v63, v62\n\tv_mfma_f32_16x16x32_bf16 v[160:163], a[160:163], v[128:131], v[160:163]\n\tds_write_b128 v2, v[200:203] offset:13504\n\tv_mfma_f32_16x16x32_bf16 v[136:139], a[164:167], v[100:103], v[136:139]\n\tds_read_b128 v[92:95], v1 offset:40128\n\tv_alignbit_b32 v196, v57, v57, 16\n\tv_mfma_f32_16x16x32_bf16 v[140:143], a[164:167], v[104:107], v[140:143]\n\tds_read_b128 v[96:99], v1 offset:40640\n\t" \
    "v_alignbit_b32 v197, v56, v56, 16\n\tv_mfma_f32_16x16x32_bf16 v[144:147], a[164:167], v[108:111], v[144:147]\n\tv_alignbit_b32 v198, v55, v55, 16\n\tv_alignbit_b32 v199, v54, v54, 16\n\tv_mfma_f32_16x16x32_bf16 v[148:151], a[164:167], v[112:115], v[148:151]\n\tds_write_b128 v2, v[196:199] offset:0\n\tv_mfma_f32_16x16x32_bf16 v[152:155], a[164:167], v[116:119], v[152:155]\n\tv_bfi_b32 v200, s46, v57, v56\n\t" \
    "v_bfi_b32 v201, s46, v56, v55\n\tv_mfma_f32_16x16x32_bf16 v[156:159], a[164:167], v[120:123], v[156:159]\n\tv_bfi_b32 v202, s46, v55, v54\n\tv_bfi_b32 v203, s46, v54, v65\n\tv_mfma_f32_16x16x32_bf16 v[160:163], a[164:167], v[124:127], v[160:163]\n\tds_write_b128 v2, v[200:203] offset:4160\n\tv_mfma_f32_16x16x32_bf16 v[140:143], a[168:171], v[100:103], v[140:143]\n\tv_alignbit_b32 v196, v56, v56, 16\n\t" \
    "v_alignbit_b32 v197, v55, v55, 16\n\tv_mfma_f32_16x16x32_bf16 v[144:147], a[168:171], v[104:107], v[144:147]\n\tv_alignbit_b32 v198, v54, v54, 16\n\tv_alignbit_b32 v199, v65, v65, 16\n\tv_mfma_f32_16x16x32_bf16 v[148:151], a[168:171], v[108:111], v[148:151]\n\tds_write_b128 v2, v[196:199] offset:8320\n\tv_mfma_f32_16x16x32_bf16 v[152:155], a[168:171], v[112:115], v[152:155]\n\tv_bfi_b32 v200, s46, v56, v55\n\t" \
    "v_bfi_b32 v201, s46, v55, v54\n\tv_mfma_f32_16x16x32_bf16 v[156:159], a[168:171], v[116:119], v[156:159]\n\tv_bfi_b32 v202, s46, v54, v65\n\tv_bfi_b32 v203, s46, v65, v64\n\tv_mfma_f32_16x16x32_bf16 v[160:163], a[168:171], v[120:123], v[160:163]\n\tds_write_b128 v2, v[200:203] offset:12480\n\tv_mfma_f32_16x16x32_bf16 v[144:147], a[172:175], v[100:103], v[144:147]\n\tv_lshl_or_b32 v66, v66, 16, v66\n\t" \
    "v_and_b32 v228, v66, v232\n\tv_mfma_f32_16x16x32_bf16 v[148:151], a[172:175], v[104:107], v[148:151]\n\tv_and_b32 v229, v66, v233\n\tv_and_b32 v230, v66, v234\n\tv_mfma_f32_16x16x32_bf16 v[152:155], a[172:175], v[108:111], v[152:155]\n\tv_and_b32 v231, v66, v235\n\tv_mfma_f32_16x16x32_bf16 v[156:159], a[172:175], v[112:115], v[156:159]\n\tv_mfma_f32_16x16x32_bf16 v[160:163], a[172:175], v[116:119], v[160:163]\n\t" \
    "v_mfma_f32_16x16x32_bf16 v[148:151], a[176:179], v[100:103], v[148:151]\n\tv_mfma_f32_16x16x32_bf16 v[152:155], a[176:179], v[104:107], v[152:155]\n\tv_mfma_f32_16x16x32_bf16 v[156:159], a[176:179], v[108:111], v[156:159]\n\tv_mfma_f32_16x16x32_bf16 v[160:163], a[176:179], v[112:115], v[160:163]\n\tv_mfma_f32_16x16x32_bf16 v[152:155], a[180:183], v[100:103], v[152:155]\n\tv_mfma_f32_16x16x32_bf16 v[156:159], a[180:183], v[104:107], v[156:159]\n\tv_mfma_f32_16x16x32_bf16 v[160:163], a[180:183], v[108:111], v[160:163]\n\tv_mfma_f32_16x16x32_bf16 v[156:159], a[184:187], v[100:103], v[156:159]\n\t" \
    "v_mfma_f32_16x16x32_bf16 v[160:163], a[184:187], v[104:107], v[160:163]\n\tv_mfma_f32_16x16x32_bf16 v[160:163], a[188:191], v[100:103], v[160:163]\n\ts_waitcnt lgkmcnt(4)\n\tv_mfma_f32_16x16x32_bf16 v[132:135], a[192:195], v[68:71], v[132:135]\n\tds_read_b128 v[100:103], v1 offset:36992\n\tv_mfma_f32_16x16x32_bf16 v[136:139], a[192:195], v[72:75], v[136:139]\n\tds_read_b128 v[104:107], v1 offset:37504\n\tv_mfma_f32_16x16x32_bf16 v[140:143], a[192:195], v[76:79], v[140:143]\n\t" \
    "ds_read_b128 v[108:111], v1 offset:38016\n\tv_mfma_f32_16x16x32_bf16 v[144:147], a[192:195], v[80:83], v[144:147]\n\tds_read_b128 v[112:115], v1 offset:38528\n\tv_mfma_f32_16x16x32_bf16 v[148:151], a[192:195], v[84:87], v[148:151]\n\tds_read_b128 v[116:119], v1 offset:39040\n\tv_mfma_f32_16x16x32_bf16 v[152:155], a[192:195], v[88:91], v[152:155]\n\tds_read_b128 v[120:123], v1 offset:39552\n\tv_mfma_f32_16x16x32_bf16 v[156:159], a[192:195], v[92:95], v[156:159]\n\t" \
    "ds_read_b128 v[124:127], v1 offset:40064\n\tv_mfma_f32_16x16x32_bf16 v[160:163], a[192:195], v[96:99], v[160:163]\n\tds_read_b128 v[128:131], v1 offset:40576\n\tv_mfma_f32_16x16x32_bf16 v[136:139], a[196:199], v[68:71], v[136:139]\n\tv_mfma_f32_16x16x32_bf16 v[140:143], a[196:199], v[72:75], v[140:143]\n\tv_mfma_f32_16x16x32_bf16 v[144:147], a[196:199], v[76:79], v[144:147]\n\tv_mfma_f32_16x16x32_bf16 v[148:151], a[196:199], v[80:83], v[148:151]\n\tv_mfma_f32_16x16x32_bf16 v[152:155], a[196:199], v[84:87], v[152:155]\n\t" \
    "v_mfma_f32_16x16x32_bf16 v[156:159], a[196:199], v[88:91], v[156:159]\n\tv_mfma_f32_16x16x32_bf16 v[160:163], a[196:199], v[92:95], v[160:163]\n\tv_mfma_f32_16x16x32_bf16 v[140:143], a[200:203], v[68:71], v[140:143]\n\tv_mfma_f32_16x16x32_bf16 v[144:147], a[200:203], v[72:75], v[144:147]\n\tv_mfma_f32_16x16x32_bf16 v[148:151], a[200:203], v[76:79], v[148:151]\n\tv_mfma_f32_16x16x32_bf16 v[152:155], a[200:203], v[80:83], v[152:155]\n\tv_mfma_f32_16x16x32_bf16 v[156:159], a[200:203], v[84:87], v[156:159]\n\tv_mfma_f32_16x16x32_bf16 v[160:163], a[200:203], v[88:91], v[160:163]\n\t" \
    "buffer_load_dword v15, v7, s[16:19], s42 offen\n\tv_mfma_f32_16x16x32_bf16 v[144:147], a[204:207], v[68:71], v[144:147]\n\ts_add_u32 s43, s43, 1\n\tv_mfma_f32_16x16x32_bf16 v[148:151], a[204:207], v[72:75], v[148:151]\n\tv_readlane_b32 s42, v237, s43\n\ts_nop 4\n\tv_mfma_f32_16x16x32_bf16 v[152:155], a[204:207], v[76:79], v[152:155]\n\tv_mfma_f32_16x16x32_bf16 v[156:159], a[204:207], v[80:83], v[156:159]\n\t" \
    "v_mfma_f32_16x16x32_bf16 v[160:163], a[204:207], v[84:87], v[160:163]\n\tv_mfma_f32_16x16x32_bf16 v[148:151], a[208:211], v[68:71], v[148:151]\n\tv_mfma_f32_16x16x32_bf16 v[152:155], a[208:211], v[72:75], v[152:155]\n\tv_mfma_f32_16x16x32_bf16 v[156:159], a[208:211], v[76:79], v[156:159]\n\tv_mfma_f32_16x16x32_bf16 v[160:163], a[208:211], v[80:83], v[160:163]\n\tv_mfma_f32_16x16x32_bf16 v[152:155], a[212:215], v[68:71], v[152:155]\n\tv_mfma_f32_16x16x32_bf16 v[156:159], a[212:215], v[72:75], v[156:159]\n\tv_mfma_f32_16x16x32_bf16 v[160:163], a[212:215], v[76:79], v[160:163]\n\t" \
    "v_mfma_f32_16x16x32_bf16 v[156:159], a[216:219], v[68:71], v[156:159]\n\tv_mfma_f32_16x16x32_bf16 v[160:163], a[216:219], v[72:75], v[160:163]\n\tv_mfma_f32_16x16x32_bf16 v[160:163], a[220:223], v[68:71], v[160:163]\n\ts_waitcnt lgkmcnt(0)\n\tv_mfma_f32_16x16x32_bf16 v[132:135], a[224:227], v[100:103], v[132:135]\n\ts_waitcnt vmcnt(2)\n\tbuffer_store_dwordx2 v[248:249], off, s[8:11], s51 sc1\n\tds_read_b64 a[0:1], v0 offset:4032\n\t" \
    "v_mfma_f32_16x16x32_bf16 v[136:139], a[224:227], v[104:107], v[136:139]\n\tds_read_b64 a[2:3], v0 offset:4040\n\tv_mfma_f32_16x16x32_bf16 v[140:143], a[224:227], v[108:111], v[140:143]\n\tds_read_b64 a[4:5], v0 offset:3520\n\tv_mfma_f32_16x16x32_bf16 v[144:147], a[224:227], v[112:115], v[144:147]\n\tds_read_b64 a[6:7], v0 offset:3528\n\tv_mfma_f32_16x16x32_bf16 v[148:151], a[224:227], v[116:119], v[148:151]\n\tds_read_b64 a[8:9], v0 offset:3008\n\t" \
    "v_mfma_f32_16x16x32_bf16 v[152:155], a[224:227], v[120:123], v[152:155]\n\tds_read_b64 a[10:11], v0 offset:3016\n\tv_mfma_f32_16x16x32_bf16 v[156:159], a[224:227], v[124:127], v[156:159]\n\tds_read_b64 a[12:13], v0 offset:2496\n\tv_mfma_f32_16x16x32_bf16 v[160:163], a[224:227], v[128:131], v[160:163]\n\tds_read_b64 a[14:15], v0 offset:2504\n\tv_mfma_f32_16x16x32_bf16 v[136:139], a[228:231], v[100:103], v[136:139]\n\tds_read_b64 a[16:17], v0 offset:1984\n\t" \
    "v_mfma_f32_16x16x32_bf16 v[140:143], a[228:231], v[104:107], v[140:143]\n\tds_read_b64 a[18:19], v0 offset:1992\n\tv_mfma_f32_16x16x32_bf16 v[144:147], a[228:231], v[108:111], v[144:147]\n\tds_read_b64 a[20:21], v0 offset:1472\n\tv_mfma_f32_16x16x32_bf16 v[148:151], a[228:231], v[112:115], v[148:151]\n\tds_read_b64 a[22:23], v0 offset:1480\n\tv_mfma_f32_16x16x32_bf16 v[152:155], a[228:231], v[116:119], v[152:155]\n\tds_read_b64 a[24:25], v0 offset:960\n\t" \
    "v_mfma_f32_16x16x32_bf16 v[156:159], a[228:231], v[120:123], v[156:159]\n\tds_read_b64 a[26:27], v0 offset:968\n\tv_mfma_f32_16x16x32_bf16 v[160:163], a[228:231], v[124:127], v[160:163]\n\tds_read_b64 a[28:29], v0 offset:448\n\tv_mfma_f32_16x16x32_bf16 v[140:143], a[232:235], v[100:103], v[140:143]\n\tds_read_b64 a[30:31], v0 offset:456\n\tv_mfma_f32_16x16x32_bf16 v[144:147], a[232:235], v[104:107], v[144:147]\n\tds_read_b128 v[68:71], v1 offset:55936\n\t" \
    "v_mfma_f32_16x16x32_bf16 v[148:151], a[232:235], v[108:111], v[148:151]\n\tds_read_b128 v[72:75], v1 offset:56448\n\tv_mfma_f32_16x16x32_bf16 v[152:155], a[232:235], v[112:115], v[152:155]\n\tds_read_b128 v[76:79], v1 offset:56960\n\tv_mfma_f32_16x16x32_bf16 v[156:159], a[232:235], v[116:119], v[156:159]\n\tds_read_b128 v[80:83], v1 offset:57472\n\tv_mfma_f32_16x16x32_bf16 v[160:163], a[232:235], v[120:123], v[160:163]\n\tds_read_b128 v[84:87], v1 offset:57984\n\t" \
    "v_mfma_f32_16x16x32_bf16 v[144:147], a[236:239], v[100:103], v[144:147]\n\tds_read_b128 v[88:91], v1 offset:58496\n\tv_mfma_f32_16x16x32_bf16 v[148:151], a[236:239], v[104:107], v[148:151]\n\tds_read_b128 v[92:95], v1 offset:59008\n\tv_mfma_f32_16x16x32_bf16 v[152:155], a[236:239], v[108:111], v[152:155]\n\tds_read_b128 v[96:99], v1 offset:59520\n\tv_mfma_f32_16x16x32_bf16 v[156:159], a[236:239], v[112:115], v[156:159]\n\tv_mfma_f32_16x16x32_bf16 v[160:163], a[236:239], v[116:119], v[160:163]\n\t" \
    "v_mfma_f32_16x16x32_bf16 v[148:151], a[240:243], v[100:103], v[148:151]\n\tv_mfma_f32_16x16x32_bf16 v[152:155], a[240:243], v[104:107], v[152:155]\n\tv_mfma_f32_16x16x32_bf16 v[156:159], a[240:243], v[108:111], v[156:159]\n\tv_mfma_f32_16x16x32_bf16 v[160:163], a[240:243], v[112:115], v[160:163]\n\tv_mfma_f32_16x16x32_bf16 v[152:155], a[244:247], v[100:103], v[152:155]\n\tv_mfma_f32_16x16x32_bf16 v[156:159], a[244:247], v[104:107], v[156:159]\n\tv_mfma_f32_16x16x32_bf16 v[160:163], a[244:247], v[108:111], v[160:163]\n\tv_mfma_f32_16x16x32_bf16 v[156:159], a[248:251], v[100:103], v[156:159]\n\t" \
    "v_mfma_f32_16x16x32_bf16 v[160:163], a[248:251], v[104:107], v[160:163]\n\tv_mfma_f32_16x16x32_bf16 v[160:163], a[252:255], v[100:103], v[160:163]\n\ts_waitcnt lgkmcnt(0)\n\tv_mfma_f32_16x16x32_bf16 v[164:167], v[228:231], v[68:71], 0\n\tds_read_b64 a[32:33], v0 offset:3968\n\tds_read_b64 a[34:35], v0 offset:3976\n\tv_mfma_f32_16x16x32_bf16 v[168:171], v[228:231], v[72:75], 0\n\tds_read_b64 a[36:37], v0 offset:3456\n\t" \
    "ds_read_b64 a[38:39], v0 offset:3464\n\tv_mfma_f32_16x16x32_bf16 v[172:175], v[228:231], v[76:79], 0\n\tds_read_b64 a[40:41], v0 offset:2944\n\tds_read_b64 a[42:43], v0 offset:2952\n\tv_mfma_f32_16x16x32_bf16 v[176:179], v[228:231], v[80:83], 0\n\tds_read_b64 a[44:45], v0 offset:2432\n\tds_read_b64 a[46:47], v0 offset:2440\n\tv_mfma_f32_16x16x32_bf16 v[180:183], v[228:231], v[84:87], 0\n\t" \
    "ds_read_b64 a[48:49], v0 offset:1920\n\tds_read_b64 a[50:51], v0 offset:1928\n\tv_mfma_f32_16x16x32_bf16 v[184:187], v[228:231], v[88:91], 0\n\tds_read_b64 a[52:53], v0 offset:1408\n\tv_cvt_pk_bf16_f32 v132, v132, v133\n\tv_mfma_f32_16x16x32_bf16 v[188:191], v[228:231], v[92:95], 0\n\tds_read_b64 a[54:55], v0 offset:1416\n\tv_cvt_pk_bf16_f32 v133, v134, v135\n\t" \
    "v_mfma_f32_16x16x32_bf16 v[192:195], v[228:231], v[96:99], 0\n\tds_read_b64 a[56:57], v0 offset:896\n\tds_write_b64 v3, v[132:133] offset:36992\n\tv_mfma_f32_16x16x32_bf16 v[164:167], a[0:3], v[68:71], v[164:167]\n\tds_read_b64 a[58:59], v0 offset:904\n\tv_cvt_pk_bf16_f32 v136, v136, v137\n\tv_mfma_f32_16x16x32_bf16 v[168:171], a[0:3], v[72:75], v[168:171]\n\tds_read_b64 a[60:61], v0 offset:384\n\t" \
    "v_cvt_pk_bf16_f32 v137, v138, v139\n\tv_mfma_f32_16x16x32_bf16 v[172:175], a[0:3], v[76:79], v[172:175]\n\tds_read_b64 a[62:63], v0 offset:392\n\tds_write_b64 v3, v[136:137] offset:37504\n\tv_mfma_f32_16x16x32_bf16 v[176:179], a[0:3], v[80:83], v[176:179]\n\tds_read_b128 v[100:103], v1 offset:55872\n\tv_cvt_pk_bf16_f32 v140, v140, v141\n\tv_mfma_f32_16x16x32_bf16 v[180:183], a[0:3], v[84:87], v[180:183]\n\t" \
    "ds_read_b128 v[104:107], v1 offset:56384\n\tv_cvt_pk_bf16_f32 v141, v142, v143\n\tv_mfma_f32_16x16x32_bf16 v[184:187], a[0:3], v[88:91], v[184:187]\n\tds_read_b128 v[108:111], v1 offset:56896\n\tds_write_b64 v3, v[140:141] offset:38016\n\tv_mfma_f32_16x16x32_bf16 v[188:191], a[0:3], v[92:95], v[188:191]\n\tds_read_b128 v[112:115], v1 offset:57408\n\tv_cvt_pk_bf16_f32 v144, v144, v145\n\t" \
    "v_mfma_f32_16x16x32_bf16 v[192:195], a[0:3], v[96:99], v[192:195]\n\tds_read_b128 v[116:119], v1 offset:57920\n\tv_cvt_pk_bf16_f32 v145, v146, v147\n\tv_mfma_f32_16x16x32_bf16 v[168:171], a[4:7], v[68:71], v[168:171]\n\tds_read_b128 v[120:123], v1 offset:58432\n\tds_write_b64 v3, v[144:145] offset:38528\n\tv_mfma_f32_16x16x32_bf16 v[172:175], a[4:7], v[72:75], v[172:175]\n\tds_read_b128 v[124:127], v1 offset:58944\n\t" \
    "v_cvt_pk_bf16_f32 v148, v148, v149\n\tv_mfma_f32_16x16x32_bf16 v[176:179], a[4:7], v[76:79], v[176:179]\n\tds_read_b128 v[128:131], v1 offset:59456\n\tv_cvt_pk_bf16_f32 v149, v150, v151\n\tv_mfma_f32_16x16x32_bf16 v[180:183], a[4:7], v[80:83], v[180:183]\n\tds_read_b64 a[64:65], v0 offset:3904\n\tds_write_b64 v3, v[148:149] offset:39040\n\tv_mfma_f32_16x16x32_bf16 v[184:187], a[4:7], v[84:87], v[184:187]\n\t" \
    "ds_read_b64 a[66:67], v0 offset:3912\n\tv_cvt_pk_bf16_f32 v152, v152, v153\n\tv_mfma_f32_16x16x32_bf16 v[188:191], a[4:7], v[88:91], v[188:191]\n\tds_read_b64 a[68:69], v0 offset:3392\n\tv_cvt_pk_bf16_f32 v153, v154, v155\n\tv_mfma_f32_16x16x32_bf16 v[192:195], a[4:7], v[92:95], v[192:195]\n\tbuffer_load_dword v15, v7, s[16:19], s42 offen\n\tds_read_b64 a[70:71], v0 offset:3400\n\t" \
    "v_mfma_f32_16x16x32_bf16 v[172:175], a[8:11], v[68:71], v[172:175]\n\ts_add_u32 s43, s43, 1\n\tds_read_b64 a[72:73], v0 offset:2880\n\tds_write_b64 v3, v[152:153] offset:39552\n\tv_mfma_f32_16x16x32_bf16 v[176:179], a[8:11], v[72:75], v[176:179]\n\tv_readlane_b32 s42, v237, s43\n\ts_nop 4\n\tds_read_b64 a[74:75], v0 offset:2888\n\t" \
    "v_cvt_pk_bf16_f32 v156, v156, v157\n\tv_mfma_f32_16x16x32_bf16 v[180:183], a[8:11], v[76:79], v[180:183]\n\tds_read_b64 a[76:77], v0 offset:2368\n\tv_cvt_pk_bf16_f32 v157, v158, v159\n\tv_mfma_f32_16x16x32_bf16 v[184:187], a[8:11], v[80:83], v[184:187]\n\tds_read_b64 a[78:79], v0 offset:2376\n\tds_write_b64 v3, v[156:157] offset:40064\n\tv_mfma_f32_16x16x32_bf16 v[188:191], a[8:11], v[84:87], v[188:191]\n\t" \
    "ds_read_b64 a[80:81], v0 offset:1856\n\tv_cvt_pk_bf16_f32 v160, v160, v161\n\tv_mfma_f32_16x16x32_bf16 v[192:195], a[8:11], v[88:91], v[192:195]\n\tds_read_b64 a[82:83], v0 offset:1864\n\tv_cvt_pk_bf16_f32 v161, v162, v163\n\tv_mfma_f32_16x16x32_bf16 v[176:179], a[12:15], v[68:71], v[176:179]\n\tds_read_b64 a[84:85], v0 offset:1344\n\tds_write_b64 v3, v[160:161] offset:40576\n\t" \
    "v_mfma_f32_16x16x32_bf16 v[180:183], a[12:15], v[72:75], v[180:183]\n\tds_read_b64 a[86:87], v0 offset:1352\n\tv_mfma_f32_16x16x32_bf16 v[184:187], a[12:15], v[76:79], v[184:187]\n\tds_read_b64 a[88:89], v0 offset:832\n\tv_mfma_f32_16x16x32_bf16 v[188:191], a[12:15], v[80:83], v[188:191]\n\tds_read_b64 a[90:91], v0 offset:840\n\tv_mfma_f32_16x16x32_bf16 v[192:195], a[12:15], v[84:87], v[192:195]\n\tds_read_b64 a[92:93], v0 offset:320\n\t" \
    "v_mfma_f32_16x16x32_bf16 v[180:183], a[16:19], v[68:71], v[180:183]\n\tds_read_b64 a[94:95], v0 offset:328\n\tv_mfma_f32_16x16x32_bf16 v[184:187], a[16:19], v[72:75], v[184:187]\n\tds_read_b64 a[96:97], v0 offset:3840\n\tv_mfma_f32_16x16x32_bf16 v[188:191], a[16:19], v[76:79], v[188:191]\n\tds_read_b64 a[98:99], v0 offset:3848\n\tv_mfma_f32_16x16x32_bf16 v[192:195], a[16:19], v[80:83], v[192:195]\n\tds_read_b64 a[100:101], v0 offset:3328\n\t" \
    "v_mfma_f32_16x16x32_bf16 v[184:187], a[20:23], v[68:71], v[184:187]\n\tds_read_b64 a[102:103], v0 offset:3336\n\tv_mfma_f32_16x16x32_bf16 v[188:191], a[20:23], v[72:75], v[188:191]\n\tv_mfma_f32_16x16x32_bf16 v[192:195], a[20:23], v[76:79], v[192:195]\n\tv_mfma_f32_16x16x32_bf16 v[188:191], a[24:27], v[68:71], v[188:191]\n\tv_mfma_f32_16x16x32_bf16 v[192:195], a[24:27], v[72:75], v[192:195]\n\tv_mfma_f32_16x16x32_bf16 v[192:195], a[28:31], v[68:71], v[192:195]\n\ts_waitcnt lgkmcnt(9)\n\t" \
    "s_barrier\n\tv_mfma_f32_16x16x32_bf16 v[164:167], a[32:35], v[100:103], v[164:167]\n\tds_read_b64 a[104:105], v0 offset:2816\n\tds_read_b64 a[106:107], v0 offset:2824\n\tv_mfma_f32_16x16x32_bf16 v[168:171], a[32:35], v[104:107], v[168:171]\n\tds_read_b64 a[108:109], v0 offset:2304\n\tds_read_b64 a[110:111], v0 offset:2312\n\tv_mfma_f32_16x16x32_bf16 v[172:175], a[32:35], v[108:111], v[172:175]\n\t" \
    "ds_read_b64 a[112:113], v0 offset:1792\n\tds_read_b64 a[114:115], v0 offset:1800\n\tv_mfma_f32_16x16x32_bf16 v[176:179], a[32:35], v[112:115], v[176:179]\n\tds_read_b64 a[116:117], v0 offset:1280\n\tds_read_b64 a[118:119], v0 offset:1288\n\tv_mfma_f32_16x16x32_bf16 v[180:183], a[32:35], v[116:119], v[180:183]\n\tds_read_b64 a[120:121], v0 offset:768\n\tds_read_b64 a[122:123], v0 offset:776\n\t" \
    "v_mfma_f32_16x16x32_bf16 v[184:187], a[32:35], v[120:123], v[184:187]\n\tds_read_b64 a[124:125], v0 offset:256\n\ts_add_u32 s51, s44, 8\n\tv_mfma_f32_16x16x32_bf16 v[188:191], a[32:35], v[124:127], v[188:191]\n\tds_read_b64 a[126:127], v0 offset:264\n\ts_mul_i32 s65, s51, 1366\n\tv_mfma_f32_16x16x32_bf16 v[192:195], a[32:35], v[128:131], v[192:195]\n\tds_read_b64 a[128:129], v0 offset:3776\n\t" \
    "s_lshr_b32 s65, s65, 16\n\tv_mfma_f32_16x16x32_bf16 v[168:171], a[36:39], v[100:103], v[168:171]\n\tds_read_b64 a[130:131], v0 offset:3784\n\ts_mul_i32 s56, s65, 48\n\tv_mfma_f32_16x16x32_bf16 v[172:175], a[36:39], v[104:107], v[172:175]\n\tds_read_b64 a[132:133], v0 offset:3264\n\ts_sub_u32 s56, s51, s56\n\tv_mfma_f32_16x16x32_bf16 v[176:179], a[36:39], v[108:111], v[176:179]\n\t" \
    "ds_read_b64 a[134:135], v0 offset:3272\n\ts_lshl_b32 s56, s56, 6\n\tv_mfma_f32_16x16x32_bf16 v[180:183], a[36:39], v[112:115], v[180:183]\n\tds_read_b128 v[68:71], v1 offset:55808\n\ts_lshr_b32 s70, s65, 2\n\tv_mfma_f32_16x16x32_bf16 v[184:187], a[36:39], v[116:119], v[184:187]\n\tds_read_b128 v[72:75], v1 offset:56320\n\ts_mul_i32 s70, s70, 252\n\t" \
    "v_mfma_f32_16x16x32_bf16 v[188:191], a[36:39], v[120:123], v[188:191]\n\tds_read_b128 v[76:79], v1 offset:56832\n\ts_add_u32 s65, s65, s70\n\tv_mfma_f32_16x16x32_bf16 v[192:195], a[36:39], v[124:127], v[192:195]\n\tds_read_b128 v[80:83], v1 offset:57344\n\ts_mul_i32 s65, s65, 3072\n\tv_mfma_f32_16x16x32_bf16 v[172:175], a[40:43], v[100:103], v[172:175]\n\tds_read_b128 v[84:87], v1 offset:57856\n\t" \
    "s_add_u32 s56, s56, s65\n\tv_mfma_f32_16x16x32_bf16 v[176:179], a[40:43], v[104:107], v[176:179]\n\tds_read_b128 v[88:91], v1 offset:58368\n\ts_add_u32 s56, s56, 4849664\n\tv_mfma_f32_16x16x32_bf16 v[180:183], a[40:43], v[108:111], v[180:183]\n\tds_read_b128 v[92:95], v1 offset:58880\n\ts_add_u32 s51, s51, s38\n\tv_mfma_f32_16x16x32_bf16 v[184:187], a[40:43], v[112:115], v[184:187]\n\t" \
    "ds_read_b128 v[96:99], v1 offset:59392\n\ts_lshl_b32 s51, s51, 3\n\tv_mfma_f32_16x16x32_bf16 v[188:191], a[40:43], v[116:119], v[188:191]\n\tds_read_b64 a[136:137], v0 offset:2752\n\ts_add_u32 s51, s51, 50176\n\tds_read_b64_tr_b16 v[204:205], v4 offset:36992\n\tv_mfma_f32_16x16x32_bf16 v[192:195], a[40:43], v[120:123], v[192:195]\n\tds_read_b64 a[138:139], v0 offset:2760\n\t" \
    "ds_read_b64_tr_b16 v[206:207], v4 offset:37000\n\tv_mfma_f32_16x16x32_bf16 v[176:179], a[44:47], v[100:103], v[176:179]\n\tds_read_b64 a[140:141], v0 offset:2240\n\tds_read_b64_tr_b16 v[208:209], v4 offset:37056\n\tv_mfma_f32_16x16x32_bf16 v[180:183], a[44:47], v[104:107], v[180:183]\n\tds_read_b64 a[142:143], v0 offset:2248\n\tds_read_b64_tr_b16 v[210:211], v4 offset:37064\n\tv_mfma_f32_16x16x32_bf16 v[184:187], a[44:47], v[108:111], v[184:187]\n\t" \
    "ds_read_b64 a[144:145], v0 offset:1728\n\tds_read_b64_tr_b16 v[212:213], v4 offset:37504\n\tv_mfma_f32_16x16x32_bf16 v[188:191], a[44:47], v[112:115], v[188:191]\n\tds_read_b64 a[146:147], v0 offset:1736\n\tds_read_b64_tr_b16 v[214:215], v4 offset:37512\n\tv_mfma_f32_16x16x32_bf16 v[192:195], a[44:47], v[116:119], v[192:195]\n\tds_read_b64 a[148:149], v0 offset:1216\n\tds_read_b64_tr_b16 v[216:217], v4 offset:37568\n\t" \
    "v_mfma_f32_16x16x32_bf16 v[180:183], a[48:51], v[100:103], v[180:183]\n\tds_read_b64 a[150:151], v0 offset:1224\n\tds_read_b64_tr_b16 v[218:219], v4 offset:37576\n\tv_mfma_f32_16x16x32_bf16 v[184:187], a[48:51], v[104:107], v[184:187]\n\tds_read_b64 a[152:153], v0 offset:704\n\tv_mfma_f32_16x16x32_bf16 v[188:191], a[48:51], v[108:111], v[188:191]\n\tds_read_b64 a[154:155], v0 offset:712\n\tv_mfma_f32_16x16x32_bf16 v[192:195], a[48:51], v[112:115], v[192:195]\n\t" \
    "ds_read_b64 a[156:157], v0 offset:192\n\tv_mfma_f32_16x16x32_bf16 v[184:187], a[52:55], v[100:103], v[184:187]\n\tds_read_b64 a[158:159], v0 offset:200\n\tv_mfma_f32_16x16x32_bf16 v[188:191], a[52:55], v[104:107], v[188:191]\n\tv_mfma_f32_16x16x32_bf16 v[192:195], a[52:55], v[108:111], v[192:195]\n\ts_waitcnt lgkmcnt(15)\n\tbuffer_store_dwordx4 v[204:207], v5, s[8:11], s56 offen sc1\n\ts_add_u32 s56, s56, 49152\n\t" \
    "v_mfma_f32_16x16x32_bf16 v[188:191], a[56:59], v[100:103], v[188:191]\n\tv_mfma_f32_16x16x32_bf16 v[192:195], a[56:59], v[104:107], v[192:195]\n\tv_mfma_f32_16x16x32_bf16 v[192:195], a[60:63], v[100:103], v[192:195]\n\tv_mfma_f32_16x16x32_bf16 v[164:167], a[64:67], v[68:71], v[164:167]\n\tds_read_b64 a[160:161], v0 offset:3712\n\tds_read_b64 a[162:163], v0 offset:3720\n\tv_mfma_f32_16x16x32_bf16 v[168:171], a[64:67], v[72:75], v[168:171]\n\tds_read_b64 a[164:165], v0 offset:3200\n\t" \
    "ds_read_b64 a[166:167], v0 offset:3208\n\tv_mfma_f32_16x16x32_bf16 v[172:175], a[64:67], v[76:79], v[172:175]\n\tds_read_b64 a[168:169], v0 offset:2688\n\tds_read_b64 a[170:171], v0 offset:2696\n\tv_mfma_f32_16x16x32_bf16 v[176:179], a[64:67], v[80:83], v[176:179]\n\tds_read_b64 a[172:173], v0 offset:2176\n\tds_read_b64 a[174:175], v0 offset:2184\n\tv_mfma_f32_16x16x32_bf16 v[180:183], a[64:67], v[84:87], v[180:183]\n\t" \
    "ds_read_b64 a[176:177], v0 offset:1664\n\tds_read_b64 a[178:179], v0 offset:1672\n\tv_mfma_f32_16x16x32_bf16 v[184:187], a[64:67], v[88:91], v[184:187]\n\tds_read_b64 a[180:181], v0 offset:1152\n\tv_mfma_f32_16x16x32_bf16 v[188:191], a[64:67], v[92:95], v[188:191]\n\tds_read_b64 a[182:183], v0 offset:1160\n\tv_mfma_f32_16x16x32_bf16 v[192:195], a[64:67], v[96:99], v[192:195]\n\tds_read_b64 a[184:185], v0 offset:640\n\t" \
    "v_mfma_f32_16x16x32_bf16 v[168:171], a[68:71], v[68:71], v[168:171]\n\tds_read_b64 a[186:187], v0 offset:648\n\tv_mfma_f32_16x16x32_bf16 v[172:175], a[68:71], v[72:75], v[172:175]\n\tds_read_b64 a[188:189], v0 offset:128\n\tv_mfma_f32_16x16x32_bf16 v[176:179], a[68:71], v[76:79], v[176:179]\n\tds_read_b64 a[190:191], v0 offset:136\n\tv_mfma_f32_16x16x32_bf16 v[180:183], a[68:71], v[80:83], v[180:183]\n\tds_read_b128 v[100:103], v1 offset:55744\n\t" \
    "v_mfma_f32_16x16x32_bf16 v[184:187], a[68:71], v[84:87], v[184:187]\n\tds_read_b128 v[104:107], v1 offset:56256\n\tv_mfma_f32_16x16x32_bf16 v[188:191], a[68:71], v[88:91], v[188:191]\n\tds_read_b128 v[108:111], v1 offset:56768\n\tv_mfma_f32_16x16x32_bf16 v[192:195], a[68:71], v[92:95], v[192:195]\n\tds_read_b128 v[112:115], v1 offset:57280\n\ts_waitcnt lgkmcnt(15)\n\tbuffer_store_dwordx4 v[208:211], v5, s[8:11], s56 offen sc1\n\t" \
    "v_mfma_f32_16x16x32_bf16 v[172:175], a[72:75], v[68:71], v[172:175]\n\tds_read_b128 v[116:119], v1 offset:57792\n\ts_add_u32 s56, s56, 49152\n\tv_mfma_f32_16x16x32_bf16 v[176:179], a[72:75], v[72:75], v[176:179]\n\tds_read_b128 v[120:123], v1 offset:58304\n\tv_mfma_f32_16x16x32_bf16 v[180:183], a[72:75], v[76:79], v[180:183]\n\tds_read_b128 v[124:127], v1 offset:58816\n\tv_mfma_f32_16x16x32_bf16 v[184:187], a[72:75], v[80:83], v[184:187]\n\t" \
    "ds_read_b128 v[128:131], v1 offset:59328\n\tv_mfma_f32_16x16x32_bf16 v[188:191], a[72:75], v[84:87], v[188:191]\n\tds_read_b64 a[192:193], v0 offset:3648\n\tv_mfma_f32_16x16x32_bf16 v[192:195], a[72:75], v[88:91], v[192:195]\n\tds_read_b64 a[194:195], v0 offset:3656\n\tv_mfma_f32_16x16x32_bf16 v[176:179], a[76:79], v[68:71], v[176:179]\n\tds_read_b64 a[196:197], v0 offset:3136\n\tv_mfma_f32_16x16x32_bf16 v[180:183], a[76:79], v[72:75], v[180:183]\n\t" \
    "ds_read_b64 a[198:199], v0 offset:3144\n\tv_mfma_f32_16x16x32_bf16 v[184:187], a[76:79], v[76:79], v[184:187]\n\tds_read_b64 a[200:201], v0 offset:2624\n\tv_mfma_f32_16x16x32_bf16 v[188:191], a[76:79], v[80:83], v[188:191]\n\tds_read_b64 a[202:203], v0 offset:2632\n\tv_mfma_f32_16x16x32_bf16 v[192:195], a[76:79], v[84:87], v[192:195]\n\tds_read_b64 a[204:205], v0 offset:2112\n\tv_mfma_f32_16x16x32_bf16 v[180:183], a[80:83], v[68:71], v[180:183]\n\t" \
    "ds_read_b64 a[206:207], v0 offset:2120\n\tv_mfma_f32_16x16x32_bf16 v[184:187], a[80:83], v[72:75], v[184:187]\n\tds_read_b64 a[208:209], v0 offset:1600\n\tv_mfma_f32_16x16x32_bf16 v[188:191], a[80:83], v[76:79], v[188:191]\n\tds_read_b64 a[210:211], v0 offset:1608\n\tv_mfma_f32_16x16x32_bf16 v[192:195], a[80:83], v[80:83], v[192:195]\n\tds_read_b64 a[212:213], v0 offset:1088\n\tv_mfma_f32_16x16x32_bf16 v[184:187], a[84:87], v[68:71], v[184:187]\n\t" \
    "ds_read_b64 a[214:215], v0 offset:1096\n\tv_mfma_f32_16x16x32_bf16 v[188:191], a[84:87], v[72:75], v[188:191]\n\tbuffer_store_dwordx4 v[212:215], v5, s[8:11], s56 offen sc1\n\ts_add_u32 s56, s56, 49152\n\tv_mfma_f32_16x16x32_bf16 v[192:195], a[84:87], v[76:79], v[192:195]\n\tv_mfma_f32_16x16x32_bf16 v[188:191], a[88:91], v[68:71], v[188:191]\n\tv_mfma_f32_16x16x32_bf16 v[192:195], a[88:91], v[72:75], v[192:195]\n\tv_mfma_f32_16x16x32_bf16 v[192:195], a[92:95], v[68:71], v[192:195]\n\t" \
    "s_waitcnt lgkmcnt(12)\n\tv_mfma_f32_16x16x32_bf16 v[164:167], a[96:99], v[100:103], v[164:167]\n\tds_read_b64 a[216:217], v0 offset:576\n\tv_mfma_f32_16x16x32_bf16 v[168:171], a[96:99], v[104:107], v[168:171]\n\tds_read_b64 a[218:219], v0 offset:584\n\tv_mfma_f32_16x16x32_bf16 v[172:175], a[96:99], v[108:111], v[172:175]\n\tds_read_b64 a[220:221], v0 offset:64\n\tv_mfma_f32_16x16x32_bf16 v[176:179], a[96:99], v[112:115], v[176:179]\n\t" \
    "ds_read_b64 a[222:223], v0 offset:72\n\tv_mfma_f32_16x16x32_bf16 v[180:183], a[96:99], v[116:119], v[180:183]\n\tds_read_b64 a[224:225], v0 offset:3584\n\tv_mfma_f32_16x16x32_bf16 v[184:187], a[96:99], v[120:123], v[184:187]\n\tds_read_b64 a[226:227], v0 offset:3592\n\tv_mfma_f32_16x16x32_bf16 v[188:191], a[96:99], v[124:127], v[188:191]\n\tds_read_b64 a[228:229], v0 offset:3072\n\tv_mfma_f32_16x16x32_bf16 v[192:195], a[96:99], v[128:131], v[192:195]\n\t" \
    "ds_read_b64 a[230:231], v0 offset:3080\n\tbuffer_store_dwordx4 v[216:219], v5, s[8:11], s56 offen sc1\n\tv_mfma_f32_16x16x32_bf16 v[168:171], a[100:103], v[100:103], v[168:171]\n\tds_read_b64 a[232:233], v0 offset:2560\n\ts_add_u32 s56, s56, 49152\n\tv_mfma_f32_16x16x32_bf16 v[172:175], a[100:103], v[104:107], v[172:175]\n\tds_read_b64 a[234:235], v0 offset:2568\n\tv_mfma_f32_16x16x32_bf16 v[176:179], a[100:103], v[108:111], v[176:179]\n\t" \
    "ds_read_b64 a[236:237], v0 offset:2048\n\tv_mfma_f32_16x16x32_bf16 v[180:183], a[100:103], v[112:115], v[180:183]\n\tds_read_b64 a[238:239], v0 offset:2056\n\tv_mfma_f32_16x16x32_bf16 v[184:187], a[100:103], v[116:119], v[184:187]\n\tds_read_b64 a[240:241], v0 offset:1536\n\tv_mfma_f32_16x16x32_bf16 v[188:191], a[100:103], v[120:123], v[188:191]\n\tds_read_b64 a[242:243], v0 offset:1544\n\tv_mfma_f32_16x16x32_bf16 v[192:195], a[100:103], v[124:127], v[192:195]\n\t" \
    "ds_read_b64 a[244:245], v0 offset:1024\n\tv_mfma_f32_16x16x32_bf16 v[172:175], a[104:107], v[100:103], v[172:175]\n\tds_read_b64 a[246:247], v0 offset:1032\n\tv_mfma_f32_16x16x32_bf16 v[176:179], a[104:107], v[104:107], v[176:179]\n\tds_read_b128 v[68:71], v1 offset:55680\n\tv_mfma_f32_16x16x32_bf16 v[180:183], a[104:107], v[108:111], v[180:183]\n\tds_read_b128 v[72:75], v1 offset:56192\n\tv_mfma_f32_16x16x32_bf16 v[184:187], a[104:107], v[112:115], v[184:187]\n\t" \
    "ds_read_b128 v[76:79], v1 offset:56704\n\tv_mfma_f32_16x16x32_bf16 v[188:191], a[104:107], v[116:119], v[188:191]\n\tds_read_b128 v[80:83], v1 offset:57216\n\tv_mfma_f32_16x16x32_bf16 v[192:195], a[104:107], v[120:123], v[192:195]\n\tds_read_b128 v[84:87], v1 offset:57728\n\tv_mfma_f32_16x16x32_bf16 v[176:179], a[108:111], v[100:103], v[176:179]\n\tds_read_b128 v[88:91], v1 offset:58240\n\tv_mfma_f32_16x16x32_bf16 v[180:183], a[108:111], v[104:107], v[180:183]\n\t" \
    "ds_read_b128 v[92:95], v1 offset:58752\n\tv_mfma_f32_16x16x32_bf16 v[184:187], a[108:111], v[108:111], v[184:187]\n\tds_read_b128 v[96:99], v1 offset:59264\n\tv_mfma_f32_16x16x32_bf16 v[188:191], a[108:111], v[112:115], v[188:191]\n\tds_read_b64 a[248:249], v0 offset:512\n\tv_mfma_f32_16x16x32_bf16 v[192:195], a[108:111], v[116:119], v[192:195]\n\tds_read_b64 a[250:251], v0 offset:520\n\tv_mfma_f32_16x16x32_bf16 v[180:183], a[112:115], v[100:103], v[180:183]\n\t" \
    "ds_read_b64 a[252:253], v0 offset:0\n\tv_mfma_f32_16x16x32_bf16 v[184:187], a[112:115], v[104:107], v[184:187]\n\tds_read_b64 a[254:255], v0 offset:8\n\tv_mfma_f32_16x16x32_bf16 v[188:191], a[112:115], v[108:111], v[188:191]\n\tv_mfma_f32_16x16x32_bf16 v[192:195], a[112:115], v[112:115], v[192:195]\n\tv_mfma_f32_16x16x32_bf16 v[184:187], a[116:119], v[100:103], v[184:187]\n\tv_mfma_f32_16x16x32_bf16 v[188:191], a[116:119], v[104:107], v[188:191]\n\tv_mfma_f32_16x16x32_bf16 v[192:195], a[116:119], v[108:111], v[192:195]\n\t" \
    "v_mfma_f32_16x16x32_bf16 v[188:191], a[120:123], v[100:103], v[188:191]\n\tv_mfma_f32_16x16x32_bf16 v[192:195], a[120:123], v[104:107], v[192:195]\n\tv_mfma_f32_16x16x32_bf16 v[192:195], a[124:127], v[100:103], v[192:195]\n\ts_waitcnt lgkmcnt(4)\n\tv_mfma_f32_16x16x32_bf16 v[164:167], a[128:131], v[68:71], v[164:167]\n\tds_read_b128 v[100:103], v1 offset:55616\n\tv_mfma_f32_16x16x32_bf16 v[168:171], a[128:131], v[72:75], v[168:171]\n\tds_read_b128 v[104:107], v1 offset:56128\n\t" \
    "v_mfma_f32_16x16x32_bf16 v[172:175], a[128:131], v[76:79], v[172:175]\n\tds_read_b128 v[108:111], v1 offset:56640\n\tv_mfma_f32_16x16x32_bf16 v[176:179], a[128:131], v[80:83], v[176:179]\n\tds_read_b128 v[112:115], v1 offset:57152\n\tv_mfma_f32_16x16x32_bf16 v[180:183], a[128:131], v[84:87], v[180:183]\n\tds_read_b128 v[116:119], v1 offset:57664\n\tv_mfma_f32_16x16x32_bf16 v[184:187], a[128:131], v[88:91], v[184:187]\n\tds_read_b128 v[120:123], v1 offset:58176\n\t" \
    "v_mfma_f32_16x16x32_bf16 v[188:191], a[128:131], v[92:95], v[188:191]\n\tds_read_b128 v[124:127], v1 offset:58688\n\tv_mfma_f32_16x16x32_bf16 v[192:195], a[128:131], v[96:99], v[192:195]\n\tds_read_b128 v[128:131], v1 offset:59200\n\tv_mfma_f32_16x16x32_bf16 v[168:171], a[132:135], v[68:71], v[168:171]\n\tv_mfma_f32_16x16x32_bf16 v[172:175], a[132:135], v[72:75], v[172:175]\n\tv_mfma_f32_16x16x32_bf16 v[176:179], a[132:135], v[76:79], v[176:179]\n\tv_mfma_f32_16x16x32_bf16 v[180:183], a[132:135], v[80:83], v[180:183]\n\t" \
    "v_mfma_f32_16x16x32_bf16 v[184:187], a[132:135], v[84:87], v[184:187]\n\tv_mfma_f32_16x16x32_bf16 v[188:191], a[132:135], v[88:91], v[188:191]\n\tv_mfma_f32_16x16x32_bf16 v[192:195], a[132:135], v[92:95], v[192:195]\n\tv_mfma_f32_16x16x32_bf16 v[172:175], a[136:139], v[68:71], v[172:175]\n\tv_mfma_f32_16x16x32_bf16 v[176:179], a[136:139], v[72:75], v[176:179]\n\tv_mfma_f32_16x16x32_bf16 v[180:183], a[136:139], v[76:79], v[180:183]\n\tv_mfma_f32_16x16x32_bf16 v[184:187], a[136:139], v[80:83], v[184:187]\n\tv_mfma_f32_16x16x32_bf16 v[188:191], a[136:139], v[84:87], v[188:191]\n\t" \
    "v_mfma_f32_16x16x32_bf16 v[192:195], a[136:139], v[88:91], v[192:195]\n\tv_mfma_f32_16x16x32_bf16 v[176:179], a[140:143], v[68:71], v[176:179]\n\tv_mfma_f32_16x16x32_bf16 v[180:183], a[140:143], v[72:75], v[180:183]\n\tv_mfma_f32_16x16x32_bf16 v[184:187], a[140:143], v[76:79], v[184:187]\n\tv_mfma_f32_16x16x32_bf16 v[188:191], a[140:143], v[80:83], v[188:191]\n\tv_mfma_f32_16x16x32_bf16 v[192:195], a[140:143], v[84:87], v[192:195]\n\tv_mfma_f32_16x16x32_bf16 v[180:183], a[144:147], v[68:71], v[180:183]\n\tv_mfma_f32_16x16x32_bf16 v[184:187], a[144:147], v[72:75], v[184:187]\n\t" \
    "v_mfma_f32_16x16x32_bf16 v[188:191], a[144:147], v[76:79], v[188:191]\n\tv_mfma_f32_16x16x32_bf16 v[192:195], a[144:147], v[80:83], v[192:195]\n\tv_mfma_f32_16x16x32_bf16 v[184:187], a[148:151], v[68:71], v[184:187]\n\tv_mfma_f32_16x16x32_bf16 v[188:191], a[148:151], v[72:75], v[188:191]\n\tv_mfma_f32_16x16x32_bf16 v[192:195], a[148:151], v[76:79], v[192:195]\n\tv_mfma_f32_16x16x32_bf16 v[188:191], a[152:155], v[68:71], v[188:191]\n\tv_mfma_f32_16x16x32_bf16 v[192:195], a[152:155], v[72:75], v[192:195]\n\tv_mfma_f32_16x16x32_bf16 v[192:195], a[156:159], v[68:71], v[192:195]\n\t" \
    "s_waitcnt lgkmcnt(0)\n\tv_mfma_f32_16x16x32_bf16 v[164:167], a[160:163], v[100:103], v[164:167]\n\tds_read_b128 v[68:71], v1 offset:55552\n\tv_mfma_f32_16x16x32_bf16 v[168:171], a[160:163], v[104:107], v[168:171]\n\tds_read_b128 v[72:75], v1 offset:56064\n\tv_mfma_f32_16x16x32_bf16 v[172:175], a[160:163], v[108:111], v[172:175]\n\tds_read_b128 v[76:79], v1 offset:56576\n\tv_mfma_f32_16x16x32_bf16 v[176:179], a[160:163], v[112:115], v[176:179]\n\t" \
    "ds_read_b128 v[80:83], v1 offset:57088\n\tv_mfma_f32_16x16x32_bf16 v[180:183], a[160:163], v[116:119], v[180:183]\n\tds_read_b128 v[84:87], v1 offset:57600\n\tv_mfma_f32_16x16x32_bf16 v[184:187], a[160:163], v[120:123], v[184:187]\n\tds_read_b128 v[88:91], v1 offset:58112\n\tv_mfma_f32_16x16x32_bf16 v[188:191], a[160:163], v[124:127], v[188:191]\n\tds_read_b128 v[92:95], v1 offset:58624\n\tv_mfma_f32_16x16x32_bf16 v[192:195], a[160:163], v[128:131], v[192:195]\n\t" \
    "ds_read_b128 v[96:99], v1 offset:59136\n\tv_mfma_f32_16x16x32_bf16 v[168:171], a[164:167], v[100:103], v[168:171]\n\tv_mfma_f32_16x16x32_bf16 v[172:175], a[164:167], v[104:107], v[172:175]\n\tv_mfma_f32_16x16x32_bf16 v[176:179], a[164:167], v[108:111], v[176:179]\n\tv_mfma_f32_16x16x32_bf16 v[180:183], a[164:167], v[112:115], v[180:183]\n\tv_mfma_f32_16x16x32_bf16 v[184:187], a[164:167], v[116:119], v[184:187]\n\tv_mfma_f32_16x16x32_bf16 v[188:191], a[164:167], v[120:123], v[188:191]\n\tv_mfma_f32_16x16x32_bf16 v[192:195], a[164:167], v[124:127], v[192:195]\n\t" \
    "v_mfma_f32_16x16x32_bf16 v[172:175], a[168:171], v[100:103], v[172:175]\n\tv_mfma_f32_16x16x32_bf16 v[176:179], a[168:171], v[104:107], v[176:179]\n\tv_mfma_f32_16x16x32_bf16 v[180:183], a[168:171], v[108:111], v[180:183]\n\tv_mfma_f32_16x16x32_bf16 v[184:187], a[168:171], v[112:115], v[184:187]\n\tv_mfma_f32_16x16x32_bf16 v[188:191], a[168:171], v[116:119], v[188:191]\n\tv_mfma_f32_16x16x32_bf16 v[192:195], a[168:171], v[120:123], v[192:195]\n\tv_mfma_f32_16x16x32_bf16 v[176:179], a[172:175], v[100:103], v[176:179]\n\tv_mfma_f32_16x16x32_bf16 v[180:183], a[172:175], v[104:107], v[180:183]\n\t" \
    "v_mfma_f32_16x16x32_bf16 v[184:187], a[172:175], v[108:111], v[184:187]\n\tv_mfma_f32_16x16x32_bf16 v[188:191], a[172:175], v[112:115], v[188:191]\n\tv_mfma_f32_16x16x32_bf16 v[192:195], a[172:175], v[116:119], v[192:195]\n\tv_mfma_f32_16x16x32_bf16 v[180:183], a[176:179], v[100:103], v[180:183]\n\tv_mfma_f32_16x16x32_bf16 v[184:187], a[176:179], v[104:107], v[184:187]\n\tv_mfma_f32_16x16x32_bf16 v[188:191], a[176:179], v[108:111], v[188:191]\n\tv_mfma_f32_16x16x32_bf16 v[192:195], a[176:179], v[112:115], v[192:195]\n\tv_mfma_f32_16x16x32_bf16 v[184:187], a[180:183], v[100:103], v[184:187]\n\t" \
    "v_mfma_f32_16x16x32_bf16 v[188:191], a[180:183], v[104:107], v[188:191]\n\tv_mfma_f32_16x16x32_bf16 v[192:195], a[180:183], v[108:111], v[192:195]\n\tv_mfma_f32_16x16x32_bf16 v[188:191], a[184:187], v[100:103], v[188:191]\n\tv_mfma_f32_16x16x32_bf16 v[192:195], a[184:187], v[104:107], v[192:195]\n\tv_mfma_f32_16x16x32_bf16 v[192:195], a[188:191], v[100:103], v[192:195]\n\ts_waitcnt lgkmcnt(0)\n\tv_mfma_f32_16x16x32_bf16 v[164:167], a[192:195], v[68:71], v[164:167]\n\tds_read_b128 v[100:103], v1 offset:55488\n\t" \
    "v_mfma_f32_16x16x32_bf16 v[168:171], a[192:195], v[72:75], v[168:171]\n\tds_read_b128 v[104:107], v1 offset:56000\n\tv_mfma_f32_16x16x32_bf16 v[172:175], a[192:195], v[76:79], v[172:175]\n\tds_read_b128 v[108:111], v1 offset:56512\n\tv_mfma_f32_16x16x32_bf16 v[176:179], a[192:195], v[80:83], v[176:179]\n\tds_read_b128 v[112:115], v1 offset:57024\n\tv_mfma_f32_16x16x32_bf16 v[180:183], a[192:195], v[84:87], v[180:183]\n\tds_read_b128 v[116:119], v1 offset:57536\n\t" \
    "v_mfma_f32_16x16x32_bf16 v[184:187], a[192:195], v[88:91], v[184:187]\n\tds_read_b128 v[120:123], v1 offset:58048\n\tv_mfma_f32_16x16x32_bf16 v[188:191], a[192:195], v[92:95], v[188:191]\n\tds_read_b128 v[124:127], v1 offset:58560\n\tv_mfma_f32_16x16x32_bf16 v[192:195], a[192:195], v[96:99], v[192:195]\n\tds_read_b128 v[128:131], v1 offset:59072\n\tv_mfma_f32_16x16x32_bf16 v[168:171], a[196:199], v[68:71], v[168:171]\n\tv_mfma_f32_16x16x32_bf16 v[172:175], a[196:199], v[72:75], v[172:175]\n\t" \
    "v_mfma_f32_16x16x32_bf16 v[176:179], a[196:199], v[76:79], v[176:179]\n\tv_mfma_f32_16x16x32_bf16 v[180:183], a[196:199], v[80:83], v[180:183]\n\tv_mfma_f32_16x16x32_bf16 v[184:187], a[196:199], v[84:87], v[184:187]\n\tv_mfma_f32_16x16x32_bf16 v[188:191], a[196:199], v[88:91], v[188:191]\n\tv_mfma_f32_16x16x32_bf16 v[192:195], a[196:199], v[92:95], v[192:195]\n\tv_mfma_f32_16x16x32_bf16 v[172:175], a[200:203], v[68:71], v[172:175]\n\tv_mfma_f32_16x16x32_bf16 v[176:179], a[200:203], v[72:75], v[176:179]\n\tv_mfma_f32_16x16x32_bf16 v[180:183], a[200:203], v[76:79], v[180:183]\n\t" \
    "v_mfma_f32_16x16x32_bf16 v[184:187], a[200:203], v[80:83], v[184:187]\n\tv_mfma_f32_16x16x32_bf16 v[188:191], a[200:203], v[84:87], v[188:191]\n\tv_mfma_f32_16x16x32_bf16 v[192:195], a[200:203], v[88:91], v[192:195]\n\tv_mfma_f32_16x16x32_bf16 v[176:179], a[204:207], v[68:71], v[176:179]\n\tv_mfma_f32_16x16x32_bf16 v[180:183], a[204:207], v[72:75], v[180:183]\n\tv_mfma_f32_16x16x32_bf16 v[184:187], a[204:207], v[76:79], v[184:187]\n\tv_mfma_f32_16x16x32_bf16 v[188:191], a[204:207], v[80:83], v[188:191]\n\tv_mfma_f32_16x16x32_bf16 v[192:195], a[204:207], v[84:87], v[192:195]\n\t" \
    "v_mfma_f32_16x16x32_bf16 v[180:183], a[208:211], v[68:71], v[180:183]\n\tv_mfma_f32_16x16x32_bf16 v[184:187], a[208:211], v[72:75], v[184:187]\n\tv_mfma_f32_16x16x32_bf16 v[188:191], a[208:211], v[76:79], v[188:191]\n\tv_mfma_f32_16x16x32_bf16 v[192:195], a[208:211], v[80:83], v[192:195]\n\tv_mfma_f32_16x16x32_bf16 v[184:187], a[212:215], v[68:71], v[184:187]\n\tv_mfma_f32_16x16x32_bf16 v[188:191], a[212:215], v[72:75], v[188:191]\n\tv_mfma_f32_16x16x32_bf16 v[192:195], a[212:215], v[76:79], v[192:195]\n\tv_mfma_f32_16x16x32_bf16 v[188:191], a[216:219], v[68:71], v[188:191]\n\t" \
    "v_mfma_f32_16x16x32_bf16 v[192:195], a[216:219], v[72:75], v[192:195]\n\tv_mfma_f32_16x16x32_bf16 v[192:195], a[220:223], v[68:71], v[192:195]\n\ts_waitcnt lgkmcnt(0)\n\tv_mfma_f32_16x16x32_bf16 v[164:167], a[224:227], v[100:103], v[164:167]\n\ts_waitcnt vmcnt(0)\n\tbuffer_store_dwordx2 v[248:249], off, s[8:11], s51 sc1\n\tv_mfma_f32_16x16x32_bf16 v[168:171], a[224:227], v[104:107], v[168:171]\n\tv_mfma_f32_16x16x32_bf16 v[172:175], a[224:227], v[108:111], v[172:175]\n\t" \
    "v_mfma_f32_16x16x32_bf16 v[176:179], a[224:227], v[112:115], v[176:179]\n\tv_mfma_f32_16x16x32_bf16 v[180:183], a[224:227], v[116:119], v[180:183]\n\tv_mfma_f32_16x16x32_bf16 v[184:187], a[224:227], v[120:123], v[184:187]\n\tv_mfma_f32_16x16x32_bf16 v[188:191], a[224:227], v[124:127], v[188:191]\n\tv_mfma_f32_16x16x32_bf16 v[192:195], a[224:227], v[128:131], v[192:195]\n\tv_mfma_f32_16x16x32_bf16 v[168:171], a[228:231], v[100:103], v[168:171]\n\tv_mfma_f32_16x16x32_bf16 v[172:175], a[228:231], v[104:107], v[172:175]\n\tv_mfma_f32_16x16x32_bf16 v[176:179], a[228:231], v[108:111], v[176:179]\n\t" \
    "v_mfma_f32_16x16x32_bf16 v[180:183], a[228:231], v[112:115], v[180:183]\n\tv_mfma_f32_16x16x32_bf16 v[184:187], a[228:231], v[116:119], v[184:187]\n\tv_mfma_f32_16x16x32_bf16 v[188:191], a[228:231], v[120:123], v[188:191]\n\tv_mfma_f32_16x16x32_bf16 v[192:195], a[228:231], v[124:127], v[192:195]\n\tv_mfma_f32_16x16x32_bf16 v[172:175], a[232:235], v[100:103], v[172:175]\n\tv_mfma_f32_16x16x32_bf16 v[176:179], a[232:235], v[104:107], v[176:179]\n\tv_mfma_f32_16x16x32_bf16 v[180:183], a[232:235], v[108:111], v[180:183]\n\tv_mfma_f32_16x16x32_bf16 v[184:187], a[232:235], v[112:115], v[184:187]\n\t" \
    "v_mfma_f32_16x16x32_bf16 v[188:191], a[232:235], v[116:119], v[188:191]\n\tv_mfma_f32_16x16x32_bf16 v[192:195], a[232:235], v[120:123], v[192:195]\n\tv_mfma_f32_16x16x32_bf16 v[176:179], a[236:239], v[100:103], v[176:179]\n\tv_mfma_f32_16x16x32_bf16 v[180:183], a[236:239], v[104:107], v[180:183]\n\tv_mfma_f32_16x16x32_bf16 v[184:187], a[236:239], v[108:111], v[184:187]\n\tv_mfma_f32_16x16x32_bf16 v[188:191], a[236:239], v[112:115], v[188:191]\n\tv_mfma_f32_16x16x32_bf16 v[192:195], a[236:239], v[116:119], v[192:195]\n\tv_mfma_f32_16x16x32_bf16 v[180:183], a[240:243], v[100:103], v[180:183]\n\t" \
    "v_mfma_f32_16x16x32_bf16 v[184:187], a[240:243], v[104:107], v[184:187]\n\tv_mfma_f32_16x16x32_bf16 v[188:191], a[240:243], v[108:111], v[188:191]\n\tv_mfma_f32_16x16x32_bf16 v[192:195], a[240:243], v[112:115], v[192:195]\n\tv_mfma_f32_16x16x32_bf16 v[184:187], a[244:247], v[100:103], v[184:187]\n\tv_mfma_f32_16x16x32_bf16 v[188:191], a[244:247], v[104:107], v[188:191]\n\tv_mfma_f32_16x16x32_bf16 v[192:195], a[244:247], v[108:111], v[192:195]\n\tv_mfma_f32_16x16x32_bf16 v[188:191], a[248:251], v[100:103], v[188:191]\n\tv_mfma_f32_16x16x32_bf16 v[192:195], a[248:251], v[104:107], v[192:195]\n\t" \
    "v_mfma_f32_16x16x32_bf16 v[192:195], a[252:255], v[100:103], v[192:195]\n\tv_cvt_pk_bf16_f32 v164, v164, v165\n\tv_cvt_pk_bf16_f32 v165, v166, v167\n\tds_write_b64 v3, v[164:165] offset:55488\n\tv_cvt_pk_bf16_f32 v168, v168, v169\n\tv_cvt_pk_bf16_f32 v169, v170, v171\n\tds_write_b64 v3, v[168:169] offset:56000\n\tv_cvt_pk_bf16_f32 v172, v172, v173\n\t" \
    "v_cvt_pk_bf16_f32 v173, v174, v175\n\tds_write_b64 v3, v[172:173] offset:56512\n\tv_cvt_pk_bf16_f32 v176, v176, v177\n\tv_cvt_pk_bf16_f32 v177, v178, v179\n\tds_write_b64 v3, v[176:177] offset:57024\n\tv_cvt_pk_bf16_f32 v180, v180, v181\n\tv_cvt_pk_bf16_f32 v181, v182, v183\n\tds_write_b64 v3, v[180:181] offset:57536\n\t" \
    "v_cvt_pk_bf16_f32 v184, v184, v185\n\tv_cvt_pk_bf16_f32 v185, v186, v187\n\tds_write_b64 v3, v[184:185] offset:58048\n\tv_cvt_pk_bf16_f32 v188, v188, v189\n\tv_cvt_pk_bf16_f32 v189, v190, v191\n\tds_write_b64 v3, v[188:189] offset:58560\n\tv_cvt_pk_bf16_f32 v192, v192, v193\n\tv_cvt_pk_bf16_f32 v193, v194, v195\n\t" \
    "ds_write_b64 v3, v[192:193] offset:59072\n\ts_waitcnt lgkmcnt(0)\n\ts_barrier\n\ts_add_u32 s51, s44, 12\n\ts_mul_i32 s65, s51, 1366\n\ts_lshr_b32 s65, s65, 16\n\ts_mul_i32 s56, s65, 48\n\ts_sub_u32 s56, s51, s56\n\t" \
    "s_lshl_b32 s56, s56, 6\n\ts_lshr_b32 s70, s65, 2\n\ts_mul_i32 s70, s70, 252\n\ts_add_u32 s65, s65, s70\n\ts_mul_i32 s65, s65, 3072\n\ts_add_u32 s56, s56, s65\n\ts_add_u32 s56, s56, 4849664\n\ts_add_u32 s51, s51, s38\n\t" \
    "s_lshl_b32 s51, s51, 3\n\ts_add_u32 s51, s51, 50176\n\ts_or_b32 s56, s56, s45\n\ts_or_b32 s51, s51, s45\n\tds_read_b64_tr_b16 v[204:205], v4 offset:55488\n\tds_read_b64_tr_b16 v[206:207], v4 offset:55496\n\tds_read_b64_tr_b16 v[208:209], v4 offset:55552\n\tds_read_b64_tr_b16 v[210:211], v4 offset:55560\n\t" \
    "ds_read_b64_tr_b16 v[212:213], v4 offset:56000\n\tds_read_b64_tr_b16 v[214:215], v4 offset:56008\n\tds_read_b64_tr_b16 v[216:217], v4 offset:56064\n\tds_read_b64_tr_b16 v[218:219], v4 offset:56072\n\ts_waitcnt lgkmcnt(6)\n\tbuffer_store_dwordx4 v[204:207], v5, s[8:11], s56 offen sc1\n\ts_add_u32 s56, s56, 49152\n\ts_waitcnt lgkmcnt(4)\n\t" \
    "buffer_store_dwordx4 v[208:211], v5, s[8:11], s56 offen sc1\n\ts_add_u32 s56, s56, 49152\n\ts_waitcnt lgkmcnt(2)\n\tbuffer_store_dwordx4 v[212:215], v5, s[8:11], s56 offen sc1\n\ts_add_u32 s56, s56, 49152\n\ts_waitcnt lgkmcnt(0)\n\tbuffer_store_dwordx4 v[216:219], v5, s[8:11], s56 offen sc1\n\ts_add_u32 s56, s56, 49152\n\t" \
    "s_waitcnt vmcnt(0)\n\tbuffer_store_dwordx2 v[248:249], off, s[8:11], s51 sc1\n\ts_waitcnt vmcnt(0)\n\t103:\n\t98:\n\ts_nop 1\n\t" \
    ""
#define MAIN_CLOB "s2", "s3", "s4", "s5", "s6", "s7", "s8", "s9", "s10", "s11", "s12", "s13", "s14", "s15", "s16", "s17", "s18", "s19", "s20", "s21", "s22", "s23", "s24", "s25", "s26", "s27", "s28", "s29", "s30", "s31", "s33", "s34", "s35", "s36", "s37", "s39", "s41", "s42", "s43", "s44", "s45", "s46", "s47", "s48", "s49", "s50", "s51", "s52", "s53", "s54", "s55", "s56", "s57", "s58", "s59", "s60", "s61", "s62", "s63", "s64", "s65", "s66", "s67", "s68", "s69", "s70", "s71", "s72", "s73", "s74", "s75", "s76", "s77", "s78", "s79", "s80", "s81", "s82", "s83", "s84", "s85", "s86", "s87", "v0", "v1", "v2", "v3", "v4", "v5", "v6", "v7", "v8", "v9", "v10", "v11", "v12", "v13", "v14", "v15", "v16", "v17", "v18", "v19", "v20", "v21", "v22", "v23", "v24", "v25", "v26", "v27", "v28", "v29", "v30", "v31", "v32", "v33", "v34", "v35", "v36", "v37", "v38", "v39", "v40", "v41", "v42", "v43", "v44", "v45", "v46", "v47", "v48", "v49", "v50", "v51", "v52", "v53", "v54", "v55", "v56", "v57", "v58", "v59", "v60", "v61", "v62", "v63", "v64", "v65", "v66", "v67", "v68", "v69", "v70", "v71", "v72", "v73", "v74", "v75", "v76", "v77", "v78", "v79", "v80", "v81", "v82", "v83", "v84", "v85", "v86", "v87", "v88", "v89", "v90", "v91", "v92", "v93", "v94", "v95", "v96", "v97", "v98", "v99", "v100", "v101", "v102", "v103", "v104", "v105", "v106", "v107", "v108", "v109", "v110", "v111", "v112", "v113", "v114", "v115", "v116", "v117", "v118", "v119", "v120", "v121", "v122", "v123", "v124", "v125", "v126", "v127", "v128", "v129", "v130", "v131", "v132", "v133", "v134", "v135", "v136", "v137", "v138", "v139", "v140", "v141", "v142", "v143", "v144", "v145", "v146", "v147", "v148", "v149", "v150", "v151", "v152", "v153", "v154", "v155", "v156", "v157", "v158", "v159", "v160", "v161", "v162", "v163", "v164", "v165", "v166", "v167", "v168", "v169", "v170", "v171", "v172", "v173", "v174", "v175", "v176", "v177", "v178", "v179", "v180", "v181", "v182", "v183", "v184", "v185", "v186", "v187", "v188", "v189", "v190", "v191", "v192", "v193", "v194", "v195", "v196", "v197", "v198", "v199", "v200", "v201", "v202", "v203", "v204", "v205", "v206", "v207", "v208", "v209", "v210", "v211", "v212", "v213", "v214", "v215", "v216", "v217", "v218", "v219", "v220", "v221", "v222", "v223", "v224", "v225", "v226", "v227", "v228", "v229", "v230", "v231", "v232", "v233", "v234", "v235", "v236", "v237", "v238", "v239", "v241", "v242", "v243", "v244", "v245", "v246", "v247", "v248", "v249", "v250", "v251", "v252", "v253", "a0", "a1", "a2", "a3", "a4", "a5", "a6", "a7", "a8", "a9", "a10", "a11", "a12", "a13", "a14", "a15", "a16", "a17", "a18", "a19", "a20", "a21", "a22", "a23", "a24", "a25", "a26", "a27", "a28", "a29", "a30", "a31", "a32", "a33", "a34", "a35", "a36", "a37", "a38", "a39", "a40", "a41", "a42", "a43", "a44", "a45", "a46", "a47", "a48", "a49", "a50", "a51", "a52", "a53", "a54", "a55", "a56", "a57", "a58", "a59", "a60", "a61", "a62", "a63", "a64", "a65", "a66", "a67", "a68", "a69", "a70", "a71", "a72", "a73", "a74", "a75", "a76", "a77", "a78", "a79", "a80", "a81", "a82", "a83", "a84", "a85", "a86", "a87", "a88", "a89", "a90", "a91", "a92", "a93", "a94", "a95", "a96", "a97", "a98", "a99", "a100", "a101", "a102", "a103", "a104", "a105", "a106", "a107", "a108", "a109", "a110", "a111", "a112", "a113", "a114", "a115", "a116", "a117", "a118", "a119", "a120", "a121", "a122", "a123", "a124", "a125", "a126", "a127", "a128", "a129", "a130", "a131", "a132", "a133", "a134", "a135", "a136", "a137", "a138", "a139", "a140", "a141", "a142", "a143", "a144", "a145", "a146", "a147", "a148", "a149", "a150", "a151", "a152", "a153", "a154", "a155", "a156", "a157", "a158", "a159", "a160", "a161", "a162", "a163", "a164", "a165", "a166", "a167", "a168", "a169", "a170", "a171", "a172", "a173", "a174", "a175", "a176", "a177", "a178", "a179", "a180", "a181", "a182", "a183", "a184", "a185", "a186", "a187", "a188", "a189", "a190", "a191", "a192", "a193", "a194", "a195", "a196", "a197", "a198", "a199", "a200", "a201", "a202", "a203", "a204", "a205", "a206", "a207", "a208", "a209", "a210", "a211", "a212", "a213", "a214", "a215", "a216", "a217", "a218", "a219", "a220", "a221", "a222", "a223", "a224", "a225", "a226", "a227", "a228", "a229", "a230", "a231", "a232", "a233", "a234", "a235", "a236", "a237", "a238", "a239", "a240", "a241", "a242", "a243", "a244", "a245", "a246", "a247", "a248", "a249", "a250", "a251", "a252", "a253", "a254", "a255"

#define ARG(i) const void* p##i, unsigned n##i, unsigned f##i
__global__ __launch_bounds__(256, 1) void fused_kernel(ARG(0), ARG(1), ARG(2), ARG(3), ARG(4), ARG(5), ARG(6), ARG(7), ARG(8), ARG(9), ARG(10),
                                                       ARG(11)) {
#define MAIN_OUT
    const int b = blockIdx.x;
    unsigned tid = threadIdx.x;
    u64 ka = (u64)__builtin_amdgcn_kernarg_segment_ptr();
    int w;
    asm volatile(MAIN_ASM
                 : "=&{s38}"(w), "+{s[0:1]}"(ka), "+{v240}"(tid) MAIN_OUT
                 : "{s40}"(b)
                 : "memory", "scc", "vcc", MAIN_CLOB);
}

extern "C" void kernel_launch(void* const* d_in, const int* in_sizes, int n_in, void* d_out, int out_size,
                              void* d_ws, size_t ws_size, hipStream_t stream) {
    (void)hipFuncSetAttribute((const void*)fused_kernel, hipFuncAttributeMaxDynamicSharedMemorySize, LDS_BYTES);
    const unsigned F = 0x20000u, NX = S_LEN * D_DIM * 2, NW = S_LEN * F_DIM * 2;
    fused_kernel<<<256, 256, LDS_BYTES, stream>>>(d_ws, WS_END, F, d_in[7], NW, F, d_in[9], NW, F, d_in[0], NX, F, d_out, NX, F, d_in[8], F_DIM * 2, F,
                                                  d_in[10], S_LEN * 2, F, d_in[1], NX, F, d_in[2], NX, F, d_in[3], D_DIM * 8, F, d_in[4], NX, F,
                                                  d_in[5], D_DIM * 2, F);
}
```

```cpp
#include <hip/hip_runtime.h>

typedef unsigned long long u64;

#define S_LEN 2048
#define D_DIM 576
#define F_DIM 4608
#define LDS_BYTES 163840

#define WS_END 22347776u
#define MAIN_ASM \
    "s_load_dwordx16 s[8:23], s[0:1], 0\n\ts_getpc_b64 s[2:3]\n\t99:\n\tv_lshrrev_b32 v20, 5, v240\n\tv_mul_u32_u24 v20, 1152, v20\n\tv_and_b32 v21, 31, v240\n\tv_lshl_add_u32 v22, v21, 2, v20\n\tv_lshl_add_u32 v21, v21, 4, v20\n\t" \
    "s_mul_i32 s68, s40, 9216\n\tv_lshrrev_b32 v245, 6, v240\n\tv_and_b32 v240, 63, v240\n\ts_and_b32 s2, s2, 0xffffff80\n\ts_mov_b32 s4, s2\n\ts_mov_b32 s5, s3\n\ts_mov_b32 s6, 98f-99b\n\ts_mov_b32 s7, 0x20000\n\t" \
    "v_lshl_add_u32 v16, v245, 16, v240\n\tv_lshlrev_b32 v16, 7, v16\n\ts_lshr_b32 s65, s40, 3\n\ts_lshl_b32 s73, s65, 13\n\tbuffer_load_dword v57, v16, s[4:7], s73 offen\n\ts_waitcnt lgkmcnt(0)\n\tbuffer_load_dwordx4 v[48:51], v21, s[20:23], s68 offen\n\tbuffer_load_dwordx4 v[52:55], v21, s[20:23], s68 offen offset:512\n\t" \
    "buffer_load_dword v56, v22, s[20:23], s68 offen offset:1024\n\tbuffer_load_dwordx2 v[58:59], off, s[8:11], 0 sc1\n\ts_load_dwordx8 s[76:83], s[0:1], 112\n\ts_load_dwordx4 s[84:87], s[0:1], 144\n\ts_load_dwordx8 s[24:31], s[0:1], 64\n\ts_load_dwordx4 s[32:35], s[0:1], 96\n\tv_readfirstlane_b32 s38, v245\n\tv_lshlrev_b32 v8, 4, v240\n\t" \
    "v_and_b32 v244, 0xf0, v8\n\tv_lshrrev_b32 v245, 4, v240\n\ts_lshr_b32 s57, s38, 1\n\ts_and_b32 s58, s38, 1\n\ts_cmp_lt_u32 s65, 27\n\ts_cselect_b32 s39, 1, 0\n\ts_cmp_lt_u32 s65, 24\n\ts_cselect_b32 s39, 2, s39\n\t" \
    "s_sub_u32 s66, s65, 27\n\ts_mul_i32 s66, s66, 6\n\ts_cmp_lt_u32 s65, 27\n\ts_cselect_b32 s65, s65, s66\n\ts_and_b32 s66, s40, 7\n\ts_mul_i32 s66, s66, 27\n\ts_add_u32 s65, s65, s66\n\ts_mul_i32 s42, s65, 171\n\t" \
    "s_lshr_b32 s42, s42, 10\n\ts_mul_i32 s66, s42, 6\n\ts_sub_u32 s41, s65, s66\n\ts_mul_i32 s65, s42, 57\n\ts_lshr_b32 s65, s65, 9\n\ts_mul_i32 s65, s65, 9\n\ts_sub_u32 s65, s42, s65\n\ts_lshl_b32 s66, s41, 2\n\t" \
    "s_lshr_b32 s66, 5444640, s66\n\ts_and_b32 s66, s66, 15\n\ts_cmp_eq_u32 s65, 4\n\ts_cselect_b32 s43, s66, s41\n\ts_mul_i32 s43, s43, 688\n\ts_lshr_b32 s43, s43, 8\n\ts_and_b32 s65, s40, 7\n\ts_mul_i32 s52, s65, 0\n\t" \
    "s_mul_i32 s65, s65, 0\n\ts_lshr_b32 s65, s65, 1\n\ts_add_u32 s43, s43, s65\n\ts_cmp_ge_u32 s43, 16\n\ts_cselect_b32 s66, 16, 0\n\ts_sub_u32 s43, s43, s66\n\ts_cmp_eq_u32 s39, 0\n\ts_cselect_b32 s43, 0, s43\n\t" \
    "s_cselect_b32 s67, 4718592, 0\n\ts_sub_u32 s70, s43, 16\n\ts_mul_i32 s65, s57, 9437184\n\ts_add_u32 s67, s67, s65\n\ts_lshl_b32 s66, s42, 8\n\ts_add_u32 s67, s67, s66\n\tv_lshrrev_b32 v16, 3, v240\n\ts_lshl_b32 s73, s58, 4\n\t" \
    "v_add_u32 v16, s73, v16\n\tv_mul_u32_u24 v16, 9216, v16\n\tv_and_b32 v17, 0x70, v8\n\tv_and_b32 v18, 16, v240\n\tv_lshlrev_b32 v18, 1, v18\n\tv_xor_b32 v17, v17, v18\n\tv_add_u32 v10, v16, v17\n\tv_xor_b32 v11, 64, v10\n\t" \
    "v_add_u32 v11, 73728, v11\n\ts_lshl_b32 s72, s57, 16\n\ts_cmp_eq_u32 s39, 0\n\ts_cselect_b32 s72, 0, s72\n\ts_lshl_b32 s73, s58, 11\n\ts_add_u32 s72, s72, s73\n\ts_lshl_b32 s54, s38, 10\n\tv_add_u32 v9, 4096, v8\n\t" \
    "v_lshlrev_b32 v239, 3, v240\n\ts_mul_i32 s73, s40, 9216\n\ts_add_u32 s73, s73, s54\n\tv_add_u32 v23, s73, v8\n\tv_subrev_u32 v19, 3456, v23\n\ts_add_u32 s12, s12, s67\n\ts_addc_u32 s13, s13, 0\n\ts_sub_u32 s14, s14, s67\n\t" \
    "s_lshl_b32 s73, s58, 6\n\tv_add_u32 v14, s73, v240\n\tv_mul_u32_u24 v14, 9216, v14\n\tv_mov_b32 v15, 2146435072\n\ts_cmp_eq_u32 s39, 0\n\ts_cselect_b64 vcc, -1, 0\n\tv_cndmask_b32 v10, v10, v15, vcc\n\tv_cndmask_b32 v11, v11, v15, vcc\n\t" \
    "v_cndmask_b32 v14, v15, v14, vcc\n\tv_and_b32 v88, 31, v240\n\tv_lshrrev_b32 v89, 5, v240\n\ts_lshl_b32 s73, s38, 1\n\tv_add_u32 v89, s73, v89\n\tv_mul_u32_u24 v90, 1168, v89\n\tv_add_u32 v90, 153600, v90\n\tv_lshl_add_u32 v91, v88, 2, v90\n\t" \
    "v_lshl_add_u32 v90, v88, 4, v90\n\tv_cmp_eq_u32 vcc, 0, v88\n\tv_lshlrev_b32 v89, 3, v89\n\tv_mov_b32 v92, 0x7ff00000\n\tv_cndmask_b32 v89, v92, v89, vcc\n\tv_bfe_u32 v88, v240, 2, 2\n\tv_and_b32 v92, 3, v240\n\tv_lshlrev_b32 v92, 3, v92\n\t" \
    "v_lshl_add_u32 v92, v245, 5, v92\n\ts_lshl_b32 s73, s38, 7\n\tv_add_u32 v93, s73, v92\n\tv_mul_u32_u24 v94, 1168, v88\n\tv_add_u32 v94, 153600, v94\n\tv_add_u32 v95, v94, v92\n\tv_add_u32 v94, v94, v93\n\tv_mul_u32_u24 v88, 1152, v88\n\t" \
    "v_add_u32 v88, 131072, v88\n\tv_add_u32 v92, v88, v92\n\tv_add_u32 v93, v88, v93\n\tv_lshl_add_u32 v88, v245, 10, v244\n\ts_waitcnt lgkmcnt(0)\n\ts_lshr_b32 s61, s40, 2\n\ts_mul_i32 s61, s61, 36864\n\ts_and_b32 s65, s40, 3\n\t" \
    "s_lshl_b32 s65, s65, 8\n\ts_add_u32 s61, s61, s65\n\ts_add_u32 s61, s61, 131072\n\ts_mul_i32 s62, s40, 9216\n\ts_add_u32 s62, s62, 2490368\n\ts_lshl_b32 s63, s40, 3\n\ts_lshl_b32 s48, s38, 11\n\ts_add_u32 s48, s48, s63\n\t" \
    "s_add_u32 s48, s48, 17408\n\ts_add_u32 s63, s63, 1024\n\ts_cmp_eq_u32 s38, 0\n\ts_cselect_b32 s63, s63, 2146435072\n\ts_lshl_b32 s64, s40, 6\n\ts_add_u32 s64, s64, 58368\n\ts_add_u32 m0, s54, 131072\n\ts_nop 0\n\t" \
    "buffer_load_dwordx4 v19, s[76:79], 0 offen lds\n\ts_and_b32 s4, s40, 7\n\ts_lshl_b32 s47, s4, 11\n\ts_lshl_b32 s65, s57, 10\n\ts_add_u32 s47, s47, s65\n\tv_add_u32 v19, 4096, v19\n\ts_add_u32 m0, s54, 135168\n\ts_nop 0\n\t" \
    "buffer_load_dwordx4 v19, s[76:79], 0 offen lds\n\ts_add_u32 s47, s47, 1024\n\ts_mul_i32 s44, s57, 1179648\n\ts_mul_i32 s65, s41, 6144\n\ts_add_u32 s44, s44, s65\n\tv_add_u32 v19, 4096, v19\n\ts_add_u32 m0, s54, 139264\n\ts_nop 0\n\t" \
    "buffer_load_dwordx4 v19, s[76:79], 0 offen lds\n\ts_mul_i32 s65, s58, 3072\n\ts_add_u32 s44, s44, s65\n\ts_add_u32 s44, s44, 131072\n\ts_lshr_b32 s5, s4, 1\n\tv_add_u32 v19, 4096, v19\n\ts_add_u32 m0, s54, 144384\n\ts_nop 0\n\t" \
    "buffer_load_dwordx4 v23, s[80:83], 0 offen lds\n\tv_bfe_u32 v16, v240, 2, 2\n\tv_and_b32 v17, 3, v240\n\tv_lshlrev_b32 v17, 3, v17\n\tv_lshl_add_u32 v16, v16, 7, v17\n\tv_add_u32 v23, 4096, v23\n\ts_add_u32 m0, s54, 148480\n\ts_nop 0\n\t" \
    "buffer_load_dwordx4 v23, s[80:83], 0 offen lds\n\tv_bfe_u32 v17, v240, 3, 2\n\tv_lshl_add_u32 v16, v17, 5, v16\n\ts_mul_i32 s65, s57, 65600\n\tv_xor_b32 v17, s65, v16\n\tv_add_u32 v23, 4096, v23\n\tv_subrev_u32 v19, s54, v19\n\tv_subrev_u32 v23, s54, v23\n\t" \
    "s_cmp_eq_u32 s58, 1\n\ts_cselect_b64 vcc, -1, 0\n\ts_cselect_b64 s[76:77], s[80:81], s[76:77]\n\ts_mov_b32 m0, 143360\n\ts_cselect_b32 m0, 152576, m0\n\tv_cndmask_b32 v19, v19, v23, vcc\n\tbuffer_load_dwordx4 v19, s[76:79], 0 offen lds\n\tv_lshl_add_u32 v0, v245, 10, v17\n\t" \
    "s_and_b32 s65, s5, 1\n\ts_xor_b32 s65, s65, 1\n\ts_lshl_b32 s65, s65, 6\n\ts_lshr_b32 s71, s54, 1\n\tbuffer_load_dwordx2 v[60:61], v239, s[84:87], s71 offen\n\tv_xor_b32 v17, s65, v16\n\tv_lshl_add_u32 v4, v245, 9, v17\n\tv_xor_b32 v1, 32, v0\n\t" \
    "v_xor_b32 v5, 32, v4\n\tbuffer_load_dwordx2 v[62:63], v239, s[84:87], s71 offen offset:2048\n\tv_xor_b32 v2, 64, v0\n\tv_xor_b32 v6, 64, v4\n\tv_xor_b32 v3, 96, v0\n\tv_xor_b32 v7, 96, v4\n\ts_mov_b32 s71, 4096\n\tbuffer_load_dwordx2 v[64:65], v239, s[84:87], s71 offen\n\t" \
    "v_add_u32 v224, 0x10000, v4\n\tv_add_u32 v225, 0x10000, v5\n\tv_add_u32 v226, 0x10000, v6\n\tv_add_u32 v227, 0x10000, v7\n\ts_cmp_ge_u32 s43, 16\n\ts_cselect_b32 s69, s70, s43\n\ts_mul_i32 s69, s69, 294912\n\ts_add_u32 s69, s69, 0\n\t" \
    "s_add_u32 m0, s72, 0\n\ts_nop 0\n\tbuffer_load_dwordx4 v10, s[12:15], s69 offen lds\n\ts_mul_i32 s65, s38, 6144\n\ts_add_u32 s65, s65, 131072\n\tv_add_u32 v237, s65, v8\n\ts_xor_b32 s65, s38, 2\n\ts_add_u32 m0, s72, 1024\n\t" \
    "s_nop 0\n\tbuffer_load_dwordx4 v11, s[12:15], s69 offen lds\n\ts_mul_i32 s65, s65, 6144\n\ts_add_u32 s65, s65, 131072\n\tv_add_u32 v238, s65, v8\n\tv_mov_b32 v246, 0xbdd2d3e8\n\ts_waitcnt vmcnt(12)\n\ts_load_dwordx8 s[76:83], s[0:1], 160\n\t" \
    "v_lshlrev_b32 v24, 16, v48\n\tv_and_b32 v25, 0xffff0000, v48\n\tv_lshlrev_b32 v26, 16, v49\n\tv_and_b32 v27, 0xffff0000, v49\n\tv_lshlrev_b32 v28, 16, v50\n\tv_and_b32 v29, 0xffff0000, v50\n\tv_lshlrev_b32 v30, 16, v51\n\tv_and_b32 v31, 0xffff0000, v51\n\t" \
    "v_lshlrev_b32 v32, 16, v52\n\tv_and_b32 v33, 0xffff0000, v52\n\tv_lshlrev_b32 v34, 16, v53\n\tv_and_b32 v35, 0xffff0000, v53\n\tv_lshlrev_b32 v36, 16, v54\n\tv_and_b32 v37, 0xffff0000, v54\n\tv_lshlrev_b32 v38, 16, v55\n\tv_and_b32 v39, 0xffff0000, v55\n\t" \
    "v_lshlrev_b32 v40, 16, v56\n\tv_and_b32 v41, 0xffff0000, v56\n\tv_pk_add_f32 v[70:71], v[24:25], v[26:27]\n\tv_pk_add_f32 v[72:73], v[28:29], v[30:31]\n\tv_pk_add_f32 v[74:75], v[32:33], v[34:35]\n\tv_pk_add_f32 v[76:77], v[36:37], v[38:39]\n\tv_pk_add_f32 v[70:71], v[70:71], v[72:73]\n\tv_pk_add_f32 v[74:75], v[74:75], v[76:77]\n\t" \
    "v_pk_add_f32 v[70:71], v[70:71], v[40:41]\n\tv_pk_add_f32 v[70:71], v[70:71], v[74:75]\n\tv_add_f32 v66, v70, v71\n\ts_nop 1\n\tv_add_f32_dpp v66, v66, v66 row_ror:8 row_mask:0xf bank_mask:0xf\n\ts_nop 1\n\tv_add_f32_dpp v66, v66, v66 row_ror:4 row_mask:0xf bank_mask:0xf\n\ts_nop 1\n\t" \
    "v_add_f32_dpp v66, v66, v66 row_ror:2 row_mask:0xf bank_mask:0xf\n\ts_nop 1\n\tv_add_f32_dpp v66, v66, v66 row_ror:1 row_mask:0xf bank_mask:0xf\n\tv_mov_b32 v67, v66\n\ts_nop 1\n\tv_permlane16_swap_b32 v66, v67\n\tv_add_f32 v66, v66, v67\n\tv_mul_f32 v66, 0x3ae38e39, v66\n\t" \
    "v_pk_add_f32 v[70:71], v[24:25], v[66:67] op_sel_hi:[1,0] neg_lo:[0,1] neg_hi:[0,1]\n\tv_pk_add_f32 v[72:73], v[26:27], v[66:67] op_sel_hi:[1,0] neg_lo:[0,1] neg_hi:[0,1]\n\tv_pk_add_f32 v[74:75], v[28:29], v[66:67] op_sel_hi:[1,0] neg_lo:[0,1] neg_hi:[0,1]\n\tv_pk_add_f32 v[76:77], v[30:31], v[66:67] op_sel_hi:[1,0] neg_lo:[0,1] neg_hi:[0,1]\n\tv_pk_add_f32 v[78:79], v[32:33], v[66:67] op_sel_hi:[1,0] neg_lo:[0,1] neg_hi:[0,1]\n\tv_pk_add_f32 v[80:81], v[34:35], v[66:67] op_sel_hi:[1,0] neg_lo:[0,1] neg_hi:[0,1]\n\tv_pk_add_f32 v[82:83], v[36:37], v[66:67] op_sel_hi:[1,0] neg_lo:[0,1] neg_hi:[0,1]\n\tv_pk_add_f32 v[84:85], v[38:39], v[66:67] op_sel_hi:[1,0] neg_lo:[0,1] neg_hi:[0,1]\n\t" \
    "v_pk_add_f32 v[86:87], v[40:41], v[66:67] op_sel_hi:[1,0] neg_lo:[0,1] neg_hi:[0,1]\n\tv_pk_mul_f32 v[44:45], v[70:71], v[70:71]\n\tv_pk_mul_f32 v[46:47], v[72:73], v[72:73]\n\tv_pk_fma_f32 v[44:45], v[74:75], v[74:75], v[44:45]\n\tv_pk_fma_f32 v[46:47], v[76:77], v[76:77], v[46:47]\n\tv_pk_fma_f32 v[44:45], v[78:79], v[78:79], v[44:45]\n\tv_pk_fma_f32 v[46:47], v[80:81], v[80:81], v[46:47]\n\tv_pk_fma_f32 v[44:45], v[82:83], v[82:83], v[44:45]\n\t" \
    "v_pk_fma_f32 v[46:47], v[84:85], v[84:85], v[46:47]\n\tv_pk_fma_f32 v[44:45], v[86:87], v[86:87], v[44:45]\n\tv_pk_add_f32 v[44:45], v[44:45], v[46:47]\n\tv_add_f32 v68, v44, v45\n\ts_nop 1\n\tv_add_f32_dpp v68, v68, v68 row_ror:8 row_mask:0xf bank_mask:0xf\n\ts_nop 1\n\tv_add_f32_dpp v68, v68, v68 row_ror:4 row_mask:0xf bank_mask:0xf\n\t" \
    "s_nop 1\n\tv_add_f32_dpp v68, v68, v68 row_ror:2 row_mask:0xf bank_mask:0xf\n\ts_nop 1\n\tv_add_f32_dpp v68, v68, v68 row_ror:1 row_mask:0xf bank_mask:0xf\n\tv_mov_b32 v69, v68\n\ts_nop 1\n\tv_permlane16_swap_b32 v68, v69\n\tv_add_f32 v68, v68, v69\n\t" \
    "v_mul_f32 v68, 0x3ae38e39, v68\n\tv_cvt_pk_bf16_f32 v68, v68, v68\n\tv_lshlrev_b32 v68, 16, v68\n\tv_add_f32 v68, 0x3727c5ac, v68\n\tv_cvt_pk_bf16_f32 v68, v68, v68\n\tv_lshlrev_b32 v68, 16, v68\n\tv_sqrt_f32 v68, v68\n\tv_cvt_pk_bf16_f32 v66, v66, v66\n\t" \
    "v_lshlrev_b32 v66, 16, v66\n\tv_cvt_pk_bf16_f32 v68, v68, v68\n\tv_lshlrev_b32 v68, 16, v68\n\tv_rcp_f32 v68, v68\n\ts_nop 0\n\tv_mul_f32 v69, v66, v68\n\tv_xor_b32 v69, 0x80000000, v69\n\tv_pk_fma_f32 v[24:25], v[24:25], v[68:69], v[68:69] op_sel:[0,0,1] op_sel_hi:[1,0,1]\n\t" \
    "v_pk_fma_f32 v[26:27], v[26:27], v[68:69], v[68:69] op_sel:[0,0,1] op_sel_hi:[1,0,1]\n\tv_pk_fma_f32 v[28:29], v[28:29], v[68:69], v[68:69] op_sel:[0,0,1] op_sel_hi:[1,0,1]\n\tv_pk_fma_f32 v[30:31], v[30:31], v[68:69], v[68:69] op_sel:[0,0,1] op_sel_hi:[1,0,1]\n\tv_pk_fma_f32 v[32:33], v[32:33], v[68:69], v[68:69] op_sel:[0,0,1] op_sel_hi:[1,0,1]\n\tv_pk_fma_f32 v[34:35], v[34:35], v[68:69], v[68:69] op_sel:[0,0,1] op_sel_hi:[1,0,1]\n\tv_pk_fma_f32 v[36:37], v[36:37], v[68:69], v[68:69] op_sel:[0,0,1] op_sel_hi:[1,0,1]\n\tv_pk_fma_f32 v[38:39], v[38:39], v[68:69], v[68:69] op_sel:[0,0,1] op_sel_hi:[1,0,1]\n\tv_pk_fma_f32 v[40:41], v[40:41], v[68:69], v[68:69] op_sel:[0,0,1] op_sel_hi:[1,0,1]\n\t" \
    "v_cvt_pk_bf16_f32 v48, v24, v25\n\tv_cvt_pk_bf16_f32 v49, v26, v27\n\tv_cvt_pk_bf16_f32 v50, v28, v29\n\tv_cvt_pk_bf16_f32 v51, v30, v31\n\tv_cvt_pk_bf16_f32 v52, v32, v33\n\tv_cvt_pk_bf16_f32 v53, v34, v35\n\tv_cvt_pk_bf16_f32 v54, v36, v37\n\tv_cvt_pk_bf16_f32 v55, v38, v39\n\t" \
    "v_cvt_pk_bf16_f32 v56, v40, v41\n\tv_lshlrev_b32 v24, 16, v48\n\tv_and_b32 v25, 0xffff0000, v48\n\tv_lshlrev_b32 v26, 16, v49\n\tv_and_b32 v27, 0xffff0000, v49\n\tv_lshlrev_b32 v28, 16, v50\n\tv_and_b32 v29, 0xffff0000, v50\n\tv_lshlrev_b32 v30, 16, v51\n\t" \
    "v_and_b32 v31, 0xffff0000, v51\n\tv_lshlrev_b32 v32, 16, v52\n\tv_and_b32 v33, 0xffff0000, v52\n\tv_lshlrev_b32 v34, 16, v53\n\tv_and_b32 v35, 0xffff0000, v53\n\tv_lshlrev_b32 v36, 16, v54\n\tv_and_b32 v37, 0xffff0000, v54\n\tv_lshlrev_b32 v38, 16, v55\n\t" \
    "v_and_b32 v39, 0xffff0000, v55\n\tv_lshlrev_b32 v40, 16, v56\n\tv_and_b32 v41, 0xffff0000, v56\n\tv_pk_add_f32 v[70:71], v[24:25], v[26:27]\n\tv_pk_add_f32 v[72:73], v[28:29], v[30:31]\n\tv_pk_add_f32 v[74:75], v[32:33], v[34:35]\n\tv_pk_add_f32 v[76:77], v[36:37], v[38:39]\n\tv_pk_mul_f32 v[44:45], v[24:25], v[24:25]\n\t" \
    "v_pk_mul_f32 v[46:47], v[26:27], v[26:27]\n\tv_pk_add_f32 v[70:71], v[70:71], v[72:73]\n\tv_pk_add_f32 v[74:75], v[74:75], v[76:77]\n\tv_pk_fma_f32 v[44:45], v[28:29], v[28:29], v[44:45]\n\tv_pk_fma_f32 v[46:47], v[30:31], v[30:31], v[46:47]\n\tv_pk_add_f32 v[70:71], v[70:71], v[40:41]\n\tv_pk_fma_f32 v[44:45], v[32:33], v[32:33], v[44:45]\n\tv_pk_fma_f32 v[46:47], v[34:35], v[34:35], v[46:47]\n\t" \
    "v_pk_add_f32 v[70:71], v[70:71], v[74:75]\n\tv_pk_fma_f32 v[44:45], v[36:37], v[36:37], v[44:45]\n\tv_pk_fma_f32 v[46:47], v[38:39], v[38:39], v[46:47]\n\tv_pk_fma_f32 v[44:45], v[40:41], v[40:41], v[44:45]\n\tv_pk_add_f32 v[44:45], v[44:45], v[46:47]\n\tv_add_f32 v66, v70, v71\n\tv_add_f32 v42, v44, v45\n\ts_nop 0\n\t" \
    "v_add_f32_dpp v66, v66, v66 row_ror:8 row_mask:0xf bank_mask:0xf\n\tv_add_f32_dpp v42, v42, v42 row_ror:8 row_mask:0xf bank_mask:0xf\n\ts_nop 0\n\tv_add_f32_dpp v66, v66, v66 row_ror:4 row_mask:0xf bank_mask:0xf\n\tv_add_f32_dpp v42, v42, v42 row_ror:4 row_mask:0xf bank_mask:0xf\n\ts_nop 0\n\tv_add_f32_dpp v66, v66, v66 row_ror:2 row_mask:0xf bank_mask:0xf\n\tv_add_f32_dpp v42, v42, v42 row_ror:2 row_mask:0xf bank_mask:0xf\n\t" \
    "s_nop 0\n\tv_add_f32_dpp v66, v66, v66 row_ror:1 row_mask:0xf bank_mask:0xf\n\tv_add_f32_dpp v42, v42, v42 row_ror:1 row_mask:0xf bank_mask:0xf\n\tv_mov_b32 v67, v66\n\tv_mov_b32 v43, v42\n\ts_nop 0\n\tv_permlane16_swap_b32 v66, v67\n\tv_permlane16_swap_b32 v42, v43\n\t" \
    "v_add_f32 v66, v66, v67\n\tv_add_f32 v42, v42, v43\n\tv_mul_f32 v66, 0x3ae38e39, v66\n\tv_mul_f32 v42, 0x3ae38e39, v42\n\tv_fma_f32 v42, -v66, v66, v42\n\tv_max_f32 v42, 0, v42\n\tv_cvt_pk_bf16_f32 v42, v42, v42\n\tv_lshlrev_b32 v42, 16, v42\n\t" \
    "v_add_f32 v42, 0x3727c5ac, v42\n\tv_cvt_pk_bf16_f32 v42, v42, v42\n\tv_lshlrev_b32 v42, 16, v42\n\tv_sqrt_f32 v42, v42\n\tv_cvt_pk_bf16_f32 v66, v66, v66\n\tv_lshlrev_b32 v66, 16, v66\n\tv_cvt_pk_bf16_f32 v42, v42, v42\n\tv_lshlrev_b32 v42, 16, v42\n\t" \
    "v_rcp_f32 v42, v42\n\ts_nop 0\n\tv_mul_f32 v43, v66, v42\n\tv_xor_b32 v43, 0x80000000, v43\n\tv_pk_fma_f32 v[24:25], v[24:25], v[42:43], v[42:43] op_sel:[0,0,1] op_sel_hi:[1,0,1]\n\tv_pk_fma_f32 v[26:27], v[26:27], v[42:43], v[42:43] op_sel:[0,0,1] op_sel_hi:[1,0,1]\n\tv_pk_fma_f32 v[28:29], v[28:29], v[42:43], v[42:43] op_sel:[0,0,1] op_sel_hi:[1,0,1]\n\tv_pk_fma_f32 v[30:31], v[30:31], v[42:43], v[42:43] op_sel:[0,0,1] op_sel_hi:[1,0,1]\n\t" \
    "v_pk_fma_f32 v[32:33], v[32:33], v[42:43], v[42:43] op_sel:[0,0,1] op_sel_hi:[1,0,1]\n\tv_pk_fma_f32 v[34:35], v[34:35], v[42:43], v[42:43] op_sel:[0,0,1] op_sel_hi:[1,0,1]\n\tv_pk_fma_f32 v[36:37], v[36:37], v[42:43], v[42:43] op_sel:[0,0,1] op_sel_hi:[1,0,1]\n\tv_pk_fma_f32 v[38:39], v[38:39], v[42:43], v[42:43] op_sel:[0,0,1] op_sel_hi:[1,0,1]\n\tv_pk_fma_f32 v[40:41], v[40:41], v[42:43], v[42:43] op_sel:[0,0,1] op_sel_hi:[1,0,1]\n\tv_cvt_pk_bf16_f32 v48, v24, v25\n\tv_cvt_pk_bf16_f32 v49, v26, v27\n\tv_cvt_pk_bf16_f32 v50, v28, v29\n\t" \
    "v_cvt_pk_bf16_f32 v51, v30, v31\n\tv_cvt_pk_bf16_f32 v52, v32, v33\n\tv_cvt_pk_bf16_f32 v53, v34, v35\n\tv_cvt_pk_bf16_f32 v54, v36, v37\n\tv_cvt_pk_bf16_f32 v55, v38, v39\n\tv_cvt_pk_bf16_f32 v56, v40, v41\n\tds_write_b128 v90, v[48:51]\n\tds_write_b128 v90, v[52:55] offset:512\n\t" \
    "ds_write_b32 v91, v56 offset:1024\n\ts_waitcnt vmcnt(11) lgkmcnt(0)\n\ts_barrier\n\tds_read_b64_tr_b16 v[24:25], v94 offset:0\n\tds_read_b64_tr_b16 v[26:27], v94 offset:4672\n\tds_read_b64_tr_b16 v[28:29], v94 offset:512\n\tds_read_b64_tr_b16 v[30:31], v94 offset:5184\n\tds_read_b64_tr_b16 v[32:33], v95 offset:1024\n\t" \
    "ds_read_b64_tr_b16 v[34:35], v95 offset:5696\n\tbuffer_store_dwordx2 v[68:69], v89, s[8:11], s64 offen sc1\n\tv_readfirstlane_b32 s36, v58\n\tv_readfirstlane_b32 s37, v59\n\ts_lshl_b32 s73, s38, 12\n\ts_add_u32 s68, s61, s73\n\ts_add_u32 s36, s36, 0x7f4a7c15\n\ts_addc_u32 s37, s37, 0x9e3779b9\n\t" \
    "s_waitcnt lgkmcnt(4)\n\tbuffer_store_dwordx4 v[24:27], v88, s[8:11], s68 offen sc1\n\ts_add_u32 s68, s68, 16384\n\ts_waitcnt lgkmcnt(2)\n\tbuffer_store_dwordx4 v[28:31], v88, s[8:11], s68 offen sc1\n\ts_add_u32 s68, s61, 32768\n\ts_cmp_eq_u32 s38, 0\n\ts_cselect_b32 s68, s68, 0x7ff00000\n\t" \
    "s_waitcnt lgkmcnt(0)\n\tbuffer_store_dwordx4 v[32:35], v88, s[8:11], s68 offen sc1\n\tv_mov_b64 v[248:249], s[36:37]\n\tv_and_b32 v89, 7, v240\n\tv_lshlrev_b32 v89, 11, v89\n\ts_cmp_ge_u32 s43, 16\n\ts_cselect_b32 s69, s70, s43\n\ts_mul_i32 s69, s69, 294912\n\t" \
    "s_add_u32 s69, s69, 128\n\ts_add_u32 m0, s72, 4096\n\ts_nop 0\n\tbuffer_load_dwordx4 v10, s[12:15], s69 offen lds\n\tv_mov_b32 v247, 0xc0135761\n\ts_lshr_b32 s65, s40, 3\n\ts_mul_i32 s66, s65, 43\n\ts_lshr_b32 s66, s66, 7\n\t" \
    "s_mul_i32 s6, s66, 3\n\ts_sub_u32 s6, s65, s6\n\ts_and_b32 s66, s66, 7\n\ts_and_b32 s7, s4, 1\n\ts_lshl_b32 s7, s7, 3\n\ts_add_u32 s7, s7, s66\n\ts_mul_i32 s65, s5, 4718592\n\ts_lshl_b32 s67, s7, 8\n\t" \
    "s_add_u32 s65, s65, s67\n\ts_add_u32 s16, s16, s65\n\ts_addc_u32 s17, s17, 0\n\ts_sub_u32 s18, s18, s65\n\ts_cmp_eq_u32 s39, 2\n\ts_cselect_b32 s18, s18, 0\n\tv_lshrrev_b32 v16, 3, v240\n\ts_lshl_b32 s65, s38, 3\n\t" \
    "v_add_u32 v16, s65, v16\n\tv_and_b32 v17, 0x70, v8\n\tv_lshlrev_b32 v18, 5, v245\n\tv_xor_b32 v17, v17, v18\n\tv_lshl_add_u32 v16, v16, 12, v17\n\ts_lshr_b32 s65, s5, 1\n\ts_lshl_b32 s65, s65, 7\n\tv_add_u32 v13, s65, v16\n\t" \
    "v_xor_b32 v12, 128, v13\n\ts_lshl_b32 s65, s7, 7\n\ts_lshl_b32 s66, s5, 5\n\ts_add_u32 s65, s65, s66\n\ts_add_u32 m0, s72, 5120\n\ts_nop 0\n\tbuffer_load_dwordx4 v11, s[12:15], s69 offen lds\n\ts_lshl_b32 s59, s65, 3\n\t" \
    "s_add_u32 s59, s59, 58368\n\ts_lshl_b32 s66, s65, 1\n\ts_add_u32 s32, s32, s66\n\ts_addc_u32 s33, s33, 0\n\ts_sub_u32 s34, s34, s66\n\ts_lshl_b32 s66, s6, 2\n\ts_add_u32 s66, s66, s38\n\ts_mul_i32 s67, s66, 96\n\t" \
    "s_mul_i32 s65, s65, 1152\n\ts_add_u32 s65, s65, s67\n\ts_add_u32 s20, s20, s65\n\ts_addc_u32 s21, s21, 0\n\ts_sub_u32 s22, s22, s65\n\ts_add_u32 s24, s24, s65\n\ts_addc_u32 s25, s25, 0\n\ts_sub_u32 s26, s26, s65\n\t" \
    "s_lshl_b32 s51, s67, 2\n\ts_add_u32 s51, s51, 50176\n\ts_lshl_b32 s66, s66, 2\n\ts_add_u32 s66, s66, s5\n\ts_mul_i32 s46, s66, 110592\n\ts_add_u32 s46, s46, 16646144\n\ts_cmp_eq_u32 s39, 2\n\ts_cselect_b32 s46, s46, 2146435072\n\t" \
    "s_lshl_b32 s50, s66, 13\n\ts_add_u32 s50, s50, 21954560\n\tv_and_b32 v16, 31, v240\n\tv_mul_u32_u24 v243, 1152, v16\n\tv_lshrrev_b32 v16, 5, v240\n\tv_lshl_add_u32 v243, v16, 4, v243\n\tv_lshrrev_b32 v242, 1, v244\n\ts_mul_i32 s65, s42, 57\n\t" \
    "s_lshr_b32 s65, s65, 9\n\ts_cmp_ge_u32 s43, 15\n\ts_cselect_b32 s69, s70, s43\n\ts_mul_i32 s69, s69, 294912\n\ts_add_u32 s69, s69, 294912\n\ts_add_u32 m0, s72, 8192\n\ts_nop 0\n\tbuffer_load_dwordx4 v10, s[12:15], s69 offen lds\n\t" \
    "s_mul_i32 s66, s65, 9\n\ts_sub_u32 s66, s42, s66\n\ts_lshl_b32 s66, s66, 1\n\ts_add_u32 s66, s66, s57\n\ts_lshl_b32 s67, s41, 1\n\ts_add_u32 s67, s67, s58\n\ts_lshl_b32 s67, s67, 2\n\ts_add_u32 s65, s65, s67\n\t" \
    "s_lshl_b32 s49, s65, 13\n\ts_lshl_b32 s67, s66, 7\n\ts_add_u32 s49, s49, s67\n\ts_add_u32 s49, s49, 21954560\n\ts_mul_i32 s45, s65, 36\n\ts_add_u32 s45, s45, s66\n\ts_mul_i32 s45, s45, 3072\n\ts_add_u32 s45, s45, 16646144\n\t" \
    "v_min_u32 v241, 17, v240\n\tv_lshlrev_b32 v241, 7, v241\n\ts_lshl_b32 s65, s6, 4\n\ts_add_u32 s65, s65, s7\n\ts_lshl_b32 s65, s65, 2\n\ts_add_u32 s65, s65, s5\n\ts_lshl_b32 s65, s65, 2\n\ts_add_u32 s53, s65, s38\n\t" \
    "s_mul_i32 s55, s53, 12288\n\ts_add_u32 s55, s55, 7208960\n\ts_cmp_eq_u32 s39, 2\n\ts_cselect_b32 s55, s55, 2146435072\n\ts_mul_i32 s56, s53, 3072\n\ts_add_u32 s56, s56, 4849664\n\ts_or_b32 s65, s40, s38\n\ts_cselect_b32 s60, 2146435072, 0\n\t" \
    "s_add_u32 m0, s72, 9216\n\ts_nop 0\n\tbuffer_load_dwordx4 v11, s[12:15], s69 offen lds\n\ts_waitcnt vmcnt(10)\n\ts_barrier\n\tds_read_b64_tr_b16 v[36:37], v93 offset:0\n\tds_read_b64_tr_b16 v[38:39], v93 offset:4608\n\tds_read_b64_tr_b16 v[40:41], v93 offset:9216\n\t" \
    "ds_read_b64_tr_b16 v[42:43], v93 offset:13312\n\tds_read_b64_tr_b16 v[44:45], v93 offset:17920\n\tds_read_b64_tr_b16 v[46:47], v93 offset:512\n\tds_read_b64_tr_b16 v[48:49], v93 offset:5120\n\tds_read_b64_tr_b16 v[50:51], v93 offset:9728\n\tds_read_b64_tr_b16 v[52:53], v93 offset:13824\n\tds_read_b64_tr_b16 v[54:55], v93 offset:18432\n\tds_read_b64_tr_b16 v[120:121], v92 offset:1024\n\t" \
    "ds_read_b64_tr_b16 v[122:123], v92 offset:5632\n\tds_read_b64_tr_b16 v[124:125], v92 offset:10240\n\tds_read_b64_tr_b16 v[126:127], v92 offset:14336\n\tds_read_b64_tr_b16 v[128:129], v92 offset:18944\n\tv_lshlrev_b32 v78, 16, v60\n\ts_cmp_ge_u32 s43, 15\n\ts_cselect_b32 s69, s70, s43\n\ts_mul_i32 s69, s69, 294912\n\t" \
    "s_add_u32 s69, s69, 295040\n\ts_add_u32 m0, s72, 12288\n\tv_and_b32 v79, 0xffff0000, v60\n\tv_lshlrev_b32 v80, 16, v61\n\tv_and_b32 v81, 0xffff0000, v61\n\ts_waitcnt lgkmcnt(10)\n\tv_lshlrev_b32 v66, 16, v36\n\tv_and_b32 v67, 0xffff0000, v36\n\t" \
    "v_lshlrev_b32 v68, 16, v37\n\tbuffer_load_dwordx4 v10, s[12:15], s69 offen lds\n\ts_add_u32 m0, s72, 13312\n\tv_and_b32 v69, 0xffff0000, v37\n\tv_lshlrev_b32 v70, 16, v38\n\tv_and_b32 v71, 0xffff0000, v38\n\tv_lshlrev_b32 v72, 16, v39\n\tv_and_b32 v73, 0xffff0000, v39\n\t" \
    "v_lshlrev_b32 v74, 16, v40\n\tv_and_b32 v75, 0xffff0000, v40\n\tv_lshlrev_b32 v76, 16, v41\n\tv_mul_f32 v16, v66, v81\n\tv_mul_f32 v17, v67, v81\n\tv_mul_f32 v18, v68, v81\n\tv_mul_f32 v19, v69, v81\n\tv_mul_f32 v20, v70, v81\n\t" \
    "v_mul_f32 v21, v71, v81\n\tv_mul_f32 v22, v72, v81\n\tv_mul_f32 v23, v73, v81\n\tv_fmac_f32 v16, v67, v80\n\tv_fmac_f32 v17, v68, v80\n\tv_fmac_f32 v18, v69, v80\n\tv_fmac_f32 v19, v70, v80\n\tv_fmac_f32 v20, v71, v80\n\t" \
    "v_fmac_f32 v21, v72, v80\n\tv_fmac_f32 v22, v73, v80\n\tv_fmac_f32 v23, v74, v80\n\tv_fmac_f32 v16, v68, v79\n\tv_fmac_f32 v17, v69, v79\n\tv_fmac_f32 v18, v70, v79\n\tv_fmac_f32 v19, v71, v79\n\tv_fmac_f32 v20, v72, v79\n\t" \
    "buffer_load_dwordx4 v11, s[12:15], s69 offen lds\n\ts_cmp_ge_u32 s43, 14\n\ts_cselect_b32 s69, s70, s43\n\ts_mul_i32 s69, s69, 294912\n\ts_add_u32 s69, s69, 589824\n\ts_add_u32 m0, s72, 16384\n\tv_fmac_f32 v21, v73, v79\n\tv_fmac_f32 v22, v74, v79\n\t" \
    "v_fmac_f32 v23, v75, v79\n\tv_fmac_f32 v16, v69, v78\n\tv_fmac_f32 v17, v70, v78\n\tv_fmac_f32 v18, v71, v78\n\tv_fmac_f32 v19, v72, v78\n\tv_fmac_f32 v20, v73, v78\n\tv_fmac_f32 v21, v74, v78\n\tv_fmac_f32 v22, v75, v78\n\t" \
    "v_fmac_f32 v23, v76, v78\n\tv_cvt_pk_bf16_f32 v66, v16, v17\n\tv_cvt_pk_bf16_f32 v67, v18, v19\n\tv_cvt_pk_bf16_f32 v68, v20, v21\n\tv_cvt_pk_bf16_f32 v69, v22, v23\n\tv_lshlrev_b32 v16, 16, v66\n\tv_and_b32 v17, 0xffff0000, v66\n\tv_lshlrev_b32 v18, 16, v67\n\t" \
    "v_and_b32 v19, 0xffff0000, v67\n\tv_lshlrev_b32 v20, 16, v68\n\tv_and_b32 v21, 0xffff0000, v68\n\tv_lshlrev_b32 v22, 16, v69\n\tv_and_b32 v23, 0xffff0000, v69\n\tv_lshlrev_b32 v66, 16, v42\n\tv_and_b32 v67, 0xffff0000, v42\n\tbuffer_load_dwordx4 v10, s[12:15], s69 offen lds\n\t" \
    "s_add_u32 m0, s72, 17408\n\tv_lshlrev_b32 v68, 16, v43\n\tv_and_b32 v69, 0xffff0000, v43\n\tv_lshlrev_b32 v70, 16, v44\n\tv_and_b32 v71, 0xffff0000, v44\n\tv_lshlrev_b32 v72, 16, v45\n\tv_and_b32 v73, 0xffff0000, v45\n\tv_pk_mul_f32 v[16:17], v[16:17], v[66:67]\n\t" \
    "v_pk_mul_f32 v[18:19], v[18:19], v[68:69]\n\tv_pk_mul_f32 v[20:21], v[20:21], v[70:71]\n\tv_pk_mul_f32 v[22:23], v[22:23], v[72:73]\n\tv_cvt_pk_bf16_f32 v24, v16, v17\n\tv_cvt_pk_bf16_f32 v25, v18, v19\n\tv_cvt_pk_bf16_f32 v26, v20, v21\n\tv_cvt_pk_bf16_f32 v27, v22, v23\n\ts_add_u32 s68, s62, s54\n\t" \
    "buffer_store_dwordx4 v[24:27], v8, s[8:11], s68 offen sc1\n\ts_waitcnt vmcnt(8) lgkmcnt(0)\n\ts_barrier\n\tbuffer_store_dwordx2 v[248:249], v89, s[8:11], s63 offen sc1\n\ts_cmp_lg_u32 s39, 0\n\ts_cbranch_scc1 104f\n\ts_sub_u32 s73, s40, 216\n\ts_lshl_b32 s71, s73, 4\n\t" \
    "s_mul_i32 s68, s73, 12\n\ts_add_u32 s68, s68, 96\n\ts_cmp_lt_u32 s73, 24\n\ts_cselect_b32 s71, s71, s68\n\ts_cselect_b32 s73, 0, 2146435072\n\tv_cmp_eq_u32 vcc, 0, v240\n\tv_subrev_u32 v190, 8, v8\n\tv_mov_b32 v191, 2146435072\n\t" \
    "v_cndmask_b32 v190, v190, v191, vcc\n\ts_add_u32 s68, s71, s38\n\ts_add_u32 s68, s68, 0\n\ts_lshl_b32 s73, s68, 1\n\ts_lshl_b32 s68, s68, 12\n\tbuffer_load_dwordx4 v[164:167], v8, s[76:79], s68 offen offset:0\n\tbuffer_load_dwordx4 v[168:171], v8, s[76:79], s68 offen offset:1024\n\tbuffer_load_dwordx4 v[172:175], v8, s[76:79], s68 offen offset:2048\n\t" \
    "buffer_load_dwordx4 v[176:179], v8, s[76:79], s68 offen offset:3072\n\tbuffer_load_dwordx2 v[180:181], v190, s[76:79], s68 offen\n\tbuffer_load_dwordx2 v[182:183], v8, s[76:79], s68 offen offset:1016\n\tbuffer_load_dwordx2 v[184:185], v8, s[76:79], s68 offen offset:2040\n\tbuffer_load_dwordx2 v[186:187], v8, s[76:79], s68 offen offset:3064\n\tbuffer_load_ushort v188, off, s[80:83], s73\n\t104:\n\tv_lshlrev_b32 v78, 16, v62\n\t" \
    "ds_read_b64_tr_b16 a[48:49], v0 offset:0\n\tv_and_b32 v79, 0xffff0000, v62\n\tds_read_b64_tr_b16 a[50:51], v0 offset:512\n\tv_lshlrev_b32 v80, 16, v63\n\tds_read_b64_tr_b16 a[52:53], v1 offset:0\n\tv_and_b32 v81, 0xffff0000, v63\n\tds_read_b64_tr_b16 a[54:55], v1 offset:512\n\tv_lshlrev_b32 v66, 16, v46\n\t" \
    "ds_read_b64_tr_b16 a[56:57], v2 offset:0\n\tv_and_b32 v67, 0xffff0000, v46\n\tds_read_b64_tr_b16 a[58:59], v2 offset:512\n\tv_lshlrev_b32 v68, 16, v47\n\tds_read_b64_tr_b16 a[60:61], v3 offset:0\n\tv_and_b32 v69, 0xffff0000, v47\n\tds_read_b64_tr_b16 a[62:63], v3 offset:512\n\tv_lshlrev_b32 v70, 16, v48\n\t" \
    "v_and_b32 v71, 0xffff0000, v48\n\tv_lshlrev_b32 v72, 16, v49\n\tv_and_b32 v73, 0xffff0000, v49\n\tv_lshlrev_b32 v74, 16, v50\n\tv_and_b32 v75, 0xffff0000, v50\n\tv_lshlrev_b32 v76, 16, v51\n\tv_mul_f32 v16, v66, v81\n\tv_mul_f32 v17, v67, v81\n\t" \
    "v_mul_f32 v18, v68, v81\n\tv_mul_f32 v19, v69, v81\n\tv_mul_f32 v20, v70, v81\n\tbuffer_load_dwordx4 v11, s[12:15], s69 offen lds\n\ts_cmp_ge_u32 s43, 14\n\ts_cselect_b32 s69, s70, s43\n\ts_mul_i32 s69, s69, 294912\n\ts_add_u32 s69, s69, 589952\n\t" \
    "s_add_u32 m0, s72, 20480\n\tv_mul_f32 v21, v71, v81\n\tv_mul_f32 v22, v72, v81\n\tv_mul_f32 v23, v73, v81\n\tv_fmac_f32 v16, v67, v80\n\tv_fmac_f32 v17, v68, v80\n\tv_fmac_f32 v18, v69, v80\n\tv_fmac_f32 v19, v70, v80\n\t" \
    "v_fmac_f32 v20, v71, v80\n\tv_fmac_f32 v21, v72, v80\n\tv_fmac_f32 v22, v73, v80\n\tv_fmac_f32 v23, v74, v80\n\tv_fmac_f32 v16, v68, v79\n\tv_fmac_f32 v17, v69, v79\n\tv_fmac_f32 v18, v70, v79\n\tv_fmac_f32 v19, v71, v79\n\t" \
    "v_fmac_f32 v20, v72, v79\n\tv_fmac_f32 v21, v73, v79\n\tv_fmac_f32 v22, v74, v79\n\tv_fmac_f32 v23, v75, v79\n\tv_fmac_f32 v16, v69, v78\n\tv_fmac_f32 v17, v70, v78\n\tv_fmac_f32 v18, v71, v78\n\tv_fmac_f32 v19, v72, v78\n\t" \
    "v_fmac_f32 v20, v73, v78\n\tv_fmac_f32 v21, v74, v78\n\tbuffer_load_dwordx4 v10, s[12:15], s69 offen lds\n\ts_add_u32 m0, s72, 21504\n\tv_fmac_f32 v22, v75, v78\n\tv_fmac_f32 v23, v76, v78\n\tv_cvt_pk_bf16_f32 v66, v16, v17\n\tv_cvt_pk_bf16_f32 v67, v18, v19\n\t" \
    "v_cvt_pk_bf16_f32 v68, v20, v21\n\tv_cvt_pk_bf16_f32 v69, v22, v23\n\tv_lshlrev_b32 v16, 16, v66\n\tv_and_b32 v17, 0xffff0000, v66\n\tv_lshlrev_b32 v18, 16, v67\n\tv_and_b32 v19, 0xffff0000, v67\n\tv_lshlrev_b32 v20, 16, v68\n\tv_and_b32 v21, 0xffff0000, v68\n\t" \
    "v_lshlrev_b32 v22, 16, v69\n\tv_and_b32 v23, 0xffff0000, v69\n\tv_lshlrev_b32 v66, 16, v52\n\tv_and_b32 v67, 0xffff0000, v52\n\tv_lshlrev_b32 v68, 16, v53\n\tv_and_b32 v69, 0xffff0000, v53\n\tv_lshlrev_b32 v70, 16, v54\n\tv_and_b32 v71, 0xffff0000, v54\n\t" \
    "v_lshlrev_b32 v72, 16, v55\n\tv_and_b32 v73, 0xffff0000, v55\n\tv_pk_mul_f32 v[16:17], v[16:17], v[66:67]\n\tv_pk_mul_f32 v[18:19], v[18:19], v[68:69]\n\tv_pk_mul_f32 v[20:21], v[20:21], v[70:71]\n\tv_pk_mul_f32 v[22:23], v[22:23], v[72:73]\n\tv_cvt_pk_bf16_f32 v28, v16, v17\n\tv_cvt_pk_bf16_f32 v29, v18, v19\n\t" \
    "v_cvt_pk_bf16_f32 v30, v20, v21\n\tbuffer_load_dwordx4 v11, s[12:15], s69 offen lds\n\ts_cmp_ge_u32 s43, 13\n\ts_cselect_b32 s69, s70, s43\n\ts_mul_i32 s69, s69, 294912\n\ts_add_u32 s69, s69, 884736\n\ts_add_u32 m0, s72, 24576\n\tv_cvt_pk_bf16_f32 v31, v22, v23\n\t" \
    "s_add_u32 s68, s62, s54\n\tbuffer_store_dwordx4 v[28:31], v9, s[8:11], s68 offen sc1\n\tv_lshlrev_b32 v78, 16, v64\n\tv_and_b32 v79, 0xffff0000, v64\n\tv_lshlrev_b32 v80, 16, v65\n\tv_and_b32 v81, 0xffff0000, v65\n\tv_lshlrev_b32 v66, 16, v120\n\tv_and_b32 v67, 0xffff0000, v120\n\t" \
    "v_lshlrev_b32 v68, 16, v121\n\tv_and_b32 v69, 0xffff0000, v121\n\tv_lshlrev_b32 v70, 16, v122\n\tv_and_b32 v71, 0xffff0000, v122\n\tv_lshlrev_b32 v72, 16, v123\n\tv_and_b32 v73, 0xffff0000, v123\n\tv_lshlrev_b32 v74, 16, v124\n\tv_and_b32 v75, 0xffff0000, v124\n\t" \
    "v_lshlrev_b32 v76, 16, v125\n\tv_mul_f32 v16, v66, v81\n\tv_mul_f32 v17, v67, v81\n\tv_mul_f32 v18, v68, v81\n\tv_mul_f32 v19, v69, v81\n\tv_mul_f32 v20, v70, v81\n\tv_mul_f32 v21, v71, v81\n\tv_mul_f32 v22, v72, v81\n\t" \
    "v_mul_f32 v23, v73, v81\n\tv_fmac_f32 v16, v67, v80\n\tv_fmac_f32 v17, v68, v80\n\tv_fmac_f32 v18, v69, v80\n\tv_fmac_f32 v19, v70, v80\n\tv_fmac_f32 v20, v71, v80\n\tv_fmac_f32 v21, v72, v80\n\tv_fmac_f32 v22, v73, v80\n\t" \
    "buffer_load_dwordx4 v10, s[12:15], s69 offen lds\n\ts_add_u32 m0, s72, 25600\n\tv_fmac_f32 v23, v74, v80\n\tv_fmac_f32 v16, v68, v79\n\tv_fmac_f32 v17, v69, v79\n\tv_fmac_f32 v18, v70, v79\n\tv_fmac_f32 v19, v71, v79\n\tv_fmac_f32 v20, v72, v79\n\t" \
    "v_fmac_f32 v21, v73, v79\n\tv_fmac_f32 v22, v74, v79\n\tv_fmac_f32 v23, v75, v79\n\tv_fmac_f32 v16, v69, v78\n\tv_fmac_f32 v17, v70, v78\n\tv_fmac_f32 v18, v71, v78\n\tv_fmac_f32 v19, v72, v78\n\tv_fmac_f32 v20, v73, v78\n\t" \
    "v_fmac_f32 v21, v74, v78\n\tv_fmac_f32 v22, v75, v78\n\tv_fmac_f32 v23, v76, v78\n\tv_cvt_pk_bf16_f32 v66, v16, v17\n\tv_cvt_pk_bf16_f32 v67, v18, v19\n\tv_cvt_pk_bf16_f32 v68, v20, v21\n\tv_cvt_pk_bf16_f32 v69, v22, v23\n\tv_lshlrev_b32 v16, 16, v66\n\t" \
    "v_and_b32 v17, 0xffff0000, v66\n\tv_lshlrev_b32 v18, 16, v67\n\tv_and_b32 v19, 0xffff0000, v67\n\tv_lshlrev_b32 v20, 16, v68\n\tv_and_b32 v21, 0xffff0000, v68\n\tv_lshlrev_b32 v22, 16, v69\n\tv_and_b32 v23, 0xffff0000, v69\n\tbuffer_load_dwordx4 v11, s[12:15], s69 offen lds\n\t" \
    "s_cmp_ge_u32 s43, 13\n\ts_cselect_b32 s69, s70, s43\n\ts_mul_i32 s69, s69, 294912\n\ts_add_u32 s69, s69, 884864\n\ts_add_u32 m0, s72, 28672\n\tv_lshlrev_b32 v66, 16, v126\n\tv_and_b32 v67, 0xffff0000, v126\n\tv_lshlrev_b32 v68, 16, v127\n\t" \
    "v_and_b32 v69, 0xffff0000, v127\n\tv_lshlrev_b32 v70, 16, v128\n\tv_and_b32 v71, 0xffff0000, v128\n\tv_lshlrev_b32 v72, 16, v129\n\tv_and_b32 v73, 0xffff0000, v129\n\tv_pk_mul_f32 v[16:17], v[16:17], v[66:67]\n\tv_pk_mul_f32 v[18:19], v[18:19], v[68:69]\n\tv_pk_mul_f32 v[20:21], v[20:21], v[70:71]\n\t" \
    "v_pk_mul_f32 v[22:23], v[22:23], v[72:73]\n\tv_cvt_pk_bf16_f32 v32, v16, v17\n\tv_cvt_pk_bf16_f32 v33, v18, v19\n\tv_cvt_pk_bf16_f32 v34, v20, v21\n\tv_cvt_pk_bf16_f32 v35, v22, v23\n\ts_add_u32 s68, s62, 8192\n\ts_cmp_eq_u32 s38, 1\n\ts_cselect_b32 s68, s68, 0x7ff00000\n\t" \
    "buffer_store_dwordx4 v[32:35], v8, s[8:11], s68 offen sc1\n\ts_cmp_eq_u32 s39, 0\n\ts_cbranch_scc1 101f\n\tbuffer_load_dwordx4 v10, s[12:15], s69 offen lds\n\ts_add_u32 m0, s72, 29696\n\ts_nop 0\n\tbuffer_load_dwordx4 v11, s[12:15], s69 offen lds\n\ts_cmp_ge_u32 s43, 12\n\t" \
    "s_cselect_b32 s69, s70, s43\n\ts_mul_i32 s69, s69, 294912\n\ts_add_u32 s69, s69, 1179648\n\ts_add_u32 m0, s72, 32768\n\ts_nop 0\n\tbuffer_load_dwordx4 v10, s[12:15], s69 offen lds\n\ts_add_u32 m0, s72, 33792\n\ts_nop 0\n\t" \
    "buffer_load_dwordx4 v11, s[12:15], s69 offen lds\n\ts_cmp_ge_u32 s43, 12\n\ts_cselect_b32 s69, s70, s43\n\ts_mul_i32 s69, s69, 294912\n\ts_add_u32 s69, s69, 1179776\n\ts_add_u32 m0, s72, 36864\n\ts_nop 0\n\tbuffer_load_dwordx4 v10, s[12:15], s69 offen lds\n\t" \
    "s_add_u32 m0, s72, 37888\n\ts_nop 0\n\tbuffer_load_dwordx4 v11, s[12:15], s69 offen lds\n\ts_cmp_ge_u32 s43, 11\n\ts_cselect_b32 s69, s70, s43\n\ts_mul_i32 s69, s69, 294912\n\ts_add_u32 s69, s69, 1474560\n\ts_add_u32 m0, s72, 40960\n\t" \
    "s_nop 0\n\tbuffer_load_dwordx4 v10, s[12:15], s69 offen lds\n\ts_add_u32 m0, s72, 41984\n\ts_nop 0\n\tbuffer_load_dwordx4 v11, s[12:15], s69 offen lds\n\ts_waitcnt vmcnt(9) lgkmcnt(0)\n\ts_barrier\n\ts_xor_b32 s71, s57, 1\n\t" \
    "s_lshl_b32 s71, s71, 6\n\ts_lshl_b32 s69, s42, 8\n\ts_add_u32 s71, s71, s69\n\tv_lshlrev_b32 v96, 3, v245\n\tbuffer_load_dwordx2 v[112:113], v96, s[28:31], s71 offen offset:0\n\tds_read_b64_tr_b16 v[120:121], v0 offset:4096\n\tds_read_b64_tr_b16 v[122:123], v0 offset:4608\n\tds_read_b64_tr_b16 v[124:125], v1 offset:4096\n\t" \
    "ds_read_b64_tr_b16 v[126:127], v1 offset:4608\n\tbuffer_load_dwordx2 v[114:115], v96, s[28:31], s71 offen offset:32\n\tds_read_b64_tr_b16 v[128:129], v2 offset:4096\n\tds_read_b64_tr_b16 v[130:131], v2 offset:4608\n\tds_read_b64_tr_b16 v[132:133], v3 offset:4096\n\tds_read_b64_tr_b16 v[134:135], v3 offset:4608\n\ts_waitcnt vmcnt(10)\n\tbuffer_store_dwordx2 v[248:249], off, s[8:11], s48 sc1\n\t" \
    "buffer_load_dwordx2 v[116:117], v96, s[28:31], s71 offen offset:128\n\tds_read_b64_tr_b16 v[136:137], v0 offset:8192\n\tds_read_b64_tr_b16 v[138:139], v0 offset:8704\n\tds_read_b64_tr_b16 v[140:141], v1 offset:8192\n\tds_read_b64_tr_b16 v[142:143], v1 offset:8704\n\tbuffer_load_dwordx2 v[118:119], v96, s[28:31], s71 offen offset:160\n\tds_read_b64_tr_b16 v[144:145], v2 offset:8192\n\tds_read_b64_tr_b16 v[146:147], v2 offset:8704\n\t" \
    "ds_read_b64_tr_b16 v[148:149], v3 offset:8192\n\tds_read_b64_tr_b16 v[150:151], v3 offset:8704\n\tv_lshrrev_b32 v229, 3, v244\n\tbuffer_load_ushort v228, v229, s[32:35], 0 offen offset:0\n\tds_read_b64_tr_b16 v[152:153], v0 offset:12288\n\tds_read_b64_tr_b16 v[154:155], v0 offset:12800\n\tds_read_b64_tr_b16 v[156:157], v1 offset:12288\n\tds_read_b64_tr_b16 v[158:159], v1 offset:12800\n\t" \
    "buffer_load_ushort v232, v229, s[32:35], 0 offen offset:32\n\tds_read_b64_tr_b16 v[160:161], v2 offset:12288\n\tds_read_b64_tr_b16 v[162:163], v2 offset:12800\n\tds_read_b64_tr_b16 v[164:165], v3 offset:12288\n\tds_read_b64_tr_b16 v[166:167], v3 offset:12800\n\ts_cmp_ge_u32 s43, 11\n\ts_cselect_b32 s69, s70, s43\n\ts_mul_i32 s69, s69, 294912\n\t" \
    "s_add_u32 s69, s69, 1474688\n\ts_add_u32 m0, s72, 45056\n\ts_nop 0\n\tbuffer_load_dwordx4 v10, s[12:15], s69 offen lds\n\tds_read_b64_tr_b16 v[168:169], v0 offset:16384\n\tds_read_b64_tr_b16 v[170:171], v0 offset:16896\n\tds_read_b64_tr_b16 v[172:173], v1 offset:16384\n\tds_read_b64_tr_b16 v[174:175], v1 offset:16896\n\t" \
    "s_add_u32 m0, s72, 46080\n\ts_nop 0\n\tbuffer_load_dwordx4 v11, s[12:15], s69 offen lds\n\tds_read_b64_tr_b16 v[176:177], v2 offset:16384\n\tds_read_b64_tr_b16 v[178:179], v2 offset:16896\n\tds_read_b64_tr_b16 v[180:181], v3 offset:16384\n\tds_read_b64_tr_b16 v[182:183], v3 offset:16896\n\tds_read_b64_tr_b16 v[184:185], v0 offset:20480\n\t" \
    "ds_read_b64_tr_b16 v[186:187], v0 offset:20992\n\tds_read_b64_tr_b16 v[188:189], v1 offset:20480\n\tds_read_b64_tr_b16 v[190:191], v1 offset:20992\n\tds_read_b64_tr_b16 v[192:193], v2 offset:20480\n\tds_read_b64_tr_b16 v[194:195], v2 offset:20992\n\tds_read_b64_tr_b16 v[196:197], v3 offset:20480\n\tds_read_b64_tr_b16 v[198:199], v3 offset:20992\n\tds_read_b64_tr_b16 v[200:201], v0 offset:24576\n\t" \
    "ds_read_b64_tr_b16 v[202:203], v0 offset:25088\n\tds_read_b64_tr_b16 v[204:205], v1 offset:24576\n\tds_read_b64_tr_b16 v[206:207], v1 offset:25088\n\tds_read_b64_tr_b16 v[208:209], v2 offset:24576\n\tds_read_b64_tr_b16 v[210:211], v2 offset:25088\n\tds_read_b64_tr_b16 v[212:213], v3 offset:24576\n\tds_read_b64_tr_b16 v[214:215], v3 offset:25088\n\ts_waitcnt vmcnt(9) lgkmcnt(0)\n\t" \
    "s_barrier\n\ts_cmp_ge_u32 s43, 10\n\ts_cselect_b32 s69, s70, s43\n\ts_mul_i32 s69, s69, 294912\n\ts_add_u32 s69, s69, 1769472\n\ts_add_u32 m0, s72, 49152\n\ts_nop 0\n\tbuffer_load_dwordx4 v10, s[12:15], s69 offen lds\n\t" \
    "ds_read_b64_tr_b16 v[216:217], v0 offset:28672\n\tds_read_b64_tr_b16 v[218:219], v0 offset:29184\n\tds_read_b64_tr_b16 a[64:65], v1 offset:28672\n\tds_read_b64_tr_b16 a[66:67], v1 offset:29184\n\ts_add_u32 m0, s72, 50176\n\ts_nop 0\n\tbuffer_load_dwordx4 v11, s[12:15], s69 offen lds\n\tds_read_b64_tr_b16 a[68:69], v2 offset:28672\n\t" \
    "ds_read_b64_tr_b16 a[70:71], v2 offset:29184\n\tds_read_b64_tr_b16 a[72:73], v3 offset:28672\n\tds_read_b64_tr_b16 a[74:75], v3 offset:29184\n\ts_cmp_ge_u32 s43, 10\n\ts_cselect_b32 s69, s70, s43\n\ts_mul_i32 s69, s69, 294912\n\ts_add_u32 s69, s69, 1769600\n\ts_add_u32 m0, s72, 53248\n\t" \
    "s_nop 0\n\tbuffer_load_dwordx4 v10, s[12:15], s69 offen lds\n\tds_read_b64_tr_b16 a[76:77], v0 offset:32768\n\tds_read_b64_tr_b16 a[78:79], v0 offset:33280\n\tds_read_b64_tr_b16 a[80:81], v1 offset:32768\n\tds_read_b64_tr_b16 a[82:83], v1 offset:33280\n\ts_add_u32 m0, s72, 54272\n\ts_nop 0\n\t" \
    "buffer_load_dwordx4 v11, s[12:15], s69 offen lds\n\tds_read_b64_tr_b16 a[84:85], v2 offset:32768\n\tds_read_b64_tr_b16 a[86:87], v2 offset:33280\n\tds_read_b64_tr_b16 a[88:89], v3 offset:32768\n\tds_read_b64_tr_b16 a[90:91], v3 offset:33280\n\ts_cmp_ge_u32 s43, 9\n\ts_cselect_b32 s69, s70, s43\n\ts_mul_i32 s69, s69, 294912\n\t" \
    "s_add_u32 s69, s69, 2064384\n\ts_add_u32 m0, s72, 57344\n\ts_nop 0\n\tbuffer_load_dwordx4 v10, s[12:15], s69 offen lds\n\tds_read_b64_tr_b16 a[92:93], v0 offset:36864\n\tds_read_b64_tr_b16 a[94:95], v0 offset:37376\n\tds_read_b64_tr_b16 a[96:97], v1 offset:36864\n\tds_read_b64_tr_b16 a[98:99], v1 offset:37376\n\t" \
    "s_add_u32 m0, s72, 58368\n\ts_nop 0\n\tbuffer_load_dwordx4 v11, s[12:15], s69 offen lds\n\tds_read_b64_tr_b16 a[100:101], v2 offset:36864\n\tds_read_b64_tr_b16 a[102:103], v2 offset:37376\n\tds_read_b64_tr_b16 a[104:105], v3 offset:36864\n\tds_read_b64_tr_b16 a[106:107], v3 offset:37376\n\ts_cmp_ge_u32 s43, 9\n\t" \
    "s_cselect_b32 s69, s70, s43\n\ts_mul_i32 s69, s69, 294912\n\ts_add_u32 s69, s69, 2064512\n\ts_add_u32 m0, s72, 61440\n\ts_nop 0\n\tbuffer_load_dwordx4 v10, s[12:15], s69 offen lds\n\tds_read_b64_tr_b16 a[108:109], v0 offset:40960\n\tds_read_b64_tr_b16 a[110:111], v0 offset:41472\n\t" \
    "ds_read_b64_tr_b16 a[112:113], v1 offset:40960\n\tds_read_b64_tr_b16 a[114:115], v1 offset:41472\n\ts_add_u32 m0, s72, 62464\n\ts_nop 0\n\tbuffer_load_dwordx4 v11, s[12:15], s69 offen lds\n\tds_read_b64_tr_b16 a[116:117], v2 offset:40960\n\tds_read_b64_tr_b16 a[118:119], v2 offset:41472\n\tds_read_b64_tr_b16 a[120:121], v3 offset:40960\n\t" \
    "ds_read_b64_tr_b16 a[122:123], v3 offset:41472\n\ts_waitcnt vmcnt(8) lgkmcnt(0)\n\ts_barrier\n\ts_cmp_ge_u32 s43, 8\n\ts_cselect_b32 s69, s70, s43\n\ts_mul_i32 s69, s69, 294912\n\ts_add_u32 s69, s69, 2359296\n\ts_add_u32 m0, s72, 0\n\t" \
    "s_nop 0\n\tbuffer_load_dwordx4 v10, s[12:15], s69 offen lds\n\tds_read_b64_tr_b16 a[124:125], v0 offset:45056\n\tds_read_b64_tr_b16 a[126:127], v0 offset:45568\n\tds_read_b64_tr_b16 a[128:129], v1 offset:45056\n\tds_read_b64_tr_b16 a[130:131], v1 offset:45568\n\ts_add_u32 m0, s72, 1024\n\ts_nop 0\n\t" \
    "buffer_load_dwordx4 v11, s[12:15], s69 offen lds\n\tds_read_b64_tr_b16 a[132:133], v2 offset:45056\n\tds_read_b64_tr_b16 a[134:135], v2 offset:45568\n\tds_read_b64_tr_b16 a[136:137], v3 offset:45056\n\tds_read_b64_tr_b16 a[138:139], v3 offset:45568\n\ts_cmp_ge_u32 s43, 8\n\ts_cselect_b32 s69, s70, s43\n\ts_mul_i32 s69, s69, 294912\n\t" \
    "s_add_u32 s69, s69, 2359424\n\ts_add_u32 m0, s72, 4096\n\ts_nop 0\n\tbuffer_load_dwordx4 v10, s[12:15], s69 offen lds\n\ts_add_u32 m0, s72, 5120\n\ts_nop 0\n\tbuffer_load_dwordx4 v11, s[12:15], s69 offen lds\n\ts_cmp_ge_u32 s43, 7\n\t" \
    "s_cselect_b32 s69, s70, s43\n\ts_mul_i32 s69, s69, 294912\n\ts_add_u32 s69, s69, 2654208\n\ts_add_u32 m0, s72, 8192\n\ts_nop 0\n\tbuffer_load_dwordx4 v10, s[12:15], s69 offen lds\n\ts_add_u32 m0, s72, 9216\n\ts_nop 0\n\t" \
    "buffer_load_dwordx4 v11, s[12:15], s69 offen lds\n\ts_cmp_ge_u32 s43, 7\n\ts_cselect_b32 s69, s70, s43\n\ts_mul_i32 s69, s69, 294912\n\ts_add_u32 s69, s69, 2654336\n\ts_add_u32 m0, s72, 12288\n\ts_nop 0\n\tbuffer_load_dwordx4 v10, s[12:15], s69 offen lds\n\t" \
    "s_add_u32 m0, s72, 13312\n\ts_nop 0\n\tbuffer_load_dwordx4 v11, s[12:15], s69 offen lds\n\ts_waitcnt lgkmcnt(0)\n\ts_barrier\n\ts_cmp_ge_u32 s43, 6\n\ts_cselect_b32 s69, s70, s43\n\ts_mul_i32 s69, s69, 294912\n\t" \
    "s_add_u32 s69, s69, 2949120\n\ts_add_u32 m0, s72, 16384\n\ts_nop 0\n\tbuffer_load_dwordx4 v10, s[12:15], s69 offen lds\n\ts_add_u32 m0, s72, 17408\n\ts_nop 0\n\tbuffer_load_dwordx4 v11, s[12:15], s69 offen lds\n\tbuffer_load_dwordx4 v[250:253], v8, s[8:11], s47 offen sc1\n\t" \
    "s_cmp_ge_u32 s43, 6\n\ts_cselect_b32 s69, s70, s43\n\ts_mul_i32 s69, s69, 294912\n\ts_add_u32 s69, s69, 2949248\n\ts_add_u32 m0, s72, 20480\n\ts_nop 0\n\tbuffer_load_dwordx4 v10, s[12:15], s69 offen lds\n\ts_add_u32 m0, s72, 21504\n\t" \
    "s_nop 0\n\tbuffer_load_dwordx4 v11, s[12:15], s69 offen lds\n\ts_cmp_ge_u32 s43, 5\n\ts_cselect_b32 s69, s70, s43\n\ts_mul_i32 s69, s69, 294912\n\ts_add_u32 s69, s69, 3244032\n\ts_add_u32 m0, s72, 24576\n\ts_nop 0\n\t" \
    "buffer_load_dwordx4 v10, s[12:15], s69 offen lds\n\ts_add_u32 m0, s72, 25600\n\ts_nop 0\n\tbuffer_load_dwordx4 v11, s[12:15], s69 offen lds\n\ts_cmp_ge_u32 s43, 5\n\ts_cselect_b32 s69, s70, s43\n\ts_mul_i32 s69, s69, 294912\n\ts_add_u32 s69, s69, 3244160\n\t" \
    "s_add_u32 m0, s72, 28672\n\ts_nop 0\n\tbuffer_load_dwordx4 v10, s[12:15], s69 offen lds\n\ts_add_u32 m0, s72, 29696\n\ts_nop 0\n\tbuffer_load_dwordx4 v11, s[12:15], s69 offen lds\n\ts_cmp_ge_u32 s43, 4\n\ts_cselect_b32 s69, s70, s43\n\t" \
    "s_mul_i32 s69, s69, 294912\n\ts_add_u32 s69, s69, 3538944\n\ts_add_u32 m0, s72, 32768\n\ts_nop 0\n\tbuffer_load_dwordx4 v10, s[12:15], s69 offen lds\n\ts_add_u32 m0, s72, 33792\n\ts_nop 0\n\tbuffer_load_dwordx4 v11, s[12:15], s69 offen lds\n\t" \
    "s_cmp_ge_u32 s43, 4\n\ts_cselect_b32 s69, s70, s43\n\ts_mul_i32 s69, s69, 294912\n\ts_add_u32 s69, s69, 3539072\n\ts_add_u32 m0, s72, 36864\n\ts_nop 0\n\tbuffer_load_dwordx4 v10, s[12:15], s69 offen lds\n\ts_add_u32 m0, s72, 37888\n\t" \
    "s_nop 0\n\tbuffer_load_dwordx4 v11, s[12:15], s69 offen lds\n\ts_cmp_ge_u32 s43, 3\n\ts_cselect_b32 s69, s70, s43\n\ts_mul_i32 s69, s69, 294912\n\ts_add_u32 s69, s69, 3833856\n\ts_add_u32 m0, s72, 40960\n\ts_nop 0\n\t" \
    "buffer_load_dwordx4 v10, s[12:15], s69 offen lds\n\ts_add_u32 m0, s72, 41984\n\ts_nop 0\n\tbuffer_load_dwordx4 v11, s[12:15], s69 offen lds\n\ts_cmp_ge_u32 s43, 3\n\ts_cselect_b32 s69, s70, s43\n\ts_mul_i32 s69, s69, 294912\n\ts_add_u32 s69, s69, 3833984\n\t" \
    "s_add_u32 m0, s72, 45056\n\ts_nop 0\n\tbuffer_load_dwordx4 v10, s[12:15], s69 offen lds\n\ts_add_u32 m0, s72, 46080\n\ts_nop 0\n\tbuffer_load_dwordx4 v11, s[12:15], s69 offen lds\n\ts_waitcnt vmcnt(14)\n\tv_cmp_ne_u64 vcc, s[36:37], v[250:251]\n\t" \
    "v_cmp_ne_u64 s[74:75], s[36:37], v[252:253]\n\ts_or_b64 s[74:75], s[74:75], vcc\n\ts_cbranch_scc0 106f\n\ts_mov_b32 s71, 1048576\n\t105:\n\ts_sleep 1\n\tbuffer_load_dwordx4 v[250:253], v8, s[8:11], s47 offen sc1\n\ts_waitcnt vmcnt(0)\n\t" \
    "v_cmp_ne_u64 vcc, s[36:37], v[250:251]\n\tv_cmp_ne_u64 s[74:75], s[36:37], v[252:253]\n\ts_or_b64 s[74:75], s[74:75], vcc\n\ts_cbranch_scc0 106f\n\ts_sub_u32 s71, s71, 1\n\ts_cbranch_scc0 105b\n\t106:\n\ts_cmp_ge_u32 s43, 16\n\t" \
    "s_cselect_b32 s68, s70, s43\n\ts_mul_i32 s68, s68, 36864\n\ts_add_u32 s68, s68, s44\n\ts_add_u32 s68, s68, 0\n\tbuffer_load_dwordx4 a[140:143], v8, s[8:11], s68 offen offset:0 sc1\n\tbuffer_load_dwordx4 a[144:147], v8, s[8:11], s68 offen offset:1024 sc1\n\tbuffer_load_dwordx4 a[148:151], v8, s[8:11], s68 offen offset:2048 sc1\n\ts_cmp_ge_u32 s43, 15\n\t" \
    "s_cselect_b32 s68, s70, s43\n\ts_mul_i32 s68, s68, 36864\n\ts_add_u32 s68, s68, s44\n\ts_add_u32 s68, s68, 36864\n\tbuffer_load_dwordx4 a[152:155], v8, s[8:11], s68 offen offset:0 sc1\n\tbuffer_load_dwordx4 a[156:159], v8, s[8:11], s68 offen offset:1024 sc1\n\tbuffer_load_dwordx4 a[160:163], v8, s[8:11], s68 offen offset:2048 sc1\n\ts_cmp_ge_u32 s43, 14\n\t" \
    "s_cselect_b32 s68, s70, s43\n\ts_mul_i32 s68, s68, 36864\n\ts_add_u32 s68, s68, s44\n\ts_add_u32 s68, s68, 73728\n\tbuffer_load_dwordx4 a[164:167], v8, s[8:11], s68 offen offset:0 sc1\n\tbuffer_load_dwordx4 a[168:171], v8, s[8:11], s68 offen offset:1024 sc1\n\tbuffer_load_dwordx4 a[172:175], v8, s[8:11], s68 offen offset:2048 sc1\n\ts_cmp_ge_u32 s43, 13\n\t" \
    "s_cselect_b32 s68, s70, s43\n\ts_mul_i32 s68, s68, 36864\n\ts_add_u32 s68, s68, s44\n\ts_add_u32 s68, s68, 110592\n\tbuffer_load_dwordx4 a[176:179], v8, s[8:11], s68 offen offset:0 sc1\n\tbuffer_load_dwordx4 a[180:183], v8, s[8:11], s68 offen offset:1024 sc1\n\tbuffer_load_dwordx4 a[184:187], v8, s[8:11], s68 offen offset:2048 sc1\n\tv_lshlrev_b32 v96, 16, v112\n\t" \
    "v_and_b32 v97, 0xffff0000, v112\n\tv_lshlrev_b32 v98, 16, v113\n\tv_and_b32 v99, 0xffff0000, v113\n\tv_lshlrev_b32 v100, 16, v114\n\tv_and_b32 v101, 0xffff0000, v114\n\tv_lshlrev_b32 v102, 16, v115\n\tv_and_b32 v103, 0xffff0000, v115\n\tv_lshlrev_b32 v104, 16, v116\n\t" \
    "v_and_b32 v105, 0xffff0000, v116\n\tv_lshlrev_b32 v106, 16, v117\n\tv_and_b32 v107, 0xffff0000, v117\n\tv_lshlrev_b32 v108, 16, v118\n\tv_and_b32 v109, 0xffff0000, v118\n\tv_lshlrev_b32 v110, 16, v119\n\tv_and_b32 v111, 0xffff0000, v119\n\tv_lshlrev_b32 v228, 16, v228\n\t" \
    "v_mov_b32 v229, v228\n\tv_mov_b32 v230, v228\n\tv_mov_b32 v231, v228\n\tv_lshlrev_b32 v232, 16, v232\n\tv_mov_b32 v233, v232\n\tv_mov_b32 v234, v232\n\tv_mov_b32 v235, v232\n\ts_barrier\n\t" \
    "s_waitcnt vmcnt(9)\n\tv_mfma_f32_16x16x32_bf16 a[0:3], a[48:51], a[140:143], 0\n\ts_cmp_ge_u32 s43, 12\n\ts_cselect_b32 s68, s70, s43\n\ts_mul_i32 s68, s68, 36864\n\ts_add_u32 s68, s68, s44\n\tv_mfma_f32_16x16x32_bf16 a[8:11], a[48:51], a[144:147], 0\n\ts_add_u32 s68, s68, 147456\n\t" \
    "v_mfma_f32_16x16x32_bf16 a[16:19], a[48:51], a[148:151], 0\n\tv_mfma_f32_16x16x32_bf16 a[4:7], a[52:55], a[140:143], 0\n\tv_mfma_f32_16x16x32_bf16 a[12:15], a[52:55], a[144:147], 0\n\tbuffer_load_dwordx4 a[188:191], v8, s[8:11], s68 offen offset:0 sc1\n\tv_mfma_f32_16x16x32_bf16 a[20:23], a[52:55], a[148:151], 0\n\tv_mfma_f32_16x16x32_bf16 v[48:51], a[56:59], a[140:143], v[96:99]\n\tv_mfma_f32_16x16x32_bf16 v[56:59], a[56:59], a[144:147], v[96:99]\n\tbuffer_load_dwordx4 a[192:195], v8, s[8:11], s68 offen offset:1024 sc1\n\t" \
    "v_mfma_f32_16x16x32_bf16 v[64:67], a[56:59], a[148:151], v[96:99]\n\tv_mfma_f32_16x16x32_bf16 v[52:55], a[60:63], a[140:143], v[100:103]\n\tv_mfma_f32_16x16x32_bf16 v[60:63], a[60:63], a[144:147], v[100:103]\n\tbuffer_load_dwordx4 a[196:199], v8, s[8:11], s68 offen offset:2048 sc1\n\ts_cmp_ge_u32 s43, 11\n\ts_cselect_b32 s68, s70, s43\n\ts_mul_i32 s68, s68, 36864\n\tv_mfma_f32_16x16x32_bf16 v[68:71], a[60:63], a[148:151], v[100:103]\n\t" \
    "s_add_u32 s68, s68, s44\n\ts_add_u32 s68, s68, 184320\n\tv_mfma_f32_16x16x32_bf16 a[24:27], v[120:123], a[140:143], 0\n\tv_mfma_f32_16x16x32_bf16 a[32:35], v[120:123], a[144:147], 0\n\tbuffer_load_dwordx4 a[48:51], v8, s[8:11], s68 offen offset:0 sc1\n\tv_mfma_f32_16x16x32_bf16 a[40:43], v[120:123], a[148:151], 0\n\tv_mfma_f32_16x16x32_bf16 a[28:31], v[124:127], a[140:143], 0\n\tv_mfma_f32_16x16x32_bf16 a[36:39], v[124:127], a[144:147], 0\n\t" \
    "buffer_load_dwordx4 a[52:55], v8, s[8:11], s68 offen offset:1024 sc1\n\tv_mfma_f32_16x16x32_bf16 a[44:47], v[124:127], a[148:151], 0\n\tv_mfma_f32_16x16x32_bf16 v[72:75], v[128:131], a[140:143], v[104:107]\n\tv_mfma_f32_16x16x32_bf16 v[80:83], v[128:131], a[144:147], v[104:107]\n\tbuffer_load_dwordx4 a[56:59], v8, s[8:11], s68 offen offset:2048 sc1\n\ts_cmp_ge_u32 s43, 10\n\ts_cselect_b32 s68, s70, s43\n\ts_mul_i32 s68, s68, 36864\n\t" \
    "v_mfma_f32_16x16x32_bf16 v[88:91], v[128:131], a[148:151], v[104:107]\n\ts_add_u32 s68, s68, s44\n\ts_add_u32 s68, s68, 221184\n\tv_mfma_f32_16x16x32_bf16 v[76:79], v[132:135], a[140:143], v[108:111]\n\tv_mfma_f32_16x16x32_bf16 v[84:87], v[132:135], a[144:147], v[108:111]\n\tbuffer_load_dwordx4 v[120:123], v8, s[8:11], s68 offen offset:0 sc1\n\tv_mfma_f32_16x16x32_bf16 v[92:95], v[132:135], a[148:151], v[108:111]\n\ts_barrier\n\t" \
    "s_waitcnt vmcnt(13)\n\tv_mfma_f32_16x16x32_bf16 a[0:3], v[136:139], a[152:155], a[0:3]\n\tv_mfma_f32_16x16x32_bf16 a[8:11], v[136:139], a[156:159], a[8:11]\n\tbuffer_load_dwordx4 v[124:127], v8, s[8:11], s68 offen offset:1024 sc1\n\tv_mfma_f32_16x16x32_bf16 a[16:19], v[136:139], a[160:163], a[16:19]\n\tv_mfma_f32_16x16x32_bf16 a[4:7], v[140:143], a[152:155], a[4:7]\n\tv_mfma_f32_16x16x32_bf16 a[12:15], v[140:143], a[156:159], a[12:15]\n\tbuffer_load_dwordx4 a[60:63], v8, s[8:11], s68 offen offset:2048 sc1\n\t" \
    "s_cmp_ge_u32 s43, 9\n\ts_cselect_b32 s68, s70, s43\n\ts_mul_i32 s68, s68, 36864\n\tv_mfma_f32_16x16x32_bf16 a[20:23], v[140:143], a[160:163], a[20:23]\n\ts_add_u32 s68, s68, s44\n\ts_add_u32 s68, s68, 258048\n\tv_mfma_f32_16x16x32_bf16 v[48:51], v[144:147], a[152:155], v[48:51]\n\tv_mfma_f32_16x16x32_bf16 v[56:59], v[144:147], a[156:159], v[56:59]\n\t" \
    "v_mfma_f32_16x16x32_bf16 v[64:67], v[144:147], a[160:163], v[64:67]\n\tv_mfma_f32_16x16x32_bf16 v[52:55], v[148:151], a[152:155], v[52:55]\n\tv_mfma_f32_16x16x32_bf16 v[60:63], v[148:151], a[156:159], v[60:63]\n\tbuffer_load_dwordx4 v[128:131], v8, s[8:11], s68 offen offset:0 sc1\n\tv_mfma_f32_16x16x32_bf16 v[68:71], v[148:151], a[160:163], v[68:71]\n\tv_mfma_f32_16x16x32_bf16 a[24:27], v[152:155], a[152:155], a[24:27]\n\tv_mfma_f32_16x16x32_bf16 a[32:35], v[152:155], a[156:159], a[32:35]\n\tbuffer_load_dwordx4 v[132:135], v8, s[8:11], s68 offen offset:1024 sc1\n\t" \
    "v_mfma_f32_16x16x32_bf16 a[40:43], v[152:155], a[160:163], a[40:43]\n\tv_mfma_f32_16x16x32_bf16 a[28:31], v[156:159], a[152:155], a[28:31]\n\tv_mfma_f32_16x16x32_bf16 a[36:39], v[156:159], a[156:159], a[36:39]\n\tbuffer_load_dwordx4 v[136:139], v8, s[8:11], s68 offen offset:2048 sc1\n\ts_cmp_ge_u32 s43, 8\n\ts_cselect_b32 s68, s70, s43\n\ts_mul_i32 s68, s68, 36864\n\tv_mfma_f32_16x16x32_bf16 a[44:47], v[156:159], a[160:163], a[44:47]\n\t" \
    "s_add_u32 s68, s68, s44\n\ts_add_u32 s68, s68, 294912\n\tv_mfma_f32_16x16x32_bf16 v[72:75], v[160:163], a[152:155], v[72:75]\n\tv_mfma_f32_16x16x32_bf16 v[80:83], v[160:163], a[156:159], v[80:83]\n\tv_mfma_f32_16x16x32_bf16 v[88:91], v[160:163], a[160:163], v[88:91]\n\tv_mfma_f32_16x16x32_bf16 v[76:79], v[164:167], a[152:155], v[76:79]\n\tv_mfma_f32_16x16x32_bf16 v[84:87], v[164:167], a[156:159], v[84:87]\n\tbuffer_load_dwordx4 v[140:143], v8, s[8:11], s68 offen offset:0 sc1\n\t" \
    "v_mfma_f32_16x16x32_bf16 v[92:95], v[164:167], a[160:163], v[92:95]\n\ts_barrier\n\ts_waitcnt vmcnt(16)\n\tv_mfma_f32_16x16x32_bf16 a[0:3], v[168:171], a[164:167], a[0:3]\n\tv_mfma_f32_16x16x32_bf16 a[8:11], v[168:171], a[168:171], a[8:11]\n\tbuffer_load_dwordx4 v[144:147], v8, s[8:11], s68 offen offset:1024 sc1\n\tv_mfma_f32_16x16x32_bf16 a[16:19], v[168:171], a[172:175], a[16:19]\n\tv_mfma_f32_16x16x32_bf16 a[4:7], v[172:175], a[164:167], a[4:7]\n\t" \
    "v_mfma_f32_16x16x32_bf16 a[12:15], v[172:175], a[168:171], a[12:15]\n\tbuffer_load_dwordx4 v[148:151], v8, s[8:11], s68 offen offset:2048 sc1\n\ts_cmp_ge_u32 s43, 7\n\ts_cselect_b32 s68, s70, s43\n\ts_mul_i32 s68, s68, 36864\n\tv_mfma_f32_16x16x32_bf16 a[20:23], v[172:175], a[172:175], a[20:23]\n\ts_add_u32 s68, s68, s44\n\ts_add_u32 s68, s68, 331776\n\t" \
    "v_mfma_f32_16x16x32_bf16 v[48:51], v[176:179], a[164:167], v[48:51]\n\tv_mfma_f32_16x16x32_bf16 v[56:59], v[176:179], a[168:171], v[56:59]\n\tbuffer_load_dwordx4 v[152:155], v8, s[8:11], s68 offen offset:0 sc1\n\tv_mfma_f32_16x16x32_bf16 v[64:67], v[176:179], a[172:175], v[64:67]\n\tv_mfma_f32_16x16x32_bf16 v[52:55], v[180:183], a[164:167], v[52:55]\n\tv_mfma_f32_16x16x32_bf16 v[60:63], v[180:183], a[168:171], v[60:63]\n\tv_mfma_f32_16x16x32_bf16 v[68:71], v[180:183], a[172:175], v[68:71]\n\tv_mfma_f32_16x16x32_bf16 a[24:27], v[184:187], a[164:167], a[24:27]\n\t" \
    "v_mfma_f32_16x16x32_bf16 a[32:35], v[184:187], a[168:171], a[32:35]\n\tbuffer_load_dwordx4 v[156:159], v8, s[8:11], s68 offen offset:1024 sc1\n\tv_mfma_f32_16x16x32_bf16 a[40:43], v[184:187], a[172:175], a[40:43]\n\tv_mfma_f32_16x16x32_bf16 a[28:31], v[188:191], a[164:167], a[28:31]\n\tv_mfma_f32_16x16x32_bf16 a[36:39], v[188:191], a[168:171], a[36:39]\n\tbuffer_load_dwordx4 v[160:163], v8, s[8:11], s68 offen offset:2048 sc1\n\ts_cmp_ge_u32 s43, 6\n\ts_cselect_b32 s68, s70, s43\n\t" \
    "s_mul_i32 s68, s68, 36864\n\tv_mfma_f32_16x16x32_bf16 a[44:47], v[188:191], a[172:175], a[44:47]\n\ts_add_u32 s68, s68, s44\n\ts_add_u32 s68, s68, 368640\n\tv_mfma_f32_16x16x32_bf16 v[72:75], v[192:195], a[164:167], v[72:75]\n\tv_mfma_f32_16x16x32_bf16 v[80:83], v[192:195], a[168:171], v[80:83]\n\tbuffer_load_dwordx4 v[164:167], v8, s[8:11], s68 offen offset:0 sc1\n\tv_mfma_f32_16x16x32_bf16 v[88:91], v[192:195], a[172:175], v[88:91]\n\t" \
    "v_mfma_f32_16x16x32_bf16 v[76:79], v[196:199], a[164:167], v[76:79]\n\tv_mfma_f32_16x16x32_bf16 v[84:87], v[196:199], a[168:171], v[84:87]\n\tv_mfma_f32_16x16x32_bf16 v[92:95], v[196:199], a[172:175], v[92:95]\n\ts_barrier\n\ts_waitcnt vmcnt(19)\n\tv_mfma_f32_16x16x32_bf16 a[0:3], v[200:203], a[176:179], a[0:3]\n\tv_mfma_f32_16x16x32_bf16 a[8:11], v[200:203], a[180:183], a[8:11]\n\tbuffer_load_dwordx4 v[168:171], v8, s[8:11], s68 offen offset:1024 sc1\n\t" \
    "v_mfma_f32_16x16x32_bf16 a[16:19], v[200:203], a[184:187], a[16:19]\n\tv_mfma_f32_16x16x32_bf16 a[4:7], v[204:207], a[176:179], a[4:7]\n\tv_mfma_f32_16x16x32_bf16 a[12:15], v[204:207], a[180:183], a[12:15]\n\tbuffer_load_dwordx4 v[172:175], v8, s[8:11], s68 offen offset:2048 sc1\n\ts_cmp_ge_u32 s43, 5\n\ts_cselect_b32 s68, s70, s43\n\ts_mul_i32 s68, s68, 36864\n\tv_mfma_f32_16x16x32_bf16 a[20:23], v[204:207], a[184:187], a[20:23]\n\t" \
    "s_add_u32 s68, s68, s44\n\ts_add_u32 s68, s68, 405504\n\tv_mfma_f32_16x16x32_bf16 v[48:51], v[208:211], a[176:179], v[48:51]\n\tv_mfma_f32_16x16x32_bf16 v[56:59], v[208:211], a[180:183], v[56:59]\n\tbuffer_load_dwordx4 v[176:179], v8, s[8:11], s68 offen offset:0 sc1\n\tv_mfma_f32_16x16x32_bf16 v[64:67], v[208:211], a[184:187], v[64:67]\n\tv_mfma_f32_16x16x32_bf16 v[52:55], v[212:215], a[176:179], v[52:55]\n\tv_mfma_f32_16x16x32_bf16 v[60:63], v[212:215], a[180:183], v[60:63]\n\t" \
    "buffer_load_dwordx4 v[180:183], v8, s[8:11], s68 offen offset:1024 sc1\n\tv_mfma_f32_16x16x32_bf16 v[68:71], v[212:215], a[184:187], v[68:71]\n\tv_mfma_f32_16x16x32_bf16 a[24:27], v[216:219], a[176:179], a[24:27]\n\tv_mfma_f32_16x16x32_bf16 a[32:35], v[216:219], a[180:183], a[32:35]\n\tv_mfma_f32_16x16x32_bf16 a[40:43], v[216:219], a[184:187], a[40:43]\n\tv_mfma_f32_16x16x32_bf16 a[28:31], a[64:67], a[176:179], a[28:31]\n\tv_mfma_f32_16x16x32_bf16 a[36:39], a[64:67], a[180:183], a[36:39]\n\tbuffer_load_dwordx4 v[184:187], v8, s[8:11], s68 offen offset:2048 sc1\n\t" \
    "s_cmp_ge_u32 s43, 4\n\ts_cselect_b32 s68, s70, s43\n\ts_mul_i32 s68, s68, 36864\n\tv_mfma_f32_16x16x32_bf16 a[44:47], a[64:67], a[184:187], a[44:47]\n\ts_add_u32 s68, s68, s44\n\ts_add_u32 s68, s68, 442368\n\tv_mfma_f32_16x16x32_bf16 v[72:75], a[68:71], a[176:179], v[72:75]\n\tv_mfma_f32_16x16x32_bf16 v[80:83], a[68:71], a[180:183], v[80:83]\n\t" \
    "buffer_load_dwordx4 v[188:191], v8, s[8:11], s68 offen offset:0 sc1\n\tv_mfma_f32_16x16x32_bf16 v[88:91], a[68:71], a[184:187], v[88:91]\n\tv_mfma_f32_16x16x32_bf16 v[76:79], a[72:75], a[176:179], v[76:79]\n\tv_mfma_f32_16x16x32_bf16 v[84:87], a[72:75], a[180:183], v[84:87]\n\tbuffer_load_dwordx4 v[192:195], v8, s[8:11], s68 offen offset:1024 sc1\n\tv_mfma_f32_16x16x32_bf16 v[92:95], a[72:75], a[184:187], v[92:95]\n\ts_barrier\n\ts_waitcnt vmcnt(23)\n\t" \
    "v_mfma_f32_16x16x32_bf16 a[0:3], a[76:79], a[188:191], a[0:3]\n\tv_mfma_f32_16x16x32_bf16 a[8:11], a[76:79], a[192:195], a[8:11]\n\tbuffer_load_dwordx4 v[196:199], v8, s[8:11], s68 offen offset:2048 sc1\n\ts_cmp_ge_u32 s43, 3\n\ts_cselect_b32 s68, s70, s43\n\ts_mul_i32 s68, s68, 36864\n\tv_mfma_f32_16x16x32_bf16 a[16:19], a[76:79], a[196:199], a[16:19]\n\ts_add_u32 s68, s68, s44\n\t" \
    "s_add_u32 s68, s68, 479232\n\tv_mfma_f32_16x16x32_bf16 a[4:7], a[80:83], a[188:191], a[4:7]\n\tv_mfma_f32_16x16x32_bf16 a[12:15], a[80:83], a[192:195], a[12:15]\n\tbuffer_load_dwordx4 v[200:203], v8, s[8:11], s68 offen offset:0 sc1\n\tv_mfma_f32_16x16x32_bf16 a[20:23], a[80:83], a[196:199], a[20:23]\n\tv_mfma_f32_16x16x32_bf16 v[48:51], a[84:87], a[188:191], v[48:51]\n\tv_mfma_f32_16x16x32_bf16 v[56:59], a[84:87], a[192:195], v[56:59]\n\tv_mfma_f32_16x16x32_bf16 v[64:67], a[84:87], a[196:199], v[64:67]\n\t" \
    "v_mfma_f32_16x16x32_bf16 v[52:55], a[88:91], a[188:191], v[52:55]\n\tv_mfma_f32_16x16x32_bf16 v[60:63], a[88:91], a[192:195], v[60:63]\n\tbuffer_load_dwordx4 v[204:207], v8, s[8:11], s68 offen offset:1024 sc1\n\tv_mfma_f32_16x16x32_bf16 v[68:71], a[88:91], a[196:199], v[68:71]\n\tv_mfma_f32_16x16x32_bf16 a[24:27], a[92:95], a[188:191], a[24:27]\n\tv_mfma_f32_16x16x32_bf16 a[32:35], a[92:95], a[192:195], a[32:35]\n\tbuffer_load_dwordx4 v[208:211], v8, s[8:11], s68 offen offset:2048 sc1\n\ts_cmp_ge_u32 s43, 2\n\t" \
    "s_cselect_b32 s68, s70, s43\n\ts_mul_i32 s68, s68, 36864\n\tv_mfma_f32_16x16x32_bf16 a[40:43], a[92:95], a[196:199], a[40:43]\n\ts_add_u32 s68, s68, s44\n\ts_add_u32 s68, s68, 516096\n\tv_mfma_f32_16x16x32_bf16 a[28:31], a[96:99], a[188:191], a[28:31]\n\tv_mfma_f32_16x16x32_bf16 a[36:39], a[96:99], a[192:195], a[36:39]\n\tbuffer_load_dwordx4 v[212:215], v8, s[8:11], s68 offen offset:0 sc1\n\t" \
    "v_mfma_f32_16x16x32_bf16 a[44:47], a[96:99], a[196:199], a[44:47]\n\tv_mfma_f32_16x16x32_bf16 v[72:75], a[100:103], a[188:191], v[72:75]\n\tv_mfma_f32_16x16x32_bf16 v[80:83], a[100:103], a[192:195], v[80:83]\n\tv_mfma_f32_16x16x32_bf16 v[88:91], a[100:103], a[196:199], v[88:91]\n\tv_mfma_f32_16x16x32_bf16 v[76:79], a[104:107], a[188:191], v[76:79]\n\tv_mfma_f32_16x16x32_bf16 v[84:87], a[104:107], a[192:195], v[84:87]\n\tbuffer_load_dwordx4 v[216:219], v8, s[8:11], s68 offen offset:1024 sc1\n\tv_mfma_f32_16x16x32_bf16 v[92:95], a[104:107], a[196:199], v[92:95]\n\t" \
    "s_barrier\n\ts_waitcnt vmcnt(26)\n\tv_mfma_f32_16x16x32_bf16 a[0:3], a[108:111], a[48:51], a[0:3]\n\tv_mfma_f32_16x16x32_bf16 a[8:11], a[108:111], a[52:55], a[8:11]\n\tbuffer_load_dwordx4 a[64:67], v8, s[8:11], s68 offen offset:2048 sc1\n\ts_cmp_ge_u32 s43, 1\n\ts_cselect_b32 s68, s70, s43\n\ts_mul_i32 s68, s68, 36864\n\t" \
    "v_mfma_f32_16x16x32_bf16 a[16:19], a[108:111], a[56:59], a[16:19]\n\ts_add_u32 s68, s68, s44\n\ts_add_u32 s68, s68, 552960\n\tv_mfma_f32_16x16x32_bf16 a[4:7], a[112:115], a[48:51], a[4:7]\n\tv_mfma_f32_16x16x32_bf16 a[12:15], a[112:115], a[52:55], a[12:15]\n\tbuffer_load_dwordx4 a[68:71], v8, s[8:11], s68 offen offset:0 sc1\n\tv_mfma_f32_16x16x32_bf16 a[20:23], a[112:115], a[56:59], a[20:23]\n\tv_mfma_f32_16x16x32_bf16 v[48:51], a[116:119], a[48:51], v[48:51]\n\t" \
    "v_mfma_f32_16x16x32_bf16 v[56:59], a[116:119], a[52:55], v[56:59]\n\tbuffer_load_dwordx4 a[72:75], v8, s[8:11], s68 offen offset:1024 sc1\n\tv_mfma_f32_16x16x32_bf16 v[64:67], a[116:119], a[56:59], v[64:67]\n\tv_mfma_f32_16x16x32_bf16 v[52:55], a[120:123], a[48:51], v[52:55]\n\tds_read_b64_tr_b16 v[16:17], v0 offset:49152\n\tds_read_b64_tr_b16 v[18:19], v0 offset:49664\n\tv_mfma_f32_16x16x32_bf16 v[60:63], a[120:123], a[52:55], v[60:63]\n\tv_mfma_f32_16x16x32_bf16 v[68:71], a[120:123], a[56:59], v[68:71]\n\t" \
    "v_mfma_f32_16x16x32_bf16 a[24:27], a[124:127], a[48:51], a[24:27]\n\tds_read_b64_tr_b16 v[20:21], v1 offset:49152\n\tds_read_b64_tr_b16 v[22:23], v1 offset:49664\n\tv_mfma_f32_16x16x32_bf16 a[32:35], a[124:127], a[52:55], a[32:35]\n\tbuffer_load_dwordx4 a[76:79], v8, s[8:11], s68 offen offset:2048 sc1\n\ts_add_u32 s68, s44, 589824\n\tv_mfma_f32_16x16x32_bf16 a[40:43], a[124:127], a[56:59], a[40:43]\n\tv_mfma_f32_16x16x32_bf16 a[28:31], a[128:131], a[48:51], a[28:31]\n\t" \
    "ds_read_b64_tr_b16 v[24:25], v2 offset:49152\n\tds_read_b64_tr_b16 v[26:27], v2 offset:49664\n\tv_mfma_f32_16x16x32_bf16 a[36:39], a[128:131], a[52:55], a[36:39]\n\tbuffer_load_dwordx4 a[80:83], v8, s[8:11], s68 offen offset:0 sc1\n\tv_mfma_f32_16x16x32_bf16 a[44:47], a[128:131], a[56:59], a[44:47]\n\tv_mfma_f32_16x16x32_bf16 v[72:75], a[132:135], a[48:51], v[72:75]\n\tds_read_b64_tr_b16 v[28:29], v3 offset:49152\n\tds_read_b64_tr_b16 v[30:31], v3 offset:49664\n\t" \
    "v_mfma_f32_16x16x32_bf16 v[80:83], a[132:135], a[52:55], v[80:83]\n\tbuffer_load_dwordx4 a[84:87], v8, s[8:11], s68 offen offset:1024 sc1\n\tv_mfma_f32_16x16x32_bf16 v[88:91], a[132:135], a[56:59], v[88:91]\n\tv_mfma_f32_16x16x32_bf16 v[76:79], a[136:139], a[48:51], v[76:79]\n\tds_read_b64_tr_b16 v[32:33], v0 offset:53248\n\tds_read_b64_tr_b16 v[34:35], v0 offset:53760\n\tv_mfma_f32_16x16x32_bf16 v[84:87], a[136:139], a[52:55], v[84:87]\n\tv_mfma_f32_16x16x32_bf16 v[92:95], a[136:139], a[56:59], v[92:95]\n\t" \
    "s_barrier\n\ts_waitcnt vmcnt(29) lgkmcnt(8)\n\tv_mfma_f32_16x16x32_bf16 a[0:3], v[16:19], v[120:123], a[0:3]\n\tds_read_b64_tr_b16 v[36:37], v1 offset:53248\n\tds_read_b64_tr_b16 v[38:39], v1 offset:53760\n\tv_mfma_f32_16x16x32_bf16 a[8:11], v[16:19], v[124:127], a[8:11]\n\tbuffer_load_dwordx4 a[88:91], v8, s[8:11], s68 offen offset:2048 sc1\n\ts_add_u32 s68, s44, 626688\n\t" \
    "v_mfma_f32_16x16x32_bf16 a[16:19], v[16:19], a[60:63], a[16:19]\n\ts_waitcnt lgkmcnt(8)\n\tv_mfma_f32_16x16x32_bf16 a[4:7], v[20:23], v[120:123], a[4:7]\n\tds_read_b64_tr_b16 v[40:41], v2 offset:53248\n\tds_read_b64_tr_b16 v[42:43], v2 offset:53760\n\tv_mfma_f32_16x16x32_bf16 a[12:15], v[20:23], v[124:127], a[12:15]\n\tbuffer_load_dwordx4 a[48:51], v8, s[8:11], s68 offen offset:0 sc1\n\tv_mfma_f32_16x16x32_bf16 a[20:23], v[20:23], a[60:63], a[20:23]\n\t" \
    "s_waitcnt lgkmcnt(8)\n\tv_mfma_f32_16x16x32_bf16 v[48:51], v[24:27], v[120:123], v[48:51]\n\tds_read_b64_tr_b16 v[44:45], v3 offset:53248\n\tds_read_b64_tr_b16 v[46:47], v3 offset:53760\n\tv_mfma_f32_16x16x32_bf16 v[56:59], v[24:27], v[124:127], v[56:59]\n\tbuffer_load_dwordx4 a[52:55], v8, s[8:11], s68 offen offset:1024 sc1\n\tv_mfma_f32_16x16x32_bf16 v[64:67], v[24:27], a[60:63], v[64:67]\n\ts_waitcnt lgkmcnt(8)\n\t" \
    "v_mfma_f32_16x16x32_bf16 v[52:55], v[28:31], v[120:123], v[52:55]\n\tds_read_b64_tr_b16 v[16:17], v0 offset:57344\n\tds_read_b64_tr_b16 v[18:19], v0 offset:57856\n\tv_mfma_f32_16x16x32_bf16 v[60:63], v[28:31], v[124:127], v[60:63]\n\tbuffer_load_dwordx4 a[56:59], v8, s[8:11], s68 offen offset:2048 sc1\n\ts_add_u32 s68, s44, 663552\n\tv_mfma_f32_16x16x32_bf16 v[68:71], v[28:31], a[60:63], v[68:71]\n\ts_waitcnt lgkmcnt(8)\n\t" \
    "v_mfma_f32_16x16x32_bf16 a[24:27], v[32:35], v[120:123], a[24:27]\n\tds_read_b64_tr_b16 v[20:21], v1 offset:57344\n\tds_read_b64_tr_b16 v[22:23], v1 offset:57856\n\tv_mfma_f32_16x16x32_bf16 a[32:35], v[32:35], v[124:127], a[32:35]\n\tv_mfma_f32_16x16x32_bf16 a[40:43], v[32:35], a[60:63], a[40:43]\n\ts_waitcnt lgkmcnt(8)\n\tv_mfma_f32_16x16x32_bf16 a[28:31], v[36:39], v[120:123], a[28:31]\n\tds_read_b64_tr_b16 v[24:25], v2 offset:57344\n\t" \
    "ds_read_b64_tr_b16 v[26:27], v2 offset:57856\n\tv_mfma_f32_16x16x32_bf16 a[36:39], v[36:39], v[124:127], a[36:39]\n\tbuffer_load_dwordx4 a[92:95], v8, s[8:11], s68 offen offset:0 sc1\n\tv_mfma_f32_16x16x32_bf16 a[44:47], v[36:39], a[60:63], a[44:47]\n\ts_waitcnt lgkmcnt(8)\n\tv_mfma_f32_16x16x32_bf16 v[72:75], v[40:43], v[120:123], v[72:75]\n\tds_read_b64_tr_b16 v[28:29], v3 offset:57344\n\tds_read_b64_tr_b16 v[30:31], v3 offset:57856\n\t" \
    "v_mfma_f32_16x16x32_bf16 v[80:83], v[40:43], v[124:127], v[80:83]\n\tbuffer_load_dwordx4 a[96:99], v8, s[8:11], s68 offen offset:1024 sc1\n\tv_mfma_f32_16x16x32_bf16 v[88:91], v[40:43], a[60:63], v[88:91]\n\ts_waitcnt lgkmcnt(8)\n\tv_mfma_f32_16x16x32_bf16 v[76:79], v[44:47], v[120:123], v[76:79]\n\tds_read_b64_tr_b16 v[32:33], v0 offset:61440\n\tds_read_b64_tr_b16 v[34:35], v0 offset:61952\n\tv_mfma_f32_16x16x32_bf16 v[84:87], v[44:47], v[124:127], v[84:87]\n\t" \
    "buffer_load_dwordx4 a[100:103], v8, s[8:11], s68 offen offset:2048 sc1\n\ts_add_u32 s68, s44, 700416\n\tv_mfma_f32_16x16x32_bf16 v[92:95], v[44:47], a[60:63], v[92:95]\n\ts_barrier\n\ts_waitcnt vmcnt(33) lgkmcnt(8)\n\tv_mfma_f32_16x16x32_bf16 a[0:3], v[16:19], v[128:131], a[0:3]\n\tds_read_b64_tr_b16 v[36:37], v1 offset:61440\n\tds_read_b64_tr_b16 v[38:39], v1 offset:61952\n\t" \
    "v_mfma_f32_16x16x32_bf16 a[8:11], v[16:19], v[132:135], a[8:11]\n\tbuffer_load_dwordx4 a[104:107], v8, s[8:11], s68 offen offset:0 sc1\n\tv_mfma_f32_16x16x32_bf16 a[16:19], v[16:19], v[136:139], a[16:19]\n\ts_waitcnt lgkmcnt(8)\n\tv_mfma_f32_16x16x32_bf16 a[4:7], v[20:23], v[128:131], a[4:7]\n\tds_read_b64_tr_b16 v[40:41], v2 offset:61440\n\tds_read_b64_tr_b16 v[42:43], v2 offset:61952\n\tv_mfma_f32_16x16x32_bf16 a[12:15], v[20:23], v[132:135], a[12:15]\n\t" \
    "buffer_load_dwordx4 a[108:111], v8, s[8:11], s68 offen offset:1024 sc1\n\tv_mfma_f32_16x16x32_bf16 a[20:23], v[20:23], v[136:139], a[20:23]\n\ts_waitcnt lgkmcnt(8)\n\tv_mfma_f32_16x16x32_bf16 v[48:51], v[24:27], v[128:131], v[48:51]\n\tds_read_b64_tr_b16 v[44:45], v3 offset:61440\n\tds_read_b64_tr_b16 v[46:47], v3 offset:61952\n\tv_mfma_f32_16x16x32_bf16 v[56:59], v[24:27], v[132:135], v[56:59]\n\tv_mfma_f32_16x16x32_bf16 v[64:67], v[24:27], v[136:139], v[64:67]\n\t" \
    "s_waitcnt lgkmcnt(8)\n\tv_mfma_f32_16x16x32_bf16 v[52:55], v[28:31], v[128:131], v[52:55]\n\tds_read_b64_tr_b16 v[16:17], v0 offset:0\n\tds_read_b64_tr_b16 v[18:19], v0 offset:512\n\tv_mfma_f32_16x16x32_bf16 v[60:63], v[28:31], v[132:135], v[60:63]\n\tbuffer_load_dwordx4 a[112:115], v8, s[8:11], s68 offen offset:2048 sc1\n\ts_cmp_ge_u32 s43, 2\n\ts_cselect_b32 s69, s70, s43\n\t" \
    "s_mul_i32 s69, s69, 294912\n\tv_mfma_f32_16x16x32_bf16 v[68:71], v[28:31], v[136:139], v[68:71]\n\ts_add_u32 s69, s69, 4128768\n\ts_add_u32 m0, s72, 49152\n\ts_waitcnt lgkmcnt(8)\n\tv_mfma_f32_16x16x32_bf16 a[24:27], v[32:35], v[128:131], a[24:27]\n\tds_read_b64_tr_b16 v[20:21], v1 offset:0\n\tds_read_b64_tr_b16 v[22:23], v1 offset:512\n\t" \
    "v_mfma_f32_16x16x32_bf16 a[32:35], v[32:35], v[132:135], a[32:35]\n\tbuffer_load_dwordx4 v10, s[12:15], s69 offen lds\n\ts_add_u32 m0, s72, 50176\n\tv_mfma_f32_16x16x32_bf16 a[40:43], v[32:35], v[136:139], a[40:43]\n\ts_waitcnt lgkmcnt(8)\n\tv_mfma_f32_16x16x32_bf16 a[28:31], v[36:39], v[128:131], a[28:31]\n\tds_read_b64_tr_b16 v[24:25], v2 offset:0\n\tds_read_b64_tr_b16 v[26:27], v2 offset:512\n\t" \
    "v_mfma_f32_16x16x32_bf16 a[36:39], v[36:39], v[132:135], a[36:39]\n\tv_mfma_f32_16x16x32_bf16 a[44:47], v[36:39], v[136:139], a[44:47]\n\ts_waitcnt lgkmcnt(8)\n\tv_mfma_f32_16x16x32_bf16 v[72:75], v[40:43], v[128:131], v[72:75]\n\tds_read_b64_tr_b16 v[28:29], v3 offset:0\n\tds_read_b64_tr_b16 v[30:31], v3 offset:512\n\tv_mfma_f32_16x16x32_bf16 v[80:83], v[40:43], v[132:135], v[80:83]\n\tbuffer_load_dwordx4 v11, s[12:15], s69 offen lds\n\t" \
    "s_cmp_ge_u32 s43, 2\n\ts_cselect_b32 s69, s70, s43\n\ts_mul_i32 s69, s69, 294912\n\tv_mfma_f32_16x16x32_bf16 v[88:91], v[40:43], v[136:139], v[88:91]\n\ts_add_u32 s69, s69, 4128896\n\ts_add_u32 m0, s72, 53248\n\ts_waitcnt lgkmcnt(8)\n\tv_mfma_f32_16x16x32_bf16 v[76:79], v[44:47], v[128:131], v[76:79]\n\t" \
    "ds_read_b64_tr_b16 v[32:33], v0 offset:4096\n\tds_read_b64_tr_b16 v[34:35], v0 offset:4608\n\tv_mfma_f32_16x16x32_bf16 v[84:87], v[44:47], v[132:135], v[84:87]\n\tbuffer_load_dwordx4 v10, s[12:15], s69 offen lds\n\ts_add_u32 m0, s72, 54272\n\tv_mfma_f32_16x16x32_bf16 v[92:95], v[44:47], v[136:139], v[92:95]\n\ts_barrier\n\ts_waitcnt vmcnt(36) lgkmcnt(8)\n\t" \
    "v_mfma_f32_16x16x32_bf16 a[0:3], v[16:19], v[140:143], a[0:3]\n\tds_read_b64_tr_b16 v[36:37], v1 offset:4096\n\tds_read_b64_tr_b16 v[38:39], v1 offset:4608\n\tv_mfma_f32_16x16x32_bf16 a[8:11], v[16:19], v[144:147], a[8:11]\n\tbuffer_load_dwordx4 v11, s[12:15], s69 offen lds\n\ts_cmp_ge_u32 s43, 1\n\ts_cselect_b32 s69, s70, s43\n\ts_mul_i32 s69, s69, 294912\n\t" \
    "v_mfma_f32_16x16x32_bf16 a[16:19], v[16:19], v[148:151], a[16:19]\n\ts_add_u32 s69, s69, 4423680\n\ts_add_u32 m0, s72, 57344\n\ts_waitcnt lgkmcnt(8)\n\tv_mfma_f32_16x16x32_bf16 a[4:7], v[20:23], v[140:143], a[4:7]\n\tds_read_b64_tr_b16 v[40:41], v2 offset:4096\n\tds_read_b64_tr_b16 v[42:43], v2 offset:4608\n\tv_mfma_f32_16x16x32_bf16 a[12:15], v[20:23], v[144:147], a[12:15]\n\t" \
    "buffer_load_dwordx4 v10, s[12:15], s69 offen lds\n\ts_add_u32 m0, s72, 58368\n\tv_mfma_f32_16x16x32_bf16 a[20:23], v[20:23], v[148:151], a[20:23]\n\ts_waitcnt lgkmcnt(8)\n\tv_mfma_f32_16x16x32_bf16 v[48:51], v[24:27], v[140:143], v[48:51]\n\tds_read_b64_tr_b16 v[44:45], v3 offset:4096\n\tds_read_b64_tr_b16 v[46:47], v3 offset:4608\n\tv_mfma_f32_16x16x32_bf16 v[56:59], v[24:27], v[144:147], v[56:59]\n\t" \
    "buffer_load_dwordx4 v11, s[12:15], s69 offen lds\n\ts_cmp_ge_u32 s43, 1\n\ts_cselect_b32 s69, s70, s43\n\ts_mul_i32 s69, s69, 294912\n\tv_mfma_f32_16x16x32_bf16 v[64:67], v[24:27], v[148:151], v[64:67]\n\ts_add_u32 s69, s69, 4423808\n\ts_add_u32 m0, s72, 61440\n\ts_waitcnt lgkmcnt(8)\n\t" \
    "v_mfma_f32_16x16x32_bf16 v[52:55], v[28:31], v[140:143], v[52:55]\n\tds_read_b64_tr_b16 v[16:17], v0 offset:8192\n\tds_read_b64_tr_b16 v[18:19], v0 offset:8704\n\tv_mfma_f32_16x16x32_bf16 v[60:63], v[28:31], v[144:147], v[60:63]\n\tv_mfma_f32_16x16x32_bf16 v[68:71], v[28:31], v[148:151], v[68:71]\n\ts_waitcnt lgkmcnt(8)\n\tv_mfma_f32_16x16x32_bf16 a[24:27], v[32:35], v[140:143], a[24:27]\n\tds_read_b64_tr_b16 v[20:21], v1 offset:8192\n\t" \
    "ds_read_b64_tr_b16 v[22:23], v1 offset:8704\n\tv_mfma_f32_16x16x32_bf16 a[32:35], v[32:35], v[144:147], a[32:35]\n\tbuffer_load_dwordx4 v10, s[12:15], s69 offen lds\n\ts_add_u32 m0, s72, 62464\n\tv_mfma_f32_16x16x32_bf16 a[40:43], v[32:35], v[148:151], a[40:43]\n\ts_waitcnt lgkmcnt(8)\n\tv_mfma_f32_16x16x32_bf16 a[28:31], v[36:39], v[140:143], a[28:31]\n\tds_read_b64_tr_b16 v[24:25], v2 offset:8192\n\t" \
    "ds_read_b64_tr_b16 v[26:27], v2 offset:8704\n\tv_mfma_f32_16x16x32_bf16 a[36:39], v[36:39], v[144:147], a[36:39]\n\tbuffer_load_dwordx4 v11, s[12:15], s69 offen lds\n\ts_add_u32 s68, s44, 737280\n\tv_mfma_f32_16x16x32_bf16 a[44:47], v[36:39], v[148:151], a[44:47]\n\ts_waitcnt lgkmcnt(8)\n\tv_mfma_f32_16x16x32_bf16 v[72:75], v[40:43], v[140:143], v[72:75]\n\tds_read_b64_tr_b16 v[28:29], v3 offset:8192\n\t" \
    "ds_read_b64_tr_b16 v[30:31], v3 offset:8704\n\tv_mfma_f32_16x16x32_bf16 v[80:83], v[40:43], v[144:147], v[80:83]\n\tbuffer_load_dwordx4 v[120:123], v8, s[8:11], s68 offen offset:0 sc1\n\tv_mfma_f32_16x16x32_bf16 v[88:91], v[40:43], v[148:151], v[88:91]\n\ts_waitcnt lgkmcnt(8)\n\tv_mfma_f32_16x16x32_bf16 v[76:79], v[44:47], v[140:143], v[76:79]\n\tds_read_b64_tr_b16 v[32:33], v0 offset:12288\n\tds_read_b64_tr_b16 v[34:35], v0 offset:12800\n\t" \
    "v_mfma_f32_16x16x32_bf16 v[84:87], v[44:47], v[144:147], v[84:87]\n\tv_mfma_f32_16x16x32_bf16 v[92:95], v[44:47], v[148:151], v[92:95]\n\ts_barrier\n\ts_waitcnt vmcnt(39) lgkmcnt(8)\n\tv_mfma_f32_16x16x32_bf16 a[0:3], v[16:19], v[152:155], a[0:3]\n\tds_read_b64_tr_b16 v[36:37], v1 offset:12288\n\tds_read_b64_tr_b16 v[38:39], v1 offset:12800\n\tv_mfma_f32_16x16x32_bf16 a[8:11], v[16:19], v[156:159], a[8:11]\n\t" \
    "buffer_load_dwordx4 v[124:127], v8, s[8:11], s68 offen offset:1024 sc1\n\tv_mfma_f32_16x16x32_bf16 a[16:19], v[16:19], v[160:163], a[16:19]\n\ts_waitcnt lgkmcnt(8)\n\tv_mfma_f32_16x16x32_bf16 a[4:7], v[20:23], v[152:155], a[4:7]\n\tds_read_b64_tr_b16 v[40:41], v2 offset:12288\n\tds_read_b64_tr_b16 v[42:43], v2 offset:12800\n\tv_mfma_f32_16x16x32_bf16 a[12:15], v[20:23], v[156:159], a[12:15]\n\tbuffer_load_dwordx4 v[128:131], v8, s[8:11], s68 offen offset:2048 sc1\n\t" \
    "s_mov_b32 s69, 4718592\n\ts_add_u32 m0, s72, 0\n\tv_mfma_f32_16x16x32_bf16 a[20:23], v[20:23], v[160:163], a[20:23]\n\ts_waitcnt lgkmcnt(8)\n\tv_mfma_f32_16x16x32_bf16 v[48:51], v[24:27], v[152:155], v[48:51]\n\tds_read_b64_tr_b16 v[44:45], v3 offset:12288\n\tds_read_b64_tr_b16 v[46:47], v3 offset:12800\n\tv_mfma_f32_16x16x32_bf16 v[56:59], v[24:27], v[156:159], v[56:59]\n\t" \
    "buffer_load_dwordx4 v10, s[12:15], s69 offen lds\n\ts_add_u32 m0, s72, 1024\n\tv_mfma_f32_16x16x32_bf16 v[64:67], v[24:27], v[160:163], v[64:67]\n\ts_waitcnt lgkmcnt(8)\n\tv_mfma_f32_16x16x32_bf16 v[52:55], v[28:31], v[152:155], v[52:55]\n\tds_read_b64_tr_b16 v[16:17], v0 offset:16384\n\tds_read_b64_tr_b16 v[18:19], v0 offset:16896\n\tv_mfma_f32_16x16x32_bf16 v[60:63], v[28:31], v[156:159], v[60:63]\n\t" \
    "buffer_load_dwordx4 v11, s[12:15], s69 offen lds\n\ts_mov_b32 s69, 5013504\n\ts_add_u32 m0, s72, 4096\n\tv_mfma_f32_16x16x32_bf16 v[68:71], v[28:31], v[160:163], v[68:71]\n\ts_waitcnt lgkmcnt(8)\n\tv_mfma_f32_16x16x32_bf16 a[24:27], v[32:35], v[152:155], a[24:27]\n\tds_read_b64_tr_b16 v[20:21], v1 offset:16384\n\tds_read_b64_tr_b16 v[22:23], v1 offset:16896\n\t" \
    "v_mfma_f32_16x16x32_bf16 a[32:35], v[32:35], v[156:159], a[32:35]\n\tv_mfma_f32_16x16x32_bf16 a[40:43], v[32:35], v[160:163], a[40:43]\n\ts_waitcnt lgkmcnt(8)\n\tv_mfma_f32_16x16x32_bf16 a[28:31], v[36:39], v[152:155], a[28:31]\n\tds_read_b64_tr_b16 v[24:25], v2 offset:16384\n\tds_read_b64_tr_b16 v[26:27], v2 offset:16896\n\tv_mfma_f32_16x16x32_bf16 a[36:39], v[36:39], v[156:159], a[36:39]\n\tbuffer_load_dwordx4 v10, s[12:15], s69 offen lds\n\t" \
    "s_add_u32 m0, s72, 5120\n\tv_mfma_f32_16x16x32_bf16 a[44:47], v[36:39], v[160:163], a[44:47]\n\ts_waitcnt lgkmcnt(8)\n\tv_mfma_f32_16x16x32_bf16 v[72:75], v[40:43], v[152:155], v[72:75]\n\tds_read_b64_tr_b16 v[28:29], v3 offset:16384\n\tds_read_b64_tr_b16 v[30:31], v3 offset:16896\n\tv_mfma_f32_16x16x32_bf16 v[80:83], v[40:43], v[156:159], v[80:83]\n\tbuffer_load_dwordx4 v11, s[12:15], s69 offen lds\n\t" \
    "s_add_u32 s68, s44, 774144\n\tv_mfma_f32_16x16x32_bf16 v[88:91], v[40:43], v[160:163], v[88:91]\n\ts_waitcnt lgkmcnt(8)\n\tv_mfma_f32_16x16x32_bf16 v[76:79], v[44:47], v[152:155], v[76:79]\n\tds_read_b64_tr_b16 v[32:33], v0 offset:20480\n\tds_read_b64_tr_b16 v[34:35], v0 offset:20992\n\tv_mfma_f32_16x16x32_bf16 v[84:87], v[44:47], v[156:159], v[84:87]\n\tv_mfma_f32_16x16x32_bf16 v[92:95], v[44:47], v[160:163], v[92:95]\n\t" \
    "s_barrier\n\ts_waitcnt vmcnt(42) lgkmcnt(8)\n\tv_mfma_f32_16x16x32_bf16 a[0:3], v[16:19], v[164:167], a[0:3]\n\tds_read_b64_tr_b16 v[36:37], v1 offset:20480\n\tds_read_b64_tr_b16 v[38:39], v1 offset:20992\n\tv_mfma_f32_16x16x32_bf16 a[8:11], v[16:19], v[168:171], a[8:11]\n\tbuffer_load_dwordx4 v[132:135], v8, s[8:11], s68 offen offset:0 sc1\n\tv_mfma_f32_16x16x32_bf16 a[16:19], v[16:19], v[172:175], a[16:19]\n\t" \
    "s_waitcnt lgkmcnt(8)\n\tv_mfma_f32_16x16x32_bf16 a[4:7], v[20:23], v[164:167], a[4:7]\n\tds_read_b64_tr_b16 v[40:41], v2 offset:20480\n\tds_read_b64_tr_b16 v[42:43], v2 offset:20992\n\tv_mfma_f32_16x16x32_bf16 a[12:15], v[20:23], v[168:171], a[12:15]\n\tbuffer_load_dwordx4 v[136:139], v8, s[8:11], s68 offen offset:1024 sc1\n\tv_mfma_f32_16x16x32_bf16 a[20:23], v[20:23], v[172:175], a[20:23]\n\ts_waitcnt lgkmcnt(8)\n\t" \
    "v_mfma_f32_16x16x32_bf16 v[48:51], v[24:27], v[164:167], v[48:51]\n\tds_read_b64_tr_b16 v[44:45], v3 offset:20480\n\tds_read_b64_tr_b16 v[46:47], v3 offset:20992\n\tv_mfma_f32_16x16x32_bf16 v[56:59], v[24:27], v[168:171], v[56:59]\n\tbuffer_load_dwordx4 v[140:143], v8, s[8:11], s68 offen offset:2048 sc1\n\ts_mov_b32 s69, 5308416\n\ts_add_u32 m0, s72, 8192\n\tv_mfma_f32_16x16x32_bf16 v[64:67], v[24:27], v[172:175], v[64:67]\n\t" \
    "s_waitcnt lgkmcnt(8)\n\tv_mfma_f32_16x16x32_bf16 v[52:55], v[28:31], v[164:167], v[52:55]\n\tds_read_b64_tr_b16 v[16:17], v0 offset:24576\n\tds_read_b64_tr_b16 v[18:19], v0 offset:25088\n\tv_mfma_f32_16x16x32_bf16 v[60:63], v[28:31], v[168:171], v[60:63]\n\tbuffer_load_dwordx4 v10, s[12:15], s69 offen lds\n\ts_add_u32 m0, s72, 9216\n\tv_mfma_f32_16x16x32_bf16 v[68:71], v[28:31], v[172:175], v[68:71]\n\t" \
    "s_waitcnt lgkmcnt(8)\n\tv_mfma_f32_16x16x32_bf16 a[24:27], v[32:35], v[164:167], a[24:27]\n\tds_read_b64_tr_b16 v[20:21], v1 offset:24576\n\tds_read_b64_tr_b16 v[22:23], v1 offset:25088\n\tv_mfma_f32_16x16x32_bf16 a[32:35], v[32:35], v[168:171], a[32:35]\n\tbuffer_load_dwordx4 v11, s[12:15], s69 offen lds\n\ts_mov_b32 s69, 5603328\n\ts_add_u32 m0, s72, 12288\n\t" \
    "v_mfma_f32_16x16x32_bf16 a[40:43], v[32:35], v[172:175], a[40:43]\n\ts_waitcnt lgkmcnt(8)\n\tv_mfma_f32_16x16x32_bf16 a[28:31], v[36:39], v[164:167], a[28:31]\n\tds_read_b64_tr_b16 v[24:25], v2 offset:24576\n\tds_read_b64_tr_b16 v[26:27], v2 offset:25088\n\tv_mfma_f32_16x16x32_bf16 a[36:39], v[36:39], v[168:171], a[36:39]\n\tv_mfma_f32_16x16x32_bf16 a[44:47], v[36:39], v[172:175], a[44:47]\n\ts_waitcnt lgkmcnt(8)\n\t" \
    "v_mfma_f32_16x16x32_bf16 v[72:75], v[40:43], v[164:167], v[72:75]\n\tds_read_b64_tr_b16 v[28:29], v3 offset:24576\n\tds_read_b64_tr_b16 v[30:31], v3 offset:25088\n\tv_mfma_f32_16x16x32_bf16 v[80:83], v[40:43], v[168:171], v[80:83]\n\tbuffer_load_dwordx4 v10, s[12:15], s69 offen lds\n\ts_add_u32 m0, s72, 13312\n\tv_mfma_f32_16x16x32_bf16 v[88:91], v[40:43], v[172:175], v[88:91]\n\ts_waitcnt lgkmcnt(8)\n\t" \
    "v_mfma_f32_16x16x32_bf16 v[76:79], v[44:47], v[164:167], v[76:79]\n\tds_read_b64_tr_b16 v[32:33], v0 offset:28672\n\tds_read_b64_tr_b16 v[34:35], v0 offset:29184\n\tv_mfma_f32_16x16x32_bf16 v[84:87], v[44:47], v[168:171], v[84:87]\n\tbuffer_load_dwordx4 v11, s[12:15], s69 offen lds\n\ts_add_u32 s68, s44, 811008\n\tv_mfma_f32_16x16x32_bf16 v[92:95], v[44:47], v[172:175], v[92:95]\n\ts_barrier\n\t" \
    "s_waitcnt vmcnt(46) lgkmcnt(8)\n\tv_mfma_f32_16x16x32_bf16 a[0:3], v[16:19], v[176:179], a[0:3]\n\tds_read_b64_tr_b16 v[36:37], v1 offset:28672\n\tds_read_b64_tr_b16 v[38:39], v1 offset:29184\n\tv_mfma_f32_16x16x32_bf16 a[8:11], v[16:19], v[180:183], a[8:11]\n\tbuffer_load_dwordx4 v[144:147], v8, s[8:11], s68 offen offset:0 sc1\n\tv_mfma_f32_16x16x32_bf16 a[16:19], v[16:19], v[184:187], a[16:19]\n\ts_waitcnt lgkmcnt(8)\n\t" \
    "v_mfma_f32_16x16x32_bf16 a[4:7], v[20:23], v[176:179], a[4:7]\n\tds_read_b64_tr_b16 v[40:41], v2 offset:28672\n\tds_read_b64_tr_b16 v[42:43], v2 offset:29184\n\tv_mfma_f32_16x16x32_bf16 a[12:15], v[20:23], v[180:183], a[12:15]\n\tbuffer_load_dwordx4 v[148:151], v8, s[8:11], s68 offen offset:1024 sc1\n\tv_mfma_f32_16x16x32_bf16 a[20:23], v[20:23], v[184:187], a[20:23]\n\ts_waitcnt lgkmcnt(8)\n\tv_mfma_f32_16x16x32_bf16 v[48:51], v[24:27], v[176:179], v[48:51]\n\t" \
    "ds_read_b64_tr_b16 v[44:45], v3 offset:28672\n\tds_read_b64_tr_b16 v[46:47], v3 offset:29184\n\tv_mfma_f32_16x16x32_bf16 v[56:59], v[24:27], v[180:183], v[56:59]\n\tv_mfma_f32_16x16x32_bf16 v[64:67], v[24:27], v[184:187], v[64:67]\n\ts_waitcnt lgkmcnt(8)\n\tv_mfma_f32_16x16x32_bf16 v[52:55], v[28:31], v[176:179], v[52:55]\n\tds_read_b64_tr_b16 v[16:17], v0 offset:32768\n\tds_read_b64_tr_b16 v[18:19], v0 offset:33280\n\t" \
    "v_mfma_f32_16x16x32_bf16 v[60:63], v[28:31], v[180:183], v[60:63]\n\tbuffer_load_dwordx4 v[152:155], v8, s[8:11], s68 offen offset:2048 sc1\n\ts_mov_b32 s69, 5898240\n\ts_add_u32 m0, s72, 16384\n\tv_mfma_f32_16x16x32_bf16 v[68:71], v[28:31], v[184:187], v[68:71]\n\ts_waitcnt lgkmcnt(8)\n\tv_mfma_f32_16x16x32_bf16 a[24:27], v[32:35], v[176:179], a[24:27]\n\tds_read_b64_tr_b16 v[20:21], v1 offset:32768\n\t" \
    "ds_read_b64_tr_b16 v[22:23], v1 offset:33280\n\tv_mfma_f32_16x16x32_bf16 a[32:35], v[32:35], v[180:183], a[32:35]\n\tbuffer_load_dwordx4 v10, s[12:15], s69 offen lds\n\ts_add_u32 m0, s72, 17408\n\tv_mfma_f32_16x16x32_bf16 a[40:43], v[32:35], v[184:187], a[40:43]\n\ts_waitcnt lgkmcnt(8)\n\tv_mfma_f32_16x16x32_bf16 a[28:31], v[36:39], v[176:179], a[28:31]\n\tds_read_b64_tr_b16 v[24:25], v2 offset:32768\n\t" \
    "ds_read_b64_tr_b16 v[26:27], v2 offset:33280\n\tv_mfma_f32_16x16x32_bf16 a[36:39], v[36:39], v[180:183], a[36:39]\n\tbuffer_load_dwordx4 v11, s[12:15], s69 offen lds\n\ts_mov_b32 s69, 6193152\n\ts_add_u32 m0, s72, 20480\n\tv_mfma_f32_16x16x32_bf16 a[44:47], v[36:39], v[184:187], a[44:47]\n\ts_waitcnt lgkmcnt(8)\n\tv_mfma_f32_16x16x32_bf16 v[72:75], v[40:43], v[176:179], v[72:75]\n\t" \
    "ds_read_b64_tr_b16 v[28:29], v3 offset:32768\n\tds_read_b64_tr_b16 v[30:31], v3 offset:33280\n\tv_mfma_f32_16x16x32_bf16 v[80:83], v[40:43], v[180:183], v[80:83]\n\tv_mfma_f32_16x16x32_bf16 v[88:91], v[40:43], v[184:187], v[88:91]\n\ts_waitcnt lgkmcnt(8)\n\tv_mfma_f32_16x16x32_bf16 v[76:79], v[44:47], v[176:179], v[76:79]\n\tds_read_b64_tr_b16 v[32:33], v0 offset:36864\n\tds_read_b64_tr_b16 v[34:35], v0 offset:37376\n\t" \
    "v_mfma_f32_16x16x32_bf16 v[84:87], v[44:47], v[180:183], v[84:87]\n\tbuffer_load_dwordx4 v10, s[12:15], s69 offen lds\n\ts_add_u32 m0, s72, 21504\n\tv_mfma_f32_16x16x32_bf16 v[92:95], v[44:47], v[184:187], v[92:95]\n\ts_barrier\n\ts_waitcnt vmcnt(49) lgkmcnt(8)\n\tv_mfma_f32_16x16x32_bf16 a[0:3], v[16:19], v[188:191], a[0:3]\n\tds_read_b64_tr_b16 v[36:37], v1 offset:36864\n\t" \
    "ds_read_b64_tr_b16 v[38:39], v1 offset:37376\n\tv_mfma_f32_16x16x32_bf16 a[8:11], v[16:19], v[192:195], a[8:11]\n\tbuffer_load_dwordx4 v11, s[12:15], s69 offen lds\n\ts_mov_b32 s69, 6488064\n\ts_add_u32 m0, s72, 24576\n\tv_mfma_f32_16x16x32_bf16 a[16:19], v[16:19], v[196:199], a[16:19]\n\ts_waitcnt lgkmcnt(8)\n\tv_mfma_f32_16x16x32_bf16 a[4:7], v[20:23], v[188:191], a[4:7]\n\t" \
    "ds_read_b64_tr_b16 v[40:41], v2 offset:36864\n\tds_read_b64_tr_b16 v[42:43], v2 offset:37376\n\tv_mfma_f32_16x16x32_bf16 a[12:15], v[20:23], v[192:195], a[12:15]\n\tbuffer_load_dwordx4 v10, s[12:15], s69 offen lds\n\ts_add_u32 m0, s72, 25600\n\tv_mfma_f32_16x16x32_bf16 a[20:23], v[20:23], v[196:199], a[20:23]\n\ts_waitcnt lgkmcnt(8)\n\tv_mfma_f32_16x16x32_bf16 v[48:51], v[24:27], v[188:191], v[48:51]\n\t" \
    "ds_read_b64_tr_b16 v[44:45], v3 offset:36864\n\tds_read_b64_tr_b16 v[46:47], v3 offset:37376\n\tv_mfma_f32_16x16x32_bf16 v[56:59], v[24:27], v[192:195], v[56:59]\n\tbuffer_load_dwordx4 v11, s[12:15], s69 offen lds\n\ts_mov_b32 s69, 6782976\n\ts_add_u32 m0, s72, 28672\n\tv_mfma_f32_16x16x32_bf16 v[64:67], v[24:27], v[196:199], v[64:67]\n\ts_waitcnt lgkmcnt(8)\n\t" \
    "v_mfma_f32_16x16x32_bf16 v[52:55], v[28:31], v[188:191], v[52:55]\n\tds_read_b64_tr_b16 v[16:17], v0 offset:40960\n\tds_read_b64_tr_b16 v[18:19], v0 offset:41472\n\tv_mfma_f32_16x16x32_bf16 v[60:63], v[28:31], v[192:195], v[60:63]\n\tv_mfma_f32_16x16x32_bf16 v[68:71], v[28:31], v[196:199], v[68:71]\n\ts_waitcnt lgkmcnt(8)\n\tv_mfma_f32_16x16x32_bf16 a[24:27], v[32:35], v[188:191], a[24:27]\n\tds_read_b64_tr_b16 v[20:21], v1 offset:40960\n\t" \
    "ds_read_b64_tr_b16 v[22:23], v1 offset:41472\n\tv_mfma_f32_16x16x32_bf16 a[32:35], v[32:35], v[192:195], a[32:35]\n\tbuffer_load_dwordx4 v10, s[12:15], s69 offen lds\n\ts_add_u32 m0, s72, 29696\n\tv_mfma_f32_16x16x32_bf16 a[40:43], v[32:35], v[196:199], a[40:43]\n\ts_waitcnt lgkmcnt(8)\n\tv_mfma_f32_16x16x32_bf16 a[28:31], v[36:39], v[188:191], a[28:31]\n\tds_read_b64_tr_b16 v[24:25], v2 offset:40960\n\t" \
    "ds_read_b64_tr_b16 v[26:27], v2 offset:41472\n\tv_mfma_f32_16x16x32_bf16 a[36:39], v[36:39], v[192:195], a[36:39]\n\tbuffer_load_dwordx4 v11, s[12:15], s69 offen lds\n\ts_add_u32 s68, s44, 847872\n\tv_mfma_f32_16x16x32_bf16 a[44:47], v[36:39], v[196:199], a[44:47]\n\ts_waitcnt lgkmcnt(8)\n\tv_mfma_f32_16x16x32_bf16 v[72:75], v[40:43], v[188:191], v[72:75]\n\tds_read_b64_tr_b16 v[28:29], v3 offset:40960\n\t" \
    "ds_read_b64_tr_b16 v[30:31], v3 offset:41472\n\tv_mfma_f32_16x16x32_bf16 v[80:83], v[40:43], v[192:195], v[80:83]\n\tv_mfma_f32_16x16x32_bf16 v[88:91], v[40:43], v[196:199], v[88:91]\n\ts_waitcnt lgkmcnt(8)\n\tv_mfma_f32_16x16x32_bf16 v[76:79], v[44:47], v[188:191], v[76:79]\n\tds_read_b64_tr_b16 v[32:33], v0 offset:45056\n\tds_read_b64_tr_b16 v[34:35], v0 offset:45568\n\tv_mfma_f32_16x16x32_bf16 v[84:87], v[44:47], v[192:195], v[84:87]\n\t" \
    "buffer_load_dwordx4 v[156:159], v8, s[8:11], s68 offen offset:0 sc1\n\tv_mfma_f32_16x16x32_bf16 v[92:95], v[44:47], v[196:199], v[92:95]\n\ts_waitcnt vmcnt(30)\n\ts_barrier\n\ts_waitcnt lgkmcnt(8)\n\tv_mfma_f32_16x16x32_bf16 a[0:3], v[16:19], v[200:203], a[0:3]\n\tds_read_b64_tr_b16 v[36:37], v1 offset:45056\n\tds_read_b64_tr_b16 v[38:39], v1 offset:45568\n\t" \
    "v_mfma_f32_16x16x32_bf16 a[8:11], v[16:19], v[204:207], a[8:11]\n\tbuffer_load_dwordx4 v[160:163], v8, s[8:11], s68 offen offset:1024 sc1\n\tv_mfma_f32_16x16x32_bf16 a[16:19], v[16:19], v[208:211], a[16:19]\n\ts_waitcnt lgkmcnt(8)\n\tv_mfma_f32_16x16x32_bf16 a[4:7], v[20:23], v[200:203], a[4:7]\n\tds_read_b64_tr_b16 v[40:41], v2 offset:45056\n\tds_read_b64_tr_b16 v[42:43], v2 offset:45568\n\tv_mfma_f32_16x16x32_bf16 a[12:15], v[20:23], v[204:207], a[12:15]\n\t" \
    "buffer_load_dwordx4 v[164:167], v8, s[8:11], s68 offen offset:2048 sc1\n\ts_mov_b32 s69, 7077888\n\ts_add_u32 m0, s72, 32768\n\tv_mfma_f32_16x16x32_bf16 a[20:23], v[20:23], v[208:211], a[20:23]\n\ts_waitcnt lgkmcnt(8)\n\tv_mfma_f32_16x16x32_bf16 v[48:51], v[24:27], v[200:203], v[48:51]\n\tds_read_b64_tr_b16 v[44:45], v3 offset:45056\n\tds_read_b64_tr_b16 v[46:47], v3 offset:45568\n\t" \
    "v_mfma_f32_16x16x32_bf16 v[56:59], v[24:27], v[204:207], v[56:59]\n\tbuffer_load_dwordx4 v10, s[12:15], s69 offen lds\n\ts_add_u32 m0, s72, 33792\n\tv_mfma_f32_16x16x32_bf16 v[64:67], v[24:27], v[208:211], v[64:67]\n\ts_waitcnt lgkmcnt(8)\n\tv_mfma_f32_16x16x32_bf16 v[52:55], v[28:31], v[200:203], v[52:55]\n\tds_read_b64_tr_b16 v[16:17], v0 offset:49152\n\tds_read_b64_tr_b16 v[18:19], v0 offset:49664\n\t" \
    "v_mfma_f32_16x16x32_bf16 v[60:63], v[28:31], v[204:207], v[60:63]\n\tbuffer_load_dwordx4 v11, s[12:15], s69 offen lds\n\ts_mov_b32 s69, 7372800\n\ts_add_u32 m0, s72, 36864\n\tv_mfma_f32_16x16x32_bf16 v[68:71], v[28:31], v[208:211], v[68:71]\n\ts_waitcnt lgkmcnt(8)\n\tv_mfma_f32_16x16x32_bf16 a[24:27], v[32:35], v[200:203], a[24:27]\n\tds_read_b64_tr_b16 v[20:21], v1 offset:49152\n\t" \
    "ds_read_b64_tr_b16 v[22:23], v1 offset:49664\n\tv_mfma_f32_16x16x32_bf16 a[32:35], v[32:35], v[204:207], a[32:35]\n\tv_mfma_f32_16x16x32_bf16 a[40:43], v[32:35], v[208:211], a[40:43]\n\ts_waitcnt lgkmcnt(8)\n\tv_mfma_f32_16x16x32_bf16 a[28:31], v[36:39], v[200:203], a[28:31]\n\tds_read_b64_tr_b16 v[24:25], v2 offset:49152\n\tds_read_b64_tr_b16 v[26:27], v2 offset:49664\n\tv_mfma_f32_16x16x32_bf16 a[36:39], v[36:39], v[204:207], a[36:39]\n\t" \
    "buffer_load_dwordx4 v10, s[12:15], s69 offen lds\n\ts_add_u32 m0, s72, 37888\n\tv_mfma_f32_16x16x32_bf16 a[44:47], v[36:39], v[208:211], a[44:47]\n\ts_waitcnt lgkmcnt(8)\n\tv_mfma_f32_16x16x32_bf16 v[72:75], v[40:43], v[200:203], v[72:75]\n\tds_read_b64_tr_b16 v[28:29], v3 offset:49152\n\tds_read_b64_tr_b16 v[30:31], v3 offset:49664\n\tv_mfma_f32_16x16x32_bf16 v[80:83], v[40:43], v[204:207], v[80:83]\n\t" \
    "buffer_load_dwordx4 v11, s[12:15], s69 offen lds\n\ts_add_u32 s68, s44, 884736\n\tv_mfma_f32_16x16x32_bf16 v[88:91], v[40:43], v[208:211], v[88:91]\n\ts_waitcnt lgkmcnt(8)\n\tv_mfma_f32_16x16x32_bf16 v[76:79], v[44:47], v[200:203], v[76:79]\n\tds_read_b64_tr_b16 v[32:33], v0 offset:53248\n\tds_read_b64_tr_b16 v[34:35], v0 offset:53760\n\tv_mfma_f32_16x16x32_bf16 v[84:87], v[44:47], v[204:207], v[84:87]\n\t" \
    "v_mfma_f32_16x16x32_bf16 v[92:95], v[44:47], v[208:211], v[92:95]\n\ts_waitcnt vmcnt(32)\n\ts_barrier\n\ts_waitcnt lgkmcnt(8)\n\tv_mfma_f32_16x16x32_bf16 a[0:3], v[16:19], v[212:215], a[0:3]\n\tds_read_b64_tr_b16 v[36:37], v1 offset:53248\n\tds_read_b64_tr_b16 v[38:39], v1 offset:53760\n\tv_mfma_f32_16x16x32_bf16 a[8:11], v[16:19], v[216:219], a[8:11]\n\t" \
    "buffer_load_dwordx4 v[168:171], v8, s[8:11], s68 offen offset:0 sc1\n\tv_mfma_f32_16x16x32_bf16 a[16:19], v[16:19], a[64:67], a[16:19]\n\ts_waitcnt lgkmcnt(8)\n\tv_mfma_f32_16x16x32_bf16 a[4:7], v[20:23], v[212:215], a[4:7]\n\tds_read_b64_tr_b16 v[40:41], v2 offset:53248\n\tds_read_b64_tr_b16 v[42:43], v2 offset:53760\n\tv_mfma_f32_16x16x32_bf16 a[12:15], v[20:23], v[216:219], a[12:15]\n\tbuffer_load_dwordx4 v[172:175], v8, s[8:11], s68 offen offset:1024 sc1\n\t" \
    "v_mfma_f32_16x16x32_bf16 a[20:23], v[20:23], a[64:67], a[20:23]\n\ts_waitcnt lgkmcnt(8)\n\tv_mfma_f32_16x16x32_bf16 v[48:51], v[24:27], v[212:215], v[48:51]\n\tds_read_b64_tr_b16 v[44:45], v3 offset:53248\n\tds_read_b64_tr_b16 v[46:47], v3 offset:53760\n\tv_mfma_f32_16x16x32_bf16 v[56:59], v[24:27], v[216:219], v[56:59]\n\tbuffer_load_dwordx4 v[176:179], v8, s[8:11], s68 offen offset:2048 sc1\n\ts_mov_b32 s69, 7667712\n\t" \
    "s_add_u32 m0, s72, 40960\n\tv_mfma_f32_16x16x32_bf16 v[64:67], v[24:27], a[64:67], v[64:67]\n\ts_waitcnt lgkmcnt(8)\n\tv_mfma_f32_16x16x32_bf16 v[52:55], v[28:31], v[212:215], v[52:55]\n\tds_read_b64_tr_b16 v[16:17], v0 offset:57344\n\tds_read_b64_tr_b16 v[18:19], v0 offset:57856\n\tv_mfma_f32_16x16x32_bf16 v[60:63], v[28:31], v[216:219], v[60:63]\n\tbuffer_load_dwordx4 v10, s[12:15], s69 offen lds\n\t" \
    "s_add_u32 m0, s72, 41984\n\tv_mfma_f32_16x16x32_bf16 v[68:71], v[28:31], a[64:67], v[68:71]\n\ts_waitcnt lgkmcnt(8)\n\tv_mfma_f32_16x16x32_bf16 a[24:27], v[32:35], v[212:215], a[24:27]\n\tds_read_b64_tr_b16 v[20:21], v1 offset:57344\n\tds_read_b64_tr_b16 v[22:23], v1 offset:57856\n\tv_mfma_f32_16x16x32_bf16 a[32:35], v[32:35], v[216:219], a[32:35]\n\tbuffer_load_dwordx4 v11, s[12:15], s69 offen lds\n\t" \
    "s_mov_b32 s69, 7962624\n\ts_add_u32 m0, s72, 45056\n\tv_mfma_f32_16x16x32_bf16 a[40:43], v[32:35], a[64:67], a[40:43]\n\ts_waitcnt lgkmcnt(8)\n\tv_mfma_f32_16x16x32_bf16 a[28:31], v[36:39], v[212:215], a[28:31]\n\tds_read_b64_tr_b16 v[24:25], v2 offset:57344\n\tds_read_b64_tr_b16 v[26:27], v2 offset:57856\n\tv_mfma_f32_16x16x32_bf16 a[36:39], v[36:39], v[216:219], a[36:39]\n\t" \
    "v_mfma_f32_16x16x32_bf16 a[44:47], v[36:39], a[64:67], a[44:47]\n\ts_waitcnt lgkmcnt(8)\n\tv_mfma_f32_16x16x32_bf16 v[72:75], v[40:43], v[212:215], v[72:75]\n\tds_read_b64_tr_b16 v[28:29], v3 offset:57344\n\tds_read_b64_tr_b16 v[30:31], v3 offset:57856\n\tv_mfma_f32_16x16x32_bf16 v[80:83], v[40:43], v[216:219], v[80:83]\n\tbuffer_load_dwordx4 v10, s[12:15], s69 offen lds\n\ts_add_u32 m0, s72, 46080\n\t" \
    "v_mfma_f32_16x16x32_bf16 v[88:91], v[40:43], a[64:67], v[88:91]\n\ts_waitcnt lgkmcnt(8)\n\tv_mfma_f32_16x16x32_bf16 v[76:79], v[44:47], v[212:215], v[76:79]\n\tds_read_b64_tr_b16 v[32:33], v0 offset:61440\n\tds_read_b64_tr_b16 v[34:35], v0 offset:61952\n\tv_mfma_f32_16x16x32_bf16 v[84:87], v[44:47], v[216:219], v[84:87]\n\tbuffer_load_dwordx4 v11, s[12:15], s69 offen lds\n\ts_add_u32 s68, s44, 921600\n\t" \
    "v_mfma_f32_16x16x32_bf16 v[92:95], v[44:47], a[64:67], v[92:95]\n\ts_waitcnt vmcnt(32)\n\ts_barrier\n\ts_waitcnt lgkmcnt(8)\n\tv_mfma_f32_16x16x32_bf16 a[0:3], v[16:19], a[68:71], a[0:3]\n\tds_read_b64_tr_b16 v[36:37], v1 offset:61440\n\tds_read_b64_tr_b16 v[38:39], v1 offset:61952\n\tv_mfma_f32_16x16x32_bf16 a[8:11], v[16:19], a[72:75], a[8:11]\n\t" \
    "buffer_load_dwordx4 v[180:183], v8, s[8:11], s68 offen offset:0 sc1\n\tv_mfma_f32_16x16x32_bf16 a[16:19], v[16:19], a[76:79], a[16:19]\n\ts_waitcnt lgkmcnt(8)\n\tv_mfma_f32_16x16x32_bf16 a[4:7], v[20:23], a[68:71], a[4:7]\n\tds_read_b64_tr_b16 v[40:41], v2 offset:61440\n\tds_read_b64_tr_b16 v[42:43], v2 offset:61952\n\tv_mfma_f32_16x16x32_bf16 a[12:15], v[20:23], a[72:75], a[12:15]\n\tbuffer_load_dwordx4 v[184:187], v8, s[8:11], s68 offen offset:1024 sc1\n\t" \
    "v_mfma_f32_16x16x32_bf16 a[20:23], v[20:23], a[76:79], a[20:23]\n\ts_waitcnt lgkmcnt(8)\n\tv_mfma_f32_16x16x32_bf16 v[48:51], v[24:27], a[68:71], v[48:51]\n\tds_read_b64_tr_b16 v[44:45], v3 offset:61440\n\tds_read_b64_tr_b16 v[46:47], v3 offset:61952\n\tv_mfma_f32_16x16x32_bf16 v[56:59], v[24:27], a[72:75], v[56:59]\n\tv_mfma_f32_16x16x32_bf16 v[64:67], v[24:27], a[76:79], v[64:67]\n\ts_waitcnt lgkmcnt(8)\n\t" \
    "v_mfma_f32_16x16x32_bf16 v[52:55], v[28:31], a[68:71], v[52:55]\n\tds_read_b64_tr_b16 v[16:17], v0 offset:0\n\tds_read_b64_tr_b16 v[18:19], v0 offset:512\n\tv_mfma_f32_16x16x32_bf16 v[60:63], v[28:31], a[72:75], v[60:63]\n\tbuffer_load_dwordx4 v[188:191], v8, s[8:11], s68 offen offset:2048 sc1\n\ts_mov_b32 s69, 8257536\n\ts_add_u32 m0, s72, 49152\n\tv_mfma_f32_16x16x32_bf16 v[68:71], v[28:31], a[76:79], v[68:71]\n\t" \
    "s_waitcnt lgkmcnt(8)\n\tv_mfma_f32_16x16x32_bf16 a[24:27], v[32:35], a[68:71], a[24:27]\n\tds_read_b64_tr_b16 v[20:21], v1 offset:0\n\tds_read_b64_tr_b16 v[22:23], v1 offset:512\n\tv_mfma_f32_16x16x32_bf16 a[32:35], v[32:35], a[72:75], a[32:35]\n\tbuffer_load_dwordx4 v10, s[12:15], s69 offen lds\n\ts_add_u32 m0, s72, 50176\n\tv_mfma_f32_16x16x32_bf16 a[40:43], v[32:35], a[76:79], a[40:43]\n\t" \
    "s_waitcnt lgkmcnt(8)\n\tv_mfma_f32_16x16x32_bf16 a[28:31], v[36:39], a[68:71], a[28:31]\n\tds_read_b64_tr_b16 v[24:25], v2 offset:0\n\tds_read_b64_tr_b16 v[26:27], v2 offset:512\n\tv_mfma_f32_16x16x32_bf16 a[36:39], v[36:39], a[72:75], a[36:39]\n\tv_mfma_f32_16x16x32_bf16 a[44:47], v[36:39], a[76:79], a[44:47]\n\ts_waitcnt lgkmcnt(8)\n\tv_mfma_f32_16x16x32_bf16 v[72:75], v[40:43], a[68:71], v[72:75]\n\t" \
    "ds_read_b64_tr_b16 v[28:29], v3 offset:0\n\tds_read_b64_tr_b16 v[30:31], v3 offset:512\n\tv_mfma_f32_16x16x32_bf16 v[80:83], v[40:43], a[72:75], v[80:83]\n\tbuffer_load_dwordx4 v11, s[12:15], s69 offen lds\n\ts_mov_b32 s69, 8552448\n\ts_add_u32 m0, s72, 53248\n\tv_mfma_f32_16x16x32_bf16 v[88:91], v[40:43], a[76:79], v[88:91]\n\ts_waitcnt lgkmcnt(8)\n\t" \
    "v_mfma_f32_16x16x32_bf16 v[76:79], v[44:47], a[68:71], v[76:79]\n\tds_read_b64_tr_b16 v[32:33], v0 offset:4096\n\tds_read_b64_tr_b16 v[34:35], v0 offset:4608\n\tv_mfma_f32_16x16x32_bf16 v[84:87], v[44:47], a[72:75], v[84:87]\n\tbuffer_load_dwordx4 v10, s[12:15], s69 offen lds\n\ts_add_u32 m0, s72, 54272\n\tv_mfma_f32_16x16x32_bf16 v[92:95], v[44:47], a[76:79], v[92:95]\n\ts_waitcnt vmcnt(31)\n\t" \
    "s_barrier\n\ts_waitcnt lgkmcnt(8)\n\tv_mfma_f32_16x16x32_bf16 a[0:3], v[16:19], a[80:83], a[0:3]\n\tds_read_b64_tr_b16 v[36:37], v1 offset:4096\n\tds_read_b64_tr_b16 v[38:39], v1 offset:4608\n\tv_mfma_f32_16x16x32_bf16 a[8:11], v[16:19], a[84:87], a[8:11]\n\tbuffer_load_dwordx4 v11, s[12:15], s69 offen lds\n\ts_mov_b32 s69, 8847360\n\t" \
    "s_add_u32 m0, s72, 57344\n\tv_mfma_f32_16x16x32_bf16 a[16:19], v[16:19], a[88:91], a[16:19]\n\ts_waitcnt lgkmcnt(8)\n\tv_mfma_f32_16x16x32_bf16 a[4:7], v[20:23], a[80:83], a[4:7]\n\tds_read_b64_tr_b16 v[40:41], v2 offset:4096\n\tds_read_b64_tr_b16 v[42:43], v2 offset:4608\n\tv_mfma_f32_16x16x32_bf16 a[12:15], v[20:23], a[84:87], a[12:15]\n\tbuffer_load_dwordx4 v10, s[12:15], s69 offen lds\n\t" \
    "s_add_u32 m0, s72, 58368\n\tv_mfma_f32_16x16x32_bf16 a[20:23], v[20:23], a[88:91], a[20:23]\n\ts_waitcnt lgkmcnt(8)\n\tv_mfma_f32_16x16x32_bf16 v[48:51], v[24:27], a[80:83], v[48:51]\n\tds_read_b64_tr_b16 v[44:45], v3 offset:4096\n\tds_read_b64_tr_b16 v[46:47], v3 offset:4608\n\tv_mfma_f32_16x16x32_bf16 v[56:59], v[24:27], a[84:87], v[56:59]\n\tbuffer_load_dwordx4 v11, s[12:15], s69 offen lds\n\t" \
    "s_add_u32 s68, s44, 958464\n\tv_mfma_f32_16x16x32_bf16 v[64:67], v[24:27], a[88:91], v[64:67]\n\ts_waitcnt lgkmcnt(8)\n\tv_mfma_f32_16x16x32_bf16 v[52:55], v[28:31], a[80:83], v[52:55]\n\tds_read_b64_tr_b16 v[16:17], v0 offset:8192\n\tds_read_b64_tr_b16 v[18:19], v0 offset:8704\n\tv_mfma_f32_16x16x32_bf16 v[60:63], v[28:31], a[84:87], v[60:63]\n\tv_mfma_f32_16x16x32_bf16 v[68:71], v[28:31], a[88:91], v[68:71]\n\t" \
    "s_waitcnt lgkmcnt(8)\n\tv_mfma_f32_16x16x32_bf16 a[0:3], v[32:35], a[48:51], a[0:3]\n\tds_read_b64_tr_b16 v[20:21], v1 offset:8192\n\tds_read_b64_tr_b16 v[22:23], v1 offset:8704\n\tv_mfma_f32_16x16x32_bf16 a[8:11], v[32:35], a[52:55], a[8:11]\n\tbuffer_load_dwordx4 v[192:195], v8, s[8:11], s68 offen offset:0 sc1\n\tv_mfma_f32_16x16x32_bf16 a[16:19], v[32:35], a[56:59], a[16:19]\n\ts_waitcnt lgkmcnt(8)\n\t" \
    "v_mfma_f32_16x16x32_bf16 a[4:7], v[36:39], a[48:51], a[4:7]\n\tds_read_b64_tr_b16 v[24:25], v2 offset:8192\n\tds_read_b64_tr_b16 v[26:27], v2 offset:8704\n\tv_mfma_f32_16x16x32_bf16 a[12:15], v[36:39], a[52:55], a[12:15]\n\tbuffer_load_dwordx4 v[196:199], v8, s[8:11], s68 offen offset:1024 sc1\n\tv_mfma_f32_16x16x32_bf16 a[20:23], v[36:39], a[56:59], a[20:23]\n\ts_waitcnt lgkmcnt(8)\n\tv_mfma_f32_16x16x32_bf16 v[48:51], v[40:43], a[48:51], v[48:51]\n\t" \
    "ds_read_b64_tr_b16 v[28:29], v3 offset:8192\n\tds_read_b64_tr_b16 v[30:31], v3 offset:8704\n\tv_mfma_f32_16x16x32_bf16 v[56:59], v[40:43], a[52:55], v[56:59]\n\tv_mfma_f32_16x16x32_bf16 v[64:67], v[40:43], a[56:59], v[64:67]\n\ts_waitcnt lgkmcnt(8)\n\tv_mfma_f32_16x16x32_bf16 v[52:55], v[44:47], a[48:51], v[52:55]\n\tds_read_b64_tr_b16 v[32:33], v0 offset:12288\n\tds_read_b64_tr_b16 v[34:35], v0 offset:12800\n\t" \
    "v_mfma_f32_16x16x32_bf16 v[60:63], v[44:47], a[52:55], v[60:63]\n\tbuffer_load_dwordx4 v[200:203], v8, s[8:11], s68 offen offset:2048 sc1\n\ts_add_u32 s68, s44, 995328\n\tv_mfma_f32_16x16x32_bf16 v[68:71], v[44:47], a[56:59], v[68:71]\n\ts_waitcnt vmcnt(30)\n\ts_barrier\n\ts_waitcnt lgkmcnt(8)\n\tv_mfma_f32_16x16x32_bf16 a[0:3], v[16:19], a[92:95], a[0:3]\n\t" \
    "ds_read_b64_tr_b16 v[36:37], v1 offset:12288\n\tds_read_b64_tr_b16 v[38:39], v1 offset:12800\n\tv_mfma_f32_16x16x32_bf16 a[8:11], v[16:19], a[96:99], a[8:11]\n\tbuffer_load_dwordx4 v[204:207], v8, s[8:11], s68 offen offset:0 sc1\n\tv_mfma_f32_16x16x32_bf16 a[16:19], v[16:19], a[100:103], a[16:19]\n\ts_waitcnt lgkmcnt(8)\n\tv_mfma_f32_16x16x32_bf16 a[4:7], v[20:23], a[92:95], a[4:7]\n\tds_read_b64_tr_b16 v[40:41], v2 offset:12288\n\t" \
    "ds_read_b64_tr_b16 v[42:43], v2 offset:12800\n\tv_mfma_f32_16x16x32_bf16 a[12:15], v[20:23], a[96:99], a[12:15]\n\tbuffer_load_dwordx4 v[208:211], v8, s[8:11], s68 offen offset:1024 sc1\n\tv_mfma_f32_16x16x32_bf16 a[20:23], v[20:23], a[100:103], a[20:23]\n\ts_waitcnt lgkmcnt(8)\n\tv_mfma_f32_16x16x32_bf16 v[48:51], v[24:27], a[92:95], v[48:51]\n\tds_read_b64_tr_b16 v[44:45], v3 offset:12288\n\tds_read_b64_tr_b16 v[46:47], v3 offset:12800\n\t" \
    "v_mfma_f32_16x16x32_bf16 v[56:59], v[24:27], a[96:99], v[56:59]\n\tbuffer_load_dwordx4 v[212:215], v8, s[8:11], s68 offen offset:2048 sc1\n\ts_mov_b32 s69, 9142272\n\ts_add_u32 m0, s72, 61440\n\tv_mfma_f32_16x16x32_bf16 v[64:67], v[24:27], a[100:103], v[64:67]\n\ts_waitcnt lgkmcnt(8)\n\tv_mfma_f32_16x16x32_bf16 v[52:55], v[28:31], a[92:95], v[52:55]\n\tds_read_b64_tr_b16 v[16:17], v0 offset:16384\n\t" \
    "ds_read_b64_tr_b16 v[18:19], v0 offset:16896\n\tv_mfma_f32_16x16x32_bf16 v[60:63], v[28:31], a[96:99], v[60:63]\n\tbuffer_load_dwordx4 v10, s[12:15], s69 offen lds\n\ts_add_u32 m0, s72, 62464\n\tv_mfma_f32_16x16x32_bf16 v[68:71], v[28:31], a[100:103], v[68:71]\n\ts_mul_i32 s43, s6, 0\n\ts_sub_u32 s70, s43, 0\n\ts_waitcnt lgkmcnt(8)\n\t" \
    "v_mfma_f32_16x16x32_bf16 a[0:3], v[32:35], a[104:107], a[0:3]\n\tds_read_b64_tr_b16 v[20:21], v1 offset:16384\n\tds_read_b64_tr_b16 v[22:23], v1 offset:16896\n\tv_mfma_f32_16x16x32_bf16 a[8:11], v[32:35], a[108:111], a[8:11]\n\tv_mfma_f32_16x16x32_bf16 a[16:19], v[32:35], a[112:115], a[16:19]\n\ts_waitcnt lgkmcnt(8)\n\tv_mfma_f32_16x16x32_bf16 a[4:7], v[36:39], a[104:107], a[4:7]\n\tds_read_b64_tr_b16 v[24:25], v2 offset:16384\n\t" \
    "ds_read_b64_tr_b16 v[26:27], v2 offset:16896\n\tv_mfma_f32_16x16x32_bf16 a[12:15], v[36:39], a[108:111], a[12:15]\n\tbuffer_load_dwordx4 v11, s[12:15], s69 offen lds\n\ts_add_u32 s68, s44, 1032192\n\tv_mfma_f32_16x16x32_bf16 a[20:23], v[36:39], a[112:115], a[20:23]\n\ts_waitcnt lgkmcnt(8)\n\tv_mfma_f32_16x16x32_bf16 v[48:51], v[40:43], a[104:107], v[48:51]\n\tds_read_b64_tr_b16 v[28:29], v3 offset:16384\n\t" \
    "ds_read_b64_tr_b16 v[30:31], v3 offset:16896\n\tv_mfma_f32_16x16x32_bf16 v[56:59], v[40:43], a[108:111], v[56:59]\n\tbuffer_load_dwordx4 v[216:219], v8, s[8:11], s68 offen offset:0 sc1\n\tv_mfma_f32_16x16x32_bf16 v[64:67], v[40:43], a[112:115], v[64:67]\n\ts_waitcnt lgkmcnt(8)\n\tv_mfma_f32_16x16x32_bf16 v[52:55], v[44:47], a[104:107], v[52:55]\n\tds_read_b64_tr_b16 v[32:33], v0 offset:20480\n\tds_read_b64_tr_b16 v[34:35], v0 offset:20992\n\t" \
    "v_mfma_f32_16x16x32_bf16 v[60:63], v[44:47], a[108:111], v[60:63]\n\tv_mfma_f32_16x16x32_bf16 v[68:71], v[44:47], a[112:115], v[68:71]\n\ts_waitcnt vmcnt(32)\n\ts_barrier\n\ts_waitcnt lgkmcnt(8)\n\tv_mfma_f32_16x16x32_bf16 a[0:3], v[16:19], v[120:123], a[0:3]\n\tds_read_b64_tr_b16 v[36:37], v1 offset:20480\n\tds_read_b64_tr_b16 v[38:39], v1 offset:20992\n\t" \
    "v_mfma_f32_16x16x32_bf16 a[8:11], v[16:19], v[124:127], a[8:11]\n\tbuffer_load_dwordx4 a[60:63], v8, s[8:11], s68 offen offset:1024 sc1\n\tv_mfma_f32_16x16x32_bf16 a[16:19], v[16:19], v[128:131], a[16:19]\n\ts_waitcnt lgkmcnt(8)\n\tv_mfma_f32_16x16x32_bf16 a[4:7], v[20:23], v[120:123], a[4:7]\n\tds_read_b64_tr_b16 v[40:41], v2 offset:20480\n\tds_read_b64_tr_b16 v[42:43], v2 offset:20992\n\tv_mfma_f32_16x16x32_bf16 a[12:15], v[20:23], v[124:127], a[12:15]\n\t" \
    "buffer_load_dwordx4 a[64:67], v8, s[8:11], s68 offen offset:2048 sc1\n\ts_add_u32 s68, s44, 1069056\n\tv_mfma_f32_16x16x32_bf16 a[20:23], v[20:23], v[128:131], a[20:23]\n\ts_waitcnt lgkmcnt(8)\n\tv_mfma_f32_16x16x32_bf16 v[48:51], v[24:27], v[120:123], v[48:51]\n\tds_read_b64_tr_b16 v[44:45], v3 offset:20480\n\tds_read_b64_tr_b16 v[46:47], v3 offset:20992\n\tv_mfma_f32_16x16x32_bf16 v[56:59], v[24:27], v[124:127], v[56:59]\n\t" \
    "buffer_load_dwordx4 a[68:71], v8, s[8:11], s68 offen offset:0 sc1\n\tv_mfma_f32_16x16x32_bf16 v[64:67], v[24:27], v[128:131], v[64:67]\n\ts_waitcnt lgkmcnt(8)\n\tv_mfma_f32_16x16x32_bf16 v[52:55], v[28:31], v[120:123], v[52:55]\n\tds_read_b64_tr_b16 v[16:17], v0 offset:24576\n\tds_read_b64_tr_b16 v[18:19], v0 offset:25088\n\tv_mfma_f32_16x16x32_bf16 v[60:63], v[28:31], v[124:127], v[60:63]\n\tbuffer_load_dwordx4 a[72:75], v8, s[8:11], s68 offen offset:1024 sc1\n\t" \
    "v_mfma_f32_16x16x32_bf16 v[68:71], v[28:31], v[128:131], v[68:71]\n\ts_waitcnt lgkmcnt(8)\n\tv_mfma_f32_16x16x32_bf16 a[0:3], v[32:35], v[132:135], a[0:3]\n\tds_read_b64_tr_b16 v[20:21], v1 offset:24576\n\tds_read_b64_tr_b16 v[22:23], v1 offset:25088\n\tv_mfma_f32_16x16x32_bf16 a[8:11], v[32:35], v[136:139], a[8:11]\n\tv_mfma_f32_16x16x32_bf16 a[16:19], v[32:35], v[140:143], a[16:19]\n\ts_waitcnt lgkmcnt(8)\n\t" \
    "v_mfma_f32_16x16x32_bf16 a[4:7], v[36:39], v[132:135], a[4:7]\n\tds_read_b64_tr_b16 v[24:25], v2 offset:24576\n\tds_read_b64_tr_b16 v[26:27], v2 offset:25088\n\tv_mfma_f32_16x16x32_bf16 a[12:15], v[36:39], v[136:139], a[12:15]\n\tbuffer_load_dwordx4 a[76:79], v8, s[8:11], s68 offen offset:2048 sc1\n\ts_add_u32 s68, s44, 1105920\n\tv_mfma_f32_16x16x32_bf16 a[20:23], v[36:39], v[140:143], a[20:23]\n\ts_waitcnt lgkmcnt(8)\n\t" \
    "v_mfma_f32_16x16x32_bf16 v[48:51], v[40:43], v[132:135], v[48:51]\n\tds_read_b64_tr_b16 v[28:29], v3 offset:24576\n\tds_read_b64_tr_b16 v[30:31], v3 offset:25088\n\tv_mfma_f32_16x16x32_bf16 v[56:59], v[40:43], v[136:139], v[56:59]\n\tbuffer_load_dwordx4 a[116:119], v8, s[8:11], s68 offen offset:0 sc1\n\tv_mfma_f32_16x16x32_bf16 v[64:67], v[40:43], v[140:143], v[64:67]\n\ts_waitcnt lgkmcnt(8)\n\tv_mfma_f32_16x16x32_bf16 v[52:55], v[44:47], v[132:135], v[52:55]\n\t" \
    "ds_read_b64_tr_b16 v[32:33], v0 offset:28672\n\tds_read_b64_tr_b16 v[34:35], v0 offset:29184\n\tv_mfma_f32_16x16x32_bf16 v[60:63], v[44:47], v[136:139], v[60:63]\n\tbuffer_load_dwordx4 a[120:123], v8, s[8:11], s68 offen offset:1024 sc1\n\tv_mfma_f32_16x16x32_bf16 v[68:71], v[44:47], v[140:143], v[68:71]\n\ts_waitcnt vmcnt(32)\n\ts_barrier\n\ts_waitcnt lgkmcnt(8)\n\t" \
    "v_mfma_f32_16x16x32_bf16 a[0:3], v[16:19], v[144:147], a[0:3]\n\tds_read_b64_tr_b16 v[36:37], v1 offset:28672\n\tds_read_b64_tr_b16 v[38:39], v1 offset:29184\n\tv_mfma_f32_16x16x32_bf16 a[8:11], v[16:19], v[148:151], a[8:11]\n\tbuffer_load_dwordx4 a[124:127], v8, s[8:11], s68 offen offset:2048 sc1\n\ts_add_u32 s68, s44, 1142784\n\tv_mfma_f32_16x16x32_bf16 a[16:19], v[16:19], v[152:155], a[16:19]\n\ts_waitcnt lgkmcnt(8)\n\t" \
    "v_mfma_f32_16x16x32_bf16 a[4:7], v[20:23], v[144:147], a[4:7]\n\tds_read_b64_tr_b16 v[40:41], v2 offset:28672\n\tds_read_b64_tr_b16 v[42:43], v2 offset:29184\n\tv_mfma_f32_16x16x32_bf16 a[12:15], v[20:23], v[148:151], a[12:15]\n\tbuffer_load_dwordx4 a[128:131], v8, s[8:11], s68 offen offset:0 sc1\n\tv_mfma_f32_16x16x32_bf16 a[20:23], v[20:23], v[152:155], a[20:23]\n\ts_waitcnt lgkmcnt(8)\n\tv_mfma_f32_16x16x32_bf16 v[48:51], v[24:27], v[144:147], v[48:51]\n\t" \
    "ds_read_b64_tr_b16 v[44:45], v3 offset:28672\n\tds_read_b64_tr_b16 v[46:47], v3 offset:29184\n\tv_mfma_f32_16x16x32_bf16 v[56:59], v[24:27], v[148:151], v[56:59]\n\tv_mfma_f32_16x16x32_bf16 v[64:67], v[24:27], v[152:155], v[64:67]\n\ts_waitcnt lgkmcnt(8)\n\tv_mfma_f32_16x16x32_bf16 v[52:55], v[28:31], v[144:147], v[52:55]\n\tds_read_b64_tr_b16 v[16:17], v0 offset:32768\n\tds_read_b64_tr_b16 v[18:19], v0 offset:33280\n\t" \
    "v_mfma_f32_16x16x32_bf16 v[60:63], v[28:31], v[148:151], v[60:63]\n\tbuffer_load_dwordx4 a[132:135], v8, s[8:11], s68 offen offset:1024 sc1\n\tv_mfma_f32_16x16x32_bf16 v[68:71], v[28:31], v[152:155], v[68:71]\n\ts_waitcnt lgkmcnt(8)\n\tv_mfma_f32_16x16x32_bf16 a[0:3], v[32:35], v[156:159], a[0:3]\n\tds_read_b64_tr_b16 v[20:21], v1 offset:32768\n\tds_read_b64_tr_b16 v[22:23], v1 offset:33280\n\tv_mfma_f32_16x16x32_bf16 a[8:11], v[32:35], v[160:163], a[8:11]\n\t" \
    "buffer_load_dwordx4 a[136:139], v8, s[8:11], s68 offen offset:2048 sc1\n\ts_mov_b32 s69, 4718720\n\ts_add_u32 m0, s72, 0\n\tv_mfma_f32_16x16x32_bf16 a[16:19], v[32:35], v[164:167], a[16:19]\n\ts_waitcnt lgkmcnt(8)\n\tv_mfma_f32_16x16x32_bf16 a[4:7], v[36:39], v[156:159], a[4:7]\n\tds_read_b64_tr_b16 v[24:25], v2 offset:32768\n\tds_read_b64_tr_b16 v[26:27], v2 offset:33280\n\t" \
    "v_mfma_f32_16x16x32_bf16 a[12:15], v[36:39], v[160:163], a[12:15]\n\tv_mfma_f32_16x16x32_bf16 a[20:23], v[36:39], v[164:167], a[20:23]\n\ts_waitcnt lgkmcnt(8)\n\tv_mfma_f32_16x16x32_bf16 v[48:51], v[40:43], v[156:159], v[48:51]\n\tds_read_b64_tr_b16 v[28:29], v3 offset:32768\n\tds_read_b64_tr_b16 v[30:31], v3 offset:33280\n\tv_mfma_f32_16x16x32_bf16 v[56:59], v[40:43], v[160:163], v[56:59]\n\tbuffer_load_dwordx4 v10, s[12:15], s69 offen lds\n\t" \
    "s_add_u32 m0, s72, 1024\n\tv_mfma_f32_16x16x32_bf16 v[64:67], v[40:43], v[164:167], v[64:67]\n\ts_waitcnt lgkmcnt(8)\n\tv_mfma_f32_16x16x32_bf16 v[52:55], v[44:47], v[156:159], v[52:55]\n\tds_read_b64_tr_b16 v[32:33], v0 offset:36864\n\tds_read_b64_tr_b16 v[34:35], v0 offset:37376\n\tv_mfma_f32_16x16x32_bf16 v[60:63], v[44:47], v[160:163], v[60:63]\n\tbuffer_load_dwordx4 v11, s[12:15], s69 offen lds\n\t" \
    "s_mov_b32 s69, 5013632\n\ts_add_u32 m0, s72, 4096\n\tv_mfma_f32_16x16x32_bf16 v[68:71], v[44:47], v[164:167], v[68:71]\n\ts_waitcnt vmcnt(31)\n\ts_barrier\n\ts_waitcnt lgkmcnt(8)\n\tv_mfma_f32_16x16x32_bf16 a[0:3], v[16:19], v[168:171], a[0:3]\n\tds_read_b64_tr_b16 v[36:37], v1 offset:36864\n\t" \
    "ds_read_b64_tr_b16 v[38:39], v1 offset:37376\n\tv_mfma_f32_16x16x32_bf16 a[8:11], v[16:19], v[172:175], a[8:11]\n\tbuffer_load_dwordx4 v10, s[12:15], s69 offen lds\n\ts_add_u32 m0, s72, 5120\n\tv_mfma_f32_16x16x32_bf16 a[16:19], v[16:19], v[176:179], a[16:19]\n\ts_waitcnt lgkmcnt(8)\n\tv_mfma_f32_16x16x32_bf16 a[4:7], v[20:23], v[168:171], a[4:7]\n\tds_read_b64_tr_b16 v[40:41], v2 offset:36864\n\t" \
    "ds_read_b64_tr_b16 v[42:43], v2 offset:37376\n\tv_mfma_f32_16x16x32_bf16 a[12:15], v[20:23], v[172:175], a[12:15]\n\tbuffer_load_dwordx4 v11, s[12:15], s69 offen lds\n\ts_mov_b32 s69, 5308544\n\ts_add_u32 m0, s72, 8192\n\tv_mfma_f32_16x16x32_bf16 a[20:23], v[20:23], v[176:179], a[20:23]\n\ts_waitcnt lgkmcnt(8)\n\tv_mfma_f32_16x16x32_bf16 v[48:51], v[24:27], v[168:171], v[48:51]\n\t" \
    "ds_read_b64_tr_b16 v[44:45], v3 offset:36864\n\tds_read_b64_tr_b16 v[46:47], v3 offset:37376\n\tv_mfma_f32_16x16x32_bf16 v[56:59], v[24:27], v[172:175], v[56:59]\n\tbuffer_load_dwordx4 v10, s[12:15], s69 offen lds\n\ts_add_u32 m0, s72, 9216\n\tv_mfma_f32_16x16x32_bf16 v[64:67], v[24:27], v[176:179], v[64:67]\n\ts_waitcnt lgkmcnt(8)\n\tv_mfma_f32_16x16x32_bf16 v[52:55], v[28:31], v[168:171], v[52:55]\n\t" \
    "ds_read_b64_tr_b16 v[16:17], v0 offset:40960\n\tds_read_b64_tr_b16 v[18:19], v0 offset:41472\n\tv_mfma_f32_16x16x32_bf16 v[60:63], v[28:31], v[172:175], v[60:63]\n\tv_mfma_f32_16x16x32_bf16 v[68:71], v[28:31], v[176:179], v[68:71]\n\ts_waitcnt vmcnt(31) lgkmcnt(8)\n\tv_mfma_f32_16x16x32_bf16 a[0:3], v[32:35], v[180:183], a[0:3]\n\tds_read_b64_tr_b16 v[20:21], v1 offset:40960\n\tds_read_b64_tr_b16 v[22:23], v1 offset:41472\n\t" \
    "v_mfma_f32_16x16x32_bf16 a[8:11], v[32:35], v[184:187], a[8:11]\n\tbuffer_load_dwordx4 v11, s[12:15], s69 offen lds\n\ts_mov_b32 s69, 5603456\n\ts_add_u32 m0, s72, 12288\n\tv_mfma_f32_16x16x32_bf16 a[16:19], v[32:35], v[188:191], a[16:19]\n\ts_waitcnt lgkmcnt(8)\n\tv_mfma_f32_16x16x32_bf16 a[4:7], v[36:39], v[180:183], a[4:7]\n\tds_read_b64_tr_b16 v[24:25], v2 offset:40960\n\t" \
    "ds_read_b64_tr_b16 v[26:27], v2 offset:41472\n\tv_mfma_f32_16x16x32_bf16 a[12:15], v[36:39], v[184:187], a[12:15]\n\tbuffer_load_dwordx4 v10, s[12:15], s69 offen lds\n\ts_add_u32 m0, s72, 13312\n\tv_mfma_f32_16x16x32_bf16 a[20:23], v[36:39], v[188:191], a[20:23]\n\ts_waitcnt lgkmcnt(8)\n\tv_mfma_f32_16x16x32_bf16 v[48:51], v[40:43], v[180:183], v[48:51]\n\tds_read_b64_tr_b16 v[28:29], v3 offset:40960\n\t" \
    "ds_read_b64_tr_b16 v[30:31], v3 offset:41472\n\tv_mfma_f32_16x16x32_bf16 v[56:59], v[40:43], v[184:187], v[56:59]\n\tv_mfma_f32_16x16x32_bf16 v[64:67], v[40:43], v[188:191], v[64:67]\n\ts_waitcnt lgkmcnt(8)\n\tv_mfma_f32_16x16x32_bf16 v[52:55], v[44:47], v[180:183], v[52:55]\n\tds_read_b64_tr_b16 v[32:33], v0 offset:45056\n\tds_read_b64_tr_b16 v[34:35], v0 offset:45568\n\tv_mfma_f32_16x16x32_bf16 v[60:63], v[44:47], v[184:187], v[60:63]\n\t" \
    "buffer_load_dwordx4 v11, s[12:15], s69 offen lds\n\ts_mov_b32 s69, 5898368\n\ts_add_u32 m0, s72, 16384\n\tv_mfma_f32_16x16x32_bf16 v[68:71], v[44:47], v[188:191], v[68:71]\n\ts_waitcnt vmcnt(28)\n\ts_barrier\n\ts_waitcnt vmcnt(25) lgkmcnt(8)\n\tv_mfma_f32_16x16x32_bf16 a[0:3], v[16:19], v[192:195], a[0:3]\n\t" \
    "ds_read_b64_tr_b16 v[36:37], v1 offset:45056\n\tds_read_b64_tr_b16 v[38:39], v1 offset:45568\n\tv_mfma_f32_16x16x32_bf16 a[8:11], v[16:19], v[196:199], a[8:11]\n\tbuffer_load_dwordx4 v10, s[12:15], s69 offen lds\n\ts_add_u32 m0, s72, 17408\n\tv_mfma_f32_16x16x32_bf16 a[16:19], v[16:19], v[200:203], a[16:19]\n\ts_waitcnt lgkmcnt(8)\n\tv_mfma_f32_16x16x32_bf16 a[4:7], v[20:23], v[192:195], a[4:7]\n\t" \
    "ds_read_b64_tr_b16 v[40:41], v2 offset:45056\n\tds_read_b64_tr_b16 v[42:43], v2 offset:45568\n\tv_mfma_f32_16x16x32_bf16 a[12:15], v[20:23], v[196:199], a[12:15]\n\tbuffer_load_dwordx4 v11, s[12:15], s69 offen lds\n\ts_mov_b32 s69, 6193280\n\ts_add_u32 m0, s72, 20480\n\tv_mfma_f32_16x16x32_bf16 a[20:23], v[20:23], v[200:203], a[20:23]\n\ts_waitcnt lgkmcnt(8)\n\t" \
    "v_mfma_f32_16x16x32_bf16 v[48:51], v[24:27], v[192:195], v[48:51]\n\tds_read_b64_tr_b16 v[44:45], v3 offset:45056\n\tds_read_b64_tr_b16 v[46:47], v3 offset:45568\n\tv_mfma_f32_16x16x32_bf16 v[56:59], v[24:27], v[196:199], v[56:59]\n\tbuffer_load_dwordx4 v10, s[12:15], s69 offen lds\n\ts_add_u32 m0, s72, 21504\n\tv_mfma_f32_16x16x32_bf16 v[64:67], v[24:27], v[200:203], v[64:67]\n\ts_waitcnt lgkmcnt(8)\n\t" \
    "v_mfma_f32_16x16x32_bf16 v[52:55], v[28:31], v[192:195], v[52:55]\n\tds_read_b64_tr_b16 v[16:17], v0 offset:49152\n\tds_read_b64_tr_b16 v[18:19], v0 offset:49664\n\tv_mfma_f32_16x16x32_bf16 v[60:63], v[28:31], v[196:199], v[60:63]\n\tv_mfma_f32_16x16x32_bf16 v[68:71], v[28:31], v[200:203], v[68:71]\n\ts_waitcnt vmcnt(25) lgkmcnt(8)\n\tv_mfma_f32_16x16x32_bf16 a[0:3], v[32:35], v[204:207], a[0:3]\n\tds_read_b64_tr_b16 v[20:21], v1 offset:49152\n\t" \
    "ds_read_b64_tr_b16 v[22:23], v1 offset:49664\n\tv_mfma_f32_16x16x32_bf16 a[8:11], v[32:35], v[208:211], a[8:11]\n\tbuffer_load_dwordx4 v11, s[12:15], s69 offen lds\n\ts_mov_b32 s69, 6488192\n\ts_add_u32 m0, s72, 24576\n\tv_mfma_f32_16x16x32_bf16 a[16:19], v[32:35], v[212:215], a[16:19]\n\ts_waitcnt lgkmcnt(8)\n\tv_mfma_f32_16x16x32_bf16 a[4:7], v[36:39], v[204:207], a[4:7]\n\t" \
    "ds_read_b64_tr_b16 v[24:25], v0 offset:53248\n\tds_read_b64_tr_b16 v[26:27], v0 offset:53760\n\tv_mfma_f32_16x16x32_bf16 a[12:15], v[36:39], v[208:211], a[12:15]\n\tbuffer_load_dwordx4 v10, s[12:15], s69 offen lds\n\ts_add_u32 m0, s72, 25600\n\tv_mfma_f32_16x16x32_bf16 a[20:23], v[36:39], v[212:215], a[20:23]\n\ts_waitcnt lgkmcnt(8)\n\tv_mfma_f32_16x16x32_bf16 v[48:51], v[40:43], v[204:207], v[48:51]\n\t" \
    "ds_read_b64_tr_b16 v[28:29], v1 offset:53248\n\tds_read_b64_tr_b16 v[30:31], v1 offset:53760\n\tv_mfma_f32_16x16x32_bf16 v[56:59], v[40:43], v[208:211], v[56:59]\n\tbuffer_load_dwordx4 v11, s[12:15], s69 offen lds\n\ts_mov_b32 s69, 6783104\n\ts_add_u32 m0, s72, 28672\n\tv_mfma_f32_16x16x32_bf16 v[64:67], v[40:43], v[212:215], v[64:67]\n\ts_waitcnt lgkmcnt(8)\n\t" \
    "v_mfma_f32_16x16x32_bf16 v[52:55], v[44:47], v[204:207], v[52:55]\n\tds_read_b64_tr_b16 v[32:33], v0 offset:57344\n\tds_read_b64_tr_b16 v[34:35], v0 offset:57856\n\tv_mfma_f32_16x16x32_bf16 v[60:63], v[44:47], v[208:211], v[60:63]\n\tv_mfma_f32_16x16x32_bf16 v[68:71], v[44:47], v[212:215], v[68:71]\n\ts_waitcnt vmcnt(26)\n\ts_barrier\n\ts_waitcnt vmcnt(23) lgkmcnt(8)\n\t" \
    "v_mfma_f32_16x16x32_bf16 a[0:3], v[16:19], v[216:219], a[0:3]\n\tds_read_b64_tr_b16 v[36:37], v1 offset:57344\n\tds_read_b64_tr_b16 v[38:39], v1 offset:57856\n\tv_mfma_f32_16x16x32_bf16 a[8:11], v[16:19], a[60:63], a[8:11]\n\tv_mfma_f32_16x16x32_bf16 a[16:19], v[16:19], a[64:67], a[16:19]\n\ts_waitcnt lgkmcnt(8)\n\tv_mfma_f32_16x16x32_bf16 a[4:7], v[20:23], v[216:219], a[4:7]\n\tds_read_b64_tr_b16 v[40:41], v0 offset:61440\n\t" \
    "ds_read_b64_tr_b16 v[42:43], v0 offset:61952\n\tv_mfma_f32_16x16x32_bf16 a[12:15], v[20:23], a[60:63], a[12:15]\n\tv_mfma_f32_16x16x32_bf16 a[20:23], v[20:23], a[64:67], a[20:23]\n\ts_waitcnt vmcnt(20) lgkmcnt(8)\n\tv_mfma_f32_16x16x32_bf16 a[0:3], v[24:27], a[68:71], a[0:3]\n\tds_read_b64_tr_b16 v[44:45], v1 offset:61440\n\tds_read_b64_tr_b16 v[46:47], v1 offset:61952\n\tv_mfma_f32_16x16x32_bf16 a[8:11], v[24:27], a[72:75], a[8:11]\n\t" \
    "v_mfma_f32_16x16x32_bf16 a[16:19], v[24:27], a[76:79], a[16:19]\n\ts_waitcnt lgkmcnt(8)\n\tv_mfma_f32_16x16x32_bf16 a[4:7], v[28:31], a[68:71], a[4:7]\n\tds_read_b64_tr_b16 v[16:17], v2 offset:49152\n\tds_read_b64_tr_b16 v[18:19], v2 offset:49664\n\tv_mfma_f32_16x16x32_bf16 a[12:15], v[28:31], a[72:75], a[12:15]\n\tv_mfma_f32_16x16x32_bf16 a[20:23], v[28:31], a[76:79], a[20:23]\n\ts_waitcnt vmcnt(17) lgkmcnt(8)\n\t" \
    "v_mfma_f32_16x16x32_bf16 a[0:3], v[32:35], a[116:119], a[0:3]\n\tds_read_b64_tr_b16 v[20:21], v3 offset:49152\n\tds_read_b64_tr_b16 v[22:23], v3 offset:49664\n\tv_mfma_f32_16x16x32_bf16 a[8:11], v[32:35], a[120:123], a[8:11]\n\tv_mfma_f32_16x16x32_bf16 a[16:19], v[32:35], a[124:127], a[16:19]\n\ts_waitcnt lgkmcnt(8)\n\tv_mfma_f32_16x16x32_bf16 a[4:7], v[36:39], a[116:119], a[4:7]\n\tds_read_b64_tr_b16 v[24:25], v2 offset:53248\n\t" \
    "ds_read_b64_tr_b16 v[26:27], v2 offset:53760\n\tv_mfma_f32_16x16x32_bf16 a[12:15], v[36:39], a[120:123], a[12:15]\n\tv_mfma_f32_16x16x32_bf16 a[20:23], v[36:39], a[124:127], a[20:23]\n\ts_waitcnt vmcnt(14) lgkmcnt(8)\n\tv_mfma_f32_16x16x32_bf16 a[0:3], v[40:43], a[128:131], a[0:3]\n\tds_read_b64_tr_b16 v[28:29], v3 offset:53248\n\tds_read_b64_tr_b16 v[30:31], v3 offset:53760\n\tv_mfma_f32_16x16x32_bf16 a[8:11], v[40:43], a[132:135], a[8:11]\n\t" \
    "v_mfma_f32_16x16x32_bf16 a[16:19], v[40:43], a[136:139], a[16:19]\n\ts_waitcnt lgkmcnt(8)\n\tv_mfma_f32_16x16x32_bf16 a[4:7], v[44:47], a[128:131], a[4:7]\n\tds_read_b64_tr_b16 v[32:33], v2 offset:57344\n\tds_read_b64_tr_b16 v[34:35], v2 offset:57856\n\tv_mfma_f32_16x16x32_bf16 a[12:15], v[44:47], a[132:135], a[12:15]\n\tv_mfma_f32_16x16x32_bf16 a[20:23], v[44:47], a[136:139], a[20:23]\n\ts_waitcnt vmcnt(12)\n\t" \
    "s_barrier\n\ts_waitcnt lgkmcnt(8)\n\tv_mfma_f32_16x16x32_bf16 v[48:51], v[16:19], v[216:219], v[48:51]\n\tds_read_b64_tr_b16 v[36:37], v3 offset:57344\n\tds_read_b64_tr_b16 v[38:39], v3 offset:57856\n\tv_mfma_f32_16x16x32_bf16 v[56:59], v[16:19], a[60:63], v[56:59]\n\tv_mfma_f32_16x16x32_bf16 v[64:67], v[16:19], a[64:67], v[64:67]\n\tds_write_b128 v237, a[0:3] offset:0\n\t" \
    "s_waitcnt lgkmcnt(9)\n\tv_mfma_f32_16x16x32_bf16 v[52:55], v[20:23], v[216:219], v[52:55]\n\tds_read_b64_tr_b16 v[40:41], v2 offset:61440\n\tds_read_b64_tr_b16 v[42:43], v2 offset:61952\n\tv_mfma_f32_16x16x32_bf16 v[60:63], v[20:23], a[60:63], v[60:63]\n\tv_mfma_f32_16x16x32_bf16 v[68:71], v[20:23], a[64:67], v[68:71]\n\tds_write_b128 v237, a[8:11] offset:2048\n\ts_waitcnt lgkmcnt(10)\n\t" \
    "v_mfma_f32_16x16x32_bf16 v[48:51], v[24:27], a[68:71], v[48:51]\n\tds_read_b64_tr_b16 v[44:45], v3 offset:61440\n\tds_read_b64_tr_b16 v[46:47], v3 offset:61952\n\tv_mfma_f32_16x16x32_bf16 v[56:59], v[24:27], a[72:75], v[56:59]\n\tv_mfma_f32_16x16x32_bf16 v[64:67], v[24:27], a[76:79], v[64:67]\n\tds_write_b128 v237, a[16:19] offset:4096\n\ts_waitcnt lgkmcnt(11)\n\tv_mfma_f32_16x16x32_bf16 v[52:55], v[28:31], a[68:71], v[52:55]\n\t" \
    "ds_read_b64_tr_b16 v[16:17], v0 offset:0\n\tds_read_b64_tr_b16 v[18:19], v0 offset:512\n\tv_mfma_f32_16x16x32_bf16 v[60:63], v[28:31], a[72:75], v[60:63]\n\tv_mfma_f32_16x16x32_bf16 v[68:71], v[28:31], a[76:79], v[68:71]\n\tds_write_b128 v237, a[4:7] offset:1024\n\tds_write_b128 v237, a[12:15] offset:3072\n\tds_write_b128 v237, a[20:23] offset:5120\n\ts_waitcnt vmcnt(10) lgkmcnt(0)\n\t" \
    "s_barrier\n\tv_mfma_f32_16x16x32_bf16 v[48:51], v[32:35], a[116:119], v[48:51]\n\tds_read_b64_tr_b16 v[20:21], v1 offset:0\n\tds_read_b64_tr_b16 v[22:23], v1 offset:512\n\tv_mfma_f32_16x16x32_bf16 v[56:59], v[32:35], a[120:123], v[56:59]\n\tbuffer_load_dwordx4 v10, s[12:15], s69 offen lds\n\tds_read_b128 v[96:99], v238 offset:0\n\ts_add_u32 m0, s72, 29696\n\t" \
    "v_mfma_f32_16x16x32_bf16 v[64:67], v[32:35], a[124:127], v[64:67]\n\tds_read_b128 v[100:103], v238 offset:1024\n\tds_read_b128 v[104:107], v238 offset:2048\n\tv_mfma_f32_16x16x32_bf16 v[52:55], v[36:39], a[116:119], v[52:55]\n\tds_read_b64_tr_b16 v[24:25], v2 offset:0\n\tds_read_b64_tr_b16 v[26:27], v2 offset:512\n\tv_mfma_f32_16x16x32_bf16 v[60:63], v[36:39], a[120:123], v[60:63]\n\tbuffer_load_dwordx4 v11, s[12:15], s69 offen lds\n\t" \
    "ds_read_b128 v[108:111], v238 offset:3072\n\ts_mov_b32 s69, 7078016\n\tv_mfma_f32_16x16x32_bf16 v[68:71], v[36:39], a[124:127], v[68:71]\n\tds_read_b128 v[112:115], v238 offset:4096\n\tds_read_b128 v[116:119], v238 offset:5120\n\tv_mfma_f32_16x16x32_bf16 v[48:51], v[40:43], a[128:131], v[48:51]\n\tds_read_b64_tr_b16 v[28:29], v3 offset:0\n\tds_read_b64_tr_b16 v[30:31], v3 offset:512\n\t" \
    "s_add_u32 m0, s72, 32768\n\tv_mfma_f32_16x16x32_bf16 v[56:59], v[40:43], a[132:135], v[56:59]\n\tbuffer_load_dwordx4 v10, s[12:15], s69 offen lds\n\ts_add_u32 m0, s72, 33792\n\tv_mfma_f32_16x16x32_bf16 v[64:67], v[40:43], a[136:139], v[64:67]\n\tv_mfma_f32_16x16x32_bf16 v[52:55], v[44:47], a[128:131], v[52:55]\n\tds_read_b64_tr_b16 v[32:33], v0 offset:4096\n\tds_read_b64_tr_b16 v[34:35], v0 offset:4608\n\t" \
    "v_mfma_f32_16x16x32_bf16 v[60:63], v[44:47], a[132:135], v[60:63]\n\tbuffer_load_dwordx4 v11, s[12:15], s69 offen lds\n\ts_mov_b32 s69, 7372928\n\ts_add_u32 m0, s72, 36864\n\tv_mfma_f32_16x16x32_bf16 v[68:71], v[44:47], a[136:139], v[68:71]\n\ts_waitcnt lgkmcnt(10)\n\tv_pk_add_f32 v[48:49], v[48:49], v[96:97]\n\tv_pk_add_f32 v[50:51], v[50:51], v[98:99]\n\t" \
    "v_pk_add_f32 v[52:53], v[52:53], v[100:101]\n\tbuffer_load_dwordx4 v10, s[12:15], s69 offen lds\n\tv_pk_add_f32 v[54:55], v[54:55], v[102:103]\n\ts_add_u32 m0, s72, 37888\n\tv_pk_mul_f32 v[96:97], v[48:49], v[48:49]\n\tv_pk_mul_f32 v[98:99], v[50:51], v[50:51]\n\tv_pk_mul_f32 v[100:101], v[52:53], v[52:53]\n\tv_pk_mul_f32 v[102:103], v[54:55], v[54:55]\n\t" \
    "v_pk_fma_f32 v[96:97], v[96:97], v[246:247], v[246:247] op_sel:[0,0,1] op_sel_hi:[1,0,1]\n\tv_pk_fma_f32 v[98:99], v[98:99], v[246:247], v[246:247] op_sel:[0,0,1] op_sel_hi:[1,0,1]\n\tv_pk_fma_f32 v[100:101], v[100:101], v[246:247], v[246:247] op_sel:[0,0,1] op_sel_hi:[1,0,1]\n\tv_pk_fma_f32 v[102:103], v[102:103], v[246:247], v[246:247] op_sel:[0,0,1] op_sel_hi:[1,0,1]\n\tv_pk_mul_f32 v[96:97], v[96:97], v[48:49]\n\tv_pk_mul_f32 v[98:99], v[98:99], v[50:51]\n\tv_pk_mul_f32 v[100:101], v[100:101], v[52:53]\n\tbuffer_load_dwordx4 v11, s[12:15], s69 offen lds\n\t" \
    "v_pk_mul_f32 v[102:103], v[102:103], v[54:55]\n\ts_mov_b32 s69, 7667840\n\ts_waitcnt lgkmcnt(6)\n\tv_exp_f32 v96, v96\n\ts_add_u32 m0, s72, 40960\n\tv_pk_add_f32 v[56:57], v[56:57], v[104:105]\n\tv_exp_f32 v97, v97\n\tv_pk_add_f32 v[58:59], v[58:59], v[106:107]\n\t" \
    "v_exp_f32 v98, v98\n\tv_pk_add_f32 v[60:61], v[60:61], v[108:109]\n\tv_exp_f32 v99, v99\n\tbuffer_load_dwordx4 v10, s[12:15], s69 offen lds\n\tv_pk_add_f32 v[62:63], v[62:63], v[110:111]\n\ts_add_u32 m0, s72, 41984\n\tv_exp_f32 v100, v100\n\tv_pk_mul_f32 v[104:105], v[56:57], v[56:57]\n\t" \
    "v_exp_f32 v101, v101\n\tv_pk_mul_f32 v[106:107], v[58:59], v[58:59]\n\tv_exp_f32 v102, v102\n\tv_pk_mul_f32 v[108:109], v[60:61], v[60:61]\n\tv_exp_f32 v103, v103\n\tv_pk_mul_f32 v[110:111], v[62:63], v[62:63]\n\tv_pk_add_f32 v[96:97], v[96:97], 1.0 op_sel_hi:[1,0]\n\tv_pk_add_f32 v[98:99], v[98:99], 1.0 op_sel_hi:[1,0]\n\t" \
    "buffer_load_dwordx4 v11, s[12:15], s69 offen lds\n\tv_pk_add_f32 v[100:101], v[100:101], 1.0 op_sel_hi:[1,0]\n\ts_mov_b32 s69, 7962752\n\tv_pk_add_f32 v[102:103], v[102:103], 1.0 op_sel_hi:[1,0]\n\ts_add_u32 m0, s72, 45056\n\tv_pk_fma_f32 v[104:105], v[104:105], v[246:247], v[246:247] op_sel:[0,0,1] op_sel_hi:[1,0,1]\n\tv_rcp_f32 v96, v96\n\tv_pk_fma_f32 v[106:107], v[106:107], v[246:247], v[246:247] op_sel:[0,0,1] op_sel_hi:[1,0,1]\n\t" \
    "v_rcp_f32 v97, v97\n\tv_pk_fma_f32 v[108:109], v[108:109], v[246:247], v[246:247] op_sel:[0,0,1] op_sel_hi:[1,0,1]\n\tv_rcp_f32 v98, v98\n\tv_pk_fma_f32 v[110:111], v[110:111], v[246:247], v[246:247] op_sel:[0,0,1] op_sel_hi:[1,0,1]\n\tv_rcp_f32 v99, v99\n\tbuffer_load_dwordx4 v10, s[12:15], s69 offen lds\n\tv_pk_mul_f32 v[104:105], v[104:105], v[56:57]\n\ts_add_u32 m0, s72, 46080\n\t" \
    "v_rcp_f32 v100, v100\n\tv_pk_mul_f32 v[106:107], v[106:107], v[58:59]\n\tv_rcp_f32 v101, v101\n\tv_pk_mul_f32 v[108:109], v[108:109], v[60:61]\n\tv_rcp_f32 v102, v102\n\tv_pk_mul_f32 v[110:111], v[110:111], v[62:63]\n\tv_rcp_f32 v103, v103\n\ts_waitcnt lgkmcnt(4)\n\t" \
    "v_exp_f32 v104, v104\n\tbuffer_load_dwordx4 v11, s[12:15], s69 offen lds\n\tv_pk_mul_f32 v[96:97], v[96:97], v[48:49]\n\ts_mov_b32 s69, 8257664\n\tv_exp_f32 v105, v105\n\ts_add_u32 m0, s72, 49152\n\tv_pk_mul_f32 v[98:99], v[98:99], v[50:51]\n\tv_exp_f32 v106, v106\n\t" \
    "v_pk_mul_f32 v[100:101], v[100:101], v[52:53]\n\tv_exp_f32 v107, v107\n\tv_pk_mul_f32 v[102:103], v[102:103], v[54:55]\n\tv_exp_f32 v108, v108\n\tbuffer_load_dwordx4 v10, s[12:15], s69 offen lds\n\tv_cvt_pk_bf16_f32 v48, v96, v97\n\ts_add_u32 m0, s72, 50176\n\tv_exp_f32 v109, v109\n\t" \
    "v_cvt_pk_bf16_f32 v49, v98, v99\n\tv_exp_f32 v110, v110\n\tv_cvt_pk_bf16_f32 v50, v100, v101\n\tv_exp_f32 v111, v111\n\tv_cvt_pk_bf16_f32 v51, v102, v103\n\tbuffer_store_dwordx4 v[48:51], v8, s[8:11], s45 offen offset:0 sc1\n\tv_pk_add_f32 v[104:105], v[104:105], 1.0 op_sel_hi:[1,0]\n\tv_pk_add_f32 v[106:107], v[106:107], 1.0 op_sel_hi:[1,0]\n\t" \
    "v_pk_add_f32 v[108:109], v[108:109], 1.0 op_sel_hi:[1,0]\n\tv_pk_add_f32 v[110:111], v[110:111], 1.0 op_sel_hi:[1,0]\n\tv_pk_add_f32 v[64:65], v[64:65], v[112:113]\n\tv_rcp_f32 v104, v104\n\tv_pk_add_f32 v[66:67], v[66:67], v[114:115]\n\tv_rcp_f32 v105, v105\n\tv_pk_add_f32 v[68:69], v[68:69], v[116:117]\n\tv_rcp_f32 v106, v106\n\t" \
    "v_pk_add_f32 v[70:71], v[70:71], v[118:119]\n\tv_rcp_f32 v107, v107\n\tv_pk_mul_f32 v[112:113], v[64:65], v[64:65]\n\tv_rcp_f32 v108, v108\n\tv_pk_mul_f32 v[114:115], v[66:67], v[66:67]\n\tv_rcp_f32 v109, v109\n\tbuffer_load_dwordx4 v11, s[12:15], s69 offen lds\n\tv_pk_mul_f32 v[116:117], v[68:69], v[68:69]\n\t" \
    "s_mov_b32 s69, 8552576\n\tv_rcp_f32 v110, v110\n\ts_add_u32 m0, s72, 53248\n\tv_pk_mul_f32 v[118:119], v[70:71], v[70:71]\n\tv_rcp_f32 v111, v111\n\tv_pk_fma_f32 v[112:113], v[112:113], v[246:247], v[246:247] op_sel:[0,0,1] op_sel_hi:[1,0,1]\n\tv_pk_mul_f32 v[104:105], v[104:105], v[56:57]\n\tv_pk_mul_f32 v[106:107], v[106:107], v[58:59]\n\t" \
    "v_pk_mul_f32 v[108:109], v[108:109], v[60:61]\n\tv_pk_mul_f32 v[110:111], v[110:111], v[62:63]\n\tv_cvt_pk_bf16_f32 v56, v104, v105\n\tv_cvt_pk_bf16_f32 v57, v106, v107\n\tv_cvt_pk_bf16_f32 v58, v108, v109\n\tbuffer_load_dwordx4 v10, s[12:15], s69 offen lds\n\tv_cvt_pk_bf16_f32 v59, v110, v111\n\ts_add_u32 m0, s72, 54272\n\t" \
    "buffer_store_dwordx4 v[56:59], v8, s[8:11], s45 offen offset:1024 sc1\n\tv_pk_fma_f32 v[114:115], v[114:115], v[246:247], v[246:247] op_sel:[0,0,1] op_sel_hi:[1,0,1]\n\tv_pk_fma_f32 v[116:117], v[116:117], v[246:247], v[246:247] op_sel:[0,0,1] op_sel_hi:[1,0,1]\n\tv_pk_fma_f32 v[118:119], v[118:119], v[246:247], v[246:247] op_sel:[0,0,1] op_sel_hi:[1,0,1]\n\tv_pk_mul_f32 v[112:113], v[112:113], v[64:65]\n\tv_pk_mul_f32 v[114:115], v[114:115], v[66:67]\n\tv_pk_mul_f32 v[116:117], v[116:117], v[68:69]\n\tv_pk_mul_f32 v[118:119], v[118:119], v[70:71]\n\t" \
    "v_exp_f32 v112, v112\n\tv_exp_f32 v113, v113\n\tv_exp_f32 v114, v114\n\tv_exp_f32 v115, v115\n\tv_exp_f32 v116, v116\n\tv_exp_f32 v117, v117\n\tv_exp_f32 v118, v118\n\tv_exp_f32 v119, v119\n\t" \
    "v_pk_add_f32 v[112:113], v[112:113], 1.0 op_sel_hi:[1,0]\n\tv_pk_add_f32 v[114:115], v[114:115], 1.0 op_sel_hi:[1,0]\n\tv_pk_add_f32 v[116:117], v[116:117], 1.0 op_sel_hi:[1,0]\n\tv_pk_add_f32 v[118:119], v[118:119], 1.0 op_sel_hi:[1,0]\n\tv_rcp_f32 v112, v112\n\tv_rcp_f32 v113, v113\n\tbuffer_load_dwordx4 v11, s[12:15], s69 offen lds\n\tv_rcp_f32 v114, v114\n\t" \
    "v_rcp_f32 v115, v115\n\tv_rcp_f32 v116, v116\n\tv_rcp_f32 v117, v117\n\tv_rcp_f32 v118, v118\n\tv_rcp_f32 v119, v119\n\tv_pk_mul_f32 v[112:113], v[112:113], v[64:65]\n\tv_pk_mul_f32 v[114:115], v[114:115], v[66:67]\n\tv_pk_mul_f32 v[116:117], v[116:117], v[68:69]\n\t" \
    "buffer_store_dwordx2 v[248:249], off, s[8:11], s60 sc1\n\tv_pk_mul_f32 v[118:119], v[118:119], v[70:71]\n\tv_cvt_pk_bf16_f32 v64, v112, v113\n\tv_cvt_pk_bf16_f32 v65, v114, v115\n\tv_cvt_pk_bf16_f32 v66, v116, v117\n\tv_cvt_pk_bf16_f32 v67, v118, v119\n\tbuffer_store_dwordx4 v[64:67], v8, s[8:11], s45 offen offset:2048 sc1\n\ts_waitcnt vmcnt(24)\n\t" \
    "s_barrier\n\tv_mfma_f32_16x16x32_bf16 a[24:27], v[16:19], a[80:83], a[24:27]\n\tds_read_b64_tr_b16 v[36:37], v1 offset:4096\n\tds_read_b64_tr_b16 v[38:39], v1 offset:4608\n\ts_mov_b32 s69, 8847488\n\tv_mfma_f32_16x16x32_bf16 a[32:35], v[16:19], a[84:87], a[32:35]\n\ts_add_u32 m0, s72, 57344\n\tv_mfma_f32_16x16x32_bf16 a[40:43], v[16:19], a[88:91], a[40:43]\n\t" \
    "v_mfma_f32_16x16x32_bf16 a[28:31], v[20:23], a[80:83], a[28:31]\n\tds_read_b64_tr_b16 v[40:41], v2 offset:4096\n\tds_read_b64_tr_b16 v[42:43], v2 offset:4608\n\tv_mfma_f32_16x16x32_bf16 a[36:39], v[20:23], a[84:87], a[36:39]\n\tbuffer_load_dwordx4 v10, s[12:15], s69 offen lds\n\ts_add_u32 m0, s72, 58368\n\tv_mfma_f32_16x16x32_bf16 a[44:47], v[20:23], a[88:91], a[44:47]\n\tv_mfma_f32_16x16x32_bf16 v[72:75], v[24:27], a[80:83], v[72:75]\n\t" \
    "ds_read_b64_tr_b16 v[44:45], v3 offset:4096\n\tds_read_b64_tr_b16 v[46:47], v3 offset:4608\n\tv_mfma_f32_16x16x32_bf16 v[80:83], v[24:27], a[84:87], v[80:83]\n\tv_mfma_f32_16x16x32_bf16 v[88:91], v[24:27], a[88:91], v[88:91]\n\ts_waitcnt lgkmcnt(8)\n\tv_mfma_f32_16x16x32_bf16 v[76:79], v[28:31], a[80:83], v[76:79]\n\tds_read_b64_tr_b16 v[16:17], v0 offset:8192\n\tds_read_b64_tr_b16 v[18:19], v0 offset:8704\n\t" \
    "v_mfma_f32_16x16x32_bf16 v[84:87], v[28:31], a[84:87], v[84:87]\n\tbuffer_load_dwordx4 v11, s[12:15], s69 offen lds\n\ts_mov_b32 s69, 9142400\n\ts_add_u32 m0, s72, 61440\n\tv_mfma_f32_16x16x32_bf16 v[92:95], v[28:31], a[88:91], v[92:95]\n\ts_waitcnt lgkmcnt(8)\n\tv_mfma_f32_16x16x32_bf16 a[24:27], v[32:35], a[48:51], a[24:27]\n\tds_read_b64_tr_b16 v[20:21], v1 offset:8192\n\t" \
    "ds_read_b64_tr_b16 v[22:23], v1 offset:8704\n\tv_mfma_f32_16x16x32_bf16 a[32:35], v[32:35], a[52:55], a[32:35]\n\tbuffer_load_dwordx4 v10, s[12:15], s69 offen lds\n\ts_add_u32 m0, s72, 62464\n\tv_mfma_f32_16x16x32_bf16 a[40:43], v[32:35], a[56:59], a[40:43]\n\ts_waitcnt lgkmcnt(8)\n\tv_mfma_f32_16x16x32_bf16 a[28:31], v[36:39], a[48:51], a[28:31]\n\tds_read_b64_tr_b16 v[24:25], v2 offset:8192\n\t" \
    "ds_read_b64_tr_b16 v[26:27], v2 offset:8704\n\tv_mfma_f32_16x16x32_bf16 a[36:39], v[36:39], a[52:55], a[36:39]\n\tbuffer_load_dwordx4 v11, s[12:15], s69 offen lds\n\tv_mfma_f32_16x16x32_bf16 a[44:47], v[36:39], a[56:59], a[44:47]\n\ts_waitcnt lgkmcnt(8)\n\tv_mfma_f32_16x16x32_bf16 v[72:75], v[40:43], a[48:51], v[72:75]\n\tds_read_b64_tr_b16 v[28:29], v3 offset:8192\n\tds_read_b64_tr_b16 v[30:31], v3 offset:8704\n\t" \
    "v_mfma_f32_16x16x32_bf16 v[80:83], v[40:43], a[52:55], v[80:83]\n\tv_mfma_f32_16x16x32_bf16 v[88:91], v[40:43], a[56:59], v[88:91]\n\ts_waitcnt lgkmcnt(8)\n\tv_mfma_f32_16x16x32_bf16 v[76:79], v[44:47], a[48:51], v[76:79]\n\tds_read_b64_tr_b16 v[32:33], v0 offset:12288\n\tds_read_b64_tr_b16 v[34:35], v0 offset:12800\n\tv_mfma_f32_16x16x32_bf16 v[84:87], v[44:47], a[52:55], v[84:87]\n\tv_mfma_f32_16x16x32_bf16 v[92:95], v[44:47], a[56:59], v[92:95]\n\t" \
    "s_waitcnt vmcnt(24)\n\ts_barrier\n\ts_waitcnt lgkmcnt(8)\n\tv_mfma_f32_16x16x32_bf16 a[24:27], v[16:19], a[92:95], a[24:27]\n\tds_read_b64_tr_b16 v[36:37], v1 offset:12288\n\tds_read_b64_tr_b16 v[38:39], v1 offset:12800\n\ts_mov_b32 s69, 131072\n\tv_mfma_f32_16x16x32_bf16 a[32:35], v[16:19], a[96:99], a[32:35]\n\t" \
    "s_add_u32 m0, s54, 0\n\tv_mfma_f32_16x16x32_bf16 a[40:43], v[16:19], a[100:103], a[40:43]\n\ts_waitcnt lgkmcnt(8)\n\tv_mfma_f32_16x16x32_bf16 a[28:31], v[20:23], a[92:95], a[28:31]\n\tds_read_b64_tr_b16 v[40:41], v2 offset:12288\n\tds_read_b64_tr_b16 v[42:43], v2 offset:12800\n\tv_mfma_f32_16x16x32_bf16 a[36:39], v[20:23], a[96:99], a[36:39]\n\tbuffer_load_dwordx4 v12, s[16:19], s69 offen lds\n\t" \
    "s_mov_b32 s69, 131072\n\ts_add_u32 m0, s54, 65536\n\tv_mfma_f32_16x16x32_bf16 a[44:47], v[20:23], a[100:103], a[44:47]\n\ts_waitcnt lgkmcnt(8)\n\tv_mfma_f32_16x16x32_bf16 v[72:75], v[24:27], a[92:95], v[72:75]\n\tds_read_b64_tr_b16 v[44:45], v3 offset:12288\n\tds_read_b64_tr_b16 v[46:47], v3 offset:12800\n\tv_mfma_f32_16x16x32_bf16 v[80:83], v[24:27], a[96:99], v[80:83]\n\t" \
    "buffer_load_dwordx4 v13, s[16:19], s69 offen lds\n\ts_mov_b32 s69, 0\n\ts_add_u32 m0, s54, 4096\n\tv_mfma_f32_16x16x32_bf16 v[88:91], v[24:27], a[100:103], v[88:91]\n\ts_waitcnt lgkmcnt(8)\n\tv_mfma_f32_16x16x32_bf16 v[76:79], v[28:31], a[92:95], v[76:79]\n\tds_read_b64_tr_b16 v[16:17], v0 offset:16384\n\tds_read_b64_tr_b16 v[18:19], v0 offset:16896\n\t" \
    "v_mfma_f32_16x16x32_bf16 v[84:87], v[28:31], a[96:99], v[84:87]\n\tbuffer_load_dwordx4 v12, s[16:19], s69 offen lds\n\ts_mov_b32 s69, 0\n\ts_add_u32 m0, s54, 69632\n\tv_mfma_f32_16x16x32_bf16 v[92:95], v[28:31], a[100:103], v[92:95]\n\ts_waitcnt lgkmcnt(8)\n\tv_mfma_f32_16x16x32_bf16 a[24:27], v[32:35], a[104:107], a[24:27]\n\tds_read_b64_tr_b16 v[20:21], v1 offset:16384\n\t" \
    "ds_read_b64_tr_b16 v[22:23], v1 offset:16896\n\tv_mfma_f32_16x16x32_bf16 a[32:35], v[32:35], a[108:111], a[32:35]\n\tbuffer_load_dwordx4 v13, s[16:19], s69 offen lds\n\tv_mfma_f32_16x16x32_bf16 a[40:43], v[32:35], a[112:115], a[40:43]\n\ts_waitcnt lgkmcnt(8)\n\tv_mfma_f32_16x16x32_bf16 a[28:31], v[36:39], a[104:107], a[28:31]\n\tds_read_b64_tr_b16 v[24:25], v2 offset:16384\n\tds_read_b64_tr_b16 v[26:27], v2 offset:16896\n\t" \
    "v_mfma_f32_16x16x32_bf16 a[36:39], v[36:39], a[108:111], a[36:39]\n\tv_mfma_f32_16x16x32_bf16 a[44:47], v[36:39], a[112:115], a[44:47]\n\ts_waitcnt lgkmcnt(8)\n\tv_mfma_f32_16x16x32_bf16 v[72:75], v[40:43], a[104:107], v[72:75]\n\tds_read_b64_tr_b16 v[28:29], v3 offset:16384\n\tds_read_b64_tr_b16 v[30:31], v3 offset:16896\n\tv_mfma_f32_16x16x32_bf16 v[80:83], v[40:43], a[108:111], v[80:83]\n\tv_mfma_f32_16x16x32_bf16 v[88:91], v[40:43], a[112:115], v[88:91]\n\t" \
    "s_waitcnt lgkmcnt(8)\n\tv_mfma_f32_16x16x32_bf16 v[76:79], v[44:47], a[104:107], v[76:79]\n\tds_read_b64_tr_b16 v[32:33], v0 offset:20480\n\tds_read_b64_tr_b16 v[34:35], v0 offset:20992\n\tv_mfma_f32_16x16x32_bf16 v[84:87], v[44:47], a[108:111], v[84:87]\n\tv_mfma_f32_16x16x32_bf16 v[92:95], v[44:47], a[112:115], v[92:95]\n\ts_waitcnt vmcnt(24)\n\ts_barrier\n\t" \
    "s_waitcnt lgkmcnt(8)\n\tv_mfma_f32_16x16x32_bf16 a[24:27], v[16:19], v[120:123], a[24:27]\n\tds_read_b64_tr_b16 v[36:37], v1 offset:20480\n\tds_read_b64_tr_b16 v[38:39], v1 offset:20992\n\ts_mov_b32 s69, 655360\n\tv_mfma_f32_16x16x32_bf16 a[32:35], v[16:19], v[124:127], a[32:35]\n\ts_add_u32 m0, s54, 8192\n\tv_mfma_f32_16x16x32_bf16 a[40:43], v[16:19], v[128:131], a[40:43]\n\t" \
    "s_waitcnt lgkmcnt(8)\n\tv_mfma_f32_16x16x32_bf16 a[28:31], v[20:23], v[120:123], a[28:31]\n\tds_read_b64_tr_b16 v[40:41], v2 offset:20480\n\tds_read_b64_tr_b16 v[42:43], v2 offset:20992\n\tv_mfma_f32_16x16x32_bf16 a[36:39], v[20:23], v[124:127], a[36:39]\n\tbuffer_load_dwordx4 v12, s[16:19], s69 offen lds\n\ts_mov_b32 s69, 655360\n\ts_add_u32 m0, s54, 73728\n\t" \
    "v_mfma_f32_16x16x32_bf16 a[44:47], v[20:23], v[128:131], a[44:47]\n\ts_waitcnt lgkmcnt(8)\n\tv_mfma_f32_16x16x32_bf16 v[72:75], v[24:27], v[120:123], v[72:75]\n\tds_read_b64_tr_b16 v[44:45], v3 offset:20480\n\tds_read_b64_tr_b16 v[46:47], v3 offset:20992\n\tv_mfma_f32_16x16x32_bf16 v[80:83], v[24:27], v[124:127], v[80:83]\n\tbuffer_load_dwordx4 v13, s[16:19], s69 offen lds\n\ts_mov_b32 s69, 524288\n\t" \
    "s_add_u32 m0, s54, 12288\n\tv_mfma_f32_16x16x32_bf16 v[88:91], v[24:27], v[128:131], v[88:91]\n\ts_waitcnt lgkmcnt(8)\n\tv_mfma_f32_16x16x32_bf16 v[76:79], v[28:31], v[120:123], v[76:79]\n\tds_read_b64_tr_b16 v[16:17], v0 offset:24576\n\tds_read_b64_tr_b16 v[18:19], v0 offset:25088\n\tv_mfma_f32_16x16x32_bf16 v[84:87], v[28:31], v[124:127], v[84:87]\n\tbuffer_load_dwordx4 v12, s[16:19], s69 offen lds\n\t" \
    "s_mov_b32 s69, 524288\n\ts_add_u32 m0, s54, 77824\n\tv_mfma_f32_16x16x32_bf16 v[92:95], v[28:31], v[128:131], v[92:95]\n\ts_waitcnt lgkmcnt(8)\n\tv_mfma_f32_16x16x32_bf16 a[24:27], v[32:35], v[132:135], a[24:27]\n\tds_read_b64_tr_b16 v[20:21], v1 offset:24576\n\tds_read_b64_tr_b16 v[22:23], v1 offset:25088\n\tv_mfma_f32_16x16x32_bf16 a[32:35], v[32:35], v[136:139], a[32:35]\n\t" \
    "buffer_load_dwordx4 v13, s[16:19], s69 offen lds\n\tv_mfma_f32_16x16x32_bf16 a[40:43], v[32:35], v[140:143], a[40:43]\n\ts_waitcnt lgkmcnt(8)\n\tv_mfma_f32_16x16x32_bf16 a[28:31], v[36:39], v[132:135], a[28:31]\n\tds_read_b64_tr_b16 v[24:25], v2 offset:24576\n\tds_read_b64_tr_b16 v[26:27], v2 offset:25088\n\tv_mfma_f32_16x16x32_bf16 a[36:39], v[36:39], v[136:139], a[36:39]\n\tv_mfma_f32_16x16x32_bf16 a[44:47], v[36:39], v[140:143], a[44:47]\n\t" \
    "s_waitcnt lgkmcnt(8)\n\tv_mfma_f32_16x16x32_bf16 v[72:75], v[40:43], v[132:135], v[72:75]\n\tds_read_b64_tr_b16 v[28:29], v3 offset:24576\n\tds_read_b64_tr_b16 v[30:31], v3 offset:25088\n\tv_mfma_f32_16x16x32_bf16 v[80:83], v[40:43], v[136:139], v[80:83]\n\tv_mfma_f32_16x16x32_bf16 v[88:91], v[40:43], v[140:143], v[88:91]\n\ts_waitcnt lgkmcnt(8)\n\tv_mfma_f32_16x16x32_bf16 v[76:79], v[44:47], v[132:135], v[76:79]\n\t" \
    "ds_read_b64_tr_b16 v[32:33], v0 offset:28672\n\tds_read_b64_tr_b16 v[34:35], v0 offset:29184\n\tv_mfma_f32_16x16x32_bf16 v[84:87], v[44:47], v[136:139], v[84:87]\n\tv_mfma_f32_16x16x32_bf16 v[92:95], v[44:47], v[140:143], v[92:95]\n\ts_waitcnt vmcnt(12)\n\tbuffer_store_dwordx2 v[248:249], off, s[8:11], s49 sc1\n\ts_barrier\n\ts_waitcnt lgkmcnt(8)\n\t" \
    "v_mfma_f32_16x16x32_bf16 a[24:27], v[16:19], v[144:147], a[24:27]\n\tds_read_b64_tr_b16 v[36:37], v1 offset:28672\n\tds_read_b64_tr_b16 v[38:39], v1 offset:29184\n\ts_mov_b32 s69, 1179648\n\tv_mfma_f32_16x16x32_bf16 a[32:35], v[16:19], v[148:151], a[32:35]\n\ts_add_u32 m0, s54, 16384\n\tv_mfma_f32_16x16x32_bf16 a[40:43], v[16:19], v[152:155], a[40:43]\n\ts_waitcnt lgkmcnt(8)\n\t" \
    "v_mfma_f32_16x16x32_bf16 a[28:31], v[20:23], v[144:147], a[28:31]\n\tds_read_b64_tr_b16 v[40:41], v2 offset:28672\n\tds_read_b64_tr_b16 v[42:43], v2 offset:29184\n\tv_mfma_f32_16x16x32_bf16 a[36:39], v[20:23], v[148:151], a[36:39]\n\tbuffer_load_dwordx4 v12, s[16:19], s69 offen lds\n\ts_mov_b32 s69, 1179648\n\ts_add_u32 m0, s54, 81920\n\tv_mfma_f32_16x16x32_bf16 a[44:47], v[20:23], v[152:155], a[44:47]\n\t" \
    "s_waitcnt lgkmcnt(8)\n\tv_mfma_f32_16x16x32_bf16 v[72:75], v[24:27], v[144:147], v[72:75]\n\tds_read_b64_tr_b16 v[44:45], v3 offset:28672\n\tds_read_b64_tr_b16 v[46:47], v3 offset:29184\n\tv_mfma_f32_16x16x32_bf16 v[80:83], v[24:27], v[148:151], v[80:83]\n\tbuffer_load_dwordx4 v13, s[16:19], s69 offen lds\n\ts_mov_b32 s69, 1048576\n\ts_add_u32 m0, s54, 20480\n\t" \
    "v_mfma_f32_16x16x32_bf16 v[88:91], v[24:27], v[152:155], v[88:91]\n\ts_waitcnt lgkmcnt(8)\n\tv_mfma_f32_16x16x32_bf16 v[76:79], v[28:31], v[144:147], v[76:79]\n\tds_read_b64_tr_b16 v[16:17], v0 offset:32768\n\tds_read_b64_tr_b16 v[18:19], v0 offset:33280\n\tv_mfma_f32_16x16x32_bf16 v[84:87], v[28:31], v[148:151], v[84:87]\n\tbuffer_load_dwordx4 v12, s[16:19], s69 offen lds\n\ts_mov_b32 s69, 1048576\n\t" \
    "s_add_u32 m0, s54, 86016\n\tv_mfma_f32_16x16x32_bf16 v[92:95], v[28:31], v[152:155], v[92:95]\n\ts_waitcnt lgkmcnt(8)\n\tv_mfma_f32_16x16x32_bf16 a[24:27], v[32:35], v[156:159], a[24:27]\n\tds_read_b64_tr_b16 v[20:21], v1 offset:32768\n\tds_read_b64_tr_b16 v[22:23], v1 offset:33280\n\tv_mfma_f32_16x16x32_bf16 a[32:35], v[32:35], v[160:163], a[32:35]\n\tbuffer_load_dwordx4 v13, s[16:19], s69 offen lds\n\t" \
    "v_mfma_f32_16x16x32_bf16 a[40:43], v[32:35], v[164:167], a[40:43]\n\ts_waitcnt lgkmcnt(8)\n\tv_mfma_f32_16x16x32_bf16 a[28:31], v[36:39], v[156:159], a[28:31]\n\tds_read_b64_tr_b16 v[24:25], v2 offset:32768\n\tds_read_b64_tr_b16 v[26:27], v2 offset:33280\n\tv_mfma_f32_16x16x32_bf16 a[36:39], v[36:39], v[160:163], a[36:39]\n\tv_mfma_f32_16x16x32_bf16 a[44:47], v[36:39], v[164:167], a[44:47]\n\ts_waitcnt lgkmcnt(8)\n\t" \
    "v_mfma_f32_16x16x32_bf16 v[72:75], v[40:43], v[156:159], v[72:75]\n\tds_read_b64_tr_b16 v[28:29], v3 offset:32768\n\tds_read_b64_tr_b16 v[30:31], v3 offset:33280\n\tv_mfma_f32_16x16x32_bf16 v[80:83], v[40:43], v[160:163], v[80:83]\n\tv_mfma_f32_16x16x32_bf16 v[88:91], v[40:43], v[164:167], v[88:91]\n\ts_waitcnt lgkmcnt(8)\n\tv_mfma_f32_16x16x32_bf16 v[76:79], v[44:47], v[156:159], v[76:79]\n\tds_read_b64_tr_b16 v[32:33], v0 offset:36864\n\t" \
    "ds_read_b64_tr_b16 v[34:35], v0 offset:37376\n\tv_mfma_f32_16x16x32_bf16 v[84:87], v[44:47], v[160:163], v[84:87]\n\tv_mfma_f32_16x16x32_bf16 v[92:95], v[44:47], v[164:167], v[92:95]\n\ts_barrier\n\ts_waitcnt lgkmcnt(8)\n\tv_mfma_f32_16x16x32_bf16 a[24:27], v[16:19], v[168:171], a[24:27]\n\tds_read_b64_tr_b16 v[36:37], v1 offset:36864\n\tds_read_b64_tr_b16 v[38:39], v1 offset:37376\n\t" \
    "s_mov_b32 s69, 1703936\n\tv_mfma_f32_16x16x32_bf16 a[32:35], v[16:19], v[172:175], a[32:35]\n\ts_add_u32 m0, s54, 24576\n\tv_mfma_f32_16x16x32_bf16 a[40:43], v[16:19], v[176:179], a[40:43]\n\ts_waitcnt lgkmcnt(8)\n\tv_mfma_f32_16x16x32_bf16 a[28:31], v[20:23], v[168:171], a[28:31]\n\tds_read_b64_tr_b16 v[40:41], v2 offset:36864\n\tds_read_b64_tr_b16 v[42:43], v2 offset:37376\n\t" \
    "v_mfma_f32_16x16x32_bf16 a[36:39], v[20:23], v[172:175], a[36:39]\n\tbuffer_load_dwordx4 v12, s[16:19], s69 offen lds\n\ts_mov_b32 s69, 1703936\n\ts_add_u32 m0, s54, 90112\n\tv_mfma_f32_16x16x32_bf16 a[44:47], v[20:23], v[176:179], a[44:47]\n\ts_waitcnt lgkmcnt(8)\n\tv_mfma_f32_16x16x32_bf16 v[72:75], v[24:27], v[168:171], v[72:75]\n\tds_read_b64_tr_b16 v[44:45], v3 offset:36864\n\t" \
    "ds_read_b64_tr_b16 v[46:47], v3 offset:37376\n\tv_mfma_f32_16x16x32_bf16 v[80:83], v[24:27], v[172:175], v[80:83]\n\tbuffer_load_dwordx4 v13, s[16:19], s69 offen lds\n\ts_mov_b32 s69, 1572864\n\ts_add_u32 m0, s54, 28672\n\tv_mfma_f32_16x16x32_bf16 v[88:91], v[24:27], v[176:179], v[88:91]\n\ts_waitcnt lgkmcnt(8)\n\tv_mfma_f32_16x16x32_bf16 v[76:79], v[28:31], v[168:171], v[76:79]\n\t" \
    "ds_read_b64_tr_b16 v[16:17], v0 offset:40960\n\tds_read_b64_tr_b16 v[18:19], v0 offset:41472\n\tv_mfma_f32_16x16x32_bf16 v[84:87], v[28:31], v[172:175], v[84:87]\n\tbuffer_load_dwordx4 v12, s[16:19], s69 offen lds\n\ts_mov_b32 s69, 1572864\n\ts_add_u32 m0, s54, 94208\n\tv_mfma_f32_16x16x32_bf16 v[92:95], v[28:31], v[176:179], v[92:95]\n\ts_waitcnt lgkmcnt(8)\n\t" \
    "v_mfma_f32_16x16x32_bf16 a[24:27], v[32:35], v[180:183], a[24:27]\n\tds_read_b64_tr_b16 v[20:21], v1 offset:40960\n\tds_read_b64_tr_b16 v[22:23], v1 offset:41472\n\tv_mfma_f32_16x16x32_bf16 a[32:35], v[32:35], v[184:187], a[32:35]\n\tbuffer_load_dwordx4 v13, s[16:19], s69 offen lds\n\tv_mfma_f32_16x16x32_bf16 a[40:43], v[32:35], v[188:191], a[40:43]\n\ts_waitcnt lgkmcnt(8)\n\tv_mfma_f32_16x16x32_bf16 a[28:31], v[36:39], v[180:183], a[28:31]\n\t" \
    "ds_read_b64_tr_b16 v[24:25], v2 offset:40960\n\tds_read_b64_tr_b16 v[26:27], v2 offset:41472\n\tv_mfma_f32_16x16x32_bf16 a[36:39], v[36:39], v[184:187], a[36:39]\n\tv_mfma_f32_16x16x32_bf16 a[44:47], v[36:39], v[188:191], a[44:47]\n\ts_waitcnt lgkmcnt(8)\n\tv_mfma_f32_16x16x32_bf16 v[72:75], v[40:43], v[180:183], v[72:75]\n\tds_read_b64_tr_b16 v[28:29], v3 offset:40960\n\tds_read_b64_tr_b16 v[30:31], v3 offset:41472\n\t" \
    "v_mfma_f32_16x16x32_bf16 v[80:83], v[40:43], v[184:187], v[80:83]\n\tv_mfma_f32_16x16x32_bf16 v[88:91], v[40:43], v[188:191], v[88:91]\n\ts_waitcnt lgkmcnt(8)\n\tv_mfma_f32_16x16x32_bf16 v[76:79], v[44:47], v[180:183], v[76:79]\n\tds_read_b64_tr_b16 v[32:33], v0 offset:45056\n\tds_read_b64_tr_b16 v[34:35], v0 offset:45568\n\tv_mfma_f32_16x16x32_bf16 v[84:87], v[44:47], v[184:187], v[84:87]\n\tv_mfma_f32_16x16x32_bf16 v[92:95], v[44:47], v[188:191], v[92:95]\n\t" \
    "s_waitcnt vmcnt(19)\n\ts_barrier\n\ts_waitcnt lgkmcnt(8)\n\tv_mfma_f32_16x16x32_bf16 a[24:27], v[16:19], v[192:195], a[24:27]\n\tds_read_b64_tr_b16 v[36:37], v1 offset:45056\n\tds_read_b64_tr_b16 v[38:39], v1 offset:45568\n\ts_mov_b32 s69, 2228224\n\tv_mfma_f32_16x16x32_bf16 a[32:35], v[16:19], v[196:199], a[32:35]\n\t" \
    "s_add_u32 m0, s54, 32768\n\tv_mfma_f32_16x16x32_bf16 a[40:43], v[16:19], v[200:203], a[40:43]\n\ts_waitcnt lgkmcnt(8)\n\tv_mfma_f32_16x16x32_bf16 a[28:31], v[20:23], v[192:195], a[28:31]\n\tds_read_b64_tr_b16 v[40:41], v2 offset:45056\n\tds_read_b64_tr_b16 v[42:43], v2 offset:45568\n\tv_mfma_f32_16x16x32_bf16 a[36:39], v[20:23], v[196:199], a[36:39]\n\tbuffer_load_dwordx4 v12, s[16:19], s69 offen lds\n\t" \
    "s_mov_b32 s69, 2228224\n\ts_add_u32 m0, s54, 98304\n\tv_mfma_f32_16x16x32_bf16 a[44:47], v[20:23], v[200:203], a[44:47]\n\ts_waitcnt lgkmcnt(8)\n\tv_mfma_f32_16x16x32_bf16 v[72:75], v[24:27], v[192:195], v[72:75]\n\tds_read_b64_tr_b16 v[44:45], v3 offset:45056\n\tds_read_b64_tr_b16 v[46:47], v3 offset:45568\n\tv_mfma_f32_16x16x32_bf16 v[80:83], v[24:27], v[196:199], v[80:83]\n\t" \
    "buffer_load_dwordx4 v13, s[16:19], s69 offen lds\n\ts_mov_b32 s69, 2097152\n\ts_add_u32 m0, s54, 36864\n\tv_mfma_f32_16x16x32_bf16 v[88:91], v[24:27], v[200:203], v[88:91]\n\ts_waitcnt lgkmcnt(8)\n\tv_mfma_f32_16x16x32_bf16 v[76:79], v[28:31], v[192:195], v[76:79]\n\tds_read_b64_tr_b16 v[16:17], v0 offset:49152\n\tds_read_b64_tr_b16 v[18:19], v0 offset:49664\n\t" \
    "v_mfma_f32_16x16x32_bf16 v[84:87], v[28:31], v[196:199], v[84:87]\n\tbuffer_load_dwordx4 v12, s[16:19], s69 offen lds\n\ts_mov_b32 s69, 2097152\n\ts_add_u32 m0, s54, 102400\n\tv_mfma_f32_16x16x32_bf16 v[92:95], v[28:31], v[200:203], v[92:95]\n\ts_waitcnt lgkmcnt(8)\n\tv_mfma_f32_16x16x32_bf16 a[24:27], v[32:35], v[204:207], a[24:27]\n\tds_read_b64_tr_b16 v[20:21], v1 offset:49152\n\t" \
    "ds_read_b64_tr_b16 v[22:23], v1 offset:49664\n\tv_mfma_f32_16x16x32_bf16 a[32:35], v[32:35], v[208:211], a[32:35]\n\tbuffer_load_dwordx4 v13, s[16:19], s69 offen lds\n\tv_mfma_f32_16x16x32_bf16 a[40:43], v[32:35], v[212:215], a[40:43]\n\ts_waitcnt lgkmcnt(8)\n\tv_mfma_f32_16x16x32_bf16 a[28:31], v[36:39], v[204:207], a[28:31]\n\tds_read_b64_tr_b16 v[24:25], v0 offset:53248\n\tds_read_b64_tr_b16 v[26:27], v0 offset:53760\n\t" \
    "v_mfma_f32_16x16x32_bf16 a[36:39], v[36:39], v[208:211], a[36:39]\n\tv_mfma_f32_16x16x32_bf16 a[44:47], v[36:39], v[212:215], a[44:47]\n\ts_waitcnt lgkmcnt(8)\n\tv_mfma_f32_16x16x32_bf16 v[72:75], v[40:43], v[204:207], v[72:75]\n\tds_read_b64_tr_b16 v[28:29], v1 offset:53248\n\tds_read_b64_tr_b16 v[30:31], v1 offset:53760\n\tv_mfma_f32_16x16x32_bf16 v[80:83], v[40:43], v[208:211], v[80:83]\n\tv_mfma_f32_16x16x32_bf16 v[88:91], v[40:43], v[212:215], v[88:91]\n\t" \
    "s_waitcnt lgkmcnt(8)\n\tv_mfma_f32_16x16x32_bf16 v[76:79], v[44:47], v[204:207], v[76:79]\n\tds_read_b64_tr_b16 v[32:33], v0 offset:57344\n\tds_read_b64_tr_b16 v[34:35], v0 offset:57856\n\tv_mfma_f32_16x16x32_bf16 v[84:87], v[44:47], v[208:211], v[84:87]\n\tv_mfma_f32_16x16x32_bf16 v[92:95], v[44:47], v[212:215], v[92:95]\n\ts_waitcnt vmcnt(21)\n\ts_barrier\n\t" \
    "s_waitcnt lgkmcnt(8)\n\tv_mfma_f32_16x16x32_bf16 a[24:27], v[16:19], v[216:219], a[24:27]\n\tds_read_b64_tr_b16 v[36:37], v1 offset:57344\n\tds_read_b64_tr_b16 v[38:39], v1 offset:57856\n\ts_mov_b32 s69, 2752512\n\tv_mfma_f32_16x16x32_bf16 a[32:35], v[16:19], a[60:63], a[32:35]\n\ts_add_u32 m0, s54, 40960\n\tv_mfma_f32_16x16x32_bf16 a[40:43], v[16:19], a[64:67], a[40:43]\n\t" \
    "s_waitcnt lgkmcnt(8)\n\tv_mfma_f32_16x16x32_bf16 a[28:31], v[20:23], v[216:219], a[28:31]\n\tds_read_b64_tr_b16 v[40:41], v0 offset:61440\n\tds_read_b64_tr_b16 v[42:43], v0 offset:61952\n\tv_mfma_f32_16x16x32_bf16 a[36:39], v[20:23], a[60:63], a[36:39]\n\tv_mfma_f32_16x16x32_bf16 a[44:47], v[20:23], a[64:67], a[44:47]\n\ts_waitcnt lgkmcnt(8)\n\tv_mfma_f32_16x16x32_bf16 a[24:27], v[24:27], a[68:71], a[24:27]\n\t" \
    "ds_read_b64_tr_b16 v[44:45], v1 offset:61440\n\tds_read_b64_tr_b16 v[46:47], v1 offset:61952\n\tv_mfma_f32_16x16x32_bf16 a[32:35], v[24:27], a[72:75], a[32:35]\n\tv_mfma_f32_16x16x32_bf16 a[40:43], v[24:27], a[76:79], a[40:43]\n\ts_waitcnt lgkmcnt(8)\n\tv_mfma_f32_16x16x32_bf16 a[28:31], v[28:31], a[68:71], a[28:31]\n\tds_read_b64_tr_b16 v[16:17], v2 offset:49152\n\tds_read_b64_tr_b16 v[18:19], v2 offset:49664\n\t" \
    "v_mfma_f32_16x16x32_bf16 a[36:39], v[28:31], a[72:75], a[36:39]\n\tv_mfma_f32_16x16x32_bf16 a[44:47], v[28:31], a[76:79], a[44:47]\n\tbuffer_load_dwordx2 v[250:251], v241, s[8:11], s50 offen sc1\n\ts_waitcnt lgkmcnt(8)\n\tv_mfma_f32_16x16x32_bf16 a[24:27], v[32:35], a[116:119], a[24:27]\n\tds_read_b64_tr_b16 v[20:21], v3 offset:49152\n\tds_read_b64_tr_b16 v[22:23], v3 offset:49664\n\tv_mfma_f32_16x16x32_bf16 a[32:35], v[32:35], a[120:123], a[32:35]\n\t" \
    "v_mfma_f32_16x16x32_bf16 a[40:43], v[32:35], a[124:127], a[40:43]\n\ts_waitcnt lgkmcnt(8)\n\tv_mfma_f32_16x16x32_bf16 a[28:31], v[36:39], a[116:119], a[28:31]\n\tds_read_b64_tr_b16 v[24:25], v2 offset:53248\n\tds_read_b64_tr_b16 v[26:27], v2 offset:53760\n\tv_mfma_f32_16x16x32_bf16 a[36:39], v[36:39], a[120:123], a[36:39]\n\tv_mfma_f32_16x16x32_bf16 a[44:47], v[36:39], a[124:127], a[44:47]\n\ts_waitcnt lgkmcnt(8)\n\t" \
    "v_mfma_f32_16x16x32_bf16 a[24:27], v[40:43], a[128:131], a[24:27]\n\tds_read_b64_tr_b16 v[28:29], v3 offset:53248\n\tds_read_b64_tr_b16 v[30:31], v3 offset:53760\n\tv_mfma_f32_16x16x32_bf16 a[32:35], v[40:43], a[132:135], a[32:35]\n\tv_mfma_f32_16x16x32_bf16 a[40:43], v[40:43], a[136:139], a[40:43]\n\ts_waitcnt lgkmcnt(8)\n\tv_mfma_f32_16x16x32_bf16 a[28:31], v[44:47], a[128:131], a[28:31]\n\tds_read_b64_tr_b16 v[32:33], v2 offset:57344\n\t" \
    "ds_read_b64_tr_b16 v[34:35], v2 offset:57856\n\tv_mfma_f32_16x16x32_bf16 a[36:39], v[44:47], a[132:135], a[36:39]\n\tv_mfma_f32_16x16x32_bf16 a[44:47], v[44:47], a[136:139], a[44:47]\n\ts_waitcnt vmcnt(21)\n\ts_barrier\n\ts_waitcnt lgkmcnt(8)\n\tv_mfma_f32_16x16x32_bf16 v[72:75], v[16:19], v[216:219], v[72:75]\n\tds_read_b64_tr_b16 v[36:37], v3 offset:57344\n\t" \
    "ds_read_b64_tr_b16 v[38:39], v3 offset:57856\n\tv_mfma_f32_16x16x32_bf16 v[80:83], v[16:19], a[60:63], v[80:83]\n\tv_mfma_f32_16x16x32_bf16 v[88:91], v[16:19], a[64:67], v[88:91]\n\tds_write_b128 v237, a[24:27] offset:0\n\ts_waitcnt lgkmcnt(9)\n\tv_mfma_f32_16x16x32_bf16 v[76:79], v[20:23], v[216:219], v[76:79]\n\tds_read_b64_tr_b16 v[40:41], v2 offset:61440\n\tds_read_b64_tr_b16 v[42:43], v2 offset:61952\n\t" \
    "v_mfma_f32_16x16x32_bf16 v[84:87], v[20:23], a[60:63], v[84:87]\n\tv_mfma_f32_16x16x32_bf16 v[92:95], v[20:23], a[64:67], v[92:95]\n\tds_write_b128 v237, a[32:35] offset:2048\n\ts_waitcnt lgkmcnt(10)\n\tv_mfma_f32_16x16x32_bf16 v[72:75], v[24:27], a[68:71], v[72:75]\n\tds_read_b64_tr_b16 v[44:45], v3 offset:61440\n\tds_read_b64_tr_b16 v[46:47], v3 offset:61952\n\tv_mfma_f32_16x16x32_bf16 v[80:83], v[24:27], a[72:75], v[80:83]\n\t" \
    "v_mfma_f32_16x16x32_bf16 v[88:91], v[24:27], a[76:79], v[88:91]\n\tds_write_b128 v237, a[40:43] offset:4096\n\ts_waitcnt lgkmcnt(11)\n\tv_mfma_f32_16x16x32_bf16 v[76:79], v[28:31], a[68:71], v[76:79]\n\tds_read_b64_tr_b16 v[16:17], v4 offset:0\n\tds_read_b64_tr_b16 v[18:19], v4 offset:2048\n\tv_mfma_f32_16x16x32_bf16 v[84:87], v[28:31], a[72:75], v[84:87]\n\tv_mfma_f32_16x16x32_bf16 v[92:95], v[28:31], a[76:79], v[92:95]\n\t" \
    "ds_write_b128 v237, a[28:31] offset:1024\n\tds_write_b128 v237, a[36:39] offset:3072\n\tds_write_b128 v237, a[44:47] offset:5120\n\ts_waitcnt vmcnt(20) lgkmcnt(0)\n\ts_barrier\n\tv_mfma_f32_16x16x32_bf16 v[72:75], v[32:35], a[116:119], v[72:75]\n\tds_read_b64_tr_b16 v[20:21], v5 offset:0\n\tds_read_b64_tr_b16 v[22:23], v5 offset:2048\n\t" \
    "v_mfma_f32_16x16x32_bf16 v[80:83], v[32:35], a[120:123], v[80:83]\n\tbuffer_load_dwordx4 v12, s[16:19], s69 offen lds\n\tds_read_b128 v[96:99], v238 offset:0\n\ts_mov_b32 s69, 2752512\n\tv_mfma_f32_16x16x32_bf16 v[88:91], v[32:35], a[124:127], v[88:91]\n\tds_read_b128 v[100:103], v238 offset:1024\n\tds_read_b128 v[104:107], v238 offset:2048\n\tv_mfma_f32_16x16x32_bf16 v[76:79], v[36:39], a[116:119], v[76:79]\n\t" \
    "ds_read_b64_tr_b16 v[24:25], v6 offset:0\n\tds_read_b64_tr_b16 v[26:27], v6 offset:2048\n\ts_add_u32 m0, s54, 106496\n\tv_mfma_f32_16x16x32_bf16 v[84:87], v[36:39], a[120:123], v[84:87]\n\tbuffer_load_dwordx4 v13, s[16:19], s69 offen lds\n\tds_read_b128 v[108:111], v238 offset:3072\n\ts_mov_b32 s69, 2621440\n\tv_mfma_f32_16x16x32_bf16 v[92:95], v[36:39], a[124:127], v[92:95]\n\t" \
    "ds_read_b128 v[112:115], v238 offset:4096\n\tds_read_b128 v[116:119], v238 offset:5120\n\tv_mfma_f32_16x16x32_bf16 v[72:75], v[40:43], a[128:131], v[72:75]\n\tds_read_b64_tr_b16 v[28:29], v7 offset:0\n\tds_read_b64_tr_b16 v[30:31], v7 offset:2048\n\ts_add_u32 m0, s54, 45056\n\tv_mfma_f32_16x16x32_bf16 v[80:83], v[40:43], a[132:135], v[80:83]\n\tbuffer_load_dwordx4 v12, s[16:19], s69 offen lds\n\t" \
    "s_mov_b32 s69, 2621440\n\ts_add_u32 m0, s54, 110592\n\tv_mfma_f32_16x16x32_bf16 v[88:91], v[40:43], a[136:139], v[88:91]\n\tv_mfma_f32_16x16x32_bf16 v[76:79], v[44:47], a[128:131], v[76:79]\n\tds_read_b64_tr_b16 v[32:33], v224 offset:0\n\tds_read_b64_tr_b16 v[34:35], v224 offset:2048\n\tv_mfma_f32_16x16x32_bf16 v[84:87], v[44:47], a[132:135], v[84:87]\n\tbuffer_load_dwordx4 v13, s[16:19], s69 offen lds\n\t" \
    "s_mov_b32 s69, 3276800\n\ts_add_u32 m0, s54, 49152\n\tv_mfma_f32_16x16x32_bf16 v[92:95], v[44:47], a[136:139], v[92:95]\n\tbuffer_load_dwordx4 v12, s[16:19], s69 offen lds\n\ts_cmp_lg_u32 s39, 2\n\ts_cbranch_scc1 107f\n\ts_waitcnt vmcnt(5)\n\tv_cmp_ne_u64 vcc, s[36:37], v[250:251]\n\t" \
    "s_cbranch_vccz 107f\n\ts_add_u32 s73, s45, 55296\n\ts_waitcnt lgkmcnt(10)\n\tv_pk_add_f32 v[72:73], v[72:73], v[96:97]\n\ts_mov_b32 s69, 3276800\n\ts_add_u32 m0, s54, 114688\n\tv_pk_add_f32 v[74:75], v[74:75], v[98:99]\n\tv_pk_add_f32 v[76:77], v[76:77], v[100:101]\n\t" \
    "v_pk_add_f32 v[78:79], v[78:79], v[102:103]\n\tv_pk_mul_f32 v[96:97], v[72:73], v[72:73]\n\tv_pk_mul_f32 v[98:99], v[74:75], v[74:75]\n\tv_pk_mul_f32 v[100:101], v[76:77], v[76:77]\n\tbuffer_load_dwordx4 v13, s[16:19], s69 offen lds\n\ts_mov_b32 s69, 3145728\n\ts_add_u32 m0, s54, 53248\n\tv_pk_mul_f32 v[102:103], v[78:79], v[78:79]\n\t" \
    "v_pk_fma_f32 v[96:97], v[96:97], v[246:247], v[246:247] op_sel:[0,0,1] op_sel_hi:[1,0,1]\n\tv_pk_fma_f32 v[98:99], v[98:99], v[246:247], v[246:247] op_sel:[0,0,1] op_sel_hi:[1,0,1]\n\tv_pk_fma_f32 v[100:101], v[100:101], v[246:247], v[246:247] op_sel:[0,0,1] op_sel_hi:[1,0,1]\n\tv_pk_fma_f32 v[102:103], v[102:103], v[246:247], v[246:247] op_sel:[0,0,1] op_sel_hi:[1,0,1]\n\tv_pk_mul_f32 v[96:97], v[96:97], v[72:73]\n\tv_pk_mul_f32 v[98:99], v[98:99], v[74:75]\n\tv_pk_mul_f32 v[100:101], v[100:101], v[76:77]\n\tv_pk_mul_f32 v[102:103], v[102:103], v[78:79]\n\t" \
    "s_waitcnt lgkmcnt(6)\n\tv_exp_f32 v96, v96\n\tv_pk_add_f32 v[80:81], v[80:81], v[104:105]\n\tv_exp_f32 v97, v97\n\tbuffer_load_dwordx4 v12, s[16:19], s69 offen lds\n\ts_mov_b32 s69, 3145728\n\ts_add_u32 m0, s54, 118784\n\tv_pk_add_f32 v[82:83], v[82:83], v[106:107]\n\t" \
    "v_exp_f32 v98, v98\n\tv_pk_add_f32 v[84:85], v[84:85], v[108:109]\n\tv_exp_f32 v99, v99\n\tv_pk_add_f32 v[86:87], v[86:87], v[110:111]\n\tv_exp_f32 v100, v100\n\tv_pk_mul_f32 v[104:105], v[80:81], v[80:81]\n\tv_exp_f32 v101, v101\n\tv_pk_mul_f32 v[106:107], v[82:83], v[82:83]\n\t" \
    "v_exp_f32 v102, v102\n\tbuffer_load_dwordx4 v13, s[16:19], s69 offen lds\n\tv_pk_mul_f32 v[108:109], v[84:85], v[84:85]\n\tv_exp_f32 v103, v103\n\tv_pk_mul_f32 v[110:111], v[86:87], v[86:87]\n\tv_pk_add_f32 v[96:97], v[96:97], 1.0 op_sel_hi:[1,0]\n\tv_pk_add_f32 v[98:99], v[98:99], 1.0 op_sel_hi:[1,0]\n\tv_pk_add_f32 v[100:101], v[100:101], 1.0 op_sel_hi:[1,0]\n\t" \
    "v_pk_add_f32 v[102:103], v[102:103], 1.0 op_sel_hi:[1,0]\n\tv_pk_fma_f32 v[104:105], v[104:105], v[246:247], v[246:247] op_sel:[0,0,1] op_sel_hi:[1,0,1]\n\tv_rcp_f32 v96, v96\n\tv_pk_fma_f32 v[106:107], v[106:107], v[246:247], v[246:247] op_sel:[0,0,1] op_sel_hi:[1,0,1]\n\tv_rcp_f32 v97, v97\n\tv_pk_fma_f32 v[108:109], v[108:109], v[246:247], v[246:247] op_sel:[0,0,1] op_sel_hi:[1,0,1]\n\tv_rcp_f32 v98, v98\n\tv_pk_fma_f32 v[110:111], v[110:111], v[246:247], v[246:247] op_sel:[0,0,1] op_sel_hi:[1,0,1]\n\t" \
    "v_rcp_f32 v99, v99\n\tv_pk_mul_f32 v[104:105], v[104:105], v[80:81]\n\tv_rcp_f32 v100, v100\n\tv_pk_mul_f32 v[106:107], v[106:107], v[82:83]\n\tv_rcp_f32 v101, v101\n\tv_pk_mul_f32 v[108:109], v[108:109], v[84:85]\n\tv_rcp_f32 v102, v102\n\tv_pk_mul_f32 v[110:111], v[110:111], v[86:87]\n\t" \
    "v_rcp_f32 v103, v103\n\ts_waitcnt lgkmcnt(4)\n\tv_exp_f32 v104, v104\n\tv_pk_mul_f32 v[96:97], v[96:97], v[72:73]\n\tv_exp_f32 v105, v105\n\tv_pk_mul_f32 v[98:99], v[98:99], v[74:75]\n\tv_exp_f32 v106, v106\n\tv_pk_mul_f32 v[100:101], v[100:101], v[76:77]\n\t" \
    "v_exp_f32 v107, v107\n\tv_pk_mul_f32 v[102:103], v[102:103], v[78:79]\n\tv_exp_f32 v108, v108\n\tv_cvt_pk_bf16_f32 v72, v96, v97\n\tv_exp_f32 v109, v109\n\tv_cvt_pk_bf16_f32 v73, v98, v99\n\tv_exp_f32 v110, v110\n\tv_cvt_pk_bf16_f32 v74, v100, v101\n\t" \
    "v_exp_f32 v111, v111\n\tv_cvt_pk_bf16_f32 v75, v102, v103\n\tv_pk_add_f32 v[104:105], v[104:105], 1.0 op_sel_hi:[1,0]\n\tv_pk_add_f32 v[106:107], v[106:107], 1.0 op_sel_hi:[1,0]\n\tv_pk_add_f32 v[108:109], v[108:109], 1.0 op_sel_hi:[1,0]\n\tv_pk_add_f32 v[110:111], v[110:111], 1.0 op_sel_hi:[1,0]\n\tv_pk_add_f32 v[88:89], v[88:89], v[112:113]\n\tv_rcp_f32 v104, v104\n\t" \
    "v_pk_add_f32 v[90:91], v[90:91], v[114:115]\n\tv_rcp_f32 v105, v105\n\tv_pk_add_f32 v[92:93], v[92:93], v[116:117]\n\tv_rcp_f32 v106, v106\n\tv_pk_add_f32 v[94:95], v[94:95], v[118:119]\n\tv_rcp_f32 v107, v107\n\tv_pk_mul_f32 v[112:113], v[88:89], v[88:89]\n\tv_rcp_f32 v108, v108\n\t" \
    "v_pk_mul_f32 v[114:115], v[90:91], v[90:91]\n\tv_rcp_f32 v109, v109\n\tv_pk_mul_f32 v[116:117], v[92:93], v[92:93]\n\tv_rcp_f32 v110, v110\n\tv_pk_mul_f32 v[118:119], v[94:95], v[94:95]\n\tv_rcp_f32 v111, v111\n\tv_pk_fma_f32 v[112:113], v[112:113], v[246:247], v[246:247] op_sel:[0,0,1] op_sel_hi:[1,0,1]\n\tv_pk_mul_f32 v[104:105], v[104:105], v[80:81]\n\t" \
    "v_pk_mul_f32 v[106:107], v[106:107], v[82:83]\n\tv_pk_mul_f32 v[108:109], v[108:109], v[84:85]\n\tv_pk_mul_f32 v[110:111], v[110:111], v[86:87]\n\tv_cvt_pk_bf16_f32 v80, v104, v105\n\tv_cvt_pk_bf16_f32 v81, v106, v107\n\tv_cvt_pk_bf16_f32 v82, v108, v109\n\tv_cvt_pk_bf16_f32 v83, v110, v111\n\tbuffer_load_dwordx2 v[250:251], v241, s[8:11], s50 offen sc1\n\t" \
    "v_pk_fma_f32 v[114:115], v[114:115], v[246:247], v[246:247] op_sel:[0,0,1] op_sel_hi:[1,0,1]\n\tv_pk_fma_f32 v[116:117], v[116:117], v[246:247], v[246:247] op_sel:[0,0,1] op_sel_hi:[1,0,1]\n\tv_pk_fma_f32 v[118:119], v[118:119], v[246:247], v[246:247] op_sel:[0,0,1] op_sel_hi:[1,0,1]\n\tv_pk_mul_f32 v[112:113], v[112:113], v[88:89]\n\tv_pk_mul_f32 v[114:115], v[114:115], v[90:91]\n\tv_pk_mul_f32 v[116:117], v[116:117], v[92:93]\n\tv_pk_mul_f32 v[118:119], v[118:119], v[94:95]\n\tv_exp_f32 v112, v112\n\t" \
    "v_exp_f32 v113, v113\n\tv_exp_f32 v114, v114\n\tv_exp_f32 v115, v115\n\tv_exp_f32 v116, v116\n\tv_exp_f32 v117, v117\n\tv_exp_f32 v118, v118\n\tv_exp_f32 v119, v119\n\tv_pk_add_f32 v[112:113], v[112:113], 1.0 op_sel_hi:[1,0]\n\t" \
    "v_pk_add_f32 v[114:115], v[114:115], 1.0 op_sel_hi:[1,0]\n\tv_pk_add_f32 v[116:117], v[116:117], 1.0 op_sel_hi:[1,0]\n\tv_pk_add_f32 v[118:119], v[118:119], 1.0 op_sel_hi:[1,0]\n\tv_rcp_f32 v112, v112\n\tv_rcp_f32 v113, v113\n\tv_rcp_f32 v114, v114\n\tv_rcp_f32 v115, v115\n\tv_rcp_f32 v116, v116\n\t" \
    "v_rcp_f32 v117, v117\n\tv_rcp_f32 v118, v118\n\tv_rcp_f32 v119, v119\n\tv_pk_mul_f32 v[112:113], v[112:113], v[88:89]\n\tv_pk_mul_f32 v[114:115], v[114:115], v[90:91]\n\tv_pk_mul_f32 v[116:117], v[116:117], v[92:93]\n\tv_pk_mul_f32 v[118:119], v[118:119], v[94:95]\n\tv_cvt_pk_bf16_f32 v88, v112, v113\n\t" \
    "v_cvt_pk_bf16_f32 v89, v114, v115\n\tv_cvt_pk_bf16_f32 v90, v116, v117\n\tv_cvt_pk_bf16_f32 v91, v118, v119\n\ts_waitcnt vmcnt(0)\n\tv_cmp_ne_u64 vcc, s[36:37], v[250:251]\n\ts_cbranch_vccz 110f\n\ts_mov_b32 s71, 1048576\n\t109:\n\t" \
    "s_sleep 1\n\tbuffer_load_dwordx2 v[250:251], v241, s[8:11], s50 offen sc1\n\ts_waitcnt vmcnt(0)\n\tv_cmp_ne_u64 vcc, s[36:37], v[250:251]\n\ts_cbranch_vccz 110f\n\ts_sub_u32 s71, s71, 1\n\ts_cbranch_scc0 109b\n\t110:\n\t" \
    "s_add_u32 s68, s46, 0\n\tbuffer_load_dwordx4 v[192:195], v8, s[8:11], s68 offen offset:0 sc1\n\tbuffer_load_dwordx4 v[196:199], v8, s[8:11], s68 offen offset:1024 sc1\n\tbuffer_load_dwordx4 v[200:203], v8, s[8:11], s68 offen offset:2048 sc1\n\ts_add_u32 s68, s46, 3072\n\tbuffer_load_dwordx4 v[204:207], v8, s[8:11], s68 offen offset:0 sc1\n\tbuffer_load_dwordx4 v[208:211], v8, s[8:11], s68 offen offset:1024 sc1\n\tbuffer_load_dwordx4 v[212:215], v8, s[8:11], s68 offen offset:2048 sc1\n\t" \
    "s_add_u32 s68, s46, 6144\n\tbuffer_load_dwordx4 v[216:219], v8, s[8:11], s68 offen offset:0 sc1\n\tbuffer_load_dwordx4 a[48:51], v8, s[8:11], s68 offen offset:1024 sc1\n\tbuffer_load_dwordx4 a[52:55], v8, s[8:11], s68 offen offset:2048 sc1\n\ts_add_u32 s68, s46, 9216\n\tbuffer_load_dwordx4 a[56:59], v8, s[8:11], s68 offen offset:0 sc1\n\tbuffer_load_dwordx4 a[60:63], v8, s[8:11], s68 offen offset:1024 sc1\n\tbuffer_load_dwordx4 a[64:67], v8, s[8:11], s68 offen offset:2048 sc1\n\t" \
    "s_add_u32 s68, s46, 12288\n\tbuffer_load_dwordx4 a[68:71], v8, s[8:11], s68 offen offset:0 sc1\n\tbuffer_load_dwordx4 a[72:75], v8, s[8:11], s68 offen offset:1024 sc1\n\tbuffer_load_dwordx4 a[76:79], v8, s[8:11], s68 offen offset:2048 sc1\n\tbuffer_store_dwordx4 v[72:75], v8, s[8:11], s73 offen offset:0 sc1\n\tbuffer_store_dwordx4 v[80:83], v8, s[8:11], s73 offen offset:1024 sc1\n\tbuffer_store_dwordx4 v[88:91], v8, s[8:11], s73 offen offset:2048 sc1\n\ts_branch 108f\n\t" \
    "107:\n\ts_add_u32 s68, s46, 0\n\tbuffer_load_dwordx4 v[192:195], v8, s[8:11], s68 offen offset:0 sc1\n\tbuffer_load_dwordx4 v[196:199], v8, s[8:11], s68 offen offset:1024 sc1\n\tbuffer_load_dwordx4 v[200:203], v8, s[8:11], s68 offen offset:2048 sc1\n\ts_add_u32 s73, s45, 55296\n\ts_waitcnt lgkmcnt(10)\n\tv_pk_add_f32 v[72:73], v[72:73], v[96:97]\n\t" \
    "s_add_u32 s68, s46, 3072\n\tv_pk_add_f32 v[74:75], v[74:75], v[98:99]\n\tv_pk_add_f32 v[76:77], v[76:77], v[100:101]\n\tv_pk_add_f32 v[78:79], v[78:79], v[102:103]\n\tv_pk_mul_f32 v[96:97], v[72:73], v[72:73]\n\tv_pk_mul_f32 v[98:99], v[74:75], v[74:75]\n\tv_pk_mul_f32 v[100:101], v[76:77], v[76:77]\n\tv_pk_mul_f32 v[102:103], v[78:79], v[78:79]\n\t" \
    "v_pk_fma_f32 v[96:97], v[96:97], v[246:247], v[246:247] op_sel:[0,0,1] op_sel_hi:[1,0,1]\n\tv_pk_fma_f32 v[98:99], v[98:99], v[246:247], v[246:247] op_sel:[0,0,1] op_sel_hi:[1,0,1]\n\tv_pk_fma_f32 v[100:101], v[100:101], v[246:247], v[246:247] op_sel:[0,0,1] op_sel_hi:[1,0,1]\n\tv_pk_fma_f32 v[102:103], v[102:103], v[246:247], v[246:247] op_sel:[0,0,1] op_sel_hi:[1,0,1]\n\tv_pk_mul_f32 v[96:97], v[96:97], v[72:73]\n\tbuffer_load_dwordx4 v[204:207], v8, s[8:11], s68 offen offset:0 sc1\n\tv_pk_mul_f32 v[98:99], v[98:99], v[74:75]\n\tv_pk_mul_f32 v[100:101], v[100:101], v[76:77]\n\t" \
    "v_pk_mul_f32 v[102:103], v[102:103], v[78:79]\n\ts_waitcnt lgkmcnt(6)\n\tv_exp_f32 v96, v96\n\tv_pk_add_f32 v[80:81], v[80:81], v[104:105]\n\tv_exp_f32 v97, v97\n\tv_pk_add_f32 v[82:83], v[82:83], v[106:107]\n\tv_exp_f32 v98, v98\n\tv_pk_add_f32 v[84:85], v[84:85], v[108:109]\n\t" \
    "v_exp_f32 v99, v99\n\tv_pk_add_f32 v[86:87], v[86:87], v[110:111]\n\tbuffer_load_dwordx4 v[208:211], v8, s[8:11], s68 offen offset:1024 sc1\n\tv_exp_f32 v100, v100\n\tv_pk_mul_f32 v[104:105], v[80:81], v[80:81]\n\tv_exp_f32 v101, v101\n\tv_pk_mul_f32 v[106:107], v[82:83], v[82:83]\n\tv_exp_f32 v102, v102\n\t" \
    "v_pk_mul_f32 v[108:109], v[84:85], v[84:85]\n\tv_exp_f32 v103, v103\n\tv_pk_mul_f32 v[110:111], v[86:87], v[86:87]\n\tv_pk_add_f32 v[96:97], v[96:97], 1.0 op_sel_hi:[1,0]\n\tv_pk_add_f32 v[98:99], v[98:99], 1.0 op_sel_hi:[1,0]\n\tv_pk_add_f32 v[100:101], v[100:101], 1.0 op_sel_hi:[1,0]\n\tv_pk_add_f32 v[102:103], v[102:103], 1.0 op_sel_hi:[1,0]\n\tbuffer_load_dwordx4 v[212:215], v8, s[8:11], s68 offen offset:2048 sc1\n\t" \
    "s_add_u32 s68, s46, 6144\n\tv_pk_fma_f32 v[104:105], v[104:105], v[246:247], v[246:247] op_sel:[0,0,1] op_sel_hi:[1,0,1]\n\tv_rcp_f32 v96, v96\n\tv_pk_fma_f32 v[106:107], v[106:107], v[246:247], v[246:247] op_sel:[0,0,1] op_sel_hi:[1,0,1]\n\tv_rcp_f32 v97, v97\n\tv_pk_fma_f32 v[108:109], v[108:109], v[246:247], v[246:247] op_sel:[0,0,1] op_sel_hi:[1,0,1]\n\tv_rcp_f32 v98, v98\n\tv_pk_fma_f32 v[110:111], v[110:111], v[246:247], v[246:247] op_sel:[0,0,1] op_sel_hi:[1,0,1]\n\t" \
    "v_rcp_f32 v99, v99\n\tv_pk_mul_f32 v[104:105], v[104:105], v[80:81]\n\tv_rcp_f32 v100, v100\n\tbuffer_load_dwordx4 v[216:219], v8, s[8:11], s68 offen offset:0 sc1\n\tv_pk_mul_f32 v[106:107], v[106:107], v[82:83]\n\tv_rcp_f32 v101, v101\n\tv_pk_mul_f32 v[108:109], v[108:109], v[84:85]\n\tv_rcp_f32 v102, v102\n\t" \
    "v_pk_mul_f32 v[110:111], v[110:111], v[86:87]\n\tv_rcp_f32 v103, v103\n\ts_waitcnt lgkmcnt(4)\n\tv_exp_f32 v104, v104\n\tv_pk_mul_f32 v[96:97], v[96:97], v[72:73]\n\tv_exp_f32 v105, v105\n\tv_pk_mul_f32 v[98:99], v[98:99], v[74:75]\n\tbuffer_load_dwordx4 a[48:51], v8, s[8:11], s68 offen offset:1024 sc1\n\t" \
    "v_exp_f32 v106, v106\n\tv_pk_mul_f32 v[100:101], v[100:101], v[76:77]\n\tv_exp_f32 v107, v107\n\tv_pk_mul_f32 v[102:103], v[102:103], v[78:79]\n\tv_exp_f32 v108, v108\n\tv_cvt_pk_bf16_f32 v72, v96, v97\n\tv_exp_f32 v109, v109\n\tv_cvt_pk_bf16_f32 v73, v98, v99\n\t" \
    "v_exp_f32 v110, v110\n\tv_cvt_pk_bf16_f32 v74, v100, v101\n\tv_exp_f32 v111, v111\n\tbuffer_load_dwordx4 a[52:55], v8, s[8:11], s68 offen offset:2048 sc1\n\ts_add_u32 s68, s46, 9216\n\tv_cvt_pk_bf16_f32 v75, v102, v103\n\tv_pk_add_f32 v[104:105], v[104:105], 1.0 op_sel_hi:[1,0]\n\tv_pk_add_f32 v[106:107], v[106:107], 1.0 op_sel_hi:[1,0]\n\t" \
    "v_pk_add_f32 v[108:109], v[108:109], 1.0 op_sel_hi:[1,0]\n\tv_pk_add_f32 v[110:111], v[110:111], 1.0 op_sel_hi:[1,0]\n\tv_pk_add_f32 v[88:89], v[88:89], v[112:113]\n\tv_rcp_f32 v104, v104\n\tv_pk_add_f32 v[90:91], v[90:91], v[114:115]\n\tv_rcp_f32 v105, v105\n\tv_pk_add_f32 v[92:93], v[92:93], v[116:117]\n\tv_rcp_f32 v106, v106\n\t" \
    "v_pk_add_f32 v[94:95], v[94:95], v[118:119]\n\tbuffer_load_dwordx4 a[56:59], v8, s[8:11], s68 offen offset:0 sc1\n\tv_rcp_f32 v107, v107\n\tv_pk_mul_f32 v[112:113], v[88:89], v[88:89]\n\tv_rcp_f32 v108, v108\n\tv_pk_mul_f32 v[114:115], v[90:91], v[90:91]\n\tv_rcp_f32 v109, v109\n\tv_pk_mul_f32 v[116:117], v[92:93], v[92:93]\n\t" \
    "v_rcp_f32 v110, v110\n\tv_pk_mul_f32 v[118:119], v[94:95], v[94:95]\n\tv_rcp_f32 v111, v111\n\tv_pk_fma_f32 v[112:113], v[112:113], v[246:247], v[246:247] op_sel:[0,0,1] op_sel_hi:[1,0,1]\n\tv_pk_mul_f32 v[104:105], v[104:105], v[80:81]\n\tbuffer_load_dwordx4 a[60:63], v8, s[8:11], s68 offen offset:1024 sc1\n\tv_pk_mul_f32 v[106:107], v[106:107], v[82:83]\n\tv_pk_mul_f32 v[108:109], v[108:109], v[84:85]\n\t" \
    "v_pk_mul_f32 v[110:111], v[110:111], v[86:87]\n\tv_cvt_pk_bf16_f32 v80, v104, v105\n\tv_cvt_pk_bf16_f32 v81, v106, v107\n\tv_cvt_pk_bf16_f32 v82, v108, v109\n\tv_cvt_pk_bf16_f32 v83, v110, v111\n\tv_pk_fma_f32 v[114:115], v[114:115], v[246:247], v[246:247] op_sel:[0,0,1] op_sel_hi:[1,0,1]\n\tv_pk_fma_f32 v[116:117], v[116:117], v[246:247], v[246:247] op_sel:[0,0,1] op_sel_hi:[1,0,1]\n\tv_pk_fma_f32 v[118:119], v[118:119], v[246:247], v[246:247] op_sel:[0,0,1] op_sel_hi:[1,0,1]\n\t" \
    "v_pk_mul_f32 v[112:113], v[112:113], v[88:89]\n\tv_pk_mul_f32 v[114:115], v[114:115], v[90:91]\n\tv_pk_mul_f32 v[116:117], v[116:117], v[92:93]\n\tv_pk_mul_f32 v[118:119], v[118:119], v[94:95]\n\tv_exp_f32 v112, v112\n\tbuffer_load_dwordx4 a[64:67], v8, s[8:11], s68 offen offset:2048 sc1\n\ts_add_u32 s68, s46, 12288\n\tv_exp_f32 v113, v113\n\t" \
    "v_exp_f32 v114, v114\n\tv_exp_f32 v115, v115\n\tv_exp_f32 v116, v116\n\tv_exp_f32 v117, v117\n\tv_exp_f32 v118, v118\n\tv_exp_f32 v119, v119\n\tv_pk_add_f32 v[112:113], v[112:113], 1.0 op_sel_hi:[1,0]\n\tbuffer_load_dwordx4 a[68:71], v8, s[8:11], s68 offen offset:0 sc1\n\t" \
    "v_pk_add_f32 v[114:115], v[114:115], 1.0 op_sel_hi:[1,0]\n\tv_pk_add_f32 v[116:117], v[116:117], 1.0 op_sel_hi:[1,0]\n\tv_pk_add_f32 v[118:119], v[118:119], 1.0 op_sel_hi:[1,0]\n\tv_rcp_f32 v112, v112\n\tv_rcp_f32 v113, v113\n\tv_rcp_f32 v114, v114\n\tv_rcp_f32 v115, v115\n\tv_rcp_f32 v116, v116\n\t" \
    "v_rcp_f32 v117, v117\n\tv_rcp_f32 v118, v118\n\tbuffer_load_dwordx4 a[72:75], v8, s[8:11], s68 offen offset:1024 sc1\n\tv_rcp_f32 v119, v119\n\tv_pk_mul_f32 v[112:113], v[112:113], v[88:89]\n\tv_pk_mul_f32 v[114:115], v[114:115], v[90:91]\n\tv_pk_mul_f32 v[116:117], v[116:117], v[92:93]\n\tv_pk_mul_f32 v[118:119], v[118:119], v[94:95]\n\t" \
    "v_cvt_pk_bf16_f32 v88, v112, v113\n\tv_cvt_pk_bf16_f32 v89, v114, v115\n\tv_cvt_pk_bf16_f32 v90, v116, v117\n\tv_cvt_pk_bf16_f32 v91, v118, v119\n\tbuffer_load_dwordx4 a[76:79], v8, s[8:11], s68 offen offset:2048 sc1\n\ts_mov_b32 s69, 3276800\n\ts_add_u32 m0, s54, 114688\n\ts_nop 0\n\t" \
    "buffer_load_dwordx4 v13, s[16:19], s69 offen lds\n\ts_mov_b32 s69, 3145728\n\ts_add_u32 m0, s54, 53248\n\ts_nop 0\n\tbuffer_load_dwordx4 v12, s[16:19], s69 offen lds\n\ts_mov_b32 s69, 3145728\n\ts_add_u32 m0, s54, 118784\n\ts_nop 0\n\t" \
    "buffer_load_dwordx4 v13, s[16:19], s69 offen lds\n\tbuffer_store_dwordx4 v[72:75], v8, s[8:11], s73 offen offset:0 sc1\n\tbuffer_store_dwordx4 v[80:83], v8, s[8:11], s73 offen offset:1024 sc1\n\tbuffer_store_dwordx4 v[88:91], v8, s[8:11], s73 offen offset:2048 sc1\n\ts_cmp_lg_u32 s39, 2\n\ts_cbranch_scc1 102f\n\t108:\n\ts_waitcnt vmcnt(15)\n\t" \
    "s_barrier\n\tv_mfma_f32_16x16x32_bf16 v[120:123], v[192:195], v[16:19], 0\n\tds_read_b64_tr_b16 v[36:37], v225 offset:0\n\tds_read_b64_tr_b16 v[38:39], v225 offset:2048\n\ts_add_u32 s68, s46, 15360\n\tv_mfma_f32_16x16x32_bf16 v[128:131], v[196:199], v[16:19], 0\n\tv_mfma_f32_16x16x32_bf16 v[136:139], v[200:203], v[16:19], 0\n\tv_mfma_f32_16x16x32_bf16 v[124:127], v[192:195], v[20:23], 0\n\t" \
    "ds_read_b64_tr_b16 v[40:41], v226 offset:0\n\tds_read_b64_tr_b16 v[42:43], v226 offset:2048\n\tv_mfma_f32_16x16x32_bf16 v[132:135], v[196:199], v[20:23], 0\n\tv_mfma_f32_16x16x32_bf16 v[140:143], v[200:203], v[20:23], 0\n\tv_mfma_f32_16x16x32_bf16 v[144:147], v[192:195], v[24:27], 0\n\tds_read_b64_tr_b16 v[44:45], v227 offset:0\n\tds_read_b64_tr_b16 v[46:47], v227 offset:2048\n\tv_mfma_f32_16x16x32_bf16 v[152:155], v[196:199], v[24:27], 0\n\t" \
    "v_mfma_f32_16x16x32_bf16 v[160:163], v[200:203], v[24:27], 0\n\ts_waitcnt lgkmcnt(8)\n\tv_mfma_f32_16x16x32_bf16 v[148:151], v[192:195], v[28:31], 0\n\tds_read_b64_tr_b16 v[16:17], v4 offset:4096\n\tds_read_b64_tr_b16 v[18:19], v4 offset:6144\n\tv_mfma_f32_16x16x32_bf16 v[156:159], v[196:199], v[28:31], 0\n\tv_mfma_f32_16x16x32_bf16 v[164:167], v[200:203], v[28:31], 0\n\ts_waitcnt lgkmcnt(8)\n\t" \
    "v_mfma_f32_16x16x32_bf16 v[168:171], v[192:195], v[32:35], 0\n\tds_read_b64_tr_b16 v[20:21], v5 offset:4096\n\tds_read_b64_tr_b16 v[22:23], v5 offset:6144\n\tv_mfma_f32_16x16x32_bf16 v[176:179], v[196:199], v[32:35], 0\n\tv_mfma_f32_16x16x32_bf16 v[184:187], v[200:203], v[32:35], 0\n\ts_waitcnt lgkmcnt(8)\n\tv_mfma_f32_16x16x32_bf16 v[172:175], v[192:195], v[36:39], 0\n\tds_read_b64_tr_b16 v[24:25], v6 offset:4096\n\t" \
    "ds_read_b64_tr_b16 v[26:27], v6 offset:6144\n\tv_mfma_f32_16x16x32_bf16 v[180:183], v[196:199], v[36:39], 0\n\tv_mfma_f32_16x16x32_bf16 v[188:191], v[200:203], v[36:39], 0\n\ts_waitcnt lgkmcnt(8)\n\tv_mfma_f32_16x16x32_bf16 v[48:51], v[192:195], v[40:43], v[228:231]\n\tds_read_b64_tr_b16 v[28:29], v7 offset:4096\n\tds_read_b64_tr_b16 v[30:31], v7 offset:6144\n\tv_mfma_f32_16x16x32_bf16 v[56:59], v[196:199], v[40:43], v[228:231]\n\t" \
    "v_mfma_f32_16x16x32_bf16 v[64:67], v[200:203], v[40:43], v[228:231]\n\ts_waitcnt lgkmcnt(8)\n\tv_mfma_f32_16x16x32_bf16 v[52:55], v[192:195], v[44:47], v[232:235]\n\tds_read_b64_tr_b16 v[32:33], v224 offset:4096\n\tds_read_b64_tr_b16 v[34:35], v224 offset:6144\n\tv_mfma_f32_16x16x32_bf16 v[60:63], v[196:199], v[44:47], v[232:235]\n\tv_mfma_f32_16x16x32_bf16 v[68:71], v[200:203], v[44:47], v[232:235]\n\ts_waitcnt vmcnt(12) lgkmcnt(8)\n\t" \
    "v_mfma_f32_16x16x32_bf16 v[120:123], v[204:207], v[16:19], v[120:123]\n\tds_read_b64_tr_b16 v[36:37], v225 offset:4096\n\tds_read_b64_tr_b16 v[38:39], v225 offset:6144\n\tv_mfma_f32_16x16x32_bf16 v[128:131], v[208:211], v[16:19], v[128:131]\n\tv_mfma_f32_16x16x32_bf16 v[136:139], v[212:215], v[16:19], v[136:139]\n\ts_waitcnt lgkmcnt(8)\n\tv_mfma_f32_16x16x32_bf16 v[124:127], v[204:207], v[20:23], v[124:127]\n\tds_read_b64_tr_b16 v[40:41], v226 offset:4096\n\t" \
    "ds_read_b64_tr_b16 v[42:43], v226 offset:6144\n\tv_mfma_f32_16x16x32_bf16 v[132:135], v[208:211], v[20:23], v[132:135]\n\tbuffer_load_dwordx4 v[72:75], v8, s[8:11], s68 offen offset:0 sc1\n\tv_mfma_f32_16x16x32_bf16 v[140:143], v[212:215], v[20:23], v[140:143]\n\ts_waitcnt lgkmcnt(8)\n\tv_mfma_f32_16x16x32_bf16 v[144:147], v[204:207], v[24:27], v[144:147]\n\tds_read_b64_tr_b16 v[44:45], v227 offset:4096\n\tds_read_b64_tr_b16 v[46:47], v227 offset:6144\n\t" \
    "v_mfma_f32_16x16x32_bf16 v[152:155], v[208:211], v[24:27], v[152:155]\n\tbuffer_load_dwordx4 v[76:79], v8, s[8:11], s68 offen offset:1024 sc1\n\tv_mfma_f32_16x16x32_bf16 v[160:163], v[212:215], v[24:27], v[160:163]\n\ts_waitcnt lgkmcnt(8)\n\tv_mfma_f32_16x16x32_bf16 v[148:151], v[204:207], v[28:31], v[148:151]\n\tds_read_b64_tr_b16 v[16:17], v4 offset:8192\n\tds_read_b64_tr_b16 v[18:19], v4 offset:10240\n\tv_mfma_f32_16x16x32_bf16 v[156:159], v[208:211], v[28:31], v[156:159]\n\t" \
    "v_mfma_f32_16x16x32_bf16 v[164:167], v[212:215], v[28:31], v[164:167]\n\ts_waitcnt lgkmcnt(8)\n\tv_mfma_f32_16x16x32_bf16 v[168:171], v[204:207], v[32:35], v[168:171]\n\tds_read_b64_tr_b16 v[20:21], v5 offset:8192\n\tds_read_b64_tr_b16 v[22:23], v5 offset:10240\n\tv_mfma_f32_16x16x32_bf16 v[176:179], v[208:211], v[32:35], v[176:179]\n\tbuffer_load_dwordx4 v[80:83], v8, s[8:11], s68 offen offset:2048 sc1\n\ts_add_u32 s68, s46, 18432\n\t" \
    "v_mfma_f32_16x16x32_bf16 v[184:187], v[212:215], v[32:35], v[184:187]\n\ts_waitcnt lgkmcnt(8)\n\tv_mfma_f32_16x16x32_bf16 v[172:175], v[204:207], v[36:39], v[172:175]\n\tds_read_b64_tr_b16 v[24:25], v6 offset:8192\n\tds_read_b64_tr_b16 v[26:27], v6 offset:10240\n\tv_mfma_f32_16x16x32_bf16 v[180:183], v[208:211], v[36:39], v[180:183]\n\tbuffer_load_dwordx4 v[192:195], v8, s[8:11], s68 offen offset:0 sc1\n\tv_mfma_f32_16x16x32_bf16 v[188:191], v[212:215], v[36:39], v[188:191]\n\t" \
    "s_waitcnt lgkmcnt(8)\n\tv_mfma_f32_16x16x32_bf16 v[48:51], v[204:207], v[40:43], v[48:51]\n\tds_read_b64_tr_b16 v[28:29], v7 offset:8192\n\tds_read_b64_tr_b16 v[30:31], v7 offset:10240\n\tv_mfma_f32_16x16x32_bf16 v[56:59], v[208:211], v[40:43], v[56:59]\n\tv_mfma_f32_16x16x32_bf16 v[64:67], v[212:215], v[40:43], v[64:67]\n\ts_waitcnt lgkmcnt(8)\n\tv_mfma_f32_16x16x32_bf16 v[52:55], v[204:207], v[44:47], v[52:55]\n\t" \
    "ds_read_b64_tr_b16 v[32:33], v224 offset:8192\n\tds_read_b64_tr_b16 v[34:35], v224 offset:10240\n\tv_mfma_f32_16x16x32_bf16 v[60:63], v[208:211], v[44:47], v[60:63]\n\tbuffer_load_dwordx4 v[196:199], v8, s[8:11], s68 offen offset:1024 sc1\n\tv_mfma_f32_16x16x32_bf16 v[68:71], v[212:215], v[44:47], v[68:71]\n\ts_barrier\n\ts_waitcnt vmcnt(14) lgkmcnt(8)\n\tv_mfma_f32_16x16x32_bf16 v[120:123], v[216:219], v[16:19], v[120:123]\n\t" \
    "ds_read_b64_tr_b16 v[36:37], v225 offset:8192\n\tds_read_b64_tr_b16 v[38:39], v225 offset:10240\n\tv_mfma_f32_16x16x32_bf16 v[128:131], a[48:51], v[16:19], v[128:131]\n\tbuffer_load_dwordx4 v[200:203], v8, s[8:11], s68 offen offset:2048 sc1\n\ts_add_u32 s68, s46, 21504\n\tv_mfma_f32_16x16x32_bf16 v[136:139], a[52:55], v[16:19], v[136:139]\n\ts_waitcnt lgkmcnt(8)\n\tv_mfma_f32_16x16x32_bf16 v[124:127], v[216:219], v[20:23], v[124:127]\n\t" \
    "ds_read_b64_tr_b16 v[40:41], v226 offset:8192\n\tds_read_b64_tr_b16 v[42:43], v226 offset:10240\n\tv_mfma_f32_16x16x32_bf16 v[132:135], a[48:51], v[20:23], v[132:135]\n\tbuffer_load_dwordx4 v[204:207], v8, s[8:11], s68 offen offset:0 sc1\n\tv_mfma_f32_16x16x32_bf16 v[140:143], a[52:55], v[20:23], v[140:143]\n\ts_waitcnt lgkmcnt(8)\n\tv_mfma_f32_16x16x32_bf16 v[144:147], v[216:219], v[24:27], v[144:147]\n\tds_read_b64_tr_b16 v[44:45], v227 offset:8192\n\t" \
    "ds_read_b64_tr_b16 v[46:47], v227 offset:10240\n\tv_mfma_f32_16x16x32_bf16 v[152:155], a[48:51], v[24:27], v[152:155]\n\tbuffer_load_dwordx4 v[208:211], v8, s[8:11], s68 offen offset:1024 sc1\n\tv_mfma_f32_16x16x32_bf16 v[160:163], a[52:55], v[24:27], v[160:163]\n\ts_waitcnt lgkmcnt(8)\n\tv_mfma_f32_16x16x32_bf16 v[148:151], v[216:219], v[28:31], v[148:151]\n\tds_read_b64_tr_b16 v[16:17], v4 offset:12288\n\tds_read_b64_tr_b16 v[18:19], v4 offset:14336\n\t" \
    "v_mfma_f32_16x16x32_bf16 v[156:159], a[48:51], v[28:31], v[156:159]\n\tbuffer_load_dwordx4 v[212:215], v8, s[8:11], s68 offen offset:2048 sc1\n\ts_add_u32 s68, s46, 24576\n\tv_mfma_f32_16x16x32_bf16 v[164:167], a[52:55], v[28:31], v[164:167]\n\ts_waitcnt lgkmcnt(8)\n\tv_mfma_f32_16x16x32_bf16 v[168:171], v[216:219], v[32:35], v[168:171]\n\tds_read_b64_tr_b16 v[20:21], v5 offset:12288\n\tds_read_b64_tr_b16 v[22:23], v5 offset:14336\n\t" \
    "v_mfma_f32_16x16x32_bf16 v[176:179], a[48:51], v[32:35], v[176:179]\n\tv_mfma_f32_16x16x32_bf16 v[184:187], a[52:55], v[32:35], v[184:187]\n\ts_waitcnt lgkmcnt(8)\n\tv_mfma_f32_16x16x32_bf16 v[172:175], v[216:219], v[36:39], v[172:175]\n\tds_read_b64_tr_b16 v[24:25], v6 offset:12288\n\tds_read_b64_tr_b16 v[26:27], v6 offset:14336\n\tv_mfma_f32_16x16x32_bf16 v[180:183], a[48:51], v[36:39], v[180:183]\n\tbuffer_load_dwordx4 v[84:87], v8, s[8:11], s68 offen offset:0 sc1\n\t" \
    "v_mfma_f32_16x16x32_bf16 v[188:191], a[52:55], v[36:39], v[188:191]\n\ts_waitcnt lgkmcnt(8)\n\tv_mfma_f32_16x16x32_bf16 v[48:51], v[216:219], v[40:43], v[48:51]\n\tds_read_b64_tr_b16 v[28:29], v7 offset:12288\n\tds_read_b64_tr_b16 v[30:31], v7 offset:14336\n\tv_mfma_f32_16x16x32_bf16 v[56:59], a[48:51], v[40:43], v[56:59]\n\tbuffer_load_dwordx4 v[88:91], v8, s[8:11], s68 offen offset:1024 sc1\n\tv_mfma_f32_16x16x32_bf16 v[64:67], a[52:55], v[40:43], v[64:67]\n\t" \
    "s_waitcnt lgkmcnt(8)\n\tv_mfma_f32_16x16x32_bf16 v[52:55], v[216:219], v[44:47], v[52:55]\n\tds_read_b64_tr_b16 v[32:33], v224 offset:12288\n\tds_read_b64_tr_b16 v[34:35], v224 offset:14336\n\tv_mfma_f32_16x16x32_bf16 v[60:63], a[48:51], v[44:47], v[60:63]\n\tv_mfma_f32_16x16x32_bf16 v[68:71], a[52:55], v[44:47], v[68:71]\n\ts_waitcnt vmcnt(17) lgkmcnt(8)\n\tv_mfma_f32_16x16x32_bf16 v[120:123], a[56:59], v[16:19], v[120:123]\n\t" \
    "ds_read_b64_tr_b16 v[36:37], v225 offset:12288\n\tds_read_b64_tr_b16 v[38:39], v225 offset:14336\n\tv_mfma_f32_16x16x32_bf16 v[128:131], a[60:63], v[16:19], v[128:131]\n\tbuffer_load_dwordx4 v[92:95], v8, s[8:11], s68 offen offset:2048 sc1\n\ts_add_u32 s68, s46, 27648\n\tv_mfma_f32_16x16x32_bf16 v[136:139], a[64:67], v[16:19], v[136:139]\n\ts_waitcnt lgkmcnt(8)\n\tv_mfma_f32_16x16x32_bf16 v[124:127], a[56:59], v[20:23], v[124:127]\n\t" \
    "ds_read_b64_tr_b16 v[40:41], v226 offset:12288\n\tds_read_b64_tr_b16 v[42:43], v226 offset:14336\n\tv_mfma_f32_16x16x32_bf16 v[132:135], a[60:63], v[20:23], v[132:135]\n\tbuffer_load_dwordx4 v[216:219], v8, s[8:11], s68 offen offset:0 sc1\n\tv_mfma_f32_16x16x32_bf16 v[140:143], a[64:67], v[20:23], v[140:143]\n\ts_waitcnt lgkmcnt(8)\n\tv_mfma_f32_16x16x32_bf16 v[144:147], a[56:59], v[24:27], v[144:147]\n\tds_read_b64_tr_b16 v[44:45], v227 offset:12288\n\t" \
    "ds_read_b64_tr_b16 v[46:47], v227 offset:14336\n\tv_mfma_f32_16x16x32_bf16 v[152:155], a[60:63], v[24:27], v[152:155]\n\tv_mfma_f32_16x16x32_bf16 v[160:163], a[64:67], v[24:27], v[160:163]\n\ts_waitcnt lgkmcnt(8)\n\tv_mfma_f32_16x16x32_bf16 v[148:151], a[56:59], v[28:31], v[148:151]\n\tds_read_b64_tr_b16 v[16:17], v4 offset:16384\n\tds_read_b64_tr_b16 v[18:19], v4 offset:18432\n\tv_mfma_f32_16x16x32_bf16 v[156:159], a[60:63], v[28:31], v[156:159]\n\t" \
    "buffer_load_dwordx4 v[96:99], v8, s[8:11], s68 offen offset:1024 sc1\n\tv_mfma_f32_16x16x32_bf16 v[164:167], a[64:67], v[28:31], v[164:167]\n\ts_waitcnt vmcnt(14)\n\ts_add_u32 s73, s49, 4096\n\tbuffer_store_dwordx2 v[248:249], off, s[8:11], s73 sc1\n\ts_waitcnt lgkmcnt(8)\n\tv_mfma_f32_16x16x32_bf16 v[168:171], a[56:59], v[32:35], v[168:171]\n\tds_read_b64_tr_b16 v[20:21], v5 offset:16384\n\t" \
    "ds_read_b64_tr_b16 v[22:23], v5 offset:18432\n\tv_mfma_f32_16x16x32_bf16 v[176:179], a[60:63], v[32:35], v[176:179]\n\tv_mfma_f32_16x16x32_bf16 v[184:187], a[64:67], v[32:35], v[184:187]\n\ts_waitcnt lgkmcnt(8)\n\tv_mfma_f32_16x16x32_bf16 v[172:175], a[56:59], v[36:39], v[172:175]\n\tds_read_b64_tr_b16 v[24:25], v6 offset:16384\n\tds_read_b64_tr_b16 v[26:27], v6 offset:18432\n\tv_mfma_f32_16x16x32_bf16 v[180:183], a[60:63], v[36:39], v[180:183]\n\t" \
    "v_mfma_f32_16x16x32_bf16 v[188:191], a[64:67], v[36:39], v[188:191]\n\ts_waitcnt lgkmcnt(8)\n\tv_mfma_f32_16x16x32_bf16 v[48:51], a[56:59], v[40:43], v[48:51]\n\tds_read_b64_tr_b16 v[28:29], v7 offset:16384\n\tds_read_b64_tr_b16 v[30:31], v7 offset:18432\n\tv_mfma_f32_16x16x32_bf16 v[56:59], a[60:63], v[40:43], v[56:59]\n\tbuffer_load_dwordx4 v[100:103], v8, s[8:11], s68 offen offset:2048 sc1\n\ts_add_u32 s68, s46, 30720\n\t" \
    "v_mfma_f32_16x16x32_bf16 v[64:67], a[64:67], v[40:43], v[64:67]\n\ts_waitcnt lgkmcnt(8)\n\tv_mfma_f32_16x16x32_bf16 v[52:55], a[56:59], v[44:47], v[52:55]\n\tds_read_b64_tr_b16 v[32:33], v224 offset:16384\n\tds_read_b64_tr_b16 v[34:35], v224 offset:18432\n\tv_mfma_f32_16x16x32_bf16 v[60:63], a[60:63], v[44:47], v[60:63]\n\tbuffer_load_dwordx4 v[104:107], v8, s[8:11], s68 offen offset:0 sc1\n\tv_mfma_f32_16x16x32_bf16 v[68:71], a[64:67], v[44:47], v[68:71]\n\t" \
    "s_barrier\n\ts_waitcnt lgkmcnt(8)\n\tv_mfma_f32_16x16x32_bf16 v[120:123], a[68:71], v[16:19], v[120:123]\n\tds_read_b64_tr_b16 v[36:37], v225 offset:16384\n\tds_read_b64_tr_b16 v[38:39], v225 offset:18432\n\tv_mfma_f32_16x16x32_bf16 v[128:131], a[72:75], v[16:19], v[128:131]\n\tbuffer_load_dwordx4 v[108:111], v8, s[8:11], s68 offen offset:1024 sc1\n\tv_mfma_f32_16x16x32_bf16 v[136:139], a[76:79], v[16:19], v[136:139]\n\t" \
    "s_waitcnt lgkmcnt(8)\n\tv_mfma_f32_16x16x32_bf16 v[124:127], a[68:71], v[20:23], v[124:127]\n\tds_read_b64_tr_b16 v[40:41], v226 offset:16384\n\tds_read_b64_tr_b16 v[42:43], v226 offset:18432\n\tv_mfma_f32_16x16x32_bf16 v[132:135], a[72:75], v[20:23], v[132:135]\n\tv_mfma_f32_16x16x32_bf16 v[140:143], a[76:79], v[20:23], v[140:143]\n\ts_waitcnt lgkmcnt(8)\n\tv_mfma_f32_16x16x32_bf16 v[144:147], a[68:71], v[24:27], v[144:147]\n\t" \
    "ds_read_b64_tr_b16 v[44:45], v227 offset:16384\n\tds_read_b64_tr_b16 v[46:47], v227 offset:18432\n\tv_mfma_f32_16x16x32_bf16 v[152:155], a[72:75], v[24:27], v[152:155]\n\tbuffer_load_dwordx4 v[112:115], v8, s[8:11], s68 offen offset:2048 sc1\n\ts_add_u32 s68, s46, 33792\n\tv_mfma_f32_16x16x32_bf16 v[160:163], a[76:79], v[24:27], v[160:163]\n\ts_waitcnt lgkmcnt(8)\n\tv_mfma_f32_16x16x32_bf16 v[148:151], a[68:71], v[28:31], v[148:151]\n\t" \
    "ds_read_b64_tr_b16 v[16:17], v4 offset:20480\n\tds_read_b64_tr_b16 v[18:19], v4 offset:22528\n\tv_mfma_f32_16x16x32_bf16 v[156:159], a[72:75], v[28:31], v[156:159]\n\tbuffer_load_dwordx4 v[116:119], v8, s[8:11], s68 offen offset:0 sc1\n\tv_mfma_f32_16x16x32_bf16 v[164:167], a[76:79], v[28:31], v[164:167]\n\ts_waitcnt lgkmcnt(8)\n\tv_mfma_f32_16x16x32_bf16 v[168:171], a[68:71], v[32:35], v[168:171]\n\tds_read_b64_tr_b16 v[20:21], v5 offset:20480\n\t" \
    "ds_read_b64_tr_b16 v[22:23], v5 offset:22528\n\tv_mfma_f32_16x16x32_bf16 v[176:179], a[72:75], v[32:35], v[176:179]\n\tv_mfma_f32_16x16x32_bf16 v[184:187], a[76:79], v[32:35], v[184:187]\n\ts_waitcnt lgkmcnt(8)\n\tv_mfma_f32_16x16x32_bf16 v[172:175], a[68:71], v[36:39], v[172:175]\n\tds_read_b64_tr_b16 v[24:25], v6 offset:20480\n\tds_read_b64_tr_b16 v[26:27], v6 offset:22528\n\tv_mfma_f32_16x16x32_bf16 v[180:183], a[72:75], v[36:39], v[180:183]\n\t" \
    "buffer_load_dwordx4 a[0:3], v8, s[8:11], s68 offen offset:1024 sc1\n\tv_mfma_f32_16x16x32_bf16 v[188:191], a[76:79], v[36:39], v[188:191]\n\ts_waitcnt lgkmcnt(8)\n\tv_mfma_f32_16x16x32_bf16 v[48:51], a[68:71], v[40:43], v[48:51]\n\tds_read_b64_tr_b16 v[28:29], v7 offset:20480\n\tds_read_b64_tr_b16 v[30:31], v7 offset:22528\n\tv_mfma_f32_16x16x32_bf16 v[56:59], a[72:75], v[40:43], v[56:59]\n\tbuffer_load_dwordx4 a[4:7], v8, s[8:11], s68 offen offset:2048 sc1\n\t" \
    "s_mov_b32 s69, 3801088\n\ts_add_u32 m0, s54, 57344\n\tv_mfma_f32_16x16x32_bf16 v[64:67], a[76:79], v[40:43], v[64:67]\n\ts_waitcnt lgkmcnt(8)\n\tv_mfma_f32_16x16x32_bf16 v[52:55], a[68:71], v[44:47], v[52:55]\n\tds_read_b64_tr_b16 v[32:33], v224 offset:20480\n\tds_read_b64_tr_b16 v[34:35], v224 offset:22528\n\tv_mfma_f32_16x16x32_bf16 v[60:63], a[72:75], v[44:47], v[60:63]\n\t" \
    "v_mfma_f32_16x16x32_bf16 v[68:71], a[76:79], v[44:47], v[68:71]\n\ts_waitcnt vmcnt(19) lgkmcnt(8)\n\tv_mfma_f32_16x16x32_bf16 v[120:123], v[72:75], v[16:19], v[120:123]\n\tds_read_b64_tr_b16 v[36:37], v225 offset:20480\n\tds_read_b64_tr_b16 v[38:39], v225 offset:22528\n\tv_mfma_f32_16x16x32_bf16 v[128:131], v[76:79], v[16:19], v[128:131]\n\tbuffer_load_dwordx4 v12, s[16:19], s69 offen lds\n\ts_mov_b32 s69, 3801088\n\t" \
    "s_add_u32 m0, s54, 122880\n\tv_mfma_f32_16x16x32_bf16 v[136:139], v[80:83], v[16:19], v[136:139]\n\ts_waitcnt lgkmcnt(8)\n\tv_mfma_f32_16x16x32_bf16 v[124:127], v[72:75], v[20:23], v[124:127]\n\tds_read_b64_tr_b16 v[40:41], v226 offset:20480\n\tds_read_b64_tr_b16 v[42:43], v226 offset:22528\n\tv_mfma_f32_16x16x32_bf16 v[132:135], v[76:79], v[20:23], v[132:135]\n\tbuffer_load_dwordx4 v13, s[16:19], s69 offen lds\n\t" \
    "s_add_u32 s68, s46, 36864\n\tv_mfma_f32_16x16x32_bf16 v[140:143], v[80:83], v[20:23], v[140:143]\n\ts_waitcnt lgkmcnt(8)\n\tv_mfma_f32_16x16x32_bf16 v[144:147], v[72:75], v[24:27], v[144:147]\n\tds_read_b64_tr_b16 v[44:45], v227 offset:20480\n\tds_read_b64_tr_b16 v[46:47], v227 offset:22528\n\tv_mfma_f32_16x16x32_bf16 v[152:155], v[76:79], v[24:27], v[152:155]\n\tv_mfma_f32_16x16x32_bf16 v[160:163], v[80:83], v[24:27], v[160:163]\n\t" \
    "s_waitcnt lgkmcnt(8)\n\tv_mfma_f32_16x16x32_bf16 v[148:151], v[72:75], v[28:31], v[148:151]\n\tds_read_b64_tr_b16 v[16:17], v4 offset:24576\n\tds_read_b64_tr_b16 v[18:19], v4 offset:26624\n\tv_mfma_f32_16x16x32_bf16 v[156:159], v[76:79], v[28:31], v[156:159]\n\tbuffer_load_dwordx4 a[8:11], v8, s[8:11], s68 offen offset:0 sc1\n\tv_mfma_f32_16x16x32_bf16 v[164:167], v[80:83], v[28:31], v[164:167]\n\ts_waitcnt lgkmcnt(8)\n\t" \
    "v_mfma_f32_16x16x32_bf16 v[168:171], v[72:75], v[32:35], v[168:171]\n\tds_read_b64_tr_b16 v[20:21], v5 offset:24576\n\tds_read_b64_tr_b16 v[22:23], v5 offset:26624\n\tv_mfma_f32_16x16x32_bf16 v[176:179], v[76:79], v[32:35], v[176:179]\n\tbuffer_load_dwordx4 a[12:15], v8, s[8:11], s68 offen offset:1024 sc1\n\tv_mfma_f32_16x16x32_bf16 v[184:187], v[80:83], v[32:35], v[184:187]\n\ts_waitcnt lgkmcnt(8)\n\tv_mfma_f32_16x16x32_bf16 v[172:175], v[72:75], v[36:39], v[172:175]\n\t" \
    "ds_read_b64_tr_b16 v[24:25], v6 offset:24576\n\tds_read_b64_tr_b16 v[26:27], v6 offset:26624\n\tv_mfma_f32_16x16x32_bf16 v[180:183], v[76:79], v[36:39], v[180:183]\n\tbuffer_load_dwordx4 a[16:19], v8, s[8:11], s68 offen offset:2048 sc1\n\ts_add_u32 s68, s46, 39936\n\tv_mfma_f32_16x16x32_bf16 v[188:191], v[80:83], v[36:39], v[188:191]\n\ts_waitcnt lgkmcnt(8)\n\tv_mfma_f32_16x16x32_bf16 v[48:51], v[72:75], v[40:43], v[48:51]\n\t" \
    "ds_read_b64_tr_b16 v[28:29], v7 offset:24576\n\tds_read_b64_tr_b16 v[30:31], v7 offset:26624\n\tv_mfma_f32_16x16x32_bf16 v[56:59], v[76:79], v[40:43], v[56:59]\n\tbuffer_load_dwordx4 a[20:23], v8, s[8:11], s68 offen offset:0 sc1\n\tv_mfma_f32_16x16x32_bf16 v[64:67], v[80:83], v[40:43], v[64:67]\n\ts_waitcnt lgkmcnt(8)\n\tv_mfma_f32_16x16x32_bf16 v[52:55], v[72:75], v[44:47], v[52:55]\n\tds_read_b64_tr_b16 v[32:33], v224 offset:24576\n\t" \
    "ds_read_b64_tr_b16 v[34:35], v224 offset:26624\n\tv_mfma_f32_16x16x32_bf16 v[60:63], v[76:79], v[44:47], v[60:63]\n\tv_mfma_f32_16x16x32_bf16 v[68:71], v[80:83], v[44:47], v[68:71]\n\ts_barrier\n\ts_waitcnt vmcnt(22) lgkmcnt(8)\n\tv_mfma_f32_16x16x32_bf16 v[120:123], v[192:195], v[16:19], v[120:123]\n\tds_read_b64_tr_b16 v[36:37], v225 offset:24576\n\tds_read_b64_tr_b16 v[38:39], v225 offset:26624\n\t" \
    "v_mfma_f32_16x16x32_bf16 v[128:131], v[196:199], v[16:19], v[128:131]\n\tbuffer_load_dwordx4 a[24:27], v8, s[8:11], s68 offen offset:1024 sc1\n\tv_mfma_f32_16x16x32_bf16 v[136:139], v[200:203], v[16:19], v[136:139]\n\ts_waitcnt lgkmcnt(8)\n\tv_mfma_f32_16x16x32_bf16 v[124:127], v[192:195], v[20:23], v[124:127]\n\tds_read_b64_tr_b16 v[40:41], v226 offset:24576\n\tds_read_b64_tr_b16 v[42:43], v226 offset:26624\n\tv_mfma_f32_16x16x32_bf16 v[132:135], v[196:199], v[20:23], v[132:135]\n\t" \
    "buffer_load_dwordx4 a[28:31], v8, s[8:11], s68 offen offset:2048 sc1\n\ts_mov_b32 s69, 3670016\n\ts_add_u32 m0, s54, 61440\n\tv_mfma_f32_16x16x32_bf16 v[140:143], v[200:203], v[20:23], v[140:143]\n\ts_waitcnt lgkmcnt(8)\n\tv_mfma_f32_16x16x32_bf16 v[144:147], v[192:195], v[24:27], v[144:147]\n\tds_read_b64_tr_b16 v[44:45], v227 offset:24576\n\tds_read_b64_tr_b16 v[46:47], v227 offset:26624\n\t" \
    "v_mfma_f32_16x16x32_bf16 v[152:155], v[196:199], v[24:27], v[152:155]\n\tbuffer_load_dwordx4 v12, s[16:19], s69 offen lds\n\ts_mov_b32 s69, 3670016\n\ts_add_u32 m0, s54, 126976\n\tv_mfma_f32_16x16x32_bf16 v[160:163], v[200:203], v[24:27], v[160:163]\n\ts_waitcnt lgkmcnt(8)\n\tv_mfma_f32_16x16x32_bf16 v[148:151], v[192:195], v[28:31], v[148:151]\n\tds_read_b64_tr_b16 v[16:17], v4 offset:28672\n\t" \
    "ds_read_b64_tr_b16 v[18:19], v4 offset:30720\n\tv_mfma_f32_16x16x32_bf16 v[156:159], v[196:199], v[28:31], v[156:159]\n\tv_mfma_f32_16x16x32_bf16 v[164:167], v[200:203], v[28:31], v[164:167]\n\ts_waitcnt lgkmcnt(8)\n\tv_mfma_f32_16x16x32_bf16 v[168:171], v[192:195], v[32:35], v[168:171]\n\tds_read_b64_tr_b16 v[20:21], v5 offset:28672\n\tds_read_b64_tr_b16 v[22:23], v5 offset:30720\n\tv_mfma_f32_16x16x32_bf16 v[176:179], v[196:199], v[32:35], v[176:179]\n\t" \
    "buffer_load_dwordx4 v13, s[16:19], s69 offen lds\n\ts_mov_b32 s69, 4325376\n\ts_add_u32 m0, s54, 0\n\tv_mfma_f32_16x16x32_bf16 v[184:187], v[200:203], v[32:35], v[184:187]\n\ts_waitcnt lgkmcnt(8)\n\tv_mfma_f32_16x16x32_bf16 v[172:175], v[192:195], v[36:39], v[172:175]\n\tds_read_b64_tr_b16 v[24:25], v6 offset:28672\n\tds_read_b64_tr_b16 v[26:27], v6 offset:30720\n\t" \
    "v_mfma_f32_16x16x32_bf16 v[180:183], v[196:199], v[36:39], v[180:183]\n\tbuffer_load_dwordx4 v12, s[16:19], s69 offen lds\n\ts_mov_b32 s69, 4325376\n\ts_add_u32 m0, s54, 65536\n\tv_mfma_f32_16x16x32_bf16 v[188:191], v[200:203], v[36:39], v[188:191]\n\ts_waitcnt lgkmcnt(8)\n\tv_mfma_f32_16x16x32_bf16 v[48:51], v[192:195], v[40:43], v[48:51]\n\tds_read_b64_tr_b16 v[28:29], v7 offset:28672\n\t" \
    "ds_read_b64_tr_b16 v[30:31], v7 offset:30720\n\tv_mfma_f32_16x16x32_bf16 v[56:59], v[196:199], v[40:43], v[56:59]\n\tv_mfma_f32_16x16x32_bf16 v[64:67], v[200:203], v[40:43], v[64:67]\n\ts_waitcnt lgkmcnt(8)\n\tv_mfma_f32_16x16x32_bf16 v[52:55], v[192:195], v[44:47], v[52:55]\n\tds_read_b64_tr_b16 v[32:33], v224 offset:28672\n\tds_read_b64_tr_b16 v[34:35], v224 offset:30720\n\tv_mfma_f32_16x16x32_bf16 v[60:63], v[196:199], v[44:47], v[60:63]\n\t" \
    "buffer_load_dwordx4 v13, s[16:19], s69 offen lds\n\ts_mov_b32 s69, 4194304\n\ts_add_u32 m0, s54, 4096\n\tv_mfma_f32_16x16x32_bf16 v[68:71], v[200:203], v[44:47], v[68:71]\n\ts_waitcnt vmcnt(25) lgkmcnt(8)\n\tv_mfma_f32_16x16x32_bf16 v[120:123], v[204:207], v[16:19], v[120:123]\n\tds_read_b64_tr_b16 v[36:37], v225 offset:28672\n\tds_read_b64_tr_b16 v[38:39], v225 offset:30720\n\t" \
    "v_mfma_f32_16x16x32_bf16 v[128:131], v[208:211], v[16:19], v[128:131]\n\tbuffer_load_dwordx4 v12, s[16:19], s69 offen lds\n\ts_add_u32 s68, s46, 43008\n\tv_mfma_f32_16x16x32_bf16 v[136:139], v[212:215], v[16:19], v[136:139]\n\ts_waitcnt lgkmcnt(8)\n\tv_mfma_f32_16x16x32_bf16 v[124:127], v[204:207], v[20:23], v[124:127]\n\tds_read_b64_tr_b16 v[40:41], v226 offset:28672\n\tds_read_b64_tr_b16 v[42:43], v226 offset:30720\n\t" \
    "v_mfma_f32_16x16x32_bf16 v[132:135], v[208:211], v[20:23], v[132:135]\n\tv_mfma_f32_16x16x32_bf16 v[140:143], v[212:215], v[20:23], v[140:143]\n\ts_waitcnt lgkmcnt(8)\n\tv_mfma_f32_16x16x32_bf16 v[144:147], v[204:207], v[24:27], v[144:147]\n\tds_read_b64_tr_b16 v[44:45], v227 offset:28672\n\tds_read_b64_tr_b16 v[46:47], v227 offset:30720\n\tv_mfma_f32_16x16x32_bf16 v[152:155], v[208:211], v[24:27], v[152:155]\n\tbuffer_load_dwordx4 v[192:195], v8, s[8:11], s68 offen offset:0 sc1\n\t" \
    "v_mfma_f32_16x16x32_bf16 v[160:163], v[212:215], v[24:27], v[160:163]\n\ts_waitcnt lgkmcnt(8)\n\tv_mfma_f32_16x16x32_bf16 v[148:151], v[204:207], v[28:31], v[148:151]\n\tds_read_b64_tr_b16 v[16:17], v4 offset:32768\n\tds_read_b64_tr_b16 v[18:19], v4 offset:34816\n\tv_mfma_f32_16x16x32_bf16 v[156:159], v[208:211], v[28:31], v[156:159]\n\tbuffer_load_dwordx4 v[196:199], v8, s[8:11], s68 offen offset:1024 sc1\n\tv_mfma_f32_16x16x32_bf16 v[164:167], v[212:215], v[28:31], v[164:167]\n\t" \
    "s_waitcnt lgkmcnt(8)\n\tv_mfma_f32_16x16x32_bf16 v[168:171], v[204:207], v[32:35], v[168:171]\n\tds_read_b64_tr_b16 v[20:21], v5 offset:32768\n\tds_read_b64_tr_b16 v[22:23], v5 offset:34816\n\tv_mfma_f32_16x16x32_bf16 v[176:179], v[208:211], v[32:35], v[176:179]\n\tv_mfma_f32_16x16x32_bf16 v[184:187], v[212:215], v[32:35], v[184:187]\n\ts_waitcnt lgkmcnt(8)\n\tv_mfma_f32_16x16x32_bf16 v[172:175], v[204:207], v[36:39], v[172:175]\n\t" \
    "ds_read_b64_tr_b16 v[24:25], v6 offset:32768\n\tds_read_b64_tr_b16 v[26:27], v6 offset:34816\n\tv_mfma_f32_16x16x32_bf16 v[180:183], v[208:211], v[36:39], v[180:183]\n\tbuffer_load_dwordx4 v[200:203], v8, s[8:11], s68 offen offset:2048 sc1\n\ts_add_u32 s68, s46, 46080\n\tv_mfma_f32_16x16x32_bf16 v[188:191], v[212:215], v[36:39], v[188:191]\n\ts_waitcnt lgkmcnt(8)\n\tv_mfma_f32_16x16x32_bf16 v[48:51], v[204:207], v[40:43], v[48:51]\n\t" \
    "ds_read_b64_tr_b16 v[28:29], v7 offset:32768\n\tds_read_b64_tr_b16 v[30:31], v7 offset:34816\n\tv_mfma_f32_16x16x32_bf16 v[56:59], v[208:211], v[40:43], v[56:59]\n\tbuffer_load_dwordx4 v[72:75], v8, s[8:11], s68 offen offset:0 sc1\n\tv_mfma_f32_16x16x32_bf16 v[64:67], v[212:215], v[40:43], v[64:67]\n\ts_waitcnt lgkmcnt(8)\n\tv_mfma_f32_16x16x32_bf16 v[52:55], v[204:207], v[44:47], v[52:55]\n\tds_read_b64_tr_b16 v[32:33], v224 offset:32768\n\t" \
    "ds_read_b64_tr_b16 v[34:35], v224 offset:34816\n\tv_mfma_f32_16x16x32_bf16 v[60:63], v[208:211], v[44:47], v[60:63]\n\tv_mfma_f32_16x16x32_bf16 v[68:71], v[212:215], v[44:47], v[68:71]\n\ts_barrier\n\ts_waitcnt vmcnt(27) lgkmcnt(8)\n\tv_mfma_f32_16x16x32_bf16 v[120:123], v[84:87], v[16:19], v[120:123]\n\tds_read_b64_tr_b16 v[36:37], v225 offset:32768\n\tds_read_b64_tr_b16 v[38:39], v225 offset:34816\n\t" \
    "v_mfma_f32_16x16x32_bf16 v[128:131], v[88:91], v[16:19], v[128:131]\n\tbuffer_load_dwordx4 v[76:79], v8, s[8:11], s68 offen offset:1024 sc1\n\tv_mfma_f32_16x16x32_bf16 v[136:139], v[92:95], v[16:19], v[136:139]\n\ts_waitcnt lgkmcnt(8)\n\tv_mfma_f32_16x16x32_bf16 v[124:127], v[84:87], v[20:23], v[124:127]\n\tds_read_b64_tr_b16 v[40:41], v226 offset:32768\n\tds_read_b64_tr_b16 v[42:43], v226 offset:34816\n\tv_mfma_f32_16x16x32_bf16 v[132:135], v[88:91], v[20:23], v[132:135]\n\t" \
    "buffer_load_dwordx4 v[80:83], v8, s[8:11], s68 offen offset:2048 sc1\n\ts_add_u32 s68, s46, 49152\n\tv_mfma_f32_16x16x32_bf16 v[140:143], v[92:95], v[20:23], v[140:143]\n\ts_add_u32 s50, s50, 4096\n\tbuffer_load_dwordx2 v[250:251], v241, s[8:11], s50 offen sc1\n\ts_waitcnt lgkmcnt(8)\n\tv_mfma_f32_16x16x32_bf16 v[144:147], v[84:87], v[24:27], v[144:147]\n\tds_read_b64_tr_b16 v[44:45], v227 offset:32768\n\t" \
    "ds_read_b64_tr_b16 v[46:47], v227 offset:34816\n\tv_mfma_f32_16x16x32_bf16 v[152:155], v[88:91], v[24:27], v[152:155]\n\tv_mfma_f32_16x16x32_bf16 v[160:163], v[92:95], v[24:27], v[160:163]\n\ts_waitcnt lgkmcnt(8)\n\tv_mfma_f32_16x16x32_bf16 v[148:151], v[84:87], v[28:31], v[148:151]\n\tds_read_b64_tr_b16 v[16:17], v4 offset:36864\n\tds_read_b64_tr_b16 v[18:19], v4 offset:38912\n\tv_mfma_f32_16x16x32_bf16 v[156:159], v[88:91], v[28:31], v[156:159]\n\t" \
    "v_mfma_f32_16x16x32_bf16 v[164:167], v[92:95], v[28:31], v[164:167]\n\ts_waitcnt lgkmcnt(8)\n\tv_mfma_f32_16x16x32_bf16 v[168:171], v[84:87], v[32:35], v[168:171]\n\tds_read_b64_tr_b16 v[20:21], v5 offset:36864\n\tds_read_b64_tr_b16 v[22:23], v5 offset:38912\n\tv_mfma_f32_16x16x32_bf16 v[176:179], v[88:91], v[32:35], v[176:179]\n\tbuffer_load_dwordx4 a[32:35], v8, s[8:11], s68 offen offset:0 sc1\n\tv_mfma_f32_16x16x32_bf16 v[184:187], v[92:95], v[32:35], v[184:187]\n\t" \
    "s_waitcnt lgkmcnt(8)\n\tv_mfma_f32_16x16x32_bf16 v[172:175], v[84:87], v[36:39], v[172:175]\n\tds_read_b64_tr_b16 v[24:25], v6 offset:36864\n\tds_read_b64_tr_b16 v[26:27], v6 offset:38912\n\tv_mfma_f32_16x16x32_bf16 v[180:183], v[88:91], v[36:39], v[180:183]\n\tbuffer_load_dwordx4 a[36:39], v8, s[8:11], s68 offen offset:1024 sc1\n\tv_mfma_f32_16x16x32_bf16 v[188:191], v[92:95], v[36:39], v[188:191]\n\ts_waitcnt lgkmcnt(8)\n\t" \
    "v_mfma_f32_16x16x32_bf16 v[48:51], v[84:87], v[40:43], v[48:51]\n\tds_read_b64_tr_b16 v[28:29], v7 offset:36864\n\tds_read_b64_tr_b16 v[30:31], v7 offset:38912\n\tv_mfma_f32_16x16x32_bf16 v[56:59], v[88:91], v[40:43], v[56:59]\n\tv_mfma_f32_16x16x32_bf16 v[64:67], v[92:95], v[40:43], v[64:67]\n\ts_waitcnt lgkmcnt(8)\n\tv_mfma_f32_16x16x32_bf16 v[52:55], v[84:87], v[44:47], v[52:55]\n\tds_read_b64_tr_b16 v[32:33], v224 offset:36864\n\t" \
    "ds_read_b64_tr_b16 v[34:35], v224 offset:38912\n\tv_mfma_f32_16x16x32_bf16 v[60:63], v[88:91], v[44:47], v[60:63]\n\tbuffer_load_dwordx4 a[40:43], v8, s[8:11], s68 offen offset:2048 sc1\n\ts_mov_b32 s69, 4194304\n\ts_add_u32 m0, s54, 69632\n\tv_mfma_f32_16x16x32_bf16 v[68:71], v[92:95], v[44:47], v[68:71]\n\ts_waitcnt vmcnt(29) lgkmcnt(8)\n\tv_mfma_f32_16x16x32_bf16 v[120:123], v[216:219], v[16:19], v[120:123]\n\t" \
    "ds_read_b64_tr_b16 v[36:37], v225 offset:36864\n\tds_read_b64_tr_b16 v[38:39], v225 offset:38912\n\tv_mfma_f32_16x16x32_bf16 v[128:131], v[96:99], v[16:19], v[128:131]\n\tbuffer_load_dwordx4 v13, s[16:19], s69 offen lds\n\ts_mov_b32 s69, 393216\n\ts_add_u32 m0, s54, 8192\n\tv_mfma_f32_16x16x32_bf16 v[136:139], v[100:103], v[16:19], v[136:139]\n\ts_waitcnt lgkmcnt(8)\n\t" \
    "v_mfma_f32_16x16x32_bf16 v[124:127], v[216:219], v[20:23], v[124:127]\n\tds_read_b64_tr_b16 v[40:41], v226 offset:36864\n\tds_read_b64_tr_b16 v[42:43], v226 offset:38912\n\tv_mfma_f32_16x16x32_bf16 v[132:135], v[96:99], v[20:23], v[132:135]\n\tv_mfma_f32_16x16x32_bf16 v[140:143], v[100:103], v[20:23], v[140:143]\n\ts_waitcnt lgkmcnt(8)\n\tv_mfma_f32_16x16x32_bf16 v[144:147], v[216:219], v[24:27], v[144:147]\n\tds_read_b64_tr_b16 v[44:45], v227 offset:36864\n\t" \
    "ds_read_b64_tr_b16 v[46:47], v227 offset:38912\n\tv_mfma_f32_16x16x32_bf16 v[152:155], v[96:99], v[24:27], v[152:155]\n\tbuffer_load_dwordx4 v12, s[16:19], s69 offen lds\n\ts_mov_b32 s69, 393216\n\ts_add_u32 m0, s54, 73728\n\tv_mfma_f32_16x16x32_bf16 v[160:163], v[100:103], v[24:27], v[160:163]\n\ts_waitcnt lgkmcnt(8)\n\tv_mfma_f32_16x16x32_bf16 v[148:151], v[216:219], v[28:31], v[148:151]\n\t" \
    "ds_read_b64_tr_b16 v[16:17], v4 offset:40960\n\tds_read_b64_tr_b16 v[18:19], v4 offset:43008\n\tv_mfma_f32_16x16x32_bf16 v[156:159], v[96:99], v[28:31], v[156:159]\n\tbuffer_load_dwordx4 v13, s[16:19], s69 offen lds\n\ts_mov_b32 s69, 262144\n\ts_add_u32 m0, s54, 12288\n\tv_mfma_f32_16x16x32_bf16 v[164:167], v[100:103], v[28:31], v[164:167]\n\ts_waitcnt lgkmcnt(8)\n\t" \
    "v_mfma_f32_16x16x32_bf16 v[168:171], v[216:219], v[32:35], v[168:171]\n\tds_read_b64_tr_b16 v[20:21], v5 offset:40960\n\tds_read_b64_tr_b16 v[22:23], v5 offset:43008\n\tv_mfma_f32_16x16x32_bf16 v[176:179], v[96:99], v[32:35], v[176:179]\n\tv_mfma_f32_16x16x32_bf16 v[184:187], v[100:103], v[32:35], v[184:187]\n\ts_waitcnt lgkmcnt(8)\n\tv_mfma_f32_16x16x32_bf16 v[172:175], v[216:219], v[36:39], v[172:175]\n\tds_read_b64_tr_b16 v[24:25], v6 offset:40960\n\t" \
    "ds_read_b64_tr_b16 v[26:27], v6 offset:43008\n\tv_mfma_f32_16x16x32_bf16 v[180:183], v[96:99], v[36:39], v[180:183]\n\tbuffer_load_dwordx4 v12, s[16:19], s69 offen lds\n\ts_mov_b32 s69, 262144\n\ts_add_u32 m0, s54, 77824\n\tv_mfma_f32_16x16x32_bf16 v[188:191], v[100:103], v[36:39], v[188:191]\n\ts_waitcnt lgkmcnt(8)\n\tv_mfma_f32_16x16x32_bf16 v[48:51], v[216:219], v[40:43], v[48:51]\n\t" \
    "ds_read_b64_tr_b16 v[28:29], v7 offset:40960\n\tds_read_b64_tr_b16 v[30:31], v7 offset:43008\n\tv_mfma_f32_16x16x32_bf16 v[56:59], v[96:99], v[40:43], v[56:59]\n\tbuffer_load_dwordx4 v13, s[16:19], s69 offen lds\n\ts_add_u32 s68, s46, 52224\n\tv_mfma_f32_16x16x32_bf16 v[64:67], v[100:103], v[40:43], v[64:67]\n\ts_waitcnt lgkmcnt(8)\n\tv_mfma_f32_16x16x32_bf16 v[52:55], v[216:219], v[44:47], v[52:55]\n\t" \
    "ds_read_b64_tr_b16 v[32:33], v224 offset:40960\n\tds_read_b64_tr_b16 v[34:35], v224 offset:43008\n\tv_mfma_f32_16x16x32_bf16 v[60:63], v[96:99], v[44:47], v[60:63]\n\tv_mfma_f32_16x16x32_bf16 v[68:71], v[100:103], v[44:47], v[68:71]\n\ts_barrier\n\ts_waitcnt vmcnt(31) lgkmcnt(8)\n\tv_mfma_f32_16x16x32_bf16 v[120:123], v[104:107], v[16:19], v[120:123]\n\tds_read_b64_tr_b16 v[36:37], v225 offset:40960\n\t" \
    "ds_read_b64_tr_b16 v[38:39], v225 offset:43008\n\tv_mfma_f32_16x16x32_bf16 v[128:131], v[108:111], v[16:19], v[128:131]\n\tbuffer_load_dwordx4 v[84:87], v8, s[8:11], s68 offen offset:0 sc1\n\tv_mfma_f32_16x16x32_bf16 v[136:139], v[112:115], v[16:19], v[136:139]\n\ts_waitcnt lgkmcnt(8)\n\tv_mfma_f32_16x16x32_bf16 v[124:127], v[104:107], v[20:23], v[124:127]\n\tds_read_b64_tr_b16 v[40:41], v226 offset:40960\n\tds_read_b64_tr_b16 v[42:43], v226 offset:43008\n\t" \
    "v_mfma_f32_16x16x32_bf16 v[132:135], v[108:111], v[20:23], v[132:135]\n\tbuffer_load_dwordx4 v[88:91], v8, s[8:11], s68 offen offset:1024 sc1\n\tv_mfma_f32_16x16x32_bf16 v[140:143], v[112:115], v[20:23], v[140:143]\n\ts_waitcnt lgkmcnt(8)\n\tv_mfma_f32_16x16x32_bf16 v[144:147], v[104:107], v[24:27], v[144:147]\n\tds_read_b64_tr_b16 v[44:45], v227 offset:40960\n\tds_read_b64_tr_b16 v[46:47], v227 offset:43008\n\tv_mfma_f32_16x16x32_bf16 v[152:155], v[108:111], v[24:27], v[152:155]\n\t" \
    "buffer_load_dwordx4 v[92:95], v8, s[8:11], s68 offen offset:2048 sc1\n\ts_mov_b32 s69, 917504\n\ts_add_u32 m0, s54, 16384\n\tv_mfma_f32_16x16x32_bf16 v[160:163], v[112:115], v[24:27], v[160:163]\n\ts_waitcnt lgkmcnt(8)\n\tv_mfma_f32_16x16x32_bf16 v[148:151], v[104:107], v[28:31], v[148:151]\n\tds_read_b64_tr_b16 v[16:17], v4 offset:45056\n\tds_read_b64_tr_b16 v[18:19], v4 offset:47104\n\t" \
    "v_mfma_f32_16x16x32_bf16 v[156:159], v[108:111], v[28:31], v[156:159]\n\tbuffer_load_dwordx4 v12, s[16:19], s69 offen lds\n\ts_mov_b32 s69, 917504\n\ts_add_u32 m0, s54, 81920\n\tv_mfma_f32_16x16x32_bf16 v[164:167], v[112:115], v[28:31], v[164:167]\n\ts_waitcnt lgkmcnt(8)\n\tv_mfma_f32_16x16x32_bf16 v[168:171], v[104:107], v[32:35], v[168:171]\n\tds_read_b64_tr_b16 v[20:21], v5 offset:45056\n\t" \
    "ds_read_b64_tr_b16 v[22:23], v5 offset:47104\n\tv_mfma_f32_16x16x32_bf16 v[176:179], v[108:111], v[32:35], v[176:179]\n\tbuffer_load_dwordx4 v13, s[16:19], s69 offen lds\n\ts_mov_b32 s69, 786432\n\ts_add_u32 m0, s54, 20480\n\tv_mfma_f32_16x16x32_bf16 v[184:187], v[112:115], v[32:35], v[184:187]\n\ts_waitcnt lgkmcnt(8)\n\tv_mfma_f32_16x16x32_bf16 v[172:175], v[104:107], v[36:39], v[172:175]\n\t" \
    "ds_read_b64_tr_b16 v[24:25], v6 offset:45056\n\tds_read_b64_tr_b16 v[26:27], v6 offset:47104\n\tv_mfma_f32_16x16x32_bf16 v[180:183], v[108:111], v[36:39], v[180:183]\n\tv_mfma_f32_16x16x32_bf16 v[188:191], v[112:115], v[36:39], v[188:191]\n\ts_waitcnt lgkmcnt(8)\n\tv_mfma_f32_16x16x32_bf16 v[48:51], v[104:107], v[40:43], v[48:51]\n\tds_read_b64_tr_b16 v[28:29], v7 offset:45056\n\tds_read_b64_tr_b16 v[30:31], v7 offset:47104\n\t" \
    "v_mfma_f32_16x16x32_bf16 v[56:59], v[108:111], v[40:43], v[56:59]\n\tbuffer_load_dwordx4 v12, s[16:19], s69 offen lds\n\ts_mov_b32 s69, 786432\n\ts_add_u32 m0, s54, 86016\n\tv_mfma_f32_16x16x32_bf16 v[64:67], v[112:115], v[40:43], v[64:67]\n\ts_waitcnt lgkmcnt(8)\n\tv_mfma_f32_16x16x32_bf16 v[52:55], v[104:107], v[44:47], v[52:55]\n\tds_read_b64_tr_b16 v[32:33], v224 offset:45056\n\t" \
    "ds_read_b64_tr_b16 v[34:35], v224 offset:47104\n\tv_mfma_f32_16x16x32_bf16 v[60:63], v[108:111], v[44:47], v[60:63]\n\tbuffer_load_dwordx4 v13, s[16:19], s69 offen lds\n\ts_mov_b32 s69, 1441792\n\ts_add_u32 m0, s54, 24576\n\tv_mfma_f32_16x16x32_bf16 v[68:71], v[112:115], v[44:47], v[68:71]\n\ts_waitcnt vmcnt(15)\n\tv_cmp_ne_u64 vcc, s[36:37], v[250:251]\n\t" \
    "s_cbranch_vccz 112f\n\ts_mov_b32 s71, 1048576\n\t111:\n\ts_sleep 1\n\tbuffer_load_dwordx2 v[250:251], v241, s[8:11], s50 offen sc1\n\ts_waitcnt vmcnt(0)\n\tv_cmp_ne_u64 vcc, s[36:37], v[250:251]\n\ts_cbranch_vccz 112f\n\t" \
    "s_sub_u32 s71, s71, 1\n\ts_cbranch_scc0 111b\n\t112:\n\ts_waitcnt lgkmcnt(8)\n\tv_mfma_f32_16x16x32_bf16 v[120:123], v[116:119], v[16:19], v[120:123]\n\tds_read_b64_tr_b16 v[36:37], v225 offset:45056\n\tds_read_b64_tr_b16 v[38:39], v225 offset:47104\n\tv_mfma_f32_16x16x32_bf16 v[128:131], a[0:3], v[16:19], v[128:131]\n\t" \
    "v_mfma_f32_16x16x32_bf16 v[136:139], a[4:7], v[16:19], v[136:139]\n\ts_waitcnt lgkmcnt(8)\n\tv_mfma_f32_16x16x32_bf16 v[124:127], v[116:119], v[20:23], v[124:127]\n\tds_read_b64_tr_b16 v[40:41], v226 offset:45056\n\tds_read_b64_tr_b16 v[42:43], v226 offset:47104\n\tv_mfma_f32_16x16x32_bf16 v[132:135], a[0:3], v[20:23], v[132:135]\n\tbuffer_load_dwordx4 v12, s[16:19], s69 offen lds\n\ts_add_u32 s68, s46, 55296\n\t" \
    "v_mfma_f32_16x16x32_bf16 v[140:143], a[4:7], v[20:23], v[140:143]\n\ts_waitcnt lgkmcnt(8)\n\tv_mfma_f32_16x16x32_bf16 v[144:147], v[116:119], v[24:27], v[144:147]\n\tds_read_b64_tr_b16 v[44:45], v227 offset:45056\n\tds_read_b64_tr_b16 v[46:47], v227 offset:47104\n\tv_mfma_f32_16x16x32_bf16 v[152:155], a[0:3], v[24:27], v[152:155]\n\tbuffer_load_dwordx4 v[96:99], v8, s[8:11], s68 offen offset:0 sc1\n\tv_mfma_f32_16x16x32_bf16 v[160:163], a[4:7], v[24:27], v[160:163]\n\t" \
    "s_waitcnt lgkmcnt(8)\n\tv_mfma_f32_16x16x32_bf16 v[148:151], v[116:119], v[28:31], v[148:151]\n\tds_read_b64_tr_b16 v[16:17], v4 offset:49152\n\tds_read_b64_tr_b16 v[18:19], v4 offset:51200\n\tv_mfma_f32_16x16x32_bf16 v[156:159], a[0:3], v[28:31], v[156:159]\n\tv_mfma_f32_16x16x32_bf16 v[164:167], a[4:7], v[28:31], v[164:167]\n\ts_waitcnt lgkmcnt(8)\n\tv_mfma_f32_16x16x32_bf16 v[168:171], v[116:119], v[32:35], v[168:171]\n\t" \
    "ds_read_b64_tr_b16 v[20:21], v5 offset:49152\n\tds_read_b64_tr_b16 v[22:23], v5 offset:51200\n\tv_mfma_f32_16x16x32_bf16 v[176:179], a[0:3], v[32:35], v[176:179]\n\tbuffer_load_dwordx4 v[100:103], v8, s[8:11], s68 offen offset:1024 sc1\n\tv_mfma_f32_16x16x32_bf16 v[184:187], a[4:7], v[32:35], v[184:187]\n\ts_waitcnt lgkmcnt(8)\n\tv_mfma_f32_16x16x32_bf16 v[172:175], v[116:119], v[36:39], v[172:175]\n\tds_read_b64_tr_b16 v[24:25], v6 offset:49152\n\t" \
    "ds_read_b64_tr_b16 v[26:27], v6 offset:51200\n\tv_mfma_f32_16x16x32_bf16 v[180:183], a[0:3], v[36:39], v[180:183]\n\tbuffer_load_dwordx4 v[104:107], v8, s[8:11], s68 offen offset:2048 sc1\n\ts_add_u32 s68, s46, 58368\n\tv_mfma_f32_16x16x32_bf16 v[188:191], a[4:7], v[36:39], v[188:191]\n\ts_waitcnt lgkmcnt(8)\n\tv_mfma_f32_16x16x32_bf16 v[48:51], v[116:119], v[40:43], v[48:51]\n\tds_read_b64_tr_b16 v[28:29], v7 offset:49152\n\t" \
    "ds_read_b64_tr_b16 v[30:31], v7 offset:51200\n\tv_mfma_f32_16x16x32_bf16 v[56:59], a[0:3], v[40:43], v[56:59]\n\tv_mfma_f32_16x16x32_bf16 v[64:67], a[4:7], v[40:43], v[64:67]\n\ts_waitcnt lgkmcnt(8)\n\tv_mfma_f32_16x16x32_bf16 v[52:55], v[116:119], v[44:47], v[52:55]\n\tds_read_b64_tr_b16 v[32:33], v224 offset:49152\n\tds_read_b64_tr_b16 v[34:35], v224 offset:51200\n\tv_mfma_f32_16x16x32_bf16 v[60:63], a[0:3], v[44:47], v[60:63]\n\t" \
    "buffer_load_dwordx4 a[44:47], v8, s[8:11], s68 offen offset:0 sc1\n\tv_mfma_f32_16x16x32_bf16 v[68:71], a[4:7], v[44:47], v[68:71]\n\ts_barrier\n\ts_waitcnt lgkmcnt(8)\n\tv_mfma_f32_16x16x32_bf16 v[120:123], a[8:11], v[16:19], v[120:123]\n\tds_read_b64_tr_b16 v[36:37], v225 offset:49152\n\tds_read_b64_tr_b16 v[38:39], v225 offset:51200\n\tv_mfma_f32_16x16x32_bf16 v[128:131], a[12:15], v[16:19], v[128:131]\n\t" \
    "buffer_load_dwordx4 a[48:51], v8, s[8:11], s68 offen offset:1024 sc1\n\tv_mfma_f32_16x16x32_bf16 v[136:139], a[16:19], v[16:19], v[136:139]\n\ts_waitcnt lgkmcnt(8)\n\tv_mfma_f32_16x16x32_bf16 v[124:127], a[8:11], v[20:23], v[124:127]\n\tds_read_b64_tr_b16 v[40:41], v226 offset:49152\n\tds_read_b64_tr_b16 v[42:43], v226 offset:51200\n\tv_mfma_f32_16x16x32_bf16 v[132:135], a[12:15], v[20:23], v[132:135]\n\tbuffer_load_dwordx4 a[52:55], v8, s[8:11], s68 offen offset:2048 sc1\n\t" \
    "s_add_u32 s68, s46, 61440\n\tv_mfma_f32_16x16x32_bf16 v[140:143], a[16:19], v[20:23], v[140:143]\n\ts_waitcnt lgkmcnt(8)\n\tv_mfma_f32_16x16x32_bf16 v[144:147], a[8:11], v[24:27], v[144:147]\n\tds_read_b64_tr_b16 v[44:45], v227 offset:49152\n\tds_read_b64_tr_b16 v[46:47], v227 offset:51200\n\tv_mfma_f32_16x16x32_bf16 v[152:155], a[12:15], v[24:27], v[152:155]\n\tv_mfma_f32_16x16x32_bf16 v[160:163], a[16:19], v[24:27], v[160:163]\n\t" \
    "s_waitcnt lgkmcnt(8)\n\tv_mfma_f32_16x16x32_bf16 v[148:151], a[8:11], v[28:31], v[148:151]\n\tds_read_b64_tr_b16 v[16:17], v4 offset:53248\n\tds_read_b64_tr_b16 v[18:19], v4 offset:55296\n\tv_mfma_f32_16x16x32_bf16 v[156:159], a[12:15], v[28:31], v[156:159]\n\tbuffer_load_dwordx4 a[0:3], v8, s[8:11], s68 offen offset:0 sc1\n\tv_mfma_f32_16x16x32_bf16 v[164:167], a[16:19], v[28:31], v[164:167]\n\ts_waitcnt lgkmcnt(8)\n\t" \
    "v_mfma_f32_16x16x32_bf16 v[168:171], a[8:11], v[32:35], v[168:171]\n\tds_read_b64_tr_b16 v[20:21], v5 offset:53248\n\tds_read_b64_tr_b16 v[22:23], v5 offset:55296\n\tv_mfma_f32_16x16x32_bf16 v[176:179], a[12:15], v[32:35], v[176:179]\n\tbuffer_load_dwordx4 a[4:7], v8, s[8:11], s68 offen offset:1024 sc1\n\tv_mfma_f32_16x16x32_bf16 v[184:187], a[16:19], v[32:35], v[184:187]\n\ts_waitcnt lgkmcnt(8)\n\tv_mfma_f32_16x16x32_bf16 v[172:175], a[8:11], v[36:39], v[172:175]\n\t" \
    "ds_read_b64_tr_b16 v[24:25], v6 offset:53248\n\tds_read_b64_tr_b16 v[26:27], v6 offset:55296\n\tv_mfma_f32_16x16x32_bf16 v[180:183], a[12:15], v[36:39], v[180:183]\n\tv_mfma_f32_16x16x32_bf16 v[188:191], a[16:19], v[36:39], v[188:191]\n\ts_waitcnt lgkmcnt(8)\n\tv_mfma_f32_16x16x32_bf16 v[48:51], a[8:11], v[40:43], v[48:51]\n\tds_read_b64_tr_b16 v[28:29], v7 offset:53248\n\tds_read_b64_tr_b16 v[30:31], v7 offset:55296\n\t" \
    "v_mfma_f32_16x16x32_bf16 v[56:59], a[12:15], v[40:43], v[56:59]\n\tbuffer_load_dwordx4 a[56:59], v8, s[8:11], s68 offen offset:2048 sc1\n\ts_add_u32 s68, s46, 64512\n\tv_mfma_f32_16x16x32_bf16 v[64:67], a[16:19], v[40:43], v[64:67]\n\ts_waitcnt lgkmcnt(8)\n\tv_mfma_f32_16x16x32_bf16 v[52:55], a[8:11], v[44:47], v[52:55]\n\tds_read_b64_tr_b16 v[32:33], v224 offset:53248\n\tds_read_b64_tr_b16 v[34:35], v224 offset:55296\n\t" \
    "v_mfma_f32_16x16x32_bf16 v[60:63], a[12:15], v[44:47], v[60:63]\n\tbuffer_load_dwordx4 a[60:63], v8, s[8:11], s68 offen offset:0 sc1\n\tv_mfma_f32_16x16x32_bf16 v[68:71], a[16:19], v[44:47], v[68:71]\n\ts_waitcnt lgkmcnt(8)\n\tv_mfma_f32_16x16x32_bf16 v[120:123], a[20:23], v[16:19], v[120:123]\n\tds_read_b64_tr_b16 v[36:37], v225 offset:53248\n\tds_read_b64_tr_b16 v[38:39], v225 offset:55296\n\tv_mfma_f32_16x16x32_bf16 v[128:131], a[24:27], v[16:19], v[128:131]\n\t" \
    "v_mfma_f32_16x16x32_bf16 v[136:139], a[28:31], v[16:19], v[136:139]\n\ts_waitcnt lgkmcnt(8)\n\tv_mfma_f32_16x16x32_bf16 v[124:127], a[20:23], v[20:23], v[124:127]\n\tds_read_b64_tr_b16 v[40:41], v226 offset:53248\n\tds_read_b64_tr_b16 v[42:43], v226 offset:55296\n\tv_mfma_f32_16x16x32_bf16 v[132:135], a[24:27], v[20:23], v[132:135]\n\tbuffer_load_dwordx4 a[64:67], v8, s[8:11], s68 offen offset:1024 sc1\n\tv_mfma_f32_16x16x32_bf16 v[140:143], a[28:31], v[20:23], v[140:143]\n\t" \
    "s_waitcnt lgkmcnt(8)\n\tv_mfma_f32_16x16x32_bf16 v[144:147], a[20:23], v[24:27], v[144:147]\n\tds_read_b64_tr_b16 v[44:45], v227 offset:53248\n\tds_read_b64_tr_b16 v[46:47], v227 offset:55296\n\tv_mfma_f32_16x16x32_bf16 v[152:155], a[24:27], v[24:27], v[152:155]\n\tbuffer_load_dwordx4 a[68:71], v8, s[8:11], s68 offen offset:2048 sc1\n\ts_mov_b32 s69, 1441792\n\ts_add_u32 m0, s54, 90112\n\t" \
    "v_mfma_f32_16x16x32_bf16 v[160:163], a[28:31], v[24:27], v[160:163]\n\ts_waitcnt lgkmcnt(8)\n\tv_mfma_f32_16x16x32_bf16 v[148:151], a[20:23], v[28:31], v[148:151]\n\tds_read_b64_tr_b16 v[16:17], v4 offset:57344\n\tds_read_b64_tr_b16 v[18:19], v4 offset:59392\n\tv_mfma_f32_16x16x32_bf16 v[156:159], a[24:27], v[28:31], v[156:159]\n\tv_mfma_f32_16x16x32_bf16 v[164:167], a[28:31], v[28:31], v[164:167]\n\ts_waitcnt lgkmcnt(8)\n\t" \
    "v_mfma_f32_16x16x32_bf16 v[168:171], a[20:23], v[32:35], v[168:171]\n\tds_read_b64_tr_b16 v[20:21], v5 offset:57344\n\tds_read_b64_tr_b16 v[22:23], v5 offset:59392\n\tv_mfma_f32_16x16x32_bf16 v[176:179], a[24:27], v[32:35], v[176:179]\n\tbuffer_load_dwordx4 v13, s[16:19], s69 offen lds\n\ts_mov_b32 s69, 1310720\n\ts_add_u32 m0, s54, 28672\n\tv_mfma_f32_16x16x32_bf16 v[184:187], a[28:31], v[32:35], v[184:187]\n\t" \
    "s_waitcnt lgkmcnt(8)\n\tv_mfma_f32_16x16x32_bf16 v[172:175], a[20:23], v[36:39], v[172:175]\n\tds_read_b64_tr_b16 v[24:25], v6 offset:57344\n\tds_read_b64_tr_b16 v[26:27], v6 offset:59392\n\tv_mfma_f32_16x16x32_bf16 v[180:183], a[24:27], v[36:39], v[180:183]\n\tbuffer_load_dwordx4 v12, s[16:19], s69 offen lds\n\ts_mov_b32 s69, 1310720\n\ts_add_u32 m0, s54, 94208\n\t" \
    "v_mfma_f32_16x16x32_bf16 v[188:191], a[28:31], v[36:39], v[188:191]\n\ts_waitcnt lgkmcnt(8)\n\tv_mfma_f32_16x16x32_bf16 v[48:51], a[20:23], v[40:43], v[48:51]\n\tds_read_b64_tr_b16 v[28:29], v7 offset:57344\n\tds_read_b64_tr_b16 v[30:31], v7 offset:59392\n\tv_mfma_f32_16x16x32_bf16 v[56:59], a[24:27], v[40:43], v[56:59]\n\tv_mfma_f32_16x16x32_bf16 v[64:67], a[28:31], v[40:43], v[64:67]\n\ts_waitcnt lgkmcnt(8)\n\t" \
    "v_mfma_f32_16x16x32_bf16 v[52:55], a[20:23], v[44:47], v[52:55]\n\tds_read_b64_tr_b16 v[32:33], v224 offset:57344\n\tds_read_b64_tr_b16 v[34:35], v224 offset:59392\n\tv_mfma_f32_16x16x32_bf16 v[60:63], a[24:27], v[44:47], v[60:63]\n\tbuffer_load_dwordx4 v13, s[16:19], s69 offen lds\n\ts_mov_b32 s69, 1966080\n\ts_add_u32 m0, s54, 32768\n\tv_mfma_f32_16x16x32_bf16 v[68:71], a[28:31], v[44:47], v[68:71]\n\t" \
    "s_barrier\n\ts_waitcnt lgkmcnt(8)\n\tv_mfma_f32_16x16x32_bf16 v[120:123], v[192:195], v[16:19], v[120:123]\n\tds_read_b64_tr_b16 v[36:37], v225 offset:57344\n\tds_read_b64_tr_b16 v[38:39], v225 offset:59392\n\tv_mfma_f32_16x16x32_bf16 v[128:131], v[196:199], v[16:19], v[128:131]\n\tbuffer_load_dwordx4 v12, s[16:19], s69 offen lds\n\ts_mov_b32 s69, 1966080\n\t" \
    "s_add_u32 m0, s54, 98304\n\tv_mfma_f32_16x16x32_bf16 v[136:139], v[200:203], v[16:19], v[136:139]\n\ts_waitcnt lgkmcnt(8)\n\tv_mfma_f32_16x16x32_bf16 v[124:127], v[192:195], v[20:23], v[124:127]\n\tds_read_b64_tr_b16 v[40:41], v226 offset:57344\n\tds_read_b64_tr_b16 v[42:43], v226 offset:59392\n\tv_mfma_f32_16x16x32_bf16 v[132:135], v[196:199], v[20:23], v[132:135]\n\tbuffer_load_dwordx4 v13, s[16:19], s69 offen lds\n\t" \
    "s_mov_b32 s69, 1835008\n\ts_add_u32 m0, s54, 36864\n\tv_mfma_f32_16x16x32_bf16 v[140:143], v[200:203], v[20:23], v[140:143]\n\ts_waitcnt lgkmcnt(8)\n\tv_mfma_f32_16x16x32_bf16 v[144:147], v[192:195], v[24:27], v[144:147]\n\tds_read_b64_tr_b16 v[44:45], v227 offset:57344\n\tds_read_b64_tr_b16 v[46:47], v227 offset:59392\n\tv_mfma_f32_16x16x32_bf16 v[152:155], v[196:199], v[24:27], v[152:155]\n\t" \
    "v_mfma_f32_16x16x32_bf16 v[160:163], v[200:203], v[24:27], v[160:163]\n\ts_waitcnt lgkmcnt(8)\n\tv_mfma_f32_16x16x32_bf16 v[148:151], v[192:195], v[28:31], v[148:151]\n\tds_read_b64_tr_b16 v[16:17], v4 offset:61440\n\tds_read_b64_tr_b16 v[18:19], v4 offset:63488\n\tv_mfma_f32_16x16x32_bf16 v[156:159], v[196:199], v[28:31], v[156:159]\n\tbuffer_load_dwordx4 v12, s[16:19], s69 offen lds\n\ts_add_u32 s68, s46, 67584\n\t" \
    "v_mfma_f32_16x16x32_bf16 v[164:167], v[200:203], v[28:31], v[164:167]\n\ts_waitcnt lgkmcnt(8)\n\tv_mfma_f32_16x16x32_bf16 v[168:171], v[192:195], v[32:35], v[168:171]\n\tds_read_b64_tr_b16 v[20:21], v5 offset:61440\n\tds_read_b64_tr_b16 v[22:23], v5 offset:63488\n\tv_mfma_f32_16x16x32_bf16 v[176:179], v[196:199], v[32:35], v[176:179]\n\tbuffer_load_dwordx4 a[8:11], v8, s[8:11], s68 offen offset:0 sc1\n\tv_mfma_f32_16x16x32_bf16 v[184:187], v[200:203], v[32:35], v[184:187]\n\t" \
    "s_waitcnt lgkmcnt(8)\n\tv_mfma_f32_16x16x32_bf16 v[172:175], v[192:195], v[36:39], v[172:175]\n\tds_read_b64_tr_b16 v[24:25], v6 offset:61440\n\tds_read_b64_tr_b16 v[26:27], v6 offset:63488\n\tv_mfma_f32_16x16x32_bf16 v[180:183], v[196:199], v[36:39], v[180:183]\n\tv_mfma_f32_16x16x32_bf16 v[188:191], v[200:203], v[36:39], v[188:191]\n\ts_waitcnt lgkmcnt(8)\n\tv_mfma_f32_16x16x32_bf16 v[48:51], v[192:195], v[40:43], v[48:51]\n\t" \
    "ds_read_b64_tr_b16 v[28:29], v7 offset:61440\n\tds_read_b64_tr_b16 v[30:31], v7 offset:63488\n\tv_mfma_f32_16x16x32_bf16 v[56:59], v[196:199], v[40:43], v[56:59]\n\tbuffer_load_dwordx4 a[12:15], v8, s[8:11], s68 offen offset:1024 sc1\n\tv_mfma_f32_16x16x32_bf16 v[64:67], v[200:203], v[40:43], v[64:67]\n\ts_waitcnt lgkmcnt(8)\n\tv_mfma_f32_16x16x32_bf16 v[52:55], v[192:195], v[44:47], v[52:55]\n\tds_read_b64_tr_b16 v[32:33], v224 offset:61440\n\t" \
    "ds_read_b64_tr_b16 v[34:35], v224 offset:63488\n\tv_mfma_f32_16x16x32_bf16 v[60:63], v[196:199], v[44:47], v[60:63]\n\tbuffer_load_dwordx4 a[16:19], v8, s[8:11], s68 offen offset:2048 sc1\n\ts_add_u32 s68, s46, 70656\n\tv_mfma_f32_16x16x32_bf16 v[68:71], v[200:203], v[44:47], v[68:71]\n\ts_waitcnt lgkmcnt(8)\n\tv_mfma_f32_16x16x32_bf16 v[120:123], v[72:75], v[16:19], v[120:123]\n\tds_read_b64_tr_b16 v[36:37], v225 offset:61440\n\t" \
    "ds_read_b64_tr_b16 v[38:39], v225 offset:63488\n\tv_mfma_f32_16x16x32_bf16 v[128:131], v[76:79], v[16:19], v[128:131]\n\tv_mfma_f32_16x16x32_bf16 v[136:139], v[80:83], v[16:19], v[136:139]\n\ts_waitcnt lgkmcnt(8)\n\tv_mfma_f32_16x16x32_bf16 v[124:127], v[72:75], v[20:23], v[124:127]\n\tds_read_b64_tr_b16 v[40:41], v4 offset:0\n\tds_read_b64_tr_b16 v[42:43], v4 offset:2048\n\tv_mfma_f32_16x16x32_bf16 v[132:135], v[76:79], v[20:23], v[132:135]\n\t" \
    "buffer_load_dwordx4 a[20:23], v8, s[8:11], s68 offen offset:0 sc1\n\tv_mfma_f32_16x16x32_bf16 v[140:143], v[80:83], v[20:23], v[140:143]\n\ts_waitcnt lgkmcnt(8)\n\tv_mfma_f32_16x16x32_bf16 v[144:147], v[72:75], v[24:27], v[144:147]\n\tds_read_b64_tr_b16 v[44:45], v5 offset:0\n\tds_read_b64_tr_b16 v[46:47], v5 offset:2048\n\tv_mfma_f32_16x16x32_bf16 v[152:155], v[76:79], v[24:27], v[152:155]\n\tbuffer_load_dwordx4 a[24:27], v8, s[8:11], s68 offen offset:1024 sc1\n\t" \
    "v_mfma_f32_16x16x32_bf16 v[160:163], v[80:83], v[24:27], v[160:163]\n\ts_waitcnt lgkmcnt(8)\n\tv_mfma_f32_16x16x32_bf16 v[148:151], v[72:75], v[28:31], v[148:151]\n\tds_read_b64_tr_b16 v[16:17], v6 offset:0\n\tds_read_b64_tr_b16 v[18:19], v6 offset:2048\n\tv_mfma_f32_16x16x32_bf16 v[156:159], v[76:79], v[28:31], v[156:159]\n\tv_mfma_f32_16x16x32_bf16 v[164:167], v[80:83], v[28:31], v[164:167]\n\ts_waitcnt lgkmcnt(8)\n\t" \
    "v_mfma_f32_16x16x32_bf16 v[168:171], v[72:75], v[32:35], v[168:171]\n\tds_read_b64_tr_b16 v[20:21], v7 offset:0\n\tds_read_b64_tr_b16 v[22:23], v7 offset:2048\n\tv_mfma_f32_16x16x32_bf16 v[176:179], v[76:79], v[32:35], v[176:179]\n\tbuffer_load_dwordx4 a[28:31], v8, s[8:11], s68 offen offset:2048 sc1\n\ts_add_u32 s68, s46, 73728\n\tv_mfma_f32_16x16x32_bf16 v[184:187], v[80:83], v[32:35], v[184:187]\n\ts_waitcnt lgkmcnt(8)\n\t" \
    "v_mfma_f32_16x16x32_bf16 v[172:175], v[72:75], v[36:39], v[172:175]\n\tds_read_b64_tr_b16 v[24:25], v224 offset:0\n\tds_read_b64_tr_b16 v[26:27], v224 offset:2048\n\tv_mfma_f32_16x16x32_bf16 v[180:183], v[76:79], v[36:39], v[180:183]\n\tv_mfma_f32_16x16x32_bf16 v[188:191], v[80:83], v[36:39], v[188:191]\n\ts_waitcnt vmcnt(37) lgkmcnt(8)\n\tv_mfma_f32_16x16x32_bf16 v[120:123], a[32:35], v[40:43], v[120:123]\n\tds_read_b64_tr_b16 v[28:29], v225 offset:0\n\t" \
    "ds_read_b64_tr_b16 v[30:31], v225 offset:2048\n\tv_mfma_f32_16x16x32_bf16 v[128:131], a[36:39], v[40:43], v[128:131]\n\tbuffer_load_dwordx4 a[72:75], v8, s[8:11], s68 offen offset:0 sc1\n\tv_mfma_f32_16x16x32_bf16 v[136:139], a[40:43], v[40:43], v[136:139]\n\ts_waitcnt lgkmcnt(8)\n\tv_mfma_f32_16x16x32_bf16 v[124:127], a[32:35], v[44:47], v[124:127]\n\tds_read_b64_tr_b16 v[32:33], v4 offset:4096\n\tds_read_b64_tr_b16 v[34:35], v4 offset:6144\n\t" \
    "v_mfma_f32_16x16x32_bf16 v[132:135], a[36:39], v[44:47], v[132:135]\n\tbuffer_load_dwordx4 a[76:79], v8, s[8:11], s68 offen offset:1024 sc1\n\tv_mfma_f32_16x16x32_bf16 v[140:143], a[40:43], v[44:47], v[140:143]\n\ts_waitcnt vmcnt(34)\n\ts_barrier\n\ts_waitcnt lgkmcnt(8)\n\tv_mfma_f32_16x16x32_bf16 v[144:147], a[32:35], v[16:19], v[144:147]\n\tds_read_b64_tr_b16 v[36:37], v5 offset:4096\n\t" \
    "ds_read_b64_tr_b16 v[38:39], v5 offset:6144\n\tv_mfma_f32_16x16x32_bf16 v[152:155], a[36:39], v[16:19], v[152:155]\n\tbuffer_load_dwordx4 a[80:83], v8, s[8:11], s68 offen offset:2048 sc1\n\ts_mov_b32 s69, 1835008\n\ts_add_u32 m0, s54, 102400\n\tv_mfma_f32_16x16x32_bf16 v[160:163], a[40:43], v[16:19], v[160:163]\n\ts_waitcnt lgkmcnt(8)\n\tv_mfma_f32_16x16x32_bf16 v[148:151], a[32:35], v[20:23], v[148:151]\n\t" \
    "ds_read_b64_tr_b16 v[40:41], v6 offset:4096\n\tds_read_b64_tr_b16 v[42:43], v6 offset:6144\n\tv_mfma_f32_16x16x32_bf16 v[156:159], a[36:39], v[20:23], v[156:159]\n\tbuffer_load_dwordx4 v13, s[16:19], s69 offen lds\n\ts_mov_b32 s69, 2490368\n\ts_add_u32 m0, s54, 40960\n\tv_mfma_f32_16x16x32_bf16 v[164:167], a[40:43], v[20:23], v[164:167]\n\ts_waitcnt lgkmcnt(8)\n\t" \
    "v_mfma_f32_16x16x32_bf16 v[168:171], a[32:35], v[24:27], v[168:171]\n\tds_read_b64_tr_b16 v[44:45], v7 offset:4096\n\tds_read_b64_tr_b16 v[46:47], v7 offset:6144\n\tv_mfma_f32_16x16x32_bf16 v[176:179], a[36:39], v[24:27], v[176:179]\n\tv_mfma_f32_16x16x32_bf16 v[184:187], a[40:43], v[24:27], v[184:187]\n\ts_waitcnt lgkmcnt(8)\n\tv_mfma_f32_16x16x32_bf16 v[172:175], a[32:35], v[28:31], v[172:175]\n\tds_read_b64_tr_b16 v[16:17], v224 offset:4096\n\t" \
    "ds_read_b64_tr_b16 v[18:19], v224 offset:6144\n\tv_mfma_f32_16x16x32_bf16 v[180:183], a[36:39], v[28:31], v[180:183]\n\tbuffer_load_dwordx4 v12, s[16:19], s69 offen lds\n\ts_mov_b32 s69, 2490368\n\ts_add_u32 m0, s54, 106496\n\tv_mfma_f32_16x16x32_bf16 v[188:191], a[40:43], v[28:31], v[188:191]\n\ts_waitcnt vmcnt(34) lgkmcnt(8)\n\tv_mfma_f32_16x16x32_bf16 v[120:123], v[84:87], v[32:35], v[120:123]\n\t" \
    "ds_read_b64_tr_b16 v[20:21], v225 offset:4096\n\tds_read_b64_tr_b16 v[22:23], v225 offset:6144\n\tv_mfma_f32_16x16x32_bf16 v[128:131], v[88:91], v[32:35], v[128:131]\n\tbuffer_load_dwordx4 v13, s[16:19], s69 offen lds\n\ts_mov_b32 s69, 2359296\n\ts_add_u32 m0, s54, 45056\n\tv_mfma_f32_16x16x32_bf16 v[136:139], v[92:95], v[32:35], v[136:139]\n\ts_waitcnt lgkmcnt(8)\n\t" \
    "v_mfma_f32_16x16x32_bf16 v[124:127], v[84:87], v[36:39], v[124:127]\n\tds_read_b64_tr_b16 v[24:25], v4 offset:8192\n\tds_read_b64_tr_b16 v[26:27], v4 offset:10240\n\tv_mfma_f32_16x16x32_bf16 v[132:135], v[88:91], v[36:39], v[132:135]\n\tv_mfma_f32_16x16x32_bf16 v[140:143], v[92:95], v[36:39], v[140:143]\n\ts_waitcnt lgkmcnt(8)\n\tv_mfma_f32_16x16x32_bf16 v[144:147], v[84:87], v[40:43], v[144:147]\n\tds_read_b64_tr_b16 v[28:29], v5 offset:8192\n\t" \
    "ds_read_b64_tr_b16 v[30:31], v5 offset:10240\n\tv_mfma_f32_16x16x32_bf16 v[152:155], v[88:91], v[40:43], v[152:155]\n\tbuffer_load_dwordx4 v12, s[16:19], s69 offen lds\n\ts_mov_b32 s69, 2359296\n\ts_add_u32 m0, s54, 110592\n\tv_mfma_f32_16x16x32_bf16 v[160:163], v[92:95], v[40:43], v[160:163]\n\ts_waitcnt lgkmcnt(8)\n\tv_mfma_f32_16x16x32_bf16 v[148:151], v[84:87], v[44:47], v[148:151]\n\t" \
    "ds_read_b64_tr_b16 v[32:33], v6 offset:8192\n\tds_read_b64_tr_b16 v[34:35], v6 offset:10240\n\tv_mfma_f32_16x16x32_bf16 v[156:159], v[88:91], v[44:47], v[156:159]\n\tbuffer_load_dwordx4 v13, s[16:19], s69 offen lds\n\ts_add_u32 s68, s46, 76800\n\tv_mfma_f32_16x16x32_bf16 v[164:167], v[92:95], v[44:47], v[164:167]\n\ts_waitcnt lgkmcnt(8)\n\tv_mfma_f32_16x16x32_bf16 v[168:171], v[84:87], v[16:19], v[168:171]\n\t" \
    "ds_read_b64_tr_b16 v[36:37], v7 offset:8192\n\tds_read_b64_tr_b16 v[38:39], v7 offset:10240\n\tv_mfma_f32_16x16x32_bf16 v[176:179], v[88:91], v[16:19], v[176:179]\n\tv_mfma_f32_16x16x32_bf16 v[184:187], v[92:95], v[16:19], v[184:187]\n\ts_waitcnt lgkmcnt(8)\n\tv_mfma_f32_16x16x32_bf16 v[172:175], v[84:87], v[20:23], v[172:175]\n\tds_read_b64_tr_b16 v[40:41], v224 offset:8192\n\tds_read_b64_tr_b16 v[42:43], v224 offset:10240\n\t" \
    "v_mfma_f32_16x16x32_bf16 v[180:183], v[88:91], v[20:23], v[180:183]\n\tbuffer_load_dwordx4 a[84:87], v8, s[8:11], s68 offen offset:0 sc1\n\tv_mfma_f32_16x16x32_bf16 v[188:191], v[92:95], v[20:23], v[188:191]\n\ts_waitcnt vmcnt(30) lgkmcnt(8)\n\tv_mfma_f32_16x16x32_bf16 v[120:123], v[96:99], v[24:27], v[120:123]\n\tds_read_b64_tr_b16 v[44:45], v225 offset:8192\n\tds_read_b64_tr_b16 v[46:47], v225 offset:10240\n\tv_mfma_f32_16x16x32_bf16 v[128:131], v[100:103], v[24:27], v[128:131]\n\t" \
    "v_mfma_f32_16x16x32_bf16 v[136:139], v[104:107], v[24:27], v[136:139]\n\ts_waitcnt lgkmcnt(8)\n\tv_mfma_f32_16x16x32_bf16 v[124:127], v[96:99], v[28:31], v[124:127]\n\tds_read_b64_tr_b16 v[16:17], v4 offset:12288\n\tds_read_b64_tr_b16 v[18:19], v4 offset:14336\n\tv_mfma_f32_16x16x32_bf16 v[132:135], v[100:103], v[28:31], v[132:135]\n\tbuffer_load_dwordx4 a[88:91], v8, s[8:11], s68 offen offset:1024 sc1\n\tv_mfma_f32_16x16x32_bf16 v[140:143], v[104:107], v[28:31], v[140:143]\n\t" \
    "s_waitcnt lgkmcnt(8)\n\tv_mfma_f32_16x16x32_bf16 v[144:147], v[96:99], v[32:35], v[144:147]\n\tds_read_b64_tr_b16 v[20:21], v5 offset:12288\n\tds_read_b64_tr_b16 v[22:23], v5 offset:14336\n\tv_mfma_f32_16x16x32_bf16 v[152:155], v[100:103], v[32:35], v[152:155]\n\tbuffer_load_dwordx4 a[92:95], v8, s[8:11], s68 offen offset:2048 sc1\n\ts_add_u32 s68, s46, 79872\n\tv_mfma_f32_16x16x32_bf16 v[160:163], v[104:107], v[32:35], v[160:163]\n\t" \
    "s_waitcnt lgkmcnt(8)\n\tv_mfma_f32_16x16x32_bf16 v[148:151], v[96:99], v[36:39], v[148:151]\n\tds_read_b64_tr_b16 v[24:25], v6 offset:12288\n\tds_read_b64_tr_b16 v[26:27], v6 offset:14336\n\tv_mfma_f32_16x16x32_bf16 v[156:159], v[100:103], v[36:39], v[156:159]\n\tv_mfma_f32_16x16x32_bf16 v[164:167], v[104:107], v[36:39], v[164:167]\n\ts_waitcnt lgkmcnt(8)\n\tv_mfma_f32_16x16x32_bf16 v[168:171], v[96:99], v[40:43], v[168:171]\n\t" \
    "ds_read_b64_tr_b16 v[28:29], v7 offset:12288\n\tds_read_b64_tr_b16 v[30:31], v7 offset:14336\n\tv_mfma_f32_16x16x32_bf16 v[176:179], v[100:103], v[40:43], v[176:179]\n\tbuffer_load_dwordx4 a[96:99], v8, s[8:11], s68 offen offset:0 sc1\n\tv_mfma_f32_16x16x32_bf16 v[184:187], v[104:107], v[40:43], v[184:187]\n\ts_waitcnt lgkmcnt(8)\n\tv_mfma_f32_16x16x32_bf16 v[172:175], v[96:99], v[44:47], v[172:175]\n\tds_read_b64_tr_b16 v[32:33], v224 offset:12288\n\t" \
    "ds_read_b64_tr_b16 v[34:35], v224 offset:14336\n\tv_mfma_f32_16x16x32_bf16 v[180:183], v[100:103], v[44:47], v[180:183]\n\tbuffer_load_dwordx4 a[100:103], v8, s[8:11], s68 offen offset:1024 sc1\n\tv_mfma_f32_16x16x32_bf16 v[188:191], v[104:107], v[44:47], v[188:191]\n\ts_barrier\n\ts_waitcnt vmcnt(31) lgkmcnt(8)\n\tv_mfma_f32_16x16x32_bf16 v[120:123], a[44:47], v[16:19], v[120:123]\n\tds_read_b64_tr_b16 v[36:37], v225 offset:12288\n\t" \
    "ds_read_b64_tr_b16 v[38:39], v225 offset:14336\n\tv_mfma_f32_16x16x32_bf16 v[128:131], a[48:51], v[16:19], v[128:131]\n\tbuffer_load_dwordx4 a[104:107], v8, s[8:11], s68 offen offset:2048 sc1\n\ts_mov_b32 s69, 3014656\n\ts_add_u32 m0, s54, 49152\n\tv_mfma_f32_16x16x32_bf16 v[136:139], a[52:55], v[16:19], v[136:139]\n\ts_waitcnt lgkmcnt(8)\n\tv_mfma_f32_16x16x32_bf16 v[124:127], a[44:47], v[20:23], v[124:127]\n\t" \
    "ds_read_b64_tr_b16 v[40:41], v4 offset:16384\n\tds_read_b64_tr_b16 v[42:43], v4 offset:18432\n\tv_mfma_f32_16x16x32_bf16 v[132:135], a[48:51], v[20:23], v[132:135]\n\tbuffer_load_dwordx4 v12, s[16:19], s69 offen lds\n\ts_mov_b32 s69, 3014656\n\ts_add_u32 m0, s54, 114688\n\tv_mfma_f32_16x16x32_bf16 v[140:143], a[52:55], v[20:23], v[140:143]\n\ts_waitcnt lgkmcnt(8)\n\t" \
    "v_mfma_f32_16x16x32_bf16 v[144:147], a[44:47], v[24:27], v[144:147]\n\tds_read_b64_tr_b16 v[44:45], v5 offset:16384\n\tds_read_b64_tr_b16 v[46:47], v5 offset:18432\n\tv_mfma_f32_16x16x32_bf16 v[152:155], a[48:51], v[24:27], v[152:155]\n\tv_mfma_f32_16x16x32_bf16 v[160:163], a[52:55], v[24:27], v[160:163]\n\ts_waitcnt lgkmcnt(8)\n\tv_mfma_f32_16x16x32_bf16 v[148:151], a[44:47], v[28:31], v[148:151]\n\tds_read_b64_tr_b16 v[16:17], v6 offset:16384\n\t" \
    "ds_read_b64_tr_b16 v[18:19], v6 offset:18432\n\tv_mfma_f32_16x16x32_bf16 v[156:159], a[48:51], v[28:31], v[156:159]\n\tbuffer_load_dwordx4 v13, s[16:19], s69 offen lds\n\ts_mov_b32 s69, 2883584\n\ts_add_u32 m0, s54, 53248\n\tv_mfma_f32_16x16x32_bf16 v[164:167], a[52:55], v[28:31], v[164:167]\n\ts_waitcnt lgkmcnt(8)\n\tv_mfma_f32_16x16x32_bf16 v[168:171], a[44:47], v[32:35], v[168:171]\n\t" \
    "ds_read_b64_tr_b16 v[20:21], v7 offset:16384\n\tds_read_b64_tr_b16 v[22:23], v7 offset:18432\n\tv_mfma_f32_16x16x32_bf16 v[176:179], a[48:51], v[32:35], v[176:179]\n\tv_mfma_f32_16x16x32_bf16 v[184:187], a[52:55], v[32:35], v[184:187]\n\ts_waitcnt lgkmcnt(8)\n\tv_mfma_f32_16x16x32_bf16 v[172:175], a[44:47], v[36:39], v[172:175]\n\tds_read_b64_tr_b16 v[24:25], v224 offset:16384\n\tds_read_b64_tr_b16 v[26:27], v224 offset:18432\n\t" \
    "v_mfma_f32_16x16x32_bf16 v[180:183], a[48:51], v[36:39], v[180:183]\n\tbuffer_load_dwordx4 v12, s[16:19], s69 offen lds\n\ts_mov_b32 s69, 2883584\n\ts_add_u32 m0, s54, 118784\n\tv_mfma_f32_16x16x32_bf16 v[188:191], a[52:55], v[36:39], v[188:191]\n\ts_waitcnt vmcnt(32) lgkmcnt(8)\n\tv_mfma_f32_16x16x32_bf16 v[120:123], a[0:3], v[40:43], v[120:123]\n\tds_read_b64_tr_b16 v[28:29], v225 offset:16384\n\t" \
    "ds_read_b64_tr_b16 v[30:31], v225 offset:18432\n\tv_mfma_f32_16x16x32_bf16 v[128:131], a[4:7], v[40:43], v[128:131]\n\tbuffer_load_dwordx4 v13, s[16:19], s69 offen lds\n\ts_mov_b32 s69, 3538944\n\ts_add_u32 m0, s54, 57344\n\tv_mfma_f32_16x16x32_bf16 v[136:139], a[56:59], v[40:43], v[136:139]\n\ts_waitcnt lgkmcnt(8)\n\tv_mfma_f32_16x16x32_bf16 v[124:127], a[0:3], v[44:47], v[124:127]\n\t" \
    "ds_read_b64_tr_b16 v[32:33], v4 offset:20480\n\tds_read_b64_tr_b16 v[34:35], v4 offset:22528\n\tv_mfma_f32_16x16x32_bf16 v[132:135], a[4:7], v[44:47], v[132:135]\n\tv_mfma_f32_16x16x32_bf16 v[140:143], a[56:59], v[44:47], v[140:143]\n\ts_waitcnt lgkmcnt(8)\n\tv_mfma_f32_16x16x32_bf16 v[144:147], a[0:3], v[16:19], v[144:147]\n\tds_read_b64_tr_b16 v[36:37], v5 offset:20480\n\tds_read_b64_tr_b16 v[38:39], v5 offset:22528\n\t" \
    "v_mfma_f32_16x16x32_bf16 v[152:155], a[4:7], v[16:19], v[152:155]\n\tbuffer_load_dwordx4 v12, s[16:19], s69 offen lds\n\ts_mov_b32 s69, 3407872\n\ts_add_u32 m0, s54, 122880\n\tv_mfma_f32_16x16x32_bf16 v[160:163], a[56:59], v[16:19], v[160:163]\n\ts_waitcnt lgkmcnt(8)\n\tv_mfma_f32_16x16x32_bf16 v[148:151], a[0:3], v[20:23], v[148:151]\n\tds_read_b64_tr_b16 v[40:41], v6 offset:20480\n\t" \
    "ds_read_b64_tr_b16 v[42:43], v6 offset:22528\n\tv_mfma_f32_16x16x32_bf16 v[156:159], a[4:7], v[20:23], v[156:159]\n\tbuffer_load_dwordx4 v12, s[16:19], s69 offen lds\n\ts_add_u32 s68, s46, 82944\n\tv_mfma_f32_16x16x32_bf16 v[164:167], a[56:59], v[20:23], v[164:167]\n\ts_waitcnt lgkmcnt(8)\n\tv_mfma_f32_16x16x32_bf16 v[168:171], a[0:3], v[24:27], v[168:171]\n\tds_read_b64_tr_b16 v[44:45], v7 offset:20480\n\t" \
    "ds_read_b64_tr_b16 v[46:47], v7 offset:22528\n\tv_mfma_f32_16x16x32_bf16 v[176:179], a[4:7], v[24:27], v[176:179]\n\tv_mfma_f32_16x16x32_bf16 v[184:187], a[56:59], v[24:27], v[184:187]\n\ts_waitcnt lgkmcnt(8)\n\tv_mfma_f32_16x16x32_bf16 v[172:175], a[0:3], v[28:31], v[172:175]\n\tds_read_b64_tr_b16 v[16:17], v224 offset:20480\n\tds_read_b64_tr_b16 v[18:19], v224 offset:22528\n\tv_mfma_f32_16x16x32_bf16 v[180:183], a[4:7], v[28:31], v[180:183]\n\t" \
    "buffer_load_dwordx4 a[108:111], v8, s[8:11], s68 offen offset:0 sc1\n\tv_mfma_f32_16x16x32_bf16 v[188:191], a[56:59], v[28:31], v[188:191]\n\ts_waitcnt vmcnt(33) lgkmcnt(8)\n\tv_mfma_f32_16x16x32_bf16 v[120:123], a[60:63], v[32:35], v[120:123]\n\tds_read_b64_tr_b16 v[20:21], v225 offset:20480\n\tds_read_b64_tr_b16 v[22:23], v225 offset:22528\n\tv_mfma_f32_16x16x32_bf16 v[128:131], a[64:67], v[32:35], v[128:131]\n\tbuffer_load_dwordx4 a[112:115], v8, s[8:11], s68 offen offset:1024 sc1\n\t" \
    "v_mfma_f32_16x16x32_bf16 v[136:139], a[68:71], v[32:35], v[136:139]\n\ts_waitcnt lgkmcnt(8)\n\tv_mfma_f32_16x16x32_bf16 v[124:127], a[60:63], v[36:39], v[124:127]\n\tds_read_b64_tr_b16 v[24:25], v4 offset:24576\n\tds_read_b64_tr_b16 v[26:27], v4 offset:26624\n\tv_mfma_f32_16x16x32_bf16 v[132:135], a[64:67], v[36:39], v[132:135]\n\tv_mfma_f32_16x16x32_bf16 v[140:143], a[68:71], v[36:39], v[140:143]\n\ts_waitcnt lgkmcnt(8)\n\t" \
    "v_mfma_f32_16x16x32_bf16 v[144:147], a[60:63], v[40:43], v[144:147]\n\tds_read_b64_tr_b16 v[28:29], v5 offset:24576\n\tds_read_b64_tr_b16 v[30:31], v5 offset:26624\n\tv_mfma_f32_16x16x32_bf16 v[152:155], a[64:67], v[40:43], v[152:155]\n\tbuffer_load_dwordx4 a[116:119], v8, s[8:11], s68 offen offset:2048 sc1\n\ts_add_u32 s68, s46, 86016\n\tv_mfma_f32_16x16x32_bf16 v[160:163], a[68:71], v[40:43], v[160:163]\n\ts_waitcnt lgkmcnt(8)\n\t" \
    "v_mfma_f32_16x16x32_bf16 v[148:151], a[60:63], v[44:47], v[148:151]\n\tds_read_b64_tr_b16 v[32:33], v6 offset:24576\n\tds_read_b64_tr_b16 v[34:35], v6 offset:26624\n\tv_mfma_f32_16x16x32_bf16 v[156:159], a[64:67], v[44:47], v[156:159]\n\tbuffer_load_dwordx4 a[120:123], v8, s[8:11], s68 offen offset:0 sc1\n\tv_mfma_f32_16x16x32_bf16 v[164:167], a[68:71], v[44:47], v[164:167]\n\ts_waitcnt vmcnt(30)\n\ts_barrier\n\t" \
    "s_waitcnt lgkmcnt(8)\n\tv_mfma_f32_16x16x32_bf16 v[168:171], a[60:63], v[16:19], v[168:171]\n\tds_read_b64_tr_b16 v[36:37], v7 offset:24576\n\tds_read_b64_tr_b16 v[38:39], v7 offset:26624\n\tv_mfma_f32_16x16x32_bf16 v[176:179], a[64:67], v[16:19], v[176:179]\n\tbuffer_load_dwordx4 a[124:127], v8, s[8:11], s68 offen offset:1024 sc1\n\tv_mfma_f32_16x16x32_bf16 v[184:187], a[68:71], v[16:19], v[184:187]\n\ts_waitcnt lgkmcnt(8)\n\t" \
    "v_mfma_f32_16x16x32_bf16 v[172:175], a[60:63], v[20:23], v[172:175]\n\tds_read_b64_tr_b16 v[40:41], v224 offset:24576\n\tds_read_b64_tr_b16 v[42:43], v224 offset:26624\n\tv_mfma_f32_16x16x32_bf16 v[180:183], a[64:67], v[20:23], v[180:183]\n\tv_mfma_f32_16x16x32_bf16 v[188:191], a[68:71], v[20:23], v[188:191]\n\ts_waitcnt vmcnt(28) lgkmcnt(8)\n\tv_mfma_f32_16x16x32_bf16 v[120:123], a[8:11], v[24:27], v[120:123]\n\tds_read_b64_tr_b16 v[44:45], v225 offset:24576\n\t" \
    "ds_read_b64_tr_b16 v[46:47], v225 offset:26624\n\tv_mfma_f32_16x16x32_bf16 v[128:131], a[12:15], v[24:27], v[128:131]\n\tbuffer_load_dwordx4 a[128:131], v8, s[8:11], s68 offen offset:2048 sc1\n\ts_add_u32 s68, s46, 89088\n\tv_mfma_f32_16x16x32_bf16 v[136:139], a[16:19], v[24:27], v[136:139]\n\ts_waitcnt lgkmcnt(8)\n\tv_mfma_f32_16x16x32_bf16 v[124:127], a[8:11], v[28:31], v[124:127]\n\tds_read_b64_tr_b16 v[16:17], v4 offset:28672\n\t" \
    "ds_read_b64_tr_b16 v[18:19], v4 offset:30720\n\tv_mfma_f32_16x16x32_bf16 v[132:135], a[12:15], v[28:31], v[132:135]\n\tbuffer_load_dwordx4 a[132:135], v8, s[8:11], s68 offen offset:0 sc1\n\tv_mfma_f32_16x16x32_bf16 v[140:143], a[16:19], v[28:31], v[140:143]\n\ts_waitcnt lgkmcnt(8)\n\tv_mfma_f32_16x16x32_bf16 v[144:147], a[8:11], v[32:35], v[144:147]\n\tds_read_b64_tr_b16 v[20:21], v5 offset:28672\n\tds_read_b64_tr_b16 v[22:23], v5 offset:30720\n\t" \
    "v_mfma_f32_16x16x32_bf16 v[152:155], a[12:15], v[32:35], v[152:155]\n\tv_mfma_f32_16x16x32_bf16 v[160:163], a[16:19], v[32:35], v[160:163]\n\ts_waitcnt lgkmcnt(8)\n\tv_mfma_f32_16x16x32_bf16 v[148:151], a[8:11], v[36:39], v[148:151]\n\tds_read_b64_tr_b16 v[24:25], v6 offset:28672\n\tds_read_b64_tr_b16 v[26:27], v6 offset:30720\n\tv_mfma_f32_16x16x32_bf16 v[156:159], a[12:15], v[36:39], v[156:159]\n\tbuffer_load_dwordx4 a[136:139], v8, s[8:11], s68 offen offset:1024 sc1\n\t" \
    "v_mfma_f32_16x16x32_bf16 v[164:167], a[16:19], v[36:39], v[164:167]\n\ts_waitcnt lgkmcnt(8)\n\tv_mfma_f32_16x16x32_bf16 v[168:171], a[8:11], v[40:43], v[168:171]\n\tds_read_b64_tr_b16 v[28:29], v7 offset:28672\n\tds_read_b64_tr_b16 v[30:31], v7 offset:30720\n\tv_mfma_f32_16x16x32_bf16 v[176:179], a[12:15], v[40:43], v[176:179]\n\tbuffer_load_dwordx4 a[140:143], v8, s[8:11], s68 offen offset:2048 sc1\n\ts_mov_b32 s69, 4063232\n\t" \
    "s_add_u32 m0, s54, 61440\n\tv_mfma_f32_16x16x32_bf16 v[184:187], a[16:19], v[40:43], v[184:187]\n\ts_waitcnt lgkmcnt(8)\n\tv_mfma_f32_16x16x32_bf16 v[172:175], a[8:11], v[44:47], v[172:175]\n\tds_read_b64_tr_b16 v[32:33], v224 offset:28672\n\tds_read_b64_tr_b16 v[34:35], v224 offset:30720\n\tv_mfma_f32_16x16x32_bf16 v[180:183], a[12:15], v[44:47], v[180:183]\n\tv_mfma_f32_16x16x32_bf16 v[188:191], a[16:19], v[44:47], v[188:191]\n\t" \
    "s_waitcnt vmcnt(29) lgkmcnt(8)\n\tv_mfma_f32_16x16x32_bf16 v[120:123], a[20:23], v[16:19], v[120:123]\n\tds_read_b64_tr_b16 v[36:37], v225 offset:28672\n\tds_read_b64_tr_b16 v[38:39], v225 offset:30720\n\tv_mfma_f32_16x16x32_bf16 v[128:131], a[24:27], v[16:19], v[128:131]\n\tbuffer_load_dwordx4 v12, s[16:19], s69 offen lds\n\ts_mov_b32 s69, 3932160\n\ts_add_u32 m0, s54, 0\n\t" \
    "v_mfma_f32_16x16x32_bf16 v[136:139], a[28:31], v[16:19], v[136:139]\n\ts_waitcnt lgkmcnt(8)\n\tv_mfma_f32_16x16x32_bf16 v[124:127], a[20:23], v[20:23], v[124:127]\n\tds_read_b64_tr_b16 v[40:41], v4 offset:32768\n\tds_read_b64_tr_b16 v[42:43], v4 offset:34816\n\tv_mfma_f32_16x16x32_bf16 v[132:135], a[24:27], v[20:23], v[132:135]\n\tbuffer_load_dwordx4 v12, s[16:19], s69 offen lds\n\ts_mov_b32 s69, 4587520\n\t" \
    "s_add_u32 m0, s54, 4096\n\tv_mfma_f32_16x16x32_bf16 v[140:143], a[28:31], v[20:23], v[140:143]\n\ts_waitcnt lgkmcnt(8)\n\tv_mfma_f32_16x16x32_bf16 v[144:147], a[20:23], v[24:27], v[144:147]\n\tds_read_b64_tr_b16 v[44:45], v5 offset:32768\n\tds_read_b64_tr_b16 v[46:47], v5 offset:34816\n\tv_mfma_f32_16x16x32_bf16 v[152:155], a[24:27], v[24:27], v[152:155]\n\tv_mfma_f32_16x16x32_bf16 v[160:163], a[28:31], v[24:27], v[160:163]\n\t" \
    "s_waitcnt lgkmcnt(8)\n\tv_mfma_f32_16x16x32_bf16 v[148:151], a[20:23], v[28:31], v[148:151]\n\tds_read_b64_tr_b16 v[16:17], v6 offset:32768\n\tds_read_b64_tr_b16 v[18:19], v6 offset:34816\n\tv_mfma_f32_16x16x32_bf16 v[156:159], a[24:27], v[28:31], v[156:159]\n\tbuffer_load_dwordx4 v12, s[16:19], s69 offen lds\n\ts_mov_b32 s69, 4456448\n\ts_add_u32 m0, s54, 8192\n\t" \
    "v_mfma_f32_16x16x32_bf16 v[164:167], a[28:31], v[28:31], v[164:167]\n\ts_waitcnt lgkmcnt(8)\n\tv_mfma_f32_16x16x32_bf16 v[168:171], a[20:23], v[32:35], v[168:171]\n\tds_read_b64_tr_b16 v[20:21], v7 offset:32768\n\tds_read_b64_tr_b16 v[22:23], v7 offset:34816\n\tv_mfma_f32_16x16x32_bf16 v[176:179], a[24:27], v[32:35], v[176:179]\n\tbuffer_load_dwordx4 v12, s[16:19], s69 offen lds\n\ts_add_u32 s68, s46, 92160\n\t" \
    "v_mfma_f32_16x16x32_bf16 v[184:187], a[28:31], v[32:35], v[184:187]\n\ts_waitcnt lgkmcnt(8)\n\tv_mfma_f32_16x16x32_bf16 v[172:175], a[20:23], v[36:39], v[172:175]\n\tds_read_b64_tr_b16 v[24:25], v224 offset:32768\n\tds_read_b64_tr_b16 v[26:27], v224 offset:34816\n\tv_mfma_f32_16x16x32_bf16 v[180:183], a[24:27], v[36:39], v[180:183]\n\tv_mfma_f32_16x16x32_bf16 v[188:191], a[28:31], v[36:39], v[188:191]\n\ts_waitcnt vmcnt(30) lgkmcnt(8)\n\t" \
    "v_mfma_f32_16x16x32_bf16 v[120:123], a[72:75], v[40:43], v[120:123]\n\tds_read_b64_tr_b16 v[28:29], v225 offset:32768\n\tds_read_b64_tr_b16 v[30:31], v225 offset:34816\n\tv_mfma_f32_16x16x32_bf16 v[128:131], a[76:79], v[40:43], v[128:131]\n\tbuffer_load_dwordx4 a[144:147], v8, s[8:11], s68 offen offset:0 sc1\n\tv_mfma_f32_16x16x32_bf16 v[136:139], a[80:83], v[40:43], v[136:139]\n\ts_waitcnt lgkmcnt(8)\n\tv_mfma_f32_16x16x32_bf16 v[124:127], a[72:75], v[44:47], v[124:127]\n\t" \
    "ds_read_b64_tr_b16 v[32:33], v4 offset:36864\n\tds_read_b64_tr_b16 v[34:35], v4 offset:38912\n\tv_mfma_f32_16x16x32_bf16 v[132:135], a[76:79], v[44:47], v[132:135]\n\tbuffer_load_dwordx4 a[148:151], v8, s[8:11], s68 offen offset:1024 sc1\n\tv_mfma_f32_16x16x32_bf16 v[140:143], a[80:83], v[44:47], v[140:143]\n\ts_waitcnt vmcnt(27)\n\ts_barrier\n\ts_waitcnt lgkmcnt(8)\n\t" \
    "v_mfma_f32_16x16x32_bf16 v[144:147], a[72:75], v[16:19], v[144:147]\n\tds_read_b64_tr_b16 v[36:37], v5 offset:36864\n\tds_read_b64_tr_b16 v[38:39], v5 offset:38912\n\tv_mfma_f32_16x16x32_bf16 v[152:155], a[76:79], v[16:19], v[152:155]\n\tbuffer_load_dwordx4 a[152:155], v8, s[8:11], s68 offen offset:2048 sc1\n\ts_add_u32 s68, s46, 95232\n\tv_mfma_f32_16x16x32_bf16 v[160:163], a[80:83], v[16:19], v[160:163]\n\ts_waitcnt lgkmcnt(8)\n\t" \
    "v_mfma_f32_16x16x32_bf16 v[148:151], a[72:75], v[20:23], v[148:151]\n\tds_read_b64_tr_b16 v[40:41], v6 offset:36864\n\tds_read_b64_tr_b16 v[42:43], v6 offset:38912\n\tv_mfma_f32_16x16x32_bf16 v[156:159], a[76:79], v[20:23], v[156:159]\n\tv_mfma_f32_16x16x32_bf16 v[164:167], a[80:83], v[20:23], v[164:167]\n\ts_waitcnt lgkmcnt(8)\n\tv_mfma_f32_16x16x32_bf16 v[168:171], a[72:75], v[24:27], v[168:171]\n\tds_read_b64_tr_b16 v[44:45], v7 offset:36864\n\t" \
    "ds_read_b64_tr_b16 v[46:47], v7 offset:38912\n\tv_mfma_f32_16x16x32_bf16 v[176:179], a[76:79], v[24:27], v[176:179]\n\tbuffer_load_dwordx4 a[156:159], v8, s[8:11], s68 offen offset:0 sc1\n\tv_mfma_f32_16x16x32_bf16 v[184:187], a[80:83], v[24:27], v[184:187]\n\ts_waitcnt lgkmcnt(8)\n\tv_mfma_f32_16x16x32_bf16 v[172:175], a[72:75], v[28:31], v[172:175]\n\tds_read_b64_tr_b16 v[16:17], v224 offset:36864\n\tds_read_b64_tr_b16 v[18:19], v224 offset:38912\n\t" \
    "v_mfma_f32_16x16x32_bf16 v[180:183], a[76:79], v[28:31], v[180:183]\n\tbuffer_load_dwordx4 a[160:163], v8, s[8:11], s68 offen offset:1024 sc1\n\tv_mfma_f32_16x16x32_bf16 v[188:191], a[80:83], v[28:31], v[188:191]\n\ts_waitcnt vmcnt(27) lgkmcnt(8)\n\tv_mfma_f32_16x16x32_bf16 v[120:123], a[84:87], v[32:35], v[120:123]\n\tds_read_b64_tr_b16 v[20:21], v225 offset:36864\n\tds_read_b64_tr_b16 v[22:23], v225 offset:38912\n\tv_mfma_f32_16x16x32_bf16 v[128:131], a[88:91], v[32:35], v[128:131]\n\t" \
    "v_mfma_f32_16x16x32_bf16 v[136:139], a[92:95], v[32:35], v[136:139]\n\ts_waitcnt lgkmcnt(8)\n\tv_mfma_f32_16x16x32_bf16 v[124:127], a[84:87], v[36:39], v[124:127]\n\tds_read_b64_tr_b16 v[24:25], v4 offset:40960\n\tds_read_b64_tr_b16 v[26:27], v4 offset:43008\n\tv_mfma_f32_16x16x32_bf16 v[132:135], a[88:91], v[36:39], v[132:135]\n\tbuffer_load_dwordx4 a[164:167], v8, s[8:11], s68 offen offset:2048 sc1\n\ts_add_u32 s68, s46, 98304\n\t" \
    "v_mfma_f32_16x16x32_bf16 v[140:143], a[92:95], v[36:39], v[140:143]\n\ts_waitcnt lgkmcnt(8)\n\tv_mfma_f32_16x16x32_bf16 v[144:147], a[84:87], v[40:43], v[144:147]\n\tds_read_b64_tr_b16 v[28:29], v5 offset:40960\n\tds_read_b64_tr_b16 v[30:31], v5 offset:43008\n\tv_mfma_f32_16x16x32_bf16 v[152:155], a[88:91], v[40:43], v[152:155]\n\tbuffer_load_dwordx4 a[168:171], v8, s[8:11], s68 offen offset:0 sc1\n\tv_mfma_f32_16x16x32_bf16 v[160:163], a[92:95], v[40:43], v[160:163]\n\t" \
    "s_waitcnt lgkmcnt(8)\n\tv_mfma_f32_16x16x32_bf16 v[148:151], a[84:87], v[44:47], v[148:151]\n\tds_read_b64_tr_b16 v[32:33], v6 offset:40960\n\tds_read_b64_tr_b16 v[34:35], v6 offset:43008\n\tv_mfma_f32_16x16x32_bf16 v[156:159], a[88:91], v[44:47], v[156:159]\n\tv_mfma_f32_16x16x32_bf16 v[164:167], a[92:95], v[44:47], v[164:167]\n\ts_waitcnt lgkmcnt(8)\n\tv_mfma_f32_16x16x32_bf16 v[168:171], a[84:87], v[16:19], v[168:171]\n\t" \
    "ds_read_b64_tr_b16 v[36:37], v7 offset:40960\n\tds_read_b64_tr_b16 v[38:39], v7 offset:43008\n\tv_mfma_f32_16x16x32_bf16 v[176:179], a[88:91], v[16:19], v[176:179]\n\tbuffer_load_dwordx4 a[172:175], v8, s[8:11], s68 offen offset:1024 sc1\n\tv_mfma_f32_16x16x32_bf16 v[184:187], a[92:95], v[16:19], v[184:187]\n\ts_waitcnt lgkmcnt(8)\n\tv_mfma_f32_16x16x32_bf16 v[172:175], a[84:87], v[20:23], v[172:175]\n\tds_read_b64_tr_b16 v[40:41], v224 offset:40960\n\t" \
    "ds_read_b64_tr_b16 v[42:43], v224 offset:43008\n\tv_mfma_f32_16x16x32_bf16 v[180:183], a[88:91], v[20:23], v[180:183]\n\tbuffer_load_dwordx4 a[176:179], v8, s[8:11], s68 offen offset:2048 sc1\n\ts_add_u32 s68, s46, 101376\n\tv_mfma_f32_16x16x32_bf16 v[188:191], a[92:95], v[20:23], v[188:191]\n\ts_waitcnt vmcnt(28) lgkmcnt(8)\n\tv_mfma_f32_16x16x32_bf16 v[120:123], a[96:99], v[24:27], v[120:123]\n\tds_read_b64_tr_b16 v[44:45], v225 offset:40960\n\t" \
    "ds_read_b64_tr_b16 v[46:47], v225 offset:43008\n\tv_mfma_f32_16x16x32_bf16 v[128:131], a[100:103], v[24:27], v[128:131]\n\tv_mfma_f32_16x16x32_bf16 v[136:139], a[104:107], v[24:27], v[136:139]\n\ts_waitcnt lgkmcnt(8)\n\tv_mfma_f32_16x16x32_bf16 v[124:127], a[96:99], v[28:31], v[124:127]\n\tds_read_b64_tr_b16 v[16:17], v4 offset:45056\n\tds_read_b64_tr_b16 v[18:19], v4 offset:47104\n\tv_mfma_f32_16x16x32_bf16 v[132:135], a[100:103], v[28:31], v[132:135]\n\t" \
    "buffer_load_dwordx4 a[180:183], v8, s[8:11], s68 offen offset:0 sc1\n\tv_mfma_f32_16x16x32_bf16 v[140:143], a[104:107], v[28:31], v[140:143]\n\ts_waitcnt lgkmcnt(8)\n\tv_mfma_f32_16x16x32_bf16 v[144:147], a[96:99], v[32:35], v[144:147]\n\tds_read_b64_tr_b16 v[20:21], v5 offset:45056\n\tds_read_b64_tr_b16 v[22:23], v5 offset:47104\n\tv_mfma_f32_16x16x32_bf16 v[152:155], a[100:103], v[32:35], v[152:155]\n\tbuffer_load_dwordx4 a[184:187], v8, s[8:11], s68 offen offset:1024 sc1\n\t" \
    "v_mfma_f32_16x16x32_bf16 v[160:163], a[104:107], v[32:35], v[160:163]\n\ts_waitcnt lgkmcnt(8)\n\tv_mfma_f32_16x16x32_bf16 v[148:151], a[96:99], v[36:39], v[148:151]\n\tds_read_b64_tr_b16 v[24:25], v6 offset:45056\n\tds_read_b64_tr_b16 v[26:27], v6 offset:47104\n\tv_mfma_f32_16x16x32_bf16 v[156:159], a[100:103], v[36:39], v[156:159]\n\tv_mfma_f32_16x16x32_bf16 v[164:167], a[104:107], v[36:39], v[164:167]\n\ts_waitcnt lgkmcnt(8)\n\t" \
    "v_mfma_f32_16x16x32_bf16 v[168:171], a[96:99], v[40:43], v[168:171]\n\tds_read_b64_tr_b16 v[28:29], v7 offset:45056\n\tds_read_b64_tr_b16 v[30:31], v7 offset:47104\n\tv_mfma_f32_16x16x32_bf16 v[176:179], a[100:103], v[40:43], v[176:179]\n\tbuffer_load_dwordx4 a[188:191], v8, s[8:11], s68 offen offset:2048 sc1\n\ts_add_u32 s68, s46, 104448\n\tv_mfma_f32_16x16x32_bf16 v[184:187], a[104:107], v[40:43], v[184:187]\n\ts_waitcnt lgkmcnt(8)\n\t" \
    "v_mfma_f32_16x16x32_bf16 v[172:175], a[96:99], v[44:47], v[172:175]\n\tds_read_b64_tr_b16 v[32:33], v224 offset:45056\n\tds_read_b64_tr_b16 v[34:35], v224 offset:47104\n\tv_mfma_f32_16x16x32_bf16 v[180:183], a[100:103], v[44:47], v[180:183]\n\tv_mfma_f32_16x16x32_bf16 v[188:191], a[104:107], v[44:47], v[188:191]\n\ts_waitcnt vmcnt(25)\n\ts_barrier\n\ts_waitcnt vmcnt(22) lgkmcnt(8)\n\t" \
    "v_mfma_f32_16x16x32_bf16 v[120:123], a[108:111], v[16:19], v[120:123]\n\tds_read_b64_tr_b16 v[36:37], v225 offset:45056\n\tds_read_b64_tr_b16 v[38:39], v225 offset:47104\n\tv_mfma_f32_16x16x32_bf16 v[128:131], a[112:115], v[16:19], v[128:131]\n\tbuffer_load_dwordx4 a[192:195], v8, s[8:11], s68 offen offset:0 sc1\n\tv_mfma_f32_16x16x32_bf16 v[136:139], a[116:119], v[16:19], v[136:139]\n\ts_waitcnt lgkmcnt(8)\n\tv_mfma_f32_16x16x32_bf16 v[124:127], a[108:111], v[20:23], v[124:127]\n\t" \
    "ds_read_b64_tr_b16 v[40:41], v4 offset:49152\n\tds_read_b64_tr_b16 v[42:43], v4 offset:51200\n\tv_mfma_f32_16x16x32_bf16 v[132:135], a[112:115], v[20:23], v[132:135]\n\tbuffer_load_dwordx4 a[196:199], v8, s[8:11], s68 offen offset:1024 sc1\n\tv_mfma_f32_16x16x32_bf16 v[140:143], a[116:119], v[20:23], v[140:143]\n\ts_waitcnt lgkmcnt(8)\n\tv_mfma_f32_16x16x32_bf16 v[144:147], a[108:111], v[24:27], v[144:147]\n\tds_read_b64_tr_b16 v[44:45], v5 offset:49152\n\t" \
    "ds_read_b64_tr_b16 v[46:47], v5 offset:51200\n\tv_mfma_f32_16x16x32_bf16 v[152:155], a[112:115], v[24:27], v[152:155]\n\tbuffer_load_dwordx4 a[200:203], v8, s[8:11], s68 offen offset:2048 sc1\n\ts_add_u32 s68, s46, 107520\n\tv_mfma_f32_16x16x32_bf16 v[160:163], a[116:119], v[24:27], v[160:163]\n\ts_waitcnt lgkmcnt(8)\n\tv_mfma_f32_16x16x32_bf16 v[148:151], a[108:111], v[28:31], v[148:151]\n\tds_read_b64_tr_b16 v[16:17], v6 offset:49152\n\t" \
    "ds_read_b64_tr_b16 v[18:19], v6 offset:51200\n\tv_mfma_f32_16x16x32_bf16 v[156:159], a[112:115], v[28:31], v[156:159]\n\tbuffer_load_dwordx4 a[204:207], v8, s[8:11], s68 offen offset:0 sc1\n\tv_mfma_f32_16x16x32_bf16 v[164:167], a[116:119], v[28:31], v[164:167]\n\tv_min_u32 v241, 376, v239\n\tbuffer_load_dwordx2 v[250:251], v241, s[8:11], s51 offen sc1\n\ts_waitcnt lgkmcnt(8)\n\tv_mfma_f32_16x16x32_bf16 v[168:171], a[108:111], v[32:35], v[168:171]\n\t" \
    "ds_read_b64_tr_b16 v[20:21], v7 offset:49152\n\tds_read_b64_tr_b16 v[22:23], v7 offset:51200\n\tv_mfma_f32_16x16x32_bf16 v[176:179], a[112:115], v[32:35], v[176:179]\n\tv_mfma_f32_16x16x32_bf16 v[184:187], a[116:119], v[32:35], v[184:187]\n\ts_waitcnt lgkmcnt(8)\n\tv_mfma_f32_16x16x32_bf16 v[172:175], a[108:111], v[36:39], v[172:175]\n\tds_read_b64_tr_b16 v[24:25], v224 offset:49152\n\tds_read_b64_tr_b16 v[26:27], v224 offset:51200\n\t" \
    "v_mfma_f32_16x16x32_bf16 v[180:183], a[112:115], v[36:39], v[180:183]\n\tv_mfma_f32_16x16x32_bf16 v[188:191], a[116:119], v[36:39], v[188:191]\n\ts_waitcnt vmcnt(24) lgkmcnt(8)\n\tv_mfma_f32_16x16x32_bf16 v[120:123], a[120:123], v[40:43], v[120:123]\n\tds_read_b64_tr_b16 v[28:29], v225 offset:49152\n\tds_read_b64_tr_b16 v[30:31], v225 offset:51200\n\tv_mfma_f32_16x16x32_bf16 v[128:131], a[124:127], v[40:43], v[128:131]\n\tbuffer_load_dwordx4 a[208:211], v8, s[8:11], s68 offen offset:1024 sc1\n\t" \
    "v_mfma_f32_16x16x32_bf16 v[136:139], a[128:131], v[40:43], v[136:139]\n\ts_waitcnt lgkmcnt(8)\n\tv_mfma_f32_16x16x32_bf16 v[124:127], a[120:123], v[44:47], v[124:127]\n\tds_read_b64_tr_b16 v[32:33], v4 offset:53248\n\tds_read_b64_tr_b16 v[34:35], v4 offset:55296\n\tv_mfma_f32_16x16x32_bf16 v[132:135], a[124:127], v[44:47], v[132:135]\n\tv_mfma_f32_16x16x32_bf16 v[140:143], a[128:131], v[44:47], v[140:143]\n\ts_waitcnt lgkmcnt(8)\n\t" \
    "v_mfma_f32_16x16x32_bf16 v[144:147], a[120:123], v[16:19], v[144:147]\n\tds_read_b64_tr_b16 v[36:37], v5 offset:53248\n\tds_read_b64_tr_b16 v[38:39], v5 offset:55296\n\tv_mfma_f32_16x16x32_bf16 v[152:155], a[124:127], v[16:19], v[152:155]\n\tbuffer_load_dwordx4 a[212:215], v8, s[8:11], s68 offen offset:2048 sc1\n\tv_mfma_f32_16x16x32_bf16 v[160:163], a[128:131], v[16:19], v[160:163]\n\ts_waitcnt lgkmcnt(8)\n\tv_mfma_f32_16x16x32_bf16 v[148:151], a[120:123], v[20:23], v[148:151]\n\t" \
    "ds_read_b64_tr_b16 v[40:41], v6 offset:53248\n\tds_read_b64_tr_b16 v[42:43], v6 offset:55296\n\tv_mfma_f32_16x16x32_bf16 v[156:159], a[124:127], v[20:23], v[156:159]\n\tv_mfma_f32_16x16x32_bf16 v[164:167], a[128:131], v[20:23], v[164:167]\n\ts_waitcnt lgkmcnt(8)\n\tv_mfma_f32_16x16x32_bf16 v[168:171], a[120:123], v[24:27], v[168:171]\n\tds_read_b64_tr_b16 v[44:45], v7 offset:53248\n\tds_read_b64_tr_b16 v[46:47], v7 offset:55296\n\t" \
    "v_mfma_f32_16x16x32_bf16 v[176:179], a[124:127], v[24:27], v[176:179]\n\tbuffer_load_dwordx4 v[192:195], v243, s[20:23], 0 offen offset:0\n\tv_mfma_f32_16x16x32_bf16 v[184:187], a[128:131], v[24:27], v[184:187]\n\ts_waitcnt lgkmcnt(8)\n\tv_mfma_f32_16x16x32_bf16 v[172:175], a[120:123], v[28:31], v[172:175]\n\tds_read_b64_tr_b16 v[16:17], v224 offset:53248\n\tds_read_b64_tr_b16 v[18:19], v224 offset:55296\n\tv_mfma_f32_16x16x32_bf16 v[180:183], a[124:127], v[28:31], v[180:183]\n\t" \
    "buffer_load_dwordx4 v[196:199], v243, s[20:23], 0 offen offset:32\n\tv_mfma_f32_16x16x32_bf16 v[188:191], a[128:131], v[28:31], v[188:191]\n\ts_waitcnt vmcnt(25) lgkmcnt(8)\n\tv_mfma_f32_16x16x32_bf16 v[120:123], a[132:135], v[32:35], v[120:123]\n\tds_read_b64_tr_b16 v[20:21], v225 offset:53248\n\tds_read_b64_tr_b16 v[22:23], v225 offset:55296\n\tv_mfma_f32_16x16x32_bf16 v[128:131], a[136:139], v[32:35], v[128:131]\n\tv_mfma_f32_16x16x32_bf16 v[136:139], a[140:143], v[32:35], v[136:139]\n\t" \
    "s_waitcnt lgkmcnt(8)\n\tv_mfma_f32_16x16x32_bf16 v[124:127], a[132:135], v[36:39], v[124:127]\n\tds_read_b64_tr_b16 v[24:25], v4 offset:57344\n\tds_read_b64_tr_b16 v[26:27], v4 offset:59392\n\tv_mfma_f32_16x16x32_bf16 v[132:135], a[136:139], v[36:39], v[132:135]\n\tbuffer_load_dwordx4 v[200:203], v243, s[20:23], 0 offen offset:64\n\tv_mfma_f32_16x16x32_bf16 v[140:143], a[140:143], v[36:39], v[140:143]\n\ts_waitcnt lgkmcnt(8)\n\t" \
    "v_mfma_f32_16x16x32_bf16 v[144:147], a[132:135], v[40:43], v[144:147]\n\tds_read_b64_tr_b16 v[28:29], v5 offset:57344\n\tds_read_b64_tr_b16 v[30:31], v5 offset:59392\n\tv_mfma_f32_16x16x32_bf16 v[152:155], a[136:139], v[40:43], v[152:155]\n\tbuffer_load_dwordx2 v[216:217], v242, s[8:11], s59 offen offset:0 sc1\n\tv_mfma_f32_16x16x32_bf16 v[160:163], a[140:143], v[40:43], v[160:163]\n\ts_waitcnt lgkmcnt(8)\n\tv_mfma_f32_16x16x32_bf16 v[148:151], a[132:135], v[44:47], v[148:151]\n\t" \
    "ds_read_b64_tr_b16 v[32:33], v224 offset:57344\n\tds_read_b64_tr_b16 v[34:35], v224 offset:59392\n\tv_mfma_f32_16x16x32_bf16 v[156:159], a[136:139], v[44:47], v[156:159]\n\tv_mfma_f32_16x16x32_bf16 v[164:167], a[140:143], v[44:47], v[164:167]\n\ts_waitcnt vmcnt(23)\n\ts_barrier\n\ts_waitcnt lgkmcnt(8)\n\tv_mfma_f32_16x16x32_bf16 v[168:171], a[132:135], v[16:19], v[168:171]\n\t" \
    "ds_read_b64_tr_b16 v[36:37], v225 offset:57344\n\tds_read_b64_tr_b16 v[38:39], v225 offset:59392\n\tv_mfma_f32_16x16x32_bf16 v[176:179], a[136:139], v[16:19], v[176:179]\n\tbuffer_load_dwordx2 v[218:219], v242, s[8:11], s59 offen offset:128 sc1\n\ts_mov_b32 s69, 3538944\n\ts_add_u32 m0, s54, 12288\n\tv_mfma_f32_16x16x32_bf16 v[184:187], a[140:143], v[16:19], v[184:187]\n\ts_waitcnt lgkmcnt(8)\n\t" \
    "v_mfma_f32_16x16x32_bf16 v[172:175], a[132:135], v[20:23], v[172:175]\n\tds_read_b64_tr_b16 v[40:41], v4 offset:61440\n\tds_read_b64_tr_b16 v[42:43], v4 offset:63488\n\tv_mfma_f32_16x16x32_bf16 v[180:183], a[136:139], v[20:23], v[180:183]\n\tbuffer_load_dwordx4 v13, s[16:19], s69 offen lds\n\ts_mov_b32 s69, 3407872\n\ts_add_u32 m0, s54, 16384\n\tv_mfma_f32_16x16x32_bf16 v[188:191], a[140:143], v[20:23], v[188:191]\n\t" \
    "s_waitcnt vmcnt(22) lgkmcnt(8)\n\tv_mfma_f32_16x16x32_bf16 v[120:123], a[144:147], v[24:27], v[120:123]\n\tds_read_b64_tr_b16 v[44:45], v5 offset:61440\n\tds_read_b64_tr_b16 v[46:47], v5 offset:63488\n\tv_mfma_f32_16x16x32_bf16 v[128:131], a[148:151], v[24:27], v[128:131]\n\tbuffer_load_dwordx4 v13, s[16:19], s69 offen lds\n\ts_mov_b32 s69, 4063232\n\ts_add_u32 m0, s54, 20480\n\t" \
    "v_mfma_f32_16x16x32_bf16 v[136:139], a[152:155], v[24:27], v[136:139]\n\ts_waitcnt lgkmcnt(8)\n\tv_mfma_f32_16x16x32_bf16 v[124:127], a[144:147], v[28:31], v[124:127]\n\tds_read_b64_tr_b16 v[16:17], v4 offset:0\n\tds_read_b64_tr_b16 v[18:19], v4 offset:2048\n\tv_mfma_f32_16x16x32_bf16 v[132:135], a[148:151], v[28:31], v[132:135]\n\tbuffer_load_dwordx4 v13, s[16:19], s69 offen lds\n\ts_mov_b32 s69, 3932160\n\t" \
    "s_add_u32 m0, s54, 24576\n\tv_mfma_f32_16x16x32_bf16 v[140:143], a[152:155], v[28:31], v[140:143]\n\ts_waitcnt vmcnt(21) lgkmcnt(8)\n\tv_mfma_f32_16x16x32_bf16 v[120:123], a[156:159], v[32:35], v[120:123]\n\tds_read_b64_tr_b16 v[20:21], v5 offset:0\n\tds_read_b64_tr_b16 v[22:23], v5 offset:2048\n\tv_mfma_f32_16x16x32_bf16 v[128:131], a[160:163], v[32:35], v[128:131]\n\tv_mfma_f32_16x16x32_bf16 v[136:139], a[164:167], v[32:35], v[136:139]\n\t" \
    "s_waitcnt lgkmcnt(8)\n\tv_mfma_f32_16x16x32_bf16 v[124:127], a[156:159], v[36:39], v[124:127]\n\tds_read_b64_tr_b16 v[24:25], v4 offset:4096\n\tds_read_b64_tr_b16 v[26:27], v4 offset:6144\n\tv_mfma_f32_16x16x32_bf16 v[132:135], a[160:163], v[36:39], v[132:135]\n\tbuffer_load_dwordx4 v13, s[16:19], s69 offen lds\n\ts_mov_b32 s69, 4587520\n\ts_add_u32 m0, s54, 28672\n\t" \
    "v_mfma_f32_16x16x32_bf16 v[140:143], a[164:167], v[36:39], v[140:143]\n\ts_waitcnt vmcnt(19) lgkmcnt(8)\n\tv_mfma_f32_16x16x32_bf16 v[120:123], a[168:171], v[40:43], v[120:123]\n\tds_read_b64_tr_b16 v[28:29], v5 offset:4096\n\tds_read_b64_tr_b16 v[30:31], v5 offset:6144\n\tv_mfma_f32_16x16x32_bf16 v[128:131], a[172:175], v[40:43], v[128:131]\n\tbuffer_load_dwordx4 v13, s[16:19], s69 offen lds\n\ts_mov_b32 s69, 4456448\n\t" \
    "s_add_u32 m0, s54, 32768\n\tv_mfma_f32_16x16x32_bf16 v[136:139], a[176:179], v[40:43], v[136:139]\n\ts_waitcnt lgkmcnt(8)\n\tv_mfma_f32_16x16x32_bf16 v[124:127], a[168:171], v[44:47], v[124:127]\n\tds_read_b64_tr_b16 v[32:33], v4 offset:8192\n\tds_read_b64_tr_b16 v[34:35], v4 offset:10240\n\tv_mfma_f32_16x16x32_bf16 v[132:135], a[172:175], v[44:47], v[132:135]\n\tbuffer_load_dwordx4 v13, s[16:19], s69 offen lds\n\t" \
    "v_mfma_f32_16x16x32_bf16 v[140:143], a[176:179], v[44:47], v[140:143]\n\ts_waitcnt vmcnt(18) lgkmcnt(8)\n\tv_mfma_f32_16x16x32_bf16 v[120:123], a[180:183], v[16:19], v[120:123]\n\tds_read_b64_tr_b16 v[36:37], v5 offset:8192\n\tds_read_b64_tr_b16 v[38:39], v5 offset:10240\n\tv_mfma_f32_16x16x32_bf16 v[128:131], a[184:187], v[16:19], v[128:131]\n\tv_mfma_f32_16x16x32_bf16 v[136:139], a[188:191], v[16:19], v[136:139]\n\ts_waitcnt lgkmcnt(8)\n\t" \
    "v_mfma_f32_16x16x32_bf16 v[124:127], a[180:183], v[20:23], v[124:127]\n\tds_read_b64_tr_b16 v[40:41], v6 offset:57344\n\tds_read_b64_tr_b16 v[42:43], v6 offset:59392\n\tv_mfma_f32_16x16x32_bf16 v[132:135], a[184:187], v[20:23], v[132:135]\n\tv_mfma_f32_16x16x32_bf16 v[140:143], a[188:191], v[20:23], v[140:143]\n\ts_waitcnt vmcnt(13)\n\tv_cmp_ne_u64 vcc, s[36:37], v[250:251]\n\ts_cmp_lg_u64 vcc, 0\n\t" \
    "s_cselect_b32 s70, 1, 0\n\ts_waitcnt lgkmcnt(8)\n\tv_mfma_f32_16x16x32_bf16 v[120:123], a[192:195], v[24:27], v[120:123]\n\tds_read_b64_tr_b16 v[44:45], v7 offset:57344\n\tds_read_b64_tr_b16 v[46:47], v7 offset:59392\n\tv_mfma_f32_16x16x32_bf16 v[128:131], a[196:199], v[24:27], v[128:131]\n\tbuffer_load_dwordx4 v[204:207], v8, s[8:11], s56 offen offset:0 sc1\n\tv_mfma_f32_16x16x32_bf16 v[136:139], a[200:203], v[24:27], v[136:139]\n\t" \
    "s_waitcnt lgkmcnt(8)\n\tv_mfma_f32_16x16x32_bf16 v[124:127], a[192:195], v[28:31], v[124:127]\n\tds_read_b64_tr_b16 v[16:17], v226 offset:57344\n\tds_read_b64_tr_b16 v[18:19], v226 offset:59392\n\tv_mfma_f32_16x16x32_bf16 v[132:135], a[196:199], v[28:31], v[132:135]\n\tbuffer_load_dwordx4 v[208:211], v8, s[8:11], s56 offen offset:1024 sc1\n\tv_mfma_f32_16x16x32_bf16 v[140:143], a[200:203], v[28:31], v[140:143]\n\ts_waitcnt vmcnt(13) lgkmcnt(8)\n\t" \
    "v_mfma_f32_16x16x32_bf16 v[120:123], a[204:207], v[32:35], v[120:123]\n\tds_read_b64_tr_b16 v[20:21], v227 offset:57344\n\tds_read_b64_tr_b16 v[22:23], v227 offset:59392\n\tv_mfma_f32_16x16x32_bf16 v[128:131], a[208:211], v[32:35], v[128:131]\n\tbuffer_load_dwordx4 v[212:215], v8, s[8:11], s56 offen offset:2048 sc1\n\tv_mfma_f32_16x16x32_bf16 v[136:139], a[212:215], v[32:35], v[136:139]\n\ts_waitcnt lgkmcnt(8)\n\tv_mfma_f32_16x16x32_bf16 v[124:127], a[204:207], v[36:39], v[124:127]\n\t" \
    "ds_read_b64_tr_b16 v[24:25], v6 offset:61440\n\tds_read_b64_tr_b16 v[26:27], v6 offset:63488\n\tv_mfma_f32_16x16x32_bf16 v[132:135], a[208:211], v[36:39], v[132:135]\n\tv_mfma_f32_16x16x32_bf16 v[140:143], a[212:215], v[36:39], v[140:143]\n\ts_waitcnt lgkmcnt(8)\n\tv_mfma_f32_16x16x32_bf16 v[144:147], a[144:147], v[40:43], v[144:147]\n\tds_read_b64_tr_b16 v[28:29], v7 offset:61440\n\tds_read_b64_tr_b16 v[30:31], v7 offset:63488\n\t" \
    "s_xor_b32 s68, s53, 12\n\tv_mfma_f32_16x16x32_bf16 v[152:155], a[148:151], v[40:43], v[152:155]\n\ts_mul_i32 s68, s68, 12288\n\ts_add_u32 s68, s68, 7208960\n\tv_cvt_pk_bf16_f32 v120, v120, v121\n\tv_mfma_f32_16x16x32_bf16 v[160:163], a[152:155], v[40:43], v[160:163]\n\tv_cvt_pk_bf16_f32 v121, v122, v123\n\tv_cvt_pk_bf16_f32 v122, v124, v125\n\t" \
    "s_waitcnt lgkmcnt(8)\n\tv_mfma_f32_16x16x32_bf16 v[148:151], a[144:147], v[44:47], v[148:151]\n\tds_read_b64_tr_b16 v[32:33], v6 offset:0\n\tds_read_b64_tr_b16 v[34:35], v6 offset:2048\n\tv_mov_b32 v123, s36\n\tv_mfma_f32_16x16x32_bf16 v[156:159], a[148:151], v[44:47], v[156:159]\n\tbuffer_store_dwordx4 v[120:123], v8, s[8:11], s68 offen offset:0 sc1\n\tv_cvt_pk_bf16_f32 v124, v126, v127\n\t" \
    "v_cvt_pk_bf16_f32 v125, v128, v129\n\tv_mfma_f32_16x16x32_bf16 v[164:167], a[152:155], v[44:47], v[164:167]\n\tv_cvt_pk_bf16_f32 v126, v130, v131\n\tv_mov_b32 v127, s36\n\tbuffer_store_dwordx4 v[124:127], v8, s[8:11], s68 offen offset:1024 sc1\n\tv_cvt_pk_bf16_f32 v128, v132, v133\n\tv_cvt_pk_bf16_f32 v129, v134, v135\n\tv_cvt_pk_bf16_f32 v130, v136, v137\n\t" \
    "v_mov_b32 v131, s36\n\tbuffer_store_dwordx4 v[128:131], v8, s[8:11], s68 offen offset:2048 sc1\n\tv_cvt_pk_bf16_f32 v132, v138, v139\n\tv_cvt_pk_bf16_f32 v133, v140, v141\n\tv_cvt_pk_bf16_f32 v134, v142, v143\n\tv_mov_b32 v135, s36\n\tbuffer_store_dwordx4 v[132:135], v8, s[8:11], s68 offen offset:3072 sc1\n\ts_waitcnt vmcnt(7)\n\t" \
    "s_barrier\n\ts_waitcnt lgkmcnt(8)\n\tv_mfma_f32_16x16x32_bf16 v[144:147], a[156:159], v[16:19], v[144:147]\n\tds_read_b64_tr_b16 v[36:37], v7 offset:0\n\tds_read_b64_tr_b16 v[38:39], v7 offset:2048\n\tv_mfma_f32_16x16x32_bf16 v[152:155], a[160:163], v[16:19], v[152:155]\n\tv_mfma_f32_16x16x32_bf16 v[160:163], a[164:167], v[16:19], v[160:163]\n\ts_waitcnt lgkmcnt(8)\n\t" \
    "v_mfma_f32_16x16x32_bf16 v[148:151], a[156:159], v[20:23], v[148:151]\n\tds_read_b64_tr_b16 v[40:41], v6 offset:4096\n\tds_read_b64_tr_b16 v[42:43], v6 offset:6144\n\tv_mfma_f32_16x16x32_bf16 v[156:159], a[160:163], v[20:23], v[156:159]\n\tv_mfma_f32_16x16x32_bf16 v[164:167], a[164:167], v[20:23], v[164:167]\n\ts_waitcnt lgkmcnt(8)\n\tv_mfma_f32_16x16x32_bf16 v[144:147], a[168:171], v[24:27], v[144:147]\n\tds_read_b64_tr_b16 v[44:45], v7 offset:4096\n\t" \
    "ds_read_b64_tr_b16 v[46:47], v7 offset:6144\n\tv_mfma_f32_16x16x32_bf16 v[152:155], a[172:175], v[24:27], v[152:155]\n\tv_mfma_f32_16x16x32_bf16 v[160:163], a[176:179], v[24:27], v[160:163]\n\ts_waitcnt lgkmcnt(8)\n\tv_mfma_f32_16x16x32_bf16 v[148:151], a[168:171], v[28:31], v[148:151]\n\tds_read_b64_tr_b16 v[16:17], v6 offset:8192\n\tds_read_b64_tr_b16 v[18:19], v6 offset:10240\n\tv_mfma_f32_16x16x32_bf16 v[156:159], a[172:175], v[28:31], v[156:159]\n\t" \
    "v_mfma_f32_16x16x32_bf16 v[164:167], a[176:179], v[28:31], v[164:167]\n\ts_waitcnt lgkmcnt(8)\n\tv_mfma_f32_16x16x32_bf16 v[144:147], a[180:183], v[32:35], v[144:147]\n\tds_read_b64_tr_b16 v[20:21], v7 offset:8192\n\tds_read_b64_tr_b16 v[22:23], v7 offset:10240\n\tv_mfma_f32_16x16x32_bf16 v[152:155], a[184:187], v[32:35], v[152:155]\n\tv_mfma_f32_16x16x32_bf16 v[160:163], a[188:191], v[32:35], v[160:163]\n\ts_waitcnt lgkmcnt(8)\n\t" \
    "v_mfma_f32_16x16x32_bf16 v[148:151], a[180:183], v[36:39], v[148:151]\n\tds_read_b64_tr_b16 v[24:25], v4 offset:12288\n\tds_read_b64_tr_b16 v[26:27], v4 offset:14336\n\tv_mfma_f32_16x16x32_bf16 v[156:159], a[184:187], v[36:39], v[156:159]\n\tv_mfma_f32_16x16x32_bf16 v[164:167], a[188:191], v[36:39], v[164:167]\n\ts_waitcnt lgkmcnt(8)\n\tv_mfma_f32_16x16x32_bf16 v[144:147], a[192:195], v[40:43], v[144:147]\n\tds_read_b64_tr_b16 v[28:29], v5 offset:12288\n\t" \
    "ds_read_b64_tr_b16 v[30:31], v5 offset:14336\n\tv_mfma_f32_16x16x32_bf16 v[152:155], a[196:199], v[40:43], v[152:155]\n\tv_mfma_f32_16x16x32_bf16 v[160:163], a[200:203], v[40:43], v[160:163]\n\ts_waitcnt lgkmcnt(8)\n\tv_mfma_f32_16x16x32_bf16 v[148:151], a[192:195], v[44:47], v[148:151]\n\tds_read_b64_tr_b16 v[32:33], v4 offset:16384\n\tds_read_b64_tr_b16 v[34:35], v4 offset:18432\n\tv_mfma_f32_16x16x32_bf16 v[156:159], a[196:199], v[44:47], v[156:159]\n\t" \
    "v_mfma_f32_16x16x32_bf16 v[164:167], a[200:203], v[44:47], v[164:167]\n\ts_waitcnt lgkmcnt(8)\n\tv_mfma_f32_16x16x32_bf16 v[144:147], a[204:207], v[16:19], v[144:147]\n\tds_read_b64_tr_b16 v[36:37], v5 offset:16384\n\tds_read_b64_tr_b16 v[38:39], v5 offset:18432\n\tv_mfma_f32_16x16x32_bf16 v[152:155], a[208:211], v[16:19], v[152:155]\n\tv_mfma_f32_16x16x32_bf16 v[160:163], a[212:215], v[16:19], v[160:163]\n\ts_waitcnt lgkmcnt(8)\n\t" \
    "v_mfma_f32_16x16x32_bf16 v[148:151], a[204:207], v[20:23], v[148:151]\n\tds_read_b64_tr_b16 v[40:41], v4 offset:20480\n\tds_read_b64_tr_b16 v[42:43], v4 offset:22528\n\tv_mfma_f32_16x16x32_bf16 v[156:159], a[208:211], v[20:23], v[156:159]\n\tv_mfma_f32_16x16x32_bf16 v[164:167], a[212:215], v[20:23], v[164:167]\n\ts_waitcnt lgkmcnt(8)\n\tv_mfma_f32_16x16x32_bf16 v[168:171], a[144:147], v[24:27], v[168:171]\n\tds_read_b64_tr_b16 v[44:45], v5 offset:20480\n\t" \
    "ds_read_b64_tr_b16 v[46:47], v5 offset:22528\n\ts_xor_b32 s68, s53, 8\n\tv_mfma_f32_16x16x32_bf16 v[176:179], a[148:151], v[24:27], v[176:179]\n\ts_mul_i32 s68, s68, 12288\n\ts_add_u32 s68, s68, 7213056\n\tv_cvt_pk_bf16_f32 v144, v144, v145\n\tv_mfma_f32_16x16x32_bf16 v[184:187], a[152:155], v[24:27], v[184:187]\n\tv_cvt_pk_bf16_f32 v145, v146, v147\n\t" \
    "v_cvt_pk_bf16_f32 v146, v148, v149\n\ts_waitcnt lgkmcnt(8)\n\tv_mfma_f32_16x16x32_bf16 v[172:175], a[144:147], v[28:31], v[172:175]\n\tds_read_b64_tr_b16 v[16:17], v4 offset:24576\n\tds_read_b64_tr_b16 v[18:19], v4 offset:26624\n\tv_mov_b32 v147, s36\n\tv_mfma_f32_16x16x32_bf16 v[180:183], a[148:151], v[28:31], v[180:183]\n\tbuffer_store_dwordx4 v[144:147], v8, s[8:11], s68 offen offset:0 sc1\n\t" \
    "v_cvt_pk_bf16_f32 v148, v150, v151\n\tv_cvt_pk_bf16_f32 v149, v152, v153\n\tv_mfma_f32_16x16x32_bf16 v[188:191], a[152:155], v[28:31], v[188:191]\n\tv_cvt_pk_bf16_f32 v150, v154, v155\n\tv_mov_b32 v151, s36\n\ts_waitcnt lgkmcnt(8)\n\tv_mfma_f32_16x16x32_bf16 v[168:171], a[156:159], v[32:35], v[168:171]\n\tds_read_b64_tr_b16 v[20:21], v5 offset:24576\n\t" \
    "ds_read_b64_tr_b16 v[22:23], v5 offset:26624\n\tv_mfma_f32_16x16x32_bf16 v[176:179], a[160:163], v[32:35], v[176:179]\n\tbuffer_store_dwordx4 v[148:151], v8, s[8:11], s68 offen offset:1024 sc1\n\tv_cvt_pk_bf16_f32 v152, v156, v157\n\tv_cvt_pk_bf16_f32 v153, v158, v159\n\tv_mfma_f32_16x16x32_bf16 v[184:187], a[164:167], v[32:35], v[184:187]\n\tv_cvt_pk_bf16_f32 v154, v160, v161\n\tv_mov_b32 v155, s36\n\t" \
    "s_waitcnt lgkmcnt(8)\n\tv_mfma_f32_16x16x32_bf16 v[172:175], a[156:159], v[36:39], v[172:175]\n\tds_read_b64_tr_b16 v[24:25], v4 offset:28672\n\tds_read_b64_tr_b16 v[26:27], v4 offset:30720\n\tv_mfma_f32_16x16x32_bf16 v[180:183], a[160:163], v[36:39], v[180:183]\n\tbuffer_store_dwordx4 v[152:155], v8, s[8:11], s68 offen offset:2048 sc1\n\tv_cvt_pk_bf16_f32 v156, v162, v163\n\tv_cvt_pk_bf16_f32 v157, v164, v165\n\t" \
    "v_mfma_f32_16x16x32_bf16 v[188:191], a[164:167], v[36:39], v[188:191]\n\tv_cvt_pk_bf16_f32 v158, v166, v167\n\tv_mov_b32 v159, s36\n\ts_waitcnt lgkmcnt(8)\n\tv_mfma_f32_16x16x32_bf16 v[168:171], a[168:171], v[40:43], v[168:171]\n\tds_read_b64_tr_b16 v[28:29], v5 offset:28672\n\tds_read_b64_tr_b16 v[30:31], v5 offset:30720\n\tv_mfma_f32_16x16x32_bf16 v[176:179], a[172:175], v[40:43], v[176:179]\n\t" \
    "buffer_store_dwordx4 v[156:159], v8, s[8:11], s68 offen offset:3072 sc1\n\tv_mfma_f32_16x16x32_bf16 v[184:187], a[176:179], v[40:43], v[184:187]\n\ts_waitcnt lgkmcnt(8)\n\tv_mfma_f32_16x16x32_bf16 v[172:175], a[168:171], v[44:47], v[172:175]\n\tds_read_b64_tr_b16 v[32:33], v4 offset:32768\n\tds_read_b64_tr_b16 v[34:35], v4 offset:34816\n\tv_mfma_f32_16x16x32_bf16 v[180:183], a[172:175], v[44:47], v[180:183]\n\tv_mfma_f32_16x16x32_bf16 v[188:191], a[176:179], v[44:47], v[188:191]\n\t" \
    "s_waitcnt lgkmcnt(8)\n\tv_mfma_f32_16x16x32_bf16 v[168:171], a[180:183], v[16:19], v[168:171]\n\tds_read_b64_tr_b16 v[36:37], v5 offset:32768\n\tds_read_b64_tr_b16 v[38:39], v5 offset:34816\n\tv_mfma_f32_16x16x32_bf16 v[176:179], a[184:187], v[16:19], v[176:179]\n\tv_mfma_f32_16x16x32_bf16 v[184:187], a[188:191], v[16:19], v[184:187]\n\ts_waitcnt lgkmcnt(8)\n\tv_mfma_f32_16x16x32_bf16 v[172:175], a[180:183], v[20:23], v[172:175]\n\t" \
    "ds_read_b64_tr_b16 v[40:41], v226 offset:61440\n\tds_read_b64_tr_b16 v[42:43], v226 offset:63488\n\tv_mfma_f32_16x16x32_bf16 v[180:183], a[184:187], v[20:23], v[180:183]\n\tv_mfma_f32_16x16x32_bf16 v[188:191], a[188:191], v[20:23], v[188:191]\n\ts_waitcnt lgkmcnt(8)\n\tv_mfma_f32_16x16x32_bf16 v[168:171], a[192:195], v[24:27], v[168:171]\n\tds_read_b64_tr_b16 v[44:45], v227 offset:61440\n\tds_read_b64_tr_b16 v[46:47], v227 offset:63488\n\t" \
    "v_mfma_f32_16x16x32_bf16 v[176:179], a[196:199], v[24:27], v[176:179]\n\tv_mfma_f32_16x16x32_bf16 v[184:187], a[200:203], v[24:27], v[184:187]\n\ts_waitcnt lgkmcnt(8)\n\tv_mfma_f32_16x16x32_bf16 v[172:175], a[192:195], v[28:31], v[172:175]\n\tds_read_b64_tr_b16 v[16:17], v226 offset:0\n\tds_read_b64_tr_b16 v[18:19], v226 offset:2048\n\tv_mfma_f32_16x16x32_bf16 v[180:183], a[196:199], v[28:31], v[180:183]\n\tv_mfma_f32_16x16x32_bf16 v[188:191], a[200:203], v[28:31], v[188:191]\n\t" \
    "s_waitcnt lgkmcnt(8)\n\tv_mfma_f32_16x16x32_bf16 v[168:171], a[204:207], v[32:35], v[168:171]\n\tds_read_b64_tr_b16 v[20:21], v227 offset:0\n\tds_read_b64_tr_b16 v[22:23], v227 offset:2048\n\tv_mfma_f32_16x16x32_bf16 v[176:179], a[208:211], v[32:35], v[176:179]\n\tv_mfma_f32_16x16x32_bf16 v[184:187], a[212:215], v[32:35], v[184:187]\n\ts_waitcnt lgkmcnt(8)\n\tv_mfma_f32_16x16x32_bf16 v[172:175], a[204:207], v[36:39], v[172:175]\n\t" \
    "ds_read_b64_tr_b16 v[24:25], v226 offset:4096\n\tds_read_b64_tr_b16 v[26:27], v226 offset:6144\n\tv_mfma_f32_16x16x32_bf16 v[180:183], a[208:211], v[36:39], v[180:183]\n\tv_mfma_f32_16x16x32_bf16 v[188:191], a[212:215], v[36:39], v[188:191]\n\ts_waitcnt lgkmcnt(8)\n\tv_mfma_f32_16x16x32_bf16 v[48:51], v[72:75], v[40:43], v[48:51]\n\tds_read_b64_tr_b16 v[28:29], v227 offset:4096\n\tds_read_b64_tr_b16 v[30:31], v227 offset:6144\n\t" \
    "s_xor_b32 s68, s53, 4\n\tv_mfma_f32_16x16x32_bf16 v[56:59], v[76:79], v[40:43], v[56:59]\n\ts_mul_i32 s68, s68, 12288\n\ts_add_u32 s68, s68, 7217152\n\tv_cvt_pk_bf16_f32 v168, v168, v169\n\tv_mfma_f32_16x16x32_bf16 v[64:67], v[80:83], v[40:43], v[64:67]\n\tv_cvt_pk_bf16_f32 v169, v170, v171\n\tv_cvt_pk_bf16_f32 v170, v172, v173\n\t" \
    "s_waitcnt lgkmcnt(8)\n\tv_mfma_f32_16x16x32_bf16 v[52:55], v[72:75], v[44:47], v[52:55]\n\tds_read_b64_tr_b16 v[32:33], v226 offset:8192\n\tds_read_b64_tr_b16 v[34:35], v226 offset:10240\n\tv_mov_b32 v171, s36\n\tv_mfma_f32_16x16x32_bf16 v[60:63], v[76:79], v[44:47], v[60:63]\n\tbuffer_store_dwordx4 v[168:171], v8, s[8:11], s68 offen offset:0 sc1\n\tv_cvt_pk_bf16_f32 v172, v174, v175\n\t" \
    "v_cvt_pk_bf16_f32 v173, v176, v177\n\tv_mfma_f32_16x16x32_bf16 v[68:71], v[80:83], v[44:47], v[68:71]\n\tv_cvt_pk_bf16_f32 v174, v178, v179\n\tv_mov_b32 v175, s36\n\ts_waitcnt lgkmcnt(8)\n\tv_mfma_f32_16x16x32_bf16 v[48:51], a[32:35], v[16:19], v[48:51]\n\tds_read_b64_tr_b16 v[36:37], v227 offset:8192\n\tds_read_b64_tr_b16 v[38:39], v227 offset:10240\n\t" \
    "v_mfma_f32_16x16x32_bf16 v[56:59], a[36:39], v[16:19], v[56:59]\n\tbuffer_store_dwordx4 v[172:175], v8, s[8:11], s68 offen offset:1024 sc1\n\tv_cvt_pk_bf16_f32 v176, v180, v181\n\tv_cvt_pk_bf16_f32 v177, v182, v183\n\tv_mfma_f32_16x16x32_bf16 v[64:67], a[40:43], v[16:19], v[64:67]\n\tv_cvt_pk_bf16_f32 v178, v184, v185\n\tv_mov_b32 v179, s36\n\ts_waitcnt lgkmcnt(8)\n\t" \
    "v_mfma_f32_16x16x32_bf16 v[52:55], a[32:35], v[20:23], v[52:55]\n\tds_read_b64_tr_b16 v[40:41], v226 offset:12288\n\tds_read_b64_tr_b16 v[42:43], v226 offset:14336\n\tv_mfma_f32_16x16x32_bf16 v[60:63], a[36:39], v[20:23], v[60:63]\n\tbuffer_store_dwordx4 v[176:179], v8, s[8:11], s68 offen offset:2048 sc1\n\tv_cvt_pk_bf16_f32 v180, v186, v187\n\tv_cvt_pk_bf16_f32 v181, v188, v189\n\tv_mfma_f32_16x16x32_bf16 v[68:71], a[40:43], v[20:23], v[68:71]\n\t" \
    "v_cvt_pk_bf16_f32 v182, v190, v191\n\tv_mov_b32 v183, s36\n\ts_waitcnt lgkmcnt(8)\n\tv_mfma_f32_16x16x32_bf16 v[48:51], v[84:87], v[24:27], v[48:51]\n\tds_read_b64_tr_b16 v[44:45], v227 offset:12288\n\tds_read_b64_tr_b16 v[46:47], v227 offset:14336\n\tv_mfma_f32_16x16x32_bf16 v[56:59], v[88:91], v[24:27], v[56:59]\n\tbuffer_store_dwordx4 v[180:183], v8, s[8:11], s68 offen offset:3072 sc1\n\t" \
    "v_mfma_f32_16x16x32_bf16 v[64:67], v[92:95], v[24:27], v[64:67]\n\ts_waitcnt lgkmcnt(8)\n\tv_mfma_f32_16x16x32_bf16 v[52:55], v[84:87], v[28:31], v[52:55]\n\tds_read_b64_tr_b16 v[16:17], v226 offset:16384\n\tds_read_b64_tr_b16 v[18:19], v226 offset:18432\n\tv_mfma_f32_16x16x32_bf16 v[60:63], v[88:91], v[28:31], v[60:63]\n\tv_mfma_f32_16x16x32_bf16 v[68:71], v[92:95], v[28:31], v[68:71]\n\ts_waitcnt lgkmcnt(8)\n\t" \
    "v_mfma_f32_16x16x32_bf16 v[48:51], v[96:99], v[32:35], v[48:51]\n\tds_read_b64_tr_b16 v[20:21], v227 offset:16384\n\tds_read_b64_tr_b16 v[22:23], v227 offset:18432\n\tv_mfma_f32_16x16x32_bf16 v[56:59], v[100:103], v[32:35], v[56:59]\n\tv_mfma_f32_16x16x32_bf16 v[64:67], v[104:107], v[32:35], v[64:67]\n\ts_waitcnt lgkmcnt(8)\n\tv_mfma_f32_16x16x32_bf16 v[52:55], v[96:99], v[36:39], v[52:55]\n\tds_read_b64_tr_b16 v[24:25], v226 offset:20480\n\t" \
    "ds_read_b64_tr_b16 v[26:27], v226 offset:22528\n\tv_mfma_f32_16x16x32_bf16 v[60:63], v[100:103], v[36:39], v[60:63]\n\tv_mfma_f32_16x16x32_bf16 v[68:71], v[104:107], v[36:39], v[68:71]\n\ts_waitcnt vmcnt(12) lgkmcnt(8)\n\tv_mfma_f32_16x16x32_bf16 v[48:51], a[44:47], v[40:43], v[48:51]\n\tds_read_b64_tr_b16 v[28:29], v227 offset:20480\n\tds_read_b64_tr_b16 v[30:31], v227 offset:22528\n\tv_mfma_f32_16x16x32_bf16 v[56:59], a[48:51], v[40:43], v[56:59]\n\t" \
    "v_permlane16_swap_b32 v192, v194\n\tv_permlane16_swap_b32 v193, v195\n\tv_mfma_f32_16x16x32_bf16 v[64:67], a[52:55], v[40:43], v[64:67]\n\tv_permlane16_swap_b32 v196, v198\n\tv_permlane16_swap_b32 v197, v199\n\ts_waitcnt lgkmcnt(8)\n\tv_mfma_f32_16x16x32_bf16 v[52:55], a[44:47], v[44:47], v[52:55]\n\tds_read_b64_tr_b16 v[32:33], v226 offset:24576\n\t" \
    "ds_read_b64_tr_b16 v[34:35], v226 offset:26624\n\tv_mfma_f32_16x16x32_bf16 v[60:63], a[48:51], v[44:47], v[60:63]\n\tv_permlane16_swap_b32 v200, v202\n\tv_permlane16_swap_b32 v201, v203\n\tv_mfma_f32_16x16x32_bf16 v[68:71], a[52:55], v[44:47], v[68:71]\n\tv_lshlrev_b32 v72, 16, v192\n\tv_and_b32 v73, 0xffff0000, v192\n\ts_waitcnt lgkmcnt(8)\n\t" \
    "v_mfma_f32_16x16x32_bf16 v[48:51], a[0:3], v[16:19], v[48:51]\n\tds_read_b64_tr_b16 v[36:37], v227 offset:24576\n\tds_read_b64_tr_b16 v[38:39], v227 offset:26624\n\tv_mfma_f32_16x16x32_bf16 v[56:59], a[4:7], v[16:19], v[56:59]\n\tv_lshlrev_b32 v74, 16, v193\n\tv_and_b32 v75, 0xffff0000, v193\n\tv_mfma_f32_16x16x32_bf16 v[64:67], a[56:59], v[16:19], v[64:67]\n\tv_lshlrev_b32 v76, 16, v194\n\t" \
    "v_and_b32 v77, 0xffff0000, v194\n\ts_waitcnt lgkmcnt(8)\n\tv_mfma_f32_16x16x32_bf16 v[52:55], a[0:3], v[20:23], v[52:55]\n\tds_read_b64_tr_b16 v[40:41], v226 offset:28672\n\tds_read_b64_tr_b16 v[42:43], v226 offset:30720\n\tv_mfma_f32_16x16x32_bf16 v[60:63], a[4:7], v[20:23], v[60:63]\n\tv_lshlrev_b32 v78, 16, v195\n\tv_and_b32 v79, 0xffff0000, v195\n\t" \
    "v_mfma_f32_16x16x32_bf16 v[68:71], a[56:59], v[20:23], v[68:71]\n\tv_lshlrev_b32 v80, 16, v196\n\tv_and_b32 v81, 0xffff0000, v196\n\ts_waitcnt lgkmcnt(8)\n\tv_mfma_f32_16x16x32_bf16 v[48:51], a[60:63], v[24:27], v[48:51]\n\tds_read_b64_tr_b16 v[44:45], v227 offset:28672\n\tds_read_b64_tr_b16 v[46:47], v227 offset:30720\n\tv_mfma_f32_16x16x32_bf16 v[56:59], a[64:67], v[24:27], v[56:59]\n\t" \
    "v_lshlrev_b32 v82, 16, v197\n\tv_and_b32 v83, 0xffff0000, v197\n\tv_mfma_f32_16x16x32_bf16 v[64:67], a[68:71], v[24:27], v[64:67]\n\tv_lshlrev_b32 v84, 16, v198\n\tv_and_b32 v85, 0xffff0000, v198\n\ts_waitcnt lgkmcnt(8)\n\tv_mfma_f32_16x16x32_bf16 v[52:55], a[60:63], v[28:31], v[52:55]\n\tds_read_b64_tr_b16 v[16:17], v226 offset:32768\n\t" \
    "ds_read_b64_tr_b16 v[18:19], v226 offset:34816\n\tv_mfma_f32_16x16x32_bf16 v[60:63], a[64:67], v[28:31], v[60:63]\n\tv_lshlrev_b32 v86, 16, v199\n\tv_and_b32 v87, 0xffff0000, v199\n\tv_mfma_f32_16x16x32_bf16 v[68:71], a[68:71], v[28:31], v[68:71]\n\tv_lshlrev_b32 v88, 16, v200\n\tv_and_b32 v89, 0xffff0000, v200\n\ts_waitcnt lgkmcnt(8)\n\t" \
    "v_mfma_f32_16x16x32_bf16 v[48:51], a[8:11], v[32:35], v[48:51]\n\tds_read_b64_tr_b16 v[20:21], v227 offset:32768\n\tds_read_b64_tr_b16 v[22:23], v227 offset:34816\n\tv_mfma_f32_16x16x32_bf16 v[56:59], a[12:15], v[32:35], v[56:59]\n\tv_lshlrev_b32 v90, 16, v201\n\tv_and_b32 v91, 0xffff0000, v201\n\tv_mfma_f32_16x16x32_bf16 v[64:67], a[16:19], v[32:35], v[64:67]\n\tv_lshlrev_b32 v92, 16, v202\n\t" \
    "v_and_b32 v93, 0xffff0000, v202\n\ts_waitcnt lgkmcnt(8)\n\tv_mfma_f32_16x16x32_bf16 v[52:55], a[8:11], v[36:39], v[52:55]\n\tds_read_b64_tr_b16 v[24:25], v226 offset:36864\n\tds_read_b64_tr_b16 v[26:27], v226 offset:38912\n\tv_mfma_f32_16x16x32_bf16 v[60:63], a[12:15], v[36:39], v[60:63]\n\tv_lshlrev_b32 v94, 16, v203\n\tv_and_b32 v95, 0xffff0000, v203\n\t" \
    "v_mfma_f32_16x16x32_bf16 v[68:71], a[16:19], v[36:39], v[68:71]\n\tv_fma_f32 v96, v72, v216, v217\n\tv_fma_f32 v97, v73, v216, v217\n\ts_waitcnt lgkmcnt(8)\n\tv_mfma_f32_16x16x32_bf16 v[48:51], a[20:23], v[40:43], v[48:51]\n\tds_read_b64_tr_b16 v[28:29], v227 offset:36864\n\tds_read_b64_tr_b16 v[30:31], v227 offset:38912\n\tv_mfma_f32_16x16x32_bf16 v[56:59], a[24:27], v[40:43], v[56:59]\n\t" \
    "v_fma_f32 v98, v74, v216, v217\n\tv_fma_f32 v99, v75, v216, v217\n\tv_mfma_f32_16x16x32_bf16 v[64:67], a[28:31], v[40:43], v[64:67]\n\tv_fma_f32 v100, v76, v218, v219\n\tv_fma_f32 v101, v77, v218, v219\n\ts_waitcnt lgkmcnt(8)\n\tv_mfma_f32_16x16x32_bf16 v[52:55], a[20:23], v[44:47], v[52:55]\n\tds_read_b64_tr_b16 v[32:33], v226 offset:40960\n\t" \
    "ds_read_b64_tr_b16 v[34:35], v226 offset:43008\n\tv_mfma_f32_16x16x32_bf16 v[60:63], a[24:27], v[44:47], v[60:63]\n\tv_fma_f32 v102, v78, v218, v219\n\tv_fma_f32 v103, v79, v218, v219\n\tv_mfma_f32_16x16x32_bf16 v[68:71], a[28:31], v[44:47], v[68:71]\n\tv_fma_f32 v104, v80, v216, v217\n\tv_fma_f32 v105, v81, v216, v217\n\ts_waitcnt lgkmcnt(8)\n\t" \
    "v_mfma_f32_16x16x32_bf16 v[48:51], a[72:75], v[16:19], v[48:51]\n\tds_read_b64_tr_b16 v[36:37], v227 offset:40960\n\tds_read_b64_tr_b16 v[38:39], v227 offset:43008\n\tv_mfma_f32_16x16x32_bf16 v[56:59], a[76:79], v[16:19], v[56:59]\n\tv_fma_f32 v106, v82, v216, v217\n\tv_fma_f32 v107, v83, v216, v217\n\tv_mfma_f32_16x16x32_bf16 v[64:67], a[80:83], v[16:19], v[64:67]\n\tv_fma_f32 v108, v84, v218, v219\n\t" \
    "v_fma_f32 v109, v85, v218, v219\n\ts_waitcnt lgkmcnt(8)\n\tv_mfma_f32_16x16x32_bf16 v[52:55], a[72:75], v[20:23], v[52:55]\n\tds_read_b64_tr_b16 v[40:41], v226 offset:45056\n\tds_read_b64_tr_b16 v[42:43], v226 offset:47104\n\tv_mfma_f32_16x16x32_bf16 v[60:63], a[76:79], v[20:23], v[60:63]\n\tv_fma_f32 v110, v86, v218, v219\n\tv_fma_f32 v111, v87, v218, v219\n\t" \
    "v_mfma_f32_16x16x32_bf16 v[68:71], a[80:83], v[20:23], v[68:71]\n\tv_fma_f32 v112, v88, v216, v217\n\tv_fma_f32 v113, v89, v216, v217\n\ts_waitcnt lgkmcnt(8)\n\tv_mfma_f32_16x16x32_bf16 v[48:51], a[84:87], v[24:27], v[48:51]\n\tds_read_b64_tr_b16 v[44:45], v227 offset:45056\n\tds_read_b64_tr_b16 v[46:47], v227 offset:47104\n\tv_mfma_f32_16x16x32_bf16 v[56:59], a[88:91], v[24:27], v[56:59]\n\t" \
    "v_fma_f32 v114, v90, v216, v217\n\tv_fma_f32 v115, v91, v216, v217\n\tv_mfma_f32_16x16x32_bf16 v[64:67], a[92:95], v[24:27], v[64:67]\n\tv_fma_f32 v116, v92, v218, v219\n\tv_fma_f32 v117, v93, v218, v219\n\ts_waitcnt lgkmcnt(8)\n\tv_mfma_f32_16x16x32_bf16 v[52:55], a[84:87], v[28:31], v[52:55]\n\tds_read_b64_tr_b16 v[16:17], v226 offset:49152\n\t" \
    "ds_read_b64_tr_b16 v[18:19], v226 offset:51200\n\tv_mfma_f32_16x16x32_bf16 v[60:63], a[88:91], v[28:31], v[60:63]\n\tv_fma_f32 v118, v94, v218, v219\n\tv_fma_f32 v119, v95, v218, v219\n\tv_mfma_f32_16x16x32_bf16 v[68:71], a[92:95], v[28:31], v[68:71]\n\tv_lshlrev_b32 v220, 16, v204\n\tv_and_b32 v221, 0xffff0000, v204\n\ts_waitcnt lgkmcnt(8)\n\t" \
    "v_mfma_f32_16x16x32_bf16 v[48:51], a[96:99], v[32:35], v[48:51]\n\tds_read_b64_tr_b16 v[20:21], v227 offset:49152\n\tds_read_b64_tr_b16 v[22:23], v227 offset:51200\n\tv_mfma_f32_16x16x32_bf16 v[56:59], a[100:103], v[32:35], v[56:59]\n\tv_mul_f32 v96, v96, v220\n\tv_mul_f32 v97, v97, v221\n\tv_mfma_f32_16x16x32_bf16 v[64:67], a[104:107], v[32:35], v[64:67]\n\tv_lshlrev_b32 v222, 16, v205\n\t" \
    "v_and_b32 v223, 0xffff0000, v205\n\ts_waitcnt lgkmcnt(8)\n\tv_mfma_f32_16x16x32_bf16 v[52:55], a[96:99], v[36:39], v[52:55]\n\tds_read_b64_tr_b16 v[24:25], v226 offset:53248\n\tds_read_b64_tr_b16 v[26:27], v226 offset:55296\n\tv_mfma_f32_16x16x32_bf16 v[60:63], a[100:103], v[36:39], v[60:63]\n\tv_mul_f32 v98, v98, v222\n\tv_mul_f32 v99, v99, v223\n\t" \
    "v_mfma_f32_16x16x32_bf16 v[68:71], a[104:107], v[36:39], v[68:71]\n\tv_lshlrev_b32 v220, 16, v206\n\tv_and_b32 v221, 0xffff0000, v206\n\ts_waitcnt lgkmcnt(8)\n\tv_mfma_f32_16x16x32_bf16 v[48:51], a[108:111], v[40:43], v[48:51]\n\tds_read_b64_tr_b16 v[28:29], v227 offset:53248\n\tds_read_b64_tr_b16 v[30:31], v227 offset:55296\n\tv_mfma_f32_16x16x32_bf16 v[56:59], a[112:115], v[40:43], v[56:59]\n\t" \
    "v_mul_f32 v100, v100, v220\n\tv_mul_f32 v101, v101, v221\n\tv_mfma_f32_16x16x32_bf16 v[64:67], a[116:119], v[40:43], v[64:67]\n\tv_lshlrev_b32 v222, 16, v207\n\tv_and_b32 v223, 0xffff0000, v207\n\ts_waitcnt lgkmcnt(8)\n\tv_mfma_f32_16x16x32_bf16 v[52:55], a[108:111], v[44:47], v[52:55]\n\tds_read_b64_tr_b16 v[32:33], v6 offset:12288\n\t" \
    "ds_read_b64_tr_b16 v[34:35], v6 offset:14336\n\tv_mfma_f32_16x16x32_bf16 v[60:63], a[112:115], v[44:47], v[60:63]\n\tv_mul_f32 v102, v102, v222\n\tv_mul_f32 v103, v103, v223\n\tv_mfma_f32_16x16x32_bf16 v[68:71], a[116:119], v[44:47], v[68:71]\n\tv_lshlrev_b32 v220, 16, v208\n\tv_and_b32 v221, 0xffff0000, v208\n\ts_waitcnt lgkmcnt(8)\n\t" \
    "v_mfma_f32_16x16x32_bf16 v[48:51], a[120:123], v[16:19], v[48:51]\n\tds_read_b64_tr_b16 v[36:37], v7 offset:12288\n\tds_read_b64_tr_b16 v[38:39], v7 offset:14336\n\tv_mfma_f32_16x16x32_bf16 v[56:59], a[124:127], v[16:19], v[56:59]\n\tv_mul_f32 v104, v104, v220\n\tv_mul_f32 v105, v105, v221\n\tv_mfma_f32_16x16x32_bf16 v[64:67], a[128:131], v[16:19], v[64:67]\n\tv_lshlrev_b32 v222, 16, v209\n\t" \
    "v_and_b32 v223, 0xffff0000, v209\n\ts_waitcnt lgkmcnt(8)\n\tv_mfma_f32_16x16x32_bf16 v[52:55], a[120:123], v[20:23], v[52:55]\n\tds_read_b64_tr_b16 v[40:41], v6 offset:16384\n\tds_read_b64_tr_b16 v[42:43], v6 offset:18432\n\tv_mfma_f32_16x16x32_bf16 v[60:63], a[124:127], v[20:23], v[60:63]\n\tv_mul_f32 v106, v106, v222\n\tv_mul_f32 v107, v107, v223\n\t" \
    "v_mfma_f32_16x16x32_bf16 v[68:71], a[128:131], v[20:23], v[68:71]\n\tv_lshlrev_b32 v220, 16, v210\n\tv_and_b32 v221, 0xffff0000, v210\n\ts_waitcnt lgkmcnt(8)\n\tv_mfma_f32_16x16x32_bf16 v[48:51], a[132:135], v[24:27], v[48:51]\n\tds_read_b64_tr_b16 v[44:45], v7 offset:16384\n\tds_read_b64_tr_b16 v[46:47], v7 offset:18432\n\ts_add_u32 s68, s55, 0\n\t" \
    "v_mfma_f32_16x16x32_bf16 v[56:59], a[136:139], v[24:27], v[56:59]\n\tbuffer_load_dwordx4 v[120:123], v8, s[8:11], s68 offen offset:0 sc1\n\tv_mul_f32 v108, v108, v220\n\tv_mfma_f32_16x16x32_bf16 v[64:67], a[140:143], v[24:27], v[64:67]\n\tv_mul_f32 v109, v109, v221\n\tv_lshlrev_b32 v222, 16, v211\n\ts_waitcnt lgkmcnt(8)\n\tv_mfma_f32_16x16x32_bf16 v[52:55], a[132:135], v[28:31], v[52:55]\n\t" \
    "ds_read_b64_tr_b16 v[16:17], v6 offset:20480\n\tds_read_b64_tr_b16 v[18:19], v6 offset:22528\n\tv_mfma_f32_16x16x32_bf16 v[60:63], a[136:139], v[28:31], v[60:63]\n\tbuffer_load_dwordx4 v[124:127], v8, s[8:11], s68 offen offset:1024 sc1\n\tv_and_b32 v223, 0xffff0000, v211\n\tv_mfma_f32_16x16x32_bf16 v[68:71], a[140:143], v[28:31], v[68:71]\n\tv_mul_f32 v110, v110, v222\n\tv_mul_f32 v111, v111, v223\n\t" \
    "s_waitcnt lgkmcnt(8)\n\tv_mfma_f32_16x16x32_bf16 v[48:51], a[144:147], v[32:35], v[48:51]\n\tds_read_b64_tr_b16 v[20:21], v7 offset:20480\n\tds_read_b64_tr_b16 v[22:23], v7 offset:22528\n\tv_mfma_f32_16x16x32_bf16 v[56:59], a[148:151], v[32:35], v[56:59]\n\tbuffer_load_dwordx4 v[128:131], v8, s[8:11], s68 offen offset:2048 sc1\n\tv_lshlrev_b32 v220, 16, v212\n\tv_mfma_f32_16x16x32_bf16 v[64:67], a[152:155], v[32:35], v[64:67]\n\t" \
    "v_and_b32 v221, 0xffff0000, v212\n\tv_mul_f32 v112, v112, v220\n\ts_waitcnt lgkmcnt(8)\n\tv_mfma_f32_16x16x32_bf16 v[52:55], a[144:147], v[36:39], v[52:55]\n\tds_read_b64_tr_b16 v[24:25], v6 offset:24576\n\tds_read_b64_tr_b16 v[26:27], v6 offset:26624\n\tv_mfma_f32_16x16x32_bf16 v[60:63], a[148:151], v[36:39], v[60:63]\n\tbuffer_load_dwordx4 v[132:135], v8, s[8:11], s68 offen offset:3072 sc1\n\t" \
    "v_mul_f32 v113, v113, v221\n\tv_mfma_f32_16x16x32_bf16 v[68:71], a[152:155], v[36:39], v[68:71]\n\tv_lshlrev_b32 v222, 16, v213\n\tv_and_b32 v223, 0xffff0000, v213\n\ts_waitcnt lgkmcnt(8)\n\tv_mfma_f32_16x16x32_bf16 v[48:51], a[156:159], v[40:43], v[48:51]\n\tds_read_b64_tr_b16 v[28:29], v7 offset:24576\n\tds_read_b64_tr_b16 v[30:31], v7 offset:26624\n\t" \
    "v_mfma_f32_16x16x32_bf16 v[56:59], a[160:163], v[40:43], v[56:59]\n\tv_mul_f32 v114, v114, v222\n\tv_mul_f32 v115, v115, v223\n\tv_mfma_f32_16x16x32_bf16 v[64:67], a[164:167], v[40:43], v[64:67]\n\tv_lshlrev_b32 v220, 16, v214\n\tv_and_b32 v221, 0xffff0000, v214\n\ts_waitcnt lgkmcnt(8)\n\tv_mfma_f32_16x16x32_bf16 v[52:55], a[156:159], v[44:47], v[52:55]\n\t" \
    "ds_read_b64_tr_b16 v[32:33], v6 offset:28672\n\tds_read_b64_tr_b16 v[34:35], v6 offset:30720\n\ts_add_u32 s68, s55, 4096\n\tv_mfma_f32_16x16x32_bf16 v[60:63], a[160:163], v[44:47], v[60:63]\n\tbuffer_load_dwordx4 v[144:147], v8, s[8:11], s68 offen offset:0 sc1\n\tv_mul_f32 v116, v116, v220\n\tv_mfma_f32_16x16x32_bf16 v[68:71], a[164:167], v[44:47], v[68:71]\n\tv_mul_f32 v117, v117, v221\n\t" \
    "v_lshlrev_b32 v222, 16, v215\n\ts_waitcnt lgkmcnt(8)\n\tv_mfma_f32_16x16x32_bf16 v[48:51], a[168:171], v[16:19], v[48:51]\n\tds_read_b64_tr_b16 v[36:37], v7 offset:28672\n\tds_read_b64_tr_b16 v[38:39], v7 offset:30720\n\tv_mfma_f32_16x16x32_bf16 v[56:59], a[172:175], v[16:19], v[56:59]\n\tbuffer_load_dwordx4 v[148:151], v8, s[8:11], s68 offen offset:1024 sc1\n\tv_and_b32 v223, 0xffff0000, v215\n\t" \
    "v_mfma_f32_16x16x32_bf16 v[64:67], a[176:179], v[16:19], v[64:67]\n\tv_mul_f32 v118, v118, v222\n\tv_mul_f32 v119, v119, v223\n\ts_waitcnt lgkmcnt(8)\n\tv_mfma_f32_16x16x32_bf16 v[52:55], a[168:171], v[20:23], v[52:55]\n\tds_read_b64_tr_b16 v[40:41], v6 offset:32768\n\tds_read_b64_tr_b16 v[42:43], v6 offset:34816\n\tv_mfma_f32_16x16x32_bf16 v[60:63], a[172:175], v[20:23], v[60:63]\n\t" \
    "buffer_load_dwordx4 v[152:155], v8, s[8:11], s68 offen offset:2048 sc1\n\tv_mfma_f32_16x16x32_bf16 v[68:71], a[176:179], v[20:23], v[68:71]\n\ts_waitcnt lgkmcnt(8)\n\tv_mfma_f32_16x16x32_bf16 v[48:51], a[180:183], v[24:27], v[48:51]\n\tds_read_b64_tr_b16 v[44:45], v7 offset:32768\n\tds_read_b64_tr_b16 v[46:47], v7 offset:34816\n\tv_mfma_f32_16x16x32_bf16 v[56:59], a[184:187], v[24:27], v[56:59]\n\tbuffer_load_dwordx4 v[156:159], v8, s[8:11], s68 offen offset:3072 sc1\n\t" \
    "v_mfma_f32_16x16x32_bf16 v[64:67], a[188:191], v[24:27], v[64:67]\n\ts_waitcnt lgkmcnt(8)\n\tv_mfma_f32_16x16x32_bf16 v[52:55], a[180:183], v[28:31], v[52:55]\n\tv_mfma_f32_16x16x32_bf16 v[60:63], a[184:187], v[28:31], v[60:63]\n\tv_mfma_f32_16x16x32_bf16 v[68:71], a[188:191], v[28:31], v[68:71]\n\ts_waitcnt lgkmcnt(6)\n\tv_mfma_f32_16x16x32_bf16 v[48:51], a[192:195], v[32:35], v[48:51]\n\ts_add_u32 s68, s55, 8192\n\t" \
    "v_mfma_f32_16x16x32_bf16 v[56:59], a[196:199], v[32:35], v[56:59]\n\tbuffer_load_dwordx4 v[168:171], v8, s[8:11], s68 offen offset:0 sc1\n\tv_mfma_f32_16x16x32_bf16 v[64:67], a[200:203], v[32:35], v[64:67]\n\ts_waitcnt lgkmcnt(4)\n\tv_mfma_f32_16x16x32_bf16 v[52:55], a[192:195], v[36:39], v[52:55]\n\tv_mfma_f32_16x16x32_bf16 v[60:63], a[196:199], v[36:39], v[60:63]\n\tbuffer_load_dwordx4 v[172:175], v8, s[8:11], s68 offen offset:1024 sc1\n\tv_mfma_f32_16x16x32_bf16 v[68:71], a[200:203], v[36:39], v[68:71]\n\t" \
    "s_waitcnt lgkmcnt(2)\n\tv_mfma_f32_16x16x32_bf16 v[48:51], a[204:207], v[40:43], v[48:51]\n\tv_mfma_f32_16x16x32_bf16 v[56:59], a[208:211], v[40:43], v[56:59]\n\tbuffer_load_dwordx4 v[176:179], v8, s[8:11], s68 offen offset:2048 sc1\n\tv_mfma_f32_16x16x32_bf16 v[64:67], a[212:215], v[40:43], v[64:67]\n\ts_waitcnt lgkmcnt(0)\n\tv_mfma_f32_16x16x32_bf16 v[52:55], a[204:207], v[44:47], v[52:55]\n\tv_mfma_f32_16x16x32_bf16 v[60:63], a[208:211], v[44:47], v[60:63]\n\t" \
    "v_mfma_f32_16x16x32_bf16 v[68:71], a[212:215], v[44:47], v[68:71]\n\tbuffer_load_dwordx4 v[180:183], v8, s[8:11], s68 offen offset:3072 sc1\n\ts_cmp_eq_u32 s70, 0\n\ts_cbranch_scc1 113f\n\tv_cmp_ne_u64 vcc, s[36:37], v[250:251]\n\ts_cbranch_vccz 115f\n\ts_mov_b32 s71, 1048576\n\t114:\n\t" \
    "s_sleep 1\n\tbuffer_load_dwordx2 v[250:251], v241, s[8:11], s51 offen sc1\n\ts_waitcnt vmcnt(0)\n\tv_cmp_ne_u64 vcc, s[36:37], v[250:251]\n\ts_cbranch_vccz 115f\n\ts_sub_u32 s71, s71, 1\n\ts_cbranch_scc0 114b\n\t115:\n\t" \
    "buffer_load_dwordx4 v[204:207], v8, s[8:11], s56 offen offset:0 sc1\n\tbuffer_load_dwordx4 v[208:211], v8, s[8:11], s56 offen offset:1024 sc1\n\tbuffer_load_dwordx4 v[212:215], v8, s[8:11], s56 offen offset:2048 sc1\n\tv_fma_f32 v96, v72, v216, v217\n\tv_fma_f32 v97, v73, v216, v217\n\tv_fma_f32 v98, v74, v216, v217\n\tv_fma_f32 v99, v75, v216, v217\n\tv_fma_f32 v100, v76, v218, v219\n\t" \
    "v_fma_f32 v101, v77, v218, v219\n\tv_fma_f32 v102, v78, v218, v219\n\tv_fma_f32 v103, v79, v218, v219\n\tv_fma_f32 v104, v80, v216, v217\n\tv_fma_f32 v105, v81, v216, v217\n\tv_fma_f32 v106, v82, v216, v217\n\tv_fma_f32 v107, v83, v216, v217\n\tv_fma_f32 v108, v84, v218, v219\n\t" \
    "v_fma_f32 v109, v85, v218, v219\n\tv_fma_f32 v110, v86, v218, v219\n\tv_fma_f32 v111, v87, v218, v219\n\tv_fma_f32 v112, v88, v216, v217\n\tv_fma_f32 v113, v89, v216, v217\n\tv_fma_f32 v114, v90, v216, v217\n\tv_fma_f32 v115, v91, v216, v217\n\tv_fma_f32 v116, v92, v218, v219\n\t" \
    "v_fma_f32 v117, v93, v218, v219\n\tv_fma_f32 v118, v94, v218, v219\n\tv_fma_f32 v119, v95, v218, v219\n\ts_waitcnt vmcnt(0)\n\tv_lshlrev_b32 v220, 16, v204\n\tv_and_b32 v221, 0xffff0000, v204\n\tv_mul_f32 v96, v96, v220\n\tv_mul_f32 v97, v97, v221\n\t" \
    "v_lshlrev_b32 v222, 16, v205\n\tv_and_b32 v223, 0xffff0000, v205\n\tv_mul_f32 v98, v98, v222\n\tv_mul_f32 v99, v99, v223\n\tv_lshlrev_b32 v220, 16, v206\n\tv_and_b32 v221, 0xffff0000, v206\n\tv_mul_f32 v100, v100, v220\n\tv_mul_f32 v101, v101, v221\n\t" \
    "v_lshlrev_b32 v222, 16, v207\n\tv_and_b32 v223, 0xffff0000, v207\n\tv_mul_f32 v102, v102, v222\n\tv_mul_f32 v103, v103, v223\n\tv_lshlrev_b32 v220, 16, v208\n\tv_and_b32 v221, 0xffff0000, v208\n\tv_mul_f32 v104, v104, v220\n\tv_mul_f32 v105, v105, v221\n\t" \
    "v_lshlrev_b32 v222, 16, v209\n\tv_and_b32 v223, 0xffff0000, v209\n\tv_mul_f32 v106, v106, v222\n\tv_mul_f32 v107, v107, v223\n\tv_lshlrev_b32 v220, 16, v210\n\tv_and_b32 v221, 0xffff0000, v210\n\tv_mul_f32 v108, v108, v220\n\tv_mul_f32 v109, v109, v221\n\t" \
    "v_lshlrev_b32 v222, 16, v211\n\tv_and_b32 v223, 0xffff0000, v211\n\tv_mul_f32 v110, v110, v222\n\tv_mul_f32 v111, v111, v223\n\tv_lshlrev_b32 v220, 16, v212\n\tv_and_b32 v221, 0xffff0000, v212\n\tv_mul_f32 v112, v112, v220\n\tv_mul_f32 v113, v113, v221\n\t" \
    "v_lshlrev_b32 v222, 16, v213\n\tv_and_b32 v223, 0xffff0000, v213\n\tv_mul_f32 v114, v114, v222\n\tv_mul_f32 v115, v115, v223\n\tv_lshlrev_b32 v220, 16, v214\n\tv_and_b32 v221, 0xffff0000, v214\n\tv_mul_f32 v116, v116, v220\n\tv_mul_f32 v117, v117, v221\n\t" \
    "v_lshlrev_b32 v222, 16, v215\n\tv_and_b32 v223, 0xffff0000, v215\n\tv_mul_f32 v118, v118, v222\n\tv_mul_f32 v119, v119, v223\n\t113:\n\ts_mov_b32 s71, 1048576\n\ts_waitcnt vmcnt(8)\n\t116:\n\t" \
    "v_xor_b32 v40, s36, v123\n\tv_xor_b32 v41, s36, v127\n\tv_xor_b32 v42, s36, v131\n\tv_xor_b32 v43, s36, v135\n\tv_or3_b32 v40, v40, v41, v42\n\tv_or_b32 v40, v40, v43\n\tv_cmp_ne_u32 vcc, 0, v40\n\ts_cbranch_vccz 117f\n\t" \
    "s_sub_u32 s71, s71, 1\n\ts_cbranch_scc1 117f\n\ts_add_u32 s68, s55, 0\n\tbuffer_load_dwordx4 v[120:123], v8, s[8:11], s68 offen offset:0 sc1\n\tbuffer_load_dwordx4 v[124:127], v8, s[8:11], s68 offen offset:1024 sc1\n\tbuffer_load_dwordx4 v[128:131], v8, s[8:11], s68 offen offset:2048 sc1\n\tbuffer_load_dwordx4 v[132:135], v8, s[8:11], s68 offen offset:3072 sc1\n\ts_waitcnt vmcnt(0)\n\t" \
    "s_branch 116b\n\t117:\n\tv_lshlrev_b32 v16, 16, v120\n\tv_and_b32 v17, 0xffff0000, v120\n\tv_lshlrev_b32 v18, 16, v121\n\tv_and_b32 v19, 0xffff0000, v121\n\tv_lshlrev_b32 v20, 16, v122\n\tv_and_b32 v21, 0xffff0000, v122\n\t" \
    "v_lshlrev_b32 v22, 16, v124\n\tv_and_b32 v23, 0xffff0000, v124\n\tv_lshlrev_b32 v24, 16, v125\n\tv_and_b32 v25, 0xffff0000, v125\n\tv_lshlrev_b32 v26, 16, v126\n\tv_and_b32 v27, 0xffff0000, v126\n\tv_lshlrev_b32 v28, 16, v128\n\tv_and_b32 v29, 0xffff0000, v128\n\t" \
    "v_lshlrev_b32 v30, 16, v129\n\tv_and_b32 v31, 0xffff0000, v129\n\tv_lshlrev_b32 v32, 16, v130\n\tv_and_b32 v33, 0xffff0000, v130\n\tv_lshlrev_b32 v34, 16, v132\n\tv_and_b32 v35, 0xffff0000, v132\n\tv_lshlrev_b32 v36, 16, v133\n\tv_and_b32 v37, 0xffff0000, v133\n\t" \
    "v_lshlrev_b32 v38, 16, v134\n\tv_and_b32 v39, 0xffff0000, v134\n\tv_pk_add_f32 v[48:49], v[48:49], v[16:17]\n\tv_pk_add_f32 v[50:51], v[50:51], v[18:19]\n\tv_pk_add_f32 v[52:53], v[52:53], v[20:21]\n\tv_pk_add_f32 v[54:55], v[54:55], v[22:23]\n\tv_pk_add_f32 v[56:57], v[56:57], v[24:25]\n\tv_pk_add_f32 v[58:59], v[58:59], v[26:27]\n\t" \
    "v_pk_add_f32 v[60:61], v[60:61], v[28:29]\n\tv_pk_add_f32 v[62:63], v[62:63], v[30:31]\n\tv_pk_add_f32 v[64:65], v[64:65], v[32:33]\n\tv_pk_add_f32 v[66:67], v[66:67], v[34:35]\n\tv_pk_add_f32 v[68:69], v[68:69], v[36:37]\n\tv_pk_add_f32 v[70:71], v[70:71], v[38:39]\n\ts_mov_b32 s71, 1048576\n\ts_waitcnt vmcnt(4)\n\t" \
    "118:\n\tv_xor_b32 v40, s36, v147\n\tv_xor_b32 v41, s36, v151\n\tv_xor_b32 v42, s36, v155\n\tv_xor_b32 v43, s36, v159\n\tv_or3_b32 v40, v40, v41, v42\n\tv_or_b32 v40, v40, v43\n\tv_cmp_ne_u32 vcc, 0, v40\n\t" \
    "s_cbranch_vccz 119f\n\ts_sub_u32 s71, s71, 1\n\ts_cbranch_scc1 119f\n\ts_add_u32 s68, s55, 4096\n\tbuffer_load_dwordx4 v[144:147], v8, s[8:11], s68 offen offset:0 sc1\n\tbuffer_load_dwordx4 v[148:151], v8, s[8:11], s68 offen offset:1024 sc1\n\tbuffer_load_dwordx4 v[152:155], v8, s[8:11], s68 offen offset:2048 sc1\n\tbuffer_load_dwordx4 v[156:159], v8, s[8:11], s68 offen offset:3072 sc1\n\t" \
    "s_waitcnt vmcnt(0)\n\ts_branch 118b\n\t119:\n\tv_lshlrev_b32 v16, 16, v144\n\tv_and_b32 v17, 0xffff0000, v144\n\tv_lshlrev_b32 v18, 16, v145\n\tv_and_b32 v19, 0xffff0000, v145\n\tv_lshlrev_b32 v20, 16, v146\n\t" \
    "v_and_b32 v21, 0xffff0000, v146\n\tv_lshlrev_b32 v22, 16, v148\n\tv_and_b32 v23, 0xffff0000, v148\n\tv_lshlrev_b32 v24, 16, v149\n\tv_and_b32 v25, 0xffff0000, v149\n\tv_lshlrev_b32 v26, 16, v150\n\tv_and_b32 v27, 0xffff0000, v150\n\tv_lshlrev_b32 v28, 16, v152\n\t" \
    "v_and_b32 v29, 0xffff0000, v152\n\tv_lshlrev_b32 v30, 16, v153\n\tv_and_b32 v31, 0xffff0000, v153\n\tv_lshlrev_b32 v32, 16, v154\n\tv_and_b32 v33, 0xffff0000, v154\n\tv_lshlrev_b32 v34, 16, v156\n\tv_and_b32 v35, 0xffff0000, v156\n\tv_lshlrev_b32 v36, 16, v157\n\t" \
    "v_and_b32 v37, 0xffff0000, v157\n\tv_lshlrev_b32 v38, 16, v158\n\tv_and_b32 v39, 0xffff0000, v158\n\tv_pk_add_f32 v[48:49], v[48:49], v[16:17]\n\tv_pk_add_f32 v[50:51], v[50:51], v[18:19]\n\tv_pk_add_f32 v[52:53], v[52:53], v[20:21]\n\tv_pk_add_f32 v[54:55], v[54:55], v[22:23]\n\tv_pk_add_f32 v[56:57], v[56:57], v[24:25]\n\t" \
    "v_pk_add_f32 v[58:59], v[58:59], v[26:27]\n\tv_pk_add_f32 v[60:61], v[60:61], v[28:29]\n\tv_pk_add_f32 v[62:63], v[62:63], v[30:31]\n\tv_pk_add_f32 v[64:65], v[64:65], v[32:33]\n\tv_pk_add_f32 v[66:67], v[66:67], v[34:35]\n\tv_pk_add_f32 v[68:69], v[68:69], v[36:37]\n\tv_pk_add_f32 v[70:71], v[70:71], v[38:39]\n\ts_mov_b32 s71, 1048576\n\t" \
    "s_waitcnt vmcnt(0)\n\t120:\n\tv_xor_b32 v40, s36, v171\n\tv_xor_b32 v41, s36, v175\n\tv_xor_b32 v42, s36, v179\n\tv_xor_b32 v43, s36, v183\n\tv_or3_b32 v40, v40, v41, v42\n\tv_or_b32 v40, v40, v43\n\t" \
    "v_cmp_ne_u32 vcc, 0, v40\n\ts_cbranch_vccz 121f\n\ts_sub_u32 s71, s71, 1\n\ts_cbranch_scc1 121f\n\ts_add_u32 s68, s55, 8192\n\tbuffer_load_dwordx4 v[168:171], v8, s[8:11], s68 offen offset:0 sc1\n\tbuffer_load_dwordx4 v[172:175], v8, s[8:11], s68 offen offset:1024 sc1\n\tbuffer_load_dwordx4 v[176:179], v8, s[8:11], s68 offen offset:2048 sc1\n\t" \
    "buffer_load_dwordx4 v[180:183], v8, s[8:11], s68 offen offset:3072 sc1\n\ts_waitcnt vmcnt(0)\n\ts_branch 120b\n\t121:\n\ts_mov_b32 s68, 0xbfb8aa3b\n\ts_mov_b32 s69, 0xbfb8aa3b\n\tv_lshlrev_b32 v16, 16, v168\n\tv_and_b32 v17, 0xffff0000, v168\n\t" \
    "v_lshlrev_b32 v18, 16, v169\n\tv_and_b32 v19, 0xffff0000, v169\n\tv_lshlrev_b32 v20, 16, v170\n\tv_and_b32 v21, 0xffff0000, v170\n\tv_lshlrev_b32 v22, 16, v172\n\tv_and_b32 v23, 0xffff0000, v172\n\tv_pk_add_f32 v[48:49], v[48:49], v[16:17]\n\tv_pk_add_f32 v[50:51], v[50:51], v[18:19]\n\t" \
    "v_pk_add_f32 v[52:53], v[52:53], v[20:21]\n\tv_pk_add_f32 v[54:55], v[54:55], v[22:23]\n\tv_pk_mul_f32 v[16:17], v[48:49], s[68:69]\n\tv_pk_mul_f32 v[18:19], v[50:51], s[68:69]\n\tv_pk_mul_f32 v[20:21], v[52:53], s[68:69]\n\tv_pk_mul_f32 v[22:23], v[54:55], s[68:69]\n\tv_lshlrev_b32 v24, 16, v173\n\tv_exp_f32 v16, v16\n\t" \
    "v_and_b32 v25, 0xffff0000, v173\n\tv_exp_f32 v17, v17\n\tv_lshlrev_b32 v26, 16, v174\n\tv_exp_f32 v18, v18\n\tv_and_b32 v27, 0xffff0000, v174\n\tv_exp_f32 v19, v19\n\tv_lshlrev_b32 v28, 16, v176\n\tv_exp_f32 v20, v20\n\t" \
    "v_and_b32 v29, 0xffff0000, v176\n\tv_exp_f32 v21, v21\n\tv_lshlrev_b32 v30, 16, v177\n\tv_exp_f32 v22, v22\n\tv_and_b32 v31, 0xffff0000, v177\n\tv_exp_f32 v23, v23\n\tv_pk_add_f32 v[56:57], v[56:57], v[24:25]\n\tv_pk_add_f32 v[16:17], v[16:17], 1.0 op_sel_hi:[1,0]\n\t" \
    "v_pk_add_f32 v[18:19], v[18:19], 1.0 op_sel_hi:[1,0]\n\tv_pk_add_f32 v[20:21], v[20:21], 1.0 op_sel_hi:[1,0]\n\tv_pk_add_f32 v[22:23], v[22:23], 1.0 op_sel_hi:[1,0]\n\tv_pk_add_f32 v[58:59], v[58:59], v[26:27]\n\tv_rcp_f32 v16, v16\n\tv_pk_add_f32 v[60:61], v[60:61], v[28:29]\n\tv_rcp_f32 v17, v17\n\tv_pk_add_f32 v[62:63], v[62:63], v[30:31]\n\t" \
    "v_rcp_f32 v18, v18\n\tv_pk_mul_f32 v[24:25], v[56:57], s[68:69]\n\tv_rcp_f32 v19, v19\n\tv_pk_mul_f32 v[26:27], v[58:59], s[68:69]\n\tv_rcp_f32 v20, v20\n\tv_pk_mul_f32 v[28:29], v[60:61], s[68:69]\n\tv_rcp_f32 v21, v21\n\tv_pk_mul_f32 v[30:31], v[62:63], s[68:69]\n\t" \
    "v_rcp_f32 v22, v22\n\tv_lshlrev_b32 v32, 16, v178\n\tv_rcp_f32 v23, v23\n\tv_and_b32 v33, 0xffff0000, v178\n\tv_exp_f32 v24, v24\n\tv_pk_mul_f32 v[48:49], v[48:49], v[96:97]\n\tv_exp_f32 v25, v25\n\tv_pk_mul_f32 v[50:51], v[50:51], v[98:99]\n\t" \
    "v_exp_f32 v26, v26\n\tv_pk_mul_f32 v[52:53], v[52:53], v[100:101]\n\tv_exp_f32 v27, v27\n\tv_pk_mul_f32 v[54:55], v[54:55], v[102:103]\n\tv_exp_f32 v28, v28\n\tv_pk_fma_f32 v[16:17], v[16:17], v[48:49], v[72:73]\n\tv_exp_f32 v29, v29\n\tv_pk_fma_f32 v[18:19], v[18:19], v[50:51], v[74:75]\n\t" \
    "v_exp_f32 v30, v30\n\tv_pk_fma_f32 v[20:21], v[20:21], v[52:53], v[76:77]\n\tv_exp_f32 v31, v31\n\tv_pk_fma_f32 v[22:23], v[22:23], v[54:55], v[78:79]\n\tv_cvt_pk_bf16_f32 v48, v16, v17\n\tv_cvt_pk_bf16_f32 v49, v18, v19\n\tv_cvt_pk_bf16_f32 v50, v20, v21\n\tv_cvt_pk_bf16_f32 v51, v22, v23\n\t" \
    "s_nop 0\n\tv_permlane16_swap_b32 v48, v50\n\tv_permlane16_swap_b32 v49, v51\n\tbuffer_store_dwordx4 v[48:51], v243, s[24:27], 0 offen offset:0\n\tv_pk_add_f32 v[24:25], v[24:25], 1.0 op_sel_hi:[1,0]\n\tv_pk_add_f32 v[26:27], v[26:27], 1.0 op_sel_hi:[1,0]\n\tv_pk_add_f32 v[28:29], v[28:29], 1.0 op_sel_hi:[1,0]\n\tv_pk_add_f32 v[30:31], v[30:31], 1.0 op_sel_hi:[1,0]\n\t" \
    "v_lshlrev_b32 v34, 16, v180\n\tv_rcp_f32 v24, v24\n\tv_and_b32 v35, 0xffff0000, v180\n\tv_rcp_f32 v25, v25\n\tv_lshlrev_b32 v36, 16, v181\n\tv_rcp_f32 v26, v26\n\tv_and_b32 v37, 0xffff0000, v181\n\tv_rcp_f32 v27, v27\n\t" \
    "v_lshlrev_b32 v38, 16, v182\n\tv_rcp_f32 v28, v28\n\tv_and_b32 v39, 0xffff0000, v182\n\tv_rcp_f32 v29, v29\n\tv_pk_add_f32 v[64:65], v[64:65], v[32:33]\n\tv_rcp_f32 v30, v30\n\tv_pk_add_f32 v[66:67], v[66:67], v[34:35]\n\tv_rcp_f32 v31, v31\n\t" \
    "v_pk_add_f32 v[68:69], v[68:69], v[36:37]\n\tv_pk_mul_f32 v[56:57], v[56:57], v[104:105]\n\tv_pk_mul_f32 v[58:59], v[58:59], v[106:107]\n\tv_pk_mul_f32 v[60:61], v[60:61], v[108:109]\n\tv_pk_mul_f32 v[62:63], v[62:63], v[110:111]\n\tv_pk_fma_f32 v[24:25], v[24:25], v[56:57], v[80:81]\n\tv_pk_fma_f32 v[26:27], v[26:27], v[58:59], v[82:83]\n\tv_pk_fma_f32 v[28:29], v[28:29], v[60:61], v[84:85]\n\t" \
    "v_pk_fma_f32 v[30:31], v[30:31], v[62:63], v[86:87]\n\tv_cvt_pk_bf16_f32 v56, v24, v25\n\tv_cvt_pk_bf16_f32 v57, v26, v27\n\tv_cvt_pk_bf16_f32 v58, v28, v29\n\tv_cvt_pk_bf16_f32 v59, v30, v31\n\ts_nop 0\n\tv_permlane16_swap_b32 v56, v58\n\tv_permlane16_swap_b32 v57, v59\n\t" \
    "buffer_store_dwordx4 v[56:59], v243, s[24:27], 0 offen offset:32\n\tv_pk_add_f32 v[70:71], v[70:71], v[38:39]\n\tv_pk_mul_f32 v[32:33], v[64:65], s[68:69]\n\tv_pk_mul_f32 v[34:35], v[66:67], s[68:69]\n\tv_pk_mul_f32 v[36:37], v[68:69], s[68:69]\n\tv_pk_mul_f32 v[38:39], v[70:71], s[68:69]\n\tv_exp_f32 v32, v32\n\tv_exp_f32 v33, v33\n\t" \
    "v_exp_f32 v34, v34\n\tv_exp_f32 v35, v35\n\tv_exp_f32 v36, v36\n\tv_exp_f32 v37, v37\n\tv_exp_f32 v38, v38\n\tv_exp_f32 v39, v39\n\tv_pk_add_f32 v[32:33], v[32:33], 1.0 op_sel_hi:[1,0]\n\tv_pk_add_f32 v[34:35], v[34:35], 1.0 op_sel_hi:[1,0]\n\t" \
    "v_pk_add_f32 v[36:37], v[36:37], 1.0 op_sel_hi:[1,0]\n\tv_pk_add_f32 v[38:39], v[38:39], 1.0 op_sel_hi:[1,0]\n\tv_rcp_f32 v32, v32\n\tv_rcp_f32 v33, v33\n\tv_rcp_f32 v34, v34\n\tv_rcp_f32 v35, v35\n\tv_rcp_f32 v36, v36\n\tv_rcp_f32 v37, v37\n\t" \
    "v_rcp_f32 v38, v38\n\tv_rcp_f32 v39, v39\n\tv_pk_mul_f32 v[64:65], v[64:65], v[112:113]\n\tv_pk_mul_f32 v[66:67], v[66:67], v[114:115]\n\tv_pk_mul_f32 v[68:69], v[68:69], v[116:117]\n\tv_pk_mul_f32 v[70:71], v[70:71], v[118:119]\n\tv_pk_fma_f32 v[32:33], v[32:33], v[64:65], v[88:89]\n\tv_pk_fma_f32 v[34:35], v[34:35], v[66:67], v[90:91]\n\t" \
    "v_pk_fma_f32 v[36:37], v[36:37], v[68:69], v[92:93]\n\tv_pk_fma_f32 v[38:39], v[38:39], v[70:71], v[94:95]\n\tv_cvt_pk_bf16_f32 v64, v32, v33\n\tv_cvt_pk_bf16_f32 v65, v34, v35\n\tv_cvt_pk_bf16_f32 v66, v36, v37\n\tv_cvt_pk_bf16_f32 v67, v38, v39\n\ts_nop 0\n\tv_permlane16_swap_b32 v64, v66\n\t" \
    "v_permlane16_swap_b32 v65, v67\n\tbuffer_store_dwordx4 v[64:67], v243, s[24:27], 0 offen offset:64\n\ts_branch 103f\n\t102:\n\ts_add_u32 s49, s49, 4096\n\ts_waitcnt vmcnt(0) lgkmcnt(0)\n\tbuffer_store_dwordx2 v[248:249], off, s[8:11], s49 sc1\n\ts_load_dwordx8 s[12:19], s[0:1], 16\n\t" \
    "s_lshr_b32 s67, s40, 3\n\ts_sub_u32 s67, s67, 24\n\ts_lshl_b32 s67, s67, 2\n\ts_add_u32 s67, s67, s38\n\ts_lshl_b32 s65, s67, 6\n\tv_add_u32 v6, s65, v240\n\tv_mul_u32_u24 v7, 52429, v6\n\tv_lshrrev_b32 v7, 18, v7\n\t" \
    "v_mul_u32_u24 v5, 5, v7\n\tv_sub_u32 v6, v6, v5\n\tv_lshrrev_b32 v5, 7, v7\n\tv_and_b32 v7, 127, v7\n\tv_lshl_add_u32 v7, v5, 10, v7\n\tv_mul_u32_u24 v7, 9216, v7\n\tv_lshl_add_u32 v6, v6, 8, v7\n\tv_lshlrev_b32 v7, 3, v244\n\t" \
    "v_lshl_add_u32 v7, v245, 12, v7\n\ts_and_b32 s65, s40, 7\n\ts_mul_i32 s66, s65, 9\n\ts_lshr_b32 s66, s66, 1\n\ts_lshl_b32 s66, s66, 8\n\ts_lshr_b32 s73, s65, 1\n\ts_mul_i32 s73, s73, 4718592\n\ts_and_b32 s65, s65, 1\n\t" \
    "s_lshl_b32 s65, s65, 11\n\ts_add_u32 s65, s65, s73\n\ts_waitcnt lgkmcnt(0)\n\ts_add_u32 s12, s12, s66\n\ts_addc_u32 s13, s13, 0\n\ts_sub_u32 s14, s14, s66\n\ts_add_u32 s16, s16, s65\n\ts_addc_u32 s17, s17, 0\n\t" \
    "s_sub_u32 s18, s18, s65\n\tv_mov_b32 v238, 2146435072\n\tv_mov_b32 v236, 2146435072\n\tv_mul_u32_u24 v237, 12, v240\n\ts_add_u32 s73, s67, 220\n\tv_add_u32 v237, s73, v237\n\tv_lshrrev_b32 v5, 4, v237\n\tv_lshlrev_b32 v5, 7, v5\n\t" \
    "v_cmp_le_u32 vcc, 144, v237\n\tv_mov_b32 v4, 1088\n\tv_xor_b32 v3, 8, v237\n\tv_cndmask_b32 v4, 0, v4, vcc\n\tv_and_b32 v3, 15, v3\n\tv_lshl_add_u32 v5, v3, 2, v5\n\tv_sub_u32 v5, v5, v4\n\tv_subrev_u32 v237, 220, v237\n\t" \
    "v_cmp_gt_u32 vcc, 68, v237\n\tv_lshlrev_b32 v5, 12, v5\n\ts_mov_b32 s43, 0\n\tv_cndmask_b32 v237, v238, v5, vcc\n\ts_nop 0\n\tv_readlane_b32 s41, v236, 0\n\tv_readlane_b32 s42, v237, 0\n\ts_nop 4\n\t" \
    "buffer_load_dword v15, v7, s[16:19], s42 offen\n\ts_add_u32 s43, s43, 1\n\tv_readlane_b32 s42, v237, s43\n\ts_nop 4\n\tbuffer_load_dword v15, v7, s[16:19], s42 offen\n\ts_add_u32 s43, s43, 1\n\tv_readlane_b32 s42, v237, s43\n\ts_nop 4\n\t" \
    "buffer_load_dword v15, v7, s[16:19], s42 offen\n\ts_add_u32 s43, s43, 1\n\tv_readlane_b32 s42, v237, s43\n\ts_nop 4\n\tbuffer_load_dword v15, v7, s[16:19], s42 offen\n\ts_add_u32 s43, s43, 1\n\tv_readlane_b32 s42, v237, s43\n\ts_nop 4\n\t" \
    "buffer_load_dword v15, v7, s[16:19], s42 offen\n\ts_add_u32 s43, s43, 1\n\tv_readlane_b32 s42, v237, s43\n\ts_nop 4\n\tbuffer_load_dword v15, v7, s[16:19], s42 offen\n\ts_add_u32 s43, s43, 1\n\tv_readlane_b32 s42, v237, s43\n\ts_nop 4\n\t" \
    "s_waitcnt vmcnt(0)\n\ts_branch 103f\n\t101:\n\ts_load_dwordx8 s[12:19], s[0:1], 16\n\ts_lshr_b32 s67, s40, 3\n\ts_sub_u32 s67, s67, 27\n\ts_lshl_b32 s67, s67, 2\n\ts_add_u32 s67, s67, s38\n\t" \
    "s_lshl_b32 s65, s67, 6\n\tv_add_u32 v6, s65, v240\n\tv_mul_u32_u24 v7, 52429, v6\n\tv_lshrrev_b32 v7, 18, v7\n\tv_mul_u32_u24 v5, 5, v7\n\tv_sub_u32 v6, v6, v5\n\tv_lshrrev_b32 v5, 7, v7\n\tv_and_b32 v7, 127, v7\n\t" \
    "v_lshl_add_u32 v7, v5, 10, v7\n\tv_mul_u32_u24 v7, 9216, v7\n\tv_lshl_add_u32 v6, v6, 8, v7\n\tv_lshlrev_b32 v7, 3, v244\n\tv_lshl_add_u32 v7, v245, 12, v7\n\ts_and_b32 s65, s40, 7\n\ts_mul_i32 s66, s65, 9\n\ts_lshr_b32 s66, s66, 1\n\t" \
    "s_lshl_b32 s66, s66, 8\n\ts_lshr_b32 s73, s65, 1\n\ts_mul_i32 s73, s73, 4718592\n\ts_and_b32 s65, s65, 1\n\ts_lshl_b32 s65, s65, 11\n\ts_add_u32 s65, s65, s73\n\ts_waitcnt lgkmcnt(0)\n\ts_add_u32 s12, s12, s66\n\t" \
    "s_addc_u32 s13, s13, 0\n\ts_sub_u32 s14, s14, s66\n\ts_add_u32 s16, s16, s65\n\ts_addc_u32 s17, s17, 0\n\ts_sub_u32 s18, s18, s65\n\tv_mov_b32 v238, 2146435072\n\tv_mov_b32 v236, 2146435072\n\ts_mov_b32 s73, 4718592\n\t" \
    "v_writelane_b32 v236, s73, 0\n\ts_mov_b32 s73, 5898240\n\tv_writelane_b32 v236, s73, 1\n\ts_mov_b32 s73, 7077888\n\tv_writelane_b32 v236, s73, 2\n\ts_mov_b32 s73, 8257536\n\tv_writelane_b32 v236, s73, 3\n\ts_mov_b32 s73, 4718720\n\t" \
    "v_writelane_b32 v236, s73, 4\n\ts_mov_b32 s73, 5898368\n\tv_writelane_b32 v236, s73, 5\n\ts_mov_b32 s73, 7078016\n\tv_writelane_b32 v236, s73, 6\n\ts_mov_b32 s73, 8257664\n\tv_writelane_b32 v236, s73, 7\n\tv_mul_u32_u24 v237, 20, v240\n\t" \
    "s_add_u32 s73, s67, -160\n\tv_add_u32 v237, s73, v237\n\tv_cmp_le_i32 vcc, 220, v237\n\tv_add_u32 v5, 68, v237\n\ts_nop 0\n\tv_cndmask_b32 v237, v237, v5, vcc\n\tv_lshrrev_b32 v5, 4, v237\n\tv_lshlrev_b32 v5, 7, v5\n\t" \
    "v_cmp_le_u32 vcc, 144, v237\n\tv_mov_b32 v4, 1088\n\tv_xor_b32 v3, 8, v237\n\tv_cndmask_b32 v4, 0, v4, vcc\n\tv_and_b32 v3, 15, v3\n\tv_lshl_add_u32 v5, v3, 2, v5\n\tv_sub_u32 v5, v5, v4\n\tv_subrev_u32 v237, 0, v237\n\t" \
    "v_cmp_gt_u32 vcc, 288, v237\n\tv_lshlrev_b32 v5, 12, v5\n\ts_mov_b32 s43, 0\n\tv_cndmask_b32 v237, v238, v5, vcc\n\ts_nop 0\n\tv_readlane_b32 s41, v236, 0\n\tv_readlane_b32 s42, v237, 0\n\ts_nop 4\n\t" \
    "s_sub_u32 s65, s40, 216\n\ts_lshl_b32 s44, s65, 4\n\ts_mul_i32 s66, s65, 12\n\ts_add_u32 s66, s66, 96\n\ts_cmp_lt_u32 s65, 24\n\ts_cselect_b32 s44, s44, s66\n\ts_cselect_b32 s45, 0, 2146435072\n\tv_cmp_eq_u32 vcc, 0, v240\n\t" \
    "v_subrev_u32 v13, 8, v8\n\tv_mov_b32 v12, 2146435072\n\tv_cndmask_b32 v13, v13, v12, vcc\n\ts_mov_b32 s4, s2\n\ts_mov_b32 s5, s3\n\ts_mov_b32 s6, 98f-99b\n\ts_mov_b32 s7, 0x20000\n\tv_lshlrev_b32 v14, 7, v240\n\t" \
    "s_mov_b32 s46, 0xffff\n\tv_and_b32 v204, 15, v240\n\tv_lshlrev_b32 v205, 3, v245\n\tv_sub_u32 v206, v205, v204\n\tv_add_u32 v206, 31, v206\n\tv_and_b32 v207, 3, v206\n\tv_mul_u32_u24 v207, 4160, v207\n\tv_and_b32 v206, 0xfc, v206\n\t" \
    "v_lshl_add_u32 v0, v206, 1, v207\n\ts_mul_i32 s65, s38, 16640\n\ts_add_u32 s65, s65, 73984\n\tv_add_u32 v0, s65, v0\n\ts_add_u32 s66, s65, 1040\n\tv_sub_u32 v2, s66, v8\n\tv_bfe_u32 v206, v240, 1, 2\n\tv_mul_u32_u24 v206, 4160, v206\n\t" \
    "v_and_b32 v207, 16, v8\n\tv_add3_u32 v9, v206, v207, s65\n\ts_mul_i32 s66, s38, 4624\n\tv_lshl_add_u32 v3, v204, 5, v205\n\tv_add_u32 v3, s66, v3\n\tv_add3_u32 v1, v3, v205, 32\n\tv_add_u32 v3, s66, v239\n\ts_lshl_b32 s73, s38, 4\n\t" \
    "v_add_u32 v206, s73, v245\n\tv_bfe_u32 v207, v240, 2, 2\n\tv_mul_u32_u24 v4, 4624, v207\n\tv_lshl_add_u32 v4, v245, 4, v4\n\tv_and_b32 v207, 3, v240\n\tv_lshl_add_u32 v4, v207, 7, v4\n\tv_add_u32 v4, s54, v4\n\tv_mul_u32_u24 v5, 12288, v206\n\t" \
    "v_add_u32 v5, v5, v244\n\tv_lshrrev_b32 v206, 5, v240\n\ts_lshl_b32 s73, s38, 2\n\tv_add_u32 v206, s73, v206\n\tv_mul_u32_u24 v206, 4624, v206\n\tv_and_b32 v207, 0x1f0, v8\n\tv_add_u32 v12, v206, v207\n\tv_mul_u32_u24 v10, 9216, v245\n\t" \
    "v_add_u32 v10, v10, v244\n\tv_mul_u32_u24 v11, 4624, v204\n\tv_lshl_add_u32 v11, v245, 4, v11\n\tv_add_u32 v11, s54, v11\n\ts_mul_i32 s62, s38, 589824\n\ts_lshl_b32 s73, s44, 4\n\ts_add_u32 s62, s62, s73\n\ts_add_u32 s62, s62, 2490368\n\t" \
    "v_lshrrev_b32 v206, 3, v204\n\tv_add_u32 v206, 2, v206\n\tv_cmp_eq_u32 vcc, v206, v245\n\tv_and_b32 v207, 1, v204\n\tv_lshlrev_b32 v207, 4, v207\n\tv_lshlrev_b32 v207, v207, s46\n\tv_cndmask_b32 v207, 0, v207, vcc\n\tv_bfe_u32 v206, v204, 1, 2\n\t" \
    "v_cmp_eq_u32 vcc, 0, v206\n\tv_mov_b32 v196, 0\n\ts_nop 0\n\tv_cndmask_b32 v232, 0, v207, vcc\n\tv_cmp_eq_u32 vcc, 1, v206\n\tv_mov_b32 v197, 0\n\ts_nop 0\n\tv_cndmask_b32 v233, 0, v207, vcc\n\t" \
    "v_cmp_eq_u32 vcc, 2, v206\n\tv_mov_b32 v198, 0\n\ts_nop 0\n\tv_cndmask_b32 v234, 0, v207, vcc\n\tv_cmp_eq_u32 vcc, 3, v206\n\tv_mov_b32 v199, 0\n\ts_nop 0\n\tv_cndmask_b32 v235, 0, v207, vcc\n\t" \
    "s_add_u32 s65, s44, 0\n\ts_lshr_b32 s65, s65, 6\n\ts_cmp_eq_u32 s65, 8\n\ts_cselect_b32 s65, 1, s65\n\ts_and_b32 s65, s65, 3\n\ts_lshl_b32 s47, s65, 11\n\ts_lshr_b32 s65, s54, 1\n\ts_add_u32 s47, s47, s65\n\t" \
    "s_add_u32 s47, s47, 17408\n\ts_add_u32 s65, s44, 11\n\ts_lshr_b32 s65, s65, 6\n\ts_cmp_eq_u32 s65, 8\n\ts_cselect_b32 s65, 1, s65\n\ts_and_b32 s65, s65, 3\n\ts_lshl_b32 s50, s65, 11\n\ts_lshr_b32 s65, s54, 1\n\t" \
    "s_add_u32 s50, s50, s65\n\ts_add_u32 s50, s50, 17408\n\tds_write_b128 v9, v[196:199] offset:4128\n\tbuffer_load_dword v15, v6, s[12:15], s41 offen\n\tbuffer_load_dword v15, v7, s[16:19], s42 offen\n\ts_add_u32 s43, s43, 1\n\tv_readlane_b32 s41, v236, s43\n\tv_readlane_b32 s42, v237, s43\n\t" \
    "s_nop 4\n\ts_waitcnt vmcnt(2)\n\tbuffer_store_dwordx2 v[248:249], off, s[8:11], s48 sc1\n\tv_alignbit_b32 v196, v167, v167, 16\n\tv_alignbit_b32 v197, v166, v166, 16\n\tv_alignbit_b32 v198, v165, v165, 16\n\tv_alignbit_b32 v199, v164, v164, 16\n\tds_write_b128 v2, v[196:199] offset:3072\n\t" \
    "v_bfi_b32 v200, s46, v167, v166\n\tv_bfi_b32 v201, s46, v166, v165\n\tv_bfi_b32 v202, s46, v165, v164\n\tv_bfi_b32 v203, s46, v164, v181\n\tds_write_b128 v2, v[200:203] offset:7232\n\tv_alignbit_b32 v196, v166, v166, 16\n\tv_alignbit_b32 v197, v165, v165, 16\n\tv_alignbit_b32 v198, v164, v164, 16\n\t" \
    "v_alignbit_b32 v199, v181, v181, 16\n\tds_write_b128 v2, v[196:199] offset:11392\n\tv_bfi_b32 v200, s46, v166, v165\n\tv_bfi_b32 v201, s46, v165, v164\n\tv_bfi_b32 v202, s46, v164, v181\n\tv_bfi_b32 v203, s46, v181, v180\n\tds_write_b128 v2, v[200:203] offset:15552\n\tv_alignbit_b32 v196, v171, v171, 16\n\t" \
    "v_alignbit_b32 v197, v170, v170, 16\n\tv_alignbit_b32 v198, v169, v169, 16\n\tv_alignbit_b32 v199, v168, v168, 16\n\tds_write_b128 v2, v[196:199] offset:2048\n\tv_bfi_b32 v200, s46, v171, v170\n\tv_bfi_b32 v201, s46, v170, v169\n\tv_bfi_b32 v202, s46, v169, v168\n\tv_bfi_b32 v203, s46, v168, v183\n\t" \
    "ds_write_b128 v2, v[200:203] offset:6208\n\tv_alignbit_b32 v196, v170, v170, 16\n\tv_alignbit_b32 v197, v169, v169, 16\n\tv_alignbit_b32 v198, v168, v168, 16\n\tv_alignbit_b32 v199, v183, v183, 16\n\tds_write_b128 v2, v[196:199] offset:10368\n\tv_bfi_b32 v200, s46, v170, v169\n\tv_bfi_b32 v201, s46, v169, v168\n\t" \
    "v_bfi_b32 v202, s46, v168, v183\n\tv_bfi_b32 v203, s46, v183, v182\n\tds_write_b128 v2, v[200:203] offset:14528\n\tv_alignbit_b32 v196, v175, v175, 16\n\tv_alignbit_b32 v197, v174, v174, 16\n\tv_alignbit_b32 v198, v173, v173, 16\n\tv_alignbit_b32 v199, v172, v172, 16\n\tds_write_b128 v2, v[196:199] offset:1024\n\t" \
    "v_bfi_b32 v200, s46, v175, v174\n\tv_bfi_b32 v201, s46, v174, v173\n\tv_bfi_b32 v202, s46, v173, v172\n\tv_bfi_b32 v203, s46, v172, v185\n\tds_write_b128 v2, v[200:203] offset:5184\n\tv_alignbit_b32 v196, v174, v174, 16\n\tv_alignbit_b32 v197, v173, v173, 16\n\tv_alignbit_b32 v198, v172, v172, 16\n\t" \
    "v_alignbit_b32 v199, v185, v185, 16\n\tds_write_b128 v2, v[196:199] offset:9344\n\tv_bfi_b32 v200, s46, v174, v173\n\tv_bfi_b32 v201, s46, v173, v172\n\tv_bfi_b32 v202, s46, v172, v185\n\tv_bfi_b32 v203, s46, v185, v184\n\tds_write_b128 v2, v[200:203] offset:13504\n\tv_alignbit_b32 v196, v179, v179, 16\n\t" \
    "v_alignbit_b32 v197, v178, v178, 16\n\tv_alignbit_b32 v198, v177, v177, 16\n\tv_alignbit_b32 v199, v176, v176, 16\n\tds_write_b128 v2, v[196:199] offset:0\n\tv_bfi_b32 v200, s46, v179, v178\n\tv_bfi_b32 v201, s46, v178, v177\n\tv_bfi_b32 v202, s46, v177, v176\n\tv_bfi_b32 v203, s46, v176, v187\n\t" \
    "ds_write_b128 v2, v[200:203] offset:4160\n\tv_alignbit_b32 v196, v178, v178, 16\n\tv_alignbit_b32 v197, v177, v177, 16\n\tv_alignbit_b32 v198, v176, v176, 16\n\tv_alignbit_b32 v199, v187, v187, 16\n\tds_write_b128 v2, v[196:199] offset:8320\n\tv_bfi_b32 v200, s46, v178, v177\n\tv_bfi_b32 v201, s46, v177, v176\n\t" \
    "v_bfi_b32 v202, s46, v176, v187\n\tv_bfi_b32 v203, s46, v187, v186\n\tds_write_b128 v2, v[200:203] offset:12480\n\tv_lshl_or_b32 v188, v188, 16, v188\n\tv_and_b32 v228, v188, v232\n\tv_and_b32 v229, v188, v233\n\tv_and_b32 v230, v188, v234\n\tv_and_b32 v231, v188, v235\n\t" \
    "ds_read_b64 a[0:1], v0 offset:4032\n\tds_read_b64 a[2:3], v0 offset:4040\n\tds_read_b64 a[4:5], v0 offset:3520\n\tds_read_b64 a[6:7], v0 offset:3528\n\tds_read_b64 a[8:9], v0 offset:3008\n\tds_read_b64 a[10:11], v0 offset:3016\n\tds_read_b64 a[12:13], v0 offset:2496\n\tds_read_b64 a[14:15], v0 offset:2504\n\t" \
    "ds_read_b64 a[16:17], v0 offset:1984\n\tds_read_b64 a[18:19], v0 offset:1992\n\tds_read_b64 a[20:21], v0 offset:1472\n\tds_read_b64 a[22:23], v0 offset:1480\n\tds_read_b64 a[24:25], v0 offset:960\n\tds_read_b64 a[26:27], v0 offset:968\n\tds_read_b64 a[28:29], v0 offset:448\n\tds_read_b64 a[30:31], v0 offset:456\n\t" \
    "s_barrier\n\tv_mov_b32 v196, 0\n\tv_mov_b32 v197, 0\n\tv_mov_b32 v198, 0\n\tv_mov_b32 v199, 0\n\tds_write_b128 v12, v[196:199]\n\tds_write_b128 v12, v[196:199] offset:9248\n\ts_sleep 20\n\t" \
    "s_mov_b32 s71, 1048576\n\t122:\n\tbuffer_load_dwordx2 v[250:251], v239, s[8:11], s47 offen sc1\n\tbuffer_load_dwordx2 v[252:253], v239, s[8:11], s50 offen sc1\n\ts_waitcnt vmcnt(0)\n\tv_cmp_ne_u64 vcc, s[36:37], v[250:251]\n\tv_cmp_ne_u64 s[74:75], s[36:37], v[252:253]\n\ts_or_b64 s[74:75], s[74:75], vcc\n\t" \
    "s_cbranch_scc0 123f\n\ts_sleep 16\n\ts_sub_u32 s71, s71, 1\n\ts_cbranch_scc0 122b\n\t123:\n\tbuffer_load_dwordx4 v[68:71], v10, s[8:11], s62 offen sc1\n\ts_add_u32 s62, s62, 36864\n\tbuffer_load_dwordx4 v[72:75], v10, s[8:11], s62 offen sc1\n\t" \
    "s_add_u32 s62, s62, 36864\n\tbuffer_load_dwordx4 v[76:79], v10, s[8:11], s62 offen sc1\n\ts_add_u32 s62, s62, 36864\n\tbuffer_load_dwordx4 v[80:83], v10, s[8:11], s62 offen sc1\n\ts_add_u32 s62, s62, 36864\n\tbuffer_load_dwordx4 v[84:87], v10, s[8:11], s62 offen sc1\n\ts_add_u32 s62, s62, 36864\n\tbuffer_load_dwordx4 v[88:91], v10, s[8:11], s62 offen sc1\n\t" \
    "s_add_u32 s62, s62, 36864\n\tbuffer_load_dwordx4 v[92:95], v10, s[8:11], s62 offen sc1\n\ts_add_u32 s62, s62, 36864\n\tbuffer_load_dwordx4 v[96:99], v10, s[8:11], s62 offen sc1\n\ts_add_u32 s62, s62, 36864\n\tbuffer_load_dwordx4 v[100:103], v10, s[8:11], s62 offen sc1\n\ts_add_u32 s62, s62, 36864\n\tbuffer_load_dwordx4 v[104:107], v10, s[8:11], s62 offen sc1\n\t" \
    "s_add_u32 s62, s62, 36864\n\tbuffer_load_dwordx4 v[108:111], v10, s[8:11], s62 offen sc1\n\ts_add_u32 s62, s62, 36864\n\tbuffer_load_dwordx4 v[112:115], v10, s[8:11], s62 offen sc1\n\ts_add_u32 s62, s62, 36864\n\tbuffer_load_dwordx4 v[116:119], v10, s[8:11], s62 offen sc1\n\ts_add_u32 s62, s62, 36864\n\tbuffer_load_dwordx4 v[120:123], v10, s[8:11], s62 offen sc1\n\t" \
    "s_add_u32 s62, s62, 36864\n\tbuffer_load_dwordx4 v[124:127], v10, s[8:11], s62 offen sc1\n\ts_add_u32 s62, s62, 36864\n\tbuffer_load_dwordx4 v[128:131], v10, s[8:11], s62 offen sc1\n\ts_add_u32 s62, s62, 36864\n\ts_waitcnt vmcnt(15)\n\tds_write_b128 v11, v[68:71] offset:512\n\ts_waitcnt vmcnt(14)\n\t" \
    "ds_write_b128 v11, v[72:75] offset:576\n\ts_waitcnt vmcnt(13)\n\tds_write_b128 v11, v[76:79] offset:640\n\ts_waitcnt vmcnt(12)\n\tds_write_b128 v11, v[80:83] offset:704\n\ts_waitcnt vmcnt(11)\n\tds_write_b128 v11, v[84:87] offset:768\n\ts_waitcnt vmcnt(10)\n\t" \
    "ds_write_b128 v11, v[88:91] offset:832\n\ts_waitcnt vmcnt(9)\n\tds_write_b128 v11, v[92:95] offset:896\n\ts_waitcnt vmcnt(8)\n\tds_write_b128 v11, v[96:99] offset:960\n\ts_waitcnt vmcnt(7)\n\tds_write_b128 v11, v[100:103] offset:1024\n\ts_waitcnt vmcnt(6)\n\t" \
    "ds_write_b128 v11, v[104:107] offset:1088\n\ts_waitcnt vmcnt(5)\n\tds_write_b128 v11, v[108:111] offset:1152\n\ts_waitcnt vmcnt(4)\n\tds_write_b128 v11, v[112:115] offset:1216\n\ts_waitcnt vmcnt(3)\n\tds_write_b128 v11, v[116:119] offset:1280\n\ts_waitcnt vmcnt(2)\n\t" \
    "ds_write_b128 v11, v[120:123] offset:1344\n\ts_waitcnt vmcnt(1)\n\tds_write_b128 v11, v[124:127] offset:1408\n\ts_waitcnt vmcnt(0)\n\tds_write_b128 v11, v[128:131] offset:1472\n\ts_waitcnt lgkmcnt(0)\n\ts_barrier\n\tds_read_b128 v[68:71], v1 offset:448\n\t" \
    "ds_read_b128 v[72:75], v1 offset:960\n\tds_read_b128 v[76:79], v1 offset:1472\n\tds_read_b128 v[80:83], v1 offset:1984\n\tds_read_b128 v[84:87], v1 offset:2496\n\tds_read_b128 v[88:91], v1 offset:3008\n\tds_read_b128 v[92:95], v1 offset:3520\n\tds_read_b128 v[96:99], v1 offset:4032\n\ts_add_u32 s68, s44, s38\n\t" \
    "s_add_u32 s68, s68, 4\n\ts_lshl_b32 s66, s68, 1\n\ts_lshl_b32 s68, s68, 12\n\tbuffer_load_dwordx4 v[42:45], v8, s[76:79], s68 offen offset:0\n\tbuffer_load_dwordx4 v[46:49], v8, s[76:79], s68 offen offset:1024\n\tbuffer_load_dwordx4 v[50:53], v8, s[76:79], s68 offen offset:2048\n\tbuffer_load_dwordx4 v[54:57], v8, s[76:79], s68 offen offset:3072\n\tbuffer_load_dwordx2 v[58:59], v13, s[76:79], s68 offen\n\t" \
    "buffer_load_dwordx2 v[60:61], v8, s[76:79], s68 offen offset:1016\n\tbuffer_load_dwordx2 v[62:63], v8, s[76:79], s68 offen offset:2040\n\tbuffer_load_dwordx2 v[64:65], v8, s[76:79], s68 offen offset:3064\n\tbuffer_load_ushort v66, off, s[80:83], s66\n\tbuffer_load_dword v15, v6, s[12:15], s41 offen\n\tbuffer_load_dword v15, v7, s[16:19], s42 offen\n\ts_add_u32 s43, s43, 1\n\tv_readlane_b32 s41, v236, s43\n\t" \
    "v_readlane_b32 s42, v237, s43\n\ts_nop 4\n\ts_waitcnt lgkmcnt(0)\n\tv_mfma_f32_16x16x32_bf16 v[132:135], v[228:231], v[68:71], 0\n\tds_read_b64 a[32:33], v0 offset:3968\n\tds_read_b64 a[34:35], v0 offset:3976\n\tv_mfma_f32_16x16x32_bf16 v[136:139], v[228:231], v[72:75], 0\n\tds_read_b64 a[36:37], v0 offset:3456\n\t" \
    "ds_read_b64 a[38:39], v0 offset:3464\n\tv_mfma_f32_16x16x32_bf16 v[140:143], v[228:231], v[76:79], 0\n\tds_read_b64 a[40:41], v0 offset:2944\n\tds_read_b64 a[42:43], v0 offset:2952\n\tv_mfma_f32_16x16x32_bf16 v[144:147], v[228:231], v[80:83], 0\n\tds_read_b64 a[44:45], v0 offset:2432\n\tds_read_b64 a[46:47], v0 offset:2440\n\tv_mfma_f32_16x16x32_bf16 v[148:151], v[228:231], v[84:87], 0\n\t" \
    "ds_read_b64 a[48:49], v0 offset:1920\n\tds_read_b64 a[50:51], v0 offset:1928\n\tv_mfma_f32_16x16x32_bf16 v[152:155], v[228:231], v[88:91], 0\n\tds_read_b64 a[52:53], v0 offset:1408\n\tv_mfma_f32_16x16x32_bf16 v[156:159], v[228:231], v[92:95], 0\n\tds_read_b64 a[54:55], v0 offset:1416\n\tv_mfma_f32_16x16x32_bf16 v[160:163], v[228:231], v[96:99], 0\n\tds_read_b64 a[56:57], v0 offset:896\n\t" \
    "v_mfma_f32_16x16x32_bf16 v[132:135], a[0:3], v[68:71], v[132:135]\n\tds_read_b64 a[58:59], v0 offset:904\n\tv_mfma_f32_16x16x32_bf16 v[136:139], a[0:3], v[72:75], v[136:139]\n\tds_read_b64 a[60:61], v0 offset:384\n\tv_mfma_f32_16x16x32_bf16 v[140:143], a[0:3], v[76:79], v[140:143]\n\tds_read_b64 a[62:63], v0 offset:392\n\tv_mfma_f32_16x16x32_bf16 v[144:147], a[0:3], v[80:83], v[144:147]\n\tds_read_b128 v[100:103], v1 offset:384\n\t" \
    "v_mfma_f32_16x16x32_bf16 v[148:151], a[0:3], v[84:87], v[148:151]\n\tds_read_b128 v[104:107], v1 offset:896\n\tv_mfma_f32_16x16x32_bf16 v[152:155], a[0:3], v[88:91], v[152:155]\n\tds_read_b128 v[108:111], v1 offset:1408\n\tv_mfma_f32_16x16x32_bf16 v[156:159], a[0:3], v[92:95], v[156:159]\n\tds_read_b128 v[112:115], v1 offset:1920\n\tv_mfma_f32_16x16x32_bf16 v[160:163], a[0:3], v[96:99], v[160:163]\n\tds_read_b128 v[116:119], v1 offset:2432\n\t" \
    "v_mfma_f32_16x16x32_bf16 v[136:139], a[4:7], v[68:71], v[136:139]\n\tds_read_b128 v[120:123], v1 offset:2944\n\tv_mfma_f32_16x16x32_bf16 v[140:143], a[4:7], v[72:75], v[140:143]\n\tbuffer_load_dword v15, v6, s[12:15], s41 offen\n\tds_read_b128 v[124:127], v1 offset:3456\n\tv_mfma_f32_16x16x32_bf16 v[144:147], a[4:7], v[76:79], v[144:147]\n\tbuffer_load_dword v15, v7, s[16:19], s42 offen\n\tds_read_b128 v[128:131], v1 offset:3968\n\t" \
    "v_mfma_f32_16x16x32_bf16 v[148:151], a[4:7], v[80:83], v[148:151]\n\ts_add_u32 s43, s43, 1\n\tds_read_b64 a[64:65], v0 offset:3904\n\tv_mfma_f32_16x16x32_bf16 v[152:155], a[4:7], v[84:87], v[152:155]\n\tv_readlane_b32 s41, v236, s43\n\tv_readlane_b32 s42, v237, s43\n\ts_nop 4\n\tds_read_b64 a[66:67], v0 offset:3912\n\t" \
    "v_mfma_f32_16x16x32_bf16 v[156:159], a[4:7], v[88:91], v[156:159]\n\tds_read_b64 a[68:69], v0 offset:3392\n\tv_mfma_f32_16x16x32_bf16 v[160:163], a[4:7], v[92:95], v[160:163]\n\tds_read_b64 a[70:71], v0 offset:3400\n\tv_mfma_f32_16x16x32_bf16 v[140:143], a[8:11], v[68:71], v[140:143]\n\tds_read_b64 a[72:73], v0 offset:2880\n\tv_mfma_f32_16x16x32_bf16 v[144:147], a[8:11], v[72:75], v[144:147]\n\tds_read_b64 a[74:75], v0 offset:2888\n\t" \
    "v_mfma_f32_16x16x32_bf16 v[148:151], a[8:11], v[76:79], v[148:151]\n\tds_read_b64 a[76:77], v0 offset:2368\n\tv_mfma_f32_16x16x32_bf16 v[152:155], a[8:11], v[80:83], v[152:155]\n\tds_read_b64 a[78:79], v0 offset:2376\n\tv_mfma_f32_16x16x32_bf16 v[156:159], a[8:11], v[84:87], v[156:159]\n\tds_read_b64 a[80:81], v0 offset:1856\n\tv_mfma_f32_16x16x32_bf16 v[160:163], a[8:11], v[88:91], v[160:163]\n\tds_read_b64 a[82:83], v0 offset:1864\n\t" \
    "v_mfma_f32_16x16x32_bf16 v[144:147], a[12:15], v[68:71], v[144:147]\n\tds_read_b64 a[84:85], v0 offset:1344\n\tv_mfma_f32_16x16x32_bf16 v[148:151], a[12:15], v[72:75], v[148:151]\n\tds_read_b64 a[86:87], v0 offset:1352\n\tv_mfma_f32_16x16x32_bf16 v[152:155], a[12:15], v[76:79], v[152:155]\n\tds_read_b64 a[88:89], v0 offset:832\n\tv_mfma_f32_16x16x32_bf16 v[156:159], a[12:15], v[80:83], v[156:159]\n\tds_read_b64 a[90:91], v0 offset:840\n\t" \
    "v_mfma_f32_16x16x32_bf16 v[160:163], a[12:15], v[84:87], v[160:163]\n\tds_read_b64 a[92:93], v0 offset:320\n\tv_mfma_f32_16x16x32_bf16 v[148:151], a[16:19], v[68:71], v[148:151]\n\tds_read_b64 a[94:95], v0 offset:328\n\tv_mfma_f32_16x16x32_bf16 v[152:155], a[16:19], v[72:75], v[152:155]\n\tds_read_b64 a[96:97], v0 offset:3840\n\tv_mfma_f32_16x16x32_bf16 v[156:159], a[16:19], v[76:79], v[156:159]\n\tds_read_b64 a[98:99], v0 offset:3848\n\t" \
    "v_mfma_f32_16x16x32_bf16 v[160:163], a[16:19], v[80:83], v[160:163]\n\tds_read_b64 a[100:101], v0 offset:3328\n\tv_mfma_f32_16x16x32_bf16 v[152:155], a[20:23], v[68:71], v[152:155]\n\tds_read_b64 a[102:103], v0 offset:3336\n\tv_mfma_f32_16x16x32_bf16 v[156:159], a[20:23], v[72:75], v[156:159]\n\tv_mfma_f32_16x16x32_bf16 v[160:163], a[20:23], v[76:79], v[160:163]\n\tv_mfma_f32_16x16x32_bf16 v[156:159], a[24:27], v[68:71], v[156:159]\n\tv_mfma_f32_16x16x32_bf16 v[160:163], a[24:27], v[72:75], v[160:163]\n\t" \
    "v_mfma_f32_16x16x32_bf16 v[160:163], a[28:31], v[68:71], v[160:163]\n\ts_waitcnt lgkmcnt(15)\n\tv_mfma_f32_16x16x32_bf16 v[132:135], a[32:35], v[100:103], v[132:135]\n\tds_read_b64 a[104:105], v0 offset:2816\n\tds_read_b64 a[106:107], v0 offset:2824\n\tv_mfma_f32_16x16x32_bf16 v[136:139], a[32:35], v[104:107], v[136:139]\n\tds_read_b64 a[108:109], v0 offset:2304\n\tds_read_b64 a[110:111], v0 offset:2312\n\t" \
    "v_mfma_f32_16x16x32_bf16 v[140:143], a[32:35], v[108:111], v[140:143]\n\tds_read_b64 a[112:113], v0 offset:1792\n\tds_read_b64 a[114:115], v0 offset:1800\n\tv_mfma_f32_16x16x32_bf16 v[144:147], a[32:35], v[112:115], v[144:147]\n\tds_read_b64 a[116:117], v0 offset:1280\n\tds_read_b64 a[118:119], v0 offset:1288\n\tv_mfma_f32_16x16x32_bf16 v[148:151], a[32:35], v[116:119], v[148:151]\n\tds_read_b64 a[120:121], v0 offset:768\n\t" \
    "ds_read_b64 a[122:123], v0 offset:776\n\tv_mfma_f32_16x16x32_bf16 v[152:155], a[32:35], v[120:123], v[152:155]\n\tds_read_b64 a[124:125], v0 offset:256\n\tv_mfma_f32_16x16x32_bf16 v[156:159], a[32:35], v[124:127], v[156:159]\n\tds_read_b64 a[126:127], v0 offset:264\n\tv_mfma_f32_16x16x32_bf16 v[160:163], a[32:35], v[128:131], v[160:163]\n\tds_read_b64 a[128:129], v0 offset:3776\n\tv_mfma_f32_16x16x32_bf16 v[136:139], a[36:39], v[100:103], v[136:139]\n\t" \
    "ds_read_b64 a[130:131], v0 offset:3784\n\tv_mfma_f32_16x16x32_bf16 v[140:143], a[36:39], v[104:107], v[140:143]\n\tds_read_b64 a[132:133], v0 offset:3264\n\tv_mfma_f32_16x16x32_bf16 v[144:147], a[36:39], v[108:111], v[144:147]\n\tds_read_b64 a[134:135], v0 offset:3272\n\tv_mfma_f32_16x16x32_bf16 v[148:151], a[36:39], v[112:115], v[148:151]\n\tds_read_b128 v[68:71], v1 offset:320\n\tv_mfma_f32_16x16x32_bf16 v[152:155], a[36:39], v[116:119], v[152:155]\n\t" \
    "ds_read_b128 v[72:75], v1 offset:832\n\tv_mfma_f32_16x16x32_bf16 v[156:159], a[36:39], v[120:123], v[156:159]\n\tds_read_b128 v[76:79], v1 offset:1344\n\tv_mfma_f32_16x16x32_bf16 v[160:163], a[36:39], v[124:127], v[160:163]\n\tds_read_b128 v[80:83], v1 offset:1856\n\tv_mfma_f32_16x16x32_bf16 v[140:143], a[40:43], v[100:103], v[140:143]\n\tds_read_b128 v[84:87], v1 offset:2368\n\tv_mfma_f32_16x16x32_bf16 v[144:147], a[40:43], v[104:107], v[144:147]\n\t" \
    "ds_read_b128 v[88:91], v1 offset:2880\n\tv_mfma_f32_16x16x32_bf16 v[148:151], a[40:43], v[108:111], v[148:151]\n\tds_read_b128 v[92:95], v1 offset:3392\n\tv_mfma_f32_16x16x32_bf16 v[152:155], a[40:43], v[112:115], v[152:155]\n\tds_read_b128 v[96:99], v1 offset:3904\n\tv_mfma_f32_16x16x32_bf16 v[156:159], a[40:43], v[116:119], v[156:159]\n\tds_read_b64 a[136:137], v0 offset:2752\n\tv_mfma_f32_16x16x32_bf16 v[160:163], a[40:43], v[120:123], v[160:163]\n\t" \
    "ds_read_b64 a[138:139], v0 offset:2760\n\tv_mfma_f32_16x16x32_bf16 v[144:147], a[44:47], v[100:103], v[144:147]\n\tds_read_b64 a[140:141], v0 offset:2240\n\tv_mfma_f32_16x16x32_bf16 v[148:151], a[44:47], v[104:107], v[148:151]\n\tds_read_b64 a[142:143], v0 offset:2248\n\tv_mfma_f32_16x16x32_bf16 v[152:155], a[44:47], v[108:111], v[152:155]\n\tds_read_b64 a[144:145], v0 offset:1728\n\tv_mfma_f32_16x16x32_bf16 v[156:159], a[44:47], v[112:115], v[156:159]\n\t" \
    "ds_read_b64 a[146:147], v0 offset:1736\n\tv_mfma_f32_16x16x32_bf16 v[160:163], a[44:47], v[116:119], v[160:163]\n\tds_read_b64 a[148:149], v0 offset:1216\n\tv_mfma_f32_16x16x32_bf16 v[148:151], a[48:51], v[100:103], v[148:151]\n\tds_read_b64 a[150:151], v0 offset:1224\n\tv_mfma_f32_16x16x32_bf16 v[152:155], a[48:51], v[104:107], v[152:155]\n\tds_read_b64 a[152:153], v0 offset:704\n\tv_mfma_f32_16x16x32_bf16 v[156:159], a[48:51], v[108:111], v[156:159]\n\t" \
    "ds_read_b64 a[154:155], v0 offset:712\n\tv_mfma_f32_16x16x32_bf16 v[160:163], a[48:51], v[112:115], v[160:163]\n\tds_read_b64 a[156:157], v0 offset:192\n\tv_mfma_f32_16x16x32_bf16 v[152:155], a[52:55], v[100:103], v[152:155]\n\tds_read_b64 a[158:159], v0 offset:200\n\tv_mfma_f32_16x16x32_bf16 v[156:159], a[52:55], v[104:107], v[156:159]\n\tv_mfma_f32_16x16x32_bf16 v[160:163], a[52:55], v[108:111], v[160:163]\n\tbuffer_load_dword v15, v6, s[12:15], s41 offen\n\t" \
    "v_mfma_f32_16x16x32_bf16 v[156:159], a[56:59], v[100:103], v[156:159]\n\tbuffer_load_dword v15, v7, s[16:19], s42 offen\n\tv_mfma_f32_16x16x32_bf16 v[160:163], a[56:59], v[104:107], v[160:163]\n\ts_add_u32 s43, s43, 1\n\tv_mfma_f32_16x16x32_bf16 v[160:163], a[60:63], v[100:103], v[160:163]\n\tv_readlane_b32 s41, v236, s43\n\tv_readlane_b32 s42, v237, s43\n\ts_nop 4\n\t" \
    "s_waitcnt lgkmcnt(12)\n\tv_mfma_f32_16x16x32_bf16 v[132:135], a[64:67], v[68:71], v[132:135]\n\tds_read_b64 a[160:161], v0 offset:3712\n\tds_read_b64 a[162:163], v0 offset:3720\n\tv_mfma_f32_16x16x32_bf16 v[136:139], a[64:67], v[72:75], v[136:139]\n\tds_read_b64 a[164:165], v0 offset:3200\n\tds_read_b64 a[166:167], v0 offset:3208\n\tv_mfma_f32_16x16x32_bf16 v[140:143], a[64:67], v[76:79], v[140:143]\n\t" \
    "ds_read_b64 a[168:169], v0 offset:2688\n\tds_read_b64 a[170:171], v0 offset:2696\n\tv_mfma_f32_16x16x32_bf16 v[144:147], a[64:67], v[80:83], v[144:147]\n\tds_read_b64 a[172:173], v0 offset:2176\n\tds_read_b64 a[174:175], v0 offset:2184\n\tv_mfma_f32_16x16x32_bf16 v[148:151], a[64:67], v[84:87], v[148:151]\n\tds_read_b64 a[176:177], v0 offset:1664\n\tds_read_b64 a[178:179], v0 offset:1672\n\t" \
    "v_mfma_f32_16x16x32_bf16 v[152:155], a[64:67], v[88:91], v[152:155]\n\tds_read_b64 a[180:181], v0 offset:1152\n\tv_mfma_f32_16x16x32_bf16 v[156:159], a[64:67], v[92:95], v[156:159]\n\tds_read_b64 a[182:183], v0 offset:1160\n\tv_mfma_f32_16x16x32_bf16 v[160:163], a[64:67], v[96:99], v[160:163]\n\tds_read_b64 a[184:185], v0 offset:640\n\tv_mfma_f32_16x16x32_bf16 v[136:139], a[68:71], v[68:71], v[136:139]\n\tds_read_b64 a[186:187], v0 offset:648\n\t" \
    "v_mfma_f32_16x16x32_bf16 v[140:143], a[68:71], v[72:75], v[140:143]\n\tds_read_b64 a[188:189], v0 offset:128\n\tv_mfma_f32_16x16x32_bf16 v[144:147], a[68:71], v[76:79], v[144:147]\n\tds_read_b64 a[190:191], v0 offset:136\n\tv_mfma_f32_16x16x32_bf16 v[148:151], a[68:71], v[80:83], v[148:151]\n\tds_read_b128 v[100:103], v1 offset:256\n\tv_mfma_f32_16x16x32_bf16 v[152:155], a[68:71], v[84:87], v[152:155]\n\tds_read_b128 v[104:107], v1 offset:768\n\t" \
    "v_mfma_f32_16x16x32_bf16 v[156:159], a[68:71], v[88:91], v[156:159]\n\tds_read_b128 v[108:111], v1 offset:1280\n\tv_mfma_f32_16x16x32_bf16 v[160:163], a[68:71], v[92:95], v[160:163]\n\tds_read_b128 v[112:115], v1 offset:1792\n\tv_mfma_f32_16x16x32_bf16 v[140:143], a[72:75], v[68:71], v[140:143]\n\tds_read_b128 v[116:119], v1 offset:2304\n\tv_mfma_f32_16x16x32_bf16 v[144:147], a[72:75], v[72:75], v[144:147]\n\tds_read_b128 v[120:123], v1 offset:2816\n\t" \
    "v_mfma_f32_16x16x32_bf16 v[148:151], a[72:75], v[76:79], v[148:151]\n\tds_read_b128 v[124:127], v1 offset:3328\n\tv_mfma_f32_16x16x32_bf16 v[152:155], a[72:75], v[80:83], v[152:155]\n\tds_read_b128 v[128:131], v1 offset:3840\n\tv_mfma_f32_16x16x32_bf16 v[156:159], a[72:75], v[84:87], v[156:159]\n\tds_read_b64 a[192:193], v0 offset:3648\n\tv_mfma_f32_16x16x32_bf16 v[160:163], a[72:75], v[88:91], v[160:163]\n\tds_read_b64 a[194:195], v0 offset:3656\n\t" \
    "v_mfma_f32_16x16x32_bf16 v[144:147], a[76:79], v[68:71], v[144:147]\n\tds_read_b64 a[196:197], v0 offset:3136\n\tv_mfma_f32_16x16x32_bf16 v[148:151], a[76:79], v[72:75], v[148:151]\n\tds_read_b64 a[198:199], v0 offset:3144\n\tv_mfma_f32_16x16x32_bf16 v[152:155], a[76:79], v[76:79], v[152:155]\n\tds_read_b64 a[200:201], v0 offset:2624\n\tv_mfma_f32_16x16x32_bf16 v[156:159], a[76:79], v[80:83], v[156:159]\n\tds_read_b64 a[202:203], v0 offset:2632\n\t" \
    "v_mfma_f32_16x16x32_bf16 v[160:163], a[76:79], v[84:87], v[160:163]\n\tds_read_b64 a[204:205], v0 offset:2112\n\tv_mfma_f32_16x16x32_bf16 v[148:151], a[80:83], v[68:71], v[148:151]\n\tds_read_b64 a[206:207], v0 offset:2120\n\tv_mfma_f32_16x16x32_bf16 v[152:155], a[80:83], v[72:75], v[152:155]\n\tds_read_b64 a[208:209], v0 offset:1600\n\tv_mfma_f32_16x16x32_bf16 v[156:159], a[80:83], v[76:79], v[156:159]\n\tds_read_b64 a[210:211], v0 offset:1608\n\t" \
    "v_mfma_f32_16x16x32_bf16 v[160:163], a[80:83], v[80:83], v[160:163]\n\tds_read_b64 a[212:213], v0 offset:1088\n\tv_mfma_f32_16x16x32_bf16 v[152:155], a[84:87], v[68:71], v[152:155]\n\tds_read_b64 a[214:215], v0 offset:1096\n\tv_mfma_f32_16x16x32_bf16 v[156:159], a[84:87], v[72:75], v[156:159]\n\tv_mfma_f32_16x16x32_bf16 v[160:163], a[84:87], v[76:79], v[160:163]\n\tv_mfma_f32_16x16x32_bf16 v[156:159], a[88:91], v[68:71], v[156:159]\n\tv_mfma_f32_16x16x32_bf16 v[160:163], a[88:91], v[72:75], v[160:163]\n\t" \
    "v_mfma_f32_16x16x32_bf16 v[160:163], a[92:95], v[68:71], v[160:163]\n\ts_waitcnt lgkmcnt(12)\n\tv_mfma_f32_16x16x32_bf16 v[132:135], a[96:99], v[100:103], v[132:135]\n\tds_read_b64 a[216:217], v0 offset:576\n\tv_mfma_f32_16x16x32_bf16 v[136:139], a[96:99], v[104:107], v[136:139]\n\tds_read_b64 a[218:219], v0 offset:584\n\tv_mfma_f32_16x16x32_bf16 v[140:143], a[96:99], v[108:111], v[140:143]\n\tds_read_b64 a[220:221], v0 offset:64\n\t" \
    "v_mfma_f32_16x16x32_bf16 v[144:147], a[96:99], v[112:115], v[144:147]\n\tds_read_b64 a[222:223], v0 offset:72\n\tv_mfma_f32_16x16x32_bf16 v[148:151], a[96:99], v[116:119], v[148:151]\n\tds_read_b64 a[224:225], v0 offset:3584\n\tv_mfma_f32_16x16x32_bf16 v[152:155], a[96:99], v[120:123], v[152:155]\n\tds_read_b64 a[226:227], v0 offset:3592\n\tv_mfma_f32_16x16x32_bf16 v[156:159], a[96:99], v[124:127], v[156:159]\n\tds_read_b64 a[228:229], v0 offset:3072\n\t" \
    "v_mfma_f32_16x16x32_bf16 v[160:163], a[96:99], v[128:131], v[160:163]\n\tds_read_b64 a[230:231], v0 offset:3080\n\tv_mfma_f32_16x16x32_bf16 v[136:139], a[100:103], v[100:103], v[136:139]\n\tds_read_b64 a[232:233], v0 offset:2560\n\tv_mfma_f32_16x16x32_bf16 v[140:143], a[100:103], v[104:107], v[140:143]\n\tds_read_b64 a[234:235], v0 offset:2568\n\tv_mfma_f32_16x16x32_bf16 v[144:147], a[100:103], v[108:111], v[144:147]\n\tds_read_b64 a[236:237], v0 offset:2048\n\t" \
    "v_mfma_f32_16x16x32_bf16 v[148:151], a[100:103], v[112:115], v[148:151]\n\tds_read_b64 a[238:239], v0 offset:2056\n\tv_mfma_f32_16x16x32_bf16 v[152:155], a[100:103], v[116:119], v[152:155]\n\tds_read_b64 a[240:241], v0 offset:1536\n\tv_mfma_f32_16x16x32_bf16 v[156:159], a[100:103], v[120:123], v[156:159]\n\tds_read_b64 a[242:243], v0 offset:1544\n\tv_mfma_f32_16x16x32_bf16 v[160:163], a[100:103], v[124:127], v[160:163]\n\tds_read_b64 a[244:245], v0 offset:1024\n\t" \
    "v_mfma_f32_16x16x32_bf16 v[140:143], a[104:107], v[100:103], v[140:143]\n\tds_read_b64 a[246:247], v0 offset:1032\n\tv_mfma_f32_16x16x32_bf16 v[144:147], a[104:107], v[104:107], v[144:147]\n\tds_read_b128 v[68:71], v1 offset:192\n\tv_mfma_f32_16x16x32_bf16 v[148:151], a[104:107], v[108:111], v[148:151]\n\tds_read_b128 v[72:75], v1 offset:704\n\tv_mfma_f32_16x16x32_bf16 v[152:155], a[104:107], v[112:115], v[152:155]\n\tds_read_b128 v[76:79], v1 offset:1216\n\t" \
    "v_mfma_f32_16x16x32_bf16 v[156:159], a[104:107], v[116:119], v[156:159]\n\tds_read_b128 v[80:83], v1 offset:1728\n\tv_mfma_f32_16x16x32_bf16 v[160:163], a[104:107], v[120:123], v[160:163]\n\tbuffer_load_dword v15, v6, s[12:15], s41 offen\n\tds_read_b128 v[84:87], v1 offset:2240\n\tv_mfma_f32_16x16x32_bf16 v[144:147], a[108:111], v[100:103], v[144:147]\n\tbuffer_load_dword v15, v7, s[16:19], s42 offen\n\tds_read_b128 v[88:91], v1 offset:2752\n\t" \
    "v_mfma_f32_16x16x32_bf16 v[148:151], a[108:111], v[104:107], v[148:151]\n\ts_add_u32 s43, s43, 1\n\tds_read_b128 v[92:95], v1 offset:3264\n\tv_mfma_f32_16x16x32_bf16 v[152:155], a[108:111], v[108:111], v[152:155]\n\tv_readlane_b32 s41, v236, s43\n\tv_readlane_b32 s42, v237, s43\n\ts_nop 4\n\tds_read_b128 v[96:99], v1 offset:3776\n\t" \
    "v_mfma_f32_16x16x32_bf16 v[156:159], a[108:111], v[112:115], v[156:159]\n\tds_read_b64 a[248:249], v0 offset:512\n\tv_mfma_f32_16x16x32_bf16 v[160:163], a[108:111], v[116:119], v[160:163]\n\tds_read_b64 a[250:251], v0 offset:520\n\tv_mfma_f32_16x16x32_bf16 v[148:151], a[112:115], v[100:103], v[148:151]\n\tds_read_b64 a[252:253], v0 offset:0\n\tv_mfma_f32_16x16x32_bf16 v[152:155], a[112:115], v[104:107], v[152:155]\n\tds_read_b64 a[254:255], v0 offset:8\n\t" \
    "v_mfma_f32_16x16x32_bf16 v[156:159], a[112:115], v[108:111], v[156:159]\n\tv_mfma_f32_16x16x32_bf16 v[160:163], a[112:115], v[112:115], v[160:163]\n\tv_mfma_f32_16x16x32_bf16 v[152:155], a[116:119], v[100:103], v[152:155]\n\tv_mfma_f32_16x16x32_bf16 v[156:159], a[116:119], v[104:107], v[156:159]\n\tv_mfma_f32_16x16x32_bf16 v[160:163], a[116:119], v[108:111], v[160:163]\n\tv_mfma_f32_16x16x32_bf16 v[156:159], a[120:123], v[100:103], v[156:159]\n\tv_mfma_f32_16x16x32_bf16 v[160:163], a[120:123], v[104:107], v[160:163]\n\tv_mfma_f32_16x16x32_bf16 v[160:163], a[124:127], v[100:103], v[160:163]\n\t" \
    "s_waitcnt lgkmcnt(4)\n\tv_mfma_f32_16x16x32_bf16 v[132:135], a[128:131], v[68:71], v[132:135]\n\tds_read_b128 v[100:103], v1 offset:128\n\ts_waitcnt vmcnt(8)\n\tv_alignbit_b32 v196, v45, v45, 16\n\tv_mfma_f32_16x16x32_bf16 v[136:139], a[128:131], v[72:75], v[136:139]\n\tds_read_b128 v[104:107], v1 offset:640\n\tv_alignbit_b32 v197, v44, v44, 16\n\t" \
    "v_mfma_f32_16x16x32_bf16 v[140:143], a[128:131], v[76:79], v[140:143]\n\tds_read_b128 v[108:111], v1 offset:1152\n\tv_alignbit_b32 v198, v43, v43, 16\n\tv_mfma_f32_16x16x32_bf16 v[144:147], a[128:131], v[80:83], v[144:147]\n\tds_read_b128 v[112:115], v1 offset:1664\n\tv_alignbit_b32 v199, v42, v42, 16\n\tv_mfma_f32_16x16x32_bf16 v[148:151], a[128:131], v[84:87], v[148:151]\n\tds_write_b128 v2, v[196:199] offset:3072\n\t" \
    "v_mfma_f32_16x16x32_bf16 v[152:155], a[128:131], v[88:91], v[152:155]\n\tds_read_b128 v[116:119], v1 offset:2176\n\tv_bfi_b32 v200, s46, v45, v44\n\tv_mfma_f32_16x16x32_bf16 v[156:159], a[128:131], v[92:95], v[156:159]\n\tds_read_b128 v[120:123], v1 offset:2688\n\tv_bfi_b32 v201, s46, v44, v43\n\tv_mfma_f32_16x16x32_bf16 v[160:163], a[128:131], v[96:99], v[160:163]\n\tds_read_b128 v[124:127], v1 offset:3200\n\t" \
    "v_bfi_b32 v202, s46, v43, v42\n\tv_mfma_f32_16x16x32_bf16 v[136:139], a[132:135], v[68:71], v[136:139]\n\tds_read_b128 v[128:131], v1 offset:3712\n\tv_bfi_b32 v203, s46, v42, v59\n\tv_mfma_f32_16x16x32_bf16 v[140:143], a[132:135], v[72:75], v[140:143]\n\tds_write_b128 v2, v[200:203] offset:7232\n\tv_mfma_f32_16x16x32_bf16 v[144:147], a[132:135], v[76:79], v[144:147]\n\tv_alignbit_b32 v196, v44, v44, 16\n\t" \
    "v_alignbit_b32 v197, v43, v43, 16\n\tv_mfma_f32_16x16x32_bf16 v[148:151], a[132:135], v[80:83], v[148:151]\n\tv_alignbit_b32 v198, v42, v42, 16\n\tv_alignbit_b32 v199, v59, v59, 16\n\tv_mfma_f32_16x16x32_bf16 v[152:155], a[132:135], v[84:87], v[152:155]\n\tds_write_b128 v2, v[196:199] offset:11392\n\tv_mfma_f32_16x16x32_bf16 v[156:159], a[132:135], v[88:91], v[156:159]\n\tv_bfi_b32 v200, s46, v44, v43\n\t" \
    "v_bfi_b32 v201, s46, v43, v42\n\tv_mfma_f32_16x16x32_bf16 v[160:163], a[132:135], v[92:95], v[160:163]\n\tv_bfi_b32 v202, s46, v42, v59\n\tv_bfi_b32 v203, s46, v59, v58\n\tv_mfma_f32_16x16x32_bf16 v[140:143], a[136:139], v[68:71], v[140:143]\n\tds_write_b128 v2, v[200:203] offset:15552\n\tv_mfma_f32_16x16x32_bf16 v[144:147], a[136:139], v[72:75], v[144:147]\n\tv_alignbit_b32 v196, v49, v49, 16\n\t" \
    "v_alignbit_b32 v197, v48, v48, 16\n\tv_mfma_f32_16x16x32_bf16 v[148:151], a[136:139], v[76:79], v[148:151]\n\tv_alignbit_b32 v198, v47, v47, 16\n\tv_alignbit_b32 v199, v46, v46, 16\n\tv_mfma_f32_16x16x32_bf16 v[152:155], a[136:139], v[80:83], v[152:155]\n\tds_write_b128 v2, v[196:199] offset:2048\n\tv_mfma_f32_16x16x32_bf16 v[156:159], a[136:139], v[84:87], v[156:159]\n\tv_bfi_b32 v200, s46, v49, v48\n\t" \
    "v_bfi_b32 v201, s46, v48, v47\n\tv_mfma_f32_16x16x32_bf16 v[160:163], a[136:139], v[88:91], v[160:163]\n\tv_bfi_b32 v202, s46, v47, v46\n\tv_bfi_b32 v203, s46, v46, v61\n\tv_mfma_f32_16x16x32_bf16 v[144:147], a[140:143], v[68:71], v[144:147]\n\tds_write_b128 v2, v[200:203] offset:6208\n\tv_mfma_f32_16x16x32_bf16 v[148:151], a[140:143], v[72:75], v[148:151]\n\tv_alignbit_b32 v196, v48, v48, 16\n\t" \
    "v_alignbit_b32 v197, v47, v47, 16\n\tv_mfma_f32_16x16x32_bf16 v[152:155], a[140:143], v[76:79], v[152:155]\n\tv_alignbit_b32 v198, v46, v46, 16\n\tv_alignbit_b32 v199, v61, v61, 16\n\tv_mfma_f32_16x16x32_bf16 v[156:159], a[140:143], v[80:83], v[156:159]\n\tds_write_b128 v2, v[196:199] offset:10368\n\tv_mfma_f32_16x16x32_bf16 v[160:163], a[140:143], v[84:87], v[160:163]\n\tv_bfi_b32 v200, s46, v48, v47\n\t" \
    "v_bfi_b32 v201, s46, v47, v46\n\tv_mfma_f32_16x16x32_bf16 v[148:151], a[144:147], v[68:71], v[148:151]\n\tv_bfi_b32 v202, s46, v46, v61\n\tv_bfi_b32 v203, s46, v61, v60\n\tv_mfma_f32_16x16x32_bf16 v[152:155], a[144:147], v[72:75], v[152:155]\n\tds_write_b128 v2, v[200:203] offset:14528\n\tv_mfma_f32_16x16x32_bf16 v[156:159], a[144:147], v[76:79], v[156:159]\n\tv_alignbit_b32 v196, v53, v53, 16\n\t" \
    "v_alignbit_b32 v197, v52, v52, 16\n\tv_mfma_f32_16x16x32_bf16 v[160:163], a[144:147], v[80:83], v[160:163]\n\tv_alignbit_b32 v198, v51, v51, 16\n\tv_alignbit_b32 v199, v50, v50, 16\n\tv_mfma_f32_16x16x32_bf16 v[152:155], a[148:151], v[68:71], v[152:155]\n\tds_write_b128 v2, v[196:199] offset:1024\n\tv_mfma_f32_16x16x32_bf16 v[156:159], a[148:151], v[72:75], v[156:159]\n\tv_bfi_b32 v200, s46, v53, v52\n\t" \
    "v_bfi_b32 v201, s46, v52, v51\n\tv_mfma_f32_16x16x32_bf16 v[160:163], a[148:151], v[76:79], v[160:163]\n\tv_bfi_b32 v202, s46, v51, v50\n\tv_bfi_b32 v203, s46, v50, v63\n\tv_mfma_f32_16x16x32_bf16 v[156:159], a[152:155], v[68:71], v[156:159]\n\tds_write_b128 v2, v[200:203] offset:5184\n\tv_mfma_f32_16x16x32_bf16 v[160:163], a[152:155], v[72:75], v[160:163]\n\tv_alignbit_b32 v196, v52, v52, 16\n\t" \
    "v_alignbit_b32 v197, v51, v51, 16\n\tv_mfma_f32_16x16x32_bf16 v[160:163], a[156:159], v[68:71], v[160:163]\n\tv_alignbit_b32 v198, v50, v50, 16\n\tv_alignbit_b32 v199, v63, v63, 16\n\ts_waitcnt lgkmcnt(9)\n\tv_mfma_f32_16x16x32_bf16 v[132:135], a[160:163], v[100:103], v[132:135]\n\tds_write_b128 v2, v[196:199] offset:9344\n\tv_mfma_f32_16x16x32_bf16 v[136:139], a[160:163], v[104:107], v[136:139]\n\t" \
    "ds_read_b128 v[68:71], v1 offset:64\n\tv_bfi_b32 v200, s46, v52, v51\n\tv_mfma_f32_16x16x32_bf16 v[140:143], a[160:163], v[108:111], v[140:143]\n\tds_read_b128 v[72:75], v1 offset:576\n\tv_bfi_b32 v201, s46, v51, v50\n\tv_mfma_f32_16x16x32_bf16 v[144:147], a[160:163], v[112:115], v[144:147]\n\tds_read_b128 v[76:79], v1 offset:1088\n\tv_bfi_b32 v202, s46, v50, v63\n\t" \
    "v_mfma_f32_16x16x32_bf16 v[148:151], a[160:163], v[116:119], v[148:151]\n\tds_read_b128 v[80:83], v1 offset:1600\n\tv_bfi_b32 v203, s46, v63, v62\n\tv_mfma_f32_16x16x32_bf16 v[152:155], a[160:163], v[120:123], v[152:155]\n\tds_write_b128 v2, v[200:203] offset:13504\n\tv_mfma_f32_16x16x32_bf16 v[156:159], a[160:163], v[124:127], v[156:159]\n\tds_read_b128 v[84:87], v1 offset:2112\n\tv_alignbit_b32 v196, v57, v57, 16\n\t" \
    "v_mfma_f32_16x16x32_bf16 v[160:163], a[160:163], v[128:131], v[160:163]\n\tbuffer_load_dword v15, v6, s[12:15], s41 offen\n\tds_read_b128 v[88:91], v1 offset:2624\n\tv_mfma_f32_16x16x32_bf16 v[136:139], a[164:167], v[100:103], v[136:139]\n\tbuffer_load_dword v15, v7, s[16:19], s42 offen\n\tds_read_b128 v[92:95], v1 offset:3136\n\tv_mfma_f32_16x16x32_bf16 v[140:143], a[164:167], v[104:107], v[140:143]\n\ts_add_u32 s43, s43, 1\n\t" \
    "ds_read_b128 v[96:99], v1 offset:3648\n\tv_alignbit_b32 v197, v56, v56, 16\n\tv_mfma_f32_16x16x32_bf16 v[144:147], a[164:167], v[108:111], v[144:147]\n\tv_readlane_b32 s41, v236, s43\n\tv_readlane_b32 s42, v237, s43\n\ts_nop 4\n\tv_alignbit_b32 v198, v55, v55, 16\n\tv_alignbit_b32 v199, v54, v54, 16\n\t" \
    "v_mfma_f32_16x16x32_bf16 v[148:151], a[164:167], v[112:115], v[148:151]\n\tds_write_b128 v2, v[196:199] offset:0\n\tv_mfma_f32_16x16x32_bf16 v[152:155], a[164:167], v[116:119], v[152:155]\n\tv_bfi_b32 v200, s46, v57, v56\n\tv_bfi_b32 v201, s46, v56, v55\n\tv_mfma_f32_16x16x32_bf16 v[156:159], a[164:167], v[120:123], v[156:159]\n\tv_bfi_b32 v202, s46, v55, v54\n\tv_bfi_b32 v203, s46, v54, v65\n\t" \
    "v_mfma_f32_16x16x32_bf16 v[160:163], a[164:167], v[124:127], v[160:163]\n\tds_write_b128 v2, v[200:203] offset:4160\n\tv_mfma_f32_16x16x32_bf16 v[140:143], a[168:171], v[100:103], v[140:143]\n\tv_alignbit_b32 v196, v56, v56, 16\n\tv_alignbit_b32 v197, v55, v55, 16\n\tv_mfma_f32_16x16x32_bf16 v[144:147], a[168:171], v[104:107], v[144:147]\n\tv_alignbit_b32 v198, v54, v54, 16\n\tv_alignbit_b32 v199, v65, v65, 16\n\t" \
    "v_mfma_f32_16x16x32_bf16 v[148:151], a[168:171], v[108:111], v[148:151]\n\tds_write_b128 v2, v[196:199] offset:8320\n\tv_mfma_f32_16x16x32_bf16 v[152:155], a[168:171], v[112:115], v[152:155]\n\tv_bfi_b32 v200, s46, v56, v55\n\tv_bfi_b32 v201, s46, v55, v54\n\tv_mfma_f32_16x16x32_bf16 v[156:159], a[168:171], v[116:119], v[156:159]\n\tv_bfi_b32 v202, s46, v54, v65\n\tv_bfi_b32 v203, s46, v65, v64\n\t" \
    "v_mfma_f32_16x16x32_bf16 v[160:163], a[168:171], v[120:123], v[160:163]\n\tds_write_b128 v2, v[200:203] offset:12480\n\tv_mfma_f32_16x16x32_bf16 v[144:147], a[172:175], v[100:103], v[144:147]\n\tv_lshl_or_b32 v66, v66, 16, v66\n\tv_and_b32 v228, v66, v232\n\tv_mfma_f32_16x16x32_bf16 v[148:151], a[172:175], v[104:107], v[148:151]\n\tv_and_b32 v229, v66, v233\n\tv_and_b32 v230, v66, v234\n\t" \
    "v_mfma_f32_16x16x32_bf16 v[152:155], a[172:175], v[108:111], v[152:155]\n\tv_and_b32 v231, v66, v235\n\tv_mfma_f32_16x16x32_bf16 v[156:159], a[172:175], v[112:115], v[156:159]\n\tv_mfma_f32_16x16x32_bf16 v[160:163], a[172:175], v[116:119], v[160:163]\n\tv_mfma_f32_16x16x32_bf16 v[148:151], a[176:179], v[100:103], v[148:151]\n\tv_mfma_f32_16x16x32_bf16 v[152:155], a[176:179], v[104:107], v[152:155]\n\tv_mfma_f32_16x16x32_bf16 v[156:159], a[176:179], v[108:111], v[156:159]\n\tv_mfma_f32_16x16x32_bf16 v[160:163], a[176:179], v[112:115], v[160:163]\n\t" \
    "v_mfma_f32_16x16x32_bf16 v[152:155], a[180:183], v[100:103], v[152:155]\n\tv_mfma_f32_16x16x32_bf16 v[156:159], a[180:183], v[104:107], v[156:159]\n\tv_mfma_f32_16x16x32_bf16 v[160:163], a[180:183], v[108:111], v[160:163]\n\tv_mfma_f32_16x16x32_bf16 v[156:159], a[184:187], v[100:103], v[156:159]\n\tv_mfma_f32_16x16x32_bf16 v[160:163], a[184:187], v[104:107], v[160:163]\n\tv_mfma_f32_16x16x32_bf16 v[160:163], a[188:191], v[100:103], v[160:163]\n\ts_waitcnt lgkmcnt(4)\n\tv_mfma_f32_16x16x32_bf16 v[132:135], a[192:195], v[68:71], v[132:135]\n\t" \
    "s_add_u32 s68, s44, s38\n\tds_read_b128 v[100:103], v1 offset:0\n\tv_mfma_f32_16x16x32_bf16 v[136:139], a[192:195], v[72:75], v[136:139]\n\ts_add_u32 s68, s68, 8\n\tds_read_b128 v[104:107], v1 offset:512\n\tv_mfma_f32_16x16x32_bf16 v[140:143], a[192:195], v[76:79], v[140:143]\n\ts_lshl_b32 s66, s68, 1\n\tds_read_b128 v[108:111], v1 offset:1024\n\t" \
    "v_mfma_f32_16x16x32_bf16 v[144:147], a[192:195], v[80:83], v[144:147]\n\ts_lshl_b32 s68, s68, 12\n\tbuffer_load_dwordx4 v[16:19], v8, s[76:79], s68 offen offset:0\n\tds_read_b128 v[112:115], v1 offset:1536\n\tv_mfma_f32_16x16x32_bf16 v[148:151], a[192:195], v[84:87], v[148:151]\n\tds_read_b128 v[116:119], v1 offset:2048\n\tv_mfma_f32_16x16x32_bf16 v[152:155], a[192:195], v[88:91], v[152:155]\n\tds_read_b128 v[120:123], v1 offset:2560\n\t" \
    "v_mfma_f32_16x16x32_bf16 v[156:159], a[192:195], v[92:95], v[156:159]\n\tds_read_b128 v[124:127], v1 offset:3072\n\tv_mfma_f32_16x16x32_bf16 v[160:163], a[192:195], v[96:99], v[160:163]\n\tds_read_b128 v[128:131], v1 offset:3584\n\tv_mfma_f32_16x16x32_bf16 v[136:139], a[196:199], v[68:71], v[136:139]\n\tv_mfma_f32_16x16x32_bf16 v[140:143], a[196:199], v[72:75], v[140:143]\n\tbuffer_load_dwordx4 v[20:23], v8, s[76:79], s68 offen offset:1024\n\tv_mfma_f32_16x16x32_bf16 v[144:147], a[196:199], v[76:79], v[144:147]\n\t" \
    "v_mfma_f32_16x16x32_bf16 v[148:151], a[196:199], v[80:83], v[148:151]\n\tv_mfma_f32_16x16x32_bf16 v[152:155], a[196:199], v[84:87], v[152:155]\n\tv_mfma_f32_16x16x32_bf16 v[156:159], a[196:199], v[88:91], v[156:159]\n\tv_mfma_f32_16x16x32_bf16 v[160:163], a[196:199], v[92:95], v[160:163]\n\tv_mfma_f32_16x16x32_bf16 v[140:143], a[200:203], v[68:71], v[140:143]\n\tbuffer_load_dwordx4 v[24:27], v8, s[76:79], s68 offen offset:2048\n\tv_mfma_f32_16x16x32_bf16 v[144:147], a[200:203], v[72:75], v[144:147]\n\tv_mfma_f32_16x16x32_bf16 v[148:151], a[200:203], v[76:79], v[148:151]\n\t" \
    "v_mfma_f32_16x16x32_bf16 v[152:155], a[200:203], v[80:83], v[152:155]\n\tv_mfma_f32_16x16x32_bf16 v[156:159], a[200:203], v[84:87], v[156:159]\n\tv_mfma_f32_16x16x32_bf16 v[160:163], a[200:203], v[88:91], v[160:163]\n\tv_mfma_f32_16x16x32_bf16 v[144:147], a[204:207], v[68:71], v[144:147]\n\tbuffer_load_dwordx4 v[28:31], v8, s[76:79], s68 offen offset:3072\n\tv_mfma_f32_16x16x32_bf16 v[148:151], a[204:207], v[72:75], v[148:151]\n\tv_mfma_f32_16x16x32_bf16 v[152:155], a[204:207], v[76:79], v[152:155]\n\tv_mfma_f32_16x16x32_bf16 v[156:159], a[204:207], v[80:83], v[156:159]\n\t" \
    "v_mfma_f32_16x16x32_bf16 v[160:163], a[204:207], v[84:87], v[160:163]\n\tv_mfma_f32_16x16x32_bf16 v[148:151], a[208:211], v[68:71], v[148:151]\n\tv_mfma_f32_16x16x32_bf16 v[152:155], a[208:211], v[72:75], v[152:155]\n\tbuffer_load_dwordx2 v[32:33], v13, s[76:79], s68 offen\n\tv_mfma_f32_16x16x32_bf16 v[156:159], a[208:211], v[76:79], v[156:159]\n\tv_mfma_f32_16x16x32_bf16 v[160:163], a[208:211], v[80:83], v[160:163]\n\tv_mfma_f32_16x16x32_bf16 v[152:155], a[212:215], v[68:71], v[152:155]\n\tv_mfma_f32_16x16x32_bf16 v[156:159], a[212:215], v[72:75], v[156:159]\n\t" \
    "v_mfma_f32_16x16x32_bf16 v[160:163], a[212:215], v[76:79], v[160:163]\n\tv_mfma_f32_16x16x32_bf16 v[156:159], a[216:219], v[68:71], v[156:159]\n\tbuffer_load_dwordx2 v[34:35], v8, s[76:79], s68 offen offset:1016\n\tv_mfma_f32_16x16x32_bf16 v[160:163], a[216:219], v[72:75], v[160:163]\n\tv_mfma_f32_16x16x32_bf16 v[160:163], a[220:223], v[68:71], v[160:163]\n\ts_waitcnt lgkmcnt(0)\n\tv_mfma_f32_16x16x32_bf16 v[132:135], a[224:227], v[100:103], v[132:135]\n\tds_read_b64 a[0:1], v0 offset:4032\n\t" \
    "v_mfma_f32_16x16x32_bf16 v[136:139], a[224:227], v[104:107], v[136:139]\n\tds_read_b64 a[2:3], v0 offset:4040\n\tv_mfma_f32_16x16x32_bf16 v[140:143], a[224:227], v[108:111], v[140:143]\n\tds_read_b64 a[4:5], v0 offset:3520\n\tv_mfma_f32_16x16x32_bf16 v[144:147], a[224:227], v[112:115], v[144:147]\n\tbuffer_load_dwordx2 v[36:37], v8, s[76:79], s68 offen offset:2040\n\tds_read_b64 a[6:7], v0 offset:3528\n\tv_mfma_f32_16x16x32_bf16 v[148:151], a[224:227], v[116:119], v[148:151]\n\t" \
    "ds_read_b64 a[8:9], v0 offset:3008\n\tv_mfma_f32_16x16x32_bf16 v[152:155], a[224:227], v[120:123], v[152:155]\n\tds_read_b64 a[10:11], v0 offset:3016\n\tv_mfma_f32_16x16x32_bf16 v[156:159], a[224:227], v[124:127], v[156:159]\n\tds_read_b64 a[12:13], v0 offset:2496\n\tv_mfma_f32_16x16x32_bf16 v[160:163], a[224:227], v[128:131], v[160:163]\n\tds_read_b64 a[14:15], v0 offset:2504\n\tv_mfma_f32_16x16x32_bf16 v[136:139], a[228:231], v[100:103], v[136:139]\n\t" \
    "ds_read_b64 a[16:17], v0 offset:1984\n\tv_mfma_f32_16x16x32_bf16 v[140:143], a[228:231], v[104:107], v[140:143]\n\tbuffer_load_dwordx2 v[38:39], v8, s[76:79], s68 offen offset:3064\n\tds_read_b64 a[18:19], v0 offset:1992\n\tv_mfma_f32_16x16x32_bf16 v[144:147], a[228:231], v[108:111], v[144:147]\n\tds_read_b64 a[20:21], v0 offset:1472\n\tv_mfma_f32_16x16x32_bf16 v[148:151], a[228:231], v[112:115], v[148:151]\n\tds_read_b64 a[22:23], v0 offset:1480\n\t" \
    "v_mfma_f32_16x16x32_bf16 v[152:155], a[228:231], v[116:119], v[152:155]\n\tds_read_b64 a[24:25], v0 offset:960\n\tv_mfma_f32_16x16x32_bf16 v[156:159], a[228:231], v[120:123], v[156:159]\n\tds_read_b64 a[26:27], v0 offset:968\n\tv_mfma_f32_16x16x32_bf16 v[160:163], a[228:231], v[124:127], v[160:163]\n\tds_read_b64 a[28:29], v0 offset:448\n\tv_mfma_f32_16x16x32_bf16 v[140:143], a[232:235], v[100:103], v[140:143]\n\tbuffer_load_ushort v40, off, s[80:83], s66\n\t" \
    "ds_read_b64 a[30:31], v0 offset:456\n\tv_mfma_f32_16x16x32_bf16 v[144:147], a[232:235], v[104:107], v[144:147]\n\tds_read_b128 v[68:71], v1 offset:18944\n\tv_mfma_f32_16x16x32_bf16 v[148:151], a[232:235], v[108:111], v[148:151]\n\tds_read_b128 v[72:75], v1 offset:19456\n\tv_mfma_f32_16x16x32_bf16 v[152:155], a[232:235], v[112:115], v[152:155]\n\tds_read_b128 v[76:79], v1 offset:19968\n\tv_mfma_f32_16x16x32_bf16 v[156:159], a[232:235], v[116:119], v[156:159]\n\t" \
    "ds_read_b128 v[80:83], v1 offset:20480\n\tv_mfma_f32_16x16x32_bf16 v[160:163], a[232:235], v[120:123], v[160:163]\n\tds_read_b128 v[84:87], v1 offset:20992\n\tv_mfma_f32_16x16x32_bf16 v[144:147], a[236:239], v[100:103], v[144:147]\n\tbuffer_load_dword v15, v6, s[12:15], s41 offen\n\tds_read_b128 v[88:91], v1 offset:21504\n\tv_mfma_f32_16x16x32_bf16 v[148:151], a[236:239], v[104:107], v[148:151]\n\tbuffer_load_dword v15, v7, s[16:19], s42 offen\n\t" \
    "ds_read_b128 v[92:95], v1 offset:22016\n\tv_mfma_f32_16x16x32_bf16 v[152:155], a[236:239], v[108:111], v[152:155]\n\ts_add_u32 s43, s43, 1\n\tds_read_b128 v[96:99], v1 offset:22528\n\tv_mfma_f32_16x16x32_bf16 v[156:159], a[236:239], v[112:115], v[156:159]\n\tv_readlane_b32 s41, v236, s43\n\tv_readlane_b32 s42, v237, s43\n\ts_nop 4\n\t" \
    "v_mfma_f32_16x16x32_bf16 v[160:163], a[236:239], v[116:119], v[160:163]\n\tv_mfma_f32_16x16x32_bf16 v[148:151], a[240:243], v[100:103], v[148:151]\n\tv_mfma_f32_16x16x32_bf16 v[152:155], a[240:243], v[104:107], v[152:155]\n\tv_mfma_f32_16x16x32_bf16 v[156:159], a[240:243], v[108:111], v[156:159]\n\tv_mfma_f32_16x16x32_bf16 v[160:163], a[240:243], v[112:115], v[160:163]\n\tv_mfma_f32_16x16x32_bf16 v[152:155], a[244:247], v[100:103], v[152:155]\n\tv_mfma_f32_16x16x32_bf16 v[156:159], a[244:247], v[104:107], v[156:159]\n\tv_mfma_f32_16x16x32_bf16 v[160:163], a[244:247], v[108:111], v[160:163]\n\t" \
    "v_mfma_f32_16x16x32_bf16 v[156:159], a[248:251], v[100:103], v[156:159]\n\tv_mfma_f32_16x16x32_bf16 v[160:163], a[248:251], v[104:107], v[160:163]\n\tv_mfma_f32_16x16x32_bf16 v[160:163], a[252:255], v[100:103], v[160:163]\n\ts_waitcnt lgkmcnt(0)\n\tv_mfma_f32_16x16x32_bf16 v[164:167], v[228:231], v[68:71], 0\n\tds_read_b64 a[32:33], v0 offset:3968\n\tds_read_b64 a[34:35], v0 offset:3976\n\tv_mfma_f32_16x16x32_bf16 v[168:171], v[228:231], v[72:75], 0\n\t" \
    "ds_read_b64 a[36:37], v0 offset:3456\n\tds_read_b64 a[38:39], v0 offset:3464\n\tv_mfma_f32_16x16x32_bf16 v[172:175], v[228:231], v[76:79], 0\n\tds_read_b64 a[40:41], v0 offset:2944\n\tds_read_b64 a[42:43], v0 offset:2952\n\tv_mfma_f32_16x16x32_bf16 v[176:179], v[228:231], v[80:83], 0\n\tds_read_b64 a[44:45], v0 offset:2432\n\tds_read_b64 a[46:47], v0 offset:2440\n\t" \
    "v_mfma_f32_16x16x32_bf16 v[180:183], v[228:231], v[84:87], 0\n\tds_read_b64 a[48:49], v0 offset:1920\n\tds_read_b64 a[50:51], v0 offset:1928\n\tv_mfma_f32_16x16x32_bf16 v[184:187], v[228:231], v[88:91], 0\n\tds_read_b64 a[52:53], v0 offset:1408\n\tv_cvt_pk_bf16_f32 v132, v132, v133\n\tv_mfma_f32_16x16x32_bf16 v[188:191], v[228:231], v[92:95], 0\n\tds_read_b64 a[54:55], v0 offset:1416\n\t" \
    "v_cvt_pk_bf16_f32 v133, v134, v135\n\tv_mfma_f32_16x16x32_bf16 v[192:195], v[228:231], v[96:99], 0\n\tds_read_b64 a[56:57], v0 offset:896\n\tds_write_b64 v3, v[132:133] offset:0\n\tv_mfma_f32_16x16x32_bf16 v[164:167], a[0:3], v[68:71], v[164:167]\n\tds_read_b64 a[58:59], v0 offset:904\n\tv_cvt_pk_bf16_f32 v136, v136, v137\n\tv_mfma_f32_16x16x32_bf16 v[168:171], a[0:3], v[72:75], v[168:171]\n\t" \
    "ds_read_b64 a[60:61], v0 offset:384\n\tv_cvt_pk_bf16_f32 v137, v138, v139\n\tv_mfma_f32_16x16x32_bf16 v[172:175], a[0:3], v[76:79], v[172:175]\n\tds_read_b64 a[62:63], v0 offset:392\n\tds_write_b64 v3, v[136:137] offset:512\n\tv_mfma_f32_16x16x32_bf16 v[176:179], a[0:3], v[80:83], v[176:179]\n\tds_read_b128 v[100:103], v1 offset:18880\n\tv_cvt_pk_bf16_f32 v140, v140, v141\n\t" \
    "v_mfma_f32_16x16x32_bf16 v[180:183], a[0:3], v[84:87], v[180:183]\n\tds_read_b128 v[104:107], v1 offset:19392\n\tv_cvt_pk_bf16_f32 v141, v142, v143\n\tv_mfma_f32_16x16x32_bf16 v[184:187], a[0:3], v[88:91], v[184:187]\n\tds_read_b128 v[108:111], v1 offset:19904\n\tds_write_b64 v3, v[140:141] offset:1024\n\tv_mfma_f32_16x16x32_bf16 v[188:191], a[0:3], v[92:95], v[188:191]\n\tds_read_b128 v[112:115], v1 offset:20416\n\t" \
    "v_cvt_pk_bf16_f32 v144, v144, v145\n\tv_mfma_f32_16x16x32_bf16 v[192:195], a[0:3], v[96:99], v[192:195]\n\tds_read_b128 v[116:119], v1 offset:20928\n\tv_cvt_pk_bf16_f32 v145, v146, v147\n\tv_mfma_f32_16x16x32_bf16 v[168:171], a[4:7], v[68:71], v[168:171]\n\tds_read_b128 v[120:123], v1 offset:21440\n\tds_write_b64 v3, v[144:145] offset:1536\n\tv_mfma_f32_16x16x32_bf16 v[172:175], a[4:7], v[72:75], v[172:175]\n\t" \
    "buffer_load_dword v15, v6, s[12:15], s41 offen\n\tds_read_b128 v[124:127], v1 offset:21952\n\tv_mfma_f32_16x16x32_bf16 v[176:179], a[4:7], v[76:79], v[176:179]\n\tbuffer_load_dword v15, v7, s[16:19], s42 offen\n\tds_read_b128 v[128:131], v1 offset:22464\n\tv_mfma_f32_16x16x32_bf16 v[180:183], a[4:7], v[80:83], v[180:183]\n\ts_add_u32 s43, s43, 1\n\tds_read_b64 a[64:65], v0 offset:3904\n\t" \
    "v_cvt_pk_bf16_f32 v148, v148, v149\n\tv_mfma_f32_16x16x32_bf16 v[184:187], a[4:7], v[84:87], v[184:187]\n\tv_readlane_b32 s41, v236, s43\n\tv_readlane_b32 s42, v237, s43\n\ts_nop 4\n\tds_read_b64 a[66:67], v0 offset:3912\n\tv_cvt_pk_bf16_f32 v149, v150, v151\n\tv_mfma_f32_16x16x32_bf16 v[188:191], a[4:7], v[88:91], v[188:191]\n\t" \
    "ds_read_b64 a[68:69], v0 offset:3392\n\tds_write_b64 v3, v[148:149] offset:2048\n\tv_mfma_f32_16x16x32_bf16 v[192:195], a[4:7], v[92:95], v[192:195]\n\tds_read_b64 a[70:71], v0 offset:3400\n\tv_cvt_pk_bf16_f32 v152, v152, v153\n\tv_mfma_f32_16x16x32_bf16 v[172:175], a[8:11], v[68:71], v[172:175]\n\tds_read_b64 a[72:73], v0 offset:2880\n\tv_cvt_pk_bf16_f32 v153, v154, v155\n\t" \
    "v_mfma_f32_16x16x32_bf16 v[176:179], a[8:11], v[72:75], v[176:179]\n\tds_read_b64 a[74:75], v0 offset:2888\n\tds_write_b64 v3, v[152:153] offset:2560\n\tv_mfma_f32_16x16x32_bf16 v[180:183], a[8:11], v[76:79], v[180:183]\n\tds_read_b64 a[76:77], v0 offset:2368\n\tv_cvt_pk_bf16_f32 v156, v156, v157\n\tv_mfma_f32_16x16x32_bf16 v[184:187], a[8:11], v[80:83], v[184:187]\n\tds_read_b64 a[78:79], v0 offset:2376\n\t" \
    "v_cvt_pk_bf16_f32 v157, v158, v159\n\tv_mfma_f32_16x16x32_bf16 v[188:191], a[8:11], v[84:87], v[188:191]\n\tds_read_b64 a[80:81], v0 offset:1856\n\tds_write_b64 v3, v[156:157] offset:3072\n\tv_mfma_f32_16x16x32_bf16 v[192:195], a[8:11], v[88:91], v[192:195]\n\tds_read_b64 a[82:83], v0 offset:1864\n\tv_cvt_pk_bf16_f32 v160, v160, v161\n\tv_mfma_f32_16x16x32_bf16 v[176:179], a[12:15], v[68:71], v[176:179]\n\t" \
    "ds_read_b64 a[84:85], v0 offset:1344\n\tv_cvt_pk_bf16_f32 v161, v162, v163\n\tv_mfma_f32_16x16x32_bf16 v[180:183], a[12:15], v[72:75], v[180:183]\n\tds_read_b64 a[86:87], v0 offset:1352\n\tds_write_b64 v3, v[160:161] offset:3584\n\tv_mfma_f32_16x16x32_bf16 v[184:187], a[12:15], v[76:79], v[184:187]\n\tds_read_b64 a[88:89], v0 offset:832\n\tv_mfma_f32_16x16x32_bf16 v[188:191], a[12:15], v[80:83], v[188:191]\n\t" \
    "ds_read_b64 a[90:91], v0 offset:840\n\tv_mfma_f32_16x16x32_bf16 v[192:195], a[12:15], v[84:87], v[192:195]\n\tds_read_b64 a[92:93], v0 offset:320\n\tv_mfma_f32_16x16x32_bf16 v[180:183], a[16:19], v[68:71], v[180:183]\n\tds_read_b64 a[94:95], v0 offset:328\n\tv_mfma_f32_16x16x32_bf16 v[184:187], a[16:19], v[72:75], v[184:187]\n\tds_read_b64 a[96:97], v0 offset:3840\n\tv_mfma_f32_16x16x32_bf16 v[188:191], a[16:19], v[76:79], v[188:191]\n\t" \
    "ds_read_b64 a[98:99], v0 offset:3848\n\tv_mfma_f32_16x16x32_bf16 v[192:195], a[16:19], v[80:83], v[192:195]\n\tds_read_b64 a[100:101], v0 offset:3328\n\tv_mfma_f32_16x16x32_bf16 v[184:187], a[20:23], v[68:71], v[184:187]\n\tds_read_b64 a[102:103], v0 offset:3336\n\tv_mfma_f32_16x16x32_bf16 v[188:191], a[20:23], v[72:75], v[188:191]\n\tv_mfma_f32_16x16x32_bf16 v[192:195], a[20:23], v[76:79], v[192:195]\n\tv_mfma_f32_16x16x32_bf16 v[188:191], a[24:27], v[68:71], v[188:191]\n\t" \
    "v_mfma_f32_16x16x32_bf16 v[192:195], a[24:27], v[72:75], v[192:195]\n\tv_mfma_f32_16x16x32_bf16 v[192:195], a[28:31], v[68:71], v[192:195]\n\ts_waitcnt lgkmcnt(8)\n\ts_barrier\n\tv_mfma_f32_16x16x32_bf16 v[164:167], a[32:35], v[100:103], v[164:167]\n\tds_read_b64 a[104:105], v0 offset:2816\n\tds_read_b64 a[106:107], v0 offset:2824\n\tv_mfma_f32_16x16x32_bf16 v[168:171], a[32:35], v[104:107], v[168:171]\n\t" \
    "ds_read_b64 a[108:109], v0 offset:2304\n\tds_read_b64 a[110:111], v0 offset:2312\n\tv_mfma_f32_16x16x32_bf16 v[172:175], a[32:35], v[108:111], v[172:175]\n\tds_read_b64 a[112:113], v0 offset:1792\n\tds_read_b64 a[114:115], v0 offset:1800\n\tv_mfma_f32_16x16x32_bf16 v[176:179], a[32:35], v[112:115], v[176:179]\n\tds_read_b64 a[116:117], v0 offset:1280\n\tds_read_b64 a[118:119], v0 offset:1288\n\t" \
    "v_mfma_f32_16x16x32_bf16 v[180:183], a[32:35], v[116:119], v[180:183]\n\tds_read_b64 a[120:121], v0 offset:768\n\tds_read_b64 a[122:123], v0 offset:776\n\tv_mfma_f32_16x16x32_bf16 v[184:187], a[32:35], v[120:123], v[184:187]\n\tds_read_b64 a[124:125], v0 offset:256\n\ts_add_u32 s51, s44, 0\n\tv_mfma_f32_16x16x32_bf16 v[188:191], a[32:35], v[124:127], v[188:191]\n\tds_read_b64 a[126:127], v0 offset:264\n\t" \
    "s_mul_i32 s65, s51, 1366\n\tv_mfma_f32_16x16x32_bf16 v[192:195], a[32:35], v[128:131], v[192:195]\n\tds_read_b64 a[128:129], v0 offset:3776\n\ts_lshr_b32 s65, s65, 16\n\tv_mfma_f32_16x16x32_bf16 v[168:171], a[36:39], v[100:103], v[168:171]\n\tds_read_b64 a[130:131], v0 offset:3784\n\ts_mul_i32 s56, s65, 48\n\tv_mfma_f32_16x16x32_bf16 v[172:175], a[36:39], v[104:107], v[172:175]\n\t" \
    "ds_read_b64 a[132:133], v0 offset:3264\n\ts_sub_u32 s56, s51, s56\n\tv_mfma_f32_16x16x32_bf16 v[176:179], a[36:39], v[108:111], v[176:179]\n\tds_read_b64 a[134:135], v0 offset:3272\n\ts_lshl_b32 s56, s56, 6\n\tv_mfma_f32_16x16x32_bf16 v[180:183], a[36:39], v[112:115], v[180:183]\n\tds_read_b128 v[68:71], v1 offset:18816\n\ts_lshr_b32 s70, s65, 2\n\t" \
    "v_mfma_f32_16x16x32_bf16 v[184:187], a[36:39], v[116:119], v[184:187]\n\tds_read_b128 v[72:75], v1 offset:19328\n\ts_mul_i32 s70, s70, 252\n\tv_mfma_f32_16x16x32_bf16 v[188:191], a[36:39], v[120:123], v[188:191]\n\tds_read_b128 v[76:79], v1 offset:19840\n\ts_add_u32 s65, s65, s70\n\tv_mfma_f32_16x16x32_bf16 v[192:195], a[36:39], v[124:127], v[192:195]\n\tds_read_b128 v[80:83], v1 offset:20352\n\t" \
    "s_mul_i32 s65, s65, 3072\n\tv_mfma_f32_16x16x32_bf16 v[172:175], a[40:43], v[100:103], v[172:175]\n\tds_read_b128 v[84:87], v1 offset:20864\n\ts_add_u32 s56, s56, s65\n\tv_mfma_f32_16x16x32_bf16 v[176:179], a[40:43], v[104:107], v[176:179]\n\tds_read_b128 v[88:91], v1 offset:21376\n\ts_add_u32 s56, s56, 4849664\n\tv_mfma_f32_16x16x32_bf16 v[180:183], a[40:43], v[108:111], v[180:183]\n\t" \
    "ds_read_b128 v[92:95], v1 offset:21888\n\ts_add_u32 s51, s51, s38\n\tv_mfma_f32_16x16x32_bf16 v[184:187], a[40:43], v[112:115], v[184:187]\n\tds_read_b128 v[96:99], v1 offset:22400\n\ts_lshl_b32 s51, s51, 3\n\tv_mfma_f32_16x16x32_bf16 v[188:191], a[40:43], v[116:119], v[188:191]\n\tds_read_b64 a[136:137], v0 offset:2752\n\ts_add_u32 s51, s51, 50176\n\t" \
    "ds_read_b64_tr_b16 v[204:205], v4 offset:0\n\tv_mfma_f32_16x16x32_bf16 v[192:195], a[40:43], v[120:123], v[192:195]\n\tds_read_b64 a[138:139], v0 offset:2760\n\tds_read_b64_tr_b16 v[206:207], v4 offset:8\n\tv_mfma_f32_16x16x32_bf16 v[176:179], a[44:47], v[100:103], v[176:179]\n\tds_read_b64 a[140:141], v0 offset:2240\n\tds_read_b64_tr_b16 v[208:209], v4 offset:64\n\tv_mfma_f32_16x16x32_bf16 v[180:183], a[44:47], v[104:107], v[180:183]\n\t" \
    "ds_read_b64 a[142:143], v0 offset:2248\n\tds_read_b64_tr_b16 v[210:211], v4 offset:72\n\tv_mfma_f32_16x16x32_bf16 v[184:187], a[44:47], v[108:111], v[184:187]\n\tds_read_b64 a[144:145], v0 offset:1728\n\tds_read_b64_tr_b16 v[212:213], v4 offset:512\n\tv_mfma_f32_16x16x32_bf16 v[188:191], a[44:47], v[112:115], v[188:191]\n\tds_read_b64 a[146:147], v0 offset:1736\n\tds_read_b64_tr_b16 v[214:215], v4 offset:520\n\t" \
    "v_mfma_f32_16x16x32_bf16 v[192:195], a[44:47], v[116:119], v[192:195]\n\tds_read_b64 a[148:149], v0 offset:1216\n\tds_read_b64_tr_b16 v[216:217], v4 offset:576\n\tv_mfma_f32_16x16x32_bf16 v[180:183], a[48:51], v[100:103], v[180:183]\n\tds_read_b64 a[150:151], v0 offset:1224\n\tds_read_b64_tr_b16 v[218:219], v4 offset:584\n\tv_mfma_f32_16x16x32_bf16 v[184:187], a[48:51], v[104:107], v[184:187]\n\tds_read_b64 a[152:153], v0 offset:704\n\t" \
    "v_mfma_f32_16x16x32_bf16 v[188:191], a[48:51], v[108:111], v[188:191]\n\tds_read_b64 a[154:155], v0 offset:712\n\tv_mfma_f32_16x16x32_bf16 v[192:195], a[48:51], v[112:115], v[192:195]\n\tds_read_b64 a[156:157], v0 offset:192\n\tv_mfma_f32_16x16x32_bf16 v[184:187], a[52:55], v[100:103], v[184:187]\n\tds_read_b64 a[158:159], v0 offset:200\n\tv_mfma_f32_16x16x32_bf16 v[188:191], a[52:55], v[104:107], v[188:191]\n\tv_mfma_f32_16x16x32_bf16 v[192:195], a[52:55], v[108:111], v[192:195]\n\t" \
    "buffer_load_dword v15, v7, s[16:19], s42 offen\n\ts_waitcnt lgkmcnt(15)\n\tbuffer_store_dwordx4 v[204:207], v5, s[8:11], s56 offen sc1\n\tv_mfma_f32_16x16x32_bf16 v[188:191], a[56:59], v[100:103], v[188:191]\n\ts_add_u32 s43, s43, 1\n\ts_add_u32 s56, s56, 49152\n\tv_mfma_f32_16x16x32_bf16 v[192:195], a[56:59], v[104:107], v[192:195]\n\tv_readlane_b32 s42, v237, s43\n\t" \
    "s_nop 4\n\tv_mfma_f32_16x16x32_bf16 v[192:195], a[60:63], v[100:103], v[192:195]\n\tv_mfma_f32_16x16x32_bf16 v[164:167], a[64:67], v[68:71], v[164:167]\n\tds_read_b64 a[160:161], v0 offset:3712\n\tds_read_b64 a[162:163], v0 offset:3720\n\tv_mfma_f32_16x16x32_bf16 v[168:171], a[64:67], v[72:75], v[168:171]\n\tds_read_b64 a[164:165], v0 offset:3200\n\tds_read_b64 a[166:167], v0 offset:3208\n\t" \
    "v_mfma_f32_16x16x32_bf16 v[172:175], a[64:67], v[76:79], v[172:175]\n\tds_read_b64 a[168:169], v0 offset:2688\n\tds_read_b64 a[170:171], v0 offset:2696\n\tv_mfma_f32_16x16x32_bf16 v[176:179], a[64:67], v[80:83], v[176:179]\n\tds_read_b64 a[172:173], v0 offset:2176\n\tds_read_b64 a[174:175], v0 offset:2184\n\tv_mfma_f32_16x16x32_bf16 v[180:183], a[64:67], v[84:87], v[180:183]\n\tds_read_b64 a[176:177], v0 offset:1664\n\t" \
    "ds_read_b64 a[178:179], v0 offset:1672\n\tv_mfma_f32_16x16x32_bf16 v[184:187], a[64:67], v[88:91], v[184:187]\n\tds_read_b64 a[180:181], v0 offset:1152\n\tv_mfma_f32_16x16x32_bf16 v[188:191], a[64:67], v[92:95], v[188:191]\n\tds_read_b64 a[182:183], v0 offset:1160\n\tv_mfma_f32_16x16x32_bf16 v[192:195], a[64:67], v[96:99], v[192:195]\n\tds_read_b64 a[184:185], v0 offset:640\n\tv_mfma_f32_16x16x32_bf16 v[168:171], a[68:71], v[68:71], v[168:171]\n\t" \
    "ds_read_b64 a[186:187], v0 offset:648\n\tv_mfma_f32_16x16x32_bf16 v[172:175], a[68:71], v[72:75], v[172:175]\n\tds_read_b64 a[188:189], v0 offset:128\n\tv_mfma_f32_16x16x32_bf16 v[176:179], a[68:71], v[76:79], v[176:179]\n\tds_read_b64 a[190:191], v0 offset:136\n\tv_mfma_f32_16x16x32_bf16 v[180:183], a[68:71], v[80:83], v[180:183]\n\tds_read_b128 v[100:103], v1 offset:18752\n\tv_mfma_f32_16x16x32_bf16 v[184:187], a[68:71], v[84:87], v[184:187]\n\t" \
    "ds_read_b128 v[104:107], v1 offset:19264\n\tv_mfma_f32_16x16x32_bf16 v[188:191], a[68:71], v[88:91], v[188:191]\n\tds_read_b128 v[108:111], v1 offset:19776\n\tv_mfma_f32_16x16x32_bf16 v[192:195], a[68:71], v[92:95], v[192:195]\n\tds_read_b128 v[112:115], v1 offset:20288\n\tv_mfma_f32_16x16x32_bf16 v[172:175], a[72:75], v[68:71], v[172:175]\n\tds_read_b128 v[116:119], v1 offset:20800\n\ts_waitcnt lgkmcnt(15)\n\t" \
    "buffer_store_dwordx4 v[208:211], v5, s[8:11], s56 offen sc1\n\tv_mfma_f32_16x16x32_bf16 v[176:179], a[72:75], v[72:75], v[176:179]\n\tds_read_b128 v[120:123], v1 offset:21312\n\ts_add_u32 s56, s56, 49152\n\tv_mfma_f32_16x16x32_bf16 v[180:183], a[72:75], v[76:79], v[180:183]\n\tds_read_b128 v[124:127], v1 offset:21824\n\tv_mfma_f32_16x16x32_bf16 v[184:187], a[72:75], v[80:83], v[184:187]\n\tds_read_b128 v[128:131], v1 offset:22336\n\t" \
    "v_mfma_f32_16x16x32_bf16 v[188:191], a[72:75], v[84:87], v[188:191]\n\tds_read_b64 a[192:193], v0 offset:3648\n\tv_mfma_f32_16x16x32_bf16 v[192:195], a[72:75], v[88:91], v[192:195]\n\tds_read_b64 a[194:195], v0 offset:3656\n\tv_mfma_f32_16x16x32_bf16 v[176:179], a[76:79], v[68:71], v[176:179]\n\tds_read_b64 a[196:197], v0 offset:3136\n\tv_mfma_f32_16x16x32_bf16 v[180:183], a[76:79], v[72:75], v[180:183]\n\tds_read_b64 a[198:199], v0 offset:3144\n\t" \
    "v_mfma_f32_16x16x32_bf16 v[184:187], a[76:79], v[76:79], v[184:187]\n\tds_read_b64 a[200:201], v0 offset:2624\n\tv_mfma_f32_16x16x32_bf16 v[188:191], a[76:79], v[80:83], v[188:191]\n\tds_read_b64 a[202:203], v0 offset:2632\n\tv_mfma_f32_16x16x32_bf16 v[192:195], a[76:79], v[84:87], v[192:195]\n\tds_read_b64 a[204:205], v0 offset:2112\n\tv_mfma_f32_16x16x32_bf16 v[180:183], a[80:83], v[68:71], v[180:183]\n\tds_read_b64 a[206:207], v0 offset:2120\n\t" \
    "v_mfma_f32_16x16x32_bf16 v[184:187], a[80:83], v[72:75], v[184:187]\n\tds_read_b64 a[208:209], v0 offset:1600\n\tv_mfma_f32_16x16x32_bf16 v[188:191], a[80:83], v[76:79], v[188:191]\n\tds_read_b64 a[210:211], v0 offset:1608\n\tv_mfma_f32_16x16x32_bf16 v[192:195], a[80:83], v[80:83], v[192:195]\n\tds_read_b64 a[212:213], v0 offset:1088\n\tv_mfma_f32_16x16x32_bf16 v[184:187], a[84:87], v[68:71], v[184:187]\n\tds_read_b64 a[214:215], v0 offset:1096\n\t" \
    "v_mfma_f32_16x16x32_bf16 v[188:191], a[84:87], v[72:75], v[188:191]\n\tbuffer_store_dwordx4 v[212:215], v5, s[8:11], s56 offen sc1\n\tv_mfma_f32_16x16x32_bf16 v[192:195], a[84:87], v[76:79], v[192:195]\n\ts_add_u32 s56, s56, 49152\n\tv_mfma_f32_16x16x32_bf16 v[188:191], a[88:91], v[68:71], v[188:191]\n\tv_mfma_f32_16x16x32_bf16 v[192:195], a[88:91], v[72:75], v[192:195]\n\tv_mfma_f32_16x16x32_bf16 v[192:195], a[92:95], v[68:71], v[192:195]\n\ts_waitcnt lgkmcnt(12)\n\t" \
    "v_mfma_f32_16x16x32_bf16 v[164:167], a[96:99], v[100:103], v[164:167]\n\tds_read_b64 a[216:217], v0 offset:576\n\tv_mfma_f32_16x16x32_bf16 v[168:171], a[96:99], v[104:107], v[168:171]\n\tds_read_b64 a[218:219], v0 offset:584\n\tv_mfma_f32_16x16x32_bf16 v[172:175], a[96:99], v[108:111], v[172:175]\n\tds_read_b64 a[220:221], v0 offset:64\n\tv_mfma_f32_16x16x32_bf16 v[176:179], a[96:99], v[112:115], v[176:179]\n\tds_read_b64 a[222:223], v0 offset:72\n\t" \
    "v_mfma_f32_16x16x32_bf16 v[180:183], a[96:99], v[116:119], v[180:183]\n\tds_read_b64 a[224:225], v0 offset:3584\n\tv_mfma_f32_16x16x32_bf16 v[184:187], a[96:99], v[120:123], v[184:187]\n\tds_read_b64 a[226:227], v0 offset:3592\n\tv_mfma_f32_16x16x32_bf16 v[188:191], a[96:99], v[124:127], v[188:191]\n\tds_read_b64 a[228:229], v0 offset:3072\n\tv_mfma_f32_16x16x32_bf16 v[192:195], a[96:99], v[128:131], v[192:195]\n\tds_read_b64 a[230:231], v0 offset:3080\n\t" \
    "v_mfma_f32_16x16x32_bf16 v[168:171], a[100:103], v[100:103], v[168:171]\n\tds_read_b64 a[232:233], v0 offset:2560\n\tbuffer_store_dwordx4 v[216:219], v5, s[8:11], s56 offen sc1\n\tv_mfma_f32_16x16x32_bf16 v[172:175], a[100:103], v[104:107], v[172:175]\n\tds_read_b64 a[234:235], v0 offset:2568\n\ts_add_u32 s56, s56, 49152\n\tv_mfma_f32_16x16x32_bf16 v[176:179], a[100:103], v[108:111], v[176:179]\n\tds_read_b64 a[236:237], v0 offset:2048\n\t" \
    "v_mfma_f32_16x16x32_bf16 v[180:183], a[100:103], v[112:115], v[180:183]\n\tds_read_b64 a[238:239], v0 offset:2056\n\tv_mfma_f32_16x16x32_bf16 v[184:187], a[100:103], v[116:119], v[184:187]\n\tds_read_b64 a[240:241], v0 offset:1536\n\tv_mfma_f32_16x16x32_bf16 v[188:191], a[100:103], v[120:123], v[188:191]\n\tds_read_b64 a[242:243], v0 offset:1544\n\tv_mfma_f32_16x16x32_bf16 v[192:195], a[100:103], v[124:127], v[192:195]\n\tds_read_b64 a[244:245], v0 offset:1024\n\t" \
    "v_mfma_f32_16x16x32_bf16 v[172:175], a[104:107], v[100:103], v[172:175]\n\tds_read_b64 a[246:247], v0 offset:1032\n\tv_mfma_f32_16x16x32_bf16 v[176:179], a[104:107], v[104:107], v[176:179]\n\tds_read_b128 v[68:71], v1 offset:18688\n\tv_mfma_f32_16x16x32_bf16 v[180:183], a[104:107], v[108:111], v[180:183]\n\tds_read_b128 v[72:75], v1 offset:19200\n\tv_mfma_f32_16x16x32_bf16 v[184:187], a[104:107], v[112:115], v[184:187]\n\tds_read_b128 v[76:79], v1 offset:19712\n\t" \
    "v_mfma_f32_16x16x32_bf16 v[188:191], a[104:107], v[116:119], v[188:191]\n\tds_read_b128 v[80:83], v1 offset:20224\n\tv_mfma_f32_16x16x32_bf16 v[192:195], a[104:107], v[120:123], v[192:195]\n\ts_lshl_b32 s69, s67, 13\n\tbuffer_load_dword v15, v14, s[4:7], s69 offen\n\tds_read_b128 v[84:87], v1 offset:20736\n\tv_mfma_f32_16x16x32_bf16 v[176:179], a[108:111], v[100:103], v[176:179]\n\tds_read_b128 v[88:91], v1 offset:21248\n\t" \
    "v_mfma_f32_16x16x32_bf16 v[180:183], a[108:111], v[104:107], v[180:183]\n\tds_read_b128 v[92:95], v1 offset:21760\n\tv_mfma_f32_16x16x32_bf16 v[184:187], a[108:111], v[108:111], v[184:187]\n\tds_read_b128 v[96:99], v1 offset:22272\n\tv_mfma_f32_16x16x32_bf16 v[188:191], a[108:111], v[112:115], v[188:191]\n\tds_read_b64 a[248:249], v0 offset:512\n\tv_mfma_f32_16x16x32_bf16 v[192:195], a[108:111], v[116:119], v[192:195]\n\tds_read_b64 a[250:251], v0 offset:520\n\t" \
    "v_mfma_f32_16x16x32_bf16 v[180:183], a[112:115], v[100:103], v[180:183]\n\tds_read_b64 a[252:253], v0 offset:0\n\tv_mfma_f32_16x16x32_bf16 v[184:187], a[112:115], v[104:107], v[184:187]\n\tds_read_b64 a[254:255], v0 offset:8\n\tv_mfma_f32_16x16x32_bf16 v[188:191], a[112:115], v[108:111], v[188:191]\n\tv_mfma_f32_16x16x32_bf16 v[192:195], a[112:115], v[112:115], v[192:195]\n\tv_mfma_f32_16x16x32_bf16 v[184:187], a[116:119], v[100:103], v[184:187]\n\tv_mfma_f32_16x16x32_bf16 v[188:191], a[116:119], v[104:107], v[188:191]\n\t" \
    "v_mfma_f32_16x16x32_bf16 v[192:195], a[116:119], v[108:111], v[192:195]\n\tv_mfma_f32_16x16x32_bf16 v[188:191], a[120:123], v[100:103], v[188:191]\n\tv_mfma_f32_16x16x32_bf16 v[192:195], a[120:123], v[104:107], v[192:195]\n\tv_mfma_f32_16x16x32_bf16 v[192:195], a[124:127], v[100:103], v[192:195]\n\ts_waitcnt lgkmcnt(4)\n\tv_mfma_f32_16x16x32_bf16 v[164:167], a[128:131], v[68:71], v[164:167]\n\tds_read_b128 v[100:103], v1 offset:18624\n\ts_waitcnt vmcnt(10)\n\t" \
    "v_alignbit_b32 v196, v19, v19, 16\n\tv_mfma_f32_16x16x32_bf16 v[168:171], a[128:131], v[72:75], v[168:171]\n\tds_read_b128 v[104:107], v1 offset:19136\n\tv_alignbit_b32 v197, v18, v18, 16\n\tv_mfma_f32_16x16x32_bf16 v[172:175], a[128:131], v[76:79], v[172:175]\n\tds_read_b128 v[108:111], v1 offset:19648\n\tv_alignbit_b32 v198, v17, v17, 16\n\tv_mfma_f32_16x16x32_bf16 v[176:179], a[128:131], v[80:83], v[176:179]\n\t" \
    "ds_read_b128 v[112:115], v1 offset:20160\n\tv_alignbit_b32 v199, v16, v16, 16\n\tv_mfma_f32_16x16x32_bf16 v[180:183], a[128:131], v[84:87], v[180:183]\n\tds_write_b128 v2, v[196:199] offset:3072\n\tv_mfma_f32_16x16x32_bf16 v[184:187], a[128:131], v[88:91], v[184:187]\n\tds_read_b128 v[116:119], v1 offset:20672\n\tv_bfi_b32 v200, s46, v19, v18\n\tv_mfma_f32_16x16x32_bf16 v[188:191], a[128:131], v[92:95], v[188:191]\n\t" \
    "ds_read_b128 v[120:123], v1 offset:21184\n\tv_bfi_b32 v201, s46, v18, v17\n\tv_mfma_f32_16x16x32_bf16 v[192:195], a[128:131], v[96:99], v[192:195]\n\tds_read_b128 v[124:127], v1 offset:21696\n\tv_bfi_b32 v202, s46, v17, v16\n\tv_mfma_f32_16x16x32_bf16 v[168:171], a[132:135], v[68:71], v[168:171]\n\tds_read_b128 v[128:131], v1 offset:22208\n\tv_bfi_b32 v203, s46, v16, v33\n\t" \
    "v_mfma_f32_16x16x32_bf16 v[172:175], a[132:135], v[72:75], v[172:175]\n\tds_write_b128 v2, v[200:203] offset:7232\n\tv_mfma_f32_16x16x32_bf16 v[176:179], a[132:135], v[76:79], v[176:179]\n\tv_alignbit_b32 v196, v18, v18, 16\n\tv_alignbit_b32 v197, v17, v17, 16\n\tv_mfma_f32_16x16x32_bf16 v[180:183], a[132:135], v[80:83], v[180:183]\n\tv_alignbit_b32 v198, v16, v16, 16\n\tv_alignbit_b32 v199, v33, v33, 16\n\t" \
    "v_mfma_f32_16x16x32_bf16 v[184:187], a[132:135], v[84:87], v[184:187]\n\tds_write_b128 v2, v[196:199] offset:11392\n\tv_mfma_f32_16x16x32_bf16 v[188:191], a[132:135], v[88:91], v[188:191]\n\tv_bfi_b32 v200, s46, v18, v17\n\tv_bfi_b32 v201, s46, v17, v16\n\tv_mfma_f32_16x16x32_bf16 v[192:195], a[132:135], v[92:95], v[192:195]\n\tv_bfi_b32 v202, s46, v16, v33\n\tv_bfi_b32 v203, s46, v33, v32\n\t" \
    "v_mfma_f32_16x16x32_bf16 v[172:175], a[136:139], v[68:71], v[172:175]\n\tds_write_b128 v2, v[200:203] offset:15552\n\tv_mfma_f32_16x16x32_bf16 v[176:179], a[136:139], v[72:75], v[176:179]\n\tv_alignbit_b32 v196, v23, v23, 16\n\tv_alignbit_b32 v197, v22, v22, 16\n\tv_mfma_f32_16x16x32_bf16 v[180:183], a[136:139], v[76:79], v[180:183]\n\tv_alignbit_b32 v198, v21, v21, 16\n\tv_alignbit_b32 v199, v20, v20, 16\n\t" \
    "v_mfma_f32_16x16x32_bf16 v[184:187], a[136:139], v[80:83], v[184:187]\n\tds_write_b128 v2, v[196:199] offset:2048\n\tv_mfma_f32_16x16x32_bf16 v[188:191], a[136:139], v[84:87], v[188:191]\n\tv_bfi_b32 v200, s46, v23, v22\n\tv_bfi_b32 v201, s46, v22, v21\n\tv_mfma_f32_16x16x32_bf16 v[192:195], a[136:139], v[88:91], v[192:195]\n\tv_bfi_b32 v202, s46, v21, v20\n\tv_bfi_b32 v203, s46, v20, v35\n\t" \
    "v_mfma_f32_16x16x32_bf16 v[176:179], a[140:143], v[68:71], v[176:179]\n\tds_write_b128 v2, v[200:203] offset:6208\n\tv_mfma_f32_16x16x32_bf16 v[180:183], a[140:143], v[72:75], v[180:183]\n\tv_alignbit_b32 v196, v22, v22, 16\n\tv_alignbit_b32 v197, v21, v21, 16\n\tv_mfma_f32_16x16x32_bf16 v[184:187], a[140:143], v[76:79], v[184:187]\n\tv_alignbit_b32 v198, v20, v20, 16\n\tv_alignbit_b32 v199, v35, v35, 16\n\t" \
    "v_mfma_f32_16x16x32_bf16 v[188:191], a[140:143], v[80:83], v[188:191]\n\tds_write_b128 v2, v[196:199] offset:10368\n\tv_mfma_f32_16x16x32_bf16 v[192:195], a[140:143], v[84:87], v[192:195]\n\tv_bfi_b32 v200, s46, v22, v21\n\tv_bfi_b32 v201, s46, v21, v20\n\tv_mfma_f32_16x16x32_bf16 v[180:183], a[144:147], v[68:71], v[180:183]\n\tv_bfi_b32 v202, s46, v20, v35\n\tv_bfi_b32 v203, s46, v35, v34\n\t" \
    "v_mfma_f32_16x16x32_bf16 v[184:187], a[144:147], v[72:75], v[184:187]\n\tds_write_b128 v2, v[200:203] offset:14528\n\tv_mfma_f32_16x16x32_bf16 v[188:191], a[144:147], v[76:79], v[188:191]\n\tv_alignbit_b32 v196, v27, v27, 16\n\tv_alignbit_b32 v197, v26, v26, 16\n\tv_mfma_f32_16x16x32_bf16 v[192:195], a[144:147], v[80:83], v[192:195]\n\tv_alignbit_b32 v198, v25, v25, 16\n\tv_alignbit_b32 v199, v24, v24, 16\n\t" \
    "v_mfma_f32_16x16x32_bf16 v[184:187], a[148:151], v[68:71], v[184:187]\n\tds_write_b128 v2, v[196:199] offset:1024\n\tv_mfma_f32_16x16x32_bf16 v[188:191], a[148:151], v[72:75], v[188:191]\n\tv_bfi_b32 v200, s46, v27, v26\n\tv_bfi_b32 v201, s46, v26, v25\n\tv_mfma_f32_16x16x32_bf16 v[192:195], a[148:151], v[76:79], v[192:195]\n\tv_bfi_b32 v202, s46, v25, v24\n\tv_bfi_b32 v203, s46, v24, v37\n\t" \
    "v_mfma_f32_16x16x32_bf16 v[188:191], a[152:155], v[68:71], v[188:191]\n\tds_write_b128 v2, v[200:203] offset:5184\n\tv_mfma_f32_16x16x32_bf16 v[192:195], a[152:155], v[72:75], v[192:195]\n\tv_alignbit_b32 v196, v26, v26, 16\n\tv_alignbit_b32 v197, v25, v25, 16\n\tv_mfma_f32_16x16x32_bf16 v[192:195], a[156:159], v[68:71], v[192:195]\n\tv_alignbit_b32 v198, v24, v24, 16\n\tv_alignbit_b32 v199, v37, v37, 16\n\t" \
    "s_waitcnt lgkmcnt(9)\n\tv_mfma_f32_16x16x32_bf16 v[164:167], a[160:163], v[100:103], v[164:167]\n\tds_write_b128 v2, v[196:199] offset:9344\n\tv_mfma_f32_16x16x32_bf16 v[168:171], a[160:163], v[104:107], v[168:171]\n\tds_read_b128 v[68:71], v1 offset:18560\n\tv_bfi_b32 v200, s46, v26, v25\n\tv_mfma_f32_16x16x32_bf16 v[172:175], a[160:163], v[108:111], v[172:175]\n\tds_read_b128 v[72:75], v1 offset:19072\n\t" \
    "v_bfi_b32 v201, s46, v25, v24\n\tv_mfma_f32_16x16x32_bf16 v[176:179], a[160:163], v[112:115], v[176:179]\n\tds_read_b128 v[76:79], v1 offset:19584\n\tv_bfi_b32 v202, s46, v24, v37\n\tv_mfma_f32_16x16x32_bf16 v[180:183], a[160:163], v[116:119], v[180:183]\n\tds_read_b128 v[80:83], v1 offset:20096\n\tv_bfi_b32 v203, s46, v37, v36\n\tv_mfma_f32_16x16x32_bf16 v[184:187], a[160:163], v[120:123], v[184:187]\n\t" \
    "ds_write_b128 v2, v[200:203] offset:13504\n\tv_mfma_f32_16x16x32_bf16 v[188:191], a[160:163], v[124:127], v[188:191]\n\tds_read_b128 v[84:87], v1 offset:20608\n\tv_alignbit_b32 v196, v31, v31, 16\n\tv_mfma_f32_16x16x32_bf16 v[192:195], a[160:163], v[128:131], v[192:195]\n\tbuffer_load_dword v15, v7, s[16:19], s42 offen\n\tds_read_b128 v[88:91], v1 offset:21120\n\tv_mfma_f32_16x16x32_bf16 v[168:171], a[164:167], v[100:103], v[168:171]\n\t" \
    "s_add_u32 s43, s43, 1\n\tds_read_b128 v[92:95], v1 offset:21632\n\tv_alignbit_b32 v197, v30, v30, 16\n\tv_mfma_f32_16x16x32_bf16 v[172:175], a[164:167], v[104:107], v[172:175]\n\tv_readlane_b32 s42, v237, s43\n\ts_nop 4\n\tds_read_b128 v[96:99], v1 offset:22144\n\tv_alignbit_b32 v198, v29, v29, 16\n\t" \
    "v_mfma_f32_16x16x32_bf16 v[176:179], a[164:167], v[108:111], v[176:179]\n\tv_alignbit_b32 v199, v28, v28, 16\n\tv_mfma_f32_16x16x32_bf16 v[180:183], a[164:167], v[112:115], v[180:183]\n\tds_write_b128 v2, v[196:199] offset:0\n\tv_mfma_f32_16x16x32_bf16 v[184:187], a[164:167], v[116:119], v[184:187]\n\tv_bfi_b32 v200, s46, v31, v30\n\tv_bfi_b32 v201, s46, v30, v29\n\tv_mfma_f32_16x16x32_bf16 v[188:191], a[164:167], v[120:123], v[188:191]\n\t" \
    "v_bfi_b32 v202, s46, v29, v28\n\tv_bfi_b32 v203, s46, v28, v39\n\tv_mfma_f32_16x16x32_bf16 v[192:195], a[164:167], v[124:127], v[192:195]\n\tds_write_b128 v2, v[200:203] offset:4160\n\tv_mfma_f32_16x16x32_bf16 v[172:175], a[168:171], v[100:103], v[172:175]\n\tv_alignbit_b32 v196, v30, v30, 16\n\tv_alignbit_b32 v197, v29, v29, 16\n\tv_mfma_f32_16x16x32_bf16 v[176:179], a[168:171], v[104:107], v[176:179]\n\t" \
    "v_alignbit_b32 v198, v28, v28, 16\n\tv_alignbit_b32 v199, v39, v39, 16\n\tv_mfma_f32_16x16x32_bf16 v[180:183], a[168:171], v[108:111], v[180:183]\n\tds_write_b128 v2, v[196:199] offset:8320\n\tv_mfma_f32_16x16x32_bf16 v[184:187], a[168:171], v[112:115], v[184:187]\n\tv_bfi_b32 v200, s46, v30, v29\n\tv_bfi_b32 v201, s46, v29, v28\n\tv_mfma_f32_16x16x32_bf16 v[188:191], a[168:171], v[116:119], v[188:191]\n\t" \
    "v_bfi_b32 v202, s46, v28, v39\n\tv_bfi_b32 v203, s46, v39, v38\n\tv_mfma_f32_16x16x32_bf16 v[192:195], a[168:171], v[120:123], v[192:195]\n\tds_write_b128 v2, v[200:203] offset:12480\n\tv_mfma_f32_16x16x32_bf16 v[176:179], a[172:175], v[100:103], v[176:179]\n\tv_lshl_or_b32 v40, v40, 16, v40\n\tv_and_b32 v228, v40, v232\n\tv_mfma_f32_16x16x32_bf16 v[180:183], a[172:175], v[104:107], v[180:183]\n\t" \
    "v_and_b32 v229, v40, v233\n\tv_and_b32 v230, v40, v234\n\tv_mfma_f32_16x16x32_bf16 v[184:187], a[172:175], v[108:111], v[184:187]\n\tv_and_b32 v231, v40, v235\n\tv_mfma_f32_16x16x32_bf16 v[188:191], a[172:175], v[112:115], v[188:191]\n\tv_mfma_f32_16x16x32_bf16 v[192:195], a[172:175], v[116:119], v[192:195]\n\tv_mfma_f32_16x16x32_bf16 v[180:183], a[176:179], v[100:103], v[180:183]\n\tv_mfma_f32_16x16x32_bf16 v[184:187], a[176:179], v[104:107], v[184:187]\n\t" \
    "v_mfma_f32_16x16x32_bf16 v[188:191], a[176:179], v[108:111], v[188:191]\n\tv_mfma_f32_16x16x32_bf16 v[192:195], a[176:179], v[112:115], v[192:195]\n\tv_mfma_f32_16x16x32_bf16 v[184:187], a[180:183], v[100:103], v[184:187]\n\tv_mfma_f32_16x16x32_bf16 v[188:191], a[180:183], v[104:107], v[188:191]\n\tv_mfma_f32_16x16x32_bf16 v[192:195], a[180:183], v[108:111], v[192:195]\n\tv_mfma_f32_16x16x32_bf16 v[188:191], a[184:187], v[100:103], v[188:191]\n\tv_mfma_f32_16x16x32_bf16 v[192:195], a[184:187], v[104:107], v[192:195]\n\tv_mfma_f32_16x16x32_bf16 v[192:195], a[188:191], v[100:103], v[192:195]\n\t" \
    "s_waitcnt lgkmcnt(4)\n\tv_mfma_f32_16x16x32_bf16 v[164:167], a[192:195], v[68:71], v[164:167]\n\ts_add_u32 s68, s44, s38\n\tds_read_b128 v[100:103], v1 offset:18496\n\tv_mfma_f32_16x16x32_bf16 v[168:171], a[192:195], v[72:75], v[168:171]\n\ts_add_u32 s68, s68, 12\n\tds_read_b128 v[104:107], v1 offset:19008\n\tv_mfma_f32_16x16x32_bf16 v[172:175], a[192:195], v[76:79], v[172:175]\n\t" \
    "s_lshl_b32 s66, s68, 1\n\tds_read_b128 v[108:111], v1 offset:19520\n\tv_mfma_f32_16x16x32_bf16 v[176:179], a[192:195], v[80:83], v[176:179]\n\ts_lshl_b32 s68, s68, 12\n\tds_read_b128 v[112:115], v1 offset:20032\n\tv_mfma_f32_16x16x32_bf16 v[180:183], a[192:195], v[84:87], v[180:183]\n\ts_or_b32 s68, s68, s45\n\tds_read_b128 v[116:119], v1 offset:20544\n\t" \
    "v_mfma_f32_16x16x32_bf16 v[184:187], a[192:195], v[88:91], v[184:187]\n\ts_or_b32 s66, s66, s45\n\tbuffer_load_dwordx4 v[42:45], v8, s[76:79], s68 offen offset:0\n\tds_read_b128 v[120:123], v1 offset:21056\n\tv_mfma_f32_16x16x32_bf16 v[188:191], a[192:195], v[92:95], v[188:191]\n\tds_read_b128 v[124:127], v1 offset:21568\n\tv_mfma_f32_16x16x32_bf16 v[192:195], a[192:195], v[96:99], v[192:195]\n\tds_read_b128 v[128:131], v1 offset:22080\n\t" \
    "v_mfma_f32_16x16x32_bf16 v[168:171], a[196:199], v[68:71], v[168:171]\n\tv_mfma_f32_16x16x32_bf16 v[172:175], a[196:199], v[72:75], v[172:175]\n\tv_mfma_f32_16x16x32_bf16 v[176:179], a[196:199], v[76:79], v[176:179]\n\tv_mfma_f32_16x16x32_bf16 v[180:183], a[196:199], v[80:83], v[180:183]\n\tbuffer_load_dwordx4 v[46:49], v8, s[76:79], s68 offen offset:1024\n\tv_mfma_f32_16x16x32_bf16 v[184:187], a[196:199], v[84:87], v[184:187]\n\tv_mfma_f32_16x16x32_bf16 v[188:191], a[196:199], v[88:91], v[188:191]\n\tv_mfma_f32_16x16x32_bf16 v[192:195], a[196:199], v[92:95], v[192:195]\n\t" \
    "v_mfma_f32_16x16x32_bf16 v[172:175], a[200:203], v[68:71], v[172:175]\n\tv_mfma_f32_16x16x32_bf16 v[176:179], a[200:203], v[72:75], v[176:179]\n\tv_mfma_f32_16x16x32_bf16 v[180:183], a[200:203], v[76:79], v[180:183]\n\tbuffer_load_dwordx4 v[50:53], v8, s[76:79], s68 offen offset:2048\n\tv_mfma_f32_16x16x32_bf16 v[184:187], a[200:203], v[80:83], v[184:187]\n\tv_mfma_f32_16x16x32_bf16 v[188:191], a[200:203], v[84:87], v[188:191]\n\tv_mfma_f32_16x16x32_bf16 v[192:195], a[200:203], v[88:91], v[192:195]\n\tv_mfma_f32_16x16x32_bf16 v[176:179], a[204:207], v[68:71], v[176:179]\n\t" \
    "v_mfma_f32_16x16x32_bf16 v[180:183], a[204:207], v[72:75], v[180:183]\n\tv_mfma_f32_16x16x32_bf16 v[184:187], a[204:207], v[76:79], v[184:187]\n\tbuffer_load_dwordx4 v[54:57], v8, s[76:79], s68 offen offset:3072\n\tv_mfma_f32_16x16x32_bf16 v[188:191], a[204:207], v[80:83], v[188:191]\n\tv_mfma_f32_16x16x32_bf16 v[192:195], a[204:207], v[84:87], v[192:195]\n\tv_mfma_f32_16x16x32_bf16 v[180:183], a[208:211], v[68:71], v[180:183]\n\tv_mfma_f32_16x16x32_bf16 v[184:187], a[208:211], v[72:75], v[184:187]\n\tv_mfma_f32_16x16x32_bf16 v[188:191], a[208:211], v[76:79], v[188:191]\n\t" \
    "v_mfma_f32_16x16x32_bf16 v[192:195], a[208:211], v[80:83], v[192:195]\n\tbuffer_load_dwordx2 v[58:59], v13, s[76:79], s68 offen\n\tv_mfma_f32_16x16x32_bf16 v[184:187], a[212:215], v[68:71], v[184:187]\n\tv_mfma_f32_16x16x32_bf16 v[188:191], a[212:215], v[72:75], v[188:191]\n\tv_mfma_f32_16x16x32_bf16 v[192:195], a[212:215], v[76:79], v[192:195]\n\tv_mfma_f32_16x16x32_bf16 v[188:191], a[216:219], v[68:71], v[188:191]\n\tv_mfma_f32_16x16x32_bf16 v[192:195], a[216:219], v[72:75], v[192:195]\n\tv_mfma_f32_16x16x32_bf16 v[192:195], a[220:223], v[68:71], v[192:195]\n\t" \
    "buffer_load_dwordx2 v[60:61], v8, s[76:79], s68 offen offset:1016\n\ts_waitcnt lgkmcnt(0)\n\tv_mfma_f32_16x16x32_bf16 v[164:167], a[224:227], v[100:103], v[164:167]\n\tds_read_b64 a[0:1], v0 offset:4032\n\tv_mfma_f32_16x16x32_bf16 v[168:171], a[224:227], v[104:107], v[168:171]\n\tds_read_b64 a[2:3], v0 offset:4040\n\tv_mfma_f32_16x16x32_bf16 v[172:175], a[224:227], v[108:111], v[172:175]\n\tds_read_b64 a[4:5], v0 offset:3520\n\t" \
    "v_mfma_f32_16x16x32_bf16 v[176:179], a[224:227], v[112:115], v[176:179]\n\tds_read_b64 a[6:7], v0 offset:3528\n\tv_mfma_f32_16x16x32_bf16 v[180:183], a[224:227], v[116:119], v[180:183]\n\tds_read_b64 a[8:9], v0 offset:3008\n\tv_mfma_f32_16x16x32_bf16 v[184:187], a[224:227], v[120:123], v[184:187]\n\tbuffer_load_dwordx2 v[62:63], v8, s[76:79], s68 offen offset:2040\n\tds_read_b64 a[10:11], v0 offset:3016\n\tv_mfma_f32_16x16x32_bf16 v[188:191], a[224:227], v[124:127], v[188:191]\n\t" \
    "ds_read_b64 a[12:13], v0 offset:2496\n\tv_mfma_f32_16x16x32_bf16 v[192:195], a[224:227], v[128:131], v[192:195]\n\tds_read_b64 a[14:15], v0 offset:2504\n\tv_mfma_f32_16x16x32_bf16 v[168:171], a[228:231], v[100:103], v[168:171]\n\tds_read_b64 a[16:17], v0 offset:1984\n\tv_mfma_f32_16x16x32_bf16 v[172:175], a[228:231], v[104:107], v[172:175]\n\tds_read_b64 a[18:19], v0 offset:1992\n\tv_mfma_f32_16x16x32_bf16 v[176:179], a[228:231], v[108:111], v[176:179]\n\t" \
    "ds_read_b64 a[20:21], v0 offset:1472\n\tv_mfma_f32_16x16x32_bf16 v[180:183], a[228:231], v[112:115], v[180:183]\n\tbuffer_load_dwordx2 v[64:65], v8, s[76:79], s68 offen offset:3064\n\tds_read_b64 a[22:23], v0 offset:1480\n\tv_mfma_f32_16x16x32_bf16 v[184:187], a[228:231], v[116:119], v[184:187]\n\tds_read_b64 a[24:25], v0 offset:960\n\tv_mfma_f32_16x16x32_bf16 v[188:191], a[228:231], v[120:123], v[188:191]\n\tds_read_b64 a[26:27], v0 offset:968\n\t" \
    "v_mfma_f32_16x16x32_bf16 v[192:195], a[228:231], v[124:127], v[192:195]\n\tds_read_b64 a[28:29], v0 offset:448\n\tv_mfma_f32_16x16x32_bf16 v[172:175], a[232:235], v[100:103], v[172:175]\n\tds_read_b64 a[30:31], v0 offset:456\n\tv_mfma_f32_16x16x32_bf16 v[176:179], a[232:235], v[104:107], v[176:179]\n\tds_read_b128 v[68:71], v1 offset:37440\n\tv_mfma_f32_16x16x32_bf16 v[180:183], a[232:235], v[108:111], v[180:183]\n\tbuffer_load_ushort v66, off, s[80:83], s66\n\t" \
    "ds_read_b128 v[72:75], v1 offset:37952\n\tv_mfma_f32_16x16x32_bf16 v[184:187], a[232:235], v[112:115], v[184:187]\n\tds_read_b128 v[76:79], v1 offset:38464\n\tv_mfma_f32_16x16x32_bf16 v[188:191], a[232:235], v[116:119], v[188:191]\n\tds_read_b128 v[80:83], v1 offset:38976\n\tv_mfma_f32_16x16x32_bf16 v[192:195], a[232:235], v[120:123], v[192:195]\n\tds_read_b128 v[84:87], v1 offset:39488\n\tv_mfma_f32_16x16x32_bf16 v[176:179], a[236:239], v[100:103], v[176:179]\n\t" \
    "ds_read_b128 v[88:91], v1 offset:40000\n\tv_mfma_f32_16x16x32_bf16 v[180:183], a[236:239], v[104:107], v[180:183]\n\tds_read_b128 v[92:95], v1 offset:40512\n\tv_mfma_f32_16x16x32_bf16 v[184:187], a[236:239], v[108:111], v[184:187]\n\tbuffer_load_dword v15, v7, s[16:19], s42 offen\n\tds_read_b128 v[96:99], v1 offset:41024\n\tv_mfma_f32_16x16x32_bf16 v[188:191], a[236:239], v[112:115], v[188:191]\n\ts_add_u32 s43, s43, 1\n\t" \
    "v_mfma_f32_16x16x32_bf16 v[192:195], a[236:239], v[116:119], v[192:195]\n\tv_readlane_b32 s42, v237, s43\n\ts_nop 4\n\ts_waitcnt vmcnt(12)\n\tbuffer_store_dwordx2 v[248:249], off, s[8:11], s51 sc1\n\tv_mfma_f32_16x16x32_bf16 v[180:183], a[240:243], v[100:103], v[180:183]\n\tv_mfma_f32_16x16x32_bf16 v[184:187], a[240:243], v[104:107], v[184:187]\n\tv_mfma_f32_16x16x32_bf16 v[188:191], a[240:243], v[108:111], v[188:191]\n\t" \
    "v_mfma_f32_16x16x32_bf16 v[192:195], a[240:243], v[112:115], v[192:195]\n\tv_mfma_f32_16x16x32_bf16 v[184:187], a[244:247], v[100:103], v[184:187]\n\tv_mfma_f32_16x16x32_bf16 v[188:191], a[244:247], v[104:107], v[188:191]\n\tv_mfma_f32_16x16x32_bf16 v[192:195], a[244:247], v[108:111], v[192:195]\n\tv_mfma_f32_16x16x32_bf16 v[188:191], a[248:251], v[100:103], v[188:191]\n\tv_mfma_f32_16x16x32_bf16 v[192:195], a[248:251], v[104:107], v[192:195]\n\tv_mfma_f32_16x16x32_bf16 v[192:195], a[252:255], v[100:103], v[192:195]\n\ts_waitcnt lgkmcnt(0)\n\t" \
    "v_mfma_f32_16x16x32_bf16 v[132:135], v[228:231], v[68:71], 0\n\tds_read_b64 a[32:33], v0 offset:3968\n\tds_read_b64 a[34:35], v0 offset:3976\n\tv_mfma_f32_16x16x32_bf16 v[136:139], v[228:231], v[72:75], 0\n\tds_read_b64 a[36:37], v0 offset:3456\n\tds_read_b64 a[38:39], v0 offset:3464\n\tv_mfma_f32_16x16x32_bf16 v[140:143], v[228:231], v[76:79], 0\n\tds_read_b64 a[40:41], v0 offset:2944\n\t" \
    "ds_read_b64 a[42:43], v0 offset:2952\n\tv_mfma_f32_16x16x32_bf16 v[144:147], v[228:231], v[80:83], 0\n\tds_read_b64 a[44:45], v0 offset:2432\n\tds_read_b64 a[46:47], v0 offset:2440\n\tv_mfma_f32_16x16x32_bf16 v[148:151], v[228:231], v[84:87], 0\n\tds_read_b64 a[48:49], v0 offset:1920\n\tds_read_b64 a[50:51], v0 offset:1928\n\tv_mfma_f32_16x16x32_bf16 v[152:155], v[228:231], v[88:91], 0\n\t" \
    "ds_read_b64 a[52:53], v0 offset:1408\n\tv_cvt_pk_bf16_f32 v164, v164, v165\n\tv_mfma_f32_16x16x32_bf16 v[156:159], v[228:231], v[92:95], 0\n\tds_read_b64 a[54:55], v0 offset:1416\n\tv_cvt_pk_bf16_f32 v165, v166, v167\n\tv_mfma_f32_16x16x32_bf16 v[160:163], v[228:231], v[96:99], 0\n\tds_read_b64 a[56:57], v0 offset:896\n\tds_write_b64 v3, v[164:165] offset:18496\n\t" \
    "v_mfma_f32_16x16x32_bf16 v[132:135], a[0:3], v[68:71], v[132:135]\n\tds_read_b64 a[58:59], v0 offset:904\n\tv_cvt_pk_bf16_f32 v168, v168, v169\n\tv_mfma_f32_16x16x32_bf16 v[136:139], a[0:3], v[72:75], v[136:139]\n\tds_read_b64 a[60:61], v0 offset:384\n\tv_cvt_pk_bf16_f32 v169, v170, v171\n\tv_mfma_f32_16x16x32_bf16 v[140:143], a[0:3], v[76:79], v[140:143]\n\tds_read_b64 a[62:63], v0 offset:392\n\t" \
    "ds_write_b64 v3, v[168:169] offset:19008\n\tv_mfma_f32_16x16x32_bf16 v[144:147], a[0:3], v[80:83], v[144:147]\n\tds_read_b128 v[100:103], v1 offset:37376\n\tv_cvt_pk_bf16_f32 v172, v172, v173\n\tv_mfma_f32_16x16x32_bf16 v[148:151], a[0:3], v[84:87], v[148:151]\n\tds_read_b128 v[104:107], v1 offset:37888\n\tv_cvt_pk_bf16_f32 v173, v174, v175\n\tv_mfma_f32_16x16x32_bf16 v[152:155], a[0:3], v[88:91], v[152:155]\n\t" \
    "ds_read_b128 v[108:111], v1 offset:38400\n\tds_write_b64 v3, v[172:173] offset:19520\n\tv_mfma_f32_16x16x32_bf16 v[156:159], a[0:3], v[92:95], v[156:159]\n\tds_read_b128 v[112:115], v1 offset:38912\n\tv_cvt_pk_bf16_f32 v176, v176, v177\n\tv_mfma_f32_16x16x32_bf16 v[160:163], a[0:3], v[96:99], v[160:163]\n\tds_read_b128 v[116:119], v1 offset:39424\n\tv_cvt_pk_bf16_f32 v177, v178, v179\n\t" \
    "v_mfma_f32_16x16x32_bf16 v[136:139], a[4:7], v[68:71], v[136:139]\n\tds_read_b128 v[120:123], v1 offset:39936\n\tds_write_b64 v3, v[176:177] offset:20032\n\tv_mfma_f32_16x16x32_bf16 v[140:143], a[4:7], v[72:75], v[140:143]\n\tbuffer_load_dword v15, v7, s[16:19], s42 offen\n\tds_read_b128 v[124:127], v1 offset:40448\n\tv_mfma_f32_16x16x32_bf16 v[144:147], a[4:7], v[76:79], v[144:147]\n\ts_add_u32 s43, s43, 1\n\t" \
    "ds_read_b128 v[128:131], v1 offset:40960\n\tv_cvt_pk_bf16_f32 v180, v180, v181\n\tv_mfma_f32_16x16x32_bf16 v[148:151], a[4:7], v[80:83], v[148:151]\n\tv_readlane_b32 s42, v237, s43\n\ts_nop 4\n\tds_read_b64 a[64:65], v0 offset:3904\n\tv_cvt_pk_bf16_f32 v181, v182, v183\n\tv_mfma_f32_16x16x32_bf16 v[152:155], a[4:7], v[84:87], v[152:155]\n\t" \
    "ds_read_b64 a[66:67], v0 offset:3912\n\tds_write_b64 v3, v[180:181] offset:20544\n\tv_mfma_f32_16x16x32_bf16 v[156:159], a[4:7], v[88:91], v[156:159]\n\tds_read_b64 a[68:69], v0 offset:3392\n\tv_cvt_pk_bf16_f32 v184, v184, v185\n\tv_mfma_f32_16x16x32_bf16 v[160:163], a[4:7], v[92:95], v[160:163]\n\tds_read_b64 a[70:71], v0 offset:3400\n\tv_cvt_pk_bf16_f32 v185, v186, v187\n\t" \
    "v_mfma_f32_16x16x32_bf16 v[140:143], a[8:11], v[68:71], v[140:143]\n\tds_read_b64 a[72:73], v0 offset:2880\n\tds_write_b64 v3, v[184:185] offset:21056\n\tv_mfma_f32_16x16x32_bf16 v[144:147], a[8:11], v[72:75], v[144:147]\n\tds_read_b64 a[74:75], v0 offset:2888\n\tv_cvt_pk_bf16_f32 v188, v188, v189\n\tv_mfma_f32_16x16x32_bf16 v[148:151], a[8:11], v[76:79], v[148:151]\n\tds_read_b64 a[76:77], v0 offset:2368\n\t" \
    "v_cvt_pk_bf16_f32 v189, v190, v191\n\tv_mfma_f32_16x16x32_bf16 v[152:155], a[8:11], v[80:83], v[152:155]\n\tds_read_b64 a[78:79], v0 offset:2376\n\tds_write_b64 v3, v[188:189] offset:21568\n\tv_mfma_f32_16x16x32_bf16 v[156:159], a[8:11], v[84:87], v[156:159]\n\tds_read_b64 a[80:81], v0 offset:1856\n\tv_cvt_pk_bf16_f32 v192, v192, v193\n\tv_mfma_f32_16x16x32_bf16 v[160:163], a[8:11], v[88:91], v[160:163]\n\t" \
    "ds_read_b64 a[82:83], v0 offset:1864\n\tv_cvt_pk_bf16_f32 v193, v194, v195\n\tv_mfma_f32_16x16x32_bf16 v[144:147], a[12:15], v[68:71], v[144:147]\n\tds_read_b64 a[84:85], v0 offset:1344\n\tds_write_b64 v3, v[192:193] offset:22080\n\tv_mfma_f32_16x16x32_bf16 v[148:151], a[12:15], v[72:75], v[148:151]\n\tds_read_b64 a[86:87], v0 offset:1352\n\tv_mfma_f32_16x16x32_bf16 v[152:155], a[12:15], v[76:79], v[152:155]\n\t" \
    "ds_read_b64 a[88:89], v0 offset:832\n\tv_mfma_f32_16x16x32_bf16 v[156:159], a[12:15], v[80:83], v[156:159]\n\tds_read_b64 a[90:91], v0 offset:840\n\tv_mfma_f32_16x16x32_bf16 v[160:163], a[12:15], v[84:87], v[160:163]\n\tds_read_b64 a[92:93], v0 offset:320\n\tv_mfma_f32_16x16x32_bf16 v[148:151], a[16:19], v[68:71], v[148:151]\n\tds_read_b64 a[94:95], v0 offset:328\n\tv_mfma_f32_16x16x32_bf16 v[152:155], a[16:19], v[72:75], v[152:155]\n\t" \
    "ds_read_b64 a[96:97], v0 offset:3840\n\tv_mfma_f32_16x16x32_bf16 v[156:159], a[16:19], v[76:79], v[156:159]\n\tds_read_b64 a[98:99], v0 offset:3848\n\tv_mfma_f32_16x16x32_bf16 v[160:163], a[16:19], v[80:83], v[160:163]\n\tds_read_b64 a[100:101], v0 offset:3328\n\tv_mfma_f32_16x16x32_bf16 v[152:155], a[20:23], v[68:71], v[152:155]\n\tds_read_b64 a[102:103], v0 offset:3336\n\tv_mfma_f32_16x16x32_bf16 v[156:159], a[20:23], v[72:75], v[156:159]\n\t" \
    "v_mfma_f32_16x16x32_bf16 v[160:163], a[20:23], v[76:79], v[160:163]\n\tv_mfma_f32_16x16x32_bf16 v[156:159], a[24:27], v[68:71], v[156:159]\n\tv_mfma_f32_16x16x32_bf16 v[160:163], a[24:27], v[72:75], v[160:163]\n\tv_mfma_f32_16x16x32_bf16 v[160:163], a[28:31], v[68:71], v[160:163]\n\ts_waitcnt lgkmcnt(9)\n\ts_barrier\n\tv_mfma_f32_16x16x32_bf16 v[132:135], a[32:35], v[100:103], v[132:135]\n\tds_read_b64 a[104:105], v0 offset:2816\n\t" \
    "ds_read_b64 a[106:107], v0 offset:2824\n\tv_mfma_f32_16x16x32_bf16 v[136:139], a[32:35], v[104:107], v[136:139]\n\tds_read_b64 a[108:109], v0 offset:2304\n\tds_read_b64 a[110:111], v0 offset:2312\n\tv_mfma_f32_16x16x32_bf16 v[140:143], a[32:35], v[108:111], v[140:143]\n\tds_read_b64 a[112:113], v0 offset:1792\n\tds_read_b64 a[114:115], v0 offset:1800\n\tv_mfma_f32_16x16x32_bf16 v[144:147], a[32:35], v[112:115], v[144:147]\n\t" \
    "ds_read_b64 a[116:117], v0 offset:1280\n\tds_read_b64 a[118:119], v0 offset:1288\n\tv_mfma_f32_16x16x32_bf16 v[148:151], a[32:35], v[116:119], v[148:151]\n\tds_read_b64 a[120:121], v0 offset:768\n\tds_read_b64 a[122:123], v0 offset:776\n\tv_mfma_f32_16x16x32_bf16 v[152:155], a[32:35], v[120:123], v[152:155]\n\tds_read_b64 a[124:125], v0 offset:256\n\ts_add_u32 s51, s44, 4\n\t" \
    "v_mfma_f32_16x16x32_bf16 v[156:159], a[32:35], v[124:127], v[156:159]\n\tds_read_b64 a[126:127], v0 offset:264\n\ts_mul_i32 s65, s51, 1366\n\tv_mfma_f32_16x16x32_bf16 v[160:163], a[32:35], v[128:131], v[160:163]\n\tds_read_b64 a[128:129], v0 offset:3776\n\ts_lshr_b32 s65, s65, 16\n\tv_mfma_f32_16x16x32_bf16 v[136:139], a[36:39], v[100:103], v[136:139]\n\tds_read_b64 a[130:131], v0 offset:3784\n\t" \
    "s_mul_i32 s56, s65, 48\n\tv_mfma_f32_16x16x32_bf16 v[140:143], a[36:39], v[104:107], v[140:143]\n\tds_read_b64 a[132:133], v0 offset:3264\n\ts_sub_u32 s56, s51, s56\n\tv_mfma_f32_16x16x32_bf16 v[144:147], a[36:39], v[108:111], v[144:147]\n\tds_read_b64 a[134:135], v0 offset:3272\n\ts_lshl_b32 s56, s56, 6\n\tv_mfma_f32_16x16x32_bf16 v[148:151], a[36:39], v[112:115], v[148:151]\n\t" \
    "ds_read_b128 v[68:71], v1 offset:37312\n\ts_lshr_b32 s70, s65, 2\n\tv_mfma_f32_16x16x32_bf16 v[152:155], a[36:39], v[116:119], v[152:155]\n\tds_read_b128 v[72:75], v1 offset:37824\n\ts_mul_i32 s70, s70, 252\n\tv_mfma_f32_16x16x32_bf16 v[156:159], a[36:39], v[120:123], v[156:159]\n\tds_read_b128 v[76:79], v1 offset:38336\n\ts_add_u32 s65, s65, s70\n\t" \
    "v_mfma_f32_16x16x32_bf16 v[160:163], a[36:39], v[124:127], v[160:163]\n\tds_read_b128 v[80:83], v1 offset:38848\n\ts_mul_i32 s65, s65, 3072\n\tv_mfma_f32_16x16x32_bf16 v[140:143], a[40:43], v[100:103], v[140:143]\n\tds_read_b128 v[84:87], v1 offset:39360\n\ts_add_u32 s56, s56, s65\n\tv_mfma_f32_16x16x32_bf16 v[144:147], a[40:43], v[104:107], v[144:147]\n\tds_read_b128 v[88:91], v1 offset:39872\n\t" \
    "s_add_u32 s56, s56, 4849664\n\tv_mfma_f32_16x16x32_bf16 v[148:151], a[40:43], v[108:111], v[148:151]\n\tds_read_b128 v[92:95], v1 offset:40384\n\ts_add_u32 s51, s51, s38\n\tv_mfma_f32_16x16x32_bf16 v[152:155], a[40:43], v[112:115], v[152:155]\n\tds_read_b128 v[96:99], v1 offset:40896\n\ts_lshl_b32 s51, s51, 3\n\tv_mfma_f32_16x16x32_bf16 v[156:159], a[40:43], v[116:119], v[156:159]\n\t" \
    "ds_read_b64 a[136:137], v0 offset:2752\n\ts_add_u32 s51, s51, 50176\n\tds_read_b64_tr_b16 v[204:205], v4 offset:18496\n\tv_mfma_f32_16x16x32_bf16 v[160:163], a[40:43], v[120:123], v[160:163]\n\tds_read_b64 a[138:139], v0 offset:2760\n\tds_read_b64_tr_b16 v[206:207], v4 offset:18504\n\tv_mfma_f32_16x16x32_bf16 v[144:147], a[44:47], v[100:103], v[144:147]\n\tds_read_b64 a[140:141], v0 offset:2240\n\t" \
    "ds_read_b64_tr_b16 v[208:209], v4 offset:18560\n\tv_mfma_f32_16x16x32_bf16 v[148:151], a[44:47], v[104:107], v[148:151]\n\tds_read_b64 a[142:143], v0 offset:2248\n\tds_read_b64_tr_b16 v[210:211], v4 offset:18568\n\tv_mfma_f32_16x16x32_bf16 v[152:155], a[44:47], v[108:111], v[152:155]\n\tds_read_b64 a[144:145], v0 offset:1728\n\tds_read_b64_tr_b16 v[212:213], v4 offset:19008\n\tv_mfma_f32_16x16x32_bf16 v[156:159], a[44:47], v[112:115], v[156:159]\n\t" \
    "ds_read_b64 a[146:147], v0 offset:1736\n\tds_read_b64_tr_b16 v[214:215], v4 offset:19016\n\tv_mfma_f32_16x16x32_bf16 v[160:163], a[44:47], v[116:119], v[160:163]\n\tds_read_b64 a[148:149], v0 offset:1216\n\tds_read_b64_tr_b16 v[216:217], v4 offset:19072\n\tv_mfma_f32_16x16x32_bf16 v[148:151], a[48:51], v[100:103], v[148:151]\n\tds_read_b64 a[150:151], v0 offset:1224\n\tds_read_b64_tr_b16 v[218:219], v4 offset:19080\n\t" \
    "v_mfma_f32_16x16x32_bf16 v[152:155], a[48:51], v[104:107], v[152:155]\n\tds_read_b64 a[152:153], v0 offset:704\n\tv_mfma_f32_16x16x32_bf16 v[156:159], a[48:51], v[108:111], v[156:159]\n\tds_read_b64 a[154:155], v0 offset:712\n\tv_mfma_f32_16x16x32_bf16 v[160:163], a[48:51], v[112:115], v[160:163]\n\tds_read_b64 a[156:157], v0 offset:192\n\tv_mfma_f32_16x16x32_bf16 v[152:155], a[52:55], v[100:103], v[152:155]\n\tds_read_b64 a[158:159], v0 offset:200\n\t" \
    "v_mfma_f32_16x16x32_bf16 v[156:159], a[52:55], v[104:107], v[156:159]\n\tv_mfma_f32_16x16x32_bf16 v[160:163], a[52:55], v[108:111], v[160:163]\n\tbuffer_load_dword v15, v7, s[16:19], s42 offen\n\ts_waitcnt lgkmcnt(15)\n\tbuffer_store_dwordx4 v[204:207], v5, s[8:11], s56 offen sc1\n\tv_mfma_f32_16x16x32_bf16 v[156:159], a[56:59], v[100:103], v[156:159]\n\ts_add_u32 s43, s43, 1\n\ts_add_u32 s56, s56, 49152\n\t" \
    "v_mfma_f32_16x16x32_bf16 v[160:163], a[56:59], v[104:107], v[160:163]\n\tv_readlane_b32 s42, v237, s43\n\ts_nop 4\n\tv_mfma_f32_16x16x32_bf16 v[160:163], a[60:63], v[100:103], v[160:163]\n\tv_mfma_f32_16x16x32_bf16 v[132:135], a[64:67], v[68:71], v[132:135]\n\tds_read_b64 a[160:161], v0 offset:3712\n\tds_read_b64 a[162:163], v0 offset:3720\n\tv_mfma_f32_16x16x32_bf16 v[136:139], a[64:67], v[72:75], v[136:139]\n\t" \
    "ds_read_b64 a[164:165], v0 offset:3200\n\tds_read_b64 a[166:167], v0 offset:3208\n\tv_mfma_f32_16x16x32_bf16 v[140:143], a[64:67], v[76:79], v[140:143]\n\tds_read_b64 a[168:169], v0 offset:2688\n\tds_read_b64 a[170:171], v0 offset:2696\n\tv_mfma_f32_16x16x32_bf16 v[144:147], a[64:67], v[80:83], v[144:147]\n\tds_read_b64 a[172:173], v0 offset:2176\n\tds_read_b64 a[174:175], v0 offset:2184\n\t" \
    "v_mfma_f32_16x16x32_bf16 v[148:151], a[64:67], v[84:87], v[148:151]\n\tds_read_b64 a[176:177], v0 offset:1664\n\tds_read_b64 a[178:179], v0 offset:1672\n\tv_mfma_f32_16x16x32_bf16 v[152:155], a[64:67], v[88:91], v[152:155]\n\tds_read_b64 a[180:181], v0 offset:1152\n\tv_mfma_f32_16x16x32_bf16 v[156:159], a[64:67], v[92:95], v[156:159]\n\tds_read_b64 a[182:183], v0 offset:1160\n\tv_mfma_f32_16x16x32_bf16 v[160:163], a[64:67], v[96:99], v[160:163]\n\t" \
    "ds_read_b64 a[184:185], v0 offset:640\n\tv_mfma_f32_16x16x32_bf16 v[136:139], a[68:71], v[68:71], v[136:139]\n\tds_read_b64 a[186:187], v0 offset:648\n\tv_mfma_f32_16x16x32_bf16 v[140:143], a[68:71], v[72:75], v[140:143]\n\tds_read_b64 a[188:189], v0 offset:128\n\tv_mfma_f32_16x16x32_bf16 v[144:147], a[68:71], v[76:79], v[144:147]\n\tds_read_b64 a[190:191], v0 offset:136\n\tv_mfma_f32_16x16x32_bf16 v[148:151], a[68:71], v[80:83], v[148:151]\n\t" \
    "ds_read_b128 v[100:103], v1 offset:37248\n\tv_mfma_f32_16x16x32_bf16 v[152:155], a[68:71], v[84:87], v[152:155]\n\tds_read_b128 v[104:107], v1 offset:37760\n\tv_mfma_f32_16x16x32_bf16 v[156:159], a[68:71], v[88:91], v[156:159]\n\tds_read_b128 v[108:111], v1 offset:38272\n\tv_mfma_f32_16x16x32_bf16 v[160:163], a[68:71], v[92:95], v[160:163]\n\tds_read_b128 v[112:115], v1 offset:38784\n\tv_mfma_f32_16x16x32_bf16 v[140:143], a[72:75], v[68:71], v[140:143]\n\t" \
    "ds_read_b128 v[116:119], v1 offset:39296\n\ts_waitcnt lgkmcnt(15)\n\tbuffer_store_dwordx4 v[208:211], v5, s[8:11], s56 offen sc1\n\tv_mfma_f32_16x16x32_bf16 v[144:147], a[72:75], v[72:75], v[144:147]\n\tds_read_b128 v[120:123], v1 offset:39808\n\ts_add_u32 s56, s56, 49152\n\tv_mfma_f32_16x16x32_bf16 v[148:151], a[72:75], v[76:79], v[148:151]\n\tds_read_b128 v[124:127], v1 offset:40320\n\t" \
    "v_mfma_f32_16x16x32_bf16 v[152:155], a[72:75], v[80:83], v[152:155]\n\tds_read_b128 v[128:131], v1 offset:40832\n\tv_mfma_f32_16x16x32_bf16 v[156:159], a[72:75], v[84:87], v[156:159]\n\tds_read_b64 a[192:193], v0 offset:3648\n\tv_mfma_f32_16x16x32_bf16 v[160:163], a[72:75], v[88:91], v[160:163]\n\tds_read_b64 a[194:195], v0 offset:3656\n\tv_mfma_f32_16x16x32_bf16 v[144:147], a[76:79], v[68:71], v[144:147]\n\tds_read_b64 a[196:197], v0 offset:3136\n\t" \
    "v_mfma_f32_16x16x32_bf16 v[148:151], a[76:79], v[72:75], v[148:151]\n\tds_read_b64 a[198:199], v0 offset:3144\n\tv_mfma_f32_16x16x32_bf16 v[152:155], a[76:79], v[76:79], v[152:155]\n\tds_read_b64 a[200:201], v0 offset:2624\n\tv_mfma_f32_16x16x32_bf16 v[156:159], a[76:79], v[80:83], v[156:159]\n\tds_read_b64 a[202:203], v0 offset:2632\n\tv_mfma_f32_16x16x32_bf16 v[160:163], a[76:79], v[84:87], v[160:163]\n\tds_read_b64 a[204:205], v0 offset:2112\n\t" \
    "v_mfma_f32_16x16x32_bf16 v[148:151], a[80:83], v[68:71], v[148:151]\n\tds_read_b64 a[206:207], v0 offset:2120\n\tv_mfma_f32_16x16x32_bf16 v[152:155], a[80:83], v[72:75], v[152:155]\n\tds_read_b64 a[208:209], v0 offset:1600\n\tv_mfma_f32_16x16x32_bf16 v[156:159], a[80:83], v[76:79], v[156:159]\n\tds_read_b64 a[210:211], v0 offset:1608\n\tv_mfma_f32_16x16x32_bf16 v[160:163], a[80:83], v[80:83], v[160:163]\n\tds_read_b64 a[212:213], v0 offset:1088\n\t" \
    "v_mfma_f32_16x16x32_bf16 v[152:155], a[84:87], v[68:71], v[152:155]\n\tds_read_b64 a[214:215], v0 offset:1096\n\tv_mfma_f32_16x16x32_bf16 v[156:159], a[84:87], v[72:75], v[156:159]\n\tbuffer_store_dwordx4 v[212:215], v5, s[8:11], s56 offen sc1\n\tv_mfma_f32_16x16x32_bf16 v[160:163], a[84:87], v[76:79], v[160:163]\n\ts_add_u32 s56, s56, 49152\n\tv_mfma_f32_16x16x32_bf16 v[156:159], a[88:91], v[68:71], v[156:159]\n\tv_mfma_f32_16x16x32_bf16 v[160:163], a[88:91], v[72:75], v[160:163]\n\t" \
    "v_mfma_f32_16x16x32_bf16 v[160:163], a[92:95], v[68:71], v[160:163]\n\ts_waitcnt lgkmcnt(12)\n\tv_mfma_f32_16x16x32_bf16 v[132:135], a[96:99], v[100:103], v[132:135]\n\tds_read_b64 a[216:217], v0 offset:576\n\tv_mfma_f32_16x16x32_bf16 v[136:139], a[96:99], v[104:107], v[136:139]\n\tds_read_b64 a[218:219], v0 offset:584\n\tv_mfma_f32_16x16x32_bf16 v[140:143], a[96:99], v[108:111], v[140:143]\n\tds_read_b64 a[220:221], v0 offset:64\n\t" \
    "v_mfma_f32_16x16x32_bf16 v[144:147], a[96:99], v[112:115], v[144:147]\n\tds_read_b64 a[222:223], v0 offset:72\n\tv_mfma_f32_16x16x32_bf16 v[148:151], a[96:99], v[116:119], v[148:151]\n\tds_read_b64 a[224:225], v0 offset:3584\n\tv_mfma_f32_16x16x32_bf16 v[152:155], a[96:99], v[120:123], v[152:155]\n\tds_read_b64 a[226:227], v0 offset:3592\n\tv_mfma_f32_16x16x32_bf16 v[156:159], a[96:99], v[124:127], v[156:159]\n\tds_read_b64 a[228:229], v0 offset:3072\n\t" \
    "v_mfma_f32_16x16x32_bf16 v[160:163], a[96:99], v[128:131], v[160:163]\n\tds_read_b64 a[230:231], v0 offset:3080\n\tv_mfma_f32_16x16x32_bf16 v[136:139], a[100:103], v[100:103], v[136:139]\n\tds_read_b64 a[232:233], v0 offset:2560\n\tbuffer_store_dwordx4 v[216:219], v5, s[8:11], s56 offen sc1\n\tv_mfma_f32_16x16x32_bf16 v[140:143], a[100:103], v[104:107], v[140:143]\n\tds_read_b64 a[234:235], v0 offset:2568\n\ts_add_u32 s56, s56, 49152\n\t" \
    "v_mfma_f32_16x16x32_bf16 v[144:147], a[100:103], v[108:111], v[144:147]\n\tds_read_b64 a[236:237], v0 offset:2048\n\tv_mfma_f32_16x16x32_bf16 v[148:151], a[100:103], v[112:115], v[148:151]\n\tds_read_b64 a[238:239], v0 offset:2056\n\tv_mfma_f32_16x16x32_bf16 v[152:155], a[100:103], v[116:119], v[152:155]\n\tds_read_b64 a[240:241], v0 offset:1536\n\tv_mfma_f32_16x16x32_bf16 v[156:159], a[100:103], v[120:123], v[156:159]\n\tds_read_b64 a[242:243], v0 offset:1544\n\t" \
    "v_mfma_f32_16x16x32_bf16 v[160:163], a[100:103], v[124:127], v[160:163]\n\tds_read_b64 a[244:245], v0 offset:1024\n\tv_mfma_f32_16x16x32_bf16 v[140:143], a[104:107], v[100:103], v[140:143]\n\tds_read_b64 a[246:247], v0 offset:1032\n\tv_mfma_f32_16x16x32_bf16 v[144:147], a[104:107], v[104:107], v[144:147]\n\tds_read_b128 v[68:71], v1 offset:37184\n\tv_mfma_f32_16x16x32_bf16 v[148:151], a[104:107], v[108:111], v[148:151]\n\tds_read_b128 v[72:75], v1 offset:37696\n\t" \
    "v_mfma_f32_16x16x32_bf16 v[152:155], a[104:107], v[112:115], v[152:155]\n\tds_read_b128 v[76:79], v1 offset:38208\n\tv_mfma_f32_16x16x32_bf16 v[156:159], a[104:107], v[116:119], v[156:159]\n\tds_read_b128 v[80:83], v1 offset:38720\n\tv_mfma_f32_16x16x32_bf16 v[160:163], a[104:107], v[120:123], v[160:163]\n\tbuffer_load_dword v15, v7, s[16:19], s42 offen\n\tds_read_b128 v[84:87], v1 offset:39232\n\tv_mfma_f32_16x16x32_bf16 v[144:147], a[108:111], v[100:103], v[144:147]\n\t" \
    "s_add_u32 s43, s43, 1\n\tds_read_b128 v[88:91], v1 offset:39744\n\tv_mfma_f32_16x16x32_bf16 v[148:151], a[108:111], v[104:107], v[148:151]\n\tv_readlane_b32 s42, v237, s43\n\ts_nop 4\n\tds_read_b128 v[92:95], v1 offset:40256\n\tv_mfma_f32_16x16x32_bf16 v[152:155], a[108:111], v[108:111], v[152:155]\n\tds_read_b128 v[96:99], v1 offset:40768\n\t" \
    "v_mfma_f32_16x16x32_bf16 v[156:159], a[108:111], v[112:115], v[156:159]\n\tds_read_b64 a[248:249], v0 offset:512\n\tv_mfma_f32_16x16x32_bf16 v[160:163], a[108:111], v[116:119], v[160:163]\n\tds_read_b64 a[250:251], v0 offset:520\n\tv_mfma_f32_16x16x32_bf16 v[148:151], a[112:115], v[100:103], v[148:151]\n\tds_read_b64 a[252:253], v0 offset:0\n\tv_mfma_f32_16x16x32_bf16 v[152:155], a[112:115], v[104:107], v[152:155]\n\tds_read_b64 a[254:255], v0 offset:8\n\t" \
    "v_mfma_f32_16x16x32_bf16 v[156:159], a[112:115], v[108:111], v[156:159]\n\tv_mfma_f32_16x16x32_bf16 v[160:163], a[112:115], v[112:115], v[160:163]\n\tv_mfma_f32_16x16x32_bf16 v[152:155], a[116:119], v[100:103], v[152:155]\n\tv_mfma_f32_16x16x32_bf16 v[156:159], a[116:119], v[104:107], v[156:159]\n\tv_mfma_f32_16x16x32_bf16 v[160:163], a[116:119], v[108:111], v[160:163]\n\tv_mfma_f32_16x16x32_bf16 v[156:159], a[120:123], v[100:103], v[156:159]\n\tv_mfma_f32_16x16x32_bf16 v[160:163], a[120:123], v[104:107], v[160:163]\n\tv_mfma_f32_16x16x32_bf16 v[160:163], a[124:127], v[100:103], v[160:163]\n\t" \
    "s_waitcnt lgkmcnt(4)\n\tv_mfma_f32_16x16x32_bf16 v[132:135], a[128:131], v[68:71], v[132:135]\n\tds_read_b128 v[100:103], v1 offset:37120\n\ts_waitcnt vmcnt(9)\n\tv_alignbit_b32 v196, v45, v45, 16\n\tv_mfma_f32_16x16x32_bf16 v[136:139], a[128:131], v[72:75], v[136:139]\n\tds_read_b128 v[104:107], v1 offset:37632\n\tv_alignbit_b32 v197, v44, v44, 16\n\t" \
    "v_mfma_f32_16x16x32_bf16 v[140:143], a[128:131], v[76:79], v[140:143]\n\tds_read_b128 v[108:111], v1 offset:38144\n\tv_alignbit_b32 v198, v43, v43, 16\n\tv_mfma_f32_16x16x32_bf16 v[144:147], a[128:131], v[80:83], v[144:147]\n\tds_read_b128 v[112:115], v1 offset:38656\n\tv_alignbit_b32 v199, v42, v42, 16\n\tv_mfma_f32_16x16x32_bf16 v[148:151], a[128:131], v[84:87], v[148:151]\n\tds_write_b128 v2, v[196:199] offset:3072\n\t" \
    "v_mfma_f32_16x16x32_bf16 v[152:155], a[128:131], v[88:91], v[152:155]\n\tds_read_b128 v[116:119], v1 offset:39168\n\tv_bfi_b32 v200, s46, v45, v44\n\tv_mfma_f32_16x16x32_bf16 v[156:159], a[128:131], v[92:95], v[156:159]\n\tds_read_b128 v[120:123], v1 offset:39680\n\tv_bfi_b32 v201, s46, v44, v43\n\tv_mfma_f32_16x16x32_bf16 v[160:163], a[128:131], v[96:99], v[160:163]\n\tds_read_b128 v[124:127], v1 offset:40192\n\t" \
    "v_bfi_b32 v202, s46, v43, v42\n\tv_mfma_f32_16x16x32_bf16 v[136:139], a[132:135], v[68:71], v[136:139]\n\tds_read_b128 v[128:131], v1 offset:40704\n\tv_bfi_b32 v203, s46, v42, v59\n\tv_mfma_f32_16x16x32_bf16 v[140:143], a[132:135], v[72:75], v[140:143]\n\tds_write_b128 v2, v[200:203] offset:7232\n\tv_mfma_f32_16x16x32_bf16 v[144:147], a[132:135], v[76:79], v[144:147]\n\tv_alignbit_b32 v196, v44, v44, 16\n\t" \
    "v_alignbit_b32 v197, v43, v43, 16\n\tv_mfma_f32_16x16x32_bf16 v[148:151], a[132:135], v[80:83], v[148:151]\n\tv_alignbit_b32 v198, v42, v42, 16\n\tv_alignbit_b32 v199, v59, v59, 16\n\tv_mfma_f32_16x16x32_bf16 v[152:155], a[132:135], v[84:87], v[152:155]\n\tds_write_b128 v2, v[196:199] offset:11392\n\tv_mfma_f32_16x16x32_bf16 v[156:159], a[132:135], v[88:91], v[156:159]\n\tv_bfi_b32 v200, s46, v44, v43\n\t" \
    "v_bfi_b32 v201, s46, v43, v42\n\tv_mfma_f32_16x16x32_bf16 v[160:163], a[132:135], v[92:95], v[160:163]\n\tv_bfi_b32 v202, s46, v42, v59\n\tv_bfi_b32 v203, s46, v59, v58\n\tv_mfma_f32_16x16x32_bf16 v[140:143], a[136:139], v[68:71], v[140:143]\n\tds_write_b128 v2, v[200:203] offset:15552\n\tv_mfma_f32_16x16x32_bf16 v[144:147], a[136:139], v[72:75], v[144:147]\n\tv_alignbit_b32 v196, v49, v49, 16\n\t" \
    "v_alignbit_b32 v197, v48, v48, 16\n\tv_mfma_f32_16x16x32_bf16 v[148:151], a[136:139], v[76:79], v[148:151]\n\tv_alignbit_b32 v198, v47, v47, 16\n\tv_alignbit_b32 v199, v46, v46, 16\n\tv_mfma_f32_16x16x32_bf16 v[152:155], a[136:139], v[80:83], v[152:155]\n\tds_write_b128 v2, v[196:199] offset:2048\n\tv_mfma_f32_16x16x32_bf16 v[156:159], a[136:139], v[84:87], v[156:159]\n\tv_bfi_b32 v200, s46, v49, v48\n\t" \
    "v_bfi_b32 v201, s46, v48, v47\n\tv_mfma_f32_16x16x32_bf16 v[160:163], a[136:139], v[88:91], v[160:163]\n\tv_bfi_b32 v202, s46, v47, v46\n\tv_bfi_b32 v203, s46, v46, v61\n\tv_mfma_f32_16x16x32_bf16 v[144:147], a[140:143], v[68:71], v[144:147]\n\tds_write_b128 v2, v[200:203] offset:6208\n\tv_mfma_f32_16x16x32_bf16 v[148:151], a[140:143], v[72:75], v[148:151]\n\tv_alignbit_b32 v196, v48, v48, 16\n\t" \
    "v_alignbit_b32 v197, v47, v47, 16\n\tv_mfma_f32_16x16x32_bf16 v[152:155], a[140:143], v[76:79], v[152:155]\n\tv_alignbit_b32 v198, v46, v46, 16\n\tv_alignbit_b32 v199, v61, v61, 16\n\tv_mfma_f32_16x16x32_bf16 v[156:159], a[140:143], v[80:83], v[156:159]\n\tds_write_b128 v2, v[196:199] offset:10368\n\tv_mfma_f32_16x16x32_bf16 v[160:163], a[140:143], v[84:87], v[160:163]\n\tv_bfi_b32 v200, s46, v48, v47\n\t" \
    "v_bfi_b32 v201, s46, v47, v46\n\tv_mfma_f32_16x16x32_bf16 v[148:151], a[144:147], v[68:71], v[148:151]\n\tv_bfi_b32 v202, s46, v46, v61\n\tv_bfi_b32 v203, s46, v61, v60\n\tv_mfma_f32_16x16x32_bf16 v[152:155], a[144:147], v[72:75], v[152:155]\n\tds_write_b128 v2, v[200:203] offset:14528\n\tv_mfma_f32_16x16x32_bf16 v[156:159], a[144:147], v[76:79], v[156:159]\n\tv_alignbit_b32 v196, v53, v53, 16\n\t" \
    "v_alignbit_b32 v197, v52, v52, 16\n\tv_mfma_f32_16x16x32_bf16 v[160:163], a[144:147], v[80:83], v[160:163]\n\tv_alignbit_b32 v198, v51, v51, 16\n\tv_alignbit_b32 v199, v50, v50, 16\n\tv_mfma_f32_16x16x32_bf16 v[152:155], a[148:151], v[68:71], v[152:155]\n\tds_write_b128 v2, v[196:199] offset:1024\n\tv_mfma_f32_16x16x32_bf16 v[156:159], a[148:151], v[72:75], v[156:159]\n\tv_bfi_b32 v200, s46, v53, v52\n\t" \
    "v_bfi_b32 v201, s46, v52, v51\n\tv_mfma_f32_16x16x32_bf16 v[160:163], a[148:151], v[76:79], v[160:163]\n\tv_bfi_b32 v202, s46, v51, v50\n\tv_bfi_b32 v203, s46, v50, v63\n\tv_mfma_f32_16x16x32_bf16 v[156:159], a[152:155], v[68:71], v[156:159]\n\tds_write_b128 v2, v[200:203] offset:5184\n\tv_mfma_f32_16x16x32_bf16 v[160:163], a[152:155], v[72:75], v[160:163]\n\tv_alignbit_b32 v196, v52, v52, 16\n\t" \
    "v_alignbit_b32 v197, v51, v51, 16\n\tv_mfma_f32_16x16x32_bf16 v[160:163], a[156:159], v[68:71], v[160:163]\n\tv_alignbit_b32 v198, v50, v50, 16\n\tv_alignbit_b32 v199, v63, v63, 16\n\ts_waitcnt lgkmcnt(9)\n\tv_mfma_f32_16x16x32_bf16 v[132:135], a[160:163], v[100:103], v[132:135]\n\tds_write_b128 v2, v[196:199] offset:9344\n\tv_mfma_f32_16x16x32_bf16 v[136:139], a[160:163], v[104:107], v[136:139]\n\t" \
    "ds_read_b128 v[68:71], v1 offset:37056\n\tv_bfi_b32 v200, s46, v52, v51\n\tv_mfma_f32_16x16x32_bf16 v[140:143], a[160:163], v[108:111], v[140:143]\n\tds_read_b128 v[72:75], v1 offset:37568\n\tv_bfi_b32 v201, s46, v51, v50\n\tv_mfma_f32_16x16x32_bf16 v[144:147], a[160:163], v[112:115], v[144:147]\n\tds_read_b128 v[76:79], v1 offset:38080\n\tv_bfi_b32 v202, s46, v50, v63\n\t" \
    "v_mfma_f32_16x16x32_bf16 v[148:151], a[160:163], v[116:119], v[148:151]\n\tds_read_b128 v[80:83], v1 offset:38592\n\tv_bfi_b32 v203, s46, v63, v62\n\tv_mfma_f32_16x16x32_bf16 v[152:155], a[160:163], v[120:123], v[152:155]\n\tds_write_b128 v2, v[200:203] offset:13504\n\tv_mfma_f32_16x16x32_bf16 v[156:159], a[160:163], v[124:127], v[156:159]\n\tds_read_b128 v[84:87], v1 offset:39104\n\tv_alignbit_b32 v196, v57, v57, 16\n\t" \
    "v_mfma_f32_16x16x32_bf16 v[160:163], a[160:163], v[128:131], v[160:163]\n\tbuffer_load_dword v15, v7, s[16:19], s42 offen\n\tds_read_b128 v[88:91], v1 offset:39616\n\tv_mfma_f32_16x16x32_bf16 v[136:139], a[164:167], v[100:103], v[136:139]\n\ts_add_u32 s43, s43, 1\n\tds_read_b128 v[92:95], v1 offset:40128\n\tv_alignbit_b32 v197, v56, v56, 16\n\tv_mfma_f32_16x16x32_bf16 v[140:143], a[164:167], v[104:107], v[140:143]\n\t" \
    "v_readlane_b32 s42, v237, s43\n\ts_nop 4\n\tds_read_b128 v[96:99], v1 offset:40640\n\tv_alignbit_b32 v198, v55, v55, 16\n\tv_mfma_f32_16x16x32_bf16 v[144:147], a[164:167], v[108:111], v[144:147]\n\tv_alignbit_b32 v199, v54, v54, 16\n\tv_mfma_f32_16x16x32_bf16 v[148:151], a[164:167], v[112:115], v[148:151]\n\tds_write_b128 v2, v[196:199] offset:0\n\t" \
    "v_mfma_f32_16x16x32_bf16 v[152:155], a[164:167], v[116:119], v[152:155]\n\tv_bfi_b32 v200, s46, v57, v56\n\tv_bfi_b32 v201, s46, v56, v55\n\tv_mfma_f32_16x16x32_bf16 v[156:159], a[164:167], v[120:123], v[156:159]\n\tv_bfi_b32 v202, s46, v55, v54\n\tv_bfi_b32 v203, s46, v54, v65\n\tv_mfma_f32_16x16x32_bf16 v[160:163], a[164:167], v[124:127], v[160:163]\n\tds_write_b128 v2, v[200:203] offset:4160\n\t" \
    "v_mfma_f32_16x16x32_bf16 v[140:143], a[168:171], v[100:103], v[140:143]\n\tv_alignbit_b32 v196, v56, v56, 16\n\tv_alignbit_b32 v197, v55, v55, 16\n\tv_mfma_f32_16x16x32_bf16 v[144:147], a[168:171], v[104:107], v[144:147]\n\tv_alignbit_b32 v198, v54, v54, 16\n\tv_alignbit_b32 v199, v65, v65, 16\n\tv_mfma_f32_16x16x32_bf16 v[148:151], a[168:171], v[108:111], v[148:151]\n\tds_write_b128 v2, v[196:199] offset:8320\n\t" \
    "v_mfma_f32_16x16x32_bf16 v[152:155], a[168:171], v[112:115], v[152:155]\n\tv_bfi_b32 v200, s46, v56, v55\n\tv_bfi_b32 v201, s46, v55, v54\n\tv_mfma_f32_16x16x32_bf16 v[156:159], a[168:171], v[116:119], v[156:159]\n\tv_bfi_b32 v202, s46, v54, v65\n\tv_bfi_b32 v203, s46, v65, v64\n\tv_mfma_f32_16x16x32_bf16 v[160:163], a[168:171], v[120:123], v[160:163]\n\tds_write_b128 v2, v[200:203] offset:12480\n\t" \
    "v_mfma_f32_16x16x32_bf16 v[144:147], a[172:175], v[100:103], v[144:147]\n\tv_lshl_or_b32 v66, v66, 16, v66\n\tv_and_b32 v228, v66, v232\n\tv_mfma_f32_16x16x32_bf16 v[148:151], a[172:175], v[104:107], v[148:151]\n\tv_and_b32 v229, v66, v233\n\tv_and_b32 v230, v66, v234\n\tv_mfma_f32_16x16x32_bf16 v[152:155], a[172:175], v[108:111], v[152:155]\n\tv_and_b32 v231, v66, v235\n\t" \
    "v_mfma_f32_16x16x32_bf16 v[156:159], a[172:175], v[112:115], v[156:159]\n\tv_mfma_f32_16x16x32_bf16 v[160:163], a[172:175], v[116:119], v[160:163]\n\tv_mfma_f32_16x16x32_bf16 v[148:151], a[176:179], v[100:103], v[148:151]\n\tv_mfma_f32_16x16x32_bf16 v[152:155], a[176:179], v[104:107], v[152:155]\n\tv_mfma_f32_16x16x32_bf16 v[156:159], a[176:179], v[108:111], v[156:159]\n\tv_mfma_f32_16x16x32_bf16 v[160:163], a[176:179], v[112:115], v[160:163]\n\tv_mfma_f32_16x16x32_bf16 v[152:155], a[180:183], v[100:103], v[152:155]\n\tv_mfma_f32_16x16x32_bf16 v[156:159], a[180:183], v[104:107], v[156:159]\n\t" \
    "v_mfma_f32_16x16x32_bf16 v[160:163], a[180:183], v[108:111], v[160:163]\n\tv_mfma_f32_16x16x32_bf16 v[156:159], a[184:187], v[100:103], v[156:159]\n\tv_mfma_f32_16x16x32_bf16 v[160:163], a[184:187], v[104:107], v[160:163]\n\tv_mfma_f32_16x16x32_bf16 v[160:163], a[188:191], v[100:103], v[160:163]\n\ts_waitcnt lgkmcnt(4)\n\tv_mfma_f32_16x16x32_bf16 v[132:135], a[192:195], v[68:71], v[132:135]\n\tds_read_b128 v[100:103], v1 offset:36992\n\tv_mfma_f32_16x16x32_bf16 v[136:139], a[192:195], v[72:75], v[136:139]\n\t" \
    "ds_read_b128 v[104:107], v1 offset:37504\n\tv_mfma_f32_16x16x32_bf16 v[140:143], a[192:195], v[76:79], v[140:143]\n\tds_read_b128 v[108:111], v1 offset:38016\n\tv_mfma_f32_16x16x32_bf16 v[144:147], a[192:195], v[80:83], v[144:147]\n\tds_read_b128 v[112:115], v1 offset:38528\n\tv_mfma_f32_16x16x32_bf16 v[148:151], a[192:195], v[84:87], v[148:151]\n\tds_read_b128 v[116:119], v1 offset:39040\n\tv_mfma_f32_16x16x32_bf16 v[152:155], a[192:195], v[88:91], v[152:155]\n\t" \
    "ds_read_b128 v[120:123], v1 offset:39552\n\tv_mfma_f32_16x16x32_bf16 v[156:159], a[192:195], v[92:95], v[156:159]\n\tds_read_b128 v[124:127], v1 offset:40064\n\tv_mfma_f32_16x16x32_bf16 v[160:163], a[192:195], v[96:99], v[160:163]\n\tds_read_b128 v[128:131], v1 offset:40576\n\tv_mfma_f32_16x16x32_bf16 v[136:139], a[196:199], v[68:71], v[136:139]\n\tv_mfma_f32_16x16x32_bf16 v[140:143], a[196:199], v[72:75], v[140:143]\n\tv_mfma_f32_16x16x32_bf16 v[144:147], a[196:199], v[76:79], v[144:147]\n\t" \
    "v_mfma_f32_16x16x32_bf16 v[148:151], a[196:199], v[80:83], v[148:151]\n\tv_mfma_f32_16x16x32_bf16 v[152:155], a[196:199], v[84:87], v[152:155]\n\tv_mfma_f32_16x16x32_bf16 v[156:159], a[196:199], v[88:91], v[156:159]\n\tv_mfma_f32_16x16x32_bf16 v[160:163], a[196:199], v[92:95], v[160:163]\n\tv_mfma_f32_16x16x32_bf16 v[140:143], a[200:203], v[68:71], v[140:143]\n\tv_mfma_f32_16x16x32_bf16 v[144:147], a[200:203], v[72:75], v[144:147]\n\tv_mfma_f32_16x16x32_bf16 v[148:151], a[200:203], v[76:79], v[148:151]\n\tv_mfma_f32_16x16x32_bf16 v[152:155], a[200:203], v[80:83], v[152:155]\n\t" \
    "v_mfma_f32_16x16x32_bf16 v[156:159], a[200:203], v[84:87], v[156:159]\n\tv_mfma_f32_16x16x32_bf16 v[160:163], a[200:203], v[88:91], v[160:163]\n\tv_mfma_f32_16x16x32_bf16 v[144:147], a[204:207], v[68:71], v[144:147]\n\tv_mfma_f32_16x16x32_bf16 v[148:151], a[204:207], v[72:75], v[148:151]\n\tv_mfma_f32_16x16x32_bf16 v[152:155], a[204:207], v[76:79], v[152:155]\n\tv_mfma_f32_16x16x32_bf16 v[156:159], a[204:207], v[80:83], v[156:159]\n\tv_mfma_f32_16x16x32_bf16 v[160:163], a[204:207], v[84:87], v[160:163]\n\tv_mfma_f32_16x16x32_bf16 v[148:151], a[208:211], v[68:71], v[148:151]\n\t" \
    "v_mfma_f32_16x16x32_bf16 v[152:155], a[208:211], v[72:75], v[152:155]\n\tv_mfma_f32_16x16x32_bf16 v[156:159], a[208:211], v[76:79], v[156:159]\n\tv_mfma_f32_16x16x32_bf16 v[160:163], a[208:211], v[80:83], v[160:163]\n\tv_mfma_f32_16x16x32_bf16 v[152:155], a[212:215], v[68:71], v[152:155]\n\tbuffer_load_dword v15, v7, s[16:19], s42 offen\n\tv_mfma_f32_16x16x32_bf16 v[156:159], a[212:215], v[72:75], v[156:159]\n\ts_add_u32 s43, s43, 1\n\tv_mfma_f32_16x16x32_bf16 v[160:163], a[212:215], v[76:79], v[160:163]\n\t" \
    "v_readlane_b32 s42, v237, s43\n\ts_nop 4\n\tv_mfma_f32_16x16x32_bf16 v[156:159], a[216:219], v[68:71], v[156:159]\n\tv_mfma_f32_16x16x32_bf16 v[160:163], a[216:219], v[72:75], v[160:163]\n\tv_mfma_f32_16x16x32_bf16 v[160:163], a[220:223], v[68:71], v[160:163]\n\ts_waitcnt lgkmcnt(0)\n\tv_mfma_f32_16x16x32_bf16 v[132:135], a[224:227], v[100:103], v[132:135]\n\ts_waitcnt vmcnt(3)\n\t" \
    "buffer_store_dwordx2 v[248:249], off, s[8:11], s51 sc1\n\tds_read_b64 a[0:1], v0 offset:4032\n\tv_mfma_f32_16x16x32_bf16 v[136:139], a[224:227], v[104:107], v[136:139]\n\tds_read_b64 a[2:3], v0 offset:4040\n\tv_mfma_f32_16x16x32_bf16 v[140:143], a[224:227], v[108:111], v[140:143]\n\tds_read_b64 a[4:5], v0 offset:3520\n\tv_mfma_f32_16x16x32_bf16 v[144:147], a[224:227], v[112:115], v[144:147]\n\tds_read_b64 a[6:7], v0 offset:3528\n\t" \
    "v_mfma_f32_16x16x32_bf16 v[148:151], a[224:227], v[116:119], v[148:151]\n\tds_read_b64 a[8:9], v0 offset:3008\n\tv_mfma_f32_16x16x32_bf16 v[152:155], a[224:227], v[120:123], v[152:155]\n\tds_read_b64 a[10:11], v0 offset:3016\n\tv_mfma_f32_16x16x32_bf16 v[156:159], a[224:227], v[124:127], v[156:159]\n\tds_read_b64 a[12:13], v0 offset:2496\n\tv_mfma_f32_16x16x32_bf16 v[160:163], a[224:227], v[128:131], v[160:163]\n\tds_read_b64 a[14:15], v0 offset:2504\n\t" \
    "v_mfma_f32_16x16x32_bf16 v[136:139], a[228:231], v[100:103], v[136:139]\n\tds_read_b64 a[16:17], v0 offset:1984\n\tv_mfma_f32_16x16x32_bf16 v[140:143], a[228:231], v[104:107], v[140:143]\n\tds_read_b64 a[18:19], v0 offset:1992\n\tv_mfma_f32_16x16x32_bf16 v[144:147], a[228:231], v[108:111], v[144:147]\n\tds_read_b64 a[20:21], v0 offset:1472\n\tv_mfma_f32_16x16x32_bf16 v[148:151], a[228:231], v[112:115], v[148:151]\n\tds_read_b64 a[22:23], v0 offset:1480\n\t" \
    "v_mfma_f32_16x16x32_bf16 v[152:155], a[228:231], v[116:119], v[152:155]\n\tds_read_b64 a[24:25], v0 offset:960\n\tv_mfma_f32_16x16x32_bf16 v[156:159], a[228:231], v[120:123], v[156:159]\n\tds_read_b64 a[26:27], v0 offset:968\n\tv_mfma_f32_16x16x32_bf16 v[160:163], a[228:231], v[124:127], v[160:163]\n\tds_read_b64 a[28:29], v0 offset:448\n\tv_mfma_f32_16x16x32_bf16 v[140:143], a[232:235], v[100:103], v[140:143]\n\tds_read_b64 a[30:31], v0 offset:456\n\t" \
    "v_mfma_f32_16x16x32_bf16 v[144:147], a[232:235], v[104:107], v[144:147]\n\tds_read_b128 v[68:71], v1 offset:55936\n\tv_mfma_f32_16x16x32_bf16 v[148:151], a[232:235], v[108:111], v[148:151]\n\tds_read_b128 v[72:75], v1 offset:56448\n\tv_mfma_f32_16x16x32_bf16 v[152:155], a[232:235], v[112:115], v[152:155]\n\tds_read_b128 v[76:79], v1 offset:56960\n\tv_mfma_f32_16x16x32_bf16 v[156:159], a[232:235], v[116:119], v[156:159]\n\tds_read_b128 v[80:83], v1 offset:57472\n\t" \
    "v_mfma_f32_16x16x32_bf16 v[160:163], a[232:235], v[120:123], v[160:163]\n\tds_read_b128 v[84:87], v1 offset:57984\n\tv_mfma_f32_16x16x32_bf16 v[144:147], a[236:239], v[100:103], v[144:147]\n\tds_read_b128 v[88:91], v1 offset:58496\n\tv_mfma_f32_16x16x32_bf16 v[148:151], a[236:239], v[104:107], v[148:151]\n\tds_read_b128 v[92:95], v1 offset:59008\n\tv_mfma_f32_16x16x32_bf16 v[152:155], a[236:239], v[108:111], v[152:155]\n\tds_read_b128 v[96:99], v1 offset:59520\n\t" \
    "v_mfma_f32_16x16x32_bf16 v[156:159], a[236:239], v[112:115], v[156:159]\n\tv_mfma_f32_16x16x32_bf16 v[160:163], a[236:239], v[116:119], v[160:163]\n\tv_mfma_f32_16x16x32_bf16 v[148:151], a[240:243], v[100:103], v[148:151]\n\tv_mfma_f32_16x16x32_bf16 v[152:155], a[240:243], v[104:107], v[152:155]\n\tv_mfma_f32_16x16x32_bf16 v[156:159], a[240:243], v[108:111], v[156:159]\n\tv_mfma_f32_16x16x32_bf16 v[160:163], a[240:243], v[112:115], v[160:163]\n\tv_mfma_f32_16x16x32_bf16 v[152:155], a[244:247], v[100:103], v[152:155]\n\tv_mfma_f32_16x16x32_bf16 v[156:159], a[244:247], v[104:107], v[156:159]\n\t" \
    "v_mfma_f32_16x16x32_bf16 v[160:163], a[244:247], v[108:111], v[160:163]\n\tv_mfma_f32_16x16x32_bf16 v[156:159], a[248:251], v[100:103], v[156:159]\n\tv_mfma_f32_16x16x32_bf16 v[160:163], a[248:251], v[104:107], v[160:163]\n\tv_mfma_f32_16x16x32_bf16 v[160:163], a[252:255], v[100:103], v[160:163]\n\ts_waitcnt lgkmcnt(0)\n\tv_mfma_f32_16x16x32_bf16 v[164:167], v[228:231], v[68:71], 0\n\tds_read_b64 a[32:33], v0 offset:3968\n\tds_read_b64 a[34:35], v0 offset:3976\n\t" \
    "v_mfma_f32_16x16x32_bf16 v[168:171], v[228:231], v[72:75], 0\n\tds_read_b64 a[36:37], v0 offset:3456\n\tds_read_b64 a[38:39], v0 offset:3464\n\tv_mfma_f32_16x16x32_bf16 v[172:175], v[228:231], v[76:79], 0\n\tds_read_b64 a[40:41], v0 offset:2944\n\tds_read_b64 a[42:43], v0 offset:2952\n\tv_mfma_f32_16x16x32_bf16 v[176:179], v[228:231], v[80:83], 0\n\tds_read_b64 a[44:45], v0 offset:2432\n\t" \
    "ds_read_b64 a[46:47], v0 offset:2440\n\tv_mfma_f32_16x16x32_bf16 v[180:183], v[228:231], v[84:87], 0\n\tds_read_b64 a[48:49], v0 offset:1920\n\tds_read_b64 a[50:51], v0 offset:1928\n\tv_mfma_f32_16x16x32_bf16 v[184:187], v[228:231], v[88:91], 0\n\tds_read_b64 a[52:53], v0 offset:1408\n\tv_cvt_pk_bf16_f32 v132, v132, v133\n\tv_mfma_f32_16x16x32_bf16 v[188:191], v[228:231], v[92:95], 0\n\t" \
    "ds_read_b64 a[54:55], v0 offset:1416\n\tv_cvt_pk_bf16_f32 v133, v134, v135\n\tv_mfma_f32_16x16x32_bf16 v[192:195], v[228:231], v[96:99], 0\n\tds_read_b64 a[56:57], v0 offset:896\n\tds_write_b64 v3, v[132:133] offset:36992\n\tv_mfma_f32_16x16x32_bf16 v[164:167], a[0:3], v[68:71], v[164:167]\n\tds_read_b64 a[58:59], v0 offset:904\n\tv_cvt_pk_bf16_f32 v136, v136, v137\n\t" \
    "v_mfma_f32_16x16x32_bf16 v[168:171], a[0:3], v[72:75], v[168:171]\n\tds_read_b64 a[60:61], v0 offset:384\n\tv_cvt_pk_bf16_f32 v137, v138, v139\n\tv_mfma_f32_16x16x32_bf16 v[172:175], a[0:3], v[76:79], v[172:175]\n\tds_read_b64 a[62:63], v0 offset:392\n\tds_write_b64 v3, v[136:137] offset:37504\n\tv_mfma_f32_16x16x32_bf16 v[176:179], a[0:3], v[80:83], v[176:179]\n\tds_read_b128 v[100:103], v1 offset:55872\n\t" \
    "v_cvt_pk_bf16_f32 v140, v140, v141\n\tv_mfma_f32_16x16x32_bf16 v[180:183], a[0:3], v[84:87], v[180:183]\n\tds_read_b128 v[104:107], v1 offset:56384\n\tv_cvt_pk_bf16_f32 v141, v142, v143\n\tv_mfma_f32_16x16x32_bf16 v[184:187], a[0:3], v[88:91], v[184:187]\n\tds_read_b128 v[108:111], v1 offset:56896\n\tds_write_b64 v3, v[140:141] offset:38016\n\tv_mfma_f32_16x16x32_bf16 v[188:191], a[0:3], v[92:95], v[188:191]\n\t" \
    "ds_read_b128 v[112:115], v1 offset:57408\n\tv_cvt_pk_bf16_f32 v144, v144, v145\n\tv_mfma_f32_16x16x32_bf16 v[192:195], a[0:3], v[96:99], v[192:195]\n\tds_read_b128 v[116:119], v1 offset:57920\n\tv_cvt_pk_bf16_f32 v145, v146, v147\n\tv_mfma_f32_16x16x32_bf16 v[168:171], a[4:7], v[68:71], v[168:171]\n\tds_read_b128 v[120:123], v1 offset:58432\n\tds_write_b64 v3, v[144:145] offset:38528\n\t" \
    "v_mfma_f32_16x16x32_bf16 v[172:175], a[4:7], v[72:75], v[172:175]\n\tbuffer_load_dword v15, v7, s[16:19], s42 offen\n\tds_read_b128 v[124:127], v1 offset:58944\n\tv_mfma_f32_16x16x32_bf16 v[176:179], a[4:7], v[76:79], v[176:179]\n\ts_add_u32 s43, s43, 1\n\tds_read_b128 v[128:131], v1 offset:59456\n\tv_cvt_pk_bf16_f32 v148, v148, v149\n\tv_mfma_f32_16x16x32_bf16 v[180:183], a[4:7], v[80:83], v[180:183]\n\t" \
    "v_readlane_b32 s42, v237, s43\n\ts_nop 4\n\tds_read_b64 a[64:65], v0 offset:3904\n\tv_cvt_pk_bf16_f32 v149, v150, v151\n\tv_mfma_f32_16x16x32_bf16 v[184:187], a[4:7], v[84:87], v[184:187]\n\tds_read_b64 a[66:67], v0 offset:3912\n\tds_write_b64 v3, v[148:149] offset:39040\n\tv_mfma_f32_16x16x32_bf16 v[188:191], a[4:7], v[88:91], v[188:191]\n\t" \
    "ds_read_b64 a[68:69], v0 offset:3392\n\tv_cvt_pk_bf16_f32 v152, v152, v153\n\tv_mfma_f32_16x16x32_bf16 v[192:195], a[4:7], v[92:95], v[192:195]\n\tds_read_b64 a[70:71], v0 offset:3400\n\tv_cvt_pk_bf16_f32 v153, v154, v155\n\tv_mfma_f32_16x16x32_bf16 v[172:175], a[8:11], v[68:71], v[172:175]\n\tds_read_b64 a[72:73], v0 offset:2880\n\tds_write_b64 v3, v[152:153] offset:39552\n\t" \
    "v_mfma_f32_16x16x32_bf16 v[176:179], a[8:11], v[72:75], v[176:179]\n\tds_read_b64 a[74:75], v0 offset:2888\n\tv_cvt_pk_bf16_f32 v156, v156, v157\n\tv_mfma_f32_16x16x32_bf16 v[180:183], a[8:11], v[76:79], v[180:183]\n\tds_read_b64 a[76:77], v0 offset:2368\n\tv_cvt_pk_bf16_f32 v157, v158, v159\n\tv_mfma_f32_16x16x32_bf16 v[184:187], a[8:11], v[80:83], v[184:187]\n\tds_read_b64 a[78:79], v0 offset:2376\n\t" \
    "ds_write_b64 v3, v[156:157] offset:40064\n\tv_mfma_f32_16x16x32_bf16 v[188:191], a[8:11], v[84:87], v[188:191]\n\tds_read_b64 a[80:81], v0 offset:1856\n\tv_cvt_pk_bf16_f32 v160, v160, v161\n\tv_mfma_f32_16x16x32_bf16 v[192:195], a[8:11], v[88:91], v[192:195]\n\tds_read_b64 a[82:83], v0 offset:1864\n\tv_cvt_pk_bf16_f32 v161, v162, v163\n\tv_mfma_f32_16x16x32_bf16 v[176:179], a[12:15], v[68:71], v[176:179]\n\t" \
    "ds_read_b64 a[84:85], v0 offset:1344\n\tds_write_b64 v3, v[160:161] offset:40576\n\tv_mfma_f32_16x16x32_bf16 v[180:183], a[12:15], v[72:75], v[180:183]\n\tds_read_b64 a[86:87], v0 offset:1352\n\tv_mfma_f32_16x16x32_bf16 v[184:187], a[12:15], v[76:79], v[184:187]\n\tds_read_b64 a[88:89], v0 offset:832\n\tv_mfma_f32_16x16x32_bf16 v[188:191], a[12:15], v[80:83], v[188:191]\n\tds_read_b64 a[90:91], v0 offset:840\n\t" \
    "v_mfma_f32_16x16x32_bf16 v[192:195], a[12:15], v[84:87], v[192:195]\n\tds_read_b64 a[92:93], v0 offset:320\n\tv_mfma_f32_16x16x32_bf16 v[180:183], a[16:19], v[68:71], v[180:183]\n\tds_read_b64 a[94:95], v0 offset:328\n\tv_mfma_f32_16x16x32_bf16 v[184:187], a[16:19], v[72:75], v[184:187]\n\tds_read_b64 a[96:97], v0 offset:3840\n\tv_mfma_f32_16x16x32_bf16 v[188:191], a[16:19], v[76:79], v[188:191]\n\tds_read_b64 a[98:99], v0 offset:3848\n\t" \
    "v_mfma_f32_16x16x32_bf16 v[192:195], a[16:19], v[80:83], v[192:195]\n\tds_read_b64 a[100:101], v0 offset:3328\n\tv_mfma_f32_16x16x32_bf16 v[184:187], a[20:23], v[68:71], v[184:187]\n\tds_read_b64 a[102:103], v0 offset:3336\n\tv_mfma_f32_16x16x32_bf16 v[188:191], a[20:23], v[72:75], v[188:191]\n\tv_mfma_f32_16x16x32_bf16 v[192:195], a[20:23], v[76:79], v[192:195]\n\tv_mfma_f32_16x16x32_bf16 v[188:191], a[24:27], v[68:71], v[188:191]\n\tv_mfma_f32_16x16x32_bf16 v[192:195], a[24:27], v[72:75], v[192:195]\n\t" \
    "v_mfma_f32_16x16x32_bf16 v[192:195], a[28:31], v[68:71], v[192:195]\n\ts_waitcnt lgkmcnt(9)\n\ts_barrier\n\tv_mfma_f32_16x16x32_bf16 v[164:167], a[32:35], v[100:103], v[164:167]\n\tds_read_b64 a[104:105], v0 offset:2816\n\tds_read_b64 a[106:107], v0 offset:2824\n\tv_mfma_f32_16x16x32_bf16 v[168:171], a[32:35], v[104:107], v[168:171]\n\tds_read_b64 a[108:109], v0 offset:2304\n\t" \
    "ds_read_b64 a[110:111], v0 offset:2312\n\tv_mfma_f32_16x16x32_bf16 v[172:175], a[32:35], v[108:111], v[172:175]\n\tds_read_b64 a[112:113], v0 offset:1792\n\tds_read_b64 a[114:115], v0 offset:1800\n\tv_mfma_f32_16x16x32_bf16 v[176:179], a[32:35], v[112:115], v[176:179]\n\tds_read_b64 a[116:117], v0 offset:1280\n\tds_read_b64 a[118:119], v0 offset:1288\n\tv_mfma_f32_16x16x32_bf16 v[180:183], a[32:35], v[116:119], v[180:183]\n\t" \
    "ds_read_b64 a[120:121], v0 offset:768\n\tds_read_b64 a[122:123], v0 offset:776\n\tv_mfma_f32_16x16x32_bf16 v[184:187], a[32:35], v[120:123], v[184:187]\n\tds_read_b64 a[124:125], v0 offset:256\n\ts_add_u32 s51, s44, 8\n\tv_mfma_f32_16x16x32_bf16 v[188:191], a[32:35], v[124:127], v[188:191]\n\tds_read_b64 a[126:127], v0 offset:264\n\ts_mul_i32 s65, s51, 1366\n\t" \
    "v_mfma_f32_16x16x32_bf16 v[192:195], a[32:35], v[128:131], v[192:195]\n\tds_read_b64 a[128:129], v0 offset:3776\n\ts_lshr_b32 s65, s65, 16\n\tv_mfma_f32_16x16x32_bf16 v[168:171], a[36:39], v[100:103], v[168:171]\n\tds_read_b64 a[130:131], v0 offset:3784\n\ts_mul_i32 s56, s65, 48\n\tv_mfma_f32_16x16x32_bf16 v[172:175], a[36:39], v[104:107], v[172:175]\n\tds_read_b64 a[132:133], v0 offset:3264\n\t" \
    "s_sub_u32 s56, s51, s56\n\tv_mfma_f32_16x16x32_bf16 v[176:179], a[36:39], v[108:111], v[176:179]\n\tds_read_b64 a[134:135], v0 offset:3272\n\ts_lshl_b32 s56, s56, 6\n\tv_mfma_f32_16x16x32_bf16 v[180:183], a[36:39], v[112:115], v[180:183]\n\tds_read_b128 v[68:71], v1 offset:55808\n\ts_lshr_b32 s70, s65, 2\n\tv_mfma_f32_16x16x32_bf16 v[184:187], a[36:39], v[116:119], v[184:187]\n\t" \
    "ds_read_b128 v[72:75], v1 offset:56320\n\ts_mul_i32 s70, s70, 252\n\tv_mfma_f32_16x16x32_bf16 v[188:191], a[36:39], v[120:123], v[188:191]\n\tds_read_b128 v[76:79], v1 offset:56832\n\ts_add_u32 s65, s65, s70\n\tv_mfma_f32_16x16x32_bf16 v[192:195], a[36:39], v[124:127], v[192:195]\n\tds_read_b128 v[80:83], v1 offset:57344\n\ts_mul_i32 s65, s65, 3072\n\t" \
    "v_mfma_f32_16x16x32_bf16 v[172:175], a[40:43], v[100:103], v[172:175]\n\tds_read_b128 v[84:87], v1 offset:57856\n\ts_add_u32 s56, s56, s65\n\tv_mfma_f32_16x16x32_bf16 v[176:179], a[40:43], v[104:107], v[176:179]\n\tds_read_b128 v[88:91], v1 offset:58368\n\ts_add_u32 s56, s56, 4849664\n\tv_mfma_f32_16x16x32_bf16 v[180:183], a[40:43], v[108:111], v[180:183]\n\tds_read_b128 v[92:95], v1 offset:58880\n\t" \
    "s_add_u32 s51, s51, s38\n\tv_mfma_f32_16x16x32_bf16 v[184:187], a[40:43], v[112:115], v[184:187]\n\tds_read_b128 v[96:99], v1 offset:59392\n\ts_lshl_b32 s51, s51, 3\n\tv_mfma_f32_16x16x32_bf16 v[188:191], a[40:43], v[116:119], v[188:191]\n\tds_read_b64 a[136:137], v0 offset:2752\n\ts_add_u32 s51, s51, 50176\n\tds_read_b64_tr_b16 v[204:205], v4 offset:36992\n\t" \
    "v_mfma_f32_16x16x32_bf16 v[192:195], a[40:43], v[120:123], v[192:195]\n\tds_read_b64 a[138:139], v0 offset:2760\n\tds_read_b64_tr_b16 v[206:207], v4 offset:37000\n\tv_mfma_f32_16x16x32_bf16 v[176:179], a[44:47], v[100:103], v[176:179]\n\tds_read_b64 a[140:141], v0 offset:2240\n\tds_read_b64_tr_b16 v[208:209], v4 offset:37056\n\tv_mfma_f32_16x16x32_bf16 v[180:183], a[44:47], v[104:107], v[180:183]\n\tds_read_b64 a[142:143], v0 offset:2248\n\t" \
    "ds_read_b64_tr_b16 v[210:211], v4 offset:37064\n\tv_mfma_f32_16x16x32_bf16 v[184:187], a[44:47], v[108:111], v[184:187]\n\tds_read_b64 a[144:145], v0 offset:1728\n\tds_read_b64_tr_b16 v[212:213], v4 offset:37504\n\tv_mfma_f32_16x16x32_bf16 v[188:191], a[44:47], v[112:115], v[188:191]\n\tds_read_b64 a[146:147], v0 offset:1736\n\tds_read_b64_tr_b16 v[214:215], v4 offset:37512\n\tv_mfma_f32_16x16x32_bf16 v[192:195], a[44:47], v[116:119], v[192:195]\n\t" \
    "ds_read_b64 a[148:149], v0 offset:1216\n\tds_read_b64_tr_b16 v[216:217], v4 offset:37568\n\tv_mfma_f32_16x16x32_bf16 v[180:183], a[48:51], v[100:103], v[180:183]\n\tds_read_b64 a[150:151], v0 offset:1224\n\tds_read_b64_tr_b16 v[218:219], v4 offset:37576\n\tv_mfma_f32_16x16x32_bf16 v[184:187], a[48:51], v[104:107], v[184:187]\n\tds_read_b64 a[152:153], v0 offset:704\n\tv_mfma_f32_16x16x32_bf16 v[188:191], a[48:51], v[108:111], v[188:191]\n\t" \
    "ds_read_b64 a[154:155], v0 offset:712\n\tv_mfma_f32_16x16x32_bf16 v[192:195], a[48:51], v[112:115], v[192:195]\n\tds_read_b64 a[156:157], v0 offset:192\n\tv_mfma_f32_16x16x32_bf16 v[184:187], a[52:55], v[100:103], v[184:187]\n\tds_read_b64 a[158:159], v0 offset:200\n\tv_mfma_f32_16x16x32_bf16 v[188:191], a[52:55], v[104:107], v[188:191]\n\tv_mfma_f32_16x16x32_bf16 v[192:195], a[52:55], v[108:111], v[192:195]\n\tbuffer_load_dword v15, v7, s[16:19], s42 offen\n\t" \
    "s_waitcnt lgkmcnt(15)\n\tbuffer_store_dwordx4 v[204:207], v5, s[8:11], s56 offen sc1\n\tv_mfma_f32_16x16x32_bf16 v[188:191], a[56:59], v[100:103], v[188:191]\n\ts_add_u32 s43, s43, 1\n\ts_add_u32 s56, s56, 49152\n\tv_mfma_f32_16x16x32_bf16 v[192:195], a[56:59], v[104:107], v[192:195]\n\tv_readlane_b32 s42, v237, s43\n\ts_nop 4\n\t" \
    "v_mfma_f32_16x16x32_bf16 v[192:195], a[60:63], v[100:103], v[192:195]\n\tv_mfma_f32_16x16x32_bf16 v[164:167], a[64:67], v[68:71], v[164:167]\n\tds_read_b64 a[160:161], v0 offset:3712\n\tds_read_b64 a[162:163], v0 offset:3720\n\tv_mfma_f32_16x16x32_bf16 v[168:171], a[64:67], v[72:75], v[168:171]\n\tds_read_b64 a[164:165], v0 offset:3200\n\tds_read_b64 a[166:167], v0 offset:3208\n\tv_mfma_f32_16x16x32_bf16 v[172:175], a[64:67], v[76:79], v[172:175]\n\t" \
    "ds_read_b64 a[168:169], v0 offset:2688\n\tds_read_b64 a[170:171], v0 offset:2696\n\tv_mfma_f32_16x16x32_bf16 v[176:179], a[64:67], v[80:83], v[176:179]\n\tds_read_b64 a[172:173], v0 offset:2176\n\tds_read_b64 a[174:175], v0 offset:2184\n\tv_mfma_f32_16x16x32_bf16 v[180:183], a[64:67], v[84:87], v[180:183]\n\tds_read_b64 a[176:177], v0 offset:1664\n\tds_read_b64 a[178:179], v0 offset:1672\n\t" \
    "v_mfma_f32_16x16x32_bf16 v[184:187], a[64:67], v[88:91], v[184:187]\n\tds_read_b64 a[180:181], v0 offset:1152\n\tv_mfma_f32_16x16x32_bf16 v[188:191], a[64:67], v[92:95], v[188:191]\n\tds_read_b64 a[182:183], v0 offset:1160\n\tv_mfma_f32_16x16x32_bf16 v[192:195], a[64:67], v[96:99], v[192:195]\n\tds_read_b64 a[184:185], v0 offset:640\n\tv_mfma_f32_16x16x32_bf16 v[168:171], a[68:71], v[68:71], v[168:171]\n\tds_read_b64 a[186:187], v0 offset:648\n\t" \
    "v_mfma_f32_16x16x32_bf16 v[172:175], a[68:71], v[72:75], v[172:175]\n\tds_read_b64 a[188:189], v0 offset:128\n\tv_mfma_f32_16x16x32_bf16 v[176:179], a[68:71], v[76:79], v[176:179]\n\tds_read_b64 a[190:191], v0 offset:136\n\tv_mfma_f32_16x16x32_bf16 v[180:183], a[68:71], v[80:83], v[180:183]\n\tds_read_b128 v[100:103], v1 offset:55744\n\tv_mfma_f32_16x16x32_bf16 v[184:187], a[68:71], v[84:87], v[184:187]\n\tds_read_b128 v[104:107], v1 offset:56256\n\t" \
    "v_mfma_f32_16x16x32_bf16 v[188:191], a[68:71], v[88:91], v[188:191]\n\tds_read_b128 v[108:111], v1 offset:56768\n\tv_mfma_f32_16x16x32_bf16 v[192:195], a[68:71], v[92:95], v[192:195]\n\tds_read_b128 v[112:115], v1 offset:57280\n\tv_mfma_f32_16x16x32_bf16 v[172:175], a[72:75], v[68:71], v[172:175]\n\tds_read_b128 v[116:119], v1 offset:57792\n\ts_waitcnt lgkmcnt(15)\n\tbuffer_store_dwordx4 v[208:211], v5, s[8:11], s56 offen sc1\n\t" \
    "v_mfma_f32_16x16x32_bf16 v[176:179], a[72:75], v[72:75], v[176:179]\n\tds_read_b128 v[120:123], v1 offset:58304\n\ts_add_u32 s56, s56, 49152\n\tv_mfma_f32_16x16x32_bf16 v[180:183], a[72:75], v[76:79], v[180:183]\n\tds_read_b128 v[124:127], v1 offset:58816\n\tv_mfma_f32_16x16x32_bf16 v[184:187], a[72:75], v[80:83], v[184:187]\n\tds_read_b128 v[128:131], v1 offset:59328\n\tv_mfma_f32_16x16x32_bf16 v[188:191], a[72:75], v[84:87], v[188:191]\n\t" \
    "ds_read_b64 a[192:193], v0 offset:3648\n\tv_mfma_f32_16x16x32_bf16 v[192:195], a[72:75], v[88:91], v[192:195]\n\tds_read_b64 a[194:195], v0 offset:3656\n\tv_mfma_f32_16x16x32_bf16 v[176:179], a[76:79], v[68:71], v[176:179]\n\tds_read_b64 a[196:197], v0 offset:3136\n\tv_mfma_f32_16x16x32_bf16 v[180:183], a[76:79], v[72:75], v[180:183]\n\tds_read_b64 a[198:199], v0 offset:3144\n\tv_mfma_f32_16x16x32_bf16 v[184:187], a[76:79], v[76:79], v[184:187]\n\t" \
    "ds_read_b64 a[200:201], v0 offset:2624\n\tv_mfma_f32_16x16x32_bf16 v[188:191], a[76:79], v[80:83], v[188:191]\n\tds_read_b64 a[202:203], v0 offset:2632\n\tv_mfma_f32_16x16x32_bf16 v[192:195], a[76:79], v[84:87], v[192:195]\n\tds_read_b64 a[204:205], v0 offset:2112\n\tv_mfma_f32_16x16x32_bf16 v[180:183], a[80:83], v[68:71], v[180:183]\n\tds_read_b64 a[206:207], v0 offset:2120\n\tv_mfma_f32_16x16x32_bf16 v[184:187], a[80:83], v[72:75], v[184:187]\n\t" \
    "ds_read_b64 a[208:209], v0 offset:1600\n\tv_mfma_f32_16x16x32_bf16 v[188:191], a[80:83], v[76:79], v[188:191]\n\tds_read_b64 a[210:211], v0 offset:1608\n\tv_mfma_f32_16x16x32_bf16 v[192:195], a[80:83], v[80:83], v[192:195]\n\tds_read_b64 a[212:213], v0 offset:1088\n\tv_mfma_f32_16x16x32_bf16 v[184:187], a[84:87], v[68:71], v[184:187]\n\tds_read_b64 a[214:215], v0 offset:1096\n\tv_mfma_f32_16x16x32_bf16 v[188:191], a[84:87], v[72:75], v[188:191]\n\t" \
    "buffer_store_dwordx4 v[212:215], v5, s[8:11], s56 offen sc1\n\tv_mfma_f32_16x16x32_bf16 v[192:195], a[84:87], v[76:79], v[192:195]\n\ts_add_u32 s56, s56, 49152\n\tv_mfma_f32_16x16x32_bf16 v[188:191], a[88:91], v[68:71], v[188:191]\n\tv_mfma_f32_16x16x32_bf16 v[192:195], a[88:91], v[72:75], v[192:195]\n\tv_mfma_f32_16x16x32_bf16 v[192:195], a[92:95], v[68:71], v[192:195]\n\ts_waitcnt lgkmcnt(12)\n\tv_mfma_f32_16x16x32_bf16 v[164:167], a[96:99], v[100:103], v[164:167]\n\t" \
    "ds_read_b64 a[216:217], v0 offset:576\n\tv_mfma_f32_16x16x32_bf16 v[168:171], a[96:99], v[104:107], v[168:171]\n\tds_read_b64 a[218:219], v0 offset:584\n\tv_mfma_f32_16x16x32_bf16 v[172:175], a[96:99], v[108:111], v[172:175]\n\tds_read_b64 a[220:221], v0 offset:64\n\tv_mfma_f32_16x16x32_bf16 v[176:179], a[96:99], v[112:115], v[176:179]\n\tds_read_b64 a[222:223], v0 offset:72\n\tv_mfma_f32_16x16x32_bf16 v[180:183], a[96:99], v[116:119], v[180:183]\n\t" \
    "ds_read_b64 a[224:225], v0 offset:3584\n\tv_mfma_f32_16x16x32_bf16 v[184:187], a[96:99], v[120:123], v[184:187]\n\tds_read_b64 a[226:227], v0 offset:3592\n\tv_mfma_f32_16x16x32_bf16 v[188:191], a[96:99], v[124:127], v[188:191]\n\tds_read_b64 a[228:229], v0 offset:3072\n\tv_mfma_f32_16x16x32_bf16 v[192:195], a[96:99], v[128:131], v[192:195]\n\tds_read_b64 a[230:231], v0 offset:3080\n\tv_mfma_f32_16x16x32_bf16 v[168:171], a[100:103], v[100:103], v[168:171]\n\t" \
    "ds_read_b64 a[232:233], v0 offset:2560\n\tbuffer_store_dwordx4 v[216:219], v5, s[8:11], s56 offen sc1\n\tv_mfma_f32_16x16x32_bf16 v[172:175], a[100:103], v[104:107], v[172:175]\n\tds_read_b64 a[234:235], v0 offset:2568\n\ts_add_u32 s56, s56, 49152\n\tv_mfma_f32_16x16x32_bf16 v[176:179], a[100:103], v[108:111], v[176:179]\n\tds_read_b64 a[236:237], v0 offset:2048\n\tv_mfma_f32_16x16x32_bf16 v[180:183], a[100:103], v[112:115], v[180:183]\n\t" \
    "ds_read_b64 a[238:239], v0 offset:2056\n\tv_mfma_f32_16x16x32_bf16 v[184:187], a[100:103], v[116:119], v[184:187]\n\tds_read_b64 a[240:241], v0 offset:1536\n\tv_mfma_f32_16x16x32_bf16 v[188:191], a[100:103], v[120:123], v[188:191]\n\tds_read_b64 a[242:243], v0 offset:1544\n\tv_mfma_f32_16x16x32_bf16 v[192:195], a[100:103], v[124:127], v[192:195]\n\tds_read_b64 a[244:245], v0 offset:1024\n\tv_mfma_f32_16x16x32_bf16 v[172:175], a[104:107], v[100:103], v[172:175]\n\t" \
    "ds_read_b64 a[246:247], v0 offset:1032\n\tv_mfma_f32_16x16x32_bf16 v[176:179], a[104:107], v[104:107], v[176:179]\n\tds_read_b128 v[68:71], v1 offset:55680\n\tv_mfma_f32_16x16x32_bf16 v[180:183], a[104:107], v[108:111], v[180:183]\n\tds_read_b128 v[72:75], v1 offset:56192\n\tv_mfma_f32_16x16x32_bf16 v[184:187], a[104:107], v[112:115], v[184:187]\n\tds_read_b128 v[76:79], v1 offset:56704\n\tv_mfma_f32_16x16x32_bf16 v[188:191], a[104:107], v[116:119], v[188:191]\n\t" \
    "ds_read_b128 v[80:83], v1 offset:57216\n\tv_mfma_f32_16x16x32_bf16 v[192:195], a[104:107], v[120:123], v[192:195]\n\tbuffer_load_dword v15, v7, s[16:19], s42 offen\n\tds_read_b128 v[84:87], v1 offset:57728\n\tv_mfma_f32_16x16x32_bf16 v[176:179], a[108:111], v[100:103], v[176:179]\n\ts_add_u32 s43, s43, 1\n\tds_read_b128 v[88:91], v1 offset:58240\n\tv_mfma_f32_16x16x32_bf16 v[180:183], a[108:111], v[104:107], v[180:183]\n\t" \
    "v_readlane_b32 s42, v237, s43\n\ts_nop 4\n\tds_read_b128 v[92:95], v1 offset:58752\n\tv_mfma_f32_16x16x32_bf16 v[184:187], a[108:111], v[108:111], v[184:187]\n\tds_read_b128 v[96:99], v1 offset:59264\n\tv_mfma_f32_16x16x32_bf16 v[188:191], a[108:111], v[112:115], v[188:191]\n\tds_read_b64 a[248:249], v0 offset:512\n\tv_mfma_f32_16x16x32_bf16 v[192:195], a[108:111], v[116:119], v[192:195]\n\t" \
    "ds_read_b64 a[250:251], v0 offset:520\n\tv_mfma_f32_16x16x32_bf16 v[180:183], a[112:115], v[100:103], v[180:183]\n\tds_read_b64 a[252:253], v0 offset:0\n\tv_mfma_f32_16x16x32_bf16 v[184:187], a[112:115], v[104:107], v[184:187]\n\tds_read_b64 a[254:255], v0 offset:8\n\tv_mfma_f32_16x16x32_bf16 v[188:191], a[112:115], v[108:111], v[188:191]\n\tv_mfma_f32_16x16x32_bf16 v[192:195], a[112:115], v[112:115], v[192:195]\n\tv_mfma_f32_16x16x32_bf16 v[184:187], a[116:119], v[100:103], v[184:187]\n\t" \
    "v_mfma_f32_16x16x32_bf16 v[188:191], a[116:119], v[104:107], v[188:191]\n\tv_mfma_f32_16x16x32_bf16 v[192:195], a[116:119], v[108:111], v[192:195]\n\tv_mfma_f32_16x16x32_bf16 v[188:191], a[120:123], v[100:103], v[188:191]\n\tv_mfma_f32_16x16x32_bf16 v[192:195], a[120:123], v[104:107], v[192:195]\n\tv_mfma_f32_16x16x32_bf16 v[192:195], a[124:127], v[100:103], v[192:195]\n\ts_waitcnt lgkmcnt(4)\n\tv_mfma_f32_16x16x32_bf16 v[164:167], a[128:131], v[68:71], v[164:167]\n\tds_read_b128 v[100:103], v1 offset:55616\n\t" \
    "v_mfma_f32_16x16x32_bf16 v[168:171], a[128:131], v[72:75], v[168:171]\n\tds_read_b128 v[104:107], v1 offset:56128\n\tv_mfma_f32_16x16x32_bf16 v[172:175], a[128:131], v[76:79], v[172:175]\n\tds_read_b128 v[108:111], v1 offset:56640\n\tv_mfma_f32_16x16x32_bf16 v[176:179], a[128:131], v[80:83], v[176:179]\n\tds_read_b128 v[112:115], v1 offset:57152\n\tv_mfma_f32_16x16x32_bf16 v[180:183], a[128:131], v[84:87], v[180:183]\n\tds_read_b128 v[116:119], v1 offset:57664\n\t" \
    "v_mfma_f32_16x16x32_bf16 v[184:187], a[128:131], v[88:91], v[184:187]\n\tds_read_b128 v[120:123], v1 offset:58176\n\tv_mfma_f32_16x16x32_bf16 v[188:191], a[128:131], v[92:95], v[188:191]\n\tds_read_b128 v[124:127], v1 offset:58688\n\tv_mfma_f32_16x16x32_bf16 v[192:195], a[128:131], v[96:99], v[192:195]\n\tds_read_b128 v[128:131], v1 offset:59200\n\tv_mfma_f32_16x16x32_bf16 v[168:171], a[132:135], v[68:71], v[168:171]\n\tv_mfma_f32_16x16x32_bf16 v[172:175], a[132:135], v[72:75], v[172:175]\n\t" \
    "v_mfma_f32_16x16x32_bf16 v[176:179], a[132:135], v[76:79], v[176:179]\n\tv_mfma_f32_16x16x32_bf16 v[180:183], a[132:135], v[80:83], v[180:183]\n\tv_mfma_f32_16x16x32_bf16 v[184:187], a[132:135], v[84:87], v[184:187]\n\tv_mfma_f32_16x16x32_bf16 v[188:191], a[132:135], v[88:91], v[188:191]\n\tv_mfma_f32_16x16x32_bf16 v[192:195], a[132:135], v[92:95], v[192:195]\n\tv_mfma_f32_16x16x32_bf16 v[172:175], a[136:139], v[68:71], v[172:175]\n\tv_mfma_f32_16x16x32_bf16 v[176:179], a[136:139], v[72:75], v[176:179]\n\tv_mfma_f32_16x16x32_bf16 v[180:183], a[136:139], v[76:79], v[180:183]\n\t" \
    "v_mfma_f32_16x16x32_bf16 v[184:187], a[136:139], v[80:83], v[184:187]\n\tv_mfma_f32_16x16x32_bf16 v[188:191], a[136:139], v[84:87], v[188:191]\n\tv_mfma_f32_16x16x32_bf16 v[192:195], a[136:139], v[88:91], v[192:195]\n\tv_mfma_f32_16x16x32_bf16 v[176:179], a[140:143], v[68:71], v[176:179]\n\tv_mfma_f32_16x16x32_bf16 v[180:183], a[140:143], v[72:75], v[180:183]\n\tv_mfma_f32_16x16x32_bf16 v[184:187], a[140:143], v[76:79], v[184:187]\n\tv_mfma_f32_16x16x32_bf16 v[188:191], a[140:143], v[80:83], v[188:191]\n\tv_mfma_f32_16x16x32_bf16 v[192:195], a[140:143], v[84:87], v[192:195]\n\t" \
    "v_mfma_f32_16x16x32_bf16 v[180:183], a[144:147], v[68:71], v[180:183]\n\tv_mfma_f32_16x16x32_bf16 v[184:187], a[144:147], v[72:75], v[184:187]\n\tv_mfma_f32_16x16x32_bf16 v[188:191], a[144:147], v[76:79], v[188:191]\n\tv_mfma_f32_16x16x32_bf16 v[192:195], a[144:147], v[80:83], v[192:195]\n\tv_mfma_f32_16x16x32_bf16 v[184:187], a[148:151], v[68:71], v[184:187]\n\tv_mfma_f32_16x16x32_bf16 v[188:191], a[148:151], v[72:75], v[188:191]\n\tv_mfma_f32_16x16x32_bf16 v[192:195], a[148:151], v[76:79], v[192:195]\n\tv_mfma_f32_16x16x32_bf16 v[188:191], a[152:155], v[68:71], v[188:191]\n\t" \
    "v_mfma_f32_16x16x32_bf16 v[192:195], a[152:155], v[72:75], v[192:195]\n\tv_mfma_f32_16x16x32_bf16 v[192:195], a[156:159], v[68:71], v[192:195]\n\ts_waitcnt lgkmcnt(0)\n\tv_mfma_f32_16x16x32_bf16 v[164:167], a[160:163], v[100:103], v[164:167]\n\tds_read_b128 v[68:71], v1 offset:55552\n\tv_mfma_f32_16x16x32_bf16 v[168:171], a[160:163], v[104:107], v[168:171]\n\tds_read_b128 v[72:75], v1 offset:56064\n\tv_mfma_f32_16x16x32_bf16 v[172:175], a[160:163], v[108:111], v[172:175]\n\t" \
    "ds_read_b128 v[76:79], v1 offset:56576\n\tv_mfma_f32_16x16x32_bf16 v[176:179], a[160:163], v[112:115], v[176:179]\n\tds_read_b128 v[80:83], v1 offset:57088\n\tv_mfma_f32_16x16x32_bf16 v[180:183], a[160:163], v[116:119], v[180:183]\n\tds_read_b128 v[84:87], v1 offset:57600\n\tv_mfma_f32_16x16x32_bf16 v[184:187], a[160:163], v[120:123], v[184:187]\n\tds_read_b128 v[88:91], v1 offset:58112\n\tv_mfma_f32_16x16x32_bf16 v[188:191], a[160:163], v[124:127], v[188:191]\n\t" \
    "ds_read_b128 v[92:95], v1 offset:58624\n\tv_mfma_f32_16x16x32_bf16 v[192:195], a[160:163], v[128:131], v[192:195]\n\tds_read_b128 v[96:99], v1 offset:59136\n\tv_mfma_f32_16x16x32_bf16 v[168:171], a[164:167], v[100:103], v[168:171]\n\tv_mfma_f32_16x16x32_bf16 v[172:175], a[164:167], v[104:107], v[172:175]\n\tv_mfma_f32_16x16x32_bf16 v[176:179], a[164:167], v[108:111], v[176:179]\n\tv_mfma_f32_16x16x32_bf16 v[180:183], a[164:167], v[112:115], v[180:183]\n\tv_mfma_f32_16x16x32_bf16 v[184:187], a[164:167], v[116:119], v[184:187]\n\t" \
    "v_mfma_f32_16x16x32_bf16 v[188:191], a[164:167], v[120:123], v[188:191]\n\tv_mfma_f32_16x16x32_bf16 v[192:195], a[164:167], v[124:127], v[192:195]\n\tv_mfma_f32_16x16x32_bf16 v[172:175], a[168:171], v[100:103], v[172:175]\n\tv_mfma_f32_16x16x32_bf16 v[176:179], a[168:171], v[104:107], v[176:179]\n\tv_mfma_f32_16x16x32_bf16 v[180:183], a[168:171], v[108:111], v[180:183]\n\tv_mfma_f32_16x16x32_bf16 v[184:187], a[168:171], v[112:115], v[184:187]\n\tv_mfma_f32_16x16x32_bf16 v[188:191], a[168:171], v[116:119], v[188:191]\n\tv_mfma_f32_16x16x32_bf16 v[192:195], a[168:171], v[120:123], v[192:195]\n\t" \
    "v_mfma_f32_16x16x32_bf16 v[176:179], a[172:175], v[100:103], v[176:179]\n\tv_mfma_f32_16x16x32_bf16 v[180:183], a[172:175], v[104:107], v[180:183]\n\tv_mfma_f32_16x16x32_bf16 v[184:187], a[172:175], v[108:111], v[184:187]\n\tv_mfma_f32_16x16x32_bf16 v[188:191], a[172:175], v[112:115], v[188:191]\n\tv_mfma_f32_16x16x32_bf16 v[192:195], a[172:175], v[116:119], v[192:195]\n\tv_mfma_f32_16x16x32_bf16 v[180:183], a[176:179], v[100:103], v[180:183]\n\tv_mfma_f32_16x16x32_bf16 v[184:187], a[176:179], v[104:107], v[184:187]\n\tv_mfma_f32_16x16x32_bf16 v[188:191], a[176:179], v[108:111], v[188:191]\n\t" \
    "v_mfma_f32_16x16x32_bf16 v[192:195], a[176:179], v[112:115], v[192:195]\n\tv_mfma_f32_16x16x32_bf16 v[184:187], a[180:183], v[100:103], v[184:187]\n\tv_mfma_f32_16x16x32_bf16 v[188:191], a[180:183], v[104:107], v[188:191]\n\tv_mfma_f32_16x16x32_bf16 v[192:195], a[180:183], v[108:111], v[192:195]\n\tv_mfma_f32_16x16x32_bf16 v[188:191], a[184:187], v[100:103], v[188:191]\n\tv_mfma_f32_16x16x32_bf16 v[192:195], a[184:187], v[104:107], v[192:195]\n\tv_mfma_f32_16x16x32_bf16 v[192:195], a[188:191], v[100:103], v[192:195]\n\ts_waitcnt lgkmcnt(0)\n\t" \
    "v_mfma_f32_16x16x32_bf16 v[164:167], a[192:195], v[68:71], v[164:167]\n\tds_read_b128 v[100:103], v1 offset:55488\n\tv_mfma_f32_16x16x32_bf16 v[168:171], a[192:195], v[72:75], v[168:171]\n\tds_read_b128 v[104:107], v1 offset:56000\n\tv_mfma_f32_16x16x32_bf16 v[172:175], a[192:195], v[76:79], v[172:175]\n\tds_read_b128 v[108:111], v1 offset:56512\n\tv_mfma_f32_16x16x32_bf16 v[176:179], a[192:195], v[80:83], v[176:179]\n\tds_read_b128 v[112:115], v1 offset:57024\n\t" \
    "v_mfma_f32_16x16x32_bf16 v[180:183], a[192:195], v[84:87], v[180:183]\n\tds_read_b128 v[116:119], v1 offset:57536\n\tv_mfma_f32_16x16x32_bf16 v[184:187], a[192:195], v[88:91], v[184:187]\n\tds_read_b128 v[120:123], v1 offset:58048\n\tv_mfma_f32_16x16x32_bf16 v[188:191], a[192:195], v[92:95], v[188:191]\n\tds_read_b128 v[124:127], v1 offset:58560\n\tv_mfma_f32_16x16x32_bf16 v[192:195], a[192:195], v[96:99], v[192:195]\n\tds_read_b128 v[128:131], v1 offset:59072\n\t" \
    "v_mfma_f32_16x16x32_bf16 v[168:171], a[196:199], v[68:71], v[168:171]\n\tv_mfma_f32_16x16x32_bf16 v[172:175], a[196:199], v[72:75], v[172:175]\n\tv_mfma_f32_16x16x32_bf16 v[176:179], a[196:199], v[76:79], v[176:179]\n\tv_mfma_f32_16x16x32_bf16 v[180:183], a[196:199], v[80:83], v[180:183]\n\tv_mfma_f32_16x16x32_bf16 v[184:187], a[196:199], v[84:87], v[184:187]\n\tv_mfma_f32_16x16x32_bf16 v[188:191], a[196:199], v[88:91], v[188:191]\n\tv_mfma_f32_16x16x32_bf16 v[192:195], a[196:199], v[92:95], v[192:195]\n\tv_mfma_f32_16x16x32_bf16 v[172:175], a[200:203], v[68:71], v[172:175]\n\t" \
    "v_mfma_f32_16x16x32_bf16 v[176:179], a[200:203], v[72:75], v[176:179]\n\tv_mfma_f32_16x16x32_bf16 v[180:183], a[200:203], v[76:79], v[180:183]\n\tv_mfma_f32_16x16x32_bf16 v[184:187], a[200:203], v[80:83], v[184:187]\n\tv_mfma_f32_16x16x32_bf16 v[188:191], a[200:203], v[84:87], v[188:191]\n\tv_mfma_f32_16x16x32_bf16 v[192:195], a[200:203], v[88:91], v[192:195]\n\tv_mfma_f32_16x16x32_bf16 v[176:179], a[204:207], v[68:71], v[176:179]\n\tv_mfma_f32_16x16x32_bf16 v[180:183], a[204:207], v[72:75], v[180:183]\n\tv_mfma_f32_16x16x32_bf16 v[184:187], a[204:207], v[76:79], v[184:187]\n\t" \
    "v_mfma_f32_16x16x32_bf16 v[188:191], a[204:207], v[80:83], v[188:191]\n\tv_mfma_f32_16x16x32_bf16 v[192:195], a[204:207], v[84:87], v[192:195]\n\tv_mfma_f32_16x16x32_bf16 v[180:183], a[208:211], v[68:71], v[180:183]\n\tv_mfma_f32_16x16x32_bf16 v[184:187], a[208:211], v[72:75], v[184:187]\n\tv_mfma_f32_16x16x32_bf16 v[188:191], a[208:211], v[76:79], v[188:191]\n\tv_mfma_f32_16x16x32_bf16 v[192:195], a[208:211], v[80:83], v[192:195]\n\tv_mfma_f32_16x16x32_bf16 v[184:187], a[212:215], v[68:71], v[184:187]\n\tv_mfma_f32_16x16x32_bf16 v[188:191], a[212:215], v[72:75], v[188:191]\n\t" \
    "v_mfma_f32_16x16x32_bf16 v[192:195], a[212:215], v[76:79], v[192:195]\n\tv_mfma_f32_16x16x32_bf16 v[188:191], a[216:219], v[68:71], v[188:191]\n\tv_mfma_f32_16x16x32_bf16 v[192:195], a[216:219], v[72:75], v[192:195]\n\tv_mfma_f32_16x16x32_bf16 v[192:195], a[220:223], v[68:71], v[192:195]\n\ts_waitcnt lgkmcnt(0)\n\tv_mfma_f32_16x16x32_bf16 v[164:167], a[224:227], v[100:103], v[164:167]\n\ts_waitcnt vmcnt(1)\n\tbuffer_store_dwordx2 v[248:249], off, s[8:11], s51 sc1\n\t" \
    "v_mfma_f32_16x16x32_bf16 v[168:171], a[224:227], v[104:107], v[168:171]\n\tv_mfma_f32_16x16x32_bf16 v[172:175], a[224:227], v[108:111], v[172:175]\n\tv_mfma_f32_16x16x32_bf16 v[176:179], a[224:227], v[112:115], v[176:179]\n\tv_mfma_f32_16x16x32_bf16 v[180:183], a[224:227], v[116:119], v[180:183]\n\tv_mfma_f32_16x16x32_bf16 v[184:187], a[224:227], v[120:123], v[184:187]\n\tv_mfma_f32_16x16x32_bf16 v[188:191], a[224:227], v[124:127], v[188:191]\n\tv_mfma_f32_16x16x32_bf16 v[192:195], a[224:227], v[128:131], v[192:195]\n\tv_mfma_f32_16x16x32_bf16 v[168:171], a[228:231], v[100:103], v[168:171]\n\t" \
    "v_mfma_f32_16x16x32_bf16 v[172:175], a[228:231], v[104:107], v[172:175]\n\tv_mfma_f32_16x16x32_bf16 v[176:179], a[228:231], v[108:111], v[176:179]\n\tv_mfma_f32_16x16x32_bf16 v[180:183], a[228:231], v[112:115], v[180:183]\n\tv_mfma_f32_16x16x32_bf16 v[184:187], a[228:231], v[116:119], v[184:187]\n\tv_mfma_f32_16x16x32_bf16 v[188:191], a[228:231], v[120:123], v[188:191]\n\tv_mfma_f32_16x16x32_bf16 v[192:195], a[228:231], v[124:127], v[192:195]\n\tv_mfma_f32_16x16x32_bf16 v[172:175], a[232:235], v[100:103], v[172:175]\n\tv_mfma_f32_16x16x32_bf16 v[176:179], a[232:235], v[104:107], v[176:179]\n\t" \
    "v_mfma_f32_16x16x32_bf16 v[180:183], a[232:235], v[108:111], v[180:183]\n\tv_mfma_f32_16x16x32_bf16 v[184:187], a[232:235], v[112:115], v[184:187]\n\tv_mfma_f32_16x16x32_bf16 v[188:191], a[232:235], v[116:119], v[188:191]\n\tv_mfma_f32_16x16x32_bf16 v[192:195], a[232:235], v[120:123], v[192:195]\n\tv_mfma_f32_16x16x32_bf16 v[176:179], a[236:239], v[100:103], v[176:179]\n\tv_mfma_f32_16x16x32_bf16 v[180:183], a[236:239], v[104:107], v[180:183]\n\tv_mfma_f32_16x16x32_bf16 v[184:187], a[236:239], v[108:111], v[184:187]\n\tv_mfma_f32_16x16x32_bf16 v[188:191], a[236:239], v[112:115], v[188:191]\n\t" \
    "v_mfma_f32_16x16x32_bf16 v[192:195], a[236:239], v[116:119], v[192:195]\n\tv_mfma_f32_16x16x32_bf16 v[180:183], a[240:243], v[100:103], v[180:183]\n\tv_mfma_f32_16x16x32_bf16 v[184:187], a[240:243], v[104:107], v[184:187]\n\tv_mfma_f32_16x16x32_bf16 v[188:191], a[240:243], v[108:111], v[188:191]\n\tv_mfma_f32_16x16x32_bf16 v[192:195], a[240:243], v[112:115], v[192:195]\n\tv_mfma_f32_16x16x32_bf16 v[184:187], a[244:247], v[100:103], v[184:187]\n\tv_mfma_f32_16x16x32_bf16 v[188:191], a[244:247], v[104:107], v[188:191]\n\tv_mfma_f32_16x16x32_bf16 v[192:195], a[244:247], v[108:111], v[192:195]\n\t" \
    "v_mfma_f32_16x16x32_bf16 v[188:191], a[248:251], v[100:103], v[188:191]\n\tv_mfma_f32_16x16x32_bf16 v[192:195], a[248:251], v[104:107], v[192:195]\n\tv_mfma_f32_16x16x32_bf16 v[192:195], a[252:255], v[100:103], v[192:195]\n\tv_cvt_pk_bf16_f32 v164, v164, v165\n\tv_cvt_pk_bf16_f32 v165, v166, v167\n\tds_write_b64 v3, v[164:165] offset:55488\n\tv_cvt_pk_bf16_f32 v168, v168, v169\n\tv_cvt_pk_bf16_f32 v169, v170, v171\n\t" \
    "ds_write_b64 v3, v[168:169] offset:56000\n\tv_cvt_pk_bf16_f32 v172, v172, v173\n\tv_cvt_pk_bf16_f32 v173, v174, v175\n\tds_write_b64 v3, v[172:173] offset:56512\n\tv_cvt_pk_bf16_f32 v176, v176, v177\n\tv_cvt_pk_bf16_f32 v177, v178, v179\n\tds_write_b64 v3, v[176:177] offset:57024\n\tv_cvt_pk_bf16_f32 v180, v180, v181\n\t" \
    "v_cvt_pk_bf16_f32 v181, v182, v183\n\tds_write_b64 v3, v[180:181] offset:57536\n\tv_cvt_pk_bf16_f32 v184, v184, v185\n\tv_cvt_pk_bf16_f32 v185, v186, v187\n\tds_write_b64 v3, v[184:185] offset:58048\n\tv_cvt_pk_bf16_f32 v188, v188, v189\n\tv_cvt_pk_bf16_f32 v189, v190, v191\n\tds_write_b64 v3, v[188:189] offset:58560\n\t" \
    "v_cvt_pk_bf16_f32 v192, v192, v193\n\tv_cvt_pk_bf16_f32 v193, v194, v195\n\tds_write_b64 v3, v[192:193] offset:59072\n\ts_waitcnt lgkmcnt(0)\n\ts_barrier\n\ts_add_u32 s51, s44, 12\n\ts_mul_i32 s65, s51, 1366\n\ts_lshr_b32 s65, s65, 16\n\t" \
    "s_mul_i32 s56, s65, 48\n\ts_sub_u32 s56, s51, s56\n\ts_lshl_b32 s56, s56, 6\n\ts_lshr_b32 s70, s65, 2\n\ts_mul_i32 s70, s70, 252\n\ts_add_u32 s65, s65, s70\n\ts_mul_i32 s65, s65, 3072\n\ts_add_u32 s56, s56, s65\n\t" \
    "s_add_u32 s56, s56, 4849664\n\ts_add_u32 s51, s51, s38\n\ts_lshl_b32 s51, s51, 3\n\ts_add_u32 s51, s51, 50176\n\ts_or_b32 s56, s56, s45\n\ts_or_b32 s51, s51, s45\n\tds_read_b64_tr_b16 v[204:205], v4 offset:55488\n\tds_read_b64_tr_b16 v[206:207], v4 offset:55496\n\t" \
    "ds_read_b64_tr_b16 v[208:209], v4 offset:55552\n\tds_read_b64_tr_b16 v[210:211], v4 offset:55560\n\tds_read_b64_tr_b16 v[212:213], v4 offset:56000\n\tds_read_b64_tr_b16 v[214:215], v4 offset:56008\n\tds_read_b64_tr_b16 v[216:217], v4 offset:56064\n\tds_read_b64_tr_b16 v[218:219], v4 offset:56072\n\ts_waitcnt lgkmcnt(6)\n\tbuffer_store_dwordx4 v[204:207], v5, s[8:11], s56 offen sc1\n\t" \
    "s_add_u32 s56, s56, 49152\n\ts_waitcnt lgkmcnt(4)\n\tbuffer_store_dwordx4 v[208:211], v5, s[8:11], s56 offen sc1\n\ts_add_u32 s56, s56, 49152\n\ts_waitcnt lgkmcnt(2)\n\tbuffer_store_dwordx4 v[212:215], v5, s[8:11], s56 offen sc1\n\ts_add_u32 s56, s56, 49152\n\ts_waitcnt lgkmcnt(0)\n\t" \
    "buffer_store_dwordx4 v[216:219], v5, s[8:11], s56 offen sc1\n\ts_add_u32 s56, s56, 49152\n\ts_waitcnt vmcnt(0)\n\tbuffer_store_dwordx2 v[248:249], off, s[8:11], s51 sc1\n\ts_waitcnt vmcnt(0)\n\t103:\n\t98:\n\ts_nop 1\n\t" \
    ""
#define MAIN_CLOB "s2", "s3", "s4", "s5", "s6", "s7", "s8", "s9", "s10", "s11", "s12", "s13", "s14", "s15", "s16", "s17", "s18", "s19", "s20", "s21", "s22", "s23", "s24", "s25", "s26", "s27", "s28", "s29", "s30", "s31", "s33", "s34", "s35", "s36", "s37", "s39", "s41", "s42", "s43", "s44", "s45", "s46", "s47", "s48", "s49", "s50", "s51", "s52", "s53", "s54", "s55", "s56", "s57", "s58", "s59", "s60", "s61", "s62", "s63", "s64", "s65", "s66", "s67", "s68", "s69", "s70", "s71", "s72", "s73", "s74", "s75", "s76", "s77", "s78", "s79", "s80", "s81", "s82", "s83", "s84", "s85", "s86", "s87", "v0", "v1", "v2", "v3", "v4", "v5", "v6", "v7", "v8", "v9", "v10", "v11", "v12", "v13", "v14", "v15", "v16", "v17", "v18", "v19", "v20", "v21", "v22", "v23", "v24", "v25", "v26", "v27", "v28", "v29", "v30", "v31", "v32", "v33", "v34", "v35", "v36", "v37", "v38", "v39", "v40", "v41", "v42", "v43", "v44", "v45", "v46", "v47", "v48", "v49", "v50", "v51", "v52", "v53", "v54", "v55", "v56", "v57", "v58", "v59", "v60", "v61", "v62", "v63", "v64", "v65", "v66", "v67", "v68", "v69", "v70", "v71", "v72", "v73", "v74", "v75", "v76", "v77", "v78", "v79", "v80", "v81", "v82", "v83", "v84", "v85", "v86", "v87", "v88", "v89", "v90", "v91", "v92", "v93", "v94", "v95", "v96", "v97", "v98", "v99", "v100", "v101", "v102", "v103", "v104", "v105", "v106", "v107", "v108", "v109", "v110", "v111", "v112", "v113", "v114", "v115", "v116", "v117", "v118", "v119", "v120", "v121", "v122", "v123", "v124", "v125", "v126", "v127", "v128", "v129", "v130", "v131", "v132", "v133", "v134", "v135", "v136", "v137", "v138", "v139", "v140", "v141", "v142", "v143", "v144", "v145", "v146", "v147", "v148", "v149", "v150", "v151", "v152", "v153", "v154", "v155", "v156", "v157", "v158", "v159", "v160", "v161", "v162", "v163", "v164", "v165", "v166", "v167", "v168", "v169", "v170", "v171", "v172", "v173", "v174", "v175", "v176", "v177", "v178", "v179", "v180", "v181", "v182", "v183", "v184", "v185", "v186", "v187", "v188", "v189", "v190", "v191", "v192", "v193", "v194", "v195", "v196", "v197", "v198", "v199", "v200", "v201", "v202", "v203", "v204", "v205", "v206", "v207", "v208", "v209", "v210", "v211", "v212", "v213", "v214", "v215", "v216", "v217", "v218", "v219", "v220", "v221", "v222", "v223", "v224", "v225", "v226", "v227", "v228", "v229", "v230", "v231", "v232", "v233", "v234", "v235", "v236", "v237", "v238", "v239", "v241", "v242", "v243", "v244", "v245", "v246", "v247", "v248", "v249", "v250", "v251", "v252", "v253", "a0", "a1", "a2", "a3", "a4", "a5", "a6", "a7", "a8", "a9", "a10", "a11", "a12", "a13", "a14", "a15", "a16", "a17", "a18", "a19", "a20", "a21", "a22", "a23", "a24", "a25", "a26", "a27", "a28", "a29", "a30", "a31", "a32", "a33", "a34", "a35", "a36", "a37", "a38", "a39", "a40", "a41", "a42", "a43", "a44", "a45", "a46", "a47", "a48", "a49", "a50", "a51", "a52", "a53", "a54", "a55", "a56", "a57", "a58", "a59", "a60", "a61", "a62", "a63", "a64", "a65", "a66", "a67", "a68", "a69", "a70", "a71", "a72", "a73", "a74", "a75", "a76", "a77", "a78", "a79", "a80", "a81", "a82", "a83", "a84", "a85", "a86", "a87", "a88", "a89", "a90", "a91", "a92", "a93", "a94", "a95", "a96", "a97", "a98", "a99", "a100", "a101", "a102", "a103", "a104", "a105", "a106", "a107", "a108", "a109", "a110", "a111", "a112", "a113", "a114", "a115", "a116", "a117", "a118", "a119", "a120", "a121", "a122", "a123", "a124", "a125", "a126", "a127", "a128", "a129", "a130", "a131", "a132", "a133", "a134", "a135", "a136", "a137", "a138", "a139", "a140", "a141", "a142", "a143", "a144", "a145", "a146", "a147", "a148", "a149", "a150", "a151", "a152", "a153", "a154", "a155", "a156", "a157", "a158", "a159", "a160", "a161", "a162", "a163", "a164", "a165", "a166", "a167", "a168", "a169", "a170", "a171", "a172", "a173", "a174", "a175", "a176", "a177", "a178", "a179", "a180", "a181", "a182", "a183", "a184", "a185", "a186", "a187", "a188", "a189", "a190", "a191", "a192", "a193", "a194", "a195", "a196", "a197", "a198", "a199", "a200", "a201", "a202", "a203", "a204", "a205", "a206", "a207", "a208", "a209", "a210", "a211", "a212", "a213", "a214", "a215", "a216", "a217", "a218", "a219", "a220", "a221", "a222", "a223", "a224", "a225", "a226", "a227", "a228", "a229", "a230", "a231", "a232", "a233", "a234", "a235", "a236", "a237", "a238", "a239", "a240", "a241", "a242", "a243", "a244", "a245", "a246", "a247", "a248", "a249", "a250", "a251", "a252", "a253", "a254", "a255"

#define ARG(i) const void* p##i, unsigned n##i, unsigned f##i
__global__ __launch_bounds__(256, 1) void fused_kernel(ARG(0), ARG(1), ARG(2), ARG(3), ARG(4), ARG(5), ARG(6), ARG(7), ARG(8), ARG(9), ARG(10),
                                                       ARG(11)) {
#define MAIN_OUT
    const int b = blockIdx.x;
    unsigned tid = threadIdx.x;
    u64 ka = (u64)__builtin_amdgcn_kernarg_segment_ptr();
    int w;
    asm volatile(MAIN_ASM
                 : "=&{s38}"(w), "+{s[0:1]}"(ka), "+{v240}"(tid) MAIN_OUT
                 : "{s40}"(b)
                 : "memory", "scc", "vcc", MAIN_CLOB);
}

extern "C" void kernel_launch(void* const* d_in, const int* in_sizes, int n_in, void* d_out, int out_size,
                              void* d_ws, size_t ws_size, hipStream_t stream) {
    (void)hipFuncSetAttribute((const void*)fused_kernel, hipFuncAttributeMaxDynamicSharedMemorySize, LDS_BYTES);
    const unsigned F = 0x20000u, NX = S_LEN * D_DIM * 2, NW = S_LEN * F_DIM * 2;
    fused_kernel<<<256, 256, LDS_BYTES, stream>>>(d_ws, WS_END, F, d_in[7], NW, F, d_in[9], NW, F, d_in[0], NX, F, d_out, NX, F, d_in[8], F_DIM * 2, F,
                                                  d_in[10], S_LEN * 2, F, d_in[1], NX, F, d_in[2], NX, F, d_in[3], D_DIM * 8, F, d_in[4], NX, F,
                                                  d_in[5], D_DIM * 2, F);
}
```

```cpp
#include <hip/hip_runtime.h>

typedef unsigned long long u64;

#define S_LEN 2048
#define D_DIM 576
#define F_DIM 4608
#define LDS_BYTES 163840

#define WS_END 22347776u
#define MAIN_ASM \
    "s_load_dwordx16 s[8:23], s[0:1], 0\n\ts_getpc_b64 s[2:3]\n\t99:\n\tv_lshrrev_b32 v20, 5, v240\n\tv_mul_u32_u24 v20, 1152, v20\n\tv_and_b32 v21, 31, v240\n\tv_lshl_add_u32 v22, v21, 2, v20\n\tv_lshl_add_u32 v21, v21, 4, v20\n\t" \
    "s_mul_i32 s68, s40, 9216\n\tv_lshrrev_b32 v245, 6, v240\n\tv_and_b32 v240, 63, v240\n\ts_and_b32 s2, s2, 0xffffff80\n\ts_mov_b32 s4, s2\n\ts_mov_b32 s5, s3\n\ts_mov_b32 s6, 98f-99b\n\ts_mov_b32 s7, 0x20000\n\t" \
    "v_lshl_add_u32 v16, v245, 16, v240\n\tv_lshlrev_b32 v16, 7, v16\n\ts_lshr_b32 s65, s40, 3\n\ts_lshl_b32 s73, s65, 13\n\tbuffer_load_dword v57, v16, s[4:7], s73 offen\n\ts_waitcnt lgkmcnt(0)\n\tbuffer_load_dwordx4 v[48:51], v21, s[20:23], s68 offen\n\tbuffer_load_dwordx4 v[52:55], v21, s[20:23], s68 offen offset:512\n\t" \
    "buffer_load_dword v56, v22, s[20:23], s68 offen offset:1024\n\tbuffer_load_dwordx2 v[58:59], off, s[8:11], 0 sc1\n\ts_load_dwordx8 s[76:83], s[0:1], 112\n\ts_load_dwordx4 s[84:87], s[0:1], 144\n\ts_load_dwordx8 s[24:31], s[0:1], 64\n\ts_load_dwordx4 s[32:35], s[0:1], 96\n\tv_readfirstlane_b32 s38, v245\n\tv_lshlrev_b32 v8, 4, v240\n\t" \
    "v_and_b32 v244, 0xf0, v8\n\tv_lshrrev_b32 v245, 4, v240\n\ts_lshr_b32 s57, s38, 1\n\ts_and_b32 s58, s38, 1\n\ts_cmp_lt_u32 s65, 27\n\ts_cselect_b32 s39, 1, 0\n\ts_cmp_lt_u32 s65, 24\n\ts_cselect_b32 s39, 2, s39\n\t" \
    "s_sub_u32 s66, s65, 27\n\ts_mul_i32 s66, s66, 6\n\ts_cmp_lt_u32 s65, 27\n\ts_cselect_b32 s65, s65, s66\n\ts_and_b32 s66, s40, 7\n\ts_mul_i32 s66, s66, 27\n\ts_add_u32 s65, s65, s66\n\ts_mul_i32 s42, s65, 171\n\t" \
    "s_lshr_b32 s42, s42, 10\n\ts_mul_i32 s66, s42, 6\n\ts_sub_u32 s41, s65, s66\n\ts_mul_i32 s65, s42, 57\n\ts_lshr_b32 s65, s65, 9\n\ts_mul_i32 s65, s65, 9\n\ts_sub_u32 s65, s42, s65\n\ts_lshl_b32 s66, s41, 2\n\t" \
    "s_lshr_b32 s66, 5444640, s66\n\ts_and_b32 s66, s66, 15\n\ts_cmp_eq_u32 s65, 4\n\ts_cselect_b32 s43, s66, s41\n\ts_mul_i32 s43, s43, 688\n\ts_lshr_b32 s43, s43, 8\n\ts_and_b32 s65, s40, 7\n\ts_mul_i32 s52, s65, 0\n\t" \
    "s_mul_i32 s65, s65, 0\n\ts_lshr_b32 s65, s65, 1\n\ts_add_u32 s43, s43, s65\n\ts_cmp_ge_u32 s43, 16\n\ts_cselect_b32 s66, 16, 0\n\ts_sub_u32 s43, s43, s66\n\ts_cmp_eq_u32 s39, 0\n\ts_cselect_b32 s43, 0, s43\n\t" \
    "s_cselect_b32 s67, 4718592, 0\n\ts_sub_u32 s70, s43, 16\n\ts_mul_i32 s65, s57, 9437184\n\ts_add_u32 s67, s67, s65\n\ts_lshl_b32 s66, s42, 8\n\ts_add_u32 s67, s67, s66\n\tv_lshrrev_b32 v16, 3, v240\n\ts_lshl_b32 s73, s58, 4\n\t" \
    "v_add_u32 v16, s73, v16\n\tv_mul_u32_u24 v16, 9216, v16\n\tv_and_b32 v17, 0x70, v8\n\tv_and_b32 v18, 16, v240\n\tv_lshlrev_b32 v18, 1, v18\n\tv_xor_b32 v17, v17, v18\n\tv_add_u32 v10, v16, v17\n\tv_xor_b32 v11, 64, v10\n\t" \
    "v_add_u32 v11, 73728, v11\n\ts_lshl_b32 s72, s57, 16\n\ts_cmp_eq_u32 s39, 0\n\ts_cselect_b32 s72, 0, s72\n\ts_lshl_b32 s73, s58, 11\n\ts_add_u32 s72, s72, s73\n\ts_lshl_b32 s54, s38, 10\n\tv_add_u32 v9, 4096, v8\n\t" \
    "v_lshlrev_b32 v239, 3, v240\n\ts_mul_i32 s73, s40, 9216\n\ts_add_u32 s73, s73, s54\n\tv_add_u32 v23, s73, v8\n\tv_subrev_u32 v19, 3456, v23\n\ts_add_u32 s12, s12, s67\n\ts_addc_u32 s13, s13, 0\n\ts_sub_u32 s14, s14, s67\n\t" \
    "s_lshl_b32 s73, s58, 6\n\tv_add_u32 v14, s73, v240\n\tv_mul_u32_u24 v14, 9216, v14\n\tv_mov_b32 v15, 2146435072\n\ts_cmp_eq_u32 s39, 0\n\ts_cselect_b64 vcc, -1, 0\n\tv_cndmask_b32 v10, v10, v15, vcc\n\tv_cndmask_b32 v11, v11, v15, vcc\n\t" \
    "v_cndmask_b32 v14, v15, v14, vcc\n\tv_and_b32 v88, 31, v240\n\tv_lshrrev_b32 v89, 5, v240\n\ts_lshl_b32 s73, s38, 1\n\tv_add_u32 v89, s73, v89\n\tv_mul_u32_u24 v90, 1168, v89\n\tv_add_u32 v90, 153600, v90\n\tv_lshl_add_u32 v91, v88, 2, v90\n\t" \
    "v_lshl_add_u32 v90, v88, 4, v90\n\tv_cmp_eq_u32 vcc, 0, v88\n\tv_lshlrev_b32 v89, 3, v89\n\tv_mov_b32 v92, 0x7ff00000\n\tv_cndmask_b32 v89, v92, v89, vcc\n\tv_bfe_u32 v88, v240, 2, 2\n\tv_and_b32 v92, 3, v240\n\tv_lshlrev_b32 v92, 3, v92\n\t" \
    "v_lshl_add_u32 v92, v245, 5, v92\n\ts_lshl_b32 s73, s38, 7\n\tv_add_u32 v93, s73, v92\n\tv_mul_u32_u24 v94, 1168, v88\n\tv_add_u32 v94, 153600, v94\n\tv_add_u32 v95, v94, v92\n\tv_add_u32 v94, v94, v93\n\tv_mul_u32_u24 v88, 1152, v88\n\t" \
    "v_add_u32 v88, 131072, v88\n\tv_add_u32 v92, v88, v92\n\tv_add_u32 v93, v88, v93\n\tv_lshl_add_u32 v88, v245, 10, v244\n\ts_waitcnt lgkmcnt(0)\n\ts_lshr_b32 s61, s40, 2\n\ts_mul_i32 s61, s61, 36864\n\ts_and_b32 s65, s40, 3\n\t" \
    "s_lshl_b32 s65, s65, 8\n\ts_add_u32 s61, s61, s65\n\ts_add_u32 s61, s61, 131072\n\ts_mul_i32 s62, s40, 9216\n\ts_add_u32 s62, s62, 2490368\n\ts_lshl_b32 s63, s40, 3\n\ts_lshl_b32 s48, s38, 11\n\ts_add_u32 s48, s48, s63\n\t" \
    "s_add_u32 s48, s48, 17408\n\ts_add_u32 s63, s63, 1024\n\ts_cmp_eq_u32 s38, 0\n\ts_cselect_b32 s63, s63, 2146435072\n\ts_lshl_b32 s64, s40, 6\n\ts_add_u32 s64, s64, 58368\n\ts_add_u32 m0, s54, 131072\n\ts_nop 0\n\t" \
    "buffer_load_dwordx4 v19, s[76:79], 0 offen lds\n\ts_and_b32 s4, s40, 7\n\ts_lshl_b32 s47, s4, 11\n\ts_lshl_b32 s65, s57, 10\n\ts_add_u32 s47, s47, s65\n\tv_add_u32 v19, 4096, v19\n\ts_add_u32 m0, s54, 135168\n\ts_nop 0\n\t" \
    "buffer_load_dwordx4 v19, s[76:79], 0 offen lds\n\ts_add_u32 s47, s47, 1024\n\ts_mul_i32 s44, s57, 1179648\n\ts_mul_i32 s65, s41, 6144\n\ts_add_u32 s44, s44, s65\n\tv_add_u32 v19, 4096, v19\n\ts_add_u32 m0, s54, 139264\n\ts_nop 0\n\t" \
    "buffer_load_dwordx4 v19, s[76:79], 0 offen lds\n\ts_mul_i32 s65, s58, 3072\n\ts_add_u32 s44, s44, s65\n\ts_add_u32 s44, s44, 131072\n\ts_lshr_b32 s5, s4, 1\n\tv_add_u32 v19, 4096, v19\n\ts_add_u32 m0, s54, 144384\n\ts_nop 0\n\t" \
    "buffer_load_dwordx4 v23, s[80:83], 0 offen lds\n\tv_bfe_u32 v16, v240, 2, 2\n\tv_and_b32 v17, 3, v240\n\tv_lshlrev_b32 v17, 3, v17\n\tv_lshl_add_u32 v16, v16, 7, v17\n\tv_add_u32 v23, 4096, v23\n\ts_add_u32 m0, s54, 148480\n\ts_nop 0\n\t" \
    "buffer_load_dwordx4 v23, s[80:83], 0 offen lds\n\tv_bfe_u32 v17, v240, 3, 2\n\tv_lshl_add_u32 v16, v17, 5, v16\n\ts_mul_i32 s65, s57, 65600\n\tv_xor_b32 v17, s65, v16\n\tv_add_u32 v23, 4096, v23\n\tv_subrev_u32 v19, s54, v19\n\tv_subrev_u32 v23, s54, v23\n\t" \
    "s_cmp_eq_u32 s58, 1\n\ts_cselect_b64 vcc, -1, 0\n\ts_cselect_b64 s[76:77], s[80:81], s[76:77]\n\ts_mov_b32 m0, 143360\n\ts_cselect_b32 m0, 152576, m0\n\tv_cndmask_b32 v19, v19, v23, vcc\n\tbuffer_load_dwordx4 v19, s[76:79], 0 offen lds\n\tv_lshl_add_u32 v0, v245, 10, v17\n\t" \
    "s_and_b32 s65, s5, 1\n\ts_xor_b32 s65, s65, 1\n\ts_lshl_b32 s65, s65, 6\n\ts_lshr_b32 s71, s54, 1\n\tbuffer_load_dwordx2 v[60:61], v239, s[84:87], s71 offen\n\tv_xor_b32 v17, s65, v16\n\tv_lshl_add_u32 v4, v245, 9, v17\n\tv_xor_b32 v1, 32, v0\n\t" \
    "v_xor_b32 v5, 32, v4\n\tbuffer_load_dwordx2 v[62:63], v239, s[84:87], s71 offen offset:2048\n\tv_xor_b32 v2, 64, v0\n\tv_xor_b32 v6, 64, v4\n\tv_xor_b32 v3, 96, v0\n\tv_xor_b32 v7, 96, v4\n\ts_mov_b32 s71, 4096\n\tbuffer_load_dwordx2 v[64:65], v239, s[84:87], s71 offen\n\t" \
    "v_add_u32 v224, 0x10000, v4\n\tv_add_u32 v225, 0x10000, v5\n\tv_add_u32 v226, 0x10000, v6\n\tv_add_u32 v227, 0x10000, v7\n\ts_cmp_ge_u32 s43, 16\n\ts_cselect_b32 s69, s70, s43\n\ts_mul_i32 s69, s69, 294912\n\ts_add_u32 s69, s69, 0\n\t" \
    "s_add_u32 m0, s72, 0\n\ts_nop 0\n\tbuffer_load_dwordx4 v10, s[12:15], s69 offen lds\n\ts_mul_i32 s65, s38, 6144\n\ts_add_u32 s65, s65, 131072\n\tv_add_u32 v237, s65, v8\n\ts_xor_b32 s65, s38, 2\n\ts_add_u32 m0, s72, 1024\n\t" \
    "s_nop 0\n\tbuffer_load_dwordx4 v11, s[12:15], s69 offen lds\n\ts_mul_i32 s65, s65, 6144\n\ts_add_u32 s65, s65, 131072\n\tv_add_u32 v238, s65, v8\n\tv_mov_b32 v246, 0xbdd2d3e8\n\ts_waitcnt vmcnt(12)\n\ts_load_dwordx8 s[76:83], s[0:1], 160\n\t" \
    "v_lshlrev_b32 v24, 16, v48\n\tv_and_b32 v25, 0xffff0000, v48\n\tv_lshlrev_b32 v26, 16, v49\n\tv_and_b32 v27, 0xffff0000, v49\n\tv_lshlrev_b32 v28, 16, v50\n\tv_and_b32 v29, 0xffff0000, v50\n\tv_lshlrev_b32 v30, 16, v51\n\tv_and_b32 v31, 0xffff0000, v51\n\t" \
    "v_lshlrev_b32 v32, 16, v52\n\tv_and_b32 v33, 0xffff0000, v52\n\tv_lshlrev_b32 v34, 16, v53\n\tv_and_b32 v35, 0xffff0000, v53\n\tv_lshlrev_b32 v36, 16, v54\n\tv_and_b32 v37, 0xffff0000, v54\n\tv_lshlrev_b32 v38, 16, v55\n\tv_and_b32 v39, 0xffff0000, v55\n\t" \
    "v_lshlrev_b32 v40, 16, v56\n\tv_and_b32 v41, 0xffff0000, v56\n\tv_pk_add_f32 v[70:71], v[24:25], v[26:27]\n\tv_pk_add_f32 v[72:73], v[28:29], v[30:31]\n\tv_pk_add_f32 v[74:75], v[32:33], v[34:35]\n\tv_pk_add_f32 v[76:77], v[36:37], v[38:39]\n\tv_pk_add_f32 v[70:71], v[70:71], v[72:73]\n\tv_pk_add_f32 v[74:75], v[74:75], v[76:77]\n\t" \
    "v_pk_add_f32 v[70:71], v[70:71], v[40:41]\n\tv_pk_add_f32 v[70:71], v[70:71], v[74:75]\n\tv_add_f32 v66, v70, v71\n\ts_nop 1\n\tv_add_f32_dpp v66, v66, v66 row_ror:8 row_mask:0xf bank_mask:0xf\n\ts_nop 1\n\tv_add_f32_dpp v66, v66, v66 row_ror:4 row_mask:0xf bank_mask:0xf\n\ts_nop 1\n\t" \
    "v_add_f32_dpp v66, v66, v66 row_ror:2 row_mask:0xf bank_mask:0xf\n\ts_nop 1\n\tv_add_f32_dpp v66, v66, v66 row_ror:1 row_mask:0xf bank_mask:0xf\n\tv_mov_b32 v67, v66\n\ts_nop 1\n\tv_permlane16_swap_b32 v66, v67\n\tv_add_f32 v66, v66, v67\n\tv_mul_f32 v66, 0x3ae38e39, v66\n\t" \
    "v_pk_add_f32 v[70:71], v[24:25], v[66:67] op_sel_hi:[1,0] neg_lo:[0,1] neg_hi:[0,1]\n\tv_pk_add_f32 v[72:73], v[26:27], v[66:67] op_sel_hi:[1,0] neg_lo:[0,1] neg_hi:[0,1]\n\tv_pk_add_f32 v[74:75], v[28:29], v[66:67] op_sel_hi:[1,0] neg_lo:[0,1] neg_hi:[0,1]\n\tv_pk_add_f32 v[76:77], v[30:31], v[66:67] op_sel_hi:[1,0] neg_lo:[0,1] neg_hi:[0,1]\n\tv_pk_add_f32 v[78:79], v[32:33], v[66:67] op_sel_hi:[1,0] neg_lo:[0,1] neg_hi:[0,1]\n\tv_pk_add_f32 v[80:81], v[34:35], v[66:67] op_sel_hi:[1,0] neg_lo:[0,1] neg_hi:[0,1]\n\tv_pk_add_f32 v[82:83], v[36:37], v[66:67] op_sel_hi:[1,0] neg_lo:[0,1] neg_hi:[0,1]\n\tv_pk_add_f32 v[84:85], v[38:39], v[66:67] op_sel_hi:[1,0] neg_lo:[0,1] neg_hi:[0,1]\n\t" \
    "v_pk_add_f32 v[86:87], v[40:41], v[66:67] op_sel_hi:[1,0] neg_lo:[0,1] neg_hi:[0,1]\n\tv_pk_mul_f32 v[44:45], v[70:71], v[70:71]\n\tv_pk_mul_f32 v[46:47], v[72:73], v[72:73]\n\tv_pk_fma_f32 v[44:45], v[74:75], v[74:75], v[44:45]\n\tv_pk_fma_f32 v[46:47], v[76:77], v[76:77], v[46:47]\n\tv_pk_fma_f32 v[44:45], v[78:79], v[78:79], v[44:45]\n\tv_pk_fma_f32 v[46:47], v[80:81], v[80:81], v[46:47]\n\tv_pk_fma_f32 v[44:45], v[82:83], v[82:83], v[44:45]\n\t" \
    "v_pk_fma_f32 v[46:47], v[84:85], v[84:85], v[46:47]\n\tv_pk_fma_f32 v[44:45], v[86:87], v[86:87], v[44:45]\n\tv_pk_add_f32 v[44:45], v[44:45], v[46:47]\n\tv_add_f32 v68, v44, v45\n\ts_nop 1\n\tv_add_f32_dpp v68, v68, v68 row_ror:8 row_mask:0xf bank_mask:0xf\n\ts_nop 1\n\tv_add_f32_dpp v68, v68, v68 row_ror:4 row_mask:0xf bank_mask:0xf\n\t" \
    "s_nop 1\n\tv_add_f32_dpp v68, v68, v68 row_ror:2 row_mask:0xf bank_mask:0xf\n\ts_nop 1\n\tv_add_f32_dpp v68, v68, v68 row_ror:1 row_mask:0xf bank_mask:0xf\n\tv_mov_b32 v69, v68\n\ts_nop 1\n\tv_permlane16_swap_b32 v68, v69\n\tv_add_f32 v68, v68, v69\n\t" \
    "v_mul_f32 v68, 0x3ae38e39, v68\n\tv_cvt_pk_bf16_f32 v68, v68, v68\n\tv_lshlrev_b32 v68, 16, v68\n\tv_add_f32 v68, 0x3727c5ac, v68\n\tv_cvt_pk_bf16_f32 v68, v68, v68\n\tv_lshlrev_b32 v68, 16, v68\n\tv_sqrt_f32 v68, v68\n\tv_cvt_pk_bf16_f32 v66, v66, v66\n\t" \
    "v_lshlrev_b32 v66, 16, v66\n\tv_cvt_pk_bf16_f32 v68, v68, v68\n\tv_lshlrev_b32 v68, 16, v68\n\tv_rcp_f32 v68, v68\n\ts_nop 0\n\tv_mul_f32 v69, v66, v68\n\tv_xor_b32 v69, 0x80000000, v69\n\tv_pk_fma_f32 v[24:25], v[24:25], v[68:69], v[68:69] op_sel:[0,0,1] op_sel_hi:[1,0,1]\n\t" \
    "v_pk_fma_f32 v[26:27], v[26:27], v[68:69], v[68:69] op_sel:[0,0,1] op_sel_hi:[1,0,1]\n\tv_pk_fma_f32 v[28:29], v[28:29], v[68:69], v[68:69] op_sel:[0,0,1] op_sel_hi:[1,0,1]\n\tv_pk_fma_f32 v[30:31], v[30:31], v[68:69], v[68:69] op_sel:[0,0,1] op_sel_hi:[1,0,1]\n\tv_pk_fma_f32 v[32:33], v[32:33], v[68:69], v[68:69] op_sel:[0,0,1] op_sel_hi:[1,0,1]\n\tv_pk_fma_f32 v[34:35], v[34:35], v[68:69], v[68:69] op_sel:[0,0,1] op_sel_hi:[1,0,1]\n\tv_pk_fma_f32 v[36:37], v[36:37], v[68:69], v[68:69] op_sel:[0,0,1] op_sel_hi:[1,0,1]\n\tv_pk_fma_f32 v[38:39], v[38:39], v[68:69], v[68:69] op_sel:[0,0,1] op_sel_hi:[1,0,1]\n\tv_pk_fma_f32 v[40:41], v[40:41], v[68:69], v[68:69] op_sel:[0,0,1] op_sel_hi:[1,0,1]\n\t" \
    "v_cvt_pk_bf16_f32 v48, v24, v25\n\tv_cvt_pk_bf16_f32 v49, v26, v27\n\tv_cvt_pk_bf16_f32 v50, v28, v29\n\tv_cvt_pk_bf16_f32 v51, v30, v31\n\tv_cvt_pk_bf16_f32 v52, v32, v33\n\tv_cvt_pk_bf16_f32 v53, v34, v35\n\tv_cvt_pk_bf16_f32 v54, v36, v37\n\tv_cvt_pk_bf16_f32 v55, v38, v39\n\t" \
    "v_cvt_pk_bf16_f32 v56, v40, v41\n\tv_lshlrev_b32 v24, 16, v48\n\tv_and_b32 v25, 0xffff0000, v48\n\tv_lshlrev_b32 v26, 16, v49\n\tv_and_b32 v27, 0xffff0000, v49\n\tv_lshlrev_b32 v28, 16, v50\n\tv_and_b32 v29, 0xffff0000, v50\n\tv_lshlrev_b32 v30, 16, v51\n\t" \
    "v_and_b32 v31, 0xffff0000, v51\n\tv_lshlrev_b32 v32, 16, v52\n\tv_and_b32 v33, 0xffff0000, v52\n\tv_lshlrev_b32 v34, 16, v53\n\tv_and_b32 v35, 0xffff0000, v53\n\tv_lshlrev_b32 v36, 16, v54\n\tv_and_b32 v37, 0xffff0000, v54\n\tv_lshlrev_b32 v38, 16, v55\n\t" \
    "v_and_b32 v39, 0xffff0000, v55\n\tv_lshlrev_b32 v40, 16, v56\n\tv_and_b32 v41, 0xffff0000, v56\n\tv_pk_add_f32 v[70:71], v[24:25], v[26:27]\n\tv_pk_add_f32 v[72:73], v[28:29], v[30:31]\n\tv_pk_add_f32 v[74:75], v[32:33], v[34:35]\n\tv_pk_add_f32 v[76:77], v[36:37], v[38:39]\n\tv_pk_mul_f32 v[44:45], v[24:25], v[24:25]\n\t" \
    "v_pk_mul_f32 v[46:47], v[26:27], v[26:27]\n\tv_pk_add_f32 v[70:71], v[70:71], v[72:73]\n\tv_pk_add_f32 v[74:75], v[74:75], v[76:77]\n\tv_pk_fma_f32 v[44:45], v[28:29], v[28:29], v[44:45]\n\tv_pk_fma_f32 v[46:47], v[30:31], v[30:31], v[46:47]\n\tv_pk_add_f32 v[70:71], v[70:71], v[40:41]\n\tv_pk_fma_f32 v[44:45], v[32:33], v[32:33], v[44:45]\n\tv_pk_fma_f32 v[46:47], v[34:35], v[34:35], v[46:47]\n\t" \
    "v_pk_add_f32 v[70:71], v[70:71], v[74:75]\n\tv_pk_fma_f32 v[44:45], v[36:37], v[36:37], v[44:45]\n\tv_pk_fma_f32 v[46:47], v[38:39], v[38:39], v[46:47]\n\tv_pk_fma_f32 v[44:45], v[40:41], v[40:41], v[44:45]\n\tv_pk_add_f32 v[44:45], v[44:45], v[46:47]\n\tv_add_f32 v66, v70, v71\n\tv_add_f32 v42, v44, v45\n\ts_nop 0\n\t" \
    "v_add_f32_dpp v66, v66, v66 row_ror:8 row_mask:0xf bank_mask:0xf\n\tv_add_f32_dpp v42, v42, v42 row_ror:8 row_mask:0xf bank_mask:0xf\n\ts_nop 0\n\tv_add_f32_dpp v66, v66, v66 row_ror:4 row_mask:0xf bank_mask:0xf\n\tv_add_f32_dpp v42, v42, v42 row_ror:4 row_mask:0xf bank_mask:0xf\n\ts_nop 0\n\tv_add_f32_dpp v66, v66, v66 row_ror:2 row_mask:0xf bank_mask:0xf\n\tv_add_f32_dpp v42, v42, v42 row_ror:2 row_mask:0xf bank_mask:0xf\n\t" \
    "s_nop 0\n\tv_add_f32_dpp v66, v66, v66 row_ror:1 row_mask:0xf bank_mask:0xf\n\tv_add_f32_dpp v42, v42, v42 row_ror:1 row_mask:0xf bank_mask:0xf\n\tv_mov_b32 v67, v66\n\tv_mov_b32 v43, v42\n\ts_nop 0\n\tv_permlane16_swap_b32 v66, v67\n\tv_permlane16_swap_b32 v42, v43\n\t" \
    "v_add_f32 v66, v66, v67\n\tv_add_f32 v42, v42, v43\n\tv_mul_f32 v66, 0x3ae38e39, v66\n\tv_mul_f32 v42, 0x3ae38e39, v42\n\tv_fma_f32 v42, -v66, v66, v42\n\tv_max_f32 v42, 0, v42\n\tv_cvt_pk_bf16_f32 v42, v42, v42\n\tv_lshlrev_b32 v42, 16, v42\n\t" \
    "v_add_f32 v42, 0x3727c5ac, v42\n\tv_cvt_pk_bf16_f32 v42, v42, v42\n\tv_lshlrev_b32 v42, 16, v42\n\tv_sqrt_f32 v42, v42\n\tv_cvt_pk_bf16_f32 v66, v66, v66\n\tv_lshlrev_b32 v66, 16, v66\n\tv_cvt_pk_bf16_f32 v42, v42, v42\n\tv_lshlrev_b32 v42, 16, v42\n\t" \
    "v_rcp_f32 v42, v42\n\ts_nop 0\n\tv_mul_f32 v43, v66, v42\n\tv_xor_b32 v43, 0x80000000, v43\n\tv_pk_fma_f32 v[24:25], v[24:25], v[42:43], v[42:43] op_sel:[0,0,1] op_sel_hi:[1,0,1]\n\tv_pk_fma_f32 v[26:27], v[26:27], v[42:43], v[42:43] op_sel:[0,0,1] op_sel_hi:[1,0,1]\n\tv_pk_fma_f32 v[28:29], v[28:29], v[42:43], v[42:43] op_sel:[0,0,1] op_sel_hi:[1,0,1]\n\tv_pk_fma_f32 v[30:31], v[30:31], v[42:43], v[42:43] op_sel:[0,0,1] op_sel_hi:[1,0,1]\n\t" \
    "v_pk_fma_f32 v[32:33], v[32:33], v[42:43], v[42:43] op_sel:[0,0,1] op_sel_hi:[1,0,1]\n\tv_pk_fma_f32 v[34:35], v[34:35], v[42:43], v[42:43] op_sel:[0,0,1] op_sel_hi:[1,0,1]\n\tv_pk_fma_f32 v[36:37], v[36:37], v[42:43], v[42:43] op_sel:[0,0,1] op_sel_hi:[1,0,1]\n\tv_pk_fma_f32 v[38:39], v[38:39], v[42:43], v[42:43] op_sel:[0,0,1] op_sel_hi:[1,0,1]\n\tv_pk_fma_f32 v[40:41], v[40:41], v[42:43], v[42:43] op_sel:[0,0,1] op_sel_hi:[1,0,1]\n\tv_cvt_pk_bf16_f32 v48, v24, v25\n\tv_cvt_pk_bf16_f32 v49, v26, v27\n\tv_cvt_pk_bf16_f32 v50, v28, v29\n\t" \
    "v_cvt_pk_bf16_f32 v51, v30, v31\n\tv_cvt_pk_bf16_f32 v52, v32, v33\n\tv_cvt_pk_bf16_f32 v53, v34, v35\n\tv_cvt_pk_bf16_f32 v54, v36, v37\n\tv_cvt_pk_bf16_f32 v55, v38, v39\n\tv_cvt_pk_bf16_f32 v56, v40, v41\n\tds_write_b128 v90, v[48:51]\n\tds_write_b128 v90, v[52:55] offset:512\n\t" \
    "ds_write_b32 v91, v56 offset:1024\n\ts_waitcnt vmcnt(11) lgkmcnt(0)\n\ts_barrier\n\tds_read_b64_tr_b16 v[24:25], v94 offset:0\n\tds_read_b64_tr_b16 v[26:27], v94 offset:4672\n\tds_read_b64_tr_b16 v[28:29], v94 offset:512\n\tds_read_b64_tr_b16 v[30:31], v94 offset:5184\n\tds_read_b64_tr_b16 v[32:33], v95 offset:1024\n\t" \
    "ds_read_b64_tr_b16 v[34:35], v95 offset:5696\n\tbuffer_store_dwordx2 v[68:69], v89, s[8:11], s64 offen sc1\n\tv_readfirstlane_b32 s36, v58\n\tv_readfirstlane_b32 s37, v59\n\ts_lshl_b32 s73, s38, 12\n\ts_add_u32 s68, s61, s73\n\ts_add_u32 s36, s36, 0x7f4a7c15\n\ts_addc_u32 s37, s37, 0x9e3779b9\n\t" \
    "s_waitcnt lgkmcnt(4)\n\tbuffer_store_dwordx4 v[24:27], v88, s[8:11], s68 offen sc1\n\ts_add_u32 s68, s68, 16384\n\ts_waitcnt lgkmcnt(2)\n\tbuffer_store_dwordx4 v[28:31], v88, s[8:11], s68 offen sc1\n\ts_add_u32 s68, s61, 32768\n\ts_cmp_eq_u32 s38, 0\n\ts_cselect_b32 s68, s68, 0x7ff00000\n\t" \
    "s_waitcnt lgkmcnt(0)\n\tbuffer_store_dwordx4 v[32:35], v88, s[8:11], s68 offen sc1\n\tv_mov_b64 v[248:249], s[36:37]\n\tv_and_b32 v89, 7, v240\n\tv_lshlrev_b32 v89, 11, v89\n\ts_cmp_ge_u32 s43, 16\n\ts_cselect_b32 s69, s70, s43\n\ts_mul_i32 s69, s69, 294912\n\t" \
    "s_add_u32 s69, s69, 128\n\ts_add_u32 m0, s72, 4096\n\ts_nop 0\n\tbuffer_load_dwordx4 v10, s[12:15], s69 offen lds\n\tv_mov_b32 v247, 0xc0135761\n\ts_lshr_b32 s65, s40, 3\n\ts_mul_i32 s66, s65, 43\n\ts_lshr_b32 s66, s66, 7\n\t" \
    "s_mul_i32 s6, s66, 3\n\ts_sub_u32 s6, s65, s6\n\ts_and_b32 s66, s66, 7\n\ts_and_b32 s7, s4, 1\n\ts_lshl_b32 s7, s7, 3\n\ts_add_u32 s7, s7, s66\n\ts_mul_i32 s65, s5, 4718592\n\ts_lshl_b32 s67, s7, 8\n\t" \
    "s_add_u32 s65, s65, s67\n\ts_add_u32 s16, s16, s65\n\ts_addc_u32 s17, s17, 0\n\ts_sub_u32 s18, s18, s65\n\ts_cmp_eq_u32 s39, 2\n\ts_cselect_b32 s18, s18, 0\n\tv_lshrrev_b32 v16, 3, v240\n\ts_lshl_b32 s65, s38, 3\n\t" \
    "v_add_u32 v16, s65, v16\n\tv_and_b32 v17, 0x70, v8\n\tv_lshlrev_b32 v18, 5, v245\n\tv_xor_b32 v17, v17, v18\n\tv_lshl_add_u32 v16, v16, 12, v17\n\ts_lshr_b32 s65, s5, 1\n\ts_lshl_b32 s65, s65, 7\n\tv_add_u32 v13, s65, v16\n\t" \
    "v_xor_b32 v12, 128, v13\n\ts_lshl_b32 s65, s7, 7\n\ts_lshl_b32 s66, s5, 5\n\ts_add_u32 s65, s65, s66\n\ts_add_u32 m0, s72, 5120\n\ts_nop 0\n\tbuffer_load_dwordx4 v11, s[12:15], s69 offen lds\n\ts_lshl_b32 s59, s65, 3\n\t" \
    "s_add_u32 s59, s59, 58368\n\ts_lshl_b32 s66, s65, 1\n\ts_add_u32 s32, s32, s66\n\ts_addc_u32 s33, s33, 0\n\ts_sub_u32 s34, s34, s66\n\ts_lshl_b32 s66, s6, 2\n\ts_add_u32 s66, s66, s38\n\ts_mul_i32 s67, s66, 96\n\t" \
    "s_mul_i32 s65, s65, 1152\n\ts_add_u32 s65, s65, s67\n\ts_add_u32 s24, s24, s65\n\ts_addc_u32 s25, s25, 0\n\ts_sub_u32 s26, s26, s65\n\ts_mul_i32 s73, s38, 96\n\ts_sub_u32 s65, s65, s73\n\ts_add_u32 s20, s20, s65\n\t" \
    "s_addc_u32 s21, s21, 0\n\ts_sub_u32 s22, s22, s65\n\ts_lshl_b32 s51, s67, 2\n\ts_add_u32 s51, s51, 50176\n\ts_lshl_b32 s66, s66, 2\n\ts_add_u32 s66, s66, s5\n\ts_mul_i32 s46, s66, 110592\n\ts_add_u32 s46, s46, 16646144\n\t" \
    "s_cmp_eq_u32 s39, 2\n\ts_cselect_b32 s46, s46, 2146435072\n\ts_lshl_b32 s50, s66, 13\n\ts_add_u32 s50, s50, 21954560\n\tv_and_b32 v16, 31, v240\n\tv_mul_u32_u24 v243, 1152, v16\n\tv_lshrrev_b32 v16, 5, v240\n\tv_lshl_add_u32 v243, v16, 4, v243\n\t" \
    "v_lshrrev_b32 v242, 1, v244\n\ts_cmp_ge_u32 s43, 15\n\ts_cselect_b32 s69, s70, s43\n\ts_mul_i32 s69, s69, 294912\n\ts_add_u32 s69, s69, 294912\n\ts_add_u32 m0, s72, 8192\n\ts_nop 0\n\tbuffer_load_dwordx4 v10, s[12:15], s69 offen lds\n\t" \
    "s_mul_i32 s65, s42, 57\n\ts_lshr_b32 s65, s65, 9\n\ts_mul_i32 s66, s65, 9\n\ts_sub_u32 s66, s42, s66\n\ts_lshl_b32 s66, s66, 1\n\ts_add_u32 s66, s66, s57\n\ts_lshl_b32 s67, s41, 1\n\ts_add_u32 s67, s67, s58\n\t" \
    "s_lshl_b32 s67, s67, 2\n\ts_add_u32 s65, s65, s67\n\ts_lshl_b32 s49, s65, 13\n\ts_lshl_b32 s67, s66, 7\n\ts_add_u32 s49, s49, s67\n\ts_add_u32 s49, s49, 21954560\n\ts_mul_i32 s45, s65, 36\n\ts_add_u32 s45, s45, s66\n\t" \
    "s_mul_i32 s45, s45, 3072\n\ts_add_u32 s45, s45, 16646144\n\tv_lshlrev_b32 v236, 2, v240\n\tv_min_u32 v241, 17, v240\n\tv_lshlrev_b32 v241, 7, v241\n\ts_lshl_b32 s65, s6, 4\n\ts_add_u32 s65, s65, s7\n\ts_lshl_b32 s65, s65, 2\n\t" \
    "s_add_u32 s65, s65, s5\n\ts_lshl_b32 s65, s65, 2\n\ts_add_u32 s53, s65, s38\n\ts_mul_i32 s55, s53, 9984\n\ts_add_u32 s55, s55, 7208960\n\ts_cmp_eq_u32 s39, 2\n\ts_cselect_b32 s55, s55, 2146435072\n\ts_add_u32 m0, s72, 9216\n\t" \
    "s_nop 0\n\tbuffer_load_dwordx4 v11, s[12:15], s69 offen lds\n\ts_mul_i32 s56, s53, 3072\n\ts_add_u32 s56, s56, 4849664\n\ts_or_b32 s65, s40, s38\n\ts_cselect_b32 s60, 2146435072, 0\n\ts_waitcnt vmcnt(10)\n\ts_barrier\n\t" \
    "ds_read_b64_tr_b16 v[36:37], v93 offset:0\n\tds_read_b64_tr_b16 v[38:39], v93 offset:4608\n\tds_read_b64_tr_b16 v[40:41], v93 offset:9216\n\tds_read_b64_tr_b16 v[42:43], v93 offset:13312\n\tds_read_b64_tr_b16 v[44:45], v93 offset:17920\n\tds_read_b64_tr_b16 v[46:47], v93 offset:512\n\tds_read_b64_tr_b16 v[48:49], v93 offset:5120\n\tds_read_b64_tr_b16 v[50:51], v93 offset:9728\n\t" \
    "ds_read_b64_tr_b16 v[52:53], v93 offset:13824\n\tds_read_b64_tr_b16 v[54:55], v93 offset:18432\n\tds_read_b64_tr_b16 v[120:121], v92 offset:1024\n\tds_read_b64_tr_b16 v[122:123], v92 offset:5632\n\tds_read_b64_tr_b16 v[124:125], v92 offset:10240\n\tds_read_b64_tr_b16 v[126:127], v92 offset:14336\n\tds_read_b64_tr_b16 v[128:129], v92 offset:18944\n\tv_lshlrev_b32 v78, 16, v60\n\t" \
    "s_cmp_ge_u32 s43, 15\n\ts_cselect_b32 s69, s70, s43\n\ts_mul_i32 s69, s69, 294912\n\ts_add_u32 s69, s69, 295040\n\ts_add_u32 m0, s72, 12288\n\tv_and_b32 v79, 0xffff0000, v60\n\tv_lshlrev_b32 v80, 16, v61\n\tv_and_b32 v81, 0xffff0000, v61\n\t" \
    "buffer_load_dwordx4 v10, s[12:15], s69 offen lds\n\ts_add_u32 m0, s72, 13312\n\ts_waitcnt lgkmcnt(10)\n\tv_lshlrev_b32 v66, 16, v36\n\tv_and_b32 v67, 0xffff0000, v36\n\tv_lshlrev_b32 v68, 16, v37\n\tv_and_b32 v69, 0xffff0000, v37\n\tv_lshlrev_b32 v70, 16, v38\n\t" \
    "v_and_b32 v71, 0xffff0000, v38\n\tv_lshlrev_b32 v72, 16, v39\n\tv_and_b32 v73, 0xffff0000, v39\n\tv_lshlrev_b32 v74, 16, v40\n\tv_and_b32 v75, 0xffff0000, v40\n\tv_lshlrev_b32 v76, 16, v41\n\tv_mul_f32 v16, v66, v81\n\tv_mul_f32 v17, v67, v81\n\t" \
    "v_mul_f32 v18, v68, v81\n\tv_mul_f32 v19, v69, v81\n\tv_mul_f32 v20, v70, v81\n\tv_mul_f32 v21, v71, v81\n\tv_mul_f32 v22, v72, v81\n\tv_mul_f32 v23, v73, v81\n\tv_fmac_f32 v16, v67, v80\n\tv_fmac_f32 v17, v68, v80\n\t" \
    "v_fmac_f32 v18, v69, v80\n\tv_fmac_f32 v19, v70, v80\n\tv_fmac_f32 v20, v71, v80\n\tv_fmac_f32 v21, v72, v80\n\tv_fmac_f32 v22, v73, v80\n\tv_fmac_f32 v23, v74, v80\n\tv_fmac_f32 v16, v68, v79\n\tbuffer_load_dwordx4 v11, s[12:15], s69 offen lds\n\t" \
    "s_cmp_ge_u32 s43, 14\n\ts_cselect_b32 s69, s70, s43\n\ts_mul_i32 s69, s69, 294912\n\ts_add_u32 s69, s69, 589824\n\ts_add_u32 m0, s72, 16384\n\tv_fmac_f32 v17, v69, v79\n\tv_fmac_f32 v18, v70, v79\n\tv_fmac_f32 v19, v71, v79\n\t" \
    "v_fmac_f32 v20, v72, v79\n\tv_fmac_f32 v21, v73, v79\n\tv_fmac_f32 v22, v74, v79\n\tv_fmac_f32 v23, v75, v79\n\tv_fmac_f32 v16, v69, v78\n\tv_fmac_f32 v17, v70, v78\n\tv_fmac_f32 v18, v71, v78\n\tv_fmac_f32 v19, v72, v78\n\t" \
    "v_fmac_f32 v20, v73, v78\n\tv_fmac_f32 v21, v74, v78\n\tv_fmac_f32 v22, v75, v78\n\tv_fmac_f32 v23, v76, v78\n\tv_cvt_pk_bf16_f32 v66, v16, v17\n\tv_cvt_pk_bf16_f32 v67, v18, v19\n\tv_cvt_pk_bf16_f32 v68, v20, v21\n\tv_cvt_pk_bf16_f32 v69, v22, v23\n\t" \
    "v_lshlrev_b32 v16, 16, v66\n\tv_and_b32 v17, 0xffff0000, v66\n\tv_lshlrev_b32 v18, 16, v67\n\tv_and_b32 v19, 0xffff0000, v67\n\tv_lshlrev_b32 v20, 16, v68\n\tv_and_b32 v21, 0xffff0000, v68\n\tbuffer_load_dwordx4 v10, s[12:15], s69 offen lds\n\ts_add_u32 m0, s72, 17408\n\t" \
    "v_lshlrev_b32 v22, 16, v69\n\tv_and_b32 v23, 0xffff0000, v69\n\tv_lshlrev_b32 v66, 16, v42\n\tv_and_b32 v67, 0xffff0000, v42\n\tv_lshlrev_b32 v68, 16, v43\n\tv_and_b32 v69, 0xffff0000, v43\n\tv_lshlrev_b32 v70, 16, v44\n\tv_and_b32 v71, 0xffff0000, v44\n\t" \
    "v_lshlrev_b32 v72, 16, v45\n\tv_and_b32 v73, 0xffff0000, v45\n\tv_pk_mul_f32 v[16:17], v[16:17], v[66:67]\n\tv_pk_mul_f32 v[18:19], v[18:19], v[68:69]\n\tv_pk_mul_f32 v[20:21], v[20:21], v[70:71]\n\tv_pk_mul_f32 v[22:23], v[22:23], v[72:73]\n\tv_cvt_pk_bf16_f32 v24, v16, v17\n\tv_cvt_pk_bf16_f32 v25, v18, v19\n\t" \
    "v_cvt_pk_bf16_f32 v26, v20, v21\n\tv_cvt_pk_bf16_f32 v27, v22, v23\n\ts_add_u32 s68, s62, s54\n\tbuffer_store_dwordx4 v[24:27], v8, s[8:11], s68 offen sc1\n\ts_waitcnt vmcnt(8) lgkmcnt(0)\n\ts_barrier\n\tbuffer_store_dwordx2 v[248:249], v89, s[8:11], s63 offen sc1\n\ts_cmp_lg_u32 s39, 0\n\t" \
    "s_cbranch_scc1 104f\n\ts_sub_u32 s73, s40, 216\n\ts_lshl_b32 s71, s73, 4\n\ts_mul_i32 s68, s73, 12\n\ts_add_u32 s68, s68, 96\n\ts_cmp_lt_u32 s73, 24\n\ts_cselect_b32 s71, s71, s68\n\ts_cselect_b32 s73, 0, 2146435072\n\t" \
    "v_cmp_eq_u32 vcc, 0, v240\n\tv_subrev_u32 v190, 8, v8\n\tv_mov_b32 v191, 2146435072\n\tv_cndmask_b32 v190, v190, v191, vcc\n\ts_add_u32 s68, s71, s38\n\ts_add_u32 s68, s68, 0\n\ts_lshl_b32 s73, s68, 1\n\ts_lshl_b32 s68, s68, 12\n\t" \
    "buffer_load_dwordx4 v[164:167], v8, s[76:79], s68 offen offset:0\n\tbuffer_load_dwordx4 v[168:171], v8, s[76:79], s68 offen offset:1024\n\tbuffer_load_dwordx4 v[172:175], v8, s[76:79], s68 offen offset:2048\n\tbuffer_load_dwordx4 v[176:179], v8, s[76:79], s68 offen offset:3072\n\tbuffer_load_dwordx2 v[180:181], v190, s[76:79], s68 offen\n\tbuffer_load_dwordx2 v[182:183], v8, s[76:79], s68 offen offset:1016\n\tbuffer_load_dwordx2 v[184:185], v8, s[76:79], s68 offen offset:2040\n\tbuffer_load_dwordx2 v[186:187], v8, s[76:79], s68 offen offset:3064\n\t" \
    "buffer_load_ushort v188, off, s[80:83], s73\n\t104:\n\tv_lshlrev_b32 v78, 16, v62\n\tds_read_b64_tr_b16 a[48:49], v0 offset:0\n\tv_and_b32 v79, 0xffff0000, v62\n\tds_read_b64_tr_b16 a[50:51], v0 offset:512\n\tv_lshlrev_b32 v80, 16, v63\n\tds_read_b64_tr_b16 a[52:53], v1 offset:0\n\t" \
    "v_and_b32 v81, 0xffff0000, v63\n\tds_read_b64_tr_b16 a[54:55], v1 offset:512\n\tv_lshlrev_b32 v66, 16, v46\n\tds_read_b64_tr_b16 a[56:57], v2 offset:0\n\tv_and_b32 v67, 0xffff0000, v46\n\tds_read_b64_tr_b16 a[58:59], v2 offset:512\n\tv_lshlrev_b32 v68, 16, v47\n\tds_read_b64_tr_b16 a[60:61], v3 offset:0\n\t" \
    "v_and_b32 v69, 0xffff0000, v47\n\tds_read_b64_tr_b16 a[62:63], v3 offset:512\n\tv_lshlrev_b32 v70, 16, v48\n\tv_and_b32 v71, 0xffff0000, v48\n\tv_lshlrev_b32 v72, 16, v49\n\tv_and_b32 v73, 0xffff0000, v49\n\tv_lshlrev_b32 v74, 16, v50\n\tv_and_b32 v75, 0xffff0000, v50\n\t" \
    "v_lshlrev_b32 v76, 16, v51\n\tv_mul_f32 v16, v66, v81\n\tv_mul_f32 v17, v67, v81\n\tv_mul_f32 v18, v68, v81\n\tv_mul_f32 v19, v69, v81\n\tv_mul_f32 v20, v70, v81\n\tbuffer_load_dwordx4 v11, s[12:15], s69 offen lds\n\ts_cmp_ge_u32 s43, 14\n\t" \
    "s_cselect_b32 s69, s70, s43\n\ts_mul_i32 s69, s69, 294912\n\ts_add_u32 s69, s69, 589952\n\ts_add_u32 m0, s72, 20480\n\tv_mul_f32 v21, v71, v81\n\tv_mul_f32 v22, v72, v81\n\tv_mul_f32 v23, v73, v81\n\tv_fmac_f32 v16, v67, v80\n\t" \
    "v_fmac_f32 v17, v68, v80\n\tv_fmac_f32 v18, v69, v80\n\tv_fmac_f32 v19, v70, v80\n\tv_fmac_f32 v20, v71, v80\n\tv_fmac_f32 v21, v72, v80\n\tv_fmac_f32 v22, v73, v80\n\tv_fmac_f32 v23, v74, v80\n\tv_fmac_f32 v16, v68, v79\n\t" \
    "v_fmac_f32 v17, v69, v79\n\tv_fmac_f32 v18, v70, v79\n\tv_fmac_f32 v19, v71, v79\n\tv_fmac_f32 v20, v72, v79\n\tv_fmac_f32 v21, v73, v79\n\tv_fmac_f32 v22, v74, v79\n\tv_fmac_f32 v23, v75, v79\n\tv_fmac_f32 v16, v69, v78\n\t" \
    "v_fmac_f32 v17, v70, v78\n\tv_fmac_f32 v18, v71, v78\n\tv_fmac_f32 v19, v72, v78\n\tv_fmac_f32 v20, v73, v78\n\tv_fmac_f32 v21, v74, v78\n\tbuffer_load_dwordx4 v10, s[12:15], s69 offen lds\n\ts_add_u32 m0, s72, 21504\n\tv_fmac_f32 v22, v75, v78\n\t" \
    "v_fmac_f32 v23, v76, v78\n\tv_cvt_pk_bf16_f32 v66, v16, v17\n\tv_cvt_pk_bf16_f32 v67, v18, v19\n\tv_cvt_pk_bf16_f32 v68, v20, v21\n\tv_cvt_pk_bf16_f32 v69, v22, v23\n\tv_lshlrev_b32 v16, 16, v66\n\tv_and_b32 v17, 0xffff0000, v66\n\tv_lshlrev_b32 v18, 16, v67\n\t" \
    "v_and_b32 v19, 0xffff0000, v67\n\tv_lshlrev_b32 v20, 16, v68\n\tv_and_b32 v21, 0xffff0000, v68\n\tv_lshlrev_b32 v22, 16, v69\n\tv_and_b32 v23, 0xffff0000, v69\n\tv_lshlrev_b32 v66, 16, v52\n\tv_and_b32 v67, 0xffff0000, v52\n\tv_lshlrev_b32 v68, 16, v53\n\t" \
    "v_and_b32 v69, 0xffff0000, v53\n\tv_lshlrev_b32 v70, 16, v54\n\tv_and_b32 v71, 0xffff0000, v54\n\tv_lshlrev_b32 v72, 16, v55\n\tv_and_b32 v73, 0xffff0000, v55\n\tv_pk_mul_f32 v[16:17], v[16:17], v[66:67]\n\tv_pk_mul_f32 v[18:19], v[18:19], v[68:69]\n\tv_pk_mul_f32 v[20:21], v[20:21], v[70:71]\n\t" \
    "v_pk_mul_f32 v[22:23], v[22:23], v[72:73]\n\tv_cvt_pk_bf16_f32 v28, v16, v17\n\tv_cvt_pk_bf16_f32 v29, v18, v19\n\tv_cvt_pk_bf16_f32 v30, v20, v21\n\tbuffer_load_dwordx4 v11, s[12:15], s69 offen lds\n\ts_cmp_ge_u32 s43, 13\n\ts_cselect_b32 s69, s70, s43\n\ts_mul_i32 s69, s69, 294912\n\t" \
    "s_add_u32 s69, s69, 884736\n\ts_add_u32 m0, s72, 24576\n\tv_cvt_pk_bf16_f32 v31, v22, v23\n\ts_add_u32 s68, s62, s54\n\tbuffer_store_dwordx4 v[28:31], v9, s[8:11], s68 offen sc1\n\tv_lshlrev_b32 v78, 16, v64\n\tv_and_b32 v79, 0xffff0000, v64\n\tv_lshlrev_b32 v80, 16, v65\n\t" \
    "v_and_b32 v81, 0xffff0000, v65\n\tv_lshlrev_b32 v66, 16, v120\n\tv_and_b32 v67, 0xffff0000, v120\n\tv_lshlrev_b32 v68, 16, v121\n\tv_and_b32 v69, 0xffff0000, v121\n\tv_lshlrev_b32 v70, 16, v122\n\tv_and_b32 v71, 0xffff0000, v122\n\tv_lshlrev_b32 v72, 16, v123\n\t" \
    "v_and_b32 v73, 0xffff0000, v123\n\tv_lshlrev_b32 v74, 16, v124\n\tv_and_b32 v75, 0xffff0000, v124\n\tv_lshlrev_b32 v76, 16, v125\n\tv_mul_f32 v16, v66, v81\n\tv_mul_f32 v17, v67, v81\n\tv_mul_f32 v18, v68, v81\n\tv_mul_f32 v19, v69, v81\n\t" \
    "v_mul_f32 v20, v70, v81\n\tv_mul_f32 v21, v71, v81\n\tv_mul_f32 v22, v72, v81\n\tv_mul_f32 v23, v73, v81\n\tv_fmac_f32 v16, v67, v80\n\tv_fmac_f32 v17, v68, v80\n\tv_fmac_f32 v18, v69, v80\n\tv_fmac_f32 v19, v70, v80\n\t" \
    "v_fmac_f32 v20, v71, v80\n\tv_fmac_f32 v21, v72, v80\n\tv_fmac_f32 v22, v73, v80\n\tbuffer_load_dwordx4 v10, s[12:15], s69 offen lds\n\ts_add_u32 m0, s72, 25600\n\tv_fmac_f32 v23, v74, v80\n\tv_fmac_f32 v16, v68, v79\n\tv_fmac_f32 v17, v69, v79\n\t" \
    "v_fmac_f32 v18, v70, v79\n\tv_fmac_f32 v19, v71, v79\n\tv_fmac_f32 v20, v72, v79\n\tv_fmac_f32 v21, v73, v79\n\tv_fmac_f32 v22, v74, v79\n\tv_fmac_f32 v23, v75, v79\n\tv_fmac_f32 v16, v69, v78\n\tv_fmac_f32 v17, v70, v78\n\t" \
    "v_fmac_f32 v18, v71, v78\n\tv_fmac_f32 v19, v72, v78\n\tv_fmac_f32 v20, v73, v78\n\tv_fmac_f32 v21, v74, v78\n\tv_fmac_f32 v22, v75, v78\n\tv_fmac_f32 v23, v76, v78\n\tv_cvt_pk_bf16_f32 v66, v16, v17\n\tv_cvt_pk_bf16_f32 v67, v18, v19\n\t" \
    "v_cvt_pk_bf16_f32 v68, v20, v21\n\tv_cvt_pk_bf16_f32 v69, v22, v23\n\tv_lshlrev_b32 v16, 16, v66\n\tv_and_b32 v17, 0xffff0000, v66\n\tv_lshlrev_b32 v18, 16, v67\n\tv_and_b32 v19, 0xffff0000, v67\n\tv_lshlrev_b32 v20, 16, v68\n\tv_and_b32 v21, 0xffff0000, v68\n\t" \
    "v_lshlrev_b32 v22, 16, v69\n\tv_and_b32 v23, 0xffff0000, v69\n\tbuffer_load_dwordx4 v11, s[12:15], s69 offen lds\n\ts_cmp_ge_u32 s43, 13\n\ts_cselect_b32 s69, s70, s43\n\ts_mul_i32 s69, s69, 294912\n\ts_add_u32 s69, s69, 884864\n\ts_add_u32 m0, s72, 28672\n\t" \
    "v_lshlrev_b32 v66, 16, v126\n\tv_and_b32 v67, 0xffff0000, v126\n\tv_lshlrev_b32 v68, 16, v127\n\tv_and_b32 v69, 0xffff0000, v127\n\tv_lshlrev_b32 v70, 16, v128\n\tv_and_b32 v71, 0xffff0000, v128\n\tv_lshlrev_b32 v72, 16, v129\n\tv_and_b32 v73, 0xffff0000, v129\n\t" \
    "v_pk_mul_f32 v[16:17], v[16:17], v[66:67]\n\tv_pk_mul_f32 v[18:19], v[18:19], v[68:69]\n\tv_pk_mul_f32 v[20:21], v[20:21], v[70:71]\n\tv_pk_mul_f32 v[22:23], v[22:23], v[72:73]\n\tv_cvt_pk_bf16_f32 v32, v16, v17\n\tv_cvt_pk_bf16_f32 v33, v18, v19\n\tv_cvt_pk_bf16_f32 v34, v20, v21\n\tv_cvt_pk_bf16_f32 v35, v22, v23\n\t" \
    "s_add_u32 s68, s62, 8192\n\ts_cmp_eq_u32 s38, 1\n\ts_cselect_b32 s68, s68, 0x7ff00000\n\tbuffer_store_dwordx4 v[32:35], v8, s[8:11], s68 offen sc1\n\ts_cmp_eq_u32 s39, 0\n\ts_cbranch_scc1 101f\n\tbuffer_load_dwordx4 v10, s[12:15], s69 offen lds\n\ts_add_u32 m0, s72, 29696\n\t" \
    "s_nop 0\n\tbuffer_load_dwordx4 v11, s[12:15], s69 offen lds\n\ts_cmp_ge_u32 s43, 12\n\ts_cselect_b32 s69, s70, s43\n\ts_mul_i32 s69, s69, 294912\n\ts_add_u32 s69, s69, 1179648\n\ts_add_u32 m0, s72, 32768\n\ts_nop 0\n\t" \
    "buffer_load_dwordx4 v10, s[12:15], s69 offen lds\n\ts_add_u32 m0, s72, 33792\n\ts_nop 0\n\tbuffer_load_dwordx4 v11, s[12:15], s69 offen lds\n\ts_cmp_ge_u32 s43, 12\n\ts_cselect_b32 s69, s70, s43\n\ts_mul_i32 s69, s69, 294912\n\ts_add_u32 s69, s69, 1179776\n\t" \
    "s_add_u32 m0, s72, 36864\n\ts_nop 0\n\tbuffer_load_dwordx4 v10, s[12:15], s69 offen lds\n\ts_add_u32 m0, s72, 37888\n\ts_nop 0\n\tbuffer_load_dwordx4 v11, s[12:15], s69 offen lds\n\ts_cmp_ge_u32 s43, 11\n\ts_cselect_b32 s69, s70, s43\n\t" \
    "s_mul_i32 s69, s69, 294912\n\ts_add_u32 s69, s69, 1474560\n\ts_add_u32 m0, s72, 40960\n\ts_nop 0\n\tbuffer_load_dwordx4 v10, s[12:15], s69 offen lds\n\ts_add_u32 m0, s72, 41984\n\ts_nop 0\n\tbuffer_load_dwordx4 v11, s[12:15], s69 offen lds\n\t" \
    "s_waitcnt vmcnt(9) lgkmcnt(0)\n\ts_barrier\n\ts_xor_b32 s71, s57, 1\n\ts_lshl_b32 s71, s71, 6\n\ts_lshl_b32 s69, s42, 8\n\ts_add_u32 s71, s71, s69\n\tv_lshlrev_b32 v96, 3, v245\n\tbuffer_load_dwordx2 v[112:113], v96, s[28:31], s71 offen offset:0\n\t" \
    "ds_read_b64_tr_b16 v[120:121], v0 offset:4096\n\tds_read_b64_tr_b16 v[122:123], v0 offset:4608\n\tds_read_b64_tr_b16 v[124:125], v1 offset:4096\n\tds_read_b64_tr_b16 v[126:127], v1 offset:4608\n\tbuffer_load_dwordx2 v[114:115], v96, s[28:31], s71 offen offset:32\n\tds_read_b64_tr_b16 v[128:129], v2 offset:4096\n\tds_read_b64_tr_b16 v[130:131], v2 offset:4608\n\tds_read_b64_tr_b16 v[132:133], v3 offset:4096\n\t" \
    "ds_read_b64_tr_b16 v[134:135], v3 offset:4608\n\ts_waitcnt vmcnt(10)\n\tbuffer_store_dwordx2 v[248:249], off, s[8:11], s48 sc1\n\tbuffer_load_dwordx2 v[116:117], v96, s[28:31], s71 offen offset:128\n\tds_read_b64_tr_b16 v[136:137], v0 offset:8192\n\tds_read_b64_tr_b16 v[138:139], v0 offset:8704\n\tds_read_b64_tr_b16 v[140:141], v1 offset:8192\n\tds_read_b64_tr_b16 v[142:143], v1 offset:8704\n\t" \
    "buffer_load_dwordx2 v[118:119], v96, s[28:31], s71 offen offset:160\n\tds_read_b64_tr_b16 v[144:145], v2 offset:8192\n\tds_read_b64_tr_b16 v[146:147], v2 offset:8704\n\tds_read_b64_tr_b16 v[148:149], v3 offset:8192\n\tds_read_b64_tr_b16 v[150:151], v3 offset:8704\n\tv_lshrrev_b32 v229, 3, v244\n\tbuffer_load_ushort v228, v229, s[32:35], 0 offen offset:0\n\tds_read_b64_tr_b16 v[152:153], v0 offset:12288\n\t" \
    "ds_read_b64_tr_b16 v[154:155], v0 offset:12800\n\tds_read_b64_tr_b16 v[156:157], v1 offset:12288\n\tds_read_b64_tr_b16 v[158:159], v1 offset:12800\n\tbuffer_load_ushort v232, v229, s[32:35], 0 offen offset:32\n\tds_read_b64_tr_b16 v[160:161], v2 offset:12288\n\tds_read_b64_tr_b16 v[162:163], v2 offset:12800\n\tds_read_b64_tr_b16 v[164:165], v3 offset:12288\n\tds_read_b64_tr_b16 v[166:167], v3 offset:12800\n\t" \
    "s_cmp_ge_u32 s43, 11\n\ts_cselect_b32 s69, s70, s43\n\ts_mul_i32 s69, s69, 294912\n\ts_add_u32 s69, s69, 1474688\n\ts_add_u32 m0, s72, 45056\n\ts_nop 0\n\tbuffer_load_dwordx4 v10, s[12:15], s69 offen lds\n\tds_read_b64_tr_b16 v[168:169], v0 offset:16384\n\t" \
    "ds_read_b64_tr_b16 v[170:171], v0 offset:16896\n\tds_read_b64_tr_b16 v[172:173], v1 offset:16384\n\tds_read_b64_tr_b16 v[174:175], v1 offset:16896\n\ts_add_u32 m0, s72, 46080\n\ts_nop 0\n\tbuffer_load_dwordx4 v11, s[12:15], s69 offen lds\n\tds_read_b64_tr_b16 v[176:177], v2 offset:16384\n\tds_read_b64_tr_b16 v[178:179], v2 offset:16896\n\t" \
    "ds_read_b64_tr_b16 v[180:181], v3 offset:16384\n\tds_read_b64_tr_b16 v[182:183], v3 offset:16896\n\tds_read_b64_tr_b16 v[184:185], v0 offset:20480\n\tds_read_b64_tr_b16 v[186:187], v0 offset:20992\n\tds_read_b64_tr_b16 v[188:189], v1 offset:20480\n\tds_read_b64_tr_b16 v[190:191], v1 offset:20992\n\tds_read_b64_tr_b16 v[192:193], v2 offset:20480\n\tds_read_b64_tr_b16 v[194:195], v2 offset:20992\n\t" \
    "ds_read_b64_tr_b16 v[196:197], v3 offset:20480\n\tds_read_b64_tr_b16 v[198:199], v3 offset:20992\n\tds_read_b64_tr_b16 v[200:201], v0 offset:24576\n\tds_read_b64_tr_b16 v[202:203], v0 offset:25088\n\tds_read_b64_tr_b16 v[204:205], v1 offset:24576\n\tds_read_b64_tr_b16 v[206:207], v1 offset:25088\n\tds_read_b64_tr_b16 v[208:209], v2 offset:24576\n\tds_read_b64_tr_b16 v[210:211], v2 offset:25088\n\t" \
    "ds_read_b64_tr_b16 v[212:213], v3 offset:24576\n\tds_read_b64_tr_b16 v[214:215], v3 offset:25088\n\ts_waitcnt vmcnt(9) lgkmcnt(0)\n\ts_barrier\n\ts_cmp_ge_u32 s43, 10\n\ts_cselect_b32 s69, s70, s43\n\ts_mul_i32 s69, s69, 294912\n\ts_add_u32 s69, s69, 1769472\n\t" \
    "s_add_u32 m0, s72, 49152\n\ts_nop 0\n\tbuffer_load_dwordx4 v10, s[12:15], s69 offen lds\n\tds_read_b64_tr_b16 v[216:217], v0 offset:28672\n\tds_read_b64_tr_b16 v[218:219], v0 offset:29184\n\tds_read_b64_tr_b16 a[64:65], v1 offset:28672\n\tds_read_b64_tr_b16 a[66:67], v1 offset:29184\n\ts_add_u32 m0, s72, 50176\n\t" \
    "s_nop 0\n\tbuffer_load_dwordx4 v11, s[12:15], s69 offen lds\n\tds_read_b64_tr_b16 a[68:69], v2 offset:28672\n\tds_read_b64_tr_b16 a[70:71], v2 offset:29184\n\tds_read_b64_tr_b16 a[72:73], v3 offset:28672\n\tds_read_b64_tr_b16 a[74:75], v3 offset:29184\n\ts_cmp_ge_u32 s43, 10\n\ts_cselect_b32 s69, s70, s43\n\t" \
    "s_mul_i32 s69, s69, 294912\n\ts_add_u32 s69, s69, 1769600\n\ts_add_u32 m0, s72, 53248\n\ts_nop 0\n\tbuffer_load_dwordx4 v10, s[12:15], s69 offen lds\n\tds_read_b64_tr_b16 a[76:77], v0 offset:32768\n\tds_read_b64_tr_b16 a[78:79], v0 offset:33280\n\tds_read_b64_tr_b16 a[80:81], v1 offset:32768\n\t" \
    "ds_read_b64_tr_b16 a[82:83], v1 offset:33280\n\ts_add_u32 m0, s72, 54272\n\ts_nop 0\n\tbuffer_load_dwordx4 v11, s[12:15], s69 offen lds\n\tds_read_b64_tr_b16 a[84:85], v2 offset:32768\n\tds_read_b64_tr_b16 a[86:87], v2 offset:33280\n\tds_read_b64_tr_b16 a[88:89], v3 offset:32768\n\tds_read_b64_tr_b16 a[90:91], v3 offset:33280\n\t" \
    "s_cmp_ge_u32 s43, 9\n\ts_cselect_b32 s69, s70, s43\n\ts_mul_i32 s69, s69, 294912\n\ts_add_u32 s69, s69, 2064384\n\ts_add_u32 m0, s72, 57344\n\ts_nop 0\n\tbuffer_load_dwordx4 v10, s[12:15], s69 offen lds\n\tds_read_b64_tr_b16 a[92:93], v0 offset:36864\n\t" \
    "ds_read_b64_tr_b16 a[94:95], v0 offset:37376\n\tds_read_b64_tr_b16 a[96:97], v1 offset:36864\n\tds_read_b64_tr_b16 a[98:99], v1 offset:37376\n\ts_add_u32 m0, s72, 58368\n\ts_nop 0\n\tbuffer_load_dwordx4 v11, s[12:15], s69 offen lds\n\tds_read_b64_tr_b16 a[100:101], v2 offset:36864\n\tds_read_b64_tr_b16 a[102:103], v2 offset:37376\n\t" \
    "ds_read_b64_tr_b16 a[104:105], v3 offset:36864\n\tds_read_b64_tr_b16 a[106:107], v3 offset:37376\n\ts_cmp_ge_u32 s43, 9\n\ts_cselect_b32 s69, s70, s43\n\ts_mul_i32 s69, s69, 294912\n\ts_add_u32 s69, s69, 2064512\n\ts_add_u32 m0, s72, 61440\n\ts_nop 0\n\t" \
    "buffer_load_dwordx4 v10, s[12:15], s69 offen lds\n\tds_read_b64_tr_b16 a[108:109], v0 offset:40960\n\tds_read_b64_tr_b16 a[110:111], v0 offset:41472\n\tds_read_b64_tr_b16 a[112:113], v1 offset:40960\n\tds_read_b64_tr_b16 a[114:115], v1 offset:41472\n\ts_add_u32 m0, s72, 62464\n\ts_nop 0\n\tbuffer_load_dwordx4 v11, s[12:15], s69 offen lds\n\t" \
    "ds_read_b64_tr_b16 a[116:117], v2 offset:40960\n\tds_read_b64_tr_b16 a[118:119], v2 offset:41472\n\tds_read_b64_tr_b16 a[120:121], v3 offset:40960\n\tds_read_b64_tr_b16 a[122:123], v3 offset:41472\n\ts_waitcnt vmcnt(8) lgkmcnt(0)\n\ts_barrier\n\ts_cmp_ge_u32 s43, 8\n\ts_cselect_b32 s69, s70, s43\n\t" \
    "s_mul_i32 s69, s69, 294912\n\ts_add_u32 s69, s69, 2359296\n\ts_add_u32 m0, s72, 0\n\ts_nop 0\n\tbuffer_load_dwordx4 v10, s[12:15], s69 offen lds\n\tds_read_b64_tr_b16 a[124:125], v0 offset:45056\n\tds_read_b64_tr_b16 a[126:127], v0 offset:45568\n\tds_read_b64_tr_b16 a[128:129], v1 offset:45056\n\t" \
    "ds_read_b64_tr_b16 a[130:131], v1 offset:45568\n\ts_add_u32 m0, s72, 1024\n\ts_nop 0\n\tbuffer_load_dwordx4 v11, s[12:15], s69 offen lds\n\tds_read_b64_tr_b16 a[132:133], v2 offset:45056\n\tds_read_b64_tr_b16 a[134:135], v2 offset:45568\n\tds_read_b64_tr_b16 a[136:137], v3 offset:45056\n\tds_read_b64_tr_b16 a[138:139], v3 offset:45568\n\t" \
    "s_cmp_ge_u32 s43, 8\n\ts_cselect_b32 s69, s70, s43\n\ts_mul_i32 s69, s69, 294912\n\ts_add_u32 s69, s69, 2359424\n\ts_add_u32 m0, s72, 4096\n\ts_nop 0\n\tbuffer_load_dwordx4 v10, s[12:15], s69 offen lds\n\ts_add_u32 m0, s72, 5120\n\t" \
    "s_nop 0\n\tbuffer_load_dwordx4 v11, s[12:15], s69 offen lds\n\ts_cmp_ge_u32 s43, 7\n\ts_cselect_b32 s69, s70, s43\n\ts_mul_i32 s69, s69, 294912\n\ts_add_u32 s69, s69, 2654208\n\ts_add_u32 m0, s72, 8192\n\ts_nop 0\n\t" \
    "buffer_load_dwordx4 v10, s[12:15], s69 offen lds\n\ts_add_u32 m0, s72, 9216\n\ts_nop 0\n\tbuffer_load_dwordx4 v11, s[12:15], s69 offen lds\n\ts_cmp_ge_u32 s43, 7\n\ts_cselect_b32 s69, s70, s43\n\ts_mul_i32 s69, s69, 294912\n\ts_add_u32 s69, s69, 2654336\n\t" \
    "s_add_u32 m0, s72, 12288\n\ts_nop 0\n\tbuffer_load_dwordx4 v10, s[12:15], s69 offen lds\n\ts_add_u32 m0, s72, 13312\n\ts_nop 0\n\tbuffer_load_dwordx4 v11, s[12:15], s69 offen lds\n\ts_waitcnt lgkmcnt(0)\n\ts_barrier\n\t" \
    "s_cmp_ge_u32 s43, 6\n\ts_cselect_b32 s69, s70, s43\n\ts_mul_i32 s69, s69, 294912\n\ts_add_u32 s69, s69, 2949120\n\ts_add_u32 m0, s72, 16384\n\ts_nop 0\n\tbuffer_load_dwordx4 v10, s[12:15], s69 offen lds\n\ts_add_u32 m0, s72, 17408\n\t" \
    "s_nop 0\n\tbuffer_load_dwordx4 v11, s[12:15], s69 offen lds\n\tbuffer_load_dwordx4 v[250:253], v8, s[8:11], s47 offen sc1\n\ts_cmp_ge_u32 s43, 6\n\ts_cselect_b32 s69, s70, s43\n\ts_mul_i32 s69, s69, 294912\n\ts_add_u32 s69, s69, 2949248\n\ts_add_u32 m0, s72, 20480\n\t" \
    "s_nop 0\n\tbuffer_load_dwordx4 v10, s[12:15], s69 offen lds\n\ts_add_u32 m0, s72, 21504\n\ts_nop 0\n\tbuffer_load_dwordx4 v11, s[12:15], s69 offen lds\n\ts_cmp_ge_u32 s43, 5\n\ts_cselect_b32 s69, s70, s43\n\ts_mul_i32 s69, s69, 294912\n\t" \
    "s_add_u32 s69, s69, 3244032\n\ts_add_u32 m0, s72, 24576\n\ts_nop 0\n\tbuffer_load_dwordx4 v10, s[12:15], s69 offen lds\n\ts_add_u32 m0, s72, 25600\n\ts_nop 0\n\tbuffer_load_dwordx4 v11, s[12:15], s69 offen lds\n\ts_cmp_ge_u32 s43, 5\n\t" \
    "s_cselect_b32 s69, s70, s43\n\ts_mul_i32 s69, s69, 294912\n\ts_add_u32 s69, s69, 3244160\n\ts_add_u32 m0, s72, 28672\n\ts_nop 0\n\tbuffer_load_dwordx4 v10, s[12:15], s69 offen lds\n\ts_add_u32 m0, s72, 29696\n\ts_nop 0\n\t" \
    "buffer_load_dwordx4 v11, s[12:15], s69 offen lds\n\ts_cmp_ge_u32 s43, 4\n\ts_cselect_b32 s69, s70, s43\n\ts_mul_i32 s69, s69, 294912\n\ts_add_u32 s69, s69, 3538944\n\ts_add_u32 m0, s72, 32768\n\ts_nop 0\n\tbuffer_load_dwordx4 v10, s[12:15], s69 offen lds\n\t" \
    "s_add_u32 m0, s72, 33792\n\ts_nop 0\n\tbuffer_load_dwordx4 v11, s[12:15], s69 offen lds\n\ts_cmp_ge_u32 s43, 4\n\ts_cselect_b32 s69, s70, s43\n\ts_mul_i32 s69, s69, 294912\n\ts_add_u32 s69, s69, 3539072\n\ts_add_u32 m0, s72, 36864\n\t" \
    "s_nop 0\n\tbuffer_load_dwordx4 v10, s[12:15], s69 offen lds\n\ts_add_u32 m0, s72, 37888\n\ts_nop 0\n\tbuffer_load_dwordx4 v11, s[12:15], s69 offen lds\n\ts_cmp_ge_u32 s43, 3\n\ts_cselect_b32 s69, s70, s43\n\ts_mul_i32 s69, s69, 294912\n\t" \
    "s_add_u32 s69, s69, 3833856\n\ts_add_u32 m0, s72, 40960\n\ts_nop 0\n\tbuffer_load_dwordx4 v10, s[12:15], s69 offen lds\n\ts_add_u32 m0, s72, 41984\n\ts_nop 0\n\tbuffer_load_dwordx4 v11, s[12:15], s69 offen lds\n\ts_cmp_ge_u32 s43, 3\n\t" \
    "s_cselect_b32 s69, s70, s43\n\ts_mul_i32 s69, s69, 294912\n\ts_add_u32 s69, s69, 3833984\n\ts_add_u32 m0, s72, 45056\n\ts_nop 0\n\tbuffer_load_dwordx4 v10, s[12:15], s69 offen lds\n\ts_add_u32 m0, s72, 46080\n\ts_nop 0\n\t" \
    "buffer_load_dwordx4 v11, s[12:15], s69 offen lds\n\ts_waitcnt vmcnt(14)\n\tv_cmp_ne_u64 vcc, s[36:37], v[250:251]\n\tv_cmp_ne_u64 s[74:75], s[36:37], v[252:253]\n\ts_or_b64 s[74:75], s[74:75], vcc\n\ts_cbranch_scc0 106f\n\ts_mov_b32 s71, 1048576\n\t105:\n\t" \
    "s_sleep 1\n\tbuffer_load_dwordx4 v[250:253], v8, s[8:11], s47 offen sc1\n\ts_waitcnt vmcnt(0)\n\tv_cmp_ne_u64 vcc, s[36:37], v[250:251]\n\tv_cmp_ne_u64 s[74:75], s[36:37], v[252:253]\n\ts_or_b64 s[74:75], s[74:75], vcc\n\ts_cbranch_scc0 106f\n\ts_sub_u32 s71, s71, 1\n\t" \
    "s_cbranch_scc0 105b\n\t106:\n\ts_cmp_ge_u32 s43, 16\n\ts_cselect_b32 s68, s70, s43\n\ts_mul_i32 s68, s68, 36864\n\ts_add_u32 s68, s68, s44\n\ts_add_u32 s68, s68, 0\n\tbuffer_load_dwordx4 a[140:143], v8, s[8:11], s68 offen offset:0 sc1\n\t" \
    "buffer_load_dwordx4 a[144:147], v8, s[8:11], s68 offen offset:1024 sc1\n\tbuffer_load_dwordx4 a[148:151], v8, s[8:11], s68 offen offset:2048 sc1\n\ts_cmp_ge_u32 s43, 15\n\ts_cselect_b32 s68, s70, s43\n\ts_mul_i32 s68, s68, 36864\n\ts_add_u32 s68, s68, s44\n\ts_add_u32 s68, s68, 36864\n\tbuffer_load_dwordx4 a[152:155], v8, s[8:11], s68 offen offset:0 sc1\n\t" \
    "buffer_load_dwordx4 a[156:159], v8, s[8:11], s68 offen offset:1024 sc1\n\tbuffer_load_dwordx4 a[160:163], v8, s[8:11], s68 offen offset:2048 sc1\n\ts_cmp_ge_u32 s43, 14\n\ts_cselect_b32 s68, s70, s43\n\ts_mul_i32 s68, s68, 36864\n\ts_add_u32 s68, s68, s44\n\ts_add_u32 s68, s68, 73728\n\tbuffer_load_dwordx4 a[164:167], v8, s[8:11], s68 offen offset:0 sc1\n\t" \
    "buffer_load_dwordx4 a[168:171], v8, s[8:11], s68 offen offset:1024 sc1\n\tbuffer_load_dwordx4 a[172:175], v8, s[8:11], s68 offen offset:2048 sc1\n\ts_cmp_ge_u32 s43, 13\n\ts_cselect_b32 s68, s70, s43\n\ts_mul_i32 s68, s68, 36864\n\ts_add_u32 s68, s68, s44\n\ts_add_u32 s68, s68, 110592\n\tbuffer_load_dwordx4 a[176:179], v8, s[8:11], s68 offen offset:0 sc1\n\t" \
    "buffer_load_dwordx4 a[180:183], v8, s[8:11], s68 offen offset:1024 sc1\n\tbuffer_load_dwordx4 a[184:187], v8, s[8:11], s68 offen offset:2048 sc1\n\tv_lshlrev_b32 v96, 16, v112\n\tv_and_b32 v97, 0xffff0000, v112\n\tv_lshlrev_b32 v98, 16, v113\n\tv_and_b32 v99, 0xffff0000, v113\n\tv_lshlrev_b32 v100, 16, v114\n\tv_and_b32 v101, 0xffff0000, v114\n\t" \
    "v_lshlrev_b32 v102, 16, v115\n\tv_and_b32 v103, 0xffff0000, v115\n\tv_lshlrev_b32 v104, 16, v116\n\tv_and_b32 v105, 0xffff0000, v116\n\tv_lshlrev_b32 v106, 16, v117\n\tv_and_b32 v107, 0xffff0000, v117\n\tv_lshlrev_b32 v108, 16, v118\n\tv_and_b32 v109, 0xffff0000, v118\n\t" \
    "v_lshlrev_b32 v110, 16, v119\n\tv_and_b32 v111, 0xffff0000, v119\n\tv_lshlrev_b32 v228, 16, v228\n\tv_mov_b32 v229, v228\n\tv_mov_b32 v230, v228\n\tv_mov_b32 v231, v228\n\tv_lshlrev_b32 v232, 16, v232\n\tv_mov_b32 v233, v232\n\t" \
    "v_mov_b32 v234, v232\n\tv_mov_b32 v235, v232\n\ts_barrier\n\ts_waitcnt vmcnt(9)\n\tv_mfma_f32_16x16x32_bf16 a[0:3], a[48:51], a[140:143], 0\n\ts_cmp_ge_u32 s43, 12\n\ts_cselect_b32 s68, s70, s43\n\ts_mul_i32 s68, s68, 36864\n\t" \
    "s_add_u32 s68, s68, s44\n\tv_mfma_f32_16x16x32_bf16 a[8:11], a[48:51], a[144:147], 0\n\ts_add_u32 s68, s68, 147456\n\tv_mfma_f32_16x16x32_bf16 a[16:19], a[48:51], a[148:151], 0\n\tv_mfma_f32_16x16x32_bf16 a[4:7], a[52:55], a[140:143], 0\n\tv_mfma_f32_16x16x32_bf16 a[12:15], a[52:55], a[144:147], 0\n\tbuffer_load_dwordx4 a[188:191], v8, s[8:11], s68 offen offset:0 sc1\n\tv_mfma_f32_16x16x32_bf16 a[20:23], a[52:55], a[148:151], 0\n\t" \
    "v_mfma_f32_16x16x32_bf16 v[48:51], a[56:59], a[140:143], v[96:99]\n\tv_mfma_f32_16x16x32_bf16 v[56:59], a[56:59], a[144:147], v[96:99]\n\tbuffer_load_dwordx4 a[192:195], v8, s[8:11], s68 offen offset:1024 sc1\n\tv_mfma_f32_16x16x32_bf16 v[64:67], a[56:59], a[148:151], v[96:99]\n\tv_mfma_f32_16x16x32_bf16 v[52:55], a[60:63], a[140:143], v[100:103]\n\tv_mfma_f32_16x16x32_bf16 v[60:63], a[60:63], a[144:147], v[100:103]\n\tbuffer_load_dwordx4 a[196:199], v8, s[8:11], s68 offen offset:2048 sc1\n\ts_cmp_ge_u32 s43, 11\n\t" \
    "s_cselect_b32 s68, s70, s43\n\ts_mul_i32 s68, s68, 36864\n\tv_mfma_f32_16x16x32_bf16 v[68:71], a[60:63], a[148:151], v[100:103]\n\ts_add_u32 s68, s68, s44\n\ts_add_u32 s68, s68, 184320\n\tv_mfma_f32_16x16x32_bf16 a[24:27], v[120:123], a[140:143], 0\n\tv_mfma_f32_16x16x32_bf16 a[32:35], v[120:123], a[144:147], 0\n\tbuffer_load_dwordx4 a[48:51], v8, s[8:11], s68 offen offset:0 sc1\n\t" \
    "v_mfma_f32_16x16x32_bf16 a[40:43], v[120:123], a[148:151], 0\n\tv_mfma_f32_16x16x32_bf16 a[28:31], v[124:127], a[140:143], 0\n\tv_mfma_f32_16x16x32_bf16 a[36:39], v[124:127], a[144:147], 0\n\tbuffer_load_dwordx4 a[52:55], v8, s[8:11], s68 offen offset:1024 sc1\n\tv_mfma_f32_16x16x32_bf16 a[44:47], v[124:127], a[148:151], 0\n\tv_mfma_f32_16x16x32_bf16 v[72:75], v[128:131], a[140:143], v[104:107]\n\tv_mfma_f32_16x16x32_bf16 v[80:83], v[128:131], a[144:147], v[104:107]\n\tbuffer_load_dwordx4 a[56:59], v8, s[8:11], s68 offen offset:2048 sc1\n\t" \
    "s_cmp_ge_u32 s43, 10\n\ts_cselect_b32 s68, s70, s43\n\ts_mul_i32 s68, s68, 36864\n\tv_mfma_f32_16x16x32_bf16 v[88:91], v[128:131], a[148:151], v[104:107]\n\ts_add_u32 s68, s68, s44\n\ts_add_u32 s68, s68, 221184\n\tv_mfma_f32_16x16x32_bf16 v[76:79], v[132:135], a[140:143], v[108:111]\n\tv_mfma_f32_16x16x32_bf16 v[84:87], v[132:135], a[144:147], v[108:111]\n\t" \
    "buffer_load_dwordx4 v[120:123], v8, s[8:11], s68 offen offset:0 sc1\n\tv_mfma_f32_16x16x32_bf16 v[92:95], v[132:135], a[148:151], v[108:111]\n\ts_waitcnt vmcnt(13)\n\tv_mfma_f32_16x16x32_bf16 a[0:3], v[136:139], a[152:155], a[0:3]\n\tv_mfma_f32_16x16x32_bf16 a[8:11], v[136:139], a[156:159], a[8:11]\n\tv_mfma_f32_16x16x32_bf16 a[16:19], v[136:139], a[160:163], a[16:19]\n\tv_mfma_f32_16x16x32_bf16 a[4:7], v[140:143], a[152:155], a[4:7]\n\tv_mfma_f32_16x16x32_bf16 a[12:15], v[140:143], a[156:159], a[12:15]\n\t" \
    "buffer_load_dwordx4 v[124:127], v8, s[8:11], s68 offen offset:1024 sc1\n\tv_mfma_f32_16x16x32_bf16 a[20:23], v[140:143], a[160:163], a[20:23]\n\tv_mfma_f32_16x16x32_bf16 v[48:51], v[144:147], a[152:155], v[48:51]\n\tv_mfma_f32_16x16x32_bf16 v[56:59], v[144:147], a[156:159], v[56:59]\n\tbuffer_load_dwordx4 a[60:63], v8, s[8:11], s68 offen offset:2048 sc1\n\ts_cmp_ge_u32 s43, 9\n\ts_cselect_b32 s68, s70, s43\n\ts_mul_i32 s68, s68, 36864\n\t" \
    "v_mfma_f32_16x16x32_bf16 v[64:67], v[144:147], a[160:163], v[64:67]\n\ts_add_u32 s68, s68, s44\n\ts_add_u32 s68, s68, 258048\n\tv_mfma_f32_16x16x32_bf16 v[52:55], v[148:151], a[152:155], v[52:55]\n\tv_mfma_f32_16x16x32_bf16 v[60:63], v[148:151], a[156:159], v[60:63]\n\tv_mfma_f32_16x16x32_bf16 v[68:71], v[148:151], a[160:163], v[68:71]\n\ts_barrier\n\tv_mfma_f32_16x16x32_bf16 a[24:27], v[152:155], a[152:155], a[24:27]\n\t" \
    "v_mfma_f32_16x16x32_bf16 a[32:35], v[152:155], a[156:159], a[32:35]\n\tbuffer_load_dwordx4 v[128:131], v8, s[8:11], s68 offen offset:0 sc1\n\tv_mfma_f32_16x16x32_bf16 a[40:43], v[152:155], a[160:163], a[40:43]\n\tv_mfma_f32_16x16x32_bf16 a[28:31], v[156:159], a[152:155], a[28:31]\n\tv_mfma_f32_16x16x32_bf16 a[36:39], v[156:159], a[156:159], a[36:39]\n\tbuffer_load_dwordx4 v[132:135], v8, s[8:11], s68 offen offset:1024 sc1\n\tv_mfma_f32_16x16x32_bf16 a[44:47], v[156:159], a[160:163], a[44:47]\n\tv_mfma_f32_16x16x32_bf16 v[72:75], v[160:163], a[152:155], v[72:75]\n\t" \
    "v_mfma_f32_16x16x32_bf16 v[80:83], v[160:163], a[156:159], v[80:83]\n\tbuffer_load_dwordx4 v[136:139], v8, s[8:11], s68 offen offset:2048 sc1\n\ts_cmp_ge_u32 s43, 8\n\ts_cselect_b32 s68, s70, s43\n\ts_mul_i32 s68, s68, 36864\n\tv_mfma_f32_16x16x32_bf16 v[88:91], v[160:163], a[160:163], v[88:91]\n\ts_add_u32 s68, s68, s44\n\ts_add_u32 s68, s68, 294912\n\t" \
    "v_mfma_f32_16x16x32_bf16 v[76:79], v[164:167], a[152:155], v[76:79]\n\tv_mfma_f32_16x16x32_bf16 v[84:87], v[164:167], a[156:159], v[84:87]\n\tbuffer_load_dwordx4 v[140:143], v8, s[8:11], s68 offen offset:0 sc1\n\tv_mfma_f32_16x16x32_bf16 v[92:95], v[164:167], a[160:163], v[92:95]\n\ts_waitcnt vmcnt(16)\n\tv_mfma_f32_16x16x32_bf16 a[0:3], v[168:171], a[164:167], a[0:3]\n\tv_mfma_f32_16x16x32_bf16 a[8:11], v[168:171], a[168:171], a[8:11]\n\tbuffer_load_dwordx4 v[144:147], v8, s[8:11], s68 offen offset:1024 sc1\n\t" \
    "v_mfma_f32_16x16x32_bf16 a[16:19], v[168:171], a[172:175], a[16:19]\n\tv_mfma_f32_16x16x32_bf16 a[4:7], v[172:175], a[164:167], a[4:7]\n\tv_mfma_f32_16x16x32_bf16 a[12:15], v[172:175], a[168:171], a[12:15]\n\tv_mfma_f32_16x16x32_bf16 a[20:23], v[172:175], a[172:175], a[20:23]\n\tv_mfma_f32_16x16x32_bf16 v[48:51], v[176:179], a[164:167], v[48:51]\n\tv_mfma_f32_16x16x32_bf16 v[56:59], v[176:179], a[168:171], v[56:59]\n\tbuffer_load_dwordx4 v[148:151], v8, s[8:11], s68 offen offset:2048 sc1\n\ts_cmp_ge_u32 s43, 7\n\t" \
    "s_cselect_b32 s68, s70, s43\n\ts_mul_i32 s68, s68, 36864\n\tv_mfma_f32_16x16x32_bf16 v[64:67], v[176:179], a[172:175], v[64:67]\n\ts_add_u32 s68, s68, s44\n\ts_add_u32 s68, s68, 331776\n\tv_mfma_f32_16x16x32_bf16 v[52:55], v[180:183], a[164:167], v[52:55]\n\tv_mfma_f32_16x16x32_bf16 v[60:63], v[180:183], a[168:171], v[60:63]\n\tbuffer_load_dwordx4 v[152:155], v8, s[8:11], s68 offen offset:0 sc1\n\t" \
    "v_mfma_f32_16x16x32_bf16 v[68:71], v[180:183], a[172:175], v[68:71]\n\tv_mfma_f32_16x16x32_bf16 a[24:27], v[184:187], a[164:167], a[24:27]\n\tv_mfma_f32_16x16x32_bf16 a[32:35], v[184:187], a[168:171], a[32:35]\n\tbuffer_load_dwordx4 v[156:159], v8, s[8:11], s68 offen offset:1024 sc1\n\tv_mfma_f32_16x16x32_bf16 a[40:43], v[184:187], a[172:175], a[40:43]\n\tv_mfma_f32_16x16x32_bf16 a[28:31], v[188:191], a[164:167], a[28:31]\n\tv_mfma_f32_16x16x32_bf16 a[36:39], v[188:191], a[168:171], a[36:39]\n\tv_mfma_f32_16x16x32_bf16 a[44:47], v[188:191], a[172:175], a[44:47]\n\t" \
    "v_mfma_f32_16x16x32_bf16 v[72:75], v[192:195], a[164:167], v[72:75]\n\tv_mfma_f32_16x16x32_bf16 v[80:83], v[192:195], a[168:171], v[80:83]\n\tbuffer_load_dwordx4 v[160:163], v8, s[8:11], s68 offen offset:2048 sc1\n\ts_cmp_ge_u32 s43, 6\n\ts_cselect_b32 s68, s70, s43\n\ts_mul_i32 s68, s68, 36864\n\tv_mfma_f32_16x16x32_bf16 v[88:91], v[192:195], a[172:175], v[88:91]\n\ts_add_u32 s68, s68, s44\n\t" \
    "s_add_u32 s68, s68, 368640\n\tv_mfma_f32_16x16x32_bf16 v[76:79], v[196:199], a[164:167], v[76:79]\n\tv_mfma_f32_16x16x32_bf16 v[84:87], v[196:199], a[168:171], v[84:87]\n\tbuffer_load_dwordx4 v[164:167], v8, s[8:11], s68 offen offset:0 sc1\n\tv_mfma_f32_16x16x32_bf16 v[92:95], v[196:199], a[172:175], v[92:95]\n\ts_barrier\n\ts_waitcnt vmcnt(19)\n\tv_mfma_f32_16x16x32_bf16 a[0:3], v[200:203], a[176:179], a[0:3]\n\t" \
    "v_mfma_f32_16x16x32_bf16 a[8:11], v[200:203], a[180:183], a[8:11]\n\tbuffer_load_dwordx4 v[168:171], v8, s[8:11], s68 offen offset:1024 sc1\n\tv_mfma_f32_16x16x32_bf16 a[16:19], v[200:203], a[184:187], a[16:19]\n\tv_mfma_f32_16x16x32_bf16 a[4:7], v[204:207], a[176:179], a[4:7]\n\tv_mfma_f32_16x16x32_bf16 a[12:15], v[204:207], a[180:183], a[12:15]\n\tbuffer_load_dwordx4 v[172:175], v8, s[8:11], s68 offen offset:2048 sc1\n\ts_cmp_ge_u32 s43, 5\n\ts_cselect_b32 s68, s70, s43\n\t" \
    "s_mul_i32 s68, s68, 36864\n\tv_mfma_f32_16x16x32_bf16 a[20:23], v[204:207], a[184:187], a[20:23]\n\ts_add_u32 s68, s68, s44\n\ts_add_u32 s68, s68, 405504\n\tv_mfma_f32_16x16x32_bf16 v[48:51], v[208:211], a[176:179], v[48:51]\n\tv_mfma_f32_16x16x32_bf16 v[56:59], v[208:211], a[180:183], v[56:59]\n\tv_mfma_f32_16x16x32_bf16 v[64:67], v[208:211], a[184:187], v[64:67]\n\tv_mfma_f32_16x16x32_bf16 v[52:55], v[212:215], a[176:179], v[52:55]\n\t" \
    "v_mfma_f32_16x16x32_bf16 v[60:63], v[212:215], a[180:183], v[60:63]\n\tbuffer_load_dwordx4 v[176:179], v8, s[8:11], s68 offen offset:0 sc1\n\tv_mfma_f32_16x16x32_bf16 v[68:71], v[212:215], a[184:187], v[68:71]\n\tv_mfma_f32_16x16x32_bf16 a[24:27], v[216:219], a[176:179], a[24:27]\n\tv_mfma_f32_16x16x32_bf16 a[32:35], v[216:219], a[180:183], a[32:35]\n\tbuffer_load_dwordx4 v[180:183], v8, s[8:11], s68 offen offset:1024 sc1\n\tv_mfma_f32_16x16x32_bf16 a[40:43], v[216:219], a[184:187], a[40:43]\n\tv_mfma_f32_16x16x32_bf16 a[28:31], a[64:67], a[176:179], a[28:31]\n\t" \
    "v_mfma_f32_16x16x32_bf16 a[36:39], a[64:67], a[180:183], a[36:39]\n\tbuffer_load_dwordx4 v[184:187], v8, s[8:11], s68 offen offset:2048 sc1\n\ts_cmp_ge_u32 s43, 4\n\ts_cselect_b32 s68, s70, s43\n\ts_mul_i32 s68, s68, 36864\n\tv_mfma_f32_16x16x32_bf16 a[44:47], a[64:67], a[184:187], a[44:47]\n\ts_add_u32 s68, s68, s44\n\ts_add_u32 s68, s68, 442368\n\t" \
    "v_mfma_f32_16x16x32_bf16 v[72:75], a[68:71], a[176:179], v[72:75]\n\tv_mfma_f32_16x16x32_bf16 v[80:83], a[68:71], a[180:183], v[80:83]\n\tv_mfma_f32_16x16x32_bf16 v[88:91], a[68:71], a[184:187], v[88:91]\n\tv_mfma_f32_16x16x32_bf16 v[76:79], a[72:75], a[176:179], v[76:79]\n\tv_mfma_f32_16x16x32_bf16 v[84:87], a[72:75], a[180:183], v[84:87]\n\tbuffer_load_dwordx4 v[188:191], v8, s[8:11], s68 offen offset:0 sc1\n\tv_mfma_f32_16x16x32_bf16 v[92:95], a[72:75], a[184:187], v[92:95]\n\ts_waitcnt vmcnt(22)\n\t" \
    "v_mfma_f32_16x16x32_bf16 a[0:3], a[76:79], a[188:191], a[0:3]\n\tv_mfma_f32_16x16x32_bf16 a[8:11], a[76:79], a[192:195], a[8:11]\n\tbuffer_load_dwordx4 v[192:195], v8, s[8:11], s68 offen offset:1024 sc1\n\tv_mfma_f32_16x16x32_bf16 a[16:19], a[76:79], a[196:199], a[16:19]\n\tv_mfma_f32_16x16x32_bf16 a[4:7], a[80:83], a[188:191], a[4:7]\n\tv_mfma_f32_16x16x32_bf16 a[12:15], a[80:83], a[192:195], a[12:15]\n\tv_mfma_f32_16x16x32_bf16 a[20:23], a[80:83], a[196:199], a[20:23]\n\tv_mfma_f32_16x16x32_bf16 v[48:51], a[84:87], a[188:191], v[48:51]\n\t" \
    "v_mfma_f32_16x16x32_bf16 v[56:59], a[84:87], a[192:195], v[56:59]\n\tbuffer_load_dwordx4 v[196:199], v8, s[8:11], s68 offen offset:2048 sc1\n\ts_cmp_ge_u32 s43, 3\n\ts_cselect_b32 s68, s70, s43\n\ts_mul_i32 s68, s68, 36864\n\tv_mfma_f32_16x16x32_bf16 v[64:67], a[84:87], a[196:199], v[64:67]\n\ts_add_u32 s68, s68, s44\n\ts_add_u32 s68, s68, 479232\n\t" \
    "v_mfma_f32_16x16x32_bf16 v[52:55], a[88:91], a[188:191], v[52:55]\n\tv_mfma_f32_16x16x32_bf16 v[60:63], a[88:91], a[192:195], v[60:63]\n\tbuffer_load_dwordx4 v[200:203], v8, s[8:11], s68 offen offset:0 sc1\n\tv_mfma_f32_16x16x32_bf16 v[68:71], a[88:91], a[196:199], v[68:71]\n\ts_barrier\n\tv_mfma_f32_16x16x32_bf16 a[24:27], a[92:95], a[188:191], a[24:27]\n\tv_mfma_f32_16x16x32_bf16 a[32:35], a[92:95], a[192:195], a[32:35]\n\tbuffer_load_dwordx4 v[204:207], v8, s[8:11], s68 offen offset:1024 sc1\n\t" \
    "v_mfma_f32_16x16x32_bf16 a[40:43], a[92:95], a[196:199], a[40:43]\n\tv_mfma_f32_16x16x32_bf16 a[28:31], a[96:99], a[188:191], a[28:31]\n\tv_mfma_f32_16x16x32_bf16 a[36:39], a[96:99], a[192:195], a[36:39]\n\tbuffer_load_dwordx4 v[208:211], v8, s[8:11], s68 offen offset:2048 sc1\n\ts_cmp_ge_u32 s43, 2\n\ts_cselect_b32 s68, s70, s43\n\ts_mul_i32 s68, s68, 36864\n\tv_mfma_f32_16x16x32_bf16 a[44:47], a[96:99], a[196:199], a[44:47]\n\t" \
    "s_add_u32 s68, s68, s44\n\ts_add_u32 s68, s68, 516096\n\tv_mfma_f32_16x16x32_bf16 v[72:75], a[100:103], a[188:191], v[72:75]\n\tv_mfma_f32_16x16x32_bf16 v[80:83], a[100:103], a[192:195], v[80:83]\n\tbuffer_load_dwordx4 v[212:215], v8, s[8:11], s68 offen offset:0 sc1\n\tv_mfma_f32_16x16x32_bf16 v[88:91], a[100:103], a[196:199], v[88:91]\n\tv_mfma_f32_16x16x32_bf16 v[76:79], a[104:107], a[188:191], v[76:79]\n\tv_mfma_f32_16x16x32_bf16 v[84:87], a[104:107], a[192:195], v[84:87]\n\t" \
    "v_mfma_f32_16x16x32_bf16 v[92:95], a[104:107], a[196:199], v[92:95]\n\ts_waitcnt vmcnt(25)\n\tv_mfma_f32_16x16x32_bf16 a[0:3], a[108:111], a[48:51], a[0:3]\n\tv_mfma_f32_16x16x32_bf16 a[8:11], a[108:111], a[52:55], a[8:11]\n\tbuffer_load_dwordx4 v[216:219], v8, s[8:11], s68 offen offset:1024 sc1\n\tv_mfma_f32_16x16x32_bf16 a[16:19], a[108:111], a[56:59], a[16:19]\n\tv_mfma_f32_16x16x32_bf16 a[4:7], a[112:115], a[48:51], a[4:7]\n\tv_mfma_f32_16x16x32_bf16 a[12:15], a[112:115], a[52:55], a[12:15]\n\t" \
    "buffer_load_dwordx4 a[64:67], v8, s[8:11], s68 offen offset:2048 sc1\n\ts_cmp_ge_u32 s43, 1\n\ts_cselect_b32 s68, s70, s43\n\ts_mul_i32 s68, s68, 36864\n\tv_mfma_f32_16x16x32_bf16 a[20:23], a[112:115], a[56:59], a[20:23]\n\ts_add_u32 s68, s68, s44\n\ts_add_u32 s68, s68, 552960\n\tv_mfma_f32_16x16x32_bf16 v[48:51], a[116:119], a[48:51], v[48:51]\n\t" \
    "v_mfma_f32_16x16x32_bf16 v[56:59], a[116:119], a[52:55], v[56:59]\n\tbuffer_load_dwordx4 a[68:71], v8, s[8:11], s68 offen offset:0 sc1\n\tv_mfma_f32_16x16x32_bf16 v[64:67], a[116:119], a[56:59], v[64:67]\n\tv_mfma_f32_16x16x32_bf16 v[52:55], a[120:123], a[48:51], v[52:55]\n\tds_read_b64_tr_b16 v[16:17], v0 offset:49152\n\tds_read_b64_tr_b16 v[18:19], v0 offset:49664\n\tv_mfma_f32_16x16x32_bf16 v[60:63], a[120:123], a[52:55], v[60:63]\n\tv_mfma_f32_16x16x32_bf16 v[68:71], a[120:123], a[56:59], v[68:71]\n\t" \
    "v_mfma_f32_16x16x32_bf16 a[24:27], a[124:127], a[48:51], a[24:27]\n\tds_read_b64_tr_b16 v[20:21], v1 offset:49152\n\tds_read_b64_tr_b16 v[22:23], v1 offset:49664\n\tv_mfma_f32_16x16x32_bf16 a[32:35], a[124:127], a[52:55], a[32:35]\n\tbuffer_load_dwordx4 a[72:75], v8, s[8:11], s68 offen offset:1024 sc1\n\tv_mfma_f32_16x16x32_bf16 a[40:43], a[124:127], a[56:59], a[40:43]\n\tv_mfma_f32_16x16x32_bf16 a[28:31], a[128:131], a[48:51], a[28:31]\n\tds_read_b64_tr_b16 v[24:25], v2 offset:49152\n\t" \
    "ds_read_b64_tr_b16 v[26:27], v2 offset:49664\n\tv_mfma_f32_16x16x32_bf16 a[36:39], a[128:131], a[52:55], a[36:39]\n\tbuffer_load_dwordx4 a[76:79], v8, s[8:11], s68 offen offset:2048 sc1\n\ts_add_u32 s68, s44, 589824\n\tv_mfma_f32_16x16x32_bf16 a[44:47], a[128:131], a[56:59], a[44:47]\n\tv_mfma_f32_16x16x32_bf16 v[72:75], a[132:135], a[48:51], v[72:75]\n\tds_read_b64_tr_b16 v[28:29], v3 offset:49152\n\tds_read_b64_tr_b16 v[30:31], v3 offset:49664\n\t" \
    "v_mfma_f32_16x16x32_bf16 v[80:83], a[132:135], a[52:55], v[80:83]\n\tv_mfma_f32_16x16x32_bf16 v[88:91], a[132:135], a[56:59], v[88:91]\n\tv_mfma_f32_16x16x32_bf16 v[76:79], a[136:139], a[48:51], v[76:79]\n\tds_read_b64_tr_b16 v[32:33], v0 offset:53248\n\tds_read_b64_tr_b16 v[34:35], v0 offset:53760\n\tv_mfma_f32_16x16x32_bf16 v[84:87], a[136:139], a[52:55], v[84:87]\n\tbuffer_load_dwordx4 a[80:83], v8, s[8:11], s68 offen offset:0 sc1\n\tv_mfma_f32_16x16x32_bf16 v[92:95], a[136:139], a[56:59], v[92:95]\n\t" \
    "s_barrier\n\ts_waitcnt vmcnt(28) lgkmcnt(8)\n\tv_mfma_f32_16x16x32_bf16 a[0:3], v[16:19], v[120:123], a[0:3]\n\tds_read_b64_tr_b16 v[36:37], v1 offset:53248\n\tds_read_b64_tr_b16 v[38:39], v1 offset:53760\n\tv_mfma_f32_16x16x32_bf16 a[8:11], v[16:19], v[124:127], a[8:11]\n\tbuffer_load_dwordx4 a[84:87], v8, s[8:11], s68 offen offset:1024 sc1\n\tv_mfma_f32_16x16x32_bf16 a[16:19], v[16:19], a[60:63], a[16:19]\n\t" \
    "s_waitcnt lgkmcnt(8)\n\tv_mfma_f32_16x16x32_bf16 a[4:7], v[20:23], v[120:123], a[4:7]\n\tds_read_b64_tr_b16 v[40:41], v2 offset:53248\n\tds_read_b64_tr_b16 v[42:43], v2 offset:53760\n\tv_mfma_f32_16x16x32_bf16 a[12:15], v[20:23], v[124:127], a[12:15]\n\tbuffer_load_dwordx4 a[88:91], v8, s[8:11], s68 offen offset:2048 sc1\n\ts_add_u32 s68, s44, 626688\n\tv_mfma_f32_16x16x32_bf16 a[20:23], v[20:23], a[60:63], a[20:23]\n\t" \
    "s_waitcnt lgkmcnt(8)\n\tv_mfma_f32_16x16x32_bf16 v[48:51], v[24:27], v[120:123], v[48:51]\n\tds_read_b64_tr_b16 v[44:45], v3 offset:53248\n\tds_read_b64_tr_b16 v[46:47], v3 offset:53760\n\tv_mfma_f32_16x16x32_bf16 v[56:59], v[24:27], v[124:127], v[56:59]\n\tbuffer_load_dwordx4 a[48:51], v8, s[8:11], s68 offen offset:0 sc1\n\tv_mfma_f32_16x16x32_bf16 v[64:67], v[24:27], a[60:63], v[64:67]\n\ts_waitcnt lgkmcnt(8)\n\t" \
    "v_mfma_f32_16x16x32_bf16 v[52:55], v[28:31], v[120:123], v[52:55]\n\tds_read_b64_tr_b16 v[16:17], v0 offset:57344\n\tds_read_b64_tr_b16 v[18:19], v0 offset:57856\n\tv_mfma_f32_16x16x32_bf16 v[60:63], v[28:31], v[124:127], v[60:63]\n\tbuffer_load_dwordx4 a[52:55], v8, s[8:11], s68 offen offset:1024 sc1\n\tv_mfma_f32_16x16x32_bf16 v[68:71], v[28:31], a[60:63], v[68:71]\n\ts_waitcnt lgkmcnt(8)\n\tv_mfma_f32_16x16x32_bf16 a[24:27], v[32:35], v[120:123], a[24:27]\n\t" \
    "ds_read_b64_tr_b16 v[20:21], v1 offset:57344\n\tds_read_b64_tr_b16 v[22:23], v1 offset:57856\n\tv_mfma_f32_16x16x32_bf16 a[32:35], v[32:35], v[124:127], a[32:35]\n\tv_mfma_f32_16x16x32_bf16 a[40:43], v[32:35], a[60:63], a[40:43]\n\ts_waitcnt lgkmcnt(8)\n\tv_mfma_f32_16x16x32_bf16 a[28:31], v[36:39], v[120:123], a[28:31]\n\tds_read_b64_tr_b16 v[24:25], v2 offset:57344\n\tds_read_b64_tr_b16 v[26:27], v2 offset:57856\n\t" \
    "v_mfma_f32_16x16x32_bf16 a[36:39], v[36:39], v[124:127], a[36:39]\n\tbuffer_load_dwordx4 a[56:59], v8, s[8:11], s68 offen offset:2048 sc1\n\ts_add_u32 s68, s44, 663552\n\tv_mfma_f32_16x16x32_bf16 a[44:47], v[36:39], a[60:63], a[44:47]\n\ts_waitcnt lgkmcnt(8)\n\tv_mfma_f32_16x16x32_bf16 v[72:75], v[40:43], v[120:123], v[72:75]\n\tds_read_b64_tr_b16 v[28:29], v3 offset:57344\n\tds_read_b64_tr_b16 v[30:31], v3 offset:57856\n\t" \
    "v_mfma_f32_16x16x32_bf16 v[80:83], v[40:43], v[124:127], v[80:83]\n\tbuffer_load_dwordx4 a[92:95], v8, s[8:11], s68 offen offset:0 sc1\n\tv_mfma_f32_16x16x32_bf16 v[88:91], v[40:43], a[60:63], v[88:91]\n\ts_waitcnt lgkmcnt(8)\n\tv_mfma_f32_16x16x32_bf16 v[76:79], v[44:47], v[120:123], v[76:79]\n\tds_read_b64_tr_b16 v[32:33], v0 offset:61440\n\tds_read_b64_tr_b16 v[34:35], v0 offset:61952\n\tv_mfma_f32_16x16x32_bf16 v[84:87], v[44:47], v[124:127], v[84:87]\n\t" \
    "v_mfma_f32_16x16x32_bf16 v[92:95], v[44:47], a[60:63], v[92:95]\n\ts_waitcnt vmcnt(31) lgkmcnt(8)\n\tv_mfma_f32_16x16x32_bf16 a[0:3], v[16:19], v[128:131], a[0:3]\n\tds_read_b64_tr_b16 v[36:37], v1 offset:61440\n\tds_read_b64_tr_b16 v[38:39], v1 offset:61952\n\tv_mfma_f32_16x16x32_bf16 a[8:11], v[16:19], v[132:135], a[8:11]\n\tbuffer_load_dwordx4 a[96:99], v8, s[8:11], s68 offen offset:1024 sc1\n\tv_mfma_f32_16x16x32_bf16 a[16:19], v[16:19], v[136:139], a[16:19]\n\t" \
    "s_waitcnt lgkmcnt(8)\n\tv_mfma_f32_16x16x32_bf16 a[4:7], v[20:23], v[128:131], a[4:7]\n\tds_read_b64_tr_b16 v[40:41], v2 offset:61440\n\tds_read_b64_tr_b16 v[42:43], v2 offset:61952\n\tv_mfma_f32_16x16x32_bf16 a[12:15], v[20:23], v[132:135], a[12:15]\n\tbuffer_load_dwordx4 a[100:103], v8, s[8:11], s68 offen offset:2048 sc1\n\ts_add_u32 s68, s44, 700416\n\tv_mfma_f32_16x16x32_bf16 a[20:23], v[20:23], v[136:139], a[20:23]\n\t" \
    "s_waitcnt lgkmcnt(8)\n\tv_mfma_f32_16x16x32_bf16 v[48:51], v[24:27], v[128:131], v[48:51]\n\tds_read_b64_tr_b16 v[44:45], v3 offset:61440\n\tds_read_b64_tr_b16 v[46:47], v3 offset:61952\n\tv_mfma_f32_16x16x32_bf16 v[56:59], v[24:27], v[132:135], v[56:59]\n\tv_mfma_f32_16x16x32_bf16 v[64:67], v[24:27], v[136:139], v[64:67]\n\ts_waitcnt lgkmcnt(8)\n\tv_mfma_f32_16x16x32_bf16 v[52:55], v[28:31], v[128:131], v[52:55]\n\t" \
    "ds_read_b64_tr_b16 v[16:17], v0 offset:0\n\tds_read_b64_tr_b16 v[18:19], v0 offset:512\n\tv_mfma_f32_16x16x32_bf16 v[60:63], v[28:31], v[132:135], v[60:63]\n\tbuffer_load_dwordx4 v[120:123], v8, s[8:11], s68 offen offset:0 sc1\n\tv_mfma_f32_16x16x32_bf16 v[68:71], v[28:31], v[136:139], v[68:71]\n\ts_barrier\n\ts_waitcnt lgkmcnt(8)\n\tv_mfma_f32_16x16x32_bf16 a[24:27], v[32:35], v[128:131], a[24:27]\n\t" \
    "ds_read_b64_tr_b16 v[20:21], v1 offset:0\n\tds_read_b64_tr_b16 v[22:23], v1 offset:512\n\tv_mfma_f32_16x16x32_bf16 a[32:35], v[32:35], v[132:135], a[32:35]\n\tbuffer_load_dwordx4 v[124:127], v8, s[8:11], s68 offen offset:1024 sc1\n\tv_mfma_f32_16x16x32_bf16 a[40:43], v[32:35], v[136:139], a[40:43]\n\ts_waitcnt lgkmcnt(8)\n\tv_mfma_f32_16x16x32_bf16 a[28:31], v[36:39], v[128:131], a[28:31]\n\tds_read_b64_tr_b16 v[24:25], v2 offset:0\n\t" \
    "ds_read_b64_tr_b16 v[26:27], v2 offset:512\n\tv_mfma_f32_16x16x32_bf16 a[36:39], v[36:39], v[132:135], a[36:39]\n\tbuffer_load_dwordx4 a[60:63], v8, s[8:11], s68 offen offset:2048 sc1\n\ts_cmp_ge_u32 s43, 2\n\ts_cselect_b32 s69, s70, s43\n\ts_mul_i32 s69, s69, 294912\n\tv_mfma_f32_16x16x32_bf16 a[44:47], v[36:39], v[136:139], a[44:47]\n\ts_add_u32 s69, s69, 4128768\n\t" \
    "s_add_u32 m0, s72, 49152\n\ts_waitcnt lgkmcnt(8)\n\tv_mfma_f32_16x16x32_bf16 v[72:75], v[40:43], v[128:131], v[72:75]\n\tds_read_b64_tr_b16 v[28:29], v3 offset:0\n\tds_read_b64_tr_b16 v[30:31], v3 offset:512\n\tv_mfma_f32_16x16x32_bf16 v[80:83], v[40:43], v[132:135], v[80:83]\n\tbuffer_load_dwordx4 v10, s[12:15], s69 offen lds\n\ts_add_u32 m0, s72, 50176\n\t" \
    "v_mfma_f32_16x16x32_bf16 v[88:91], v[40:43], v[136:139], v[88:91]\n\ts_waitcnt lgkmcnt(8)\n\tv_mfma_f32_16x16x32_bf16 v[76:79], v[44:47], v[128:131], v[76:79]\n\tds_read_b64_tr_b16 v[32:33], v0 offset:4096\n\tds_read_b64_tr_b16 v[34:35], v0 offset:4608\n\tv_mfma_f32_16x16x32_bf16 v[84:87], v[44:47], v[132:135], v[84:87]\n\tbuffer_load_dwordx4 v11, s[12:15], s69 offen lds\n\ts_cmp_ge_u32 s43, 2\n\t" \
    "s_cselect_b32 s69, s70, s43\n\ts_mul_i32 s69, s69, 294912\n\tv_mfma_f32_16x16x32_bf16 v[92:95], v[44:47], v[136:139], v[92:95]\n\ts_add_u32 s69, s69, 4128896\n\ts_add_u32 m0, s72, 53248\n\ts_waitcnt vmcnt(35) lgkmcnt(8)\n\tv_mfma_f32_16x16x32_bf16 a[0:3], v[16:19], v[140:143], a[0:3]\n\tds_read_b64_tr_b16 v[36:37], v1 offset:4096\n\t" \
    "ds_read_b64_tr_b16 v[38:39], v1 offset:4608\n\tv_mfma_f32_16x16x32_bf16 a[8:11], v[16:19], v[144:147], a[8:11]\n\tv_mfma_f32_16x16x32_bf16 a[16:19], v[16:19], v[148:151], a[16:19]\n\ts_waitcnt lgkmcnt(8)\n\tv_mfma_f32_16x16x32_bf16 a[4:7], v[20:23], v[140:143], a[4:7]\n\tds_read_b64_tr_b16 v[40:41], v2 offset:4096\n\tds_read_b64_tr_b16 v[42:43], v2 offset:4608\n\tv_mfma_f32_16x16x32_bf16 a[12:15], v[20:23], v[144:147], a[12:15]\n\t" \
    "buffer_load_dwordx4 v10, s[12:15], s69 offen lds\n\ts_add_u32 m0, s72, 54272\n\tv_mfma_f32_16x16x32_bf16 a[20:23], v[20:23], v[148:151], a[20:23]\n\ts_waitcnt lgkmcnt(8)\n\tv_mfma_f32_16x16x32_bf16 v[48:51], v[24:27], v[140:143], v[48:51]\n\tds_read_b64_tr_b16 v[44:45], v3 offset:4096\n\tds_read_b64_tr_b16 v[46:47], v3 offset:4608\n\tv_mfma_f32_16x16x32_bf16 v[56:59], v[24:27], v[144:147], v[56:59]\n\t" \
    "buffer_load_dwordx4 v11, s[12:15], s69 offen lds\n\ts_cmp_ge_u32 s43, 1\n\ts_cselect_b32 s69, s70, s43\n\ts_mul_i32 s69, s69, 294912\n\tv_mfma_f32_16x16x32_bf16 v[64:67], v[24:27], v[148:151], v[64:67]\n\ts_add_u32 s69, s69, 4423680\n\ts_add_u32 m0, s72, 57344\n\ts_waitcnt lgkmcnt(8)\n\t" \
    "v_mfma_f32_16x16x32_bf16 v[52:55], v[28:31], v[140:143], v[52:55]\n\tds_read_b64_tr_b16 v[16:17], v0 offset:8192\n\tds_read_b64_tr_b16 v[18:19], v0 offset:8704\n\tv_mfma_f32_16x16x32_bf16 v[60:63], v[28:31], v[144:147], v[60:63]\n\tbuffer_load_dwordx4 v10, s[12:15], s69 offen lds\n\ts_add_u32 m0, s72, 58368\n\tv_mfma_f32_16x16x32_bf16 v[68:71], v[28:31], v[148:151], v[68:71]\n\ts_waitcnt lgkmcnt(8)\n\t" \
    "v_mfma_f32_16x16x32_bf16 a[24:27], v[32:35], v[140:143], a[24:27]\n\tds_read_b64_tr_b16 v[20:21], v1 offset:8192\n\tds_read_b64_tr_b16 v[22:23], v1 offset:8704\n\tv_mfma_f32_16x16x32_bf16 a[32:35], v[32:35], v[144:147], a[32:35]\n\tv_mfma_f32_16x16x32_bf16 a[40:43], v[32:35], v[148:151], a[40:43]\n\ts_waitcnt lgkmcnt(8)\n\tv_mfma_f32_16x16x32_bf16 a[28:31], v[36:39], v[140:143], a[28:31]\n\tds_read_b64_tr_b16 v[24:25], v2 offset:8192\n\t" \
    "ds_read_b64_tr_b16 v[26:27], v2 offset:8704\n\tv_mfma_f32_16x16x32_bf16 a[36:39], v[36:39], v[144:147], a[36:39]\n\tbuffer_load_dwordx4 v11, s[12:15], s69 offen lds\n\ts_add_u32 s68, s44, 737280\n\tv_mfma_f32_16x16x32_bf16 a[44:47], v[36:39], v[148:151], a[44:47]\n\ts_waitcnt lgkmcnt(8)\n\tv_mfma_f32_16x16x32_bf16 v[72:75], v[40:43], v[140:143], v[72:75]\n\tds_read_b64_tr_b16 v[28:29], v3 offset:8192\n\t" \
    "ds_read_b64_tr_b16 v[30:31], v3 offset:8704\n\tv_mfma_f32_16x16x32_bf16 v[80:83], v[40:43], v[144:147], v[80:83]\n\tbuffer_load_dwordx4 v[128:131], v8, s[8:11], s68 offen offset:0 sc1\n\tv_mfma_f32_16x16x32_bf16 v[88:91], v[40:43], v[148:151], v[88:91]\n\ts_waitcnt lgkmcnt(8)\n\tv_mfma_f32_16x16x32_bf16 v[76:79], v[44:47], v[140:143], v[76:79]\n\tds_read_b64_tr_b16 v[32:33], v0 offset:12288\n\tds_read_b64_tr_b16 v[34:35], v0 offset:12800\n\t" \
    "v_mfma_f32_16x16x32_bf16 v[84:87], v[44:47], v[144:147], v[84:87]\n\tv_mfma_f32_16x16x32_bf16 v[92:95], v[44:47], v[148:151], v[92:95]\n\ts_barrier\n\ts_waitcnt vmcnt(37) lgkmcnt(8)\n\tv_mfma_f32_16x16x32_bf16 a[0:3], v[16:19], v[152:155], a[0:3]\n\tds_read_b64_tr_b16 v[36:37], v1 offset:12288\n\tds_read_b64_tr_b16 v[38:39], v1 offset:12800\n\tv_mfma_f32_16x16x32_bf16 a[8:11], v[16:19], v[156:159], a[8:11]\n\t" \
    "buffer_load_dwordx4 v[132:135], v8, s[8:11], s68 offen offset:1024 sc1\n\tv_mfma_f32_16x16x32_bf16 a[16:19], v[16:19], v[160:163], a[16:19]\n\ts_waitcnt lgkmcnt(8)\n\tv_mfma_f32_16x16x32_bf16 a[4:7], v[20:23], v[152:155], a[4:7]\n\tds_read_b64_tr_b16 v[40:41], v2 offset:12288\n\tds_read_b64_tr_b16 v[42:43], v2 offset:12800\n\tv_mfma_f32_16x16x32_bf16 a[12:15], v[20:23], v[156:159], a[12:15]\n\tbuffer_load_dwordx4 v[136:139], v8, s[8:11], s68 offen offset:2048 sc1\n\t" \
    "s_cmp_ge_u32 s43, 1\n\ts_cselect_b32 s69, s70, s43\n\ts_mul_i32 s69, s69, 294912\n\tv_mfma_f32_16x16x32_bf16 a[20:23], v[20:23], v[160:163], a[20:23]\n\ts_add_u32 s69, s69, 4423808\n\ts_add_u32 m0, s72, 61440\n\ts_waitcnt lgkmcnt(8)\n\tv_mfma_f32_16x16x32_bf16 v[48:51], v[24:27], v[152:155], v[48:51]\n\t" \
    "ds_read_b64_tr_b16 v[44:45], v3 offset:12288\n\tds_read_b64_tr_b16 v[46:47], v3 offset:12800\n\tv_mfma_f32_16x16x32_bf16 v[56:59], v[24:27], v[156:159], v[56:59]\n\tbuffer_load_dwordx4 v10, s[12:15], s69 offen lds\n\ts_add_u32 m0, s72, 62464\n\tv_mfma_f32_16x16x32_bf16 v[64:67], v[24:27], v[160:163], v[64:67]\n\ts_waitcnt lgkmcnt(8)\n\tv_mfma_f32_16x16x32_bf16 v[52:55], v[28:31], v[152:155], v[52:55]\n\t" \
    "ds_read_b64_tr_b16 v[16:17], v0 offset:16384\n\tds_read_b64_tr_b16 v[18:19], v0 offset:16896\n\tv_mfma_f32_16x16x32_bf16 v[60:63], v[28:31], v[156:159], v[60:63]\n\tbuffer_load_dwordx4 v11, s[12:15], s69 offen lds\n\ts_mov_b32 s69, 4718592\n\ts_add_u32 m0, s72, 0\n\tv_mfma_f32_16x16x32_bf16 v[68:71], v[28:31], v[160:163], v[68:71]\n\ts_waitcnt lgkmcnt(8)\n\t" \
    "v_mfma_f32_16x16x32_bf16 a[24:27], v[32:35], v[152:155], a[24:27]\n\tds_read_b64_tr_b16 v[20:21], v1 offset:16384\n\tds_read_b64_tr_b16 v[22:23], v1 offset:16896\n\tv_mfma_f32_16x16x32_bf16 a[32:35], v[32:35], v[156:159], a[32:35]\n\tbuffer_load_dwordx4 v10, s[12:15], s69 offen lds\n\ts_add_u32 m0, s72, 1024\n\tv_mfma_f32_16x16x32_bf16 a[40:43], v[32:35], v[160:163], a[40:43]\n\ts_waitcnt lgkmcnt(8)\n\t" \
    "v_mfma_f32_16x16x32_bf16 a[28:31], v[36:39], v[152:155], a[28:31]\n\tds_read_b64_tr_b16 v[24:25], v2 offset:16384\n\tds_read_b64_tr_b16 v[26:27], v2 offset:16896\n\tv_mfma_f32_16x16x32_bf16 a[36:39], v[36:39], v[156:159], a[36:39]\n\tv_mfma_f32_16x16x32_bf16 a[44:47], v[36:39], v[160:163], a[44:47]\n\ts_waitcnt lgkmcnt(8)\n\tv_mfma_f32_16x16x32_bf16 v[72:75], v[40:43], v[152:155], v[72:75]\n\tds_read_b64_tr_b16 v[28:29], v3 offset:16384\n\t" \
    "ds_read_b64_tr_b16 v[30:31], v3 offset:16896\n\tv_mfma_f32_16x16x32_bf16 v[80:83], v[40:43], v[156:159], v[80:83]\n\tbuffer_load_dwordx4 v11, s[12:15], s69 offen lds\n\ts_mov_b32 s69, 5013504\n\ts_add_u32 m0, s72, 4096\n\tv_mfma_f32_16x16x32_bf16 v[88:91], v[40:43], v[160:163], v[88:91]\n\ts_waitcnt lgkmcnt(8)\n\tv_mfma_f32_16x16x32_bf16 v[76:79], v[44:47], v[152:155], v[76:79]\n\t" \
    "ds_read_b64_tr_b16 v[32:33], v0 offset:20480\n\tds_read_b64_tr_b16 v[34:35], v0 offset:20992\n\tv_mfma_f32_16x16x32_bf16 v[84:87], v[44:47], v[156:159], v[84:87]\n\tbuffer_load_dwordx4 v10, s[12:15], s69 offen lds\n\ts_add_u32 m0, s72, 5120\n\tv_mfma_f32_16x16x32_bf16 v[92:95], v[44:47], v[160:163], v[92:95]\n\ts_waitcnt vmcnt(41) lgkmcnt(8)\n\tv_mfma_f32_16x16x32_bf16 a[0:3], v[16:19], v[164:167], a[0:3]\n\t" \
    "ds_read_b64_tr_b16 v[36:37], v1 offset:20480\n\tds_read_b64_tr_b16 v[38:39], v1 offset:20992\n\tv_mfma_f32_16x16x32_bf16 a[8:11], v[16:19], v[168:171], a[8:11]\n\tv_mfma_f32_16x16x32_bf16 a[16:19], v[16:19], v[172:175], a[16:19]\n\ts_waitcnt lgkmcnt(8)\n\tv_mfma_f32_16x16x32_bf16 a[4:7], v[20:23], v[164:167], a[4:7]\n\tds_read_b64_tr_b16 v[40:41], v2 offset:20480\n\tds_read_b64_tr_b16 v[42:43], v2 offset:20992\n\t" \
    "v_mfma_f32_16x16x32_bf16 a[12:15], v[20:23], v[168:171], a[12:15]\n\tbuffer_load_dwordx4 v11, s[12:15], s69 offen lds\n\ts_add_u32 s68, s44, 774144\n\tv_mfma_f32_16x16x32_bf16 a[20:23], v[20:23], v[172:175], a[20:23]\n\ts_waitcnt lgkmcnt(8)\n\tv_mfma_f32_16x16x32_bf16 v[48:51], v[24:27], v[164:167], v[48:51]\n\tds_read_b64_tr_b16 v[44:45], v3 offset:20480\n\tds_read_b64_tr_b16 v[46:47], v3 offset:20992\n\t" \
    "v_mfma_f32_16x16x32_bf16 v[56:59], v[24:27], v[168:171], v[56:59]\n\tbuffer_load_dwordx4 v[140:143], v8, s[8:11], s68 offen offset:0 sc1\n\tv_mfma_f32_16x16x32_bf16 v[64:67], v[24:27], v[172:175], v[64:67]\n\ts_waitcnt lgkmcnt(8)\n\tv_mfma_f32_16x16x32_bf16 v[52:55], v[28:31], v[164:167], v[52:55]\n\tds_read_b64_tr_b16 v[16:17], v0 offset:24576\n\tds_read_b64_tr_b16 v[18:19], v0 offset:25088\n\tv_mfma_f32_16x16x32_bf16 v[60:63], v[28:31], v[168:171], v[60:63]\n\t" \
    "buffer_load_dwordx4 v[144:147], v8, s[8:11], s68 offen offset:1024 sc1\n\tv_mfma_f32_16x16x32_bf16 v[68:71], v[28:31], v[172:175], v[68:71]\n\ts_barrier\n\ts_waitcnt lgkmcnt(8)\n\tv_mfma_f32_16x16x32_bf16 a[24:27], v[32:35], v[164:167], a[24:27]\n\tds_read_b64_tr_b16 v[20:21], v1 offset:24576\n\tds_read_b64_tr_b16 v[22:23], v1 offset:25088\n\tv_mfma_f32_16x16x32_bf16 a[32:35], v[32:35], v[168:171], a[32:35]\n\t" \
    "buffer_load_dwordx4 v[148:151], v8, s[8:11], s68 offen offset:2048 sc1\n\ts_mov_b32 s69, 5308416\n\ts_add_u32 m0, s72, 8192\n\tv_mfma_f32_16x16x32_bf16 a[40:43], v[32:35], v[172:175], a[40:43]\n\ts_waitcnt lgkmcnt(8)\n\tv_mfma_f32_16x16x32_bf16 a[28:31], v[36:39], v[164:167], a[28:31]\n\tds_read_b64_tr_b16 v[24:25], v2 offset:24576\n\tds_read_b64_tr_b16 v[26:27], v2 offset:25088\n\t" \
    "v_mfma_f32_16x16x32_bf16 a[36:39], v[36:39], v[168:171], a[36:39]\n\tbuffer_load_dwordx4 v10, s[12:15], s69 offen lds\n\ts_add_u32 m0, s72, 9216\n\tv_mfma_f32_16x16x32_bf16 a[44:47], v[36:39], v[172:175], a[44:47]\n\ts_waitcnt lgkmcnt(8)\n\tv_mfma_f32_16x16x32_bf16 v[72:75], v[40:43], v[164:167], v[72:75]\n\tds_read_b64_tr_b16 v[28:29], v3 offset:24576\n\tds_read_b64_tr_b16 v[30:31], v3 offset:25088\n\t" \
    "v_mfma_f32_16x16x32_bf16 v[80:83], v[40:43], v[168:171], v[80:83]\n\tv_mfma_f32_16x16x32_bf16 v[88:91], v[40:43], v[172:175], v[88:91]\n\ts_waitcnt lgkmcnt(8)\n\tv_mfma_f32_16x16x32_bf16 v[76:79], v[44:47], v[164:167], v[76:79]\n\tds_read_b64_tr_b16 v[32:33], v0 offset:28672\n\tds_read_b64_tr_b16 v[34:35], v0 offset:29184\n\tv_mfma_f32_16x16x32_bf16 v[84:87], v[44:47], v[168:171], v[84:87]\n\tbuffer_load_dwordx4 v11, s[12:15], s69 offen lds\n\t" \
    "s_mov_b32 s69, 5603328\n\ts_add_u32 m0, s72, 12288\n\tv_mfma_f32_16x16x32_bf16 v[92:95], v[44:47], v[172:175], v[92:95]\n\ts_waitcnt vmcnt(44) lgkmcnt(8)\n\tv_mfma_f32_16x16x32_bf16 a[0:3], v[16:19], v[176:179], a[0:3]\n\tds_read_b64_tr_b16 v[36:37], v1 offset:28672\n\tds_read_b64_tr_b16 v[38:39], v1 offset:29184\n\tv_mfma_f32_16x16x32_bf16 a[8:11], v[16:19], v[180:183], a[8:11]\n\t" \
    "buffer_load_dwordx4 v10, s[12:15], s69 offen lds\n\ts_add_u32 m0, s72, 13312\n\tv_mfma_f32_16x16x32_bf16 a[16:19], v[16:19], v[184:187], a[16:19]\n\ts_waitcnt lgkmcnt(8)\n\tv_mfma_f32_16x16x32_bf16 a[4:7], v[20:23], v[176:179], a[4:7]\n\tds_read_b64_tr_b16 v[40:41], v2 offset:28672\n\tds_read_b64_tr_b16 v[42:43], v2 offset:29184\n\tv_mfma_f32_16x16x32_bf16 a[12:15], v[20:23], v[180:183], a[12:15]\n\t" \
    "v_mfma_f32_16x16x32_bf16 a[20:23], v[20:23], v[184:187], a[20:23]\n\ts_waitcnt lgkmcnt(8)\n\tv_mfma_f32_16x16x32_bf16 v[48:51], v[24:27], v[176:179], v[48:51]\n\tds_read_b64_tr_b16 v[44:45], v3 offset:28672\n\tds_read_b64_tr_b16 v[46:47], v3 offset:29184\n\tv_mfma_f32_16x16x32_bf16 v[56:59], v[24:27], v[180:183], v[56:59]\n\tbuffer_load_dwordx4 v11, s[12:15], s69 offen lds\n\ts_mov_b32 s69, 5898240\n\t" \
    "s_add_u32 m0, s72, 16384\n\tv_mfma_f32_16x16x32_bf16 v[64:67], v[24:27], v[184:187], v[64:67]\n\ts_waitcnt lgkmcnt(8)\n\tv_mfma_f32_16x16x32_bf16 v[52:55], v[28:31], v[176:179], v[52:55]\n\tds_read_b64_tr_b16 v[16:17], v0 offset:32768\n\tds_read_b64_tr_b16 v[18:19], v0 offset:33280\n\tv_mfma_f32_16x16x32_bf16 v[60:63], v[28:31], v[180:183], v[60:63]\n\tbuffer_load_dwordx4 v10, s[12:15], s69 offen lds\n\t" \
    "s_add_u32 m0, s72, 17408\n\tv_mfma_f32_16x16x32_bf16 v[68:71], v[28:31], v[184:187], v[68:71]\n\ts_waitcnt lgkmcnt(8)\n\tv_mfma_f32_16x16x32_bf16 a[24:27], v[32:35], v[176:179], a[24:27]\n\tds_read_b64_tr_b16 v[20:21], v1 offset:32768\n\tds_read_b64_tr_b16 v[22:23], v1 offset:33280\n\tv_mfma_f32_16x16x32_bf16 a[32:35], v[32:35], v[180:183], a[32:35]\n\tv_mfma_f32_16x16x32_bf16 a[40:43], v[32:35], v[184:187], a[40:43]\n\t" \
    "s_waitcnt lgkmcnt(8)\n\tv_mfma_f32_16x16x32_bf16 a[28:31], v[36:39], v[176:179], a[28:31]\n\tds_read_b64_tr_b16 v[24:25], v2 offset:32768\n\tds_read_b64_tr_b16 v[26:27], v2 offset:33280\n\tv_mfma_f32_16x16x32_bf16 a[36:39], v[36:39], v[180:183], a[36:39]\n\tbuffer_load_dwordx4 v11, s[12:15], s69 offen lds\n\ts_add_u32 s68, s44, 811008\n\tv_mfma_f32_16x16x32_bf16 a[44:47], v[36:39], v[184:187], a[44:47]\n\t" \
    "s_waitcnt lgkmcnt(8)\n\tv_mfma_f32_16x16x32_bf16 v[72:75], v[40:43], v[176:179], v[72:75]\n\tds_read_b64_tr_b16 v[28:29], v3 offset:32768\n\tds_read_b64_tr_b16 v[30:31], v3 offset:33280\n\tv_mfma_f32_16x16x32_bf16 v[80:83], v[40:43], v[180:183], v[80:83]\n\tbuffer_load_dwordx4 v[152:155], v8, s[8:11], s68 offen offset:0 sc1\n\tv_mfma_f32_16x16x32_bf16 v[88:91], v[40:43], v[184:187], v[88:91]\n\ts_waitcnt lgkmcnt(8)\n\t" \
    "v_mfma_f32_16x16x32_bf16 v[76:79], v[44:47], v[176:179], v[76:79]\n\tds_read_b64_tr_b16 v[32:33], v0 offset:36864\n\tds_read_b64_tr_b16 v[34:35], v0 offset:37376\n\tv_mfma_f32_16x16x32_bf16 v[84:87], v[44:47], v[180:183], v[84:87]\n\tbuffer_load_dwordx4 v[156:159], v8, s[8:11], s68 offen offset:1024 sc1\n\tv_mfma_f32_16x16x32_bf16 v[92:95], v[44:47], v[184:187], v[92:95]\n\ts_waitcnt vmcnt(24)\n\ts_barrier\n\t" \
    "s_waitcnt lgkmcnt(8)\n\tv_mfma_f32_16x16x32_bf16 a[0:3], v[16:19], v[188:191], a[0:3]\n\tds_read_b64_tr_b16 v[36:37], v1 offset:36864\n\tds_read_b64_tr_b16 v[38:39], v1 offset:37376\n\tv_mfma_f32_16x16x32_bf16 a[8:11], v[16:19], v[192:195], a[8:11]\n\tbuffer_load_dwordx4 v[160:163], v8, s[8:11], s68 offen offset:2048 sc1\n\ts_mov_b32 s69, 6193152\n\ts_add_u32 m0, s72, 20480\n\t" \
    "v_mfma_f32_16x16x32_bf16 a[16:19], v[16:19], v[196:199], a[16:19]\n\ts_waitcnt lgkmcnt(8)\n\tv_mfma_f32_16x16x32_bf16 a[4:7], v[20:23], v[188:191], a[4:7]\n\tds_read_b64_tr_b16 v[40:41], v2 offset:36864\n\tds_read_b64_tr_b16 v[42:43], v2 offset:37376\n\tv_mfma_f32_16x16x32_bf16 a[12:15], v[20:23], v[192:195], a[12:15]\n\tbuffer_load_dwordx4 v10, s[12:15], s69 offen lds\n\ts_add_u32 m0, s72, 21504\n\t" \
    "v_mfma_f32_16x16x32_bf16 a[20:23], v[20:23], v[196:199], a[20:23]\n\ts_waitcnt lgkmcnt(8)\n\tv_mfma_f32_16x16x32_bf16 v[48:51], v[24:27], v[188:191], v[48:51]\n\tds_read_b64_tr_b16 v[44:45], v3 offset:36864\n\tds_read_b64_tr_b16 v[46:47], v3 offset:37376\n\tv_mfma_f32_16x16x32_bf16 v[56:59], v[24:27], v[192:195], v[56:59]\n\tv_mfma_f32_16x16x32_bf16 v[64:67], v[24:27], v[196:199], v[64:67]\n\ts_waitcnt lgkmcnt(8)\n\t" \
    "v_mfma_f32_16x16x32_bf16 v[52:55], v[28:31], v[188:191], v[52:55]\n\tds_read_b64_tr_b16 v[16:17], v0 offset:40960\n\tds_read_b64_tr_b16 v[18:19], v0 offset:41472\n\tv_mfma_f32_16x16x32_bf16 v[60:63], v[28:31], v[192:195], v[60:63]\n\tbuffer_load_dwordx4 v11, s[12:15], s69 offen lds\n\ts_mov_b32 s69, 6488064\n\ts_add_u32 m0, s72, 24576\n\tv_mfma_f32_16x16x32_bf16 v[68:71], v[28:31], v[196:199], v[68:71]\n\t" \
    "s_waitcnt lgkmcnt(8)\n\tv_mfma_f32_16x16x32_bf16 a[24:27], v[32:35], v[188:191], a[24:27]\n\tds_read_b64_tr_b16 v[20:21], v1 offset:40960\n\tds_read_b64_tr_b16 v[22:23], v1 offset:41472\n\tv_mfma_f32_16x16x32_bf16 a[32:35], v[32:35], v[192:195], a[32:35]\n\tbuffer_load_dwordx4 v10, s[12:15], s69 offen lds\n\ts_add_u32 m0, s72, 25600\n\tv_mfma_f32_16x16x32_bf16 a[40:43], v[32:35], v[196:199], a[40:43]\n\t" \
    "s_waitcnt lgkmcnt(8)\n\tv_mfma_f32_16x16x32_bf16 a[28:31], v[36:39], v[188:191], a[28:31]\n\tds_read_b64_tr_b16 v[24:25], v2 offset:40960\n\tds_read_b64_tr_b16 v[26:27], v2 offset:41472\n\tv_mfma_f32_16x16x32_bf16 a[36:39], v[36:39], v[192:195], a[36:39]\n\tv_mfma_f32_16x16x32_bf16 a[44:47], v[36:39], v[196:199], a[44:47]\n\ts_waitcnt lgkmcnt(8)\n\tv_mfma_f32_16x16x32_bf16 v[72:75], v[40:43], v[188:191], v[72:75]\n\t" \
    "ds_read_b64_tr_b16 v[28:29], v3 offset:40960\n\tds_read_b64_tr_b16 v[30:31], v3 offset:41472\n\tv_mfma_f32_16x16x32_bf16 v[80:83], v[40:43], v[192:195], v[80:83]\n\tbuffer_load_dwordx4 v11, s[12:15], s69 offen lds\n\ts_mov_b32 s69, 6782976\n\ts_add_u32 m0, s72, 28672\n\tv_mfma_f32_16x16x32_bf16 v[88:91], v[40:43], v[196:199], v[88:91]\n\ts_waitcnt lgkmcnt(8)\n\t" \
    "v_mfma_f32_16x16x32_bf16 v[76:79], v[44:47], v[188:191], v[76:79]\n\tds_read_b64_tr_b16 v[32:33], v0 offset:45056\n\tds_read_b64_tr_b16 v[34:35], v0 offset:45568\n\tv_mfma_f32_16x16x32_bf16 v[84:87], v[44:47], v[192:195], v[84:87]\n\tbuffer_load_dwordx4 v10, s[12:15], s69 offen lds\n\ts_add_u32 m0, s72, 29696\n\tv_mfma_f32_16x16x32_bf16 v[92:95], v[44:47], v[196:199], v[92:95]\n\ts_waitcnt lgkmcnt(8)\n\t" \
    "v_mfma_f32_16x16x32_bf16 a[0:3], v[16:19], v[200:203], a[0:3]\n\tds_read_b64_tr_b16 v[36:37], v1 offset:45056\n\tds_read_b64_tr_b16 v[38:39], v1 offset:45568\n\tv_mfma_f32_16x16x32_bf16 a[8:11], v[16:19], v[204:207], a[8:11]\n\tbuffer_load_dwordx4 v11, s[12:15], s69 offen lds\n\ts_add_u32 s68, s44, 847872\n\tv_mfma_f32_16x16x32_bf16 a[16:19], v[16:19], v[208:211], a[16:19]\n\ts_waitcnt lgkmcnt(8)\n\t" \
    "v_mfma_f32_16x16x32_bf16 a[4:7], v[20:23], v[200:203], a[4:7]\n\tds_read_b64_tr_b16 v[40:41], v2 offset:45056\n\tds_read_b64_tr_b16 v[42:43], v2 offset:45568\n\tv_mfma_f32_16x16x32_bf16 a[12:15], v[20:23], v[204:207], a[12:15]\n\tv_mfma_f32_16x16x32_bf16 a[20:23], v[20:23], v[208:211], a[20:23]\n\ts_waitcnt lgkmcnt(8)\n\tv_mfma_f32_16x16x32_bf16 v[48:51], v[24:27], v[200:203], v[48:51]\n\tds_read_b64_tr_b16 v[44:45], v3 offset:45056\n\t" \
    "ds_read_b64_tr_b16 v[46:47], v3 offset:45568\n\tv_mfma_f32_16x16x32_bf16 v[56:59], v[24:27], v[204:207], v[56:59]\n\tbuffer_load_dwordx4 v[164:167], v8, s[8:11], s68 offen offset:0 sc1\n\tv_mfma_f32_16x16x32_bf16 v[64:67], v[24:27], v[208:211], v[64:67]\n\ts_waitcnt lgkmcnt(8)\n\tv_mfma_f32_16x16x32_bf16 v[52:55], v[28:31], v[200:203], v[52:55]\n\tds_read_b64_tr_b16 v[16:17], v0 offset:49152\n\tds_read_b64_tr_b16 v[18:19], v0 offset:49664\n\t" \
    "v_mfma_f32_16x16x32_bf16 v[60:63], v[28:31], v[204:207], v[60:63]\n\tbuffer_load_dwordx4 v[168:171], v8, s[8:11], s68 offen offset:1024 sc1\n\tv_mfma_f32_16x16x32_bf16 v[68:71], v[28:31], v[208:211], v[68:71]\n\ts_waitcnt vmcnt(24)\n\ts_barrier\n\ts_waitcnt lgkmcnt(8)\n\tv_mfma_f32_16x16x32_bf16 a[24:27], v[32:35], v[200:203], a[24:27]\n\tds_read_b64_tr_b16 v[20:21], v1 offset:49152\n\t" \
    "ds_read_b64_tr_b16 v[22:23], v1 offset:49664\n\tv_mfma_f32_16x16x32_bf16 a[32:35], v[32:35], v[204:207], a[32:35]\n\tbuffer_load_dwordx4 v[172:175], v8, s[8:11], s68 offen offset:2048 sc1\n\ts_mov_b32 s69, 7077888\n\ts_add_u32 m0, s72, 32768\n\tv_mfma_f32_16x16x32_bf16 a[40:43], v[32:35], v[208:211], a[40:43]\n\ts_waitcnt lgkmcnt(8)\n\tv_mfma_f32_16x16x32_bf16 a[28:31], v[36:39], v[200:203], a[28:31]\n\t" \
    "ds_read_b64_tr_b16 v[24:25], v2 offset:49152\n\tds_read_b64_tr_b16 v[26:27], v2 offset:49664\n\tv_mfma_f32_16x16x32_bf16 a[36:39], v[36:39], v[204:207], a[36:39]\n\tbuffer_load_dwordx4 v10, s[12:15], s69 offen lds\n\ts_add_u32 m0, s72, 33792\n\tv_mfma_f32_16x16x32_bf16 a[44:47], v[36:39], v[208:211], a[44:47]\n\ts_waitcnt lgkmcnt(8)\n\tv_mfma_f32_16x16x32_bf16 v[72:75], v[40:43], v[200:203], v[72:75]\n\t" \
    "ds_read_b64_tr_b16 v[28:29], v3 offset:49152\n\tds_read_b64_tr_b16 v[30:31], v3 offset:49664\n\tv_mfma_f32_16x16x32_bf16 v[80:83], v[40:43], v[204:207], v[80:83]\n\tv_mfma_f32_16x16x32_bf16 v[88:91], v[40:43], v[208:211], v[88:91]\n\ts_waitcnt lgkmcnt(8)\n\tv_mfma_f32_16x16x32_bf16 v[76:79], v[44:47], v[200:203], v[76:79]\n\tds_read_b64_tr_b16 v[32:33], v0 offset:53248\n\tds_read_b64_tr_b16 v[34:35], v0 offset:53760\n\t" \
    "v_mfma_f32_16x16x32_bf16 v[84:87], v[44:47], v[204:207], v[84:87]\n\tbuffer_load_dwordx4 v11, s[12:15], s69 offen lds\n\ts_mov_b32 s69, 7372800\n\ts_add_u32 m0, s72, 36864\n\tv_mfma_f32_16x16x32_bf16 v[92:95], v[44:47], v[208:211], v[92:95]\n\ts_waitcnt lgkmcnt(8)\n\tv_mfma_f32_16x16x32_bf16 a[0:3], v[16:19], v[212:215], a[0:3]\n\tds_read_b64_tr_b16 v[36:37], v1 offset:53248\n\t" \
    "ds_read_b64_tr_b16 v[38:39], v1 offset:53760\n\tv_mfma_f32_16x16x32_bf16 a[8:11], v[16:19], v[216:219], a[8:11]\n\tbuffer_load_dwordx4 v10, s[12:15], s69 offen lds\n\ts_add_u32 m0, s72, 37888\n\tv_mfma_f32_16x16x32_bf16 a[16:19], v[16:19], a[64:67], a[16:19]\n\ts_waitcnt lgkmcnt(8)\n\tv_mfma_f32_16x16x32_bf16 a[4:7], v[20:23], v[212:215], a[4:7]\n\tds_read_b64_tr_b16 v[40:41], v2 offset:53248\n\t" \
    "ds_read_b64_tr_b16 v[42:43], v2 offset:53760\n\tv_mfma_f32_16x16x32_bf16 a[12:15], v[20:23], v[216:219], a[12:15]\n\tv_mfma_f32_16x16x32_bf16 a[20:23], v[20:23], a[64:67], a[20:23]\n\ts_waitcnt lgkmcnt(8)\n\tv_mfma_f32_16x16x32_bf16 v[48:51], v[24:27], v[212:215], v[48:51]\n\tds_read_b64_tr_b16 v[44:45], v3 offset:53248\n\tds_read_b64_tr_b16 v[46:47], v3 offset:53760\n\tv_mfma_f32_16x16x32_bf16 v[56:59], v[24:27], v[216:219], v[56:59]\n\t" \
    "buffer_load_dwordx4 v11, s[12:15], s69 offen lds\n\ts_mov_b32 s69, 7667712\n\ts_add_u32 m0, s72, 40960\n\tv_mfma_f32_16x16x32_bf16 v[64:67], v[24:27], a[64:67], v[64:67]\n\ts_waitcnt lgkmcnt(8)\n\tv_mfma_f32_16x16x32_bf16 v[52:55], v[28:31], v[212:215], v[52:55]\n\tds_read_b64_tr_b16 v[16:17], v0 offset:57344\n\tds_read_b64_tr_b16 v[18:19], v0 offset:57856\n\t" \
    "v_mfma_f32_16x16x32_bf16 v[60:63], v[28:31], v[216:219], v[60:63]\n\tbuffer_load_dwordx4 v10, s[12:15], s69 offen lds\n\ts_add_u32 m0, s72, 41984\n\tv_mfma_f32_16x16x32_bf16 v[68:71], v[28:31], a[64:67], v[68:71]\n\ts_waitcnt lgkmcnt(8)\n\tv_mfma_f32_16x16x32_bf16 a[24:27], v[32:35], v[212:215], a[24:27]\n\tds_read_b64_tr_b16 v[20:21], v1 offset:57344\n\tds_read_b64_tr_b16 v[22:23], v1 offset:57856\n\t" \
    "v_mfma_f32_16x16x32_bf16 a[32:35], v[32:35], v[216:219], a[32:35]\n\tbuffer_load_dwordx4 v11, s[12:15], s69 offen lds\n\ts_add_u32 s68, s44, 884736\n\tv_mfma_f32_16x16x32_bf16 a[40:43], v[32:35], a[64:67], a[40:43]\n\ts_waitcnt lgkmcnt(8)\n\tv_mfma_f32_16x16x32_bf16 a[28:31], v[36:39], v[212:215], a[28:31]\n\tds_read_b64_tr_b16 v[24:25], v2 offset:57344\n\tds_read_b64_tr_b16 v[26:27], v2 offset:57856\n\t" \
    "v_mfma_f32_16x16x32_bf16 a[36:39], v[36:39], v[216:219], a[36:39]\n\tv_mfma_f32_16x16x32_bf16 a[44:47], v[36:39], a[64:67], a[44:47]\n\ts_waitcnt lgkmcnt(8)\n\tv_mfma_f32_16x16x32_bf16 v[72:75], v[40:43], v[212:215], v[72:75]\n\tds_read_b64_tr_b16 v[28:29], v3 offset:57344\n\tds_read_b64_tr_b16 v[30:31], v3 offset:57856\n\tv_mfma_f32_16x16x32_bf16 v[80:83], v[40:43], v[216:219], v[80:83]\n\tbuffer_load_dwordx4 v[176:179], v8, s[8:11], s68 offen offset:0 sc1\n\t" \
    "v_mfma_f32_16x16x32_bf16 v[88:91], v[40:43], a[64:67], v[88:91]\n\ts_waitcnt lgkmcnt(8)\n\tv_mfma_f32_16x16x32_bf16 v[76:79], v[44:47], v[212:215], v[76:79]\n\tds_read_b64_tr_b16 v[32:33], v0 offset:61440\n\tds_read_b64_tr_b16 v[34:35], v0 offset:61952\n\tv_mfma_f32_16x16x32_bf16 v[84:87], v[44:47], v[216:219], v[84:87]\n\tbuffer_load_dwordx4 v[180:183], v8, s[8:11], s68 offen offset:1024 sc1\n\tv_mfma_f32_16x16x32_bf16 v[92:95], v[44:47], a[64:67], v[92:95]\n\t" \
    "s_waitcnt vmcnt(24)\n\ts_barrier\n\ts_waitcnt lgkmcnt(8)\n\tv_mfma_f32_16x16x32_bf16 a[0:3], v[16:19], a[68:71], a[0:3]\n\tds_read_b64_tr_b16 v[36:37], v1 offset:61440\n\tds_read_b64_tr_b16 v[38:39], v1 offset:61952\n\tv_mfma_f32_16x16x32_bf16 a[8:11], v[16:19], a[72:75], a[8:11]\n\tbuffer_load_dwordx4 v[184:187], v8, s[8:11], s68 offen offset:2048 sc1\n\t" \
    "s_mov_b32 s69, 7962624\n\ts_add_u32 m0, s72, 45056\n\tv_mfma_f32_16x16x32_bf16 a[16:19], v[16:19], a[76:79], a[16:19]\n\ts_waitcnt lgkmcnt(8)\n\tv_mfma_f32_16x16x32_bf16 a[4:7], v[20:23], a[68:71], a[4:7]\n\tds_read_b64_tr_b16 v[40:41], v2 offset:61440\n\tds_read_b64_tr_b16 v[42:43], v2 offset:61952\n\tv_mfma_f32_16x16x32_bf16 a[12:15], v[20:23], a[72:75], a[12:15]\n\t" \
    "buffer_load_dwordx4 v10, s[12:15], s69 offen lds\n\ts_add_u32 m0, s72, 46080\n\tv_mfma_f32_16x16x32_bf16 a[20:23], v[20:23], a[76:79], a[20:23]\n\ts_waitcnt lgkmcnt(8)\n\tv_mfma_f32_16x16x32_bf16 v[48:51], v[24:27], a[68:71], v[48:51]\n\tds_read_b64_tr_b16 v[44:45], v3 offset:61440\n\tds_read_b64_tr_b16 v[46:47], v3 offset:61952\n\tv_mfma_f32_16x16x32_bf16 v[56:59], v[24:27], a[72:75], v[56:59]\n\t" \
    "v_mfma_f32_16x16x32_bf16 v[64:67], v[24:27], a[76:79], v[64:67]\n\ts_waitcnt lgkmcnt(8)\n\tv_mfma_f32_16x16x32_bf16 v[52:55], v[28:31], a[68:71], v[52:55]\n\tds_read_b64_tr_b16 v[16:17], v0 offset:0\n\tds_read_b64_tr_b16 v[18:19], v0 offset:512\n\tv_mfma_f32_16x16x32_bf16 v[60:63], v[28:31], a[72:75], v[60:63]\n\tbuffer_load_dwordx4 v11, s[12:15], s69 offen lds\n\ts_add_u32 s68, s44, 921600\n\t" \
    "v_mfma_f32_16x16x32_bf16 v[68:71], v[28:31], a[76:79], v[68:71]\n\ts_waitcnt lgkmcnt(8)\n\tv_mfma_f32_16x16x32_bf16 a[24:27], v[32:35], a[68:71], a[24:27]\n\tds_read_b64_tr_b16 v[20:21], v1 offset:0\n\tds_read_b64_tr_b16 v[22:23], v1 offset:512\n\tv_mfma_f32_16x16x32_bf16 a[32:35], v[32:35], a[72:75], a[32:35]\n\tbuffer_load_dwordx4 v[188:191], v8, s[8:11], s68 offen offset:0 sc1\n\tv_mfma_f32_16x16x32_bf16 a[40:43], v[32:35], a[76:79], a[40:43]\n\t" \
    "s_waitcnt lgkmcnt(8)\n\tv_mfma_f32_16x16x32_bf16 a[28:31], v[36:39], a[68:71], a[28:31]\n\tds_read_b64_tr_b16 v[24:25], v2 offset:0\n\tds_read_b64_tr_b16 v[26:27], v2 offset:512\n\tv_mfma_f32_16x16x32_bf16 a[36:39], v[36:39], a[72:75], a[36:39]\n\tbuffer_load_dwordx4 v[192:195], v8, s[8:11], s68 offen offset:1024 sc1\n\tv_mfma_f32_16x16x32_bf16 a[44:47], v[36:39], a[76:79], a[44:47]\n\ts_waitcnt lgkmcnt(8)\n\t" \
    "v_mfma_f32_16x16x32_bf16 v[72:75], v[40:43], a[68:71], v[72:75]\n\tds_read_b64_tr_b16 v[28:29], v3 offset:0\n\tds_read_b64_tr_b16 v[30:31], v3 offset:512\n\tv_mfma_f32_16x16x32_bf16 v[80:83], v[40:43], a[72:75], v[80:83]\n\tv_mfma_f32_16x16x32_bf16 v[88:91], v[40:43], a[76:79], v[88:91]\n\ts_waitcnt lgkmcnt(8)\n\tv_mfma_f32_16x16x32_bf16 v[76:79], v[44:47], a[68:71], v[76:79]\n\tds_read_b64_tr_b16 v[32:33], v0 offset:4096\n\t" \
    "ds_read_b64_tr_b16 v[34:35], v0 offset:4608\n\tv_mfma_f32_16x16x32_bf16 v[84:87], v[44:47], a[72:75], v[84:87]\n\tbuffer_load_dwordx4 v[196:199], v8, s[8:11], s68 offen offset:2048 sc1\n\ts_mov_b32 s69, 8257536\n\ts_add_u32 m0, s72, 49152\n\tv_mfma_f32_16x16x32_bf16 v[92:95], v[44:47], a[76:79], v[92:95]\n\ts_waitcnt lgkmcnt(8)\n\tv_mfma_f32_16x16x32_bf16 a[0:3], v[16:19], a[80:83], a[0:3]\n\t" \
    "ds_read_b64_tr_b16 v[36:37], v1 offset:4096\n\tds_read_b64_tr_b16 v[38:39], v1 offset:4608\n\tv_mfma_f32_16x16x32_bf16 a[8:11], v[16:19], a[84:87], a[8:11]\n\tbuffer_load_dwordx4 v10, s[12:15], s69 offen lds\n\ts_add_u32 m0, s72, 50176\n\tv_mfma_f32_16x16x32_bf16 a[16:19], v[16:19], a[88:91], a[16:19]\n\ts_waitcnt lgkmcnt(8)\n\tv_mfma_f32_16x16x32_bf16 a[4:7], v[20:23], a[80:83], a[4:7]\n\t" \
    "ds_read_b64_tr_b16 v[40:41], v2 offset:4096\n\tds_read_b64_tr_b16 v[42:43], v2 offset:4608\n\tv_mfma_f32_16x16x32_bf16 a[12:15], v[20:23], a[84:87], a[12:15]\n\tv_mfma_f32_16x16x32_bf16 a[20:23], v[20:23], a[88:91], a[20:23]\n\ts_waitcnt lgkmcnt(8)\n\tv_mfma_f32_16x16x32_bf16 v[48:51], v[24:27], a[80:83], v[48:51]\n\tds_read_b64_tr_b16 v[44:45], v3 offset:4096\n\tds_read_b64_tr_b16 v[46:47], v3 offset:4608\n\t" \
    "v_mfma_f32_16x16x32_bf16 v[56:59], v[24:27], a[84:87], v[56:59]\n\tbuffer_load_dwordx4 v11, s[12:15], s69 offen lds\n\ts_mov_b32 s69, 8552448\n\ts_add_u32 m0, s72, 53248\n\tv_mfma_f32_16x16x32_bf16 v[64:67], v[24:27], a[88:91], v[64:67]\n\ts_waitcnt lgkmcnt(8)\n\tv_mfma_f32_16x16x32_bf16 v[52:55], v[28:31], a[80:83], v[52:55]\n\tds_read_b64_tr_b16 v[16:17], v0 offset:8192\n\t" \
    "ds_read_b64_tr_b16 v[18:19], v0 offset:8704\n\tv_mfma_f32_16x16x32_bf16 v[60:63], v[28:31], a[84:87], v[60:63]\n\tbuffer_load_dwordx4 v10, s[12:15], s69 offen lds\n\ts_add_u32 m0, s72, 54272\n\tv_mfma_f32_16x16x32_bf16 v[68:71], v[28:31], a[88:91], v[68:71]\n\ts_waitcnt vmcnt(24)\n\ts_barrier\n\ts_waitcnt lgkmcnt(8)\n\t" \
    "v_mfma_f32_16x16x32_bf16 a[0:3], v[32:35], a[48:51], a[0:3]\n\tds_read_b64_tr_b16 v[20:21], v1 offset:8192\n\tds_read_b64_tr_b16 v[22:23], v1 offset:8704\n\tv_mfma_f32_16x16x32_bf16 a[8:11], v[32:35], a[52:55], a[8:11]\n\tbuffer_load_dwordx4 v11, s[12:15], s69 offen lds\n\ts_mov_b32 s69, 8847360\n\ts_add_u32 m0, s72, 57344\n\tv_mfma_f32_16x16x32_bf16 a[16:19], v[32:35], a[56:59], a[16:19]\n\t" \
    "s_waitcnt lgkmcnt(8)\n\tv_mfma_f32_16x16x32_bf16 a[4:7], v[36:39], a[48:51], a[4:7]\n\tds_read_b64_tr_b16 v[24:25], v2 offset:8192\n\tds_read_b64_tr_b16 v[26:27], v2 offset:8704\n\tv_mfma_f32_16x16x32_bf16 a[12:15], v[36:39], a[52:55], a[12:15]\n\tbuffer_load_dwordx4 v10, s[12:15], s69 offen lds\n\ts_add_u32 m0, s72, 58368\n\tv_mfma_f32_16x16x32_bf16 a[20:23], v[36:39], a[56:59], a[20:23]\n\t" \
    "s_waitcnt lgkmcnt(8)\n\tv_mfma_f32_16x16x32_bf16 v[48:51], v[40:43], a[48:51], v[48:51]\n\tds_read_b64_tr_b16 v[28:29], v3 offset:8192\n\tds_read_b64_tr_b16 v[30:31], v3 offset:8704\n\tv_mfma_f32_16x16x32_bf16 v[56:59], v[40:43], a[52:55], v[56:59]\n\tv_mfma_f32_16x16x32_bf16 v[64:67], v[40:43], a[56:59], v[64:67]\n\ts_waitcnt lgkmcnt(8)\n\tv_mfma_f32_16x16x32_bf16 v[52:55], v[44:47], a[48:51], v[52:55]\n\t" \
    "ds_read_b64_tr_b16 v[32:33], v0 offset:12288\n\tds_read_b64_tr_b16 v[34:35], v0 offset:12800\n\tv_mfma_f32_16x16x32_bf16 v[60:63], v[44:47], a[52:55], v[60:63]\n\tbuffer_load_dwordx4 v11, s[12:15], s69 offen lds\n\ts_mov_b32 s69, 9142272\n\ts_add_u32 m0, s72, 61440\n\tv_mfma_f32_16x16x32_bf16 v[68:71], v[44:47], a[56:59], v[68:71]\n\ts_waitcnt lgkmcnt(8)\n\t" \
    "v_mfma_f32_16x16x32_bf16 a[0:3], v[16:19], a[92:95], a[0:3]\n\tds_read_b64_tr_b16 v[36:37], v1 offset:12288\n\tds_read_b64_tr_b16 v[38:39], v1 offset:12800\n\tv_mfma_f32_16x16x32_bf16 a[8:11], v[16:19], a[96:99], a[8:11]\n\tbuffer_load_dwordx4 v10, s[12:15], s69 offen lds\n\ts_add_u32 m0, s72, 62464\n\tv_mfma_f32_16x16x32_bf16 a[16:19], v[16:19], a[100:103], a[16:19]\n\ts_waitcnt lgkmcnt(8)\n\t" \
    "v_mfma_f32_16x16x32_bf16 a[4:7], v[20:23], a[92:95], a[4:7]\n\tds_read_b64_tr_b16 v[40:41], v2 offset:12288\n\tds_read_b64_tr_b16 v[42:43], v2 offset:12800\n\tv_mfma_f32_16x16x32_bf16 a[12:15], v[20:23], a[96:99], a[12:15]\n\tbuffer_load_dwordx4 v11, s[12:15], s69 offen lds\n\ts_add_u32 s68, s44, 958464\n\tv_mfma_f32_16x16x32_bf16 a[20:23], v[20:23], a[100:103], a[20:23]\n\ts_waitcnt lgkmcnt(8)\n\t" \
    "v_mfma_f32_16x16x32_bf16 v[48:51], v[24:27], a[92:95], v[48:51]\n\tds_read_b64_tr_b16 v[44:45], v3 offset:12288\n\tds_read_b64_tr_b16 v[46:47], v3 offset:12800\n\tv_mfma_f32_16x16x32_bf16 v[56:59], v[24:27], a[96:99], v[56:59]\n\tv_mfma_f32_16x16x32_bf16 v[64:67], v[24:27], a[100:103], v[64:67]\n\ts_waitcnt lgkmcnt(8)\n\tv_mfma_f32_16x16x32_bf16 v[52:55], v[28:31], a[92:95], v[52:55]\n\tds_read_b64_tr_b16 v[16:17], v0 offset:16384\n\t" \
    "ds_read_b64_tr_b16 v[18:19], v0 offset:16896\n\tv_mfma_f32_16x16x32_bf16 v[60:63], v[28:31], a[96:99], v[60:63]\n\tbuffer_load_dwordx4 v[200:203], v8, s[8:11], s68 offen offset:0 sc1\n\tv_mfma_f32_16x16x32_bf16 v[68:71], v[28:31], a[100:103], v[68:71]\n\ts_mul_i32 s43, s6, 0\n\ts_sub_u32 s70, s43, 0\n\ts_waitcnt lgkmcnt(8)\n\tv_mfma_f32_16x16x32_bf16 a[0:3], v[32:35], v[120:123], a[0:3]\n\t" \
    "ds_read_b64_tr_b16 v[20:21], v1 offset:16384\n\tds_read_b64_tr_b16 v[22:23], v1 offset:16896\n\tv_mfma_f32_16x16x32_bf16 a[8:11], v[32:35], v[124:127], a[8:11]\n\tbuffer_load_dwordx4 v[204:207], v8, s[8:11], s68 offen offset:1024 sc1\n\tv_mfma_f32_16x16x32_bf16 a[16:19], v[32:35], a[60:63], a[16:19]\n\ts_waitcnt lgkmcnt(8)\n\tv_mfma_f32_16x16x32_bf16 a[4:7], v[36:39], v[120:123], a[4:7]\n\tds_read_b64_tr_b16 v[24:25], v2 offset:16384\n\t" \
    "ds_read_b64_tr_b16 v[26:27], v2 offset:16896\n\tv_mfma_f32_16x16x32_bf16 a[12:15], v[36:39], v[124:127], a[12:15]\n\tv_mfma_f32_16x16x32_bf16 a[20:23], v[36:39], a[60:63], a[20:23]\n\ts_waitcnt lgkmcnt(8)\n\tv_mfma_f32_16x16x32_bf16 v[48:51], v[40:43], v[120:123], v[48:51]\n\tds_read_b64_tr_b16 v[28:29], v3 offset:16384\n\tds_read_b64_tr_b16 v[30:31], v3 offset:16896\n\tv_mfma_f32_16x16x32_bf16 v[56:59], v[40:43], v[124:127], v[56:59]\n\t" \
    "buffer_load_dwordx4 v[208:211], v8, s[8:11], s68 offen offset:2048 sc1\n\ts_add_u32 s68, s44, 995328\n\tv_mfma_f32_16x16x32_bf16 v[64:67], v[40:43], a[60:63], v[64:67]\n\ts_waitcnt lgkmcnt(8)\n\tv_mfma_f32_16x16x32_bf16 v[52:55], v[44:47], v[120:123], v[52:55]\n\tds_read_b64_tr_b16 v[32:33], v0 offset:20480\n\tds_read_b64_tr_b16 v[34:35], v0 offset:20992\n\tv_mfma_f32_16x16x32_bf16 v[60:63], v[44:47], v[124:127], v[60:63]\n\t" \
    "buffer_load_dwordx4 v[212:215], v8, s[8:11], s68 offen offset:0 sc1\n\tv_mfma_f32_16x16x32_bf16 v[68:71], v[44:47], a[60:63], v[68:71]\n\ts_waitcnt vmcnt(24)\n\ts_barrier\n\ts_waitcnt lgkmcnt(8)\n\tv_mfma_f32_16x16x32_bf16 a[0:3], v[16:19], v[128:131], a[0:3]\n\tds_read_b64_tr_b16 v[36:37], v1 offset:20480\n\tds_read_b64_tr_b16 v[38:39], v1 offset:20992\n\t" \
    "v_mfma_f32_16x16x32_bf16 a[8:11], v[16:19], v[132:135], a[8:11]\n\tbuffer_load_dwordx4 v[216:219], v8, s[8:11], s68 offen offset:1024 sc1\n\tv_mfma_f32_16x16x32_bf16 a[16:19], v[16:19], v[136:139], a[16:19]\n\ts_waitcnt lgkmcnt(8)\n\tv_mfma_f32_16x16x32_bf16 a[4:7], v[20:23], v[128:131], a[4:7]\n\tds_read_b64_tr_b16 v[40:41], v2 offset:20480\n\tds_read_b64_tr_b16 v[42:43], v2 offset:20992\n\tv_mfma_f32_16x16x32_bf16 a[12:15], v[20:23], v[132:135], a[12:15]\n\t" \
    "buffer_load_dwordx4 a[64:67], v8, s[8:11], s68 offen offset:2048 sc1\n\ts_add_u32 s68, s44, 1032192\n\tv_mfma_f32_16x16x32_bf16 a[20:23], v[20:23], v[136:139], a[20:23]\n\ts_waitcnt lgkmcnt(8)\n\tv_mfma_f32_16x16x32_bf16 v[48:51], v[24:27], v[128:131], v[48:51]\n\tds_read_b64_tr_b16 v[44:45], v3 offset:20480\n\tds_read_b64_tr_b16 v[46:47], v3 offset:20992\n\tv_mfma_f32_16x16x32_bf16 v[56:59], v[24:27], v[132:135], v[56:59]\n\t" \
    "buffer_load_dwordx4 a[68:71], v8, s[8:11], s68 offen offset:0 sc1\n\tv_mfma_f32_16x16x32_bf16 v[64:67], v[24:27], v[136:139], v[64:67]\n\ts_waitcnt lgkmcnt(8)\n\tv_mfma_f32_16x16x32_bf16 v[52:55], v[28:31], v[128:131], v[52:55]\n\tds_read_b64_tr_b16 v[16:17], v0 offset:24576\n\tds_read_b64_tr_b16 v[18:19], v0 offset:25088\n\tv_mfma_f32_16x16x32_bf16 v[60:63], v[28:31], v[132:135], v[60:63]\n\tv_mfma_f32_16x16x32_bf16 v[68:71], v[28:31], v[136:139], v[68:71]\n\t" \
    "s_waitcnt lgkmcnt(8)\n\tv_mfma_f32_16x16x32_bf16 a[0:3], v[32:35], v[140:143], a[0:3]\n\tds_read_b64_tr_b16 v[20:21], v1 offset:24576\n\tds_read_b64_tr_b16 v[22:23], v1 offset:25088\n\tv_mfma_f32_16x16x32_bf16 a[8:11], v[32:35], v[144:147], a[8:11]\n\tbuffer_load_dwordx4 a[72:75], v8, s[8:11], s68 offen offset:1024 sc1\n\tv_mfma_f32_16x16x32_bf16 a[16:19], v[32:35], v[148:151], a[16:19]\n\ts_waitcnt lgkmcnt(8)\n\t" \
    "v_mfma_f32_16x16x32_bf16 a[4:7], v[36:39], v[140:143], a[4:7]\n\tds_read_b64_tr_b16 v[24:25], v2 offset:24576\n\tds_read_b64_tr_b16 v[26:27], v2 offset:25088\n\tv_mfma_f32_16x16x32_bf16 a[12:15], v[36:39], v[144:147], a[12:15]\n\tbuffer_load_dwordx4 a[76:79], v8, s[8:11], s68 offen offset:2048 sc1\n\ts_add_u32 s68, s44, 1069056\n\tv_mfma_f32_16x16x32_bf16 a[20:23], v[36:39], v[148:151], a[20:23]\n\ts_waitcnt lgkmcnt(8)\n\t" \
    "v_mfma_f32_16x16x32_bf16 v[48:51], v[40:43], v[140:143], v[48:51]\n\tds_read_b64_tr_b16 v[28:29], v3 offset:24576\n\tds_read_b64_tr_b16 v[30:31], v3 offset:25088\n\tv_mfma_f32_16x16x32_bf16 v[56:59], v[40:43], v[144:147], v[56:59]\n\tv_mfma_f32_16x16x32_bf16 v[64:67], v[40:43], v[148:151], v[64:67]\n\ts_waitcnt lgkmcnt(8)\n\tv_mfma_f32_16x16x32_bf16 v[52:55], v[44:47], v[140:143], v[52:55]\n\tds_read_b64_tr_b16 v[32:33], v0 offset:28672\n\t" \
    "ds_read_b64_tr_b16 v[34:35], v0 offset:29184\n\tv_mfma_f32_16x16x32_bf16 v[60:63], v[44:47], v[144:147], v[60:63]\n\tbuffer_load_dwordx4 a[104:107], v8, s[8:11], s68 offen offset:0 sc1\n\tv_mfma_f32_16x16x32_bf16 v[68:71], v[44:47], v[148:151], v[68:71]\n\ts_waitcnt lgkmcnt(8)\n\tv_mfma_f32_16x16x32_bf16 a[0:3], v[16:19], v[152:155], a[0:3]\n\tds_read_b64_tr_b16 v[36:37], v1 offset:28672\n\tds_read_b64_tr_b16 v[38:39], v1 offset:29184\n\t" \
    "v_mfma_f32_16x16x32_bf16 a[8:11], v[16:19], v[156:159], a[8:11]\n\tbuffer_load_dwordx4 a[108:111], v8, s[8:11], s68 offen offset:1024 sc1\n\tv_mfma_f32_16x16x32_bf16 a[16:19], v[16:19], v[160:163], a[16:19]\n\ts_waitcnt lgkmcnt(8)\n\tv_mfma_f32_16x16x32_bf16 a[4:7], v[20:23], v[152:155], a[4:7]\n\tds_read_b64_tr_b16 v[40:41], v2 offset:28672\n\tds_read_b64_tr_b16 v[42:43], v2 offset:29184\n\tv_mfma_f32_16x16x32_bf16 a[12:15], v[20:23], v[156:159], a[12:15]\n\t" \
    "v_mfma_f32_16x16x32_bf16 a[20:23], v[20:23], v[160:163], a[20:23]\n\ts_waitcnt lgkmcnt(8)\n\tv_mfma_f32_16x16x32_bf16 v[48:51], v[24:27], v[152:155], v[48:51]\n\tds_read_b64_tr_b16 v[44:45], v3 offset:28672\n\tds_read_b64_tr_b16 v[46:47], v3 offset:29184\n\tv_mfma_f32_16x16x32_bf16 v[56:59], v[24:27], v[156:159], v[56:59]\n\tbuffer_load_dwordx4 a[112:115], v8, s[8:11], s68 offen offset:2048 sc1\n\ts_mov_b32 s69, 4718720\n\t" \
    "s_add_u32 m0, s72, 0\n\tv_mfma_f32_16x16x32_bf16 v[64:67], v[24:27], v[160:163], v[64:67]\n\ts_waitcnt lgkmcnt(8)\n\tv_mfma_f32_16x16x32_bf16 v[52:55], v[28:31], v[152:155], v[52:55]\n\tds_read_b64_tr_b16 v[16:17], v0 offset:32768\n\tds_read_b64_tr_b16 v[18:19], v0 offset:33280\n\tv_mfma_f32_16x16x32_bf16 v[60:63], v[28:31], v[156:159], v[60:63]\n\tbuffer_load_dwordx4 v10, s[12:15], s69 offen lds\n\t" \
    "s_add_u32 m0, s72, 1024\n\tv_mfma_f32_16x16x32_bf16 v[68:71], v[28:31], v[160:163], v[68:71]\n\ts_waitcnt vmcnt(24)\n\ts_barrier\n\ts_waitcnt lgkmcnt(8)\n\tv_mfma_f32_16x16x32_bf16 a[0:3], v[32:35], v[164:167], a[0:3]\n\tds_read_b64_tr_b16 v[20:21], v1 offset:32768\n\tds_read_b64_tr_b16 v[22:23], v1 offset:33280\n\t" \
    "v_mfma_f32_16x16x32_bf16 a[8:11], v[32:35], v[168:171], a[8:11]\n\tbuffer_load_dwordx4 v11, s[12:15], s69 offen lds\n\ts_add_u32 s68, s44, 1105920\n\tv_mfma_f32_16x16x32_bf16 a[16:19], v[32:35], v[172:175], a[16:19]\n\ts_waitcnt lgkmcnt(8)\n\tv_mfma_f32_16x16x32_bf16 a[4:7], v[36:39], v[164:167], a[4:7]\n\tds_read_b64_tr_b16 v[24:25], v2 offset:32768\n\tds_read_b64_tr_b16 v[26:27], v2 offset:33280\n\t" \
    "v_mfma_f32_16x16x32_bf16 a[12:15], v[36:39], v[168:171], a[12:15]\n\tbuffer_load_dwordx4 a[116:119], v8, s[8:11], s68 offen offset:0 sc1\n\tv_mfma_f32_16x16x32_bf16 a[20:23], v[36:39], v[172:175], a[20:23]\n\ts_waitcnt lgkmcnt(8)\n\tv_mfma_f32_16x16x32_bf16 v[48:51], v[40:43], v[164:167], v[48:51]\n\tds_read_b64_tr_b16 v[28:29], v3 offset:32768\n\tds_read_b64_tr_b16 v[30:31], v3 offset:33280\n\tv_mfma_f32_16x16x32_bf16 v[56:59], v[40:43], v[168:171], v[56:59]\n\t" \
    "buffer_load_dwordx4 a[120:123], v8, s[8:11], s68 offen offset:1024 sc1\n\tv_mfma_f32_16x16x32_bf16 v[64:67], v[40:43], v[172:175], v[64:67]\n\ts_waitcnt lgkmcnt(8)\n\tv_mfma_f32_16x16x32_bf16 v[52:55], v[44:47], v[164:167], v[52:55]\n\tds_read_b64_tr_b16 v[32:33], v0 offset:36864\n\tds_read_b64_tr_b16 v[34:35], v0 offset:37376\n\tv_mfma_f32_16x16x32_bf16 v[60:63], v[44:47], v[168:171], v[60:63]\n\tv_mfma_f32_16x16x32_bf16 v[68:71], v[44:47], v[172:175], v[68:71]\n\t" \
    "s_waitcnt lgkmcnt(8)\n\tv_mfma_f32_16x16x32_bf16 a[0:3], v[16:19], v[176:179], a[0:3]\n\tds_read_b64_tr_b16 v[36:37], v1 offset:36864\n\tds_read_b64_tr_b16 v[38:39], v1 offset:37376\n\tv_mfma_f32_16x16x32_bf16 a[8:11], v[16:19], v[180:183], a[8:11]\n\tbuffer_load_dwordx4 a[124:127], v8, s[8:11], s68 offen offset:2048 sc1\n\ts_add_u32 s68, s44, 1142784\n\tv_mfma_f32_16x16x32_bf16 a[16:19], v[16:19], v[184:187], a[16:19]\n\t" \
    "s_waitcnt lgkmcnt(8)\n\tv_mfma_f32_16x16x32_bf16 a[4:7], v[20:23], v[176:179], a[4:7]\n\tds_read_b64_tr_b16 v[40:41], v2 offset:36864\n\tds_read_b64_tr_b16 v[42:43], v2 offset:37376\n\tv_mfma_f32_16x16x32_bf16 a[12:15], v[20:23], v[180:183], a[12:15]\n\tbuffer_load_dwordx4 a[128:131], v8, s[8:11], s68 offen offset:0 sc1\n\tv_mfma_f32_16x16x32_bf16 a[20:23], v[20:23], v[184:187], a[20:23]\n\ts_waitcnt lgkmcnt(8)\n\t" \
    "v_mfma_f32_16x16x32_bf16 v[48:51], v[24:27], v[176:179], v[48:51]\n\tds_read_b64_tr_b16 v[44:45], v3 offset:36864\n\tds_read_b64_tr_b16 v[46:47], v3 offset:37376\n\tv_mfma_f32_16x16x32_bf16 v[56:59], v[24:27], v[180:183], v[56:59]\n\tv_mfma_f32_16x16x32_bf16 v[64:67], v[24:27], v[184:187], v[64:67]\n\ts_waitcnt lgkmcnt(8)\n\tv_mfma_f32_16x16x32_bf16 v[52:55], v[28:31], v[176:179], v[52:55]\n\tds_read_b64_tr_b16 v[16:17], v0 offset:40960\n\t" \
    "ds_read_b64_tr_b16 v[18:19], v0 offset:41472\n\tv_mfma_f32_16x16x32_bf16 v[60:63], v[28:31], v[180:183], v[60:63]\n\tbuffer_load_dwordx4 a[132:135], v8, s[8:11], s68 offen offset:1024 sc1\n\tv_mfma_f32_16x16x32_bf16 v[68:71], v[28:31], v[184:187], v[68:71]\n\ts_waitcnt vmcnt(27) lgkmcnt(8)\n\tv_mfma_f32_16x16x32_bf16 a[0:3], v[32:35], v[188:191], a[0:3]\n\tds_read_b64_tr_b16 v[20:21], v1 offset:40960\n\tds_read_b64_tr_b16 v[22:23], v1 offset:41472\n\t" \
    "v_mfma_f32_16x16x32_bf16 a[8:11], v[32:35], v[192:195], a[8:11]\n\tbuffer_load_dwordx4 a[136:139], v8, s[8:11], s68 offen offset:2048 sc1\n\ts_mov_b32 s69, 5013632\n\ts_add_u32 m0, s72, 4096\n\tv_mfma_f32_16x16x32_bf16 a[16:19], v[32:35], v[196:199], a[16:19]\n\ts_waitcnt lgkmcnt(8)\n\tv_mfma_f32_16x16x32_bf16 a[4:7], v[36:39], v[188:191], a[4:7]\n\tds_read_b64_tr_b16 v[24:25], v2 offset:40960\n\t" \
    "ds_read_b64_tr_b16 v[26:27], v2 offset:41472\n\tv_mfma_f32_16x16x32_bf16 a[12:15], v[36:39], v[192:195], a[12:15]\n\tbuffer_load_dwordx4 v10, s[12:15], s69 offen lds\n\ts_add_u32 m0, s72, 5120\n\tv_mfma_f32_16x16x32_bf16 a[20:23], v[36:39], v[196:199], a[20:23]\n\ts_waitcnt lgkmcnt(8)\n\tv_mfma_f32_16x16x32_bf16 v[48:51], v[40:43], v[188:191], v[48:51]\n\tds_read_b64_tr_b16 v[28:29], v3 offset:40960\n\t" \
    "ds_read_b64_tr_b16 v[30:31], v3 offset:41472\n\tv_mfma_f32_16x16x32_bf16 v[56:59], v[40:43], v[192:195], v[56:59]\n\tv_mfma_f32_16x16x32_bf16 v[64:67], v[40:43], v[196:199], v[64:67]\n\ts_waitcnt lgkmcnt(8)\n\tv_mfma_f32_16x16x32_bf16 v[52:55], v[44:47], v[188:191], v[52:55]\n\tds_read_b64_tr_b16 v[32:33], v0 offset:45056\n\tds_read_b64_tr_b16 v[34:35], v0 offset:45568\n\tv_mfma_f32_16x16x32_bf16 v[60:63], v[44:47], v[192:195], v[60:63]\n\t" \
    "buffer_load_dwordx4 v11, s[12:15], s69 offen lds\n\ts_mov_b32 s69, 5308544\n\ts_add_u32 m0, s72, 8192\n\tv_mfma_f32_16x16x32_bf16 v[68:71], v[44:47], v[196:199], v[68:71]\n\ts_waitcnt vmcnt(22)\n\ts_barrier\n\ts_waitcnt vmcnt(19) lgkmcnt(8)\n\tv_mfma_f32_16x16x32_bf16 a[0:3], v[16:19], v[200:203], a[0:3]\n\t" \
    "ds_read_b64_tr_b16 v[36:37], v1 offset:45056\n\tds_read_b64_tr_b16 v[38:39], v1 offset:45568\n\tv_mfma_f32_16x16x32_bf16 a[8:11], v[16:19], v[204:207], a[8:11]\n\tbuffer_load_dwordx4 v10, s[12:15], s69 offen lds\n\ts_add_u32 m0, s72, 9216\n\tv_mfma_f32_16x16x32_bf16 a[16:19], v[16:19], v[208:211], a[16:19]\n\ts_waitcnt lgkmcnt(8)\n\tv_mfma_f32_16x16x32_bf16 a[4:7], v[20:23], v[200:203], a[4:7]\n\t" \
    "ds_read_b64_tr_b16 v[40:41], v2 offset:45056\n\tds_read_b64_tr_b16 v[42:43], v2 offset:45568\n\tv_mfma_f32_16x16x32_bf16 a[12:15], v[20:23], v[204:207], a[12:15]\n\tbuffer_load_dwordx4 v11, s[12:15], s69 offen lds\n\ts_mov_b32 s69, 5603456\n\ts_add_u32 m0, s72, 12288\n\tv_mfma_f32_16x16x32_bf16 a[20:23], v[20:23], v[208:211], a[20:23]\n\ts_waitcnt lgkmcnt(8)\n\t" \
    "v_mfma_f32_16x16x32_bf16 v[48:51], v[24:27], v[200:203], v[48:51]\n\tds_read_b64_tr_b16 v[44:45], v3 offset:45056\n\tds_read_b64_tr_b16 v[46:47], v3 offset:45568\n\tv_mfma_f32_16x16x32_bf16 v[56:59], v[24:27], v[204:207], v[56:59]\n\tbuffer_load_dwordx4 v10, s[12:15], s69 offen lds\n\ts_add_u32 m0, s72, 13312\n\tv_mfma_f32_16x16x32_bf16 v[64:67], v[24:27], v[208:211], v[64:67]\n\ts_waitcnt lgkmcnt(8)\n\t" \
    "v_mfma_f32_16x16x32_bf16 v[52:55], v[28:31], v[200:203], v[52:55]\n\tds_read_b64_tr_b16 v[16:17], v0 offset:49152\n\tds_read_b64_tr_b16 v[18:19], v0 offset:49664\n\tv_mfma_f32_16x16x32_bf16 v[60:63], v[28:31], v[204:207], v[60:63]\n\tv_mfma_f32_16x16x32_bf16 v[68:71], v[28:31], v[208:211], v[68:71]\n\ts_waitcnt vmcnt(19) lgkmcnt(8)\n\tv_mfma_f32_16x16x32_bf16 a[0:3], v[32:35], v[212:215], a[0:3]\n\tds_read_b64_tr_b16 v[20:21], v1 offset:49152\n\t" \
    "ds_read_b64_tr_b16 v[22:23], v1 offset:49664\n\tv_mfma_f32_16x16x32_bf16 a[8:11], v[32:35], v[216:219], a[8:11]\n\tbuffer_load_dwordx4 v11, s[12:15], s69 offen lds\n\ts_mov_b32 s69, 5898368\n\ts_add_u32 m0, s72, 16384\n\tv_mfma_f32_16x16x32_bf16 a[16:19], v[32:35], a[64:67], a[16:19]\n\ts_waitcnt lgkmcnt(8)\n\tv_mfma_f32_16x16x32_bf16 a[4:7], v[36:39], v[212:215], a[4:7]\n\t" \
    "ds_read_b64_tr_b16 v[24:25], v0 offset:53248\n\tds_read_b64_tr_b16 v[26:27], v0 offset:53760\n\tv_mfma_f32_16x16x32_bf16 a[12:15], v[36:39], v[216:219], a[12:15]\n\tbuffer_load_dwordx4 v10, s[12:15], s69 offen lds\n\ts_add_u32 m0, s72, 17408\n\tv_mfma_f32_16x16x32_bf16 a[20:23], v[36:39], a[64:67], a[20:23]\n\ts_waitcnt lgkmcnt(8)\n\tv_mfma_f32_16x16x32_bf16 v[48:51], v[40:43], v[212:215], v[48:51]\n\t" \
    "ds_read_b64_tr_b16 v[28:29], v1 offset:53248\n\tds_read_b64_tr_b16 v[30:31], v1 offset:53760\n\tv_mfma_f32_16x16x32_bf16 v[56:59], v[40:43], v[216:219], v[56:59]\n\tv_mfma_f32_16x16x32_bf16 v[64:67], v[40:43], a[64:67], v[64:67]\n\ts_waitcnt lgkmcnt(8)\n\tv_mfma_f32_16x16x32_bf16 v[52:55], v[44:47], v[212:215], v[52:55]\n\tds_read_b64_tr_b16 v[32:33], v0 offset:57344\n\tds_read_b64_tr_b16 v[34:35], v0 offset:57856\n\t" \
    "v_mfma_f32_16x16x32_bf16 v[60:63], v[44:47], v[216:219], v[60:63]\n\tbuffer_load_dwordx4 v11, s[12:15], s69 offen lds\n\ts_mov_b32 s69, 6193280\n\ts_add_u32 m0, s72, 20480\n\tv_mfma_f32_16x16x32_bf16 v[68:71], v[44:47], a[64:67], v[68:71]\n\ts_waitcnt vmcnt(19) lgkmcnt(8)\n\tv_mfma_f32_16x16x32_bf16 a[0:3], v[16:19], a[68:71], a[0:3]\n\tds_read_b64_tr_b16 v[36:37], v1 offset:57344\n\t" \
    "ds_read_b64_tr_b16 v[38:39], v1 offset:57856\n\tv_mfma_f32_16x16x32_bf16 a[8:11], v[16:19], a[72:75], a[8:11]\n\tbuffer_load_dwordx4 v10, s[12:15], s69 offen lds\n\ts_add_u32 m0, s72, 21504\n\tv_mfma_f32_16x16x32_bf16 a[16:19], v[16:19], a[76:79], a[16:19]\n\ts_waitcnt lgkmcnt(8)\n\tv_mfma_f32_16x16x32_bf16 a[4:7], v[20:23], a[68:71], a[4:7]\n\tds_read_b64_tr_b16 v[40:41], v0 offset:61440\n\t" \
    "ds_read_b64_tr_b16 v[42:43], v0 offset:61952\n\tv_mfma_f32_16x16x32_bf16 a[12:15], v[20:23], a[72:75], a[12:15]\n\tbuffer_load_dwordx4 v11, s[12:15], s69 offen lds\n\ts_mov_b32 s69, 6488192\n\ts_add_u32 m0, s72, 24576\n\tv_mfma_f32_16x16x32_bf16 a[20:23], v[20:23], a[76:79], a[20:23]\n\ts_waitcnt vmcnt(18) lgkmcnt(8)\n\tv_mfma_f32_16x16x32_bf16 a[0:3], v[24:27], a[104:107], a[0:3]\n\t" \
    "ds_read_b64_tr_b16 v[44:45], v1 offset:61440\n\tds_read_b64_tr_b16 v[46:47], v1 offset:61952\n\tv_mfma_f32_16x16x32_bf16 a[8:11], v[24:27], a[108:111], a[8:11]\n\tv_mfma_f32_16x16x32_bf16 a[16:19], v[24:27], a[112:115], a[16:19]\n\ts_waitcnt lgkmcnt(8)\n\tv_mfma_f32_16x16x32_bf16 a[4:7], v[28:31], a[104:107], a[4:7]\n\tds_read_b64_tr_b16 v[16:17], v2 offset:49152\n\tds_read_b64_tr_b16 v[18:19], v2 offset:49664\n\t" \
    "v_mfma_f32_16x16x32_bf16 a[12:15], v[28:31], a[108:111], a[12:15]\n\tbuffer_load_dwordx4 v10, s[12:15], s69 offen lds\n\ts_add_u32 m0, s72, 25600\n\tv_mfma_f32_16x16x32_bf16 a[20:23], v[28:31], a[112:115], a[20:23]\n\ts_waitcnt vmcnt(17)\n\ts_barrier\n\ts_waitcnt vmcnt(14) lgkmcnt(8)\n\tv_mfma_f32_16x16x32_bf16 a[0:3], v[32:35], a[116:119], a[0:3]\n\t" \
    "ds_read_b64_tr_b16 v[20:21], v3 offset:49152\n\tds_read_b64_tr_b16 v[22:23], v3 offset:49664\n\tv_mfma_f32_16x16x32_bf16 a[8:11], v[32:35], a[120:123], a[8:11]\n\tv_mfma_f32_16x16x32_bf16 a[16:19], v[32:35], a[124:127], a[16:19]\n\ts_waitcnt lgkmcnt(8)\n\tv_mfma_f32_16x16x32_bf16 a[4:7], v[36:39], a[116:119], a[4:7]\n\tds_read_b64_tr_b16 v[24:25], v2 offset:53248\n\tds_read_b64_tr_b16 v[26:27], v2 offset:53760\n\t" \
    "v_mfma_f32_16x16x32_bf16 a[12:15], v[36:39], a[120:123], a[12:15]\n\tv_mfma_f32_16x16x32_bf16 a[20:23], v[36:39], a[124:127], a[20:23]\n\ts_waitcnt vmcnt(11) lgkmcnt(8)\n\tv_mfma_f32_16x16x32_bf16 a[0:3], v[40:43], a[128:131], a[0:3]\n\tds_read_b64_tr_b16 v[28:29], v3 offset:53248\n\tds_read_b64_tr_b16 v[30:31], v3 offset:53760\n\tv_mfma_f32_16x16x32_bf16 a[8:11], v[40:43], a[132:135], a[8:11]\n\tv_mfma_f32_16x16x32_bf16 a[16:19], v[40:43], a[136:139], a[16:19]\n\t" \
    "s_waitcnt lgkmcnt(8)\n\tv_mfma_f32_16x16x32_bf16 a[4:7], v[44:47], a[128:131], a[4:7]\n\tds_read_b64_tr_b16 v[32:33], v2 offset:57344\n\tds_read_b64_tr_b16 v[34:35], v2 offset:57856\n\tv_mfma_f32_16x16x32_bf16 a[12:15], v[44:47], a[132:135], a[12:15]\n\tv_mfma_f32_16x16x32_bf16 a[20:23], v[44:47], a[136:139], a[20:23]\n\ts_waitcnt lgkmcnt(8)\n\tv_mfma_f32_16x16x32_bf16 v[48:51], v[16:19], a[68:71], v[48:51]\n\t" \
    "ds_read_b64_tr_b16 v[36:37], v3 offset:57344\n\tds_read_b64_tr_b16 v[38:39], v3 offset:57856\n\tv_mfma_f32_16x16x32_bf16 v[56:59], v[16:19], a[72:75], v[56:59]\n\tv_mfma_f32_16x16x32_bf16 v[64:67], v[16:19], a[76:79], v[64:67]\n\tds_write_b128 v237, a[0:3] offset:0\n\ts_waitcnt lgkmcnt(9)\n\tv_mfma_f32_16x16x32_bf16 v[52:55], v[20:23], a[68:71], v[52:55]\n\tds_read_b64_tr_b16 v[40:41], v2 offset:61440\n\t" \
    "ds_read_b64_tr_b16 v[42:43], v2 offset:61952\n\tv_mfma_f32_16x16x32_bf16 v[60:63], v[20:23], a[72:75], v[60:63]\n\tv_mfma_f32_16x16x32_bf16 v[68:71], v[20:23], a[76:79], v[68:71]\n\tds_write_b128 v237, a[8:11] offset:2048\n\ts_waitcnt lgkmcnt(10)\n\tv_mfma_f32_16x16x32_bf16 v[48:51], v[24:27], a[104:107], v[48:51]\n\tds_read_b64_tr_b16 v[44:45], v3 offset:61440\n\tds_read_b64_tr_b16 v[46:47], v3 offset:61952\n\t" \
    "v_mfma_f32_16x16x32_bf16 v[56:59], v[24:27], a[108:111], v[56:59]\n\tv_mfma_f32_16x16x32_bf16 v[64:67], v[24:27], a[112:115], v[64:67]\n\tds_write_b128 v237, a[16:19] offset:4096\n\ts_waitcnt lgkmcnt(11)\n\tv_mfma_f32_16x16x32_bf16 v[52:55], v[28:31], a[104:107], v[52:55]\n\tds_read_b64_tr_b16 v[16:17], v0 offset:0\n\tds_read_b64_tr_b16 v[18:19], v0 offset:512\n\tv_mfma_f32_16x16x32_bf16 v[60:63], v[28:31], a[108:111], v[60:63]\n\t" \
    "v_mfma_f32_16x16x32_bf16 v[68:71], v[28:31], a[112:115], v[68:71]\n\tds_write_b128 v237, a[4:7] offset:1024\n\tds_write_b128 v237, a[12:15] offset:3072\n\tds_write_b128 v237, a[20:23] offset:5120\n\ts_waitcnt vmcnt(3) lgkmcnt(0)\n\ts_barrier\n\tv_mfma_f32_16x16x32_bf16 v[48:51], v[32:35], a[116:119], v[48:51]\n\tds_read_b64_tr_b16 v[20:21], v1 offset:0\n\t" \
    "ds_read_b64_tr_b16 v[22:23], v1 offset:512\n\tv_mfma_f32_16x16x32_bf16 v[56:59], v[32:35], a[120:123], v[56:59]\n\tbuffer_load_dwordx4 v11, s[12:15], s69 offen lds\n\tds_read_b128 v[96:99], v238 offset:0\n\ts_mov_b32 s69, 6783104\n\tv_mfma_f32_16x16x32_bf16 v[64:67], v[32:35], a[124:127], v[64:67]\n\tds_read_b128 v[100:103], v238 offset:1024\n\tds_read_b128 v[104:107], v238 offset:2048\n\t" \
    "v_mfma_f32_16x16x32_bf16 v[52:55], v[36:39], a[116:119], v[52:55]\n\tds_read_b64_tr_b16 v[24:25], v2 offset:0\n\tds_read_b64_tr_b16 v[26:27], v2 offset:512\n\ts_add_u32 m0, s72, 28672\n\tv_mfma_f32_16x16x32_bf16 v[60:63], v[36:39], a[120:123], v[60:63]\n\tbuffer_load_dwordx4 v10, s[12:15], s69 offen lds\n\tds_read_b128 v[108:111], v238 offset:3072\n\ts_add_u32 m0, s72, 29696\n\t" \
    "v_mfma_f32_16x16x32_bf16 v[68:71], v[36:39], a[124:127], v[68:71]\n\tds_read_b128 v[112:115], v238 offset:4096\n\tds_read_b128 v[116:119], v238 offset:5120\n\tv_mfma_f32_16x16x32_bf16 v[48:51], v[40:43], a[128:131], v[48:51]\n\tds_read_b64_tr_b16 v[28:29], v3 offset:0\n\tds_read_b64_tr_b16 v[30:31], v3 offset:512\n\tv_mfma_f32_16x16x32_bf16 v[56:59], v[40:43], a[132:135], v[56:59]\n\tbuffer_load_dwordx4 v11, s[12:15], s69 offen lds\n\t" \
    "s_mov_b32 s69, 7078016\n\ts_add_u32 m0, s72, 32768\n\tv_mfma_f32_16x16x32_bf16 v[64:67], v[40:43], a[136:139], v[64:67]\n\tv_mfma_f32_16x16x32_bf16 v[52:55], v[44:47], a[128:131], v[52:55]\n\tds_read_b64_tr_b16 v[32:33], v0 offset:4096\n\tds_read_b64_tr_b16 v[34:35], v0 offset:4608\n\tv_mfma_f32_16x16x32_bf16 v[60:63], v[44:47], a[132:135], v[60:63]\n\tbuffer_load_dwordx4 v10, s[12:15], s69 offen lds\n\t" \
    "s_add_u32 m0, s72, 33792\n\tv_mfma_f32_16x16x32_bf16 v[68:71], v[44:47], a[136:139], v[68:71]\n\ts_waitcnt lgkmcnt(10)\n\tv_pk_add_f32 v[48:49], v[48:49], v[96:97]\n\tv_pk_add_f32 v[50:51], v[50:51], v[98:99]\n\tv_pk_add_f32 v[52:53], v[52:53], v[100:101]\n\tbuffer_load_dwordx4 v11, s[12:15], s69 offen lds\n\tv_pk_add_f32 v[54:55], v[54:55], v[102:103]\n\t" \
    "s_mov_b32 s69, 7372928\n\tv_pk_mul_f32 v[96:97], v[48:49], v[48:49]\n\ts_add_u32 m0, s72, 36864\n\tv_pk_mul_f32 v[98:99], v[50:51], v[50:51]\n\tv_pk_mul_f32 v[100:101], v[52:53], v[52:53]\n\tv_pk_mul_f32 v[102:103], v[54:55], v[54:55]\n\tv_pk_fma_f32 v[96:97], v[96:97], v[246:247], v[246:247] op_sel:[0,0,1] op_sel_hi:[1,0,1]\n\tv_pk_fma_f32 v[98:99], v[98:99], v[246:247], v[246:247] op_sel:[0,0,1] op_sel_hi:[1,0,1]\n\t" \
    "v_pk_fma_f32 v[100:101], v[100:101], v[246:247], v[246:247] op_sel:[0,0,1] op_sel_hi:[1,0,1]\n\tv_pk_fma_f32 v[102:103], v[102:103], v[246:247], v[246:247] op_sel:[0,0,1] op_sel_hi:[1,0,1]\n\tv_pk_mul_f32 v[96:97], v[96:97], v[48:49]\n\tv_pk_mul_f32 v[98:99], v[98:99], v[50:51]\n\tbuffer_load_dwordx4 v10, s[12:15], s69 offen lds\n\tv_pk_mul_f32 v[100:101], v[100:101], v[52:53]\n\ts_add_u32 m0, s72, 37888\n\tv_pk_mul_f32 v[102:103], v[102:103], v[54:55]\n\t" \
    "s_waitcnt lgkmcnt(6)\n\tv_exp_f32 v96, v96\n\tv_pk_add_f32 v[56:57], v[56:57], v[104:105]\n\tv_exp_f32 v97, v97\n\tv_pk_add_f32 v[58:59], v[58:59], v[106:107]\n\tv_exp_f32 v98, v98\n\tv_pk_add_f32 v[60:61], v[60:61], v[108:109]\n\tv_exp_f32 v99, v99\n\t" \
    "buffer_load_dwordx4 v11, s[12:15], s69 offen lds\n\tv_pk_add_f32 v[62:63], v[62:63], v[110:111]\n\ts_mov_b32 s69, 7667840\n\tv_exp_f32 v100, v100\n\ts_add_u32 m0, s72, 40960\n\tv_pk_mul_f32 v[104:105], v[56:57], v[56:57]\n\tv_exp_f32 v101, v101\n\tv_pk_mul_f32 v[106:107], v[58:59], v[58:59]\n\t" \
    "v_exp_f32 v102, v102\n\tv_pk_mul_f32 v[108:109], v[60:61], v[60:61]\n\tv_exp_f32 v103, v103\n\tv_pk_mul_f32 v[110:111], v[62:63], v[62:63]\n\tv_pk_add_f32 v[96:97], v[96:97], 1.0 op_sel_hi:[1,0]\n\tbuffer_load_dwordx4 v10, s[12:15], s69 offen lds\n\tv_pk_add_f32 v[98:99], v[98:99], 1.0 op_sel_hi:[1,0]\n\ts_add_u32 m0, s72, 41984\n\t" \
    "v_pk_add_f32 v[100:101], v[100:101], 1.0 op_sel_hi:[1,0]\n\tv_pk_add_f32 v[102:103], v[102:103], 1.0 op_sel_hi:[1,0]\n\tv_pk_fma_f32 v[104:105], v[104:105], v[246:247], v[246:247] op_sel:[0,0,1] op_sel_hi:[1,0,1]\n\tv_rcp_f32 v96, v96\n\tv_pk_fma_f32 v[106:107], v[106:107], v[246:247], v[246:247] op_sel:[0,0,1] op_sel_hi:[1,0,1]\n\tv_rcp_f32 v97, v97\n\tv_pk_fma_f32 v[108:109], v[108:109], v[246:247], v[246:247] op_sel:[0,0,1] op_sel_hi:[1,0,1]\n\tv_rcp_f32 v98, v98\n\t" \
    "v_pk_fma_f32 v[110:111], v[110:111], v[246:247], v[246:247] op_sel:[0,0,1] op_sel_hi:[1,0,1]\n\tv_rcp_f32 v99, v99\n\tbuffer_load_dwordx4 v11, s[12:15], s69 offen lds\n\tv_pk_mul_f32 v[104:105], v[104:105], v[56:57]\n\ts_mov_b32 s69, 7962752\n\tv_rcp_f32 v100, v100\n\ts_add_u32 m0, s72, 45056\n\tv_pk_mul_f32 v[106:107], v[106:107], v[58:59]\n\t" \
    "v_rcp_f32 v101, v101\n\tv_pk_mul_f32 v[108:109], v[108:109], v[60:61]\n\tv_rcp_f32 v102, v102\n\tv_pk_mul_f32 v[110:111], v[110:111], v[62:63]\n\tv_rcp_f32 v103, v103\n\ts_waitcnt lgkmcnt(4)\n\tv_exp_f32 v104, v104\n\tbuffer_load_dwordx4 v10, s[12:15], s69 offen lds\n\t" \
    "v_pk_mul_f32 v[96:97], v[96:97], v[48:49]\n\ts_add_u32 m0, s72, 46080\n\tv_exp_f32 v105, v105\n\tv_pk_mul_f32 v[98:99], v[98:99], v[50:51]\n\tv_exp_f32 v106, v106\n\tv_pk_mul_f32 v[100:101], v[100:101], v[52:53]\n\tv_exp_f32 v107, v107\n\tv_pk_mul_f32 v[102:103], v[102:103], v[54:55]\n\t" \
    "v_exp_f32 v108, v108\n\tbuffer_load_dwordx4 v11, s[12:15], s69 offen lds\n\tv_cvt_pk_bf16_f32 v48, v96, v97\n\ts_mov_b32 s69, 8257664\n\tv_exp_f32 v109, v109\n\ts_add_u32 m0, s72, 49152\n\tv_cvt_pk_bf16_f32 v49, v98, v99\n\tv_exp_f32 v110, v110\n\t" \
    "v_cvt_pk_bf16_f32 v50, v100, v101\n\tv_exp_f32 v111, v111\n\tv_cvt_pk_bf16_f32 v51, v102, v103\n\tbuffer_store_dwordx4 v[48:51], v8, s[8:11], s45 offen offset:0 sc1\n\tv_pk_add_f32 v[104:105], v[104:105], 1.0 op_sel_hi:[1,0]\n\tv_pk_add_f32 v[106:107], v[106:107], 1.0 op_sel_hi:[1,0]\n\tv_pk_add_f32 v[108:109], v[108:109], 1.0 op_sel_hi:[1,0]\n\tv_pk_add_f32 v[110:111], v[110:111], 1.0 op_sel_hi:[1,0]\n\t" \
    "v_pk_add_f32 v[64:65], v[64:65], v[112:113]\n\tv_rcp_f32 v104, v104\n\tv_pk_add_f32 v[66:67], v[66:67], v[114:115]\n\tv_rcp_f32 v105, v105\n\tv_pk_add_f32 v[68:69], v[68:69], v[116:117]\n\tv_rcp_f32 v106, v106\n\tv_pk_add_f32 v[70:71], v[70:71], v[118:119]\n\tv_rcp_f32 v107, v107\n\t" \
    "v_pk_mul_f32 v[112:113], v[64:65], v[64:65]\n\tv_rcp_f32 v108, v108\n\tv_pk_mul_f32 v[114:115], v[66:67], v[66:67]\n\tv_rcp_f32 v109, v109\n\tbuffer_load_dwordx4 v10, s[12:15], s69 offen lds\n\tv_pk_mul_f32 v[116:117], v[68:69], v[68:69]\n\ts_add_u32 m0, s72, 50176\n\tv_rcp_f32 v110, v110\n\t" \
    "v_pk_mul_f32 v[118:119], v[70:71], v[70:71]\n\tv_rcp_f32 v111, v111\n\tv_pk_fma_f32 v[112:113], v[112:113], v[246:247], v[246:247] op_sel:[0,0,1] op_sel_hi:[1,0,1]\n\tv_pk_mul_f32 v[104:105], v[104:105], v[56:57]\n\tv_pk_mul_f32 v[106:107], v[106:107], v[58:59]\n\tv_pk_mul_f32 v[108:109], v[108:109], v[60:61]\n\tv_pk_mul_f32 v[110:111], v[110:111], v[62:63]\n\tv_cvt_pk_bf16_f32 v56, v104, v105\n\t" \
    "v_cvt_pk_bf16_f32 v57, v106, v107\n\tv_cvt_pk_bf16_f32 v58, v108, v109\n\tbuffer_load_dwordx4 v11, s[12:15], s69 offen lds\n\tv_cvt_pk_bf16_f32 v59, v110, v111\n\ts_mov_b32 s69, 8552576\n\tbuffer_store_dwordx4 v[56:59], v8, s[8:11], s45 offen offset:1024 sc1\n\tv_pk_fma_f32 v[114:115], v[114:115], v[246:247], v[246:247] op_sel:[0,0,1] op_sel_hi:[1,0,1]\n\ts_add_u32 m0, s72, 53248\n\t" \
    "v_pk_fma_f32 v[116:117], v[116:117], v[246:247], v[246:247] op_sel:[0,0,1] op_sel_hi:[1,0,1]\n\tv_pk_fma_f32 v[118:119], v[118:119], v[246:247], v[246:247] op_sel:[0,0,1] op_sel_hi:[1,0,1]\n\tv_pk_mul_f32 v[112:113], v[112:113], v[64:65]\n\tv_pk_mul_f32 v[114:115], v[114:115], v[66:67]\n\tv_pk_mul_f32 v[116:117], v[116:117], v[68:69]\n\tv_pk_mul_f32 v[118:119], v[118:119], v[70:71]\n\tv_exp_f32 v112, v112\n\tv_exp_f32 v113, v113\n\t" \
    "v_exp_f32 v114, v114\n\tv_exp_f32 v115, v115\n\tv_exp_f32 v116, v116\n\tv_exp_f32 v117, v117\n\tv_exp_f32 v118, v118\n\tv_exp_f32 v119, v119\n\tv_pk_add_f32 v[112:113], v[112:113], 1.0 op_sel_hi:[1,0]\n\tv_pk_add_f32 v[114:115], v[114:115], 1.0 op_sel_hi:[1,0]\n\t" \
    "v_pk_add_f32 v[116:117], v[116:117], 1.0 op_sel_hi:[1,0]\n\tv_pk_add_f32 v[118:119], v[118:119], 1.0 op_sel_hi:[1,0]\n\tv_rcp_f32 v112, v112\n\tbuffer_load_dwordx4 v10, s[12:15], s69 offen lds\n\tv_rcp_f32 v113, v113\n\ts_add_u32 m0, s72, 54272\n\tv_rcp_f32 v114, v114\n\tv_rcp_f32 v115, v115\n\t" \
    "v_rcp_f32 v116, v116\n\tv_rcp_f32 v117, v117\n\tv_rcp_f32 v118, v118\n\tv_rcp_f32 v119, v119\n\tv_pk_mul_f32 v[112:113], v[112:113], v[64:65]\n\tbuffer_load_dwordx4 v11, s[12:15], s69 offen lds\n\tv_pk_mul_f32 v[114:115], v[114:115], v[66:67]\n\tv_pk_mul_f32 v[116:117], v[116:117], v[68:69]\n\t" \
    "v_pk_mul_f32 v[118:119], v[118:119], v[70:71]\n\tv_cvt_pk_bf16_f32 v64, v112, v113\n\tv_cvt_pk_bf16_f32 v65, v114, v115\n\tv_cvt_pk_bf16_f32 v66, v116, v117\n\tv_cvt_pk_bf16_f32 v67, v118, v119\n\tbuffer_store_dwordx4 v[64:67], v8, s[8:11], s45 offen offset:2048 sc1\n\tv_mfma_f32_16x16x32_bf16 a[24:27], v[16:19], a[80:83], a[24:27]\n\tds_read_b64_tr_b16 v[36:37], v1 offset:4096\n\t" \
    "ds_read_b64_tr_b16 v[38:39], v1 offset:4608\n\tv_mfma_f32_16x16x32_bf16 a[32:35], v[16:19], a[84:87], a[32:35]\n\tv_mfma_f32_16x16x32_bf16 a[40:43], v[16:19], a[88:91], a[40:43]\n\tv_mfma_f32_16x16x32_bf16 a[28:31], v[20:23], a[80:83], a[28:31]\n\tds_read_b64_tr_b16 v[40:41], v2 offset:4096\n\tds_read_b64_tr_b16 v[42:43], v2 offset:4608\n\tv_mfma_f32_16x16x32_bf16 a[36:39], v[20:23], a[84:87], a[36:39]\n\tv_mfma_f32_16x16x32_bf16 a[44:47], v[20:23], a[88:91], a[44:47]\n\t" \
    "v_mfma_f32_16x16x32_bf16 v[72:75], v[24:27], a[80:83], v[72:75]\n\tds_read_b64_tr_b16 v[44:45], v3 offset:4096\n\tds_read_b64_tr_b16 v[46:47], v3 offset:4608\n\tv_mfma_f32_16x16x32_bf16 v[80:83], v[24:27], a[84:87], v[80:83]\n\tbuffer_store_dwordx2 v[248:249], off, s[8:11], s60 sc1\n\tv_mfma_f32_16x16x32_bf16 v[88:91], v[24:27], a[88:91], v[88:91]\n\ts_waitcnt lgkmcnt(8)\n\tv_mfma_f32_16x16x32_bf16 v[76:79], v[28:31], a[80:83], v[76:79]\n\t" \
    "ds_read_b64_tr_b16 v[16:17], v0 offset:8192\n\tds_read_b64_tr_b16 v[18:19], v0 offset:8704\n\tv_mfma_f32_16x16x32_bf16 v[84:87], v[28:31], a[84:87], v[84:87]\n\tv_mfma_f32_16x16x32_bf16 v[92:95], v[28:31], a[88:91], v[92:95]\n\ts_waitcnt lgkmcnt(8)\n\tv_mfma_f32_16x16x32_bf16 a[24:27], v[32:35], a[48:51], a[24:27]\n\tds_read_b64_tr_b16 v[20:21], v1 offset:8192\n\tds_read_b64_tr_b16 v[22:23], v1 offset:8704\n\t" \
    "v_mfma_f32_16x16x32_bf16 a[32:35], v[32:35], a[52:55], a[32:35]\n\tv_mfma_f32_16x16x32_bf16 a[40:43], v[32:35], a[56:59], a[40:43]\n\ts_waitcnt lgkmcnt(8)\n\tv_mfma_f32_16x16x32_bf16 a[28:31], v[36:39], a[48:51], a[28:31]\n\tds_read_b64_tr_b16 v[24:25], v2 offset:8192\n\tds_read_b64_tr_b16 v[26:27], v2 offset:8704\n\tv_mfma_f32_16x16x32_bf16 a[36:39], v[36:39], a[52:55], a[36:39]\n\tv_mfma_f32_16x16x32_bf16 a[44:47], v[36:39], a[56:59], a[44:47]\n\t" \
    "s_waitcnt lgkmcnt(8)\n\tv_mfma_f32_16x16x32_bf16 v[72:75], v[40:43], a[48:51], v[72:75]\n\tds_read_b64_tr_b16 v[28:29], v3 offset:8192\n\tds_read_b64_tr_b16 v[30:31], v3 offset:8704\n\tv_mfma_f32_16x16x32_bf16 v[80:83], v[40:43], a[52:55], v[80:83]\n\tv_mfma_f32_16x16x32_bf16 v[88:91], v[40:43], a[56:59], v[88:91]\n\ts_waitcnt lgkmcnt(8)\n\tv_mfma_f32_16x16x32_bf16 v[76:79], v[44:47], a[48:51], v[76:79]\n\t" \
    "ds_read_b64_tr_b16 v[32:33], v0 offset:12288\n\tds_read_b64_tr_b16 v[34:35], v0 offset:12800\n\tv_mfma_f32_16x16x32_bf16 v[84:87], v[44:47], a[52:55], v[84:87]\n\tv_mfma_f32_16x16x32_bf16 v[92:95], v[44:47], a[56:59], v[92:95]\n\ts_waitcnt lgkmcnt(8)\n\tv_mfma_f32_16x16x32_bf16 a[24:27], v[16:19], a[92:95], a[24:27]\n\tds_read_b64_tr_b16 v[36:37], v1 offset:12288\n\tds_read_b64_tr_b16 v[38:39], v1 offset:12800\n\t" \
    "v_mfma_f32_16x16x32_bf16 a[32:35], v[16:19], a[96:99], a[32:35]\n\tv_mfma_f32_16x16x32_bf16 a[40:43], v[16:19], a[100:103], a[40:43]\n\ts_waitcnt lgkmcnt(8)\n\tv_mfma_f32_16x16x32_bf16 a[28:31], v[20:23], a[92:95], a[28:31]\n\tds_read_b64_tr_b16 v[40:41], v2 offset:12288\n\tds_read_b64_tr_b16 v[42:43], v2 offset:12800\n\tv_mfma_f32_16x16x32_bf16 a[36:39], v[20:23], a[96:99], a[36:39]\n\tv_mfma_f32_16x16x32_bf16 a[44:47], v[20:23], a[100:103], a[44:47]\n\t" \
    "s_waitcnt lgkmcnt(8)\n\tv_mfma_f32_16x16x32_bf16 v[72:75], v[24:27], a[92:95], v[72:75]\n\tds_read_b64_tr_b16 v[44:45], v3 offset:12288\n\tds_read_b64_tr_b16 v[46:47], v3 offset:12800\n\tv_mfma_f32_16x16x32_bf16 v[80:83], v[24:27], a[96:99], v[80:83]\n\tv_mfma_f32_16x16x32_bf16 v[88:91], v[24:27], a[100:103], v[88:91]\n\ts_waitcnt lgkmcnt(8)\n\tv_mfma_f32_16x16x32_bf16 v[76:79], v[28:31], a[92:95], v[76:79]\n\t" \
    "ds_read_b64_tr_b16 v[16:17], v0 offset:16384\n\tds_read_b64_tr_b16 v[18:19], v0 offset:16896\n\tv_mfma_f32_16x16x32_bf16 v[84:87], v[28:31], a[96:99], v[84:87]\n\tv_mfma_f32_16x16x32_bf16 v[92:95], v[28:31], a[100:103], v[92:95]\n\ts_waitcnt vmcnt(16)\n\ts_barrier\n\ts_waitcnt lgkmcnt(8)\n\tv_mfma_f32_16x16x32_bf16 a[24:27], v[32:35], v[120:123], a[24:27]\n\t" \
    "ds_read_b64_tr_b16 v[20:21], v1 offset:16384\n\tds_read_b64_tr_b16 v[22:23], v1 offset:16896\n\ts_mov_b32 s69, 8847488\n\tv_mfma_f32_16x16x32_bf16 a[32:35], v[32:35], v[124:127], a[32:35]\n\ts_add_u32 m0, s72, 57344\n\tv_mfma_f32_16x16x32_bf16 a[40:43], v[32:35], a[60:63], a[40:43]\n\ts_waitcnt lgkmcnt(8)\n\tv_mfma_f32_16x16x32_bf16 a[28:31], v[36:39], v[120:123], a[28:31]\n\t" \
    "ds_read_b64_tr_b16 v[24:25], v2 offset:16384\n\tds_read_b64_tr_b16 v[26:27], v2 offset:16896\n\tv_mfma_f32_16x16x32_bf16 a[36:39], v[36:39], v[124:127], a[36:39]\n\tbuffer_load_dwordx4 v10, s[12:15], s69 offen lds\n\ts_add_u32 m0, s72, 58368\n\tv_mfma_f32_16x16x32_bf16 a[44:47], v[36:39], a[60:63], a[44:47]\n\ts_waitcnt lgkmcnt(8)\n\tv_mfma_f32_16x16x32_bf16 v[72:75], v[40:43], v[120:123], v[72:75]\n\t" \
    "ds_read_b64_tr_b16 v[28:29], v3 offset:16384\n\tds_read_b64_tr_b16 v[30:31], v3 offset:16896\n\tv_mfma_f32_16x16x32_bf16 v[80:83], v[40:43], v[124:127], v[80:83]\n\tbuffer_load_dwordx4 v11, s[12:15], s69 offen lds\n\ts_mov_b32 s69, 9142400\n\ts_add_u32 m0, s72, 61440\n\tv_mfma_f32_16x16x32_bf16 v[88:91], v[40:43], a[60:63], v[88:91]\n\ts_waitcnt lgkmcnt(8)\n\t" \
    "v_mfma_f32_16x16x32_bf16 v[76:79], v[44:47], v[120:123], v[76:79]\n\tds_read_b64_tr_b16 v[32:33], v0 offset:20480\n\tds_read_b64_tr_b16 v[34:35], v0 offset:20992\n\tv_mfma_f32_16x16x32_bf16 v[84:87], v[44:47], v[124:127], v[84:87]\n\tbuffer_load_dwordx4 v10, s[12:15], s69 offen lds\n\ts_add_u32 m0, s72, 62464\n\tv_mfma_f32_16x16x32_bf16 v[92:95], v[44:47], a[60:63], v[92:95]\n\ts_waitcnt lgkmcnt(8)\n\t" \
    "v_mfma_f32_16x16x32_bf16 a[24:27], v[16:19], v[128:131], a[24:27]\n\tds_read_b64_tr_b16 v[36:37], v1 offset:20480\n\tds_read_b64_tr_b16 v[38:39], v1 offset:20992\n\tv_mfma_f32_16x16x32_bf16 a[32:35], v[16:19], v[132:135], a[32:35]\n\tbuffer_load_dwordx4 v11, s[12:15], s69 offen lds\n\ts_mov_b32 s69, 131072\n\ts_add_u32 m0, s54, 0\n\tv_mfma_f32_16x16x32_bf16 a[40:43], v[16:19], v[136:139], a[40:43]\n\t" \
    "s_waitcnt lgkmcnt(8)\n\tv_mfma_f32_16x16x32_bf16 a[28:31], v[20:23], v[128:131], a[28:31]\n\tds_read_b64_tr_b16 v[40:41], v2 offset:20480\n\tds_read_b64_tr_b16 v[42:43], v2 offset:20992\n\tv_mfma_f32_16x16x32_bf16 a[36:39], v[20:23], v[132:135], a[36:39]\n\tbuffer_load_dwordx4 v12, s[16:19], s69 offen lds\n\ts_mov_b32 s69, 131072\n\ts_add_u32 m0, s54, 65536\n\t" \
    "v_mfma_f32_16x16x32_bf16 a[44:47], v[20:23], v[136:139], a[44:47]\n\ts_waitcnt lgkmcnt(8)\n\tv_mfma_f32_16x16x32_bf16 v[72:75], v[24:27], v[128:131], v[72:75]\n\tds_read_b64_tr_b16 v[44:45], v3 offset:20480\n\tds_read_b64_tr_b16 v[46:47], v3 offset:20992\n\tv_mfma_f32_16x16x32_bf16 v[80:83], v[24:27], v[132:135], v[80:83]\n\tbuffer_load_dwordx4 v13, s[16:19], s69 offen lds\n\ts_mov_b32 s69, 0\n\t" \
    "s_add_u32 m0, s54, 4096\n\tv_mfma_f32_16x16x32_bf16 v[88:91], v[24:27], v[136:139], v[88:91]\n\ts_waitcnt lgkmcnt(8)\n\tv_mfma_f32_16x16x32_bf16 v[76:79], v[28:31], v[128:131], v[76:79]\n\tds_read_b64_tr_b16 v[16:17], v0 offset:24576\n\tds_read_b64_tr_b16 v[18:19], v0 offset:25088\n\tv_mfma_f32_16x16x32_bf16 v[84:87], v[28:31], v[132:135], v[84:87]\n\tv_mfma_f32_16x16x32_bf16 v[92:95], v[28:31], v[136:139], v[92:95]\n\t" \
    "s_waitcnt lgkmcnt(8)\n\tv_mfma_f32_16x16x32_bf16 a[24:27], v[32:35], v[140:143], a[24:27]\n\tds_read_b64_tr_b16 v[20:21], v1 offset:24576\n\tds_read_b64_tr_b16 v[22:23], v1 offset:25088\n\tv_mfma_f32_16x16x32_bf16 a[32:35], v[32:35], v[144:147], a[32:35]\n\tbuffer_load_dwordx4 v12, s[16:19], s69 offen lds\n\ts_mov_b32 s69, 0\n\ts_add_u32 m0, s54, 69632\n\t" \
    "v_mfma_f32_16x16x32_bf16 a[40:43], v[32:35], v[148:151], a[40:43]\n\ts_waitcnt lgkmcnt(8)\n\tv_mfma_f32_16x16x32_bf16 a[28:31], v[36:39], v[140:143], a[28:31]\n\tds_read_b64_tr_b16 v[24:25], v2 offset:24576\n\tds_read_b64_tr_b16 v[26:27], v2 offset:25088\n\tv_mfma_f32_16x16x32_bf16 a[36:39], v[36:39], v[144:147], a[36:39]\n\tbuffer_load_dwordx4 v13, s[16:19], s69 offen lds\n\ts_mov_b32 s69, 655360\n\t" \
    "s_add_u32 m0, s54, 8192\n\tv_mfma_f32_16x16x32_bf16 a[44:47], v[36:39], v[148:151], a[44:47]\n\ts_waitcnt lgkmcnt(8)\n\tv_mfma_f32_16x16x32_bf16 v[72:75], v[40:43], v[140:143], v[72:75]\n\tds_read_b64_tr_b16 v[28:29], v3 offset:24576\n\tds_read_b64_tr_b16 v[30:31], v3 offset:25088\n\tv_mfma_f32_16x16x32_bf16 v[80:83], v[40:43], v[144:147], v[80:83]\n\tbuffer_load_dwordx4 v12, s[16:19], s69 offen lds\n\t" \
    "s_mov_b32 s69, 655360\n\ts_add_u32 m0, s54, 73728\n\tv_mfma_f32_16x16x32_bf16 v[88:91], v[40:43], v[148:151], v[88:91]\n\ts_waitcnt lgkmcnt(8)\n\tv_mfma_f32_16x16x32_bf16 v[76:79], v[44:47], v[140:143], v[76:79]\n\tds_read_b64_tr_b16 v[32:33], v0 offset:28672\n\tds_read_b64_tr_b16 v[34:35], v0 offset:29184\n\tv_mfma_f32_16x16x32_bf16 v[84:87], v[44:47], v[144:147], v[84:87]\n\t" \
    "v_mfma_f32_16x16x32_bf16 v[92:95], v[44:47], v[148:151], v[92:95]\n\ts_waitcnt vmcnt(10)\n\tbuffer_store_dwordx2 v[248:249], off, s[8:11], s49 sc1\n\ts_barrier\n\ts_waitcnt lgkmcnt(8)\n\tv_mfma_f32_16x16x32_bf16 a[24:27], v[16:19], v[152:155], a[24:27]\n\tds_read_b64_tr_b16 v[36:37], v1 offset:28672\n\tds_read_b64_tr_b16 v[38:39], v1 offset:29184\n\t" \
    "v_mfma_f32_16x16x32_bf16 a[32:35], v[16:19], v[156:159], a[32:35]\n\tbuffer_load_dwordx4 v13, s[16:19], s69 offen lds\n\ts_mov_b32 s69, 524288\n\ts_add_u32 m0, s54, 12288\n\tv_mfma_f32_16x16x32_bf16 a[40:43], v[16:19], v[160:163], a[40:43]\n\ts_waitcnt lgkmcnt(8)\n\tv_mfma_f32_16x16x32_bf16 a[28:31], v[20:23], v[152:155], a[28:31]\n\tds_read_b64_tr_b16 v[40:41], v2 offset:28672\n\t" \
    "ds_read_b64_tr_b16 v[42:43], v2 offset:29184\n\tv_mfma_f32_16x16x32_bf16 a[36:39], v[20:23], v[156:159], a[36:39]\n\tbuffer_load_dwordx4 v12, s[16:19], s69 offen lds\n\ts_mov_b32 s69, 524288\n\ts_add_u32 m0, s54, 77824\n\tv_mfma_f32_16x16x32_bf16 a[44:47], v[20:23], v[160:163], a[44:47]\n\ts_waitcnt lgkmcnt(8)\n\tv_mfma_f32_16x16x32_bf16 v[72:75], v[24:27], v[152:155], v[72:75]\n\t" \
    "ds_read_b64_tr_b16 v[44:45], v3 offset:28672\n\tds_read_b64_tr_b16 v[46:47], v3 offset:29184\n\tv_mfma_f32_16x16x32_bf16 v[80:83], v[24:27], v[156:159], v[80:83]\n\tbuffer_load_dwordx4 v13, s[16:19], s69 offen lds\n\ts_mov_b32 s69, 1179648\n\ts_add_u32 m0, s54, 16384\n\tv_mfma_f32_16x16x32_bf16 v[88:91], v[24:27], v[160:163], v[88:91]\n\ts_waitcnt lgkmcnt(8)\n\t" \
    "v_mfma_f32_16x16x32_bf16 v[76:79], v[28:31], v[152:155], v[76:79]\n\tds_read_b64_tr_b16 v[16:17], v0 offset:32768\n\tds_read_b64_tr_b16 v[18:19], v0 offset:33280\n\tv_mfma_f32_16x16x32_bf16 v[84:87], v[28:31], v[156:159], v[84:87]\n\tbuffer_load_dwordx4 v12, s[16:19], s69 offen lds\n\ts_mov_b32 s69, 1179648\n\ts_add_u32 m0, s54, 81920\n\tv_mfma_f32_16x16x32_bf16 v[92:95], v[28:31], v[160:163], v[92:95]\n\t" \
    "s_waitcnt lgkmcnt(8)\n\tv_mfma_f32_16x16x32_bf16 a[24:27], v[32:35], v[164:167], a[24:27]\n\tds_read_b64_tr_b16 v[20:21], v1 offset:32768\n\tds_read_b64_tr_b16 v[22:23], v1 offset:33280\n\tv_mfma_f32_16x16x32_bf16 a[32:35], v[32:35], v[168:171], a[32:35]\n\tbuffer_load_dwordx4 v13, s[16:19], s69 offen lds\n\ts_mov_b32 s69, 1048576\n\ts_add_u32 m0, s54, 20480\n\t" \
    "v_mfma_f32_16x16x32_bf16 a[40:43], v[32:35], v[172:175], a[40:43]\n\ts_waitcnt lgkmcnt(8)\n\tv_mfma_f32_16x16x32_bf16 a[28:31], v[36:39], v[164:167], a[28:31]\n\tds_read_b64_tr_b16 v[24:25], v2 offset:32768\n\tds_read_b64_tr_b16 v[26:27], v2 offset:33280\n\tv_mfma_f32_16x16x32_bf16 a[36:39], v[36:39], v[168:171], a[36:39]\n\tbuffer_load_dwordx4 v12, s[16:19], s69 offen lds\n\ts_mov_b32 s69, 1048576\n\t" \
    "s_add_u32 m0, s54, 86016\n\tv_mfma_f32_16x16x32_bf16 a[44:47], v[36:39], v[172:175], a[44:47]\n\ts_waitcnt lgkmcnt(8)\n\tv_mfma_f32_16x16x32_bf16 v[72:75], v[40:43], v[164:167], v[72:75]\n\tds_read_b64_tr_b16 v[28:29], v3 offset:32768\n\tds_read_b64_tr_b16 v[30:31], v3 offset:33280\n\tv_mfma_f32_16x16x32_bf16 v[80:83], v[40:43], v[168:171], v[80:83]\n\tv_mfma_f32_16x16x32_bf16 v[88:91], v[40:43], v[172:175], v[88:91]\n\t" \
    "s_waitcnt lgkmcnt(8)\n\tv_mfma_f32_16x16x32_bf16 v[76:79], v[44:47], v[164:167], v[76:79]\n\tds_read_b64_tr_b16 v[32:33], v0 offset:36864\n\tds_read_b64_tr_b16 v[34:35], v0 offset:37376\n\tv_mfma_f32_16x16x32_bf16 v[84:87], v[44:47], v[168:171], v[84:87]\n\tbuffer_load_dwordx4 v13, s[16:19], s69 offen lds\n\tv_mfma_f32_16x16x32_bf16 v[92:95], v[44:47], v[172:175], v[92:95]\n\ts_waitcnt lgkmcnt(8)\n\t" \
    "v_mfma_f32_16x16x32_bf16 a[24:27], v[16:19], v[176:179], a[24:27]\n\tds_read_b64_tr_b16 v[36:37], v1 offset:36864\n\tds_read_b64_tr_b16 v[38:39], v1 offset:37376\n\tv_mfma_f32_16x16x32_bf16 a[32:35], v[16:19], v[180:183], a[32:35]\n\tv_mfma_f32_16x16x32_bf16 a[40:43], v[16:19], v[184:187], a[40:43]\n\ts_waitcnt lgkmcnt(8)\n\tv_mfma_f32_16x16x32_bf16 a[28:31], v[20:23], v[176:179], a[28:31]\n\tds_read_b64_tr_b16 v[40:41], v2 offset:36864\n\t" \
    "ds_read_b64_tr_b16 v[42:43], v2 offset:37376\n\tv_mfma_f32_16x16x32_bf16 a[36:39], v[20:23], v[180:183], a[36:39]\n\tv_mfma_f32_16x16x32_bf16 a[44:47], v[20:23], v[184:187], a[44:47]\n\ts_waitcnt lgkmcnt(8)\n\tv_mfma_f32_16x16x32_bf16 v[72:75], v[24:27], v[176:179], v[72:75]\n\tds_read_b64_tr_b16 v[44:45], v3 offset:36864\n\tds_read_b64_tr_b16 v[46:47], v3 offset:37376\n\tv_mfma_f32_16x16x32_bf16 v[80:83], v[24:27], v[180:183], v[80:83]\n\t" \
    "v_mfma_f32_16x16x32_bf16 v[88:91], v[24:27], v[184:187], v[88:91]\n\ts_waitcnt lgkmcnt(8)\n\tv_mfma_f32_16x16x32_bf16 v[76:79], v[28:31], v[176:179], v[76:79]\n\tds_read_b64_tr_b16 v[16:17], v0 offset:40960\n\tds_read_b64_tr_b16 v[18:19], v0 offset:41472\n\tv_mfma_f32_16x16x32_bf16 v[84:87], v[28:31], v[180:183], v[84:87]\n\tv_mfma_f32_16x16x32_bf16 v[92:95], v[28:31], v[184:187], v[92:95]\n\ts_waitcnt vmcnt(15)\n\t" \
    "s_barrier\n\ts_waitcnt lgkmcnt(8)\n\tv_mfma_f32_16x16x32_bf16 a[24:27], v[32:35], v[188:191], a[24:27]\n\tds_read_b64_tr_b16 v[20:21], v1 offset:40960\n\tds_read_b64_tr_b16 v[22:23], v1 offset:41472\n\ts_mov_b32 s69, 1703936\n\tv_mfma_f32_16x16x32_bf16 a[32:35], v[32:35], v[192:195], a[32:35]\n\ts_add_u32 m0, s54, 24576\n\t" \
    "v_mfma_f32_16x16x32_bf16 a[40:43], v[32:35], v[196:199], a[40:43]\n\ts_waitcnt lgkmcnt(8)\n\tv_mfma_f32_16x16x32_bf16 a[28:31], v[36:39], v[188:191], a[28:31]\n\tds_read_b64_tr_b16 v[24:25], v2 offset:40960\n\tds_read_b64_tr_b16 v[26:27], v2 offset:41472\n\tv_mfma_f32_16x16x32_bf16 a[36:39], v[36:39], v[192:195], a[36:39]\n\tbuffer_load_dwordx4 v12, s[16:19], s69 offen lds\n\ts_mov_b32 s69, 1703936\n\t" \
    "s_add_u32 m0, s54, 90112\n\tv_mfma_f32_16x16x32_bf16 a[44:47], v[36:39], v[196:199], a[44:47]\n\ts_waitcnt lgkmcnt(8)\n\tv_mfma_f32_16x16x32_bf16 v[72:75], v[40:43], v[188:191], v[72:75]\n\tds_read_b64_tr_b16 v[28:29], v3 offset:40960\n\tds_read_b64_tr_b16 v[30:31], v3 offset:41472\n\tv_mfma_f32_16x16x32_bf16 v[80:83], v[40:43], v[192:195], v[80:83]\n\tbuffer_load_dwordx4 v13, s[16:19], s69 offen lds\n\t" \
    "s_mov_b32 s69, 1572864\n\ts_add_u32 m0, s54, 28672\n\tv_mfma_f32_16x16x32_bf16 v[88:91], v[40:43], v[196:199], v[88:91]\n\ts_waitcnt lgkmcnt(8)\n\tv_mfma_f32_16x16x32_bf16 v[76:79], v[44:47], v[188:191], v[76:79]\n\tds_read_b64_tr_b16 v[32:33], v0 offset:45056\n\tds_read_b64_tr_b16 v[34:35], v0 offset:45568\n\tv_mfma_f32_16x16x32_bf16 v[84:87], v[44:47], v[192:195], v[84:87]\n\t" \
    "buffer_load_dwordx4 v12, s[16:19], s69 offen lds\n\ts_mov_b32 s69, 1572864\n\ts_add_u32 m0, s54, 94208\n\tv_mfma_f32_16x16x32_bf16 v[92:95], v[44:47], v[196:199], v[92:95]\n\ts_waitcnt lgkmcnt(8)\n\tv_mfma_f32_16x16x32_bf16 a[24:27], v[16:19], v[200:203], a[24:27]\n\tds_read_b64_tr_b16 v[36:37], v1 offset:45056\n\tds_read_b64_tr_b16 v[38:39], v1 offset:45568\n\t" \
    "v_mfma_f32_16x16x32_bf16 a[32:35], v[16:19], v[204:207], a[32:35]\n\tbuffer_load_dwordx4 v13, s[16:19], s69 offen lds\n\ts_mov_b32 s69, 2228224\n\ts_add_u32 m0, s54, 32768\n\tv_mfma_f32_16x16x32_bf16 a[40:43], v[16:19], v[208:211], a[40:43]\n\ts_waitcnt lgkmcnt(8)\n\tv_mfma_f32_16x16x32_bf16 a[28:31], v[20:23], v[200:203], a[28:31]\n\tds_read_b64_tr_b16 v[40:41], v2 offset:45056\n\t" \
    "ds_read_b64_tr_b16 v[42:43], v2 offset:45568\n\tv_mfma_f32_16x16x32_bf16 a[36:39], v[20:23], v[204:207], a[36:39]\n\tbuffer_load_dwordx4 v12, s[16:19], s69 offen lds\n\ts_mov_b32 s69, 2228224\n\ts_add_u32 m0, s54, 98304\n\tv_mfma_f32_16x16x32_bf16 a[44:47], v[20:23], v[208:211], a[44:47]\n\ts_waitcnt lgkmcnt(8)\n\tv_mfma_f32_16x16x32_bf16 v[72:75], v[24:27], v[200:203], v[72:75]\n\t" \
    "ds_read_b64_tr_b16 v[44:45], v3 offset:45056\n\tds_read_b64_tr_b16 v[46:47], v3 offset:45568\n\tv_mfma_f32_16x16x32_bf16 v[80:83], v[24:27], v[204:207], v[80:83]\n\tbuffer_load_dwordx4 v13, s[16:19], s69 offen lds\n\tv_mfma_f32_16x16x32_bf16 v[88:91], v[24:27], v[208:211], v[88:91]\n\ts_waitcnt lgkmcnt(8)\n\tv_mfma_f32_16x16x32_bf16 v[76:79], v[28:31], v[200:203], v[76:79]\n\tds_read_b64_tr_b16 v[16:17], v0 offset:49152\n\t" \
    "ds_read_b64_tr_b16 v[18:19], v0 offset:49664\n\tv_mfma_f32_16x16x32_bf16 v[84:87], v[28:31], v[204:207], v[84:87]\n\tv_mfma_f32_16x16x32_bf16 v[92:95], v[28:31], v[208:211], v[92:95]\n\ts_waitcnt lgkmcnt(8)\n\tv_mfma_f32_16x16x32_bf16 a[24:27], v[32:35], v[212:215], a[24:27]\n\tds_read_b64_tr_b16 v[20:21], v1 offset:49152\n\tds_read_b64_tr_b16 v[22:23], v1 offset:49664\n\tv_mfma_f32_16x16x32_bf16 a[32:35], v[32:35], v[216:219], a[32:35]\n\t" \
    "v_mfma_f32_16x16x32_bf16 a[40:43], v[32:35], a[64:67], a[40:43]\n\ts_waitcnt lgkmcnt(8)\n\tv_mfma_f32_16x16x32_bf16 a[28:31], v[36:39], v[212:215], a[28:31]\n\tds_read_b64_tr_b16 v[24:25], v0 offset:53248\n\tds_read_b64_tr_b16 v[26:27], v0 offset:53760\n\tv_mfma_f32_16x16x32_bf16 a[36:39], v[36:39], v[216:219], a[36:39]\n\tv_mfma_f32_16x16x32_bf16 a[44:47], v[36:39], a[64:67], a[44:47]\n\ts_waitcnt lgkmcnt(8)\n\t" \
    "v_mfma_f32_16x16x32_bf16 v[72:75], v[40:43], v[212:215], v[72:75]\n\tds_read_b64_tr_b16 v[28:29], v1 offset:53248\n\tds_read_b64_tr_b16 v[30:31], v1 offset:53760\n\tv_mfma_f32_16x16x32_bf16 v[80:83], v[40:43], v[216:219], v[80:83]\n\tv_mfma_f32_16x16x32_bf16 v[88:91], v[40:43], a[64:67], v[88:91]\n\ts_waitcnt lgkmcnt(8)\n\tv_mfma_f32_16x16x32_bf16 v[76:79], v[44:47], v[212:215], v[76:79]\n\tds_read_b64_tr_b16 v[32:33], v0 offset:57344\n\t" \
    "ds_read_b64_tr_b16 v[34:35], v0 offset:57856\n\tv_mfma_f32_16x16x32_bf16 v[84:87], v[44:47], v[216:219], v[84:87]\n\tv_mfma_f32_16x16x32_bf16 v[92:95], v[44:47], a[64:67], v[92:95]\n\ts_waitcnt vmcnt(18)\n\ts_barrier\n\ts_waitcnt lgkmcnt(8)\n\tv_mfma_f32_16x16x32_bf16 a[24:27], v[16:19], a[68:71], a[24:27]\n\tds_read_b64_tr_b16 v[36:37], v1 offset:57344\n\t" \
    "ds_read_b64_tr_b16 v[38:39], v1 offset:57856\n\ts_mov_b32 s69, 2097152\n\tv_mfma_f32_16x16x32_bf16 a[32:35], v[16:19], a[72:75], a[32:35]\n\ts_add_u32 m0, s54, 36864\n\tv_mfma_f32_16x16x32_bf16 a[40:43], v[16:19], a[76:79], a[40:43]\n\ts_waitcnt lgkmcnt(8)\n\tv_mfma_f32_16x16x32_bf16 a[28:31], v[20:23], a[68:71], a[28:31]\n\tds_read_b64_tr_b16 v[40:41], v0 offset:61440\n\t" \
    "ds_read_b64_tr_b16 v[42:43], v0 offset:61952\n\tv_mfma_f32_16x16x32_bf16 a[36:39], v[20:23], a[72:75], a[36:39]\n\tbuffer_load_dwordx4 v12, s[16:19], s69 offen lds\n\ts_mov_b32 s69, 2097152\n\ts_add_u32 m0, s54, 102400\n\tv_mfma_f32_16x16x32_bf16 a[44:47], v[20:23], a[76:79], a[44:47]\n\ts_waitcnt lgkmcnt(8)\n\tv_mfma_f32_16x16x32_bf16 a[24:27], v[24:27], a[104:107], a[24:27]\n\t" \
    "ds_read_b64_tr_b16 v[44:45], v1 offset:61440\n\tds_read_b64_tr_b16 v[46:47], v1 offset:61952\n\tv_mfma_f32_16x16x32_bf16 a[32:35], v[24:27], a[108:111], a[32:35]\n\tbuffer_load_dwordx4 v13, s[16:19], s69 offen lds\n\ts_mov_b32 s69, 2752512\n\ts_add_u32 m0, s54, 40960\n\tv_mfma_f32_16x16x32_bf16 a[40:43], v[24:27], a[112:115], a[40:43]\n\ts_waitcnt lgkmcnt(8)\n\t" \
    "v_mfma_f32_16x16x32_bf16 a[28:31], v[28:31], a[104:107], a[28:31]\n\tds_read_b64_tr_b16 v[16:17], v2 offset:49152\n\tds_read_b64_tr_b16 v[18:19], v2 offset:49664\n\tv_mfma_f32_16x16x32_bf16 a[36:39], v[28:31], a[108:111], a[36:39]\n\tbuffer_load_dwordx4 v12, s[16:19], s69 offen lds\n\ts_mov_b32 s69, 2752512\n\ts_add_u32 m0, s54, 106496\n\tv_mfma_f32_16x16x32_bf16 a[44:47], v[28:31], a[112:115], a[44:47]\n\t" \
    "buffer_load_dwordx2 v[250:251], v241, s[8:11], s50 offen sc1\n\ts_waitcnt lgkmcnt(8)\n\tv_mfma_f32_16x16x32_bf16 a[24:27], v[32:35], a[116:119], a[24:27]\n\tds_read_b64_tr_b16 v[20:21], v3 offset:49152\n\tds_read_b64_tr_b16 v[22:23], v3 offset:49664\n\tv_mfma_f32_16x16x32_bf16 a[32:35], v[32:35], a[120:123], a[32:35]\n\tv_mfma_f32_16x16x32_bf16 a[40:43], v[32:35], a[124:127], a[40:43]\n\ts_waitcnt lgkmcnt(8)\n\t" \
    "v_mfma_f32_16x16x32_bf16 a[28:31], v[36:39], a[116:119], a[28:31]\n\tds_read_b64_tr_b16 v[24:25], v2 offset:53248\n\tds_read_b64_tr_b16 v[26:27], v2 offset:53760\n\tv_mfma_f32_16x16x32_bf16 a[36:39], v[36:39], a[120:123], a[36:39]\n\tv_mfma_f32_16x16x32_bf16 a[44:47], v[36:39], a[124:127], a[44:47]\n\ts_waitcnt lgkmcnt(8)\n\tv_mfma_f32_16x16x32_bf16 a[24:27], v[40:43], a[128:131], a[24:27]\n\tds_read_b64_tr_b16 v[28:29], v3 offset:53248\n\t" \
    "ds_read_b64_tr_b16 v[30:31], v3 offset:53760\n\tv_mfma_f32_16x16x32_bf16 a[32:35], v[40:43], a[132:135], a[32:35]\n\tv_mfma_f32_16x16x32_bf16 a[40:43], v[40:43], a[136:139], a[40:43]\n\ts_waitcnt lgkmcnt(8)\n\tv_mfma_f32_16x16x32_bf16 a[28:31], v[44:47], a[128:131], a[28:31]\n\tds_read_b64_tr_b16 v[32:33], v2 offset:57344\n\tds_read_b64_tr_b16 v[34:35], v2 offset:57856\n\tv_mfma_f32_16x16x32_bf16 a[36:39], v[44:47], a[132:135], a[36:39]\n\t" \
    "v_mfma_f32_16x16x32_bf16 a[44:47], v[44:47], a[136:139], a[44:47]\n\ts_waitcnt lgkmcnt(8)\n\tv_mfma_f32_16x16x32_bf16 v[72:75], v[16:19], a[68:71], v[72:75]\n\tds_read_b64_tr_b16 v[36:37], v3 offset:57344\n\tds_read_b64_tr_b16 v[38:39], v3 offset:57856\n\tv_mfma_f32_16x16x32_bf16 v[80:83], v[16:19], a[72:75], v[80:83]\n\tv_mfma_f32_16x16x32_bf16 v[88:91], v[16:19], a[76:79], v[88:91]\n\tds_write_b128 v237, a[24:27] offset:0\n\t" \
    "s_waitcnt lgkmcnt(9)\n\tv_mfma_f32_16x16x32_bf16 v[76:79], v[20:23], a[68:71], v[76:79]\n\tds_read_b64_tr_b16 v[40:41], v2 offset:61440\n\tds_read_b64_tr_b16 v[42:43], v2 offset:61952\n\tv_mfma_f32_16x16x32_bf16 v[84:87], v[20:23], a[72:75], v[84:87]\n\tv_mfma_f32_16x16x32_bf16 v[92:95], v[20:23], a[76:79], v[92:95]\n\tds_write_b128 v237, a[32:35] offset:2048\n\ts_waitcnt lgkmcnt(10)\n\t" \
    "v_mfma_f32_16x16x32_bf16 v[72:75], v[24:27], a[104:107], v[72:75]\n\tds_read_b64_tr_b16 v[44:45], v3 offset:61440\n\tds_read_b64_tr_b16 v[46:47], v3 offset:61952\n\tv_mfma_f32_16x16x32_bf16 v[80:83], v[24:27], a[108:111], v[80:83]\n\tv_mfma_f32_16x16x32_bf16 v[88:91], v[24:27], a[112:115], v[88:91]\n\tds_write_b128 v237, a[40:43] offset:4096\n\ts_waitcnt lgkmcnt(11)\n\tv_mfma_f32_16x16x32_bf16 v[76:79], v[28:31], a[104:107], v[76:79]\n\t" \
    "ds_read_b64_tr_b16 v[16:17], v4 offset:0\n\tds_read_b64_tr_b16 v[18:19], v4 offset:2048\n\tv_mfma_f32_16x16x32_bf16 v[84:87], v[28:31], a[108:111], v[84:87]\n\tv_mfma_f32_16x16x32_bf16 v[92:95], v[28:31], a[112:115], v[92:95]\n\tds_write_b128 v237, a[28:31] offset:1024\n\tds_write_b128 v237, a[36:39] offset:3072\n\tds_write_b128 v237, a[44:47] offset:5120\n\ts_waitcnt vmcnt(21) lgkmcnt(0)\n\t" \
    "s_barrier\n\tv_mfma_f32_16x16x32_bf16 v[72:75], v[32:35], a[116:119], v[72:75]\n\tds_read_b64_tr_b16 v[20:21], v5 offset:0\n\tds_read_b64_tr_b16 v[22:23], v5 offset:2048\n\tv_mfma_f32_16x16x32_bf16 v[80:83], v[32:35], a[120:123], v[80:83]\n\tbuffer_load_dwordx4 v13, s[16:19], s69 offen lds\n\tds_read_b128 v[96:99], v238 offset:0\n\ts_mov_b32 s69, 2621440\n\t" \
    "v_mfma_f32_16x16x32_bf16 v[88:91], v[32:35], a[124:127], v[88:91]\n\tds_read_b128 v[100:103], v238 offset:1024\n\tds_read_b128 v[104:107], v238 offset:2048\n\tv_mfma_f32_16x16x32_bf16 v[76:79], v[36:39], a[116:119], v[76:79]\n\tds_read_b64_tr_b16 v[24:25], v6 offset:0\n\tds_read_b64_tr_b16 v[26:27], v6 offset:2048\n\ts_add_u32 m0, s54, 45056\n\tv_mfma_f32_16x16x32_bf16 v[84:87], v[36:39], a[120:123], v[84:87]\n\t" \
    "buffer_load_dwordx4 v12, s[16:19], s69 offen lds\n\tds_read_b128 v[108:111], v238 offset:3072\n\ts_mov_b32 s69, 2621440\n\tv_mfma_f32_16x16x32_bf16 v[92:95], v[36:39], a[124:127], v[92:95]\n\tds_read_b128 v[112:115], v238 offset:4096\n\tds_read_b128 v[116:119], v238 offset:5120\n\tv_mfma_f32_16x16x32_bf16 v[72:75], v[40:43], a[128:131], v[72:75]\n\tds_read_b64_tr_b16 v[28:29], v7 offset:0\n\t" \
    "ds_read_b64_tr_b16 v[30:31], v7 offset:2048\n\ts_add_u32 m0, s54, 110592\n\tv_mfma_f32_16x16x32_bf16 v[80:83], v[40:43], a[132:135], v[80:83]\n\tbuffer_load_dwordx4 v13, s[16:19], s69 offen lds\n\ts_mov_b32 s69, 3276800\n\ts_add_u32 m0, s54, 49152\n\tv_mfma_f32_16x16x32_bf16 v[88:91], v[40:43], a[136:139], v[88:91]\n\tv_mfma_f32_16x16x32_bf16 v[76:79], v[44:47], a[128:131], v[76:79]\n\t" \
    "ds_read_b64_tr_b16 v[32:33], v224 offset:0\n\tds_read_b64_tr_b16 v[34:35], v224 offset:2048\n\tv_mfma_f32_16x16x32_bf16 v[84:87], v[44:47], a[132:135], v[84:87]\n\tbuffer_load_dwordx4 v12, s[16:19], s69 offen lds\n\ts_mov_b32 s69, 3276800\n\ts_add_u32 m0, s54, 114688\n\tv_mfma_f32_16x16x32_bf16 v[92:95], v[44:47], a[136:139], v[92:95]\n\tbuffer_load_dwordx4 v13, s[16:19], s69 offen lds\n\t" \
    "s_cmp_lg_u32 s39, 2\n\ts_cbranch_scc1 107f\n\ts_waitcnt vmcnt(5)\n\tv_cmp_ne_u64 vcc, s[36:37], v[250:251]\n\ts_cbranch_vccz 107f\n\ts_add_u32 s73, s45, 55296\n\ts_waitcnt lgkmcnt(10)\n\tv_pk_add_f32 v[72:73], v[72:73], v[96:97]\n\t" \
    "s_mov_b32 s69, 3145728\n\ts_add_u32 m0, s54, 53248\n\tv_pk_add_f32 v[74:75], v[74:75], v[98:99]\n\tv_pk_add_f32 v[76:77], v[76:77], v[100:101]\n\tv_pk_add_f32 v[78:79], v[78:79], v[102:103]\n\tv_pk_mul_f32 v[96:97], v[72:73], v[72:73]\n\tv_pk_mul_f32 v[98:99], v[74:75], v[74:75]\n\tv_pk_mul_f32 v[100:101], v[76:77], v[76:77]\n\t" \
    "buffer_load_dwordx4 v12, s[16:19], s69 offen lds\n\ts_mov_b32 s69, 3145728\n\ts_add_u32 m0, s54, 118784\n\tv_pk_mul_f32 v[102:103], v[78:79], v[78:79]\n\tv_pk_fma_f32 v[96:97], v[96:97], v[246:247], v[246:247] op_sel:[0,0,1] op_sel_hi:[1,0,1]\n\tv_pk_fma_f32 v[98:99], v[98:99], v[246:247], v[246:247] op_sel:[0,0,1] op_sel_hi:[1,0,1]\n\tv_pk_fma_f32 v[100:101], v[100:101], v[246:247], v[246:247] op_sel:[0,0,1] op_sel_hi:[1,0,1]\n\tv_pk_fma_f32 v[102:103], v[102:103], v[246:247], v[246:247] op_sel:[0,0,1] op_sel_hi:[1,0,1]\n\t" \
    "v_pk_mul_f32 v[96:97], v[96:97], v[72:73]\n\tv_pk_mul_f32 v[98:99], v[98:99], v[74:75]\n\tv_pk_mul_f32 v[100:101], v[100:101], v[76:77]\n\tv_pk_mul_f32 v[102:103], v[102:103], v[78:79]\n\ts_waitcnt lgkmcnt(6)\n\tv_exp_f32 v96, v96\n\tv_pk_add_f32 v[80:81], v[80:81], v[104:105]\n\tv_exp_f32 v97, v97\n\t" \
    "buffer_load_dwordx4 v13, s[16:19], s69 offen lds\n\tv_pk_add_f32 v[82:83], v[82:83], v[106:107]\n\tv_exp_f32 v98, v98\n\tv_pk_add_f32 v[84:85], v[84:85], v[108:109]\n\tv_exp_f32 v99, v99\n\tv_pk_add_f32 v[86:87], v[86:87], v[110:111]\n\tv_exp_f32 v100, v100\n\tv_pk_mul_f32 v[104:105], v[80:81], v[80:81]\n\t" \
    "v_exp_f32 v101, v101\n\tv_pk_mul_f32 v[106:107], v[82:83], v[82:83]\n\tv_exp_f32 v102, v102\n\tv_pk_mul_f32 v[108:109], v[84:85], v[84:85]\n\tv_exp_f32 v103, v103\n\tv_pk_mul_f32 v[110:111], v[86:87], v[86:87]\n\tv_pk_add_f32 v[96:97], v[96:97], 1.0 op_sel_hi:[1,0]\n\tv_pk_add_f32 v[98:99], v[98:99], 1.0 op_sel_hi:[1,0]\n\t" \
    "v_pk_add_f32 v[100:101], v[100:101], 1.0 op_sel_hi:[1,0]\n\tv_pk_add_f32 v[102:103], v[102:103], 1.0 op_sel_hi:[1,0]\n\tv_pk_fma_f32 v[104:105], v[104:105], v[246:247], v[246:247] op_sel:[0,0,1] op_sel_hi:[1,0,1]\n\tv_rcp_f32 v96, v96\n\tv_pk_fma_f32 v[106:107], v[106:107], v[246:247], v[246:247] op_sel:[0,0,1] op_sel_hi:[1,0,1]\n\tv_rcp_f32 v97, v97\n\tv_pk_fma_f32 v[108:109], v[108:109], v[246:247], v[246:247] op_sel:[0,0,1] op_sel_hi:[1,0,1]\n\tv_rcp_f32 v98, v98\n\t" \
    "v_pk_fma_f32 v[110:111], v[110:111], v[246:247], v[246:247] op_sel:[0,0,1] op_sel_hi:[1,0,1]\n\tv_rcp_f32 v99, v99\n\tv_pk_mul_f32 v[104:105], v[104:105], v[80:81]\n\tv_rcp_f32 v100, v100\n\tv_pk_mul_f32 v[106:107], v[106:107], v[82:83]\n\tv_rcp_f32 v101, v101\n\tv_pk_mul_f32 v[108:109], v[108:109], v[84:85]\n\tv_rcp_f32 v102, v102\n\t" \
    "v_pk_mul_f32 v[110:111], v[110:111], v[86:87]\n\tv_rcp_f32 v103, v103\n\ts_waitcnt lgkmcnt(4)\n\tv_exp_f32 v104, v104\n\tv_pk_mul_f32 v[96:97], v[96:97], v[72:73]\n\tv_exp_f32 v105, v105\n\tv_pk_mul_f32 v[98:99], v[98:99], v[74:75]\n\tv_exp_f32 v106, v106\n\t" \
    "v_pk_mul_f32 v[100:101], v[100:101], v[76:77]\n\tv_exp_f32 v107, v107\n\tv_pk_mul_f32 v[102:103], v[102:103], v[78:79]\n\tv_exp_f32 v108, v108\n\tv_cvt_pk_bf16_f32 v72, v96, v97\n\tv_exp_f32 v109, v109\n\tv_cvt_pk_bf16_f32 v73, v98, v99\n\tv_exp_f32 v110, v110\n\t" \
    "v_cvt_pk_bf16_f32 v74, v100, v101\n\tv_exp_f32 v111, v111\n\tv_cvt_pk_bf16_f32 v75, v102, v103\n\tv_pk_add_f32 v[104:105], v[104:105], 1.0 op_sel_hi:[1,0]\n\tv_pk_add_f32 v[106:107], v[106:107], 1.0 op_sel_hi:[1,0]\n\tv_pk_add_f32 v[108:109], v[108:109], 1.0 op_sel_hi:[1,0]\n\tv_pk_add_f32 v[110:111], v[110:111], 1.0 op_sel_hi:[1,0]\n\tv_pk_add_f32 v[88:89], v[88:89], v[112:113]\n\t" \
    "v_rcp_f32 v104, v104\n\tv_pk_add_f32 v[90:91], v[90:91], v[114:115]\n\tv_rcp_f32 v105, v105\n\tv_pk_add_f32 v[92:93], v[92:93], v[116:117]\n\tv_rcp_f32 v106, v106\n\tv_pk_add_f32 v[94:95], v[94:95], v[118:119]\n\tv_rcp_f32 v107, v107\n\tv_pk_mul_f32 v[112:113], v[88:89], v[88:89]\n\t" \
    "v_rcp_f32 v108, v108\n\tv_pk_mul_f32 v[114:115], v[90:91], v[90:91]\n\tv_rcp_f32 v109, v109\n\tv_pk_mul_f32 v[116:117], v[92:93], v[92:93]\n\tv_rcp_f32 v110, v110\n\tv_pk_mul_f32 v[118:119], v[94:95], v[94:95]\n\tv_rcp_f32 v111, v111\n\tv_pk_fma_f32 v[112:113], v[112:113], v[246:247], v[246:247] op_sel:[0,0,1] op_sel_hi:[1,0,1]\n\t" \
    "v_pk_mul_f32 v[104:105], v[104:105], v[80:81]\n\tv_pk_mul_f32 v[106:107], v[106:107], v[82:83]\n\tv_pk_mul_f32 v[108:109], v[108:109], v[84:85]\n\tv_pk_mul_f32 v[110:111], v[110:111], v[86:87]\n\tv_cvt_pk_bf16_f32 v80, v104, v105\n\tv_cvt_pk_bf16_f32 v81, v106, v107\n\tv_cvt_pk_bf16_f32 v82, v108, v109\n\tv_cvt_pk_bf16_f32 v83, v110, v111\n\t" \
    "buffer_load_dwordx2 v[250:251], v241, s[8:11], s50 offen sc1\n\tv_pk_fma_f32 v[114:115], v[114:115], v[246:247], v[246:247] op_sel:[0,0,1] op_sel_hi:[1,0,1]\n\tv_pk_fma_f32 v[116:117], v[116:117], v[246:247], v[246:247] op_sel:[0,0,1] op_sel_hi:[1,0,1]\n\tv_pk_fma_f32 v[118:119], v[118:119], v[246:247], v[246:247] op_sel:[0,0,1] op_sel_hi:[1,0,1]\n\tv_pk_mul_f32 v[112:113], v[112:113], v[88:89]\n\tv_pk_mul_f32 v[114:115], v[114:115], v[90:91]\n\tv_pk_mul_f32 v[116:117], v[116:117], v[92:93]\n\tv_pk_mul_f32 v[118:119], v[118:119], v[94:95]\n\t" \
    "v_exp_f32 v112, v112\n\tv_exp_f32 v113, v113\n\tv_exp_f32 v114, v114\n\tv_exp_f32 v115, v115\n\tv_exp_f32 v116, v116\n\tv_exp_f32 v117, v117\n\tv_exp_f32 v118, v118\n\tv_exp_f32 v119, v119\n\t" \
    "v_pk_add_f32 v[112:113], v[112:113], 1.0 op_sel_hi:[1,0]\n\tv_pk_add_f32 v[114:115], v[114:115], 1.0 op_sel_hi:[1,0]\n\tv_pk_add_f32 v[116:117], v[116:117], 1.0 op_sel_hi:[1,0]\n\tv_pk_add_f32 v[118:119], v[118:119], 1.0 op_sel_hi:[1,0]\n\tv_rcp_f32 v112, v112\n\tv_rcp_f32 v113, v113\n\tv_rcp_f32 v114, v114\n\tv_rcp_f32 v115, v115\n\t" \
    "v_rcp_f32 v116, v116\n\tv_rcp_f32 v117, v117\n\tv_rcp_f32 v118, v118\n\tv_rcp_f32 v119, v119\n\tv_pk_mul_f32 v[112:113], v[112:113], v[88:89]\n\tv_pk_mul_f32 v[114:115], v[114:115], v[90:91]\n\tv_pk_mul_f32 v[116:117], v[116:117], v[92:93]\n\tv_pk_mul_f32 v[118:119], v[118:119], v[94:95]\n\t" \
    "v_cvt_pk_bf16_f32 v88, v112, v113\n\tv_cvt_pk_bf16_f32 v89, v114, v115\n\tv_cvt_pk_bf16_f32 v90, v116, v117\n\tv_cvt_pk_bf16_f32 v91, v118, v119\n\ts_waitcnt vmcnt(0)\n\tv_cmp_ne_u64 vcc, s[36:37], v[250:251]\n\ts_cbranch_vccz 110f\n\ts_mov_b32 s71, 1048576\n\t" \
    "109:\n\ts_sleep 1\n\tbuffer_load_dwordx2 v[250:251], v241, s[8:11], s50 offen sc1\n\ts_waitcnt vmcnt(0)\n\tv_cmp_ne_u64 vcc, s[36:37], v[250:251]\n\ts_cbranch_vccz 110f\n\ts_sub_u32 s71, s71, 1\n\ts_cbranch_scc0 109b\n\t" \
    "110:\n\ts_add_u32 s68, s46, 0\n\tbuffer_load_dwordx4 v[192:195], v8, s[8:11], s68 offen offset:0 sc1\n\tbuffer_load_dwordx4 v[196:199], v8, s[8:11], s68 offen offset:1024 sc1\n\tbuffer_load_dwordx4 v[200:203], v8, s[8:11], s68 offen offset:2048 sc1\n\ts_add_u32 s68, s46, 3072\n\tbuffer_load_dwordx4 v[204:207], v8, s[8:11], s68 offen offset:0 sc1\n\tbuffer_load_dwordx4 v[208:211], v8, s[8:11], s68 offen offset:1024 sc1\n\t" \
    "buffer_load_dwordx4 v[212:215], v8, s[8:11], s68 offen offset:2048 sc1\n\ts_add_u32 s68, s46, 6144\n\tbuffer_load_dwordx4 v[216:219], v8, s[8:11], s68 offen offset:0 sc1\n\tbuffer_load_dwordx4 a[48:51], v8, s[8:11], s68 offen offset:1024 sc1\n\tbuffer_load_dwordx4 a[52:55], v8, s[8:11], s68 offen offset:2048 sc1\n\ts_add_u32 s68, s46, 9216\n\tbuffer_load_dwordx4 a[56:59], v8, s[8:11], s68 offen offset:0 sc1\n\tbuffer_load_dwordx4 a[60:63], v8, s[8:11], s68 offen offset:1024 sc1\n\t" \
    "buffer_load_dwordx4 a[64:67], v8, s[8:11], s68 offen offset:2048 sc1\n\ts_add_u32 s68, s46, 12288\n\tbuffer_load_dwordx4 a[68:71], v8, s[8:11], s68 offen offset:0 sc1\n\tbuffer_load_dwordx4 a[72:75], v8, s[8:11], s68 offen offset:1024 sc1\n\tbuffer_load_dwordx4 a[76:79], v8, s[8:11], s68 offen offset:2048 sc1\n\tbuffer_store_dwordx4 v[72:75], v8, s[8:11], s73 offen offset:0 sc1\n\tbuffer_store_dwordx4 v[80:83], v8, s[8:11], s73 offen offset:1024 sc1\n\tbuffer_store_dwordx4 v[88:91], v8, s[8:11], s73 offen offset:2048 sc1\n\t" \
    "s_branch 108f\n\t107:\n\ts_add_u32 s68, s46, 0\n\tbuffer_load_dwordx4 v[192:195], v8, s[8:11], s68 offen offset:0 sc1\n\tbuffer_load_dwordx4 v[196:199], v8, s[8:11], s68 offen offset:1024 sc1\n\tbuffer_load_dwordx4 v[200:203], v8, s[8:11], s68 offen offset:2048 sc1\n\ts_add_u32 s73, s45, 55296\n\ts_waitcnt lgkmcnt(10)\n\t" \
    "v_pk_add_f32 v[72:73], v[72:73], v[96:97]\n\ts_add_u32 s68, s46, 3072\n\tv_pk_add_f32 v[74:75], v[74:75], v[98:99]\n\tv_pk_add_f32 v[76:77], v[76:77], v[100:101]\n\tv_pk_add_f32 v[78:79], v[78:79], v[102:103]\n\tv_pk_mul_f32 v[96:97], v[72:73], v[72:73]\n\tv_pk_mul_f32 v[98:99], v[74:75], v[74:75]\n\tv_pk_mul_f32 v[100:101], v[76:77], v[76:77]\n\t" \
    "v_pk_mul_f32 v[102:103], v[78:79], v[78:79]\n\tv_pk_fma_f32 v[96:97], v[96:97], v[246:247], v[246:247] op_sel:[0,0,1] op_sel_hi:[1,0,1]\n\tv_pk_fma_f32 v[98:99], v[98:99], v[246:247], v[246:247] op_sel:[0,0,1] op_sel_hi:[1,0,1]\n\tv_pk_fma_f32 v[100:101], v[100:101], v[246:247], v[246:247] op_sel:[0,0,1] op_sel_hi:[1,0,1]\n\tv_pk_fma_f32 v[102:103], v[102:103], v[246:247], v[246:247] op_sel:[0,0,1] op_sel_hi:[1,0,1]\n\tv_pk_mul_f32 v[96:97], v[96:97], v[72:73]\n\tbuffer_load_dwordx4 v[204:207], v8, s[8:11], s68 offen offset:0 sc1\n\tv_pk_mul_f32 v[98:99], v[98:99], v[74:75]\n\t" \
    "v_pk_mul_f32 v[100:101], v[100:101], v[76:77]\n\tv_pk_mul_f32 v[102:103], v[102:103], v[78:79]\n\ts_waitcnt lgkmcnt(6)\n\tv_exp_f32 v96, v96\n\tv_pk_add_f32 v[80:81], v[80:81], v[104:105]\n\tv_exp_f32 v97, v97\n\tv_pk_add_f32 v[82:83], v[82:83], v[106:107]\n\tv_exp_f32 v98, v98\n\t" \
    "v_pk_add_f32 v[84:85], v[84:85], v[108:109]\n\tv_exp_f32 v99, v99\n\tv_pk_add_f32 v[86:87], v[86:87], v[110:111]\n\tbuffer_load_dwordx4 v[208:211], v8, s[8:11], s68 offen offset:1024 sc1\n\tv_exp_f32 v100, v100\n\tv_pk_mul_f32 v[104:105], v[80:81], v[80:81]\n\tv_exp_f32 v101, v101\n\tv_pk_mul_f32 v[106:107], v[82:83], v[82:83]\n\t" \
    "v_exp_f32 v102, v102\n\tv_pk_mul_f32 v[108:109], v[84:85], v[84:85]\n\tv_exp_f32 v103, v103\n\tv_pk_mul_f32 v[110:111], v[86:87], v[86:87]\n\tv_pk_add_f32 v[96:97], v[96:97], 1.0 op_sel_hi:[1,0]\n\tv_pk_add_f32 v[98:99], v[98:99], 1.0 op_sel_hi:[1,0]\n\tv_pk_add_f32 v[100:101], v[100:101], 1.0 op_sel_hi:[1,0]\n\tv_pk_add_f32 v[102:103], v[102:103], 1.0 op_sel_hi:[1,0]\n\t" \
    "buffer_load_dwordx4 v[212:215], v8, s[8:11], s68 offen offset:2048 sc1\n\ts_add_u32 s68, s46, 6144\n\tv_pk_fma_f32 v[104:105], v[104:105], v[246:247], v[246:247] op_sel:[0,0,1] op_sel_hi:[1,0,1]\n\tv_rcp_f32 v96, v96\n\tv_pk_fma_f32 v[106:107], v[106:107], v[246:247], v[246:247] op_sel:[0,0,1] op_sel_hi:[1,0,1]\n\tv_rcp_f32 v97, v97\n\tv_pk_fma_f32 v[108:109], v[108:109], v[246:247], v[246:247] op_sel:[0,0,1] op_sel_hi:[1,0,1]\n\tv_rcp_f32 v98, v98\n\t" \
    "v_pk_fma_f32 v[110:111], v[110:111], v[246:247], v[246:247] op_sel:[0,0,1] op_sel_hi:[1,0,1]\n\tv_rcp_f32 v99, v99\n\tv_pk_mul_f32 v[104:105], v[104:105], v[80:81]\n\tv_rcp_f32 v100, v100\n\tbuffer_load_dwordx4 v[216:219], v8, s[8:11], s68 offen offset:0 sc1\n\tv_pk_mul_f32 v[106:107], v[106:107], v[82:83]\n\tv_rcp_f32 v101, v101\n\tv_pk_mul_f32 v[108:109], v[108:109], v[84:85]\n\t" \
    "v_rcp_f32 v102, v102\n\tv_pk_mul_f32 v[110:111], v[110:111], v[86:87]\n\tv_rcp_f32 v103, v103\n\ts_waitcnt lgkmcnt(4)\n\tv_exp_f32 v104, v104\n\tv_pk_mul_f32 v[96:97], v[96:97], v[72:73]\n\tv_exp_f32 v105, v105\n\tv_pk_mul_f32 v[98:99], v[98:99], v[74:75]\n\t" \
    "buffer_load_dwordx4 a[48:51], v8, s[8:11], s68 offen offset:1024 sc1\n\tv_exp_f32 v106, v106\n\tv_pk_mul_f32 v[100:101], v[100:101], v[76:77]\n\tv_exp_f32 v107, v107\n\tv_pk_mul_f32 v[102:103], v[102:103], v[78:79]\n\tv_exp_f32 v108, v108\n\tv_cvt_pk_bf16_f32 v72, v96, v97\n\tv_exp_f32 v109, v109\n\t" \
    "v_cvt_pk_bf16_f32 v73, v98, v99\n\tv_exp_f32 v110, v110\n\tv_cvt_pk_bf16_f32 v74, v100, v101\n\tv_exp_f32 v111, v111\n\tbuffer_load_dwordx4 a[52:55], v8, s[8:11], s68 offen offset:2048 sc1\n\ts_add_u32 s68, s46, 9216\n\tv_cvt_pk_bf16_f32 v75, v102, v103\n\tv_pk_add_f32 v[104:105], v[104:105], 1.0 op_sel_hi:[1,0]\n\t" \
    "v_pk_add_f32 v[106:107], v[106:107], 1.0 op_sel_hi:[1,0]\n\tv_pk_add_f32 v[108:109], v[108:109], 1.0 op_sel_hi:[1,0]\n\tv_pk_add_f32 v[110:111], v[110:111], 1.0 op_sel_hi:[1,0]\n\tv_pk_add_f32 v[88:89], v[88:89], v[112:113]\n\tv_rcp_f32 v104, v104\n\tv_pk_add_f32 v[90:91], v[90:91], v[114:115]\n\tv_rcp_f32 v105, v105\n\tv_pk_add_f32 v[92:93], v[92:93], v[116:117]\n\t" \
    "v_rcp_f32 v106, v106\n\tv_pk_add_f32 v[94:95], v[94:95], v[118:119]\n\tbuffer_load_dwordx4 a[56:59], v8, s[8:11], s68 offen offset:0 sc1\n\tv_rcp_f32 v107, v107\n\tv_pk_mul_f32 v[112:113], v[88:89], v[88:89]\n\tv_rcp_f32 v108, v108\n\tv_pk_mul_f32 v[114:115], v[90:91], v[90:91]\n\tv_rcp_f32 v109, v109\n\t" \
    "v_pk_mul_f32 v[116:117], v[92:93], v[92:93]\n\tv_rcp_f32 v110, v110\n\tv_pk_mul_f32 v[118:119], v[94:95], v[94:95]\n\tv_rcp_f32 v111, v111\n\tv_pk_fma_f32 v[112:113], v[112:113], v[246:247], v[246:247] op_sel:[0,0,1] op_sel_hi:[1,0,1]\n\tv_pk_mul_f32 v[104:105], v[104:105], v[80:81]\n\tbuffer_load_dwordx4 a[60:63], v8, s[8:11], s68 offen offset:1024 sc1\n\tv_pk_mul_f32 v[106:107], v[106:107], v[82:83]\n\t" \
    "v_pk_mul_f32 v[108:109], v[108:109], v[84:85]\n\tv_pk_mul_f32 v[110:111], v[110:111], v[86:87]\n\tv_cvt_pk_bf16_f32 v80, v104, v105\n\tv_cvt_pk_bf16_f32 v81, v106, v107\n\tv_cvt_pk_bf16_f32 v82, v108, v109\n\tv_cvt_pk_bf16_f32 v83, v110, v111\n\tv_pk_fma_f32 v[114:115], v[114:115], v[246:247], v[246:247] op_sel:[0,0,1] op_sel_hi:[1,0,1]\n\tv_pk_fma_f32 v[116:117], v[116:117], v[246:247], v[246:247] op_sel:[0,0,1] op_sel_hi:[1,0,1]\n\t" \
    "v_pk_fma_f32 v[118:119], v[118:119], v[246:247], v[246:247] op_sel:[0,0,1] op_sel_hi:[1,0,1]\n\tv_pk_mul_f32 v[112:113], v[112:113], v[88:89]\n\tv_pk_mul_f32 v[114:115], v[114:115], v[90:91]\n\tv_pk_mul_f32 v[116:117], v[116:117], v[92:93]\n\tv_pk_mul_f32 v[118:119], v[118:119], v[94:95]\n\tv_exp_f32 v112, v112\n\tbuffer_load_dwordx4 a[64:67], v8, s[8:11], s68 offen offset:2048 sc1\n\ts_add_u32 s68, s46, 12288\n\t" \
    "v_exp_f32 v113, v113\n\tv_exp_f32 v114, v114\n\tv_exp_f32 v115, v115\n\tv_exp_f32 v116, v116\n\tv_exp_f32 v117, v117\n\tv_exp_f32 v118, v118\n\tv_exp_f32 v119, v119\n\tv_pk_add_f32 v[112:113], v[112:113], 1.0 op_sel_hi:[1,0]\n\t" \
    "buffer_load_dwordx4 a[68:71], v8, s[8:11], s68 offen offset:0 sc1\n\tv_pk_add_f32 v[114:115], v[114:115], 1.0 op_sel_hi:[1,0]\n\tv_pk_add_f32 v[116:117], v[116:117], 1.0 op_sel_hi:[1,0]\n\tv_pk_add_f32 v[118:119], v[118:119], 1.0 op_sel_hi:[1,0]\n\tv_rcp_f32 v112, v112\n\tv_rcp_f32 v113, v113\n\tv_rcp_f32 v114, v114\n\tv_rcp_f32 v115, v115\n\t" \
    "v_rcp_f32 v116, v116\n\tv_rcp_f32 v117, v117\n\tv_rcp_f32 v118, v118\n\tbuffer_load_dwordx4 a[72:75], v8, s[8:11], s68 offen offset:1024 sc1\n\tv_rcp_f32 v119, v119\n\tv_pk_mul_f32 v[112:113], v[112:113], v[88:89]\n\tv_pk_mul_f32 v[114:115], v[114:115], v[90:91]\n\tv_pk_mul_f32 v[116:117], v[116:117], v[92:93]\n\t" \
    "v_pk_mul_f32 v[118:119], v[118:119], v[94:95]\n\tv_cvt_pk_bf16_f32 v88, v112, v113\n\tv_cvt_pk_bf16_f32 v89, v114, v115\n\tv_cvt_pk_bf16_f32 v90, v116, v117\n\tv_cvt_pk_bf16_f32 v91, v118, v119\n\tbuffer_load_dwordx4 a[76:79], v8, s[8:11], s68 offen offset:2048 sc1\n\ts_mov_b32 s69, 3145728\n\ts_add_u32 m0, s54, 53248\n\t" \
    "s_nop 0\n\tbuffer_load_dwordx4 v12, s[16:19], s69 offen lds\n\ts_mov_b32 s69, 3145728\n\ts_add_u32 m0, s54, 118784\n\ts_nop 0\n\tbuffer_load_dwordx4 v13, s[16:19], s69 offen lds\n\tbuffer_store_dwordx4 v[72:75], v8, s[8:11], s73 offen offset:0 sc1\n\tbuffer_store_dwordx4 v[80:83], v8, s[8:11], s73 offen offset:1024 sc1\n\t" \
    "buffer_store_dwordx4 v[88:91], v8, s[8:11], s73 offen offset:2048 sc1\n\ts_cmp_lg_u32 s39, 2\n\ts_cbranch_scc1 102f\n\t108:\n\ts_waitcnt vmcnt(15)\n\ts_barrier\n\tv_mfma_f32_16x16x32_bf16 v[120:123], v[192:195], v[16:19], 0\n\tds_read_b64_tr_b16 v[36:37], v225 offset:0\n\t" \
    "ds_read_b64_tr_b16 v[38:39], v225 offset:2048\n\ts_add_u32 s68, s46, 15360\n\tv_mfma_f32_16x16x32_bf16 v[128:131], v[196:199], v[16:19], 0\n\tv_mfma_f32_16x16x32_bf16 v[136:139], v[200:203], v[16:19], 0\n\tv_mfma_f32_16x16x32_bf16 v[124:127], v[192:195], v[20:23], 0\n\tds_read_b64_tr_b16 v[40:41], v226 offset:0\n\tds_read_b64_tr_b16 v[42:43], v226 offset:2048\n\tv_mfma_f32_16x16x32_bf16 v[132:135], v[196:199], v[20:23], 0\n\t" \
    "v_mfma_f32_16x16x32_bf16 v[140:143], v[200:203], v[20:23], 0\n\tv_mfma_f32_16x16x32_bf16 v[144:147], v[192:195], v[24:27], 0\n\tds_read_b64_tr_b16 v[44:45], v227 offset:0\n\tds_read_b64_tr_b16 v[46:47], v227 offset:2048\n\tv_mfma_f32_16x16x32_bf16 v[152:155], v[196:199], v[24:27], 0\n\tv_mfma_f32_16x16x32_bf16 v[160:163], v[200:203], v[24:27], 0\n\ts_waitcnt lgkmcnt(8)\n\tv_mfma_f32_16x16x32_bf16 v[148:151], v[192:195], v[28:31], 0\n\t" \
    "ds_read_b64_tr_b16 v[16:17], v4 offset:4096\n\tds_read_b64_tr_b16 v[18:19], v4 offset:6144\n\tv_mfma_f32_16x16x32_bf16 v[156:159], v[196:199], v[28:31], 0\n\tv_mfma_f32_16x16x32_bf16 v[164:167], v[200:203], v[28:31], 0\n\ts_waitcnt lgkmcnt(8)\n\tv_mfma_f32_16x16x32_bf16 v[168:171], v[192:195], v[32:35], 0\n\tds_read_b64_tr_b16 v[20:21], v5 offset:4096\n\tds_read_b64_tr_b16 v[22:23], v5 offset:6144\n\t" \
    "v_mfma_f32_16x16x32_bf16 v[176:179], v[196:199], v[32:35], 0\n\tv_mfma_f32_16x16x32_bf16 v[184:187], v[200:203], v[32:35], 0\n\ts_waitcnt lgkmcnt(8)\n\tv_mfma_f32_16x16x32_bf16 v[172:175], v[192:195], v[36:39], 0\n\tds_read_b64_tr_b16 v[24:25], v6 offset:4096\n\tds_read_b64_tr_b16 v[26:27], v6 offset:6144\n\tv_mfma_f32_16x16x32_bf16 v[180:183], v[196:199], v[36:39], 0\n\tv_mfma_f32_16x16x32_bf16 v[188:191], v[200:203], v[36:39], 0\n\t" \
    "s_waitcnt lgkmcnt(8)\n\tv_mfma_f32_16x16x32_bf16 v[48:51], v[192:195], v[40:43], v[228:231]\n\tds_read_b64_tr_b16 v[28:29], v7 offset:4096\n\tds_read_b64_tr_b16 v[30:31], v7 offset:6144\n\tv_mfma_f32_16x16x32_bf16 v[56:59], v[196:199], v[40:43], v[228:231]\n\tv_mfma_f32_16x16x32_bf16 v[64:67], v[200:203], v[40:43], v[228:231]\n\ts_waitcnt lgkmcnt(8)\n\tv_mfma_f32_16x16x32_bf16 v[52:55], v[192:195], v[44:47], v[232:235]\n\t" \
    "ds_read_b64_tr_b16 v[32:33], v224 offset:4096\n\tds_read_b64_tr_b16 v[34:35], v224 offset:6144\n\tv_mfma_f32_16x16x32_bf16 v[60:63], v[196:199], v[44:47], v[232:235]\n\tbuffer_load_dwordx4 v[72:75], v8, s[8:11], s68 offen offset:0 sc1\n\tv_mfma_f32_16x16x32_bf16 v[68:71], v[200:203], v[44:47], v[232:235]\n\ts_waitcnt vmcnt(13) lgkmcnt(8)\n\tv_mfma_f32_16x16x32_bf16 v[120:123], v[204:207], v[16:19], v[120:123]\n\tds_read_b64_tr_b16 v[36:37], v225 offset:4096\n\t" \
    "ds_read_b64_tr_b16 v[38:39], v225 offset:6144\n\tv_mfma_f32_16x16x32_bf16 v[128:131], v[208:211], v[16:19], v[128:131]\n\tv_mfma_f32_16x16x32_bf16 v[136:139], v[212:215], v[16:19], v[136:139]\n\ts_waitcnt lgkmcnt(8)\n\tv_mfma_f32_16x16x32_bf16 v[124:127], v[204:207], v[20:23], v[124:127]\n\tds_read_b64_tr_b16 v[40:41], v226 offset:4096\n\tds_read_b64_tr_b16 v[42:43], v226 offset:6144\n\tv_mfma_f32_16x16x32_bf16 v[132:135], v[208:211], v[20:23], v[132:135]\n\t" \
    "buffer_load_dwordx4 v[76:79], v8, s[8:11], s68 offen offset:1024 sc1\n\tv_mfma_f32_16x16x32_bf16 v[140:143], v[212:215], v[20:23], v[140:143]\n\ts_waitcnt lgkmcnt(8)\n\tv_mfma_f32_16x16x32_bf16 v[144:147], v[204:207], v[24:27], v[144:147]\n\tds_read_b64_tr_b16 v[44:45], v227 offset:4096\n\tds_read_b64_tr_b16 v[46:47], v227 offset:6144\n\tv_mfma_f32_16x16x32_bf16 v[152:155], v[208:211], v[24:27], v[152:155]\n\tbuffer_load_dwordx4 v[80:83], v8, s[8:11], s68 offen offset:2048 sc1\n\t" \
    "s_add_u32 s68, s46, 18432\n\tv_mfma_f32_16x16x32_bf16 v[160:163], v[212:215], v[24:27], v[160:163]\n\ts_waitcnt lgkmcnt(8)\n\tv_mfma_f32_16x16x32_bf16 v[148:151], v[204:207], v[28:31], v[148:151]\n\tds_read_b64_tr_b16 v[16:17], v4 offset:8192\n\tds_read_b64_tr_b16 v[18:19], v4 offset:10240\n\tv_mfma_f32_16x16x32_bf16 v[156:159], v[208:211], v[28:31], v[156:159]\n\tv_mfma_f32_16x16x32_bf16 v[164:167], v[212:215], v[28:31], v[164:167]\n\t" \
    "s_waitcnt lgkmcnt(8)\n\tv_mfma_f32_16x16x32_bf16 v[168:171], v[204:207], v[32:35], v[168:171]\n\tds_read_b64_tr_b16 v[20:21], v5 offset:8192\n\tds_read_b64_tr_b16 v[22:23], v5 offset:10240\n\tv_mfma_f32_16x16x32_bf16 v[176:179], v[208:211], v[32:35], v[176:179]\n\tbuffer_load_dwordx4 v[192:195], v8, s[8:11], s68 offen offset:0 sc1\n\tv_mfma_f32_16x16x32_bf16 v[184:187], v[212:215], v[32:35], v[184:187]\n\ts_waitcnt lgkmcnt(8)\n\t" \
    "v_mfma_f32_16x16x32_bf16 v[172:175], v[204:207], v[36:39], v[172:175]\n\tds_read_b64_tr_b16 v[24:25], v6 offset:8192\n\tds_read_b64_tr_b16 v[26:27], v6 offset:10240\n\tv_mfma_f32_16x16x32_bf16 v[180:183], v[208:211], v[36:39], v[180:183]\n\tbuffer_load_dwordx4 v[196:199], v8, s[8:11], s68 offen offset:1024 sc1\n\tv_mfma_f32_16x16x32_bf16 v[188:191], v[212:215], v[36:39], v[188:191]\n\ts_waitcnt lgkmcnt(8)\n\tv_mfma_f32_16x16x32_bf16 v[48:51], v[204:207], v[40:43], v[48:51]\n\t" \
    "ds_read_b64_tr_b16 v[28:29], v7 offset:8192\n\tds_read_b64_tr_b16 v[30:31], v7 offset:10240\n\tv_mfma_f32_16x16x32_bf16 v[56:59], v[208:211], v[40:43], v[56:59]\n\tv_mfma_f32_16x16x32_bf16 v[64:67], v[212:215], v[40:43], v[64:67]\n\ts_waitcnt lgkmcnt(8)\n\tv_mfma_f32_16x16x32_bf16 v[52:55], v[204:207], v[44:47], v[52:55]\n\tds_read_b64_tr_b16 v[32:33], v224 offset:8192\n\tds_read_b64_tr_b16 v[34:35], v224 offset:10240\n\t" \
    "v_mfma_f32_16x16x32_bf16 v[60:63], v[208:211], v[44:47], v[60:63]\n\tbuffer_load_dwordx4 v[200:203], v8, s[8:11], s68 offen offset:2048 sc1\n\ts_add_u32 s68, s46, 21504\n\tv_mfma_f32_16x16x32_bf16 v[68:71], v[212:215], v[44:47], v[68:71]\n\ts_barrier\n\ts_waitcnt vmcnt(15) lgkmcnt(8)\n\tv_mfma_f32_16x16x32_bf16 v[120:123], v[216:219], v[16:19], v[120:123]\n\tds_read_b64_tr_b16 v[36:37], v225 offset:8192\n\t" \
    "ds_read_b64_tr_b16 v[38:39], v225 offset:10240\n\tv_mfma_f32_16x16x32_bf16 v[128:131], a[48:51], v[16:19], v[128:131]\n\tbuffer_load_dwordx4 v[84:87], v8, s[8:11], s68 offen offset:0 sc1\n\tv_mfma_f32_16x16x32_bf16 v[136:139], a[52:55], v[16:19], v[136:139]\n\ts_waitcnt lgkmcnt(8)\n\tv_mfma_f32_16x16x32_bf16 v[124:127], v[216:219], v[20:23], v[124:127]\n\tds_read_b64_tr_b16 v[40:41], v226 offset:8192\n\tds_read_b64_tr_b16 v[42:43], v226 offset:10240\n\t" \
    "v_mfma_f32_16x16x32_bf16 v[132:135], a[48:51], v[20:23], v[132:135]\n\tbuffer_load_dwordx4 v[88:91], v8, s[8:11], s68 offen offset:1024 sc1\n\tv_mfma_f32_16x16x32_bf16 v[140:143], a[52:55], v[20:23], v[140:143]\n\ts_waitcnt lgkmcnt(8)\n\tv_mfma_f32_16x16x32_bf16 v[144:147], v[216:219], v[24:27], v[144:147]\n\tds_read_b64_tr_b16 v[44:45], v227 offset:8192\n\tds_read_b64_tr_b16 v[46:47], v227 offset:10240\n\tv_mfma_f32_16x16x32_bf16 v[152:155], a[48:51], v[24:27], v[152:155]\n\t" \
    "buffer_load_dwordx4 v[92:95], v8, s[8:11], s68 offen offset:2048 sc1\n\ts_add_u32 s68, s46, 24576\n\tv_mfma_f32_16x16x32_bf16 v[160:163], a[52:55], v[24:27], v[160:163]\n\ts_waitcnt lgkmcnt(8)\n\tv_mfma_f32_16x16x32_bf16 v[148:151], v[216:219], v[28:31], v[148:151]\n\tds_read_b64_tr_b16 v[16:17], v4 offset:12288\n\tds_read_b64_tr_b16 v[18:19], v4 offset:14336\n\tv_mfma_f32_16x16x32_bf16 v[156:159], a[48:51], v[28:31], v[156:159]\n\t" \
    "buffer_load_dwordx4 v[204:207], v8, s[8:11], s68 offen offset:0 sc1\n\tv_mfma_f32_16x16x32_bf16 v[164:167], a[52:55], v[28:31], v[164:167]\n\ts_waitcnt lgkmcnt(8)\n\tv_mfma_f32_16x16x32_bf16 v[168:171], v[216:219], v[32:35], v[168:171]\n\tds_read_b64_tr_b16 v[20:21], v5 offset:12288\n\tds_read_b64_tr_b16 v[22:23], v5 offset:14336\n\tv_mfma_f32_16x16x32_bf16 v[176:179], a[48:51], v[32:35], v[176:179]\n\tv_mfma_f32_16x16x32_bf16 v[184:187], a[52:55], v[32:35], v[184:187]\n\t" \
    "s_waitcnt lgkmcnt(8)\n\tv_mfma_f32_16x16x32_bf16 v[172:175], v[216:219], v[36:39], v[172:175]\n\tds_read_b64_tr_b16 v[24:25], v6 offset:12288\n\tds_read_b64_tr_b16 v[26:27], v6 offset:14336\n\tv_mfma_f32_16x16x32_bf16 v[180:183], a[48:51], v[36:39], v[180:183]\n\tbuffer_load_dwordx4 v[208:211], v8, s[8:11], s68 offen offset:1024 sc1\n\tv_mfma_f32_16x16x32_bf16 v[188:191], a[52:55], v[36:39], v[188:191]\n\ts_waitcnt lgkmcnt(8)\n\t" \
    "v_mfma_f32_16x16x32_bf16 v[48:51], v[216:219], v[40:43], v[48:51]\n\tds_read_b64_tr_b16 v[28:29], v7 offset:12288\n\tds_read_b64_tr_b16 v[30:31], v7 offset:14336\n\tv_mfma_f32_16x16x32_bf16 v[56:59], a[48:51], v[40:43], v[56:59]\n\tbuffer_load_dwordx4 v[212:215], v8, s[8:11], s68 offen offset:2048 sc1\n\ts_add_u32 s68, s46, 27648\n\tv_mfma_f32_16x16x32_bf16 v[64:67], a[52:55], v[40:43], v[64:67]\n\ts_waitcnt lgkmcnt(8)\n\t" \
    "v_mfma_f32_16x16x32_bf16 v[52:55], v[216:219], v[44:47], v[52:55]\n\tds_read_b64_tr_b16 v[32:33], v224 offset:12288\n\tds_read_b64_tr_b16 v[34:35], v224 offset:14336\n\tv_mfma_f32_16x16x32_bf16 v[60:63], a[48:51], v[44:47], v[60:63]\n\tv_mfma_f32_16x16x32_bf16 v[68:71], a[52:55], v[44:47], v[68:71]\n\ts_waitcnt vmcnt(18) lgkmcnt(8)\n\tv_mfma_f32_16x16x32_bf16 v[120:123], a[56:59], v[16:19], v[120:123]\n\tds_read_b64_tr_b16 v[36:37], v225 offset:12288\n\t" \
    "ds_read_b64_tr_b16 v[38:39], v225 offset:14336\n\tv_mfma_f32_16x16x32_bf16 v[128:131], a[60:63], v[16:19], v[128:131]\n\tbuffer_load_dwordx4 v[96:99], v8, s[8:11], s68 offen offset:0 sc1\n\tv_mfma_f32_16x16x32_bf16 v[136:139], a[64:67], v[16:19], v[136:139]\n\ts_waitcnt lgkmcnt(8)\n\tv_mfma_f32_16x16x32_bf16 v[124:127], a[56:59], v[20:23], v[124:127]\n\tds_read_b64_tr_b16 v[40:41], v226 offset:12288\n\tds_read_b64_tr_b16 v[42:43], v226 offset:14336\n\t" \
    "v_mfma_f32_16x16x32_bf16 v[132:135], a[60:63], v[20:23], v[132:135]\n\tbuffer_load_dwordx4 v[100:103], v8, s[8:11], s68 offen offset:1024 sc1\n\tv_mfma_f32_16x16x32_bf16 v[140:143], a[64:67], v[20:23], v[140:143]\n\ts_waitcnt lgkmcnt(8)\n\tv_mfma_f32_16x16x32_bf16 v[144:147], a[56:59], v[24:27], v[144:147]\n\tds_read_b64_tr_b16 v[44:45], v227 offset:12288\n\tds_read_b64_tr_b16 v[46:47], v227 offset:14336\n\tv_mfma_f32_16x16x32_bf16 v[152:155], a[60:63], v[24:27], v[152:155]\n\t" \
    "v_mfma_f32_16x16x32_bf16 v[160:163], a[64:67], v[24:27], v[160:163]\n\ts_waitcnt lgkmcnt(8)\n\tv_mfma_f32_16x16x32_bf16 v[148:151], a[56:59], v[28:31], v[148:151]\n\tds_read_b64_tr_b16 v[16:17], v4 offset:16384\n\tds_read_b64_tr_b16 v[18:19], v4 offset:18432\n\tv_mfma_f32_16x16x32_bf16 v[156:159], a[60:63], v[28:31], v[156:159]\n\tbuffer_load_dwordx4 v[104:107], v8, s[8:11], s68 offen offset:2048 sc1\n\ts_add_u32 s68, s46, 30720\n\t" \
    "v_mfma_f32_16x16x32_bf16 v[164:167], a[64:67], v[28:31], v[164:167]\n\ts_waitcnt vmcnt(15)\n\ts_add_u32 s73, s49, 4096\n\tbuffer_store_dwordx2 v[248:249], off, s[8:11], s73 sc1\n\ts_waitcnt lgkmcnt(8)\n\tv_mfma_f32_16x16x32_bf16 v[168:171], a[56:59], v[32:35], v[168:171]\n\tds_read_b64_tr_b16 v[20:21], v5 offset:16384\n\tds_read_b64_tr_b16 v[22:23], v5 offset:18432\n\t" \
    "v_mfma_f32_16x16x32_bf16 v[176:179], a[60:63], v[32:35], v[176:179]\n\tv_mfma_f32_16x16x32_bf16 v[184:187], a[64:67], v[32:35], v[184:187]\n\ts_waitcnt lgkmcnt(8)\n\tv_mfma_f32_16x16x32_bf16 v[172:175], a[56:59], v[36:39], v[172:175]\n\tds_read_b64_tr_b16 v[24:25], v6 offset:16384\n\tds_read_b64_tr_b16 v[26:27], v6 offset:18432\n\tv_mfma_f32_16x16x32_bf16 v[180:183], a[60:63], v[36:39], v[180:183]\n\tv_mfma_f32_16x16x32_bf16 v[188:191], a[64:67], v[36:39], v[188:191]\n\t" \
    "s_waitcnt lgkmcnt(8)\n\tv_mfma_f32_16x16x32_bf16 v[48:51], a[56:59], v[40:43], v[48:51]\n\tds_read_b64_tr_b16 v[28:29], v7 offset:16384\n\tds_read_b64_tr_b16 v[30:31], v7 offset:18432\n\tv_mfma_f32_16x16x32_bf16 v[56:59], a[60:63], v[40:43], v[56:59]\n\tbuffer_load_dwordx4 v[216:219], v8, s[8:11], s68 offen offset:0 sc1\n\tv_mfma_f32_16x16x32_bf16 v[64:67], a[64:67], v[40:43], v[64:67]\n\ts_waitcnt lgkmcnt(8)\n\t" \
    "v_mfma_f32_16x16x32_bf16 v[52:55], a[56:59], v[44:47], v[52:55]\n\tds_read_b64_tr_b16 v[32:33], v224 offset:16384\n\tds_read_b64_tr_b16 v[34:35], v224 offset:18432\n\tv_mfma_f32_16x16x32_bf16 v[60:63], a[60:63], v[44:47], v[60:63]\n\tbuffer_load_dwordx4 v[108:111], v8, s[8:11], s68 offen offset:1024 sc1\n\tv_mfma_f32_16x16x32_bf16 v[68:71], a[64:67], v[44:47], v[68:71]\n\ts_barrier\n\ts_waitcnt lgkmcnt(8)\n\t" \
    "v_mfma_f32_16x16x32_bf16 v[120:123], a[68:71], v[16:19], v[120:123]\n\tds_read_b64_tr_b16 v[36:37], v225 offset:16384\n\tds_read_b64_tr_b16 v[38:39], v225 offset:18432\n\tv_mfma_f32_16x16x32_bf16 v[128:131], a[72:75], v[16:19], v[128:131]\n\tbuffer_load_dwordx4 v[112:115], v8, s[8:11], s68 offen offset:2048 sc1\n\ts_add_u32 s68, s46, 33792\n\tv_mfma_f32_16x16x32_bf16 v[136:139], a[76:79], v[16:19], v[136:139]\n\ts_waitcnt lgkmcnt(8)\n\t" \
    "v_mfma_f32_16x16x32_bf16 v[124:127], a[68:71], v[20:23], v[124:127]\n\tds_read_b64_tr_b16 v[40:41], v226 offset:16384\n\tds_read_b64_tr_b16 v[42:43], v226 offset:18432\n\tv_mfma_f32_16x16x32_bf16 v[132:135], a[72:75], v[20:23], v[132:135]\n\tv_mfma_f32_16x16x32_bf16 v[140:143], a[76:79], v[20:23], v[140:143]\n\ts_waitcnt lgkmcnt(8)\n\tv_mfma_f32_16x16x32_bf16 v[144:147], a[68:71], v[24:27], v[144:147]\n\tds_read_b64_tr_b16 v[44:45], v227 offset:16384\n\t" \
    "ds_read_b64_tr_b16 v[46:47], v227 offset:18432\n\tv_mfma_f32_16x16x32_bf16 v[152:155], a[72:75], v[24:27], v[152:155]\n\tbuffer_load_dwordx4 v[116:119], v8, s[8:11], s68 offen offset:0 sc1\n\tv_mfma_f32_16x16x32_bf16 v[160:163], a[76:79], v[24:27], v[160:163]\n\ts_waitcnt lgkmcnt(8)\n\tv_mfma_f32_16x16x32_bf16 v[148:151], a[68:71], v[28:31], v[148:151]\n\tds_read_b64_tr_b16 v[16:17], v4 offset:20480\n\tds_read_b64_tr_b16 v[18:19], v4 offset:22528\n\t" \
    "v_mfma_f32_16x16x32_bf16 v[156:159], a[72:75], v[28:31], v[156:159]\n\tbuffer_load_dwordx4 a[0:3], v8, s[8:11], s68 offen offset:1024 sc1\n\tv_mfma_f32_16x16x32_bf16 v[164:167], a[76:79], v[28:31], v[164:167]\n\ts_waitcnt lgkmcnt(8)\n\tv_mfma_f32_16x16x32_bf16 v[168:171], a[68:71], v[32:35], v[168:171]\n\tds_read_b64_tr_b16 v[20:21], v5 offset:20480\n\tds_read_b64_tr_b16 v[22:23], v5 offset:22528\n\tv_mfma_f32_16x16x32_bf16 v[176:179], a[72:75], v[32:35], v[176:179]\n\t" \
    "v_mfma_f32_16x16x32_bf16 v[184:187], a[76:79], v[32:35], v[184:187]\n\ts_waitcnt lgkmcnt(8)\n\tv_mfma_f32_16x16x32_bf16 v[172:175], a[68:71], v[36:39], v[172:175]\n\tds_read_b64_tr_b16 v[24:25], v6 offset:20480\n\tds_read_b64_tr_b16 v[26:27], v6 offset:22528\n\tv_mfma_f32_16x16x32_bf16 v[180:183], a[72:75], v[36:39], v[180:183]\n\tbuffer_load_dwordx4 a[4:7], v8, s[8:11], s68 offen offset:2048 sc1\n\ts_mov_b32 s69, 3801088\n\t" \
    "s_add_u32 m0, s54, 57344\n\tv_mfma_f32_16x16x32_bf16 v[188:191], a[76:79], v[36:39], v[188:191]\n\ts_waitcnt lgkmcnt(8)\n\tv_mfma_f32_16x16x32_bf16 v[48:51], a[68:71], v[40:43], v[48:51]\n\tds_read_b64_tr_b16 v[28:29], v7 offset:20480\n\tds_read_b64_tr_b16 v[30:31], v7 offset:22528\n\tv_mfma_f32_16x16x32_bf16 v[56:59], a[72:75], v[40:43], v[56:59]\n\tbuffer_load_dwordx4 v12, s[16:19], s69 offen lds\n\t" \
    "s_mov_b32 s69, 3801088\n\ts_add_u32 m0, s54, 122880\n\tv_mfma_f32_16x16x32_bf16 v[64:67], a[76:79], v[40:43], v[64:67]\n\ts_waitcnt lgkmcnt(8)\n\tv_mfma_f32_16x16x32_bf16 v[52:55], a[68:71], v[44:47], v[52:55]\n\tds_read_b64_tr_b16 v[32:33], v224 offset:20480\n\tds_read_b64_tr_b16 v[34:35], v224 offset:22528\n\tv_mfma_f32_16x16x32_bf16 v[60:63], a[72:75], v[44:47], v[60:63]\n\t" \
    "v_mfma_f32_16x16x32_bf16 v[68:71], a[76:79], v[44:47], v[68:71]\n\ts_waitcnt vmcnt(20) lgkmcnt(8)\n\tv_mfma_f32_16x16x32_bf16 v[120:123], v[72:75], v[16:19], v[120:123]\n\tds_read_b64_tr_b16 v[36:37], v225 offset:20480\n\tds_read_b64_tr_b16 v[38:39], v225 offset:22528\n\tv_mfma_f32_16x16x32_bf16 v[128:131], v[76:79], v[16:19], v[128:131]\n\tbuffer_load_dwordx4 v13, s[16:19], s69 offen lds\n\ts_add_u32 s68, s46, 36864\n\t" \
    "v_mfma_f32_16x16x32_bf16 v[136:139], v[80:83], v[16:19], v[136:139]\n\ts_waitcnt lgkmcnt(8)\n\tv_mfma_f32_16x16x32_bf16 v[124:127], v[72:75], v[20:23], v[124:127]\n\tds_read_b64_tr_b16 v[40:41], v226 offset:20480\n\tds_read_b64_tr_b16 v[42:43], v226 offset:22528\n\tv_mfma_f32_16x16x32_bf16 v[132:135], v[76:79], v[20:23], v[132:135]\n\tbuffer_load_dwordx4 a[8:11], v8, s[8:11], s68 offen offset:0 sc1\n\tv_mfma_f32_16x16x32_bf16 v[140:143], v[80:83], v[20:23], v[140:143]\n\t" \
    "s_waitcnt lgkmcnt(8)\n\tv_mfma_f32_16x16x32_bf16 v[144:147], v[72:75], v[24:27], v[144:147]\n\tds_read_b64_tr_b16 v[44:45], v227 offset:20480\n\tds_read_b64_tr_b16 v[46:47], v227 offset:22528\n\tv_mfma_f32_16x16x32_bf16 v[152:155], v[76:79], v[24:27], v[152:155]\n\tv_mfma_f32_16x16x32_bf16 v[160:163], v[80:83], v[24:27], v[160:163]\n\ts_waitcnt lgkmcnt(8)\n\tv_mfma_f32_16x16x32_bf16 v[148:151], v[72:75], v[28:31], v[148:151]\n\t" \
    "ds_read_b64_tr_b16 v[16:17], v4 offset:24576\n\tds_read_b64_tr_b16 v[18:19], v4 offset:26624\n\tv_mfma_f32_16x16x32_bf16 v[156:159], v[76:79], v[28:31], v[156:159]\n\tbuffer_load_dwordx4 a[12:15], v8, s[8:11], s68 offen offset:1024 sc1\n\tv_mfma_f32_16x16x32_bf16 v[164:167], v[80:83], v[28:31], v[164:167]\n\ts_waitcnt lgkmcnt(8)\n\tv_mfma_f32_16x16x32_bf16 v[168:171], v[72:75], v[32:35], v[168:171]\n\tds_read_b64_tr_b16 v[20:21], v5 offset:24576\n\t" \
    "ds_read_b64_tr_b16 v[22:23], v5 offset:26624\n\tv_mfma_f32_16x16x32_bf16 v[176:179], v[76:79], v[32:35], v[176:179]\n\tbuffer_load_dwordx4 a[16:19], v8, s[8:11], s68 offen offset:2048 sc1\n\ts_add_u32 s68, s46, 39936\n\tv_mfma_f32_16x16x32_bf16 v[184:187], v[80:83], v[32:35], v[184:187]\n\ts_waitcnt lgkmcnt(8)\n\tv_mfma_f32_16x16x32_bf16 v[172:175], v[72:75], v[36:39], v[172:175]\n\tds_read_b64_tr_b16 v[24:25], v6 offset:24576\n\t" \
    "ds_read_b64_tr_b16 v[26:27], v6 offset:26624\n\tv_mfma_f32_16x16x32_bf16 v[180:183], v[76:79], v[36:39], v[180:183]\n\tbuffer_load_dwordx4 a[20:23], v8, s[8:11], s68 offen offset:0 sc1\n\tv_mfma_f32_16x16x32_bf16 v[188:191], v[80:83], v[36:39], v[188:191]\n\ts_waitcnt lgkmcnt(8)\n\tv_mfma_f32_16x16x32_bf16 v[48:51], v[72:75], v[40:43], v[48:51]\n\tds_read_b64_tr_b16 v[28:29], v7 offset:24576\n\tds_read_b64_tr_b16 v[30:31], v7 offset:26624\n\t" \
    "v_mfma_f32_16x16x32_bf16 v[56:59], v[76:79], v[40:43], v[56:59]\n\tbuffer_load_dwordx4 a[24:27], v8, s[8:11], s68 offen offset:1024 sc1\n\tv_mfma_f32_16x16x32_bf16 v[64:67], v[80:83], v[40:43], v[64:67]\n\ts_waitcnt lgkmcnt(8)\n\tv_mfma_f32_16x16x32_bf16 v[52:55], v[72:75], v[44:47], v[52:55]\n\tds_read_b64_tr_b16 v[32:33], v224 offset:24576\n\tds_read_b64_tr_b16 v[34:35], v224 offset:26624\n\tv_mfma_f32_16x16x32_bf16 v[60:63], v[76:79], v[44:47], v[60:63]\n\t" \
    "v_mfma_f32_16x16x32_bf16 v[68:71], v[80:83], v[44:47], v[68:71]\n\ts_barrier\n\ts_waitcnt vmcnt(23) lgkmcnt(8)\n\tv_mfma_f32_16x16x32_bf16 v[120:123], v[192:195], v[16:19], v[120:123]\n\tds_read_b64_tr_b16 v[36:37], v225 offset:24576\n\tds_read_b64_tr_b16 v[38:39], v225 offset:26624\n\tv_mfma_f32_16x16x32_bf16 v[128:131], v[196:199], v[16:19], v[128:131]\n\tbuffer_load_dwordx4 a[28:31], v8, s[8:11], s68 offen offset:2048 sc1\n\t" \
    "s_mov_b32 s69, 3670016\n\ts_add_u32 m0, s54, 61440\n\tv_mfma_f32_16x16x32_bf16 v[136:139], v[200:203], v[16:19], v[136:139]\n\ts_waitcnt lgkmcnt(8)\n\tv_mfma_f32_16x16x32_bf16 v[124:127], v[192:195], v[20:23], v[124:127]\n\tds_read_b64_tr_b16 v[40:41], v226 offset:24576\n\tds_read_b64_tr_b16 v[42:43], v226 offset:26624\n\tv_mfma_f32_16x16x32_bf16 v[132:135], v[196:199], v[20:23], v[132:135]\n\t" \
    "buffer_load_dwordx4 v12, s[16:19], s69 offen lds\n\ts_mov_b32 s69, 3670016\n\ts_add_u32 m0, s54, 126976\n\tv_mfma_f32_16x16x32_bf16 v[140:143], v[200:203], v[20:23], v[140:143]\n\ts_waitcnt lgkmcnt(8)\n\tv_mfma_f32_16x16x32_bf16 v[144:147], v[192:195], v[24:27], v[144:147]\n\tds_read_b64_tr_b16 v[44:45], v227 offset:24576\n\tds_read_b64_tr_b16 v[46:47], v227 offset:26624\n\t" \
    "v_mfma_f32_16x16x32_bf16 v[152:155], v[196:199], v[24:27], v[152:155]\n\tbuffer_load_dwordx4 v13, s[16:19], s69 offen lds\n\ts_mov_b32 s69, 4325376\n\ts_add_u32 m0, s54, 0\n\tv_mfma_f32_16x16x32_bf16 v[160:163], v[200:203], v[24:27], v[160:163]\n\ts_waitcnt lgkmcnt(8)\n\tv_mfma_f32_16x16x32_bf16 v[148:151], v[192:195], v[28:31], v[148:151]\n\tds_read_b64_tr_b16 v[16:17], v4 offset:28672\n\t" \
    "ds_read_b64_tr_b16 v[18:19], v4 offset:30720\n\tv_mfma_f32_16x16x32_bf16 v[156:159], v[196:199], v[28:31], v[156:159]\n\tv_mfma_f32_16x16x32_bf16 v[164:167], v[200:203], v[28:31], v[164:167]\n\ts_waitcnt lgkmcnt(8)\n\tv_mfma_f32_16x16x32_bf16 v[168:171], v[192:195], v[32:35], v[168:171]\n\tds_read_b64_tr_b16 v[20:21], v5 offset:28672\n\tds_read_b64_tr_b16 v[22:23], v5 offset:30720\n\tv_mfma_f32_16x16x32_bf16 v[176:179], v[196:199], v[32:35], v[176:179]\n\t" \
    "buffer_load_dwordx4 v12, s[16:19], s69 offen lds\n\ts_mov_b32 s69, 4325376\n\ts_add_u32 m0, s54, 65536\n\tv_mfma_f32_16x16x32_bf16 v[184:187], v[200:203], v[32:35], v[184:187]\n\ts_waitcnt lgkmcnt(8)\n\tv_mfma_f32_16x16x32_bf16 v[172:175], v[192:195], v[36:39], v[172:175]\n\tds_read_b64_tr_b16 v[24:25], v6 offset:28672\n\tds_read_b64_tr_b16 v[26:27], v6 offset:30720\n\t" \
    "v_mfma_f32_16x16x32_bf16 v[180:183], v[196:199], v[36:39], v[180:183]\n\tbuffer_load_dwordx4 v13, s[16:19], s69 offen lds\n\ts_mov_b32 s69, 4194304\n\ts_add_u32 m0, s54, 4096\n\tv_mfma_f32_16x16x32_bf16 v[188:191], v[200:203], v[36:39], v[188:191]\n\ts_waitcnt lgkmcnt(8)\n\tv_mfma_f32_16x16x32_bf16 v[48:51], v[192:195], v[40:43], v[48:51]\n\tds_read_b64_tr_b16 v[28:29], v7 offset:28672\n\t" \
    "ds_read_b64_tr_b16 v[30:31], v7 offset:30720\n\tv_mfma_f32_16x16x32_bf16 v[56:59], v[196:199], v[40:43], v[56:59]\n\tv_mfma_f32_16x16x32_bf16 v[64:67], v[200:203], v[40:43], v[64:67]\n\ts_waitcnt lgkmcnt(8)\n\tv_mfma_f32_16x16x32_bf16 v[52:55], v[192:195], v[44:47], v[52:55]\n\tds_read_b64_tr_b16 v[32:33], v224 offset:28672\n\tds_read_b64_tr_b16 v[34:35], v224 offset:30720\n\tv_mfma_f32_16x16x32_bf16 v[60:63], v[196:199], v[44:47], v[60:63]\n\t" \
    "buffer_load_dwordx4 v12, s[16:19], s69 offen lds\n\ts_add_u32 s68, s46, 43008\n\tv_mfma_f32_16x16x32_bf16 v[68:71], v[200:203], v[44:47], v[68:71]\n\ts_waitcnt vmcnt(26) lgkmcnt(8)\n\tv_mfma_f32_16x16x32_bf16 v[120:123], v[84:87], v[16:19], v[120:123]\n\tds_read_b64_tr_b16 v[36:37], v225 offset:28672\n\tds_read_b64_tr_b16 v[38:39], v225 offset:30720\n\tv_mfma_f32_16x16x32_bf16 v[128:131], v[88:91], v[16:19], v[128:131]\n\t" \
    "buffer_load_dwordx4 v[72:75], v8, s[8:11], s68 offen offset:0 sc1\n\tv_mfma_f32_16x16x32_bf16 v[136:139], v[92:95], v[16:19], v[136:139]\n\ts_waitcnt lgkmcnt(8)\n\tv_mfma_f32_16x16x32_bf16 v[124:127], v[84:87], v[20:23], v[124:127]\n\tds_read_b64_tr_b16 v[40:41], v226 offset:28672\n\tds_read_b64_tr_b16 v[42:43], v226 offset:30720\n\tv_mfma_f32_16x16x32_bf16 v[132:135], v[88:91], v[20:23], v[132:135]\n\tv_mfma_f32_16x16x32_bf16 v[140:143], v[92:95], v[20:23], v[140:143]\n\t" \
    "s_waitcnt lgkmcnt(8)\n\tv_mfma_f32_16x16x32_bf16 v[144:147], v[84:87], v[24:27], v[144:147]\n\tds_read_b64_tr_b16 v[44:45], v227 offset:28672\n\tds_read_b64_tr_b16 v[46:47], v227 offset:30720\n\tv_mfma_f32_16x16x32_bf16 v[152:155], v[88:91], v[24:27], v[152:155]\n\tbuffer_load_dwordx4 v[76:79], v8, s[8:11], s68 offen offset:1024 sc1\n\tv_mfma_f32_16x16x32_bf16 v[160:163], v[92:95], v[24:27], v[160:163]\n\ts_waitcnt lgkmcnt(8)\n\t" \
    "v_mfma_f32_16x16x32_bf16 v[148:151], v[84:87], v[28:31], v[148:151]\n\tds_read_b64_tr_b16 v[16:17], v4 offset:32768\n\tds_read_b64_tr_b16 v[18:19], v4 offset:34816\n\tv_mfma_f32_16x16x32_bf16 v[156:159], v[88:91], v[28:31], v[156:159]\n\tbuffer_load_dwordx4 v[80:83], v8, s[8:11], s68 offen offset:2048 sc1\n\ts_add_u32 s68, s46, 46080\n\tv_mfma_f32_16x16x32_bf16 v[164:167], v[92:95], v[28:31], v[164:167]\n\ts_waitcnt lgkmcnt(8)\n\t" \
    "v_mfma_f32_16x16x32_bf16 v[168:171], v[84:87], v[32:35], v[168:171]\n\tds_read_b64_tr_b16 v[20:21], v5 offset:32768\n\tds_read_b64_tr_b16 v[22:23], v5 offset:34816\n\tv_mfma_f32_16x16x32_bf16 v[176:179], v[88:91], v[32:35], v[176:179]\n\tv_mfma_f32_16x16x32_bf16 v[184:187], v[92:95], v[32:35], v[184:187]\n\ts_waitcnt lgkmcnt(8)\n\tv_mfma_f32_16x16x32_bf16 v[172:175], v[84:87], v[36:39], v[172:175]\n\tds_read_b64_tr_b16 v[24:25], v6 offset:32768\n\t" \
    "ds_read_b64_tr_b16 v[26:27], v6 offset:34816\n\tv_mfma_f32_16x16x32_bf16 v[180:183], v[88:91], v[36:39], v[180:183]\n\tbuffer_load_dwordx4 a[32:35], v8, s[8:11], s68 offen offset:0 sc1\n\tv_mfma_f32_16x16x32_bf16 v[188:191], v[92:95], v[36:39], v[188:191]\n\ts_waitcnt lgkmcnt(8)\n\tv_mfma_f32_16x16x32_bf16 v[48:51], v[84:87], v[40:43], v[48:51]\n\tds_read_b64_tr_b16 v[28:29], v7 offset:32768\n\tds_read_b64_tr_b16 v[30:31], v7 offset:34816\n\t" \
    "v_mfma_f32_16x16x32_bf16 v[56:59], v[88:91], v[40:43], v[56:59]\n\tbuffer_load_dwordx4 a[36:39], v8, s[8:11], s68 offen offset:1024 sc1\n\tv_mfma_f32_16x16x32_bf16 v[64:67], v[92:95], v[40:43], v[64:67]\n\ts_waitcnt lgkmcnt(8)\n\tv_mfma_f32_16x16x32_bf16 v[52:55], v[84:87], v[44:47], v[52:55]\n\tds_read_b64_tr_b16 v[32:33], v224 offset:32768\n\tds_read_b64_tr_b16 v[34:35], v224 offset:34816\n\tv_mfma_f32_16x16x32_bf16 v[60:63], v[88:91], v[44:47], v[60:63]\n\t" \
    "v_mfma_f32_16x16x32_bf16 v[68:71], v[92:95], v[44:47], v[68:71]\n\ts_barrier\n\ts_waitcnt vmcnt(28) lgkmcnt(8)\n\tv_mfma_f32_16x16x32_bf16 v[120:123], v[204:207], v[16:19], v[120:123]\n\tds_read_b64_tr_b16 v[36:37], v225 offset:32768\n\tds_read_b64_tr_b16 v[38:39], v225 offset:34816\n\tv_mfma_f32_16x16x32_bf16 v[128:131], v[208:211], v[16:19], v[128:131]\n\tbuffer_load_dwordx4 a[40:43], v8, s[8:11], s68 offen offset:2048 sc1\n\t" \
    "s_add_u32 s68, s46, 49152\n\tv_mfma_f32_16x16x32_bf16 v[136:139], v[212:215], v[16:19], v[136:139]\n\ts_waitcnt lgkmcnt(8)\n\tv_mfma_f32_16x16x32_bf16 v[124:127], v[204:207], v[20:23], v[124:127]\n\tds_read_b64_tr_b16 v[40:41], v226 offset:32768\n\tds_read_b64_tr_b16 v[42:43], v226 offset:34816\n\tv_mfma_f32_16x16x32_bf16 v[132:135], v[208:211], v[20:23], v[132:135]\n\tbuffer_load_dwordx4 v[84:87], v8, s[8:11], s68 offen offset:0 sc1\n\t" \
    "v_mfma_f32_16x16x32_bf16 v[140:143], v[212:215], v[20:23], v[140:143]\n\ts_add_u32 s50, s50, 4096\n\tbuffer_load_dwordx2 v[250:251], v241, s[8:11], s50 offen sc1\n\ts_waitcnt lgkmcnt(8)\n\tv_mfma_f32_16x16x32_bf16 v[144:147], v[204:207], v[24:27], v[144:147]\n\tds_read_b64_tr_b16 v[44:45], v227 offset:32768\n\tds_read_b64_tr_b16 v[46:47], v227 offset:34816\n\tv_mfma_f32_16x16x32_bf16 v[152:155], v[208:211], v[24:27], v[152:155]\n\t" \
    "v_mfma_f32_16x16x32_bf16 v[160:163], v[212:215], v[24:27], v[160:163]\n\ts_waitcnt lgkmcnt(8)\n\tv_mfma_f32_16x16x32_bf16 v[148:151], v[204:207], v[28:31], v[148:151]\n\tds_read_b64_tr_b16 v[16:17], v4 offset:36864\n\tds_read_b64_tr_b16 v[18:19], v4 offset:38912\n\tv_mfma_f32_16x16x32_bf16 v[156:159], v[208:211], v[28:31], v[156:159]\n\tv_mfma_f32_16x16x32_bf16 v[164:167], v[212:215], v[28:31], v[164:167]\n\ts_waitcnt lgkmcnt(8)\n\t" \
    "v_mfma_f32_16x16x32_bf16 v[168:171], v[204:207], v[32:35], v[168:171]\n\tds_read_b64_tr_b16 v[20:21], v5 offset:36864\n\tds_read_b64_tr_b16 v[22:23], v5 offset:38912\n\tv_mfma_f32_16x16x32_bf16 v[176:179], v[208:211], v[32:35], v[176:179]\n\tbuffer_load_dwordx4 v[88:91], v8, s[8:11], s68 offen offset:1024 sc1\n\tv_mfma_f32_16x16x32_bf16 v[184:187], v[212:215], v[32:35], v[184:187]\n\ts_waitcnt lgkmcnt(8)\n\tv_mfma_f32_16x16x32_bf16 v[172:175], v[204:207], v[36:39], v[172:175]\n\t" \
    "ds_read_b64_tr_b16 v[24:25], v6 offset:36864\n\tds_read_b64_tr_b16 v[26:27], v6 offset:38912\n\tv_mfma_f32_16x16x32_bf16 v[180:183], v[208:211], v[36:39], v[180:183]\n\tbuffer_load_dwordx4 v[92:95], v8, s[8:11], s68 offen offset:2048 sc1\n\ts_mov_b32 s69, 4194304\n\ts_add_u32 m0, s54, 69632\n\tv_mfma_f32_16x16x32_bf16 v[188:191], v[212:215], v[36:39], v[188:191]\n\ts_waitcnt lgkmcnt(8)\n\t" \
    "v_mfma_f32_16x16x32_bf16 v[48:51], v[204:207], v[40:43], v[48:51]\n\tds_read_b64_tr_b16 v[28:29], v7 offset:36864\n\tds_read_b64_tr_b16 v[30:31], v7 offset:38912\n\tv_mfma_f32_16x16x32_bf16 v[56:59], v[208:211], v[40:43], v[56:59]\n\tv_mfma_f32_16x16x32_bf16 v[64:67], v[212:215], v[40:43], v[64:67]\n\ts_waitcnt lgkmcnt(8)\n\tv_mfma_f32_16x16x32_bf16 v[52:55], v[204:207], v[44:47], v[52:55]\n\tds_read_b64_tr_b16 v[32:33], v224 offset:36864\n\t" \
    "ds_read_b64_tr_b16 v[34:35], v224 offset:38912\n\tv_mfma_f32_16x16x32_bf16 v[60:63], v[208:211], v[44:47], v[60:63]\n\tbuffer_load_dwordx4 v13, s[16:19], s69 offen lds\n\ts_mov_b32 s69, 393216\n\ts_add_u32 m0, s54, 8192\n\tv_mfma_f32_16x16x32_bf16 v[68:71], v[212:215], v[44:47], v[68:71]\n\ts_waitcnt vmcnt(31) lgkmcnt(8)\n\tv_mfma_f32_16x16x32_bf16 v[120:123], v[96:99], v[16:19], v[120:123]\n\t" \
    "ds_read_b64_tr_b16 v[36:37], v225 offset:36864\n\tds_read_b64_tr_b16 v[38:39], v225 offset:38912\n\tv_mfma_f32_16x16x32_bf16 v[128:131], v[100:103], v[16:19], v[128:131]\n\tbuffer_load_dwordx4 v12, s[16:19], s69 offen lds\n\ts_mov_b32 s69, 393216\n\ts_add_u32 m0, s54, 73728\n\tv_mfma_f32_16x16x32_bf16 v[136:139], v[104:107], v[16:19], v[136:139]\n\ts_waitcnt lgkmcnt(8)\n\t" \
    "v_mfma_f32_16x16x32_bf16 v[124:127], v[96:99], v[20:23], v[124:127]\n\tds_read_b64_tr_b16 v[40:41], v226 offset:36864\n\tds_read_b64_tr_b16 v[42:43], v226 offset:38912\n\tv_mfma_f32_16x16x32_bf16 v[132:135], v[100:103], v[20:23], v[132:135]\n\tv_mfma_f32_16x16x32_bf16 v[140:143], v[104:107], v[20:23], v[140:143]\n\ts_waitcnt lgkmcnt(8)\n\tv_mfma_f32_16x16x32_bf16 v[144:147], v[96:99], v[24:27], v[144:147]\n\tds_read_b64_tr_b16 v[44:45], v227 offset:36864\n\t" \
    "ds_read_b64_tr_b16 v[46:47], v227 offset:38912\n\tv_mfma_f32_16x16x32_bf16 v[152:155], v[100:103], v[24:27], v[152:155]\n\tbuffer_load_dwordx4 v13, s[16:19], s69 offen lds\n\ts_mov_b32 s69, 262144\n\ts_add_u32 m0, s54, 12288\n\tv_mfma_f32_16x16x32_bf16 v[160:163], v[104:107], v[24:27], v[160:163]\n\ts_waitcnt lgkmcnt(8)\n\tv_mfma_f32_16x16x32_bf16 v[148:151], v[96:99], v[28:31], v[148:151]\n\t" \
    "ds_read_b64_tr_b16 v[16:17], v4 offset:40960\n\tds_read_b64_tr_b16 v[18:19], v4 offset:43008\n\tv_mfma_f32_16x16x32_bf16 v[156:159], v[100:103], v[28:31], v[156:159]\n\tbuffer_load_dwordx4 v12, s[16:19], s69 offen lds\n\ts_mov_b32 s69, 262144\n\ts_add_u32 m0, s54, 77824\n\tv_mfma_f32_16x16x32_bf16 v[164:167], v[104:107], v[28:31], v[164:167]\n\ts_waitcnt lgkmcnt(8)\n\t" \
    "v_mfma_f32_16x16x32_bf16 v[168:171], v[96:99], v[32:35], v[168:171]\n\tds_read_b64_tr_b16 v[20:21], v5 offset:40960\n\tds_read_b64_tr_b16 v[22:23], v5 offset:43008\n\tv_mfma_f32_16x16x32_bf16 v[176:179], v[100:103], v[32:35], v[176:179]\n\tv_mfma_f32_16x16x32_bf16 v[184:187], v[104:107], v[32:35], v[184:187]\n\ts_waitcnt lgkmcnt(8)\n\tv_mfma_f32_16x16x32_bf16 v[172:175], v[96:99], v[36:39], v[172:175]\n\tds_read_b64_tr_b16 v[24:25], v6 offset:40960\n\t" \
    "ds_read_b64_tr_b16 v[26:27], v6 offset:43008\n\tv_mfma_f32_16x16x32_bf16 v[180:183], v[100:103], v[36:39], v[180:183]\n\tbuffer_load_dwordx4 v13, s[16:19], s69 offen lds\n\ts_add_u32 s68, s46, 52224\n\tv_mfma_f32_16x16x32_bf16 v[188:191], v[104:107], v[36:39], v[188:191]\n\ts_waitcnt lgkmcnt(8)\n\tv_mfma_f32_16x16x32_bf16 v[48:51], v[96:99], v[40:43], v[48:51]\n\tds_read_b64_tr_b16 v[28:29], v7 offset:40960\n\t" \
    "ds_read_b64_tr_b16 v[30:31], v7 offset:43008\n\tv_mfma_f32_16x16x32_bf16 v[56:59], v[100:103], v[40:43], v[56:59]\n\tv_mfma_f32_16x16x32_bf16 v[64:67], v[104:107], v[40:43], v[64:67]\n\ts_waitcnt lgkmcnt(8)\n\tv_mfma_f32_16x16x32_bf16 v[52:55], v[96:99], v[44:47], v[52:55]\n\tds_read_b64_tr_b16 v[32:33], v224 offset:40960\n\tds_read_b64_tr_b16 v[34:35], v224 offset:43008\n\tv_mfma_f32_16x16x32_bf16 v[60:63], v[100:103], v[44:47], v[60:63]\n\t" \
    "buffer_load_dwordx4 a[44:47], v8, s[8:11], s68 offen offset:0 sc1\n\tv_mfma_f32_16x16x32_bf16 v[68:71], v[104:107], v[44:47], v[68:71]\n\ts_barrier\n\ts_waitcnt vmcnt(32) lgkmcnt(8)\n\tv_mfma_f32_16x16x32_bf16 v[120:123], v[216:219], v[16:19], v[120:123]\n\tds_read_b64_tr_b16 v[36:37], v225 offset:40960\n\tds_read_b64_tr_b16 v[38:39], v225 offset:43008\n\tv_mfma_f32_16x16x32_bf16 v[128:131], v[108:111], v[16:19], v[128:131]\n\t" \
    "buffer_load_dwordx4 a[48:51], v8, s[8:11], s68 offen offset:1024 sc1\n\tv_mfma_f32_16x16x32_bf16 v[136:139], v[112:115], v[16:19], v[136:139]\n\ts_waitcnt lgkmcnt(8)\n\tv_mfma_f32_16x16x32_bf16 v[124:127], v[216:219], v[20:23], v[124:127]\n\tds_read_b64_tr_b16 v[40:41], v226 offset:40960\n\tds_read_b64_tr_b16 v[42:43], v226 offset:43008\n\tv_mfma_f32_16x16x32_bf16 v[132:135], v[108:111], v[20:23], v[132:135]\n\tbuffer_load_dwordx4 a[52:55], v8, s[8:11], s68 offen offset:2048 sc1\n\t" \
    "s_mov_b32 s69, 917504\n\ts_add_u32 m0, s54, 16384\n\tv_mfma_f32_16x16x32_bf16 v[140:143], v[112:115], v[20:23], v[140:143]\n\ts_waitcnt lgkmcnt(8)\n\tv_mfma_f32_16x16x32_bf16 v[144:147], v[216:219], v[24:27], v[144:147]\n\tds_read_b64_tr_b16 v[44:45], v227 offset:40960\n\tds_read_b64_tr_b16 v[46:47], v227 offset:43008\n\tv_mfma_f32_16x16x32_bf16 v[152:155], v[108:111], v[24:27], v[152:155]\n\t" \
    "buffer_load_dwordx4 v12, s[16:19], s69 offen lds\n\ts_mov_b32 s69, 917504\n\ts_add_u32 m0, s54, 81920\n\tv_mfma_f32_16x16x32_bf16 v[160:163], v[112:115], v[24:27], v[160:163]\n\ts_waitcnt lgkmcnt(8)\n\tv_mfma_f32_16x16x32_bf16 v[148:151], v[216:219], v[28:31], v[148:151]\n\tds_read_b64_tr_b16 v[16:17], v4 offset:45056\n\tds_read_b64_tr_b16 v[18:19], v4 offset:47104\n\t" \
    "v_mfma_f32_16x16x32_bf16 v[156:159], v[108:111], v[28:31], v[156:159]\n\tbuffer_load_dwordx4 v13, s[16:19], s69 offen lds\n\ts_mov_b32 s69, 786432\n\ts_add_u32 m0, s54, 20480\n\tv_mfma_f32_16x16x32_bf16 v[164:167], v[112:115], v[28:31], v[164:167]\n\ts_waitcnt lgkmcnt(8)\n\tv_mfma_f32_16x16x32_bf16 v[168:171], v[216:219], v[32:35], v[168:171]\n\tds_read_b64_tr_b16 v[20:21], v5 offset:45056\n\t" \
    "ds_read_b64_tr_b16 v[22:23], v5 offset:47104\n\tv_mfma_f32_16x16x32_bf16 v[176:179], v[108:111], v[32:35], v[176:179]\n\tbuffer_load_dwordx4 v12, s[16:19], s69 offen lds\n\ts_mov_b32 s69, 786432\n\ts_add_u32 m0, s54, 86016\n\tv_mfma_f32_16x16x32_bf16 v[184:187], v[112:115], v[32:35], v[184:187]\n\ts_waitcnt lgkmcnt(8)\n\tv_mfma_f32_16x16x32_bf16 v[172:175], v[216:219], v[36:39], v[172:175]\n\t" \
    "ds_read_b64_tr_b16 v[24:25], v6 offset:45056\n\tds_read_b64_tr_b16 v[26:27], v6 offset:47104\n\tv_mfma_f32_16x16x32_bf16 v[180:183], v[108:111], v[36:39], v[180:183]\n\tv_mfma_f32_16x16x32_bf16 v[188:191], v[112:115], v[36:39], v[188:191]\n\ts_waitcnt lgkmcnt(8)\n\tv_mfma_f32_16x16x32_bf16 v[48:51], v[216:219], v[40:43], v[48:51]\n\tds_read_b64_tr_b16 v[28:29], v7 offset:45056\n\tds_read_b64_tr_b16 v[30:31], v7 offset:47104\n\t" \
    "v_mfma_f32_16x16x32_bf16 v[56:59], v[108:111], v[40:43], v[56:59]\n\tbuffer_load_dwordx4 v13, s[16:19], s69 offen lds\n\ts_mov_b32 s69, 1441792\n\ts_add_u32 m0, s54, 24576\n\tv_mfma_f32_16x16x32_bf16 v[64:67], v[112:115], v[40:43], v[64:67]\n\ts_waitcnt lgkmcnt(8)\n\tv_mfma_f32_16x16x32_bf16 v[52:55], v[216:219], v[44:47], v[52:55]\n\tds_read_b64_tr_b16 v[32:33], v224 offset:45056\n\t" \
    "ds_read_b64_tr_b16 v[34:35], v224 offset:47104\n\tv_mfma_f32_16x16x32_bf16 v[60:63], v[108:111], v[44:47], v[60:63]\n\tbuffer_load_dwordx4 v12, s[16:19], s69 offen lds\n\ts_mov_b32 s69, 1441792\n\ts_add_u32 m0, s54, 90112\n\tv_mfma_f32_16x16x32_bf16 v[68:71], v[112:115], v[44:47], v[68:71]\n\ts_waitcnt vmcnt(15)\n\tv_cmp_ne_u64 vcc, s[36:37], v[250:251]\n\t" \
    "s_cbranch_vccz 112f\n\ts_mov_b32 s71, 1048576\n\t111:\n\ts_sleep 1\n\tbuffer_load_dwordx2 v[250:251], v241, s[8:11], s50 offen sc1\n\ts_waitcnt vmcnt(0)\n\tv_cmp_ne_u64 vcc, s[36:37], v[250:251]\n\ts_cbranch_vccz 112f\n\t" \
    "s_sub_u32 s71, s71, 1\n\ts_cbranch_scc0 111b\n\t112:\n\ts_waitcnt lgkmcnt(8)\n\tv_mfma_f32_16x16x32_bf16 v[120:123], v[116:119], v[16:19], v[120:123]\n\tds_read_b64_tr_b16 v[36:37], v225 offset:45056\n\tds_read_b64_tr_b16 v[38:39], v225 offset:47104\n\tv_mfma_f32_16x16x32_bf16 v[128:131], a[0:3], v[16:19], v[128:131]\n\t" \
    "v_mfma_f32_16x16x32_bf16 v[136:139], a[4:7], v[16:19], v[136:139]\n\ts_waitcnt lgkmcnt(8)\n\tv_mfma_f32_16x16x32_bf16 v[124:127], v[116:119], v[20:23], v[124:127]\n\tds_read_b64_tr_b16 v[40:41], v226 offset:45056\n\tds_read_b64_tr_b16 v[42:43], v226 offset:47104\n\tv_mfma_f32_16x16x32_bf16 v[132:135], a[0:3], v[20:23], v[132:135]\n\tbuffer_load_dwordx4 v13, s[16:19], s69 offen lds\n\ts_add_u32 s68, s46, 55296\n\t" \
    "v_mfma_f32_16x16x32_bf16 v[140:143], a[4:7], v[20:23], v[140:143]\n\ts_waitcnt lgkmcnt(8)\n\tv_mfma_f32_16x16x32_bf16 v[144:147], v[116:119], v[24:27], v[144:147]\n\tds_read_b64_tr_b16 v[44:45], v227 offset:45056\n\tds_read_b64_tr_b16 v[46:47], v227 offset:47104\n\tv_mfma_f32_16x16x32_bf16 v[152:155], a[0:3], v[24:27], v[152:155]\n\tbuffer_load_dwordx4 v[96:99], v8, s[8:11], s68 offen offset:0 sc1\n\tv_mfma_f32_16x16x32_bf16 v[160:163], a[4:7], v[24:27], v[160:163]\n\t" \
    "s_waitcnt lgkmcnt(8)\n\tv_mfma_f32_16x16x32_bf16 v[148:151], v[116:119], v[28:31], v[148:151]\n\tds_read_b64_tr_b16 v[16:17], v4 offset:49152\n\tds_read_b64_tr_b16 v[18:19], v4 offset:51200\n\tv_mfma_f32_16x16x32_bf16 v[156:159], a[0:3], v[28:31], v[156:159]\n\tv_mfma_f32_16x16x32_bf16 v[164:167], a[4:7], v[28:31], v[164:167]\n\ts_waitcnt lgkmcnt(8)\n\tv_mfma_f32_16x16x32_bf16 v[168:171], v[116:119], v[32:35], v[168:171]\n\t" \
    "ds_read_b64_tr_b16 v[20:21], v5 offset:49152\n\tds_read_b64_tr_b16 v[22:23], v5 offset:51200\n\tv_mfma_f32_16x16x32_bf16 v[176:179], a[0:3], v[32:35], v[176:179]\n\tbuffer_load_dwordx4 v[100:103], v8, s[8:11], s68 offen offset:1024 sc1\n\tv_mfma_f32_16x16x32_bf16 v[184:187], a[4:7], v[32:35], v[184:187]\n\ts_waitcnt lgkmcnt(8)\n\tv_mfma_f32_16x16x32_bf16 v[172:175], v[116:119], v[36:39], v[172:175]\n\tds_read_b64_tr_b16 v[24:25], v6 offset:49152\n\t" \
    "ds_read_b64_tr_b16 v[26:27], v6 offset:51200\n\tv_mfma_f32_16x16x32_bf16 v[180:183], a[0:3], v[36:39], v[180:183]\n\tbuffer_load_dwordx4 v[104:107], v8, s[8:11], s68 offen offset:2048 sc1\n\ts_add_u32 s68, s46, 58368\n\tv_mfma_f32_16x16x32_bf16 v[188:191], a[4:7], v[36:39], v[188:191]\n\ts_waitcnt lgkmcnt(8)\n\tv_mfma_f32_16x16x32_bf16 v[48:51], v[116:119], v[40:43], v[48:51]\n\tds_read_b64_tr_b16 v[28:29], v7 offset:49152\n\t" \
    "ds_read_b64_tr_b16 v[30:31], v7 offset:51200\n\tv_mfma_f32_16x16x32_bf16 v[56:59], a[0:3], v[40:43], v[56:59]\n\tv_mfma_f32_16x16x32_bf16 v[64:67], a[4:7], v[40:43], v[64:67]\n\ts_waitcnt lgkmcnt(8)\n\tv_mfma_f32_16x16x32_bf16 v[52:55], v[116:119], v[44:47], v[52:55]\n\tds_read_b64_tr_b16 v[32:33], v224 offset:49152\n\tds_read_b64_tr_b16 v[34:35], v224 offset:51200\n\tv_mfma_f32_16x16x32_bf16 v[60:63], a[0:3], v[44:47], v[60:63]\n\t" \
    "buffer_load_dwordx4 a[56:59], v8, s[8:11], s68 offen offset:0 sc1\n\tv_mfma_f32_16x16x32_bf16 v[68:71], a[4:7], v[44:47], v[68:71]\n\ts_barrier\n\ts_waitcnt lgkmcnt(8)\n\tv_mfma_f32_16x16x32_bf16 v[120:123], a[8:11], v[16:19], v[120:123]\n\tds_read_b64_tr_b16 v[36:37], v225 offset:49152\n\tds_read_b64_tr_b16 v[38:39], v225 offset:51200\n\tv_mfma_f32_16x16x32_bf16 v[128:131], a[12:15], v[16:19], v[128:131]\n\t" \
    "buffer_load_dwordx4 a[60:63], v8, s[8:11], s68 offen offset:1024 sc1\n\tv_mfma_f32_16x16x32_bf16 v[136:139], a[16:19], v[16:19], v[136:139]\n\ts_waitcnt lgkmcnt(8)\n\tv_mfma_f32_16x16x32_bf16 v[124:127], a[8:11], v[20:23], v[124:127]\n\tds_read_b64_tr_b16 v[40:41], v226 offset:49152\n\tds_read_b64_tr_b16 v[42:43], v226 offset:51200\n\tv_mfma_f32_16x16x32_bf16 v[132:135], a[12:15], v[20:23], v[132:135]\n\tbuffer_load_dwordx4 a[64:67], v8, s[8:11], s68 offen offset:2048 sc1\n\t" \
    "s_add_u32 s68, s46, 61440\n\tv_mfma_f32_16x16x32_bf16 v[140:143], a[16:19], v[20:23], v[140:143]\n\ts_waitcnt lgkmcnt(8)\n\tv_mfma_f32_16x16x32_bf16 v[144:147], a[8:11], v[24:27], v[144:147]\n\tds_read_b64_tr_b16 v[44:45], v227 offset:49152\n\tds_read_b64_tr_b16 v[46:47], v227 offset:51200\n\tv_mfma_f32_16x16x32_bf16 v[152:155], a[12:15], v[24:27], v[152:155]\n\tv_mfma_f32_16x16x32_bf16 v[160:163], a[16:19], v[24:27], v[160:163]\n\t" \
    "s_waitcnt lgkmcnt(8)\n\tv_mfma_f32_16x16x32_bf16 v[148:151], a[8:11], v[28:31], v[148:151]\n\tds_read_b64_tr_b16 v[16:17], v4 offset:53248\n\tds_read_b64_tr_b16 v[18:19], v4 offset:55296\n\tv_mfma_f32_16x16x32_bf16 v[156:159], a[12:15], v[28:31], v[156:159]\n\tbuffer_load_dwordx4 a[0:3], v8, s[8:11], s68 offen offset:0 sc1\n\tv_mfma_f32_16x16x32_bf16 v[164:167], a[16:19], v[28:31], v[164:167]\n\ts_waitcnt lgkmcnt(8)\n\t" \
    "v_mfma_f32_16x16x32_bf16 v[168:171], a[8:11], v[32:35], v[168:171]\n\tds_read_b64_tr_b16 v[20:21], v5 offset:53248\n\tds_read_b64_tr_b16 v[22:23], v5 offset:55296\n\tv_mfma_f32_16x16x32_bf16 v[176:179], a[12:15], v[32:35], v[176:179]\n\tbuffer_load_dwordx4 a[4:7], v8, s[8:11], s68 offen offset:1024 sc1\n\tv_mfma_f32_16x16x32_bf16 v[184:187], a[16:19], v[32:35], v[184:187]\n\ts_waitcnt lgkmcnt(8)\n\tv_mfma_f32_16x16x32_bf16 v[172:175], a[8:11], v[36:39], v[172:175]\n\t" \
    "ds_read_b64_tr_b16 v[24:25], v6 offset:53248\n\tds_read_b64_tr_b16 v[26:27], v6 offset:55296\n\tv_mfma_f32_16x16x32_bf16 v[180:183], a[12:15], v[36:39], v[180:183]\n\tv_mfma_f32_16x16x32_bf16 v[188:191], a[16:19], v[36:39], v[188:191]\n\ts_waitcnt lgkmcnt(8)\n\tv_mfma_f32_16x16x32_bf16 v[48:51], a[8:11], v[40:43], v[48:51]\n\tds_read_b64_tr_b16 v[28:29], v7 offset:53248\n\tds_read_b64_tr_b16 v[30:31], v7 offset:55296\n\t" \
    "v_mfma_f32_16x16x32_bf16 v[56:59], a[12:15], v[40:43], v[56:59]\n\tbuffer_load_dwordx4 a[68:71], v8, s[8:11], s68 offen offset:2048 sc1\n\ts_add_u32 s68, s46, 64512\n\tv_mfma_f32_16x16x32_bf16 v[64:67], a[16:19], v[40:43], v[64:67]\n\ts_waitcnt lgkmcnt(8)\n\tv_mfma_f32_16x16x32_bf16 v[52:55], a[8:11], v[44:47], v[52:55]\n\tds_read_b64_tr_b16 v[32:33], v224 offset:53248\n\tds_read_b64_tr_b16 v[34:35], v224 offset:55296\n\t" \
    "v_mfma_f32_16x16x32_bf16 v[60:63], a[12:15], v[44:47], v[60:63]\n\tbuffer_load_dwordx4 a[72:75], v8, s[8:11], s68 offen offset:0 sc1\n\tv_mfma_f32_16x16x32_bf16 v[68:71], a[16:19], v[44:47], v[68:71]\n\ts_waitcnt lgkmcnt(8)\n\tv_mfma_f32_16x16x32_bf16 v[120:123], a[20:23], v[16:19], v[120:123]\n\tds_read_b64_tr_b16 v[36:37], v225 offset:53248\n\tds_read_b64_tr_b16 v[38:39], v225 offset:55296\n\tv_mfma_f32_16x16x32_bf16 v[128:131], a[24:27], v[16:19], v[128:131]\n\t" \
    "v_mfma_f32_16x16x32_bf16 v[136:139], a[28:31], v[16:19], v[136:139]\n\ts_waitcnt lgkmcnt(8)\n\tv_mfma_f32_16x16x32_bf16 v[124:127], a[20:23], v[20:23], v[124:127]\n\tds_read_b64_tr_b16 v[40:41], v226 offset:53248\n\tds_read_b64_tr_b16 v[42:43], v226 offset:55296\n\tv_mfma_f32_16x16x32_bf16 v[132:135], a[24:27], v[20:23], v[132:135]\n\tbuffer_load_dwordx4 a[76:79], v8, s[8:11], s68 offen offset:1024 sc1\n\tv_mfma_f32_16x16x32_bf16 v[140:143], a[28:31], v[20:23], v[140:143]\n\t" \
    "s_waitcnt lgkmcnt(8)\n\tv_mfma_f32_16x16x32_bf16 v[144:147], a[20:23], v[24:27], v[144:147]\n\tds_read_b64_tr_b16 v[44:45], v227 offset:53248\n\tds_read_b64_tr_b16 v[46:47], v227 offset:55296\n\tv_mfma_f32_16x16x32_bf16 v[152:155], a[24:27], v[24:27], v[152:155]\n\tbuffer_load_dwordx4 a[80:83], v8, s[8:11], s68 offen offset:2048 sc1\n\ts_mov_b32 s69, 1310720\n\ts_add_u32 m0, s54, 28672\n\t" \
    "v_mfma_f32_16x16x32_bf16 v[160:163], a[28:31], v[24:27], v[160:163]\n\ts_waitcnt lgkmcnt(8)\n\tv_mfma_f32_16x16x32_bf16 v[148:151], a[20:23], v[28:31], v[148:151]\n\tds_read_b64_tr_b16 v[16:17], v4 offset:57344\n\tds_read_b64_tr_b16 v[18:19], v4 offset:59392\n\tv_mfma_f32_16x16x32_bf16 v[156:159], a[24:27], v[28:31], v[156:159]\n\tv_mfma_f32_16x16x32_bf16 v[164:167], a[28:31], v[28:31], v[164:167]\n\ts_waitcnt lgkmcnt(8)\n\t" \
    "v_mfma_f32_16x16x32_bf16 v[168:171], a[20:23], v[32:35], v[168:171]\n\tds_read_b64_tr_b16 v[20:21], v5 offset:57344\n\tds_read_b64_tr_b16 v[22:23], v5 offset:59392\n\tv_mfma_f32_16x16x32_bf16 v[176:179], a[24:27], v[32:35], v[176:179]\n\tbuffer_load_dwordx4 v12, s[16:19], s69 offen lds\n\ts_mov_b32 s69, 1310720\n\ts_add_u32 m0, s54, 94208\n\tv_mfma_f32_16x16x32_bf16 v[184:187], a[28:31], v[32:35], v[184:187]\n\t" \
    "s_waitcnt lgkmcnt(8)\n\tv_mfma_f32_16x16x32_bf16 v[172:175], a[20:23], v[36:39], v[172:175]\n\tds_read_b64_tr_b16 v[24:25], v6 offset:57344\n\tds_read_b64_tr_b16 v[26:27], v6 offset:59392\n\tv_mfma_f32_16x16x32_bf16 v[180:183], a[24:27], v[36:39], v[180:183]\n\tv_mfma_f32_16x16x32_bf16 v[188:191], a[28:31], v[36:39], v[188:191]\n\ts_waitcnt lgkmcnt(8)\n\tv_mfma_f32_16x16x32_bf16 v[48:51], a[20:23], v[40:43], v[48:51]\n\t" \
    "ds_read_b64_tr_b16 v[28:29], v7 offset:57344\n\tds_read_b64_tr_b16 v[30:31], v7 offset:59392\n\tv_mfma_f32_16x16x32_bf16 v[56:59], a[24:27], v[40:43], v[56:59]\n\tbuffer_load_dwordx4 v13, s[16:19], s69 offen lds\n\ts_mov_b32 s69, 1966080\n\ts_add_u32 m0, s54, 32768\n\tv_mfma_f32_16x16x32_bf16 v[64:67], a[28:31], v[40:43], v[64:67]\n\ts_waitcnt lgkmcnt(8)\n\t" \
    "v_mfma_f32_16x16x32_bf16 v[52:55], a[20:23], v[44:47], v[52:55]\n\tds_read_b64_tr_b16 v[32:33], v224 offset:57344\n\tds_read_b64_tr_b16 v[34:35], v224 offset:59392\n\tv_mfma_f32_16x16x32_bf16 v[60:63], a[24:27], v[44:47], v[60:63]\n\tbuffer_load_dwordx4 v12, s[16:19], s69 offen lds\n\ts_mov_b32 s69, 1966080\n\ts_add_u32 m0, s54, 98304\n\tv_mfma_f32_16x16x32_bf16 v[68:71], a[28:31], v[44:47], v[68:71]\n\t" \
    "s_barrier\n\ts_waitcnt lgkmcnt(8)\n\tv_mfma_f32_16x16x32_bf16 v[120:123], v[72:75], v[16:19], v[120:123]\n\tds_read_b64_tr_b16 v[36:37], v225 offset:57344\n\tds_read_b64_tr_b16 v[38:39], v225 offset:59392\n\tv_mfma_f32_16x16x32_bf16 v[128:131], v[76:79], v[16:19], v[128:131]\n\tbuffer_load_dwordx4 v13, s[16:19], s69 offen lds\n\ts_mov_b32 s69, 1835008\n\t" \
    "s_add_u32 m0, s54, 36864\n\tv_mfma_f32_16x16x32_bf16 v[136:139], v[80:83], v[16:19], v[136:139]\n\ts_waitcnt lgkmcnt(8)\n\tv_mfma_f32_16x16x32_bf16 v[124:127], v[72:75], v[20:23], v[124:127]\n\tds_read_b64_tr_b16 v[40:41], v226 offset:57344\n\tds_read_b64_tr_b16 v[42:43], v226 offset:59392\n\tv_mfma_f32_16x16x32_bf16 v[132:135], v[76:79], v[20:23], v[132:135]\n\tbuffer_load_dwordx4 v12, s[16:19], s69 offen lds\n\t" \
    "s_add_u32 s68, s46, 67584\n\tv_mfma_f32_16x16x32_bf16 v[140:143], v[80:83], v[20:23], v[140:143]\n\ts_waitcnt lgkmcnt(8)\n\tv_mfma_f32_16x16x32_bf16 v[144:147], v[72:75], v[24:27], v[144:147]\n\tds_read_b64_tr_b16 v[44:45], v227 offset:57344\n\tds_read_b64_tr_b16 v[46:47], v227 offset:59392\n\tv_mfma_f32_16x16x32_bf16 v[152:155], v[76:79], v[24:27], v[152:155]\n\tv_mfma_f32_16x16x32_bf16 v[160:163], v[80:83], v[24:27], v[160:163]\n\t" \
    "s_waitcnt lgkmcnt(8)\n\tv_mfma_f32_16x16x32_bf16 v[148:151], v[72:75], v[28:31], v[148:151]\n\tds_read_b64_tr_b16 v[16:17], v4 offset:61440\n\tds_read_b64_tr_b16 v[18:19], v4 offset:63488\n\tv_mfma_f32_16x16x32_bf16 v[156:159], v[76:79], v[28:31], v[156:159]\n\tbuffer_load_dwordx4 a[8:11], v8, s[8:11], s68 offen offset:0 sc1\n\tv_mfma_f32_16x16x32_bf16 v[164:167], v[80:83], v[28:31], v[164:167]\n\ts_waitcnt lgkmcnt(8)\n\t" \
    "v_mfma_f32_16x16x32_bf16 v[168:171], v[72:75], v[32:35], v[168:171]\n\tds_read_b64_tr_b16 v[20:21], v5 offset:61440\n\tds_read_b64_tr_b16 v[22:23], v5 offset:63488\n\tv_mfma_f32_16x16x32_bf16 v[176:179], v[76:79], v[32:35], v[176:179]\n\tbuffer_load_dwordx4 a[12:15], v8, s[8:11], s68 offen offset:1024 sc1\n\tv_mfma_f32_16x16x32_bf16 v[184:187], v[80:83], v[32:35], v[184:187]\n\ts_waitcnt lgkmcnt(8)\n\tv_mfma_f32_16x16x32_bf16 v[172:175], v[72:75], v[36:39], v[172:175]\n\t" \
    "ds_read_b64_tr_b16 v[24:25], v6 offset:61440\n\tds_read_b64_tr_b16 v[26:27], v6 offset:63488\n\tv_mfma_f32_16x16x32_bf16 v[180:183], v[76:79], v[36:39], v[180:183]\n\tv_mfma_f32_16x16x32_bf16 v[188:191], v[80:83], v[36:39], v[188:191]\n\ts_waitcnt lgkmcnt(8)\n\tv_mfma_f32_16x16x32_bf16 v[48:51], v[72:75], v[40:43], v[48:51]\n\tds_read_b64_tr_b16 v[28:29], v7 offset:61440\n\tds_read_b64_tr_b16 v[30:31], v7 offset:63488\n\t" \
    "v_mfma_f32_16x16x32_bf16 v[56:59], v[76:79], v[40:43], v[56:59]\n\tbuffer_load_dwordx4 a[16:19], v8, s[8:11], s68 offen offset:2048 sc1\n\ts_add_u32 s68, s46, 70656\n\tv_mfma_f32_16x16x32_bf16 v[64:67], v[80:83], v[40:43], v[64:67]\n\ts_waitcnt lgkmcnt(8)\n\tv_mfma_f32_16x16x32_bf16 v[52:55], v[72:75], v[44:47], v[52:55]\n\tds_read_b64_tr_b16 v[32:33], v224 offset:61440\n\tds_read_b64_tr_b16 v[34:35], v224 offset:63488\n\t" \
    "v_mfma_f32_16x16x32_bf16 v[60:63], v[76:79], v[44:47], v[60:63]\n\tbuffer_load_dwordx4 a[20:23], v8, s[8:11], s68 offen offset:0 sc1\n\tv_mfma_f32_16x16x32_bf16 v[68:71], v[80:83], v[44:47], v[68:71]\n\ts_waitcnt lgkmcnt(8)\n\tv_mfma_f32_16x16x32_bf16 v[120:123], a[32:35], v[16:19], v[120:123]\n\tds_read_b64_tr_b16 v[36:37], v225 offset:61440\n\tds_read_b64_tr_b16 v[38:39], v225 offset:63488\n\tv_mfma_f32_16x16x32_bf16 v[128:131], a[36:39], v[16:19], v[128:131]\n\t" \
    "v_mfma_f32_16x16x32_bf16 v[136:139], a[40:43], v[16:19], v[136:139]\n\ts_waitcnt lgkmcnt(8)\n\tv_mfma_f32_16x16x32_bf16 v[124:127], a[32:35], v[20:23], v[124:127]\n\tds_read_b64_tr_b16 v[40:41], v4 offset:0\n\tds_read_b64_tr_b16 v[42:43], v4 offset:2048\n\tv_mfma_f32_16x16x32_bf16 v[132:135], a[36:39], v[20:23], v[132:135]\n\tbuffer_load_dwordx4 a[24:27], v8, s[8:11], s68 offen offset:1024 sc1\n\tv_mfma_f32_16x16x32_bf16 v[140:143], a[40:43], v[20:23], v[140:143]\n\t" \
    "s_waitcnt lgkmcnt(8)\n\tv_mfma_f32_16x16x32_bf16 v[144:147], a[32:35], v[24:27], v[144:147]\n\tds_read_b64_tr_b16 v[44:45], v5 offset:0\n\tds_read_b64_tr_b16 v[46:47], v5 offset:2048\n\tv_mfma_f32_16x16x32_bf16 v[152:155], a[36:39], v[24:27], v[152:155]\n\tv_mfma_f32_16x16x32_bf16 v[160:163], a[40:43], v[24:27], v[160:163]\n\ts_waitcnt lgkmcnt(8)\n\tv_mfma_f32_16x16x32_bf16 v[148:151], a[32:35], v[28:31], v[148:151]\n\t" \
    "ds_read_b64_tr_b16 v[16:17], v6 offset:0\n\tds_read_b64_tr_b16 v[18:19], v6 offset:2048\n\tv_mfma_f32_16x16x32_bf16 v[156:159], a[36:39], v[28:31], v[156:159]\n\tbuffer_load_dwordx4 a[28:31], v8, s[8:11], s68 offen offset:2048 sc1\n\ts_add_u32 s68, s46, 73728\n\tv_mfma_f32_16x16x32_bf16 v[164:167], a[40:43], v[28:31], v[164:167]\n\ts_waitcnt lgkmcnt(8)\n\tv_mfma_f32_16x16x32_bf16 v[168:171], a[32:35], v[32:35], v[168:171]\n\t" \
    "ds_read_b64_tr_b16 v[20:21], v7 offset:0\n\tds_read_b64_tr_b16 v[22:23], v7 offset:2048\n\tv_mfma_f32_16x16x32_bf16 v[176:179], a[36:39], v[32:35], v[176:179]\n\tbuffer_load_dwordx4 a[84:87], v8, s[8:11], s68 offen offset:0 sc1\n\tv_mfma_f32_16x16x32_bf16 v[184:187], a[40:43], v[32:35], v[184:187]\n\ts_waitcnt lgkmcnt(8)\n\tv_mfma_f32_16x16x32_bf16 v[172:175], a[32:35], v[36:39], v[172:175]\n\tds_read_b64_tr_b16 v[24:25], v224 offset:0\n\t" \
    "ds_read_b64_tr_b16 v[26:27], v224 offset:2048\n\tv_mfma_f32_16x16x32_bf16 v[180:183], a[36:39], v[36:39], v[180:183]\n\tv_mfma_f32_16x16x32_bf16 v[188:191], a[40:43], v[36:39], v[188:191]\n\ts_waitcnt vmcnt(38) lgkmcnt(8)\n\tv_mfma_f32_16x16x32_bf16 v[120:123], v[84:87], v[40:43], v[120:123]\n\tds_read_b64_tr_b16 v[28:29], v225 offset:0\n\tds_read_b64_tr_b16 v[30:31], v225 offset:2048\n\tv_mfma_f32_16x16x32_bf16 v[128:131], v[88:91], v[40:43], v[128:131]\n\t" \
    "buffer_load_dwordx4 a[88:91], v8, s[8:11], s68 offen offset:1024 sc1\n\tv_mfma_f32_16x16x32_bf16 v[136:139], v[92:95], v[40:43], v[136:139]\n\ts_waitcnt lgkmcnt(8)\n\tv_mfma_f32_16x16x32_bf16 v[124:127], v[84:87], v[44:47], v[124:127]\n\tds_read_b64_tr_b16 v[32:33], v4 offset:4096\n\tds_read_b64_tr_b16 v[34:35], v4 offset:6144\n\tv_mfma_f32_16x16x32_bf16 v[132:135], v[88:91], v[44:47], v[132:135]\n\tbuffer_load_dwordx4 a[92:95], v8, s[8:11], s68 offen offset:2048 sc1\n\t" \
    "s_mov_b32 s69, 1835008\n\ts_add_u32 m0, s54, 102400\n\tv_mfma_f32_16x16x32_bf16 v[140:143], v[92:95], v[44:47], v[140:143]\n\ts_waitcnt vmcnt(35)\n\ts_barrier\n\ts_waitcnt lgkmcnt(8)\n\tv_mfma_f32_16x16x32_bf16 v[144:147], v[84:87], v[16:19], v[144:147]\n\tds_read_b64_tr_b16 v[36:37], v5 offset:4096\n\t" \
    "ds_read_b64_tr_b16 v[38:39], v5 offset:6144\n\tv_mfma_f32_16x16x32_bf16 v[152:155], v[88:91], v[16:19], v[152:155]\n\tbuffer_load_dwordx4 v13, s[16:19], s69 offen lds\n\ts_mov_b32 s69, 2490368\n\ts_add_u32 m0, s54, 40960\n\tv_mfma_f32_16x16x32_bf16 v[160:163], v[92:95], v[16:19], v[160:163]\n\ts_waitcnt lgkmcnt(8)\n\tv_mfma_f32_16x16x32_bf16 v[148:151], v[84:87], v[20:23], v[148:151]\n\t" \
    "ds_read_b64_tr_b16 v[40:41], v6 offset:4096\n\tds_read_b64_tr_b16 v[42:43], v6 offset:6144\n\tv_mfma_f32_16x16x32_bf16 v[156:159], v[88:91], v[20:23], v[156:159]\n\tbuffer_load_dwordx4 v12, s[16:19], s69 offen lds\n\ts_mov_b32 s69, 2490368\n\ts_add_u32 m0, s54, 106496\n\tv_mfma_f32_16x16x32_bf16 v[164:167], v[92:95], v[20:23], v[164:167]\n\ts_waitcnt lgkmcnt(8)\n\t" \
    "v_mfma_f32_16x16x32_bf16 v[168:171], v[84:87], v[24:27], v[168:171]\n\tds_read_b64_tr_b16 v[44:45], v7 offset:4096\n\tds_read_b64_tr_b16 v[46:47], v7 offset:6144\n\tv_mfma_f32_16x16x32_bf16 v[176:179], v[88:91], v[24:27], v[176:179]\n\tv_mfma_f32_16x16x32_bf16 v[184:187], v[92:95], v[24:27], v[184:187]\n\ts_waitcnt lgkmcnt(8)\n\tv_mfma_f32_16x16x32_bf16 v[172:175], v[84:87], v[28:31], v[172:175]\n\tds_read_b64_tr_b16 v[16:17], v224 offset:4096\n\t" \
    "ds_read_b64_tr_b16 v[18:19], v224 offset:6144\n\tv_mfma_f32_16x16x32_bf16 v[180:183], v[88:91], v[28:31], v[180:183]\n\tbuffer_load_dwordx4 v13, s[16:19], s69 offen lds\n\ts_mov_b32 s69, 2359296\n\ts_add_u32 m0, s54, 45056\n\tv_mfma_f32_16x16x32_bf16 v[188:191], v[92:95], v[28:31], v[188:191]\n\ts_waitcnt vmcnt(35) lgkmcnt(8)\n\tv_mfma_f32_16x16x32_bf16 v[120:123], a[44:47], v[32:35], v[120:123]\n\t" \
    "ds_read_b64_tr_b16 v[20:21], v225 offset:4096\n\tds_read_b64_tr_b16 v[22:23], v225 offset:6144\n\tv_mfma_f32_16x16x32_bf16 v[128:131], a[48:51], v[32:35], v[128:131]\n\tv_mfma_f32_16x16x32_bf16 v[136:139], a[52:55], v[32:35], v[136:139]\n\ts_waitcnt lgkmcnt(8)\n\tv_mfma_f32_16x16x32_bf16 v[124:127], a[44:47], v[36:39], v[124:127]\n\tds_read_b64_tr_b16 v[24:25], v4 offset:8192\n\tds_read_b64_tr_b16 v[26:27], v4 offset:10240\n\t" \
    "v_mfma_f32_16x16x32_bf16 v[132:135], a[48:51], v[36:39], v[132:135]\n\tbuffer_load_dwordx4 v12, s[16:19], s69 offen lds\n\ts_mov_b32 s69, 2359296\n\ts_add_u32 m0, s54, 110592\n\tv_mfma_f32_16x16x32_bf16 v[140:143], a[52:55], v[36:39], v[140:143]\n\ts_waitcnt lgkmcnt(8)\n\tv_mfma_f32_16x16x32_bf16 v[144:147], a[44:47], v[40:43], v[144:147]\n\tds_read_b64_tr_b16 v[28:29], v5 offset:8192\n\t" \
    "ds_read_b64_tr_b16 v[30:31], v5 offset:10240\n\tv_mfma_f32_16x16x32_bf16 v[152:155], a[48:51], v[40:43], v[152:155]\n\tbuffer_load_dwordx4 v13, s[16:19], s69 offen lds\n\ts_add_u32 s68, s46, 76800\n\tv_mfma_f32_16x16x32_bf16 v[160:163], a[52:55], v[40:43], v[160:163]\n\ts_waitcnt lgkmcnt(8)\n\tv_mfma_f32_16x16x32_bf16 v[148:151], a[44:47], v[44:47], v[148:151]\n\tds_read_b64_tr_b16 v[32:33], v6 offset:8192\n\t" \
    "ds_read_b64_tr_b16 v[34:35], v6 offset:10240\n\tv_mfma_f32_16x16x32_bf16 v[156:159], a[48:51], v[44:47], v[156:159]\n\tv_mfma_f32_16x16x32_bf16 v[164:167], a[52:55], v[44:47], v[164:167]\n\ts_waitcnt lgkmcnt(8)\n\tv_mfma_f32_16x16x32_bf16 v[168:171], a[44:47], v[16:19], v[168:171]\n\tds_read_b64_tr_b16 v[36:37], v7 offset:8192\n\tds_read_b64_tr_b16 v[38:39], v7 offset:10240\n\tv_mfma_f32_16x16x32_bf16 v[176:179], a[48:51], v[16:19], v[176:179]\n\t" \
    "buffer_load_dwordx4 a[96:99], v8, s[8:11], s68 offen offset:0 sc1\n\tv_mfma_f32_16x16x32_bf16 v[184:187], a[52:55], v[16:19], v[184:187]\n\ts_waitcnt lgkmcnt(8)\n\tv_mfma_f32_16x16x32_bf16 v[172:175], a[44:47], v[20:23], v[172:175]\n\tds_read_b64_tr_b16 v[40:41], v224 offset:8192\n\tds_read_b64_tr_b16 v[42:43], v224 offset:10240\n\tv_mfma_f32_16x16x32_bf16 v[180:183], a[48:51], v[20:23], v[180:183]\n\tbuffer_load_dwordx4 a[100:103], v8, s[8:11], s68 offen offset:1024 sc1\n\t" \
    "v_mfma_f32_16x16x32_bf16 v[188:191], a[52:55], v[20:23], v[188:191]\n\ts_waitcnt vmcnt(30) lgkmcnt(8)\n\tv_mfma_f32_16x16x32_bf16 v[120:123], v[96:99], v[24:27], v[120:123]\n\tds_read_b64_tr_b16 v[44:45], v225 offset:8192\n\tds_read_b64_tr_b16 v[46:47], v225 offset:10240\n\tv_mfma_f32_16x16x32_bf16 v[128:131], v[100:103], v[24:27], v[128:131]\n\tv_mfma_f32_16x16x32_bf16 v[136:139], v[104:107], v[24:27], v[136:139]\n\ts_waitcnt lgkmcnt(8)\n\t" \
    "v_mfma_f32_16x16x32_bf16 v[124:127], v[96:99], v[28:31], v[124:127]\n\tds_read_b64_tr_b16 v[16:17], v4 offset:12288\n\tds_read_b64_tr_b16 v[18:19], v4 offset:14336\n\tv_mfma_f32_16x16x32_bf16 v[132:135], v[100:103], v[28:31], v[132:135]\n\tbuffer_load_dwordx4 a[104:107], v8, s[8:11], s68 offen offset:2048 sc1\n\ts_add_u32 s68, s46, 79872\n\tv_mfma_f32_16x16x32_bf16 v[140:143], v[104:107], v[28:31], v[140:143]\n\ts_waitcnt lgkmcnt(8)\n\t" \
    "v_mfma_f32_16x16x32_bf16 v[144:147], v[96:99], v[32:35], v[144:147]\n\tds_read_b64_tr_b16 v[20:21], v5 offset:12288\n\tds_read_b64_tr_b16 v[22:23], v5 offset:14336\n\tv_mfma_f32_16x16x32_bf16 v[152:155], v[100:103], v[32:35], v[152:155]\n\tbuffer_load_dwordx4 a[108:111], v8, s[8:11], s68 offen offset:0 sc1\n\tv_mfma_f32_16x16x32_bf16 v[160:163], v[104:107], v[32:35], v[160:163]\n\ts_waitcnt lgkmcnt(8)\n\tv_mfma_f32_16x16x32_bf16 v[148:151], v[96:99], v[36:39], v[148:151]\n\t" \
    "ds_read_b64_tr_b16 v[24:25], v6 offset:12288\n\tds_read_b64_tr_b16 v[26:27], v6 offset:14336\n\tv_mfma_f32_16x16x32_bf16 v[156:159], v[100:103], v[36:39], v[156:159]\n\tv_mfma_f32_16x16x32_bf16 v[164:167], v[104:107], v[36:39], v[164:167]\n\ts_waitcnt lgkmcnt(8)\n\tv_mfma_f32_16x16x32_bf16 v[168:171], v[96:99], v[40:43], v[168:171]\n\tds_read_b64_tr_b16 v[28:29], v7 offset:12288\n\tds_read_b64_tr_b16 v[30:31], v7 offset:14336\n\t" \
    "v_mfma_f32_16x16x32_bf16 v[176:179], v[100:103], v[40:43], v[176:179]\n\tbuffer_load_dwordx4 a[112:115], v8, s[8:11], s68 offen offset:1024 sc1\n\tv_mfma_f32_16x16x32_bf16 v[184:187], v[104:107], v[40:43], v[184:187]\n\ts_waitcnt lgkmcnt(8)\n\tv_mfma_f32_16x16x32_bf16 v[172:175], v[96:99], v[44:47], v[172:175]\n\tds_read_b64_tr_b16 v[32:33], v224 offset:12288\n\tds_read_b64_tr_b16 v[34:35], v224 offset:14336\n\tv_mfma_f32_16x16x32_bf16 v[180:183], v[100:103], v[44:47], v[180:183]\n\t" \
    "buffer_load_dwordx4 a[116:119], v8, s[8:11], s68 offen offset:2048 sc1\n\ts_mov_b32 s69, 3014656\n\ts_add_u32 m0, s54, 49152\n\tv_mfma_f32_16x16x32_bf16 v[188:191], v[104:107], v[44:47], v[188:191]\n\ts_barrier\n\ts_waitcnt vmcnt(31) lgkmcnt(8)\n\tv_mfma_f32_16x16x32_bf16 v[120:123], a[56:59], v[16:19], v[120:123]\n\tds_read_b64_tr_b16 v[36:37], v225 offset:12288\n\t" \
    "ds_read_b64_tr_b16 v[38:39], v225 offset:14336\n\tv_mfma_f32_16x16x32_bf16 v[128:131], a[60:63], v[16:19], v[128:131]\n\tbuffer_load_dwordx4 v12, s[16:19], s69 offen lds\n\ts_mov_b32 s69, 3014656\n\ts_add_u32 m0, s54, 114688\n\tv_mfma_f32_16x16x32_bf16 v[136:139], a[64:67], v[16:19], v[136:139]\n\ts_waitcnt lgkmcnt(8)\n\tv_mfma_f32_16x16x32_bf16 v[124:127], a[56:59], v[20:23], v[124:127]\n\t" \
    "ds_read_b64_tr_b16 v[40:41], v4 offset:16384\n\tds_read_b64_tr_b16 v[42:43], v4 offset:18432\n\tv_mfma_f32_16x16x32_bf16 v[132:135], a[60:63], v[20:23], v[132:135]\n\tv_mfma_f32_16x16x32_bf16 v[140:143], a[64:67], v[20:23], v[140:143]\n\ts_waitcnt lgkmcnt(8)\n\tv_mfma_f32_16x16x32_bf16 v[144:147], a[56:59], v[24:27], v[144:147]\n\tds_read_b64_tr_b16 v[44:45], v5 offset:16384\n\tds_read_b64_tr_b16 v[46:47], v5 offset:18432\n\t" \
    "v_mfma_f32_16x16x32_bf16 v[152:155], a[60:63], v[24:27], v[152:155]\n\tbuffer_load_dwordx4 v13, s[16:19], s69 offen lds\n\ts_mov_b32 s69, 2883584\n\ts_add_u32 m0, s54, 53248\n\tv_mfma_f32_16x16x32_bf16 v[160:163], a[64:67], v[24:27], v[160:163]\n\ts_waitcnt lgkmcnt(8)\n\tv_mfma_f32_16x16x32_bf16 v[148:151], a[56:59], v[28:31], v[148:151]\n\tds_read_b64_tr_b16 v[16:17], v6 offset:16384\n\t" \
    "ds_read_b64_tr_b16 v[18:19], v6 offset:18432\n\tv_mfma_f32_16x16x32_bf16 v[156:159], a[60:63], v[28:31], v[156:159]\n\tbuffer_load_dwordx4 v12, s[16:19], s69 offen lds\n\ts_mov_b32 s69, 2883584\n\ts_add_u32 m0, s54, 118784\n\tv_mfma_f32_16x16x32_bf16 v[164:167], a[64:67], v[28:31], v[164:167]\n\ts_waitcnt lgkmcnt(8)\n\tv_mfma_f32_16x16x32_bf16 v[168:171], a[56:59], v[32:35], v[168:171]\n\t" \
    "ds_read_b64_tr_b16 v[20:21], v7 offset:16384\n\tds_read_b64_tr_b16 v[22:23], v7 offset:18432\n\tv_mfma_f32_16x16x32_bf16 v[176:179], a[60:63], v[32:35], v[176:179]\n\tv_mfma_f32_16x16x32_bf16 v[184:187], a[64:67], v[32:35], v[184:187]\n\ts_waitcnt lgkmcnt(8)\n\tv_mfma_f32_16x16x32_bf16 v[172:175], a[56:59], v[36:39], v[172:175]\n\tds_read_b64_tr_b16 v[24:25], v224 offset:16384\n\tds_read_b64_tr_b16 v[26:27], v224 offset:18432\n\t" \
    "v_mfma_f32_16x16x32_bf16 v[180:183], a[60:63], v[36:39], v[180:183]\n\tbuffer_load_dwordx4 v13, s[16:19], s69 offen lds\n\ts_mov_b32 s69, 3538944\n\ts_add_u32 m0, s54, 57344\n\tv_mfma_f32_16x16x32_bf16 v[188:191], a[64:67], v[36:39], v[188:191]\n\ts_waitcnt vmcnt(32) lgkmcnt(8)\n\tv_mfma_f32_16x16x32_bf16 v[120:123], a[0:3], v[40:43], v[120:123]\n\tds_read_b64_tr_b16 v[28:29], v225 offset:16384\n\t" \
    "ds_read_b64_tr_b16 v[30:31], v225 offset:18432\n\tv_mfma_f32_16x16x32_bf16 v[128:131], a[4:7], v[40:43], v[128:131]\n\tbuffer_load_dwordx4 v12, s[16:19], s69 offen lds\n\ts_mov_b32 s69, 3407872\n\ts_add_u32 m0, s54, 122880\n\tv_mfma_f32_16x16x32_bf16 v[136:139], a[68:71], v[40:43], v[136:139]\n\ts_waitcnt lgkmcnt(8)\n\tv_mfma_f32_16x16x32_bf16 v[124:127], a[0:3], v[44:47], v[124:127]\n\t" \
    "ds_read_b64_tr_b16 v[32:33], v4 offset:20480\n\tds_read_b64_tr_b16 v[34:35], v4 offset:22528\n\tv_mfma_f32_16x16x32_bf16 v[132:135], a[4:7], v[44:47], v[132:135]\n\tv_mfma_f32_16x16x32_bf16 v[140:143], a[68:71], v[44:47], v[140:143]\n\ts_waitcnt lgkmcnt(8)\n\tv_mfma_f32_16x16x32_bf16 v[144:147], a[0:3], v[16:19], v[144:147]\n\tds_read_b64_tr_b16 v[36:37], v5 offset:20480\n\tds_read_b64_tr_b16 v[38:39], v5 offset:22528\n\t" \
    "v_mfma_f32_16x16x32_bf16 v[152:155], a[4:7], v[16:19], v[152:155]\n\tbuffer_load_dwordx4 v12, s[16:19], s69 offen lds\n\ts_add_u32 s68, s46, 82944\n\tv_mfma_f32_16x16x32_bf16 v[160:163], a[68:71], v[16:19], v[160:163]\n\ts_waitcnt lgkmcnt(8)\n\tv_mfma_f32_16x16x32_bf16 v[148:151], a[0:3], v[20:23], v[148:151]\n\tds_read_b64_tr_b16 v[40:41], v6 offset:20480\n\tds_read_b64_tr_b16 v[42:43], v6 offset:22528\n\t" \
    "v_mfma_f32_16x16x32_bf16 v[156:159], a[4:7], v[20:23], v[156:159]\n\tbuffer_load_dwordx4 a[120:123], v8, s[8:11], s68 offen offset:0 sc1\n\tv_mfma_f32_16x16x32_bf16 v[164:167], a[68:71], v[20:23], v[164:167]\n\ts_waitcnt lgkmcnt(8)\n\tv_mfma_f32_16x16x32_bf16 v[168:171], a[0:3], v[24:27], v[168:171]\n\tds_read_b64_tr_b16 v[44:45], v7 offset:20480\n\tds_read_b64_tr_b16 v[46:47], v7 offset:22528\n\tv_mfma_f32_16x16x32_bf16 v[176:179], a[4:7], v[24:27], v[176:179]\n\t" \
    "v_mfma_f32_16x16x32_bf16 v[184:187], a[68:71], v[24:27], v[184:187]\n\ts_waitcnt lgkmcnt(8)\n\tv_mfma_f32_16x16x32_bf16 v[172:175], a[0:3], v[28:31], v[172:175]\n\tds_read_b64_tr_b16 v[16:17], v224 offset:20480\n\tds_read_b64_tr_b16 v[18:19], v224 offset:22528\n\tv_mfma_f32_16x16x32_bf16 v[180:183], a[4:7], v[28:31], v[180:183]\n\tbuffer_load_dwordx4 a[124:127], v8, s[8:11], s68 offen offset:1024 sc1\n\tv_mfma_f32_16x16x32_bf16 v[188:191], a[68:71], v[28:31], v[188:191]\n\t" \
    "s_waitcnt vmcnt(33) lgkmcnt(8)\n\tv_mfma_f32_16x16x32_bf16 v[120:123], a[72:75], v[32:35], v[120:123]\n\tds_read_b64_tr_b16 v[20:21], v225 offset:20480\n\tds_read_b64_tr_b16 v[22:23], v225 offset:22528\n\tv_mfma_f32_16x16x32_bf16 v[128:131], a[76:79], v[32:35], v[128:131]\n\tbuffer_load_dwordx4 a[128:131], v8, s[8:11], s68 offen offset:2048 sc1\n\ts_add_u32 s68, s46, 86016\n\tv_mfma_f32_16x16x32_bf16 v[136:139], a[80:83], v[32:35], v[136:139]\n\t" \
    "s_waitcnt lgkmcnt(8)\n\tv_mfma_f32_16x16x32_bf16 v[124:127], a[72:75], v[36:39], v[124:127]\n\tds_read_b64_tr_b16 v[24:25], v4 offset:24576\n\tds_read_b64_tr_b16 v[26:27], v4 offset:26624\n\tv_mfma_f32_16x16x32_bf16 v[132:135], a[76:79], v[36:39], v[132:135]\n\tv_mfma_f32_16x16x32_bf16 v[140:143], a[80:83], v[36:39], v[140:143]\n\ts_waitcnt lgkmcnt(8)\n\tv_mfma_f32_16x16x32_bf16 v[144:147], a[72:75], v[40:43], v[144:147]\n\t" \
    "ds_read_b64_tr_b16 v[28:29], v5 offset:24576\n\tds_read_b64_tr_b16 v[30:31], v5 offset:26624\n\tv_mfma_f32_16x16x32_bf16 v[152:155], a[76:79], v[40:43], v[152:155]\n\tbuffer_load_dwordx4 a[132:135], v8, s[8:11], s68 offen offset:0 sc1\n\tv_mfma_f32_16x16x32_bf16 v[160:163], a[80:83], v[40:43], v[160:163]\n\ts_waitcnt lgkmcnt(8)\n\tv_mfma_f32_16x16x32_bf16 v[148:151], a[72:75], v[44:47], v[148:151]\n\tds_read_b64_tr_b16 v[32:33], v6 offset:24576\n\t" \
    "ds_read_b64_tr_b16 v[34:35], v6 offset:26624\n\tv_mfma_f32_16x16x32_bf16 v[156:159], a[76:79], v[44:47], v[156:159]\n\tbuffer_load_dwordx4 a[136:139], v8, s[8:11], s68 offen offset:1024 sc1\n\tv_mfma_f32_16x16x32_bf16 v[164:167], a[80:83], v[44:47], v[164:167]\n\ts_waitcnt vmcnt(31)\n\ts_barrier\n\ts_waitcnt lgkmcnt(8)\n\tv_mfma_f32_16x16x32_bf16 v[168:171], a[72:75], v[16:19], v[168:171]\n\t" \
    "ds_read_b64_tr_b16 v[36:37], v7 offset:24576\n\tds_read_b64_tr_b16 v[38:39], v7 offset:26624\n\tv_mfma_f32_16x16x32_bf16 v[176:179], a[76:79], v[16:19], v[176:179]\n\tbuffer_load_dwordx4 a[140:143], v8, s[8:11], s68 offen offset:2048 sc1\n\ts_add_u32 s68, s46, 89088\n\tv_mfma_f32_16x16x32_bf16 v[184:187], a[80:83], v[16:19], v[184:187]\n\ts_waitcnt lgkmcnt(8)\n\tv_mfma_f32_16x16x32_bf16 v[172:175], a[72:75], v[20:23], v[172:175]\n\t" \
    "ds_read_b64_tr_b16 v[40:41], v224 offset:24576\n\tds_read_b64_tr_b16 v[42:43], v224 offset:26624\n\tv_mfma_f32_16x16x32_bf16 v[180:183], a[76:79], v[20:23], v[180:183]\n\tv_mfma_f32_16x16x32_bf16 v[188:191], a[80:83], v[20:23], v[188:191]\n\ts_waitcnt vmcnt(29) lgkmcnt(8)\n\tv_mfma_f32_16x16x32_bf16 v[120:123], a[8:11], v[24:27], v[120:123]\n\tds_read_b64_tr_b16 v[44:45], v225 offset:24576\n\tds_read_b64_tr_b16 v[46:47], v225 offset:26624\n\t" \
    "v_mfma_f32_16x16x32_bf16 v[128:131], a[12:15], v[24:27], v[128:131]\n\tbuffer_load_dwordx4 a[144:147], v8, s[8:11], s68 offen offset:0 sc1\n\tv_mfma_f32_16x16x32_bf16 v[136:139], a[16:19], v[24:27], v[136:139]\n\ts_waitcnt lgkmcnt(8)\n\tv_mfma_f32_16x16x32_bf16 v[124:127], a[8:11], v[28:31], v[124:127]\n\tds_read_b64_tr_b16 v[16:17], v4 offset:28672\n\tds_read_b64_tr_b16 v[18:19], v4 offset:30720\n\tv_mfma_f32_16x16x32_bf16 v[132:135], a[12:15], v[28:31], v[132:135]\n\t" \
    "buffer_load_dwordx4 a[148:151], v8, s[8:11], s68 offen offset:1024 sc1\n\tv_mfma_f32_16x16x32_bf16 v[140:143], a[16:19], v[28:31], v[140:143]\n\ts_waitcnt lgkmcnt(8)\n\tv_mfma_f32_16x16x32_bf16 v[144:147], a[8:11], v[32:35], v[144:147]\n\tds_read_b64_tr_b16 v[20:21], v5 offset:28672\n\tds_read_b64_tr_b16 v[22:23], v5 offset:30720\n\tv_mfma_f32_16x16x32_bf16 v[152:155], a[12:15], v[32:35], v[152:155]\n\tv_mfma_f32_16x16x32_bf16 v[160:163], a[16:19], v[32:35], v[160:163]\n\t" \
    "s_waitcnt lgkmcnt(8)\n\tv_mfma_f32_16x16x32_bf16 v[148:151], a[8:11], v[36:39], v[148:151]\n\tds_read_b64_tr_b16 v[24:25], v6 offset:28672\n\tds_read_b64_tr_b16 v[26:27], v6 offset:30720\n\tv_mfma_f32_16x16x32_bf16 v[156:159], a[12:15], v[36:39], v[156:159]\n\tbuffer_load_dwordx4 a[152:155], v8, s[8:11], s68 offen offset:2048 sc1\n\ts_mov_b32 s69, 4063232\n\ts_add_u32 m0, s54, 61440\n\t" \
    "v_mfma_f32_16x16x32_bf16 v[164:167], a[16:19], v[36:39], v[164:167]\n\ts_waitcnt lgkmcnt(8)\n\tv_mfma_f32_16x16x32_bf16 v[168:171], a[8:11], v[40:43], v[168:171]\n\tds_read_b64_tr_b16 v[28:29], v7 offset:28672\n\tds_read_b64_tr_b16 v[30:31], v7 offset:30720\n\tv_mfma_f32_16x16x32_bf16 v[176:179], a[12:15], v[40:43], v[176:179]\n\tbuffer_load_dwordx4 v12, s[16:19], s69 offen lds\n\ts_mov_b32 s69, 3932160\n\t" \
    "s_add_u32 m0, s54, 0\n\tv_mfma_f32_16x16x32_bf16 v[184:187], a[16:19], v[40:43], v[184:187]\n\ts_waitcnt lgkmcnt(8)\n\tv_mfma_f32_16x16x32_bf16 v[172:175], a[8:11], v[44:47], v[172:175]\n\tds_read_b64_tr_b16 v[32:33], v224 offset:28672\n\tds_read_b64_tr_b16 v[34:35], v224 offset:30720\n\tv_mfma_f32_16x16x32_bf16 v[180:183], a[12:15], v[44:47], v[180:183]\n\tv_mfma_f32_16x16x32_bf16 v[188:191], a[16:19], v[44:47], v[188:191]\n\t" \
    "s_waitcnt vmcnt(30) lgkmcnt(8)\n\tv_mfma_f32_16x16x32_bf16 v[120:123], a[20:23], v[16:19], v[120:123]\n\tds_read_b64_tr_b16 v[36:37], v225 offset:28672\n\tds_read_b64_tr_b16 v[38:39], v225 offset:30720\n\tv_mfma_f32_16x16x32_bf16 v[128:131], a[24:27], v[16:19], v[128:131]\n\tbuffer_load_dwordx4 v12, s[16:19], s69 offen lds\n\ts_mov_b32 s69, 4587520\n\ts_add_u32 m0, s54, 4096\n\t" \
    "v_mfma_f32_16x16x32_bf16 v[136:139], a[28:31], v[16:19], v[136:139]\n\ts_waitcnt lgkmcnt(8)\n\tv_mfma_f32_16x16x32_bf16 v[124:127], a[20:23], v[20:23], v[124:127]\n\tds_read_b64_tr_b16 v[40:41], v4 offset:32768\n\tds_read_b64_tr_b16 v[42:43], v4 offset:34816\n\tv_mfma_f32_16x16x32_bf16 v[132:135], a[24:27], v[20:23], v[132:135]\n\tbuffer_load_dwordx4 v12, s[16:19], s69 offen lds\n\ts_mov_b32 s69, 4456448\n\t" \
    "s_add_u32 m0, s54, 8192\n\tv_mfma_f32_16x16x32_bf16 v[140:143], a[28:31], v[20:23], v[140:143]\n\ts_waitcnt lgkmcnt(8)\n\tv_mfma_f32_16x16x32_bf16 v[144:147], a[20:23], v[24:27], v[144:147]\n\tds_read_b64_tr_b16 v[44:45], v5 offset:32768\n\tds_read_b64_tr_b16 v[46:47], v5 offset:34816\n\tv_mfma_f32_16x16x32_bf16 v[152:155], a[24:27], v[24:27], v[152:155]\n\tv_mfma_f32_16x16x32_bf16 v[160:163], a[28:31], v[24:27], v[160:163]\n\t" \
    "s_waitcnt lgkmcnt(8)\n\tv_mfma_f32_16x16x32_bf16 v[148:151], a[20:23], v[28:31], v[148:151]\n\tds_read_b64_tr_b16 v[16:17], v6 offset:32768\n\tds_read_b64_tr_b16 v[18:19], v6 offset:34816\n\tv_mfma_f32_16x16x32_bf16 v[156:159], a[24:27], v[28:31], v[156:159]\n\tbuffer_load_dwordx4 v12, s[16:19], s69 offen lds\n\ts_add_u32 s68, s46, 92160\n\tv_mfma_f32_16x16x32_bf16 v[164:167], a[28:31], v[28:31], v[164:167]\n\t" \
    "s_waitcnt lgkmcnt(8)\n\tv_mfma_f32_16x16x32_bf16 v[168:171], a[20:23], v[32:35], v[168:171]\n\tds_read_b64_tr_b16 v[20:21], v7 offset:32768\n\tds_read_b64_tr_b16 v[22:23], v7 offset:34816\n\tv_mfma_f32_16x16x32_bf16 v[176:179], a[24:27], v[32:35], v[176:179]\n\tbuffer_load_dwordx4 a[156:159], v8, s[8:11], s68 offen offset:0 sc1\n\tv_mfma_f32_16x16x32_bf16 v[184:187], a[28:31], v[32:35], v[184:187]\n\ts_waitcnt lgkmcnt(8)\n\t" \
    "v_mfma_f32_16x16x32_bf16 v[172:175], a[20:23], v[36:39], v[172:175]\n\tds_read_b64_tr_b16 v[24:25], v224 offset:32768\n\tds_read_b64_tr_b16 v[26:27], v224 offset:34816\n\tv_mfma_f32_16x16x32_bf16 v[180:183], a[24:27], v[36:39], v[180:183]\n\tv_mfma_f32_16x16x32_bf16 v[188:191], a[28:31], v[36:39], v[188:191]\n\ts_waitcnt vmcnt(31) lgkmcnt(8)\n\tv_mfma_f32_16x16x32_bf16 v[120:123], a[84:87], v[40:43], v[120:123]\n\tds_read_b64_tr_b16 v[28:29], v225 offset:32768\n\t" \
    "ds_read_b64_tr_b16 v[30:31], v225 offset:34816\n\tv_mfma_f32_16x16x32_bf16 v[128:131], a[88:91], v[40:43], v[128:131]\n\tbuffer_load_dwordx4 a[160:163], v8, s[8:11], s68 offen offset:1024 sc1\n\tv_mfma_f32_16x16x32_bf16 v[136:139], a[92:95], v[40:43], v[136:139]\n\ts_waitcnt lgkmcnt(8)\n\tv_mfma_f32_16x16x32_bf16 v[124:127], a[84:87], v[44:47], v[124:127]\n\tds_read_b64_tr_b16 v[32:33], v4 offset:36864\n\tds_read_b64_tr_b16 v[34:35], v4 offset:38912\n\t" \
    "v_mfma_f32_16x16x32_bf16 v[132:135], a[88:91], v[44:47], v[132:135]\n\tv_mfma_f32_16x16x32_bf16 v[140:143], a[92:95], v[44:47], v[140:143]\n\ts_waitcnt vmcnt(27)\n\ts_barrier\n\ts_waitcnt lgkmcnt(8)\n\tv_mfma_f32_16x16x32_bf16 v[144:147], a[84:87], v[16:19], v[144:147]\n\tds_read_b64_tr_b16 v[36:37], v5 offset:36864\n\tds_read_b64_tr_b16 v[38:39], v5 offset:38912\n\t" \
    "v_mfma_f32_16x16x32_bf16 v[152:155], a[88:91], v[16:19], v[152:155]\n\tbuffer_load_dwordx4 a[164:167], v8, s[8:11], s68 offen offset:2048 sc1\n\ts_add_u32 s68, s46, 95232\n\tv_mfma_f32_16x16x32_bf16 v[160:163], a[92:95], v[16:19], v[160:163]\n\ts_waitcnt lgkmcnt(8)\n\tv_mfma_f32_16x16x32_bf16 v[148:151], a[84:87], v[20:23], v[148:151]\n\tds_read_b64_tr_b16 v[40:41], v6 offset:36864\n\tds_read_b64_tr_b16 v[42:43], v6 offset:38912\n\t" \
    "v_mfma_f32_16x16x32_bf16 v[156:159], a[88:91], v[20:23], v[156:159]\n\tbuffer_load_dwordx4 a[168:171], v8, s[8:11], s68 offen offset:0 sc1\n\tv_mfma_f32_16x16x32_bf16 v[164:167], a[92:95], v[20:23], v[164:167]\n\ts_waitcnt lgkmcnt(8)\n\tv_mfma_f32_16x16x32_bf16 v[168:171], a[84:87], v[24:27], v[168:171]\n\tds_read_b64_tr_b16 v[44:45], v7 offset:36864\n\tds_read_b64_tr_b16 v[46:47], v7 offset:38912\n\tv_mfma_f32_16x16x32_bf16 v[176:179], a[88:91], v[24:27], v[176:179]\n\t" \
    "buffer_load_dwordx4 a[172:175], v8, s[8:11], s68 offen offset:1024 sc1\n\tv_mfma_f32_16x16x32_bf16 v[184:187], a[92:95], v[24:27], v[184:187]\n\ts_waitcnt lgkmcnt(8)\n\tv_mfma_f32_16x16x32_bf16 v[172:175], a[84:87], v[28:31], v[172:175]\n\tds_read_b64_tr_b16 v[16:17], v224 offset:36864\n\tds_read_b64_tr_b16 v[18:19], v224 offset:38912\n\tv_mfma_f32_16x16x32_bf16 v[180:183], a[88:91], v[28:31], v[180:183]\n\tbuffer_load_dwordx4 a[176:179], v8, s[8:11], s68 offen offset:2048 sc1\n\t" \
    "s_add_u32 s68, s46, 98304\n\tv_mfma_f32_16x16x32_bf16 v[188:191], a[92:95], v[28:31], v[188:191]\n\ts_waitcnt vmcnt(28) lgkmcnt(8)\n\tv_mfma_f32_16x16x32_bf16 v[120:123], a[96:99], v[32:35], v[120:123]\n\tds_read_b64_tr_b16 v[20:21], v225 offset:36864\n\tds_read_b64_tr_b16 v[22:23], v225 offset:38912\n\tv_mfma_f32_16x16x32_bf16 v[128:131], a[100:103], v[32:35], v[128:131]\n\tv_mfma_f32_16x16x32_bf16 v[136:139], a[104:107], v[32:35], v[136:139]\n\t" \
    "s_waitcnt lgkmcnt(8)\n\tv_mfma_f32_16x16x32_bf16 v[124:127], a[96:99], v[36:39], v[124:127]\n\tds_read_b64_tr_b16 v[24:25], v4 offset:40960\n\tds_read_b64_tr_b16 v[26:27], v4 offset:43008\n\tv_mfma_f32_16x16x32_bf16 v[132:135], a[100:103], v[36:39], v[132:135]\n\tbuffer_load_dwordx4 a[180:183], v8, s[8:11], s68 offen offset:0 sc1\n\tv_mfma_f32_16x16x32_bf16 v[140:143], a[104:107], v[36:39], v[140:143]\n\ts_waitcnt lgkmcnt(8)\n\t" \
    "v_mfma_f32_16x16x32_bf16 v[144:147], a[96:99], v[40:43], v[144:147]\n\tds_read_b64_tr_b16 v[28:29], v5 offset:40960\n\tds_read_b64_tr_b16 v[30:31], v5 offset:43008\n\tv_mfma_f32_16x16x32_bf16 v[152:155], a[100:103], v[40:43], v[152:155]\n\tbuffer_load_dwordx4 a[184:187], v8, s[8:11], s68 offen offset:1024 sc1\n\tv_mfma_f32_16x16x32_bf16 v[160:163], a[104:107], v[40:43], v[160:163]\n\ts_waitcnt lgkmcnt(8)\n\tv_mfma_f32_16x16x32_bf16 v[148:151], a[96:99], v[44:47], v[148:151]\n\t" \
    "ds_read_b64_tr_b16 v[32:33], v6 offset:40960\n\tds_read_b64_tr_b16 v[34:35], v6 offset:43008\n\tv_mfma_f32_16x16x32_bf16 v[156:159], a[100:103], v[44:47], v[156:159]\n\tv_mfma_f32_16x16x32_bf16 v[164:167], a[104:107], v[44:47], v[164:167]\n\ts_waitcnt lgkmcnt(8)\n\tv_mfma_f32_16x16x32_bf16 v[168:171], a[96:99], v[16:19], v[168:171]\n\tds_read_b64_tr_b16 v[36:37], v7 offset:40960\n\tds_read_b64_tr_b16 v[38:39], v7 offset:43008\n\t" \
    "v_mfma_f32_16x16x32_bf16 v[176:179], a[100:103], v[16:19], v[176:179]\n\tbuffer_load_dwordx4 a[188:191], v8, s[8:11], s68 offen offset:2048 sc1\n\ts_add_u32 s68, s46, 101376\n\tv_mfma_f32_16x16x32_bf16 v[184:187], a[104:107], v[16:19], v[184:187]\n\ts_waitcnt lgkmcnt(8)\n\tv_mfma_f32_16x16x32_bf16 v[172:175], a[96:99], v[20:23], v[172:175]\n\tds_read_b64_tr_b16 v[40:41], v224 offset:40960\n\tds_read_b64_tr_b16 v[42:43], v224 offset:43008\n\t" \
    "v_mfma_f32_16x16x32_bf16 v[180:183], a[100:103], v[20:23], v[180:183]\n\tv_mfma_f32_16x16x32_bf16 v[188:191], a[104:107], v[20:23], v[188:191]\n\ts_waitcnt vmcnt(28) lgkmcnt(8)\n\tv_mfma_f32_16x16x32_bf16 v[120:123], a[108:111], v[24:27], v[120:123]\n\tds_read_b64_tr_b16 v[44:45], v225 offset:40960\n\tds_read_b64_tr_b16 v[46:47], v225 offset:43008\n\tv_mfma_f32_16x16x32_bf16 v[128:131], a[112:115], v[24:27], v[128:131]\n\tbuffer_load_dwordx4 a[192:195], v8, s[8:11], s68 offen offset:0 sc1\n\t" \
    "v_mfma_f32_16x16x32_bf16 v[136:139], a[116:119], v[24:27], v[136:139]\n\ts_waitcnt lgkmcnt(8)\n\tv_mfma_f32_16x16x32_bf16 v[124:127], a[108:111], v[28:31], v[124:127]\n\tds_read_b64_tr_b16 v[16:17], v4 offset:45056\n\tds_read_b64_tr_b16 v[18:19], v4 offset:47104\n\tv_mfma_f32_16x16x32_bf16 v[132:135], a[112:115], v[28:31], v[132:135]\n\tbuffer_load_dwordx4 a[196:199], v8, s[8:11], s68 offen offset:1024 sc1\n\tv_mfma_f32_16x16x32_bf16 v[140:143], a[116:119], v[28:31], v[140:143]\n\t" \
    "s_waitcnt lgkmcnt(8)\n\tv_mfma_f32_16x16x32_bf16 v[144:147], a[108:111], v[32:35], v[144:147]\n\tds_read_b64_tr_b16 v[20:21], v5 offset:45056\n\tds_read_b64_tr_b16 v[22:23], v5 offset:47104\n\tv_mfma_f32_16x16x32_bf16 v[152:155], a[112:115], v[32:35], v[152:155]\n\tv_mfma_f32_16x16x32_bf16 v[160:163], a[116:119], v[32:35], v[160:163]\n\ts_waitcnt lgkmcnt(8)\n\tv_mfma_f32_16x16x32_bf16 v[148:151], a[108:111], v[36:39], v[148:151]\n\t" \
    "ds_read_b64_tr_b16 v[24:25], v6 offset:45056\n\tds_read_b64_tr_b16 v[26:27], v6 offset:47104\n\tv_mfma_f32_16x16x32_bf16 v[156:159], a[112:115], v[36:39], v[156:159]\n\tbuffer_load_dwordx4 a[200:203], v8, s[8:11], s68 offen offset:2048 sc1\n\ts_add_u32 s68, s46, 104448\n\tv_mfma_f32_16x16x32_bf16 v[164:167], a[116:119], v[36:39], v[164:167]\n\ts_waitcnt lgkmcnt(8)\n\tv_mfma_f32_16x16x32_bf16 v[168:171], a[108:111], v[40:43], v[168:171]\n\t" \
    "ds_read_b64_tr_b16 v[28:29], v7 offset:45056\n\tds_read_b64_tr_b16 v[30:31], v7 offset:47104\n\tv_mfma_f32_16x16x32_bf16 v[176:179], a[112:115], v[40:43], v[176:179]\n\tbuffer_load_dwordx4 a[204:207], v8, s[8:11], s68 offen offset:0 sc1\n\tv_mfma_f32_16x16x32_bf16 v[184:187], a[116:119], v[40:43], v[184:187]\n\ts_waitcnt lgkmcnt(8)\n\tv_mfma_f32_16x16x32_bf16 v[172:175], a[108:111], v[44:47], v[172:175]\n\tds_read_b64_tr_b16 v[32:33], v224 offset:45056\n\t" \
    "ds_read_b64_tr_b16 v[34:35], v224 offset:47104\n\tv_mfma_f32_16x16x32_bf16 v[180:183], a[112:115], v[44:47], v[180:183]\n\tv_mfma_f32_16x16x32_bf16 v[188:191], a[116:119], v[44:47], v[188:191]\n\ts_waitcnt vmcnt(26)\n\ts_barrier\n\ts_waitcnt vmcnt(23) lgkmcnt(8)\n\tv_mfma_f32_16x16x32_bf16 v[120:123], a[120:123], v[16:19], v[120:123]\n\tds_read_b64_tr_b16 v[36:37], v225 offset:45056\n\t" \
    "ds_read_b64_tr_b16 v[38:39], v225 offset:47104\n\tv_mfma_f32_16x16x32_bf16 v[128:131], a[124:127], v[16:19], v[128:131]\n\tbuffer_load_dwordx4 a[208:211], v8, s[8:11], s68 offen offset:1024 sc1\n\tv_mfma_f32_16x16x32_bf16 v[136:139], a[128:131], v[16:19], v[136:139]\n\ts_waitcnt lgkmcnt(8)\n\tv_mfma_f32_16x16x32_bf16 v[124:127], a[120:123], v[20:23], v[124:127]\n\tds_read_b64_tr_b16 v[40:41], v4 offset:49152\n\tds_read_b64_tr_b16 v[42:43], v4 offset:51200\n\t" \
    "v_mfma_f32_16x16x32_bf16 v[132:135], a[124:127], v[20:23], v[132:135]\n\tbuffer_load_dwordx4 a[212:215], v8, s[8:11], s68 offen offset:2048 sc1\n\ts_add_u32 s68, s46, 107520\n\tv_mfma_f32_16x16x32_bf16 v[140:143], a[128:131], v[20:23], v[140:143]\n\ts_waitcnt lgkmcnt(8)\n\tv_mfma_f32_16x16x32_bf16 v[144:147], a[120:123], v[24:27], v[144:147]\n\tds_read_b64_tr_b16 v[44:45], v5 offset:49152\n\tds_read_b64_tr_b16 v[46:47], v5 offset:51200\n\t" \
    "v_mfma_f32_16x16x32_bf16 v[152:155], a[124:127], v[24:27], v[152:155]\n\tbuffer_load_dwordx4 a[216:219], v8, s[8:11], s68 offen offset:0 sc1\n\tv_mfma_f32_16x16x32_bf16 v[160:163], a[128:131], v[24:27], v[160:163]\n\ts_waitcnt lgkmcnt(8)\n\tv_mfma_f32_16x16x32_bf16 v[148:151], a[120:123], v[28:31], v[148:151]\n\tds_read_b64_tr_b16 v[16:17], v6 offset:49152\n\tds_read_b64_tr_b16 v[18:19], v6 offset:51200\n\tv_mfma_f32_16x16x32_bf16 v[156:159], a[124:127], v[28:31], v[156:159]\n\t" \
    "buffer_load_dwordx4 a[220:223], v8, s[8:11], s68 offen offset:1024 sc1\n\tv_mfma_f32_16x16x32_bf16 v[164:167], a[128:131], v[28:31], v[164:167]\n\ts_waitcnt lgkmcnt(8)\n\tv_mfma_f32_16x16x32_bf16 v[168:171], a[120:123], v[32:35], v[168:171]\n\tds_read_b64_tr_b16 v[20:21], v7 offset:49152\n\tds_read_b64_tr_b16 v[22:23], v7 offset:51200\n\tv_mfma_f32_16x16x32_bf16 v[176:179], a[124:127], v[32:35], v[176:179]\n\tv_mfma_f32_16x16x32_bf16 v[184:187], a[128:131], v[32:35], v[184:187]\n\t" \
    "s_waitcnt lgkmcnt(8)\n\tv_mfma_f32_16x16x32_bf16 v[172:175], a[120:123], v[36:39], v[172:175]\n\tds_read_b64_tr_b16 v[24:25], v224 offset:49152\n\tds_read_b64_tr_b16 v[26:27], v224 offset:51200\n\tv_mfma_f32_16x16x32_bf16 v[180:183], a[124:127], v[36:39], v[180:183]\n\tbuffer_load_dwordx4 a[224:227], v8, s[8:11], s68 offen offset:2048 sc1\n\ts_mov_b32 s69, 3538944\n\ts_add_u32 m0, s54, 12288\n\t" \
    "v_mfma_f32_16x16x32_bf16 v[188:191], a[128:131], v[36:39], v[188:191]\n\ts_waitcnt vmcnt(25) lgkmcnt(8)\n\tv_mfma_f32_16x16x32_bf16 v[120:123], a[132:135], v[40:43], v[120:123]\n\tds_read_b64_tr_b16 v[28:29], v225 offset:49152\n\tds_read_b64_tr_b16 v[30:31], v225 offset:51200\n\tv_mfma_f32_16x16x32_bf16 v[128:131], a[136:139], v[40:43], v[128:131]\n\tv_mfma_f32_16x16x32_bf16 v[136:139], a[140:143], v[40:43], v[136:139]\n\ts_waitcnt lgkmcnt(8)\n\t" \
    "v_mfma_f32_16x16x32_bf16 v[124:127], a[132:135], v[44:47], v[124:127]\n\tds_read_b64_tr_b16 v[32:33], v4 offset:53248\n\tds_read_b64_tr_b16 v[34:35], v4 offset:55296\n\tv_mfma_f32_16x16x32_bf16 v[132:135], a[136:139], v[44:47], v[132:135]\n\tbuffer_load_dwordx4 v13, s[16:19], s69 offen lds\n\ts_mov_b32 s69, 3407872\n\ts_add_u32 m0, s54, 16384\n\tv_mfma_f32_16x16x32_bf16 v[140:143], a[140:143], v[44:47], v[140:143]\n\t" \
    "s_waitcnt lgkmcnt(8)\n\tv_mfma_f32_16x16x32_bf16 v[144:147], a[132:135], v[16:19], v[144:147]\n\tds_read_b64_tr_b16 v[36:37], v5 offset:53248\n\tds_read_b64_tr_b16 v[38:39], v5 offset:55296\n\tv_mfma_f32_16x16x32_bf16 v[152:155], a[136:139], v[16:19], v[152:155]\n\tbuffer_load_dwordx4 v13, s[16:19], s69 offen lds\n\ts_mov_b32 s69, 4063232\n\ts_add_u32 m0, s54, 20480\n\t" \
    "v_mfma_f32_16x16x32_bf16 v[160:163], a[140:143], v[16:19], v[160:163]\n\ts_waitcnt lgkmcnt(8)\n\tv_mfma_f32_16x16x32_bf16 v[148:151], a[132:135], v[20:23], v[148:151]\n\tds_read_b64_tr_b16 v[40:41], v6 offset:53248\n\tds_read_b64_tr_b16 v[42:43], v6 offset:55296\n\tv_mfma_f32_16x16x32_bf16 v[156:159], a[136:139], v[20:23], v[156:159]\n\tv_mfma_f32_16x16x32_bf16 v[164:167], a[140:143], v[20:23], v[164:167]\n\ts_waitcnt lgkmcnt(8)\n\t" \
    "v_mfma_f32_16x16x32_bf16 v[168:171], a[132:135], v[24:27], v[168:171]\n\tds_read_b64_tr_b16 v[44:45], v7 offset:53248\n\tds_read_b64_tr_b16 v[46:47], v7 offset:55296\n\tv_mfma_f32_16x16x32_bf16 v[176:179], a[136:139], v[24:27], v[176:179]\n\tbuffer_load_dwordx4 v13, s[16:19], s69 offen lds\n\ts_mov_b32 s69, 3932160\n\ts_add_u32 m0, s54, 24576\n\tv_mfma_f32_16x16x32_bf16 v[184:187], a[140:143], v[24:27], v[184:187]\n\t" \
    "s_waitcnt lgkmcnt(8)\n\tv_mfma_f32_16x16x32_bf16 v[172:175], a[132:135], v[28:31], v[172:175]\n\tds_read_b64_tr_b16 v[16:17], v224 offset:53248\n\tds_read_b64_tr_b16 v[18:19], v224 offset:55296\n\tv_mfma_f32_16x16x32_bf16 v[180:183], a[136:139], v[28:31], v[180:183]\n\tbuffer_load_dwordx4 v13, s[16:19], s69 offen lds\n\ts_mov_b32 s69, 4587520\n\ts_add_u32 m0, s54, 28672\n\t" \
    "v_mfma_f32_16x16x32_bf16 v[188:191], a[140:143], v[28:31], v[188:191]\n\ts_waitcnt vmcnt(26) lgkmcnt(8)\n\tv_mfma_f32_16x16x32_bf16 v[120:123], a[144:147], v[32:35], v[120:123]\n\tds_read_b64_tr_b16 v[20:21], v225 offset:53248\n\tds_read_b64_tr_b16 v[22:23], v225 offset:55296\n\tv_mfma_f32_16x16x32_bf16 v[128:131], a[148:151], v[32:35], v[128:131]\n\tv_mfma_f32_16x16x32_bf16 v[136:139], a[152:155], v[32:35], v[136:139]\n\ts_waitcnt lgkmcnt(8)\n\t" \
    "v_mfma_f32_16x16x32_bf16 v[124:127], a[144:147], v[36:39], v[124:127]\n\tds_read_b64_tr_b16 v[24:25], v4 offset:57344\n\tds_read_b64_tr_b16 v[26:27], v4 offset:59392\n\tv_mfma_f32_16x16x32_bf16 v[132:135], a[148:151], v[36:39], v[132:135]\n\tbuffer_load_dwordx4 v13, s[16:19], s69 offen lds\n\ts_mov_b32 s69, 4456448\n\ts_add_u32 m0, s54, 32768\n\tv_mfma_f32_16x16x32_bf16 v[140:143], a[152:155], v[36:39], v[140:143]\n\t" \
    "s_waitcnt lgkmcnt(8)\n\tv_mfma_f32_16x16x32_bf16 v[144:147], a[144:147], v[40:43], v[144:147]\n\tds_read_b64_tr_b16 v[28:29], v5 offset:57344\n\tds_read_b64_tr_b16 v[30:31], v5 offset:59392\n\tv_mfma_f32_16x16x32_bf16 v[152:155], a[148:151], v[40:43], v[152:155]\n\tbuffer_load_dwordx4 v13, s[16:19], s69 offen lds\n\ts_mul_i32 s68, s38, 3072\n\tv_add_u32 v192, s68, v8\n\t" \
    "v_mfma_f32_16x16x32_bf16 v[160:163], a[152:155], v[40:43], v[160:163]\n\tv_lshrrev_b32 v193, 7, v192\n\tv_mul_u32_u24 v193, 171, v193\n\ts_waitcnt lgkmcnt(8)\n\tv_mfma_f32_16x16x32_bf16 v[148:151], a[144:147], v[44:47], v[148:151]\n\tds_read_b64_tr_b16 v[32:33], v224 offset:57344\n\tds_read_b64_tr_b16 v[34:35], v224 offset:59392\n\tv_lshrrev_b32 v193, 9, v193\n\t" \
    "v_mfma_f32_16x16x32_bf16 v[156:159], a[148:151], v[44:47], v[156:159]\n\tv_mul_u32_u24 v193, 768, v193\n\tv_add_u32 v192, v192, v193\n\ts_add_u32 m0, s68, 131072\n\tv_mfma_f32_16x16x32_bf16 v[164:167], a[152:155], v[44:47], v[164:167]\n\ts_add_u32 s68, s68, 1024\n\ts_waitcnt vmcnt(24)\n\ts_barrier\n\t" \
    "s_waitcnt lgkmcnt(8)\n\tv_mfma_f32_16x16x32_bf16 v[168:171], a[144:147], v[16:19], v[168:171]\n\tds_read_b64_tr_b16 v[36:37], v225 offset:57344\n\tds_read_b64_tr_b16 v[38:39], v225 offset:59392\n\tv_mfma_f32_16x16x32_bf16 v[176:179], a[148:151], v[16:19], v[176:179]\n\tbuffer_load_dwordx4 v192, s[20:23], 0 offen lds\n\tv_add_u32 v192, s68, v8\n\tv_lshrrev_b32 v193, 7, v192\n\t" \
    "v_mfma_f32_16x16x32_bf16 v[184:187], a[152:155], v[16:19], v[184:187]\n\tv_mul_u32_u24 v193, 171, v193\n\tv_lshrrev_b32 v193, 9, v193\n\ts_waitcnt lgkmcnt(8)\n\tv_mfma_f32_16x16x32_bf16 v[172:175], a[144:147], v[20:23], v[172:175]\n\tds_read_b64_tr_b16 v[40:41], v4 offset:61440\n\tds_read_b64_tr_b16 v[42:43], v4 offset:63488\n\tv_mul_u32_u24 v193, 768, v193\n\t" \
    "v_mfma_f32_16x16x32_bf16 v[180:183], a[148:151], v[20:23], v[180:183]\n\tv_add_u32 v192, v192, v193\n\ts_add_u32 m0, s68, 131072\n\ts_add_u32 s68, s68, 1024\n\tv_mfma_f32_16x16x32_bf16 v[188:191], a[152:155], v[20:23], v[188:191]\n\ts_waitcnt vmcnt(22) lgkmcnt(8)\n\tv_mfma_f32_16x16x32_bf16 v[120:123], a[156:159], v[24:27], v[120:123]\n\tds_read_b64_tr_b16 v[44:45], v5 offset:61440\n\t" \
    "ds_read_b64_tr_b16 v[46:47], v5 offset:63488\n\tv_mfma_f32_16x16x32_bf16 v[128:131], a[160:163], v[24:27], v[128:131]\n\tbuffer_load_dwordx4 v192, s[20:23], 0 offen lds\n\tv_add_u32 v192, s68, v8\n\tv_lshrrev_b32 v193, 7, v192\n\tv_mfma_f32_16x16x32_bf16 v[136:139], a[164:167], v[24:27], v[136:139]\n\tv_mul_u32_u24 v193, 171, v193\n\tv_lshrrev_b32 v193, 9, v193\n\t" \
    "s_waitcnt lgkmcnt(8)\n\tv_mfma_f32_16x16x32_bf16 v[124:127], a[156:159], v[28:31], v[124:127]\n\tds_read_b64_tr_b16 v[16:17], v4 offset:0\n\tds_read_b64_tr_b16 v[18:19], v4 offset:2048\n\tv_mul_u32_u24 v193, 768, v193\n\tv_mfma_f32_16x16x32_bf16 v[132:135], a[160:163], v[28:31], v[132:135]\n\tv_add_u32 v192, v192, v193\n\ts_add_u32 m0, s68, 131072\n\t" \
    "s_add_u32 s68, s68, 1024\n\tv_mfma_f32_16x16x32_bf16 v[140:143], a[164:167], v[28:31], v[140:143]\n\ts_waitcnt vmcnt(20) lgkmcnt(8)\n\tv_mfma_f32_16x16x32_bf16 v[120:123], a[168:171], v[32:35], v[120:123]\n\tds_read_b64_tr_b16 v[20:21], v5 offset:0\n\tds_read_b64_tr_b16 v[22:23], v5 offset:2048\n\tv_mfma_f32_16x16x32_bf16 v[128:131], a[172:175], v[32:35], v[128:131]\n\tbuffer_load_dwordx4 v192, s[20:23], 0 offen lds\n\t" \
    "v_mfma_f32_16x16x32_bf16 v[136:139], a[176:179], v[32:35], v[136:139]\n\ts_waitcnt lgkmcnt(8)\n\tv_mfma_f32_16x16x32_bf16 v[124:127], a[168:171], v[36:39], v[124:127]\n\tds_read_b64_tr_b16 v[24:25], v4 offset:4096\n\tds_read_b64_tr_b16 v[26:27], v4 offset:6144\n\tv_mfma_f32_16x16x32_bf16 v[132:135], a[172:175], v[36:39], v[132:135]\n\tbuffer_load_dwordx2 v[216:217], v242, s[8:11], s59 offen offset:0 sc1\n\tv_mfma_f32_16x16x32_bf16 v[140:143], a[176:179], v[36:39], v[140:143]\n\t" \
    "s_waitcnt vmcnt(19) lgkmcnt(8)\n\tv_mfma_f32_16x16x32_bf16 v[120:123], a[180:183], v[40:43], v[120:123]\n\tds_read_b64_tr_b16 v[28:29], v5 offset:4096\n\tds_read_b64_tr_b16 v[30:31], v5 offset:6144\n\tv_mfma_f32_16x16x32_bf16 v[128:131], a[184:187], v[40:43], v[128:131]\n\tbuffer_load_dwordx2 v[218:219], v242, s[8:11], s59 offen offset:128 sc1\n\tv_mfma_f32_16x16x32_bf16 v[136:139], a[188:191], v[40:43], v[136:139]\n\ts_waitcnt lgkmcnt(8)\n\t" \
    "v_mfma_f32_16x16x32_bf16 v[124:127], a[180:183], v[44:47], v[124:127]\n\tds_read_b64_tr_b16 v[32:33], v4 offset:8192\n\tds_read_b64_tr_b16 v[34:35], v4 offset:10240\n\tv_mfma_f32_16x16x32_bf16 v[132:135], a[184:187], v[44:47], v[132:135]\n\tv_mfma_f32_16x16x32_bf16 v[140:143], a[188:191], v[44:47], v[140:143]\n\ts_waitcnt vmcnt(17) lgkmcnt(8)\n\tv_mfma_f32_16x16x32_bf16 v[120:123], a[192:195], v[16:19], v[120:123]\n\tds_read_b64_tr_b16 v[36:37], v5 offset:8192\n\t" \
    "ds_read_b64_tr_b16 v[38:39], v5 offset:10240\n\tv_mfma_f32_16x16x32_bf16 v[128:131], a[196:199], v[16:19], v[128:131]\n\tv_mfma_f32_16x16x32_bf16 v[136:139], a[200:203], v[16:19], v[136:139]\n\ts_waitcnt lgkmcnt(8)\n\tv_mfma_f32_16x16x32_bf16 v[124:127], a[192:195], v[20:23], v[124:127]\n\tds_read_b64_tr_b16 v[40:41], v6 offset:57344\n\tds_read_b64_tr_b16 v[42:43], v6 offset:59392\n\tv_mfma_f32_16x16x32_bf16 v[132:135], a[196:199], v[20:23], v[132:135]\n\t" \
    "v_mfma_f32_16x16x32_bf16 v[140:143], a[200:203], v[20:23], v[140:143]\n\ts_waitcnt vmcnt(14) lgkmcnt(8)\n\tv_mfma_f32_16x16x32_bf16 v[120:123], a[204:207], v[24:27], v[120:123]\n\tds_read_b64_tr_b16 v[44:45], v7 offset:57344\n\tds_read_b64_tr_b16 v[46:47], v7 offset:59392\n\tv_mfma_f32_16x16x32_bf16 v[128:131], a[208:211], v[24:27], v[128:131]\n\tv_mfma_f32_16x16x32_bf16 v[136:139], a[212:215], v[24:27], v[136:139]\n\ts_waitcnt lgkmcnt(8)\n\t" \
    "v_mfma_f32_16x16x32_bf16 v[124:127], a[204:207], v[28:31], v[124:127]\n\tds_read_b64_tr_b16 v[16:17], v226 offset:57344\n\tds_read_b64_tr_b16 v[18:19], v226 offset:59392\n\tv_mfma_f32_16x16x32_bf16 v[132:135], a[208:211], v[28:31], v[132:135]\n\tv_mfma_f32_16x16x32_bf16 v[140:143], a[212:215], v[28:31], v[140:143]\n\ts_waitcnt vmcnt(11) lgkmcnt(8)\n\tv_mfma_f32_16x16x32_bf16 v[120:123], a[216:219], v[32:35], v[120:123]\n\tds_read_b64_tr_b16 v[20:21], v227 offset:57344\n\t" \
    "ds_read_b64_tr_b16 v[22:23], v227 offset:59392\n\tv_mfma_f32_16x16x32_bf16 v[128:131], a[220:223], v[32:35], v[128:131]\n\tv_mfma_f32_16x16x32_bf16 v[136:139], a[224:227], v[32:35], v[136:139]\n\tv_min_u32 v241, 376, v239\n\tbuffer_load_dwordx2 v[250:251], v241, s[8:11], s51 offen sc1\n\ts_waitcnt lgkmcnt(8)\n\tv_mfma_f32_16x16x32_bf16 v[124:127], a[216:219], v[36:39], v[124:127]\n\tds_read_b64_tr_b16 v[24:25], v6 offset:61440\n\t" \
    "ds_read_b64_tr_b16 v[26:27], v6 offset:63488\n\tv_mfma_f32_16x16x32_bf16 v[132:135], a[220:223], v[36:39], v[132:135]\n\tv_mfma_f32_16x16x32_bf16 v[140:143], a[224:227], v[36:39], v[140:143]\n\ts_waitcnt lgkmcnt(8)\n\tv_mfma_f32_16x16x32_bf16 v[144:147], a[156:159], v[40:43], v[144:147]\n\tds_read_b64_tr_b16 v[28:29], v7 offset:61440\n\tds_read_b64_tr_b16 v[30:31], v7 offset:63488\n\ts_xor_b32 s68, s53, 12\n\t" \
    "v_mfma_f32_16x16x32_bf16 v[152:155], a[160:163], v[40:43], v[152:155]\n\ts_mul_i32 s68, s68, 9984\n\ts_add_u32 s68, s68, 7208960\n\tv_cvt_pk_bf16_f32 v120, v120, v121\n\tv_mfma_f32_16x16x32_bf16 v[160:163], a[164:167], v[40:43], v[160:163]\n\tv_cvt_pk_bf16_f32 v121, v122, v123\n\tv_cvt_pk_bf16_f32 v122, v124, v125\n\ts_waitcnt lgkmcnt(8)\n\t" \
    "v_mfma_f32_16x16x32_bf16 v[148:151], a[156:159], v[44:47], v[148:151]\n\tds_read_b64_tr_b16 v[32:33], v6 offset:0\n\tds_read_b64_tr_b16 v[34:35], v6 offset:2048\n\tv_cvt_pk_bf16_f32 v123, v126, v127\n\tv_mfma_f32_16x16x32_bf16 v[156:159], a[160:163], v[44:47], v[156:159]\n\tbuffer_store_dwordx4 v[120:123], v8, s[8:11], s68 offen offset:0 sc1\n\tv_cvt_pk_bf16_f32 v124, v128, v129\n\tv_cvt_pk_bf16_f32 v125, v130, v131\n\t" \
    "v_mfma_f32_16x16x32_bf16 v[164:167], a[164:167], v[44:47], v[164:167]\n\tv_cvt_pk_bf16_f32 v126, v132, v133\n\tv_cvt_pk_bf16_f32 v127, v134, v135\n\ts_waitcnt vmcnt(7)\n\ts_barrier\n\ts_waitcnt lgkmcnt(8)\n\tv_mfma_f32_16x16x32_bf16 v[144:147], a[168:171], v[16:19], v[144:147]\n\tds_read_b64_tr_b16 v[36:37], v7 offset:0\n\t" \
    "ds_read_b64_tr_b16 v[38:39], v7 offset:2048\n\tv_mfma_f32_16x16x32_bf16 v[152:155], a[172:175], v[16:19], v[152:155]\n\tbuffer_store_dwordx4 v[124:127], v8, s[8:11], s68 offen offset:1024 sc1\n\tv_cvt_pk_bf16_f32 v128, v136, v137\n\tv_cvt_pk_bf16_f32 v129, v138, v139\n\tv_mfma_f32_16x16x32_bf16 v[160:163], a[176:179], v[16:19], v[160:163]\n\tv_cvt_pk_bf16_f32 v130, v140, v141\n\tv_cvt_pk_bf16_f32 v131, v142, v143\n\t" \
    "s_waitcnt lgkmcnt(8)\n\tv_mfma_f32_16x16x32_bf16 v[148:151], a[168:171], v[20:23], v[148:151]\n\tds_read_b64_tr_b16 v[40:41], v6 offset:4096\n\tds_read_b64_tr_b16 v[42:43], v6 offset:6144\n\tv_mfma_f32_16x16x32_bf16 v[156:159], a[172:175], v[20:23], v[156:159]\n\tbuffer_store_dwordx4 v[128:131], v8, s[8:11], s68 offen offset:2048 sc1\n\tv_add3_u32 v132, s36, v120, v121\n\tv_add3_u32 v133, v122, v123, v124\n\t" \
    "v_mfma_f32_16x16x32_bf16 v[164:167], a[176:179], v[20:23], v[164:167]\n\tv_add3_u32 v132, v132, v125, v126\n\tv_add3_u32 v133, v133, v127, v128\n\ts_waitcnt lgkmcnt(8)\n\tv_mfma_f32_16x16x32_bf16 v[144:147], a[180:183], v[24:27], v[144:147]\n\tds_read_b64_tr_b16 v[44:45], v7 offset:4096\n\tds_read_b64_tr_b16 v[46:47], v7 offset:6144\n\tv_add3_u32 v132, v132, v129, v130\n\t" \
    "v_mfma_f32_16x16x32_bf16 v[152:155], a[184:187], v[24:27], v[152:155]\n\tv_add3_u32 v132, v132, v133, v131\n\tv_mfma_f32_16x16x32_bf16 v[160:163], a[188:191], v[24:27], v[160:163]\n\ts_waitcnt lgkmcnt(8)\n\tv_mfma_f32_16x16x32_bf16 v[148:151], a[180:183], v[28:31], v[148:151]\n\tds_read_b64_tr_b16 v[16:17], v6 offset:8192\n\tds_read_b64_tr_b16 v[18:19], v6 offset:10240\n\tv_mfma_f32_16x16x32_bf16 v[156:159], a[184:187], v[28:31], v[156:159]\n\t" \
    "buffer_store_dword v132, v236, s[8:11], s68 offen offset:3072 sc1\n\tv_mfma_f32_16x16x32_bf16 v[164:167], a[188:191], v[28:31], v[164:167]\n\ts_waitcnt lgkmcnt(8)\n\tv_mfma_f32_16x16x32_bf16 v[144:147], a[192:195], v[32:35], v[144:147]\n\tds_read_b64_tr_b16 v[20:21], v7 offset:8192\n\tds_read_b64_tr_b16 v[22:23], v7 offset:10240\n\tv_mfma_f32_16x16x32_bf16 v[152:155], a[196:199], v[32:35], v[152:155]\n\tv_mfma_f32_16x16x32_bf16 v[160:163], a[200:203], v[32:35], v[160:163]\n\t" \
    "s_waitcnt lgkmcnt(8)\n\tv_mfma_f32_16x16x32_bf16 v[148:151], a[192:195], v[36:39], v[148:151]\n\tds_read_b64_tr_b16 v[24:25], v4 offset:12288\n\tds_read_b64_tr_b16 v[26:27], v4 offset:14336\n\tv_mfma_f32_16x16x32_bf16 v[156:159], a[196:199], v[36:39], v[156:159]\n\tv_mfma_f32_16x16x32_bf16 v[164:167], a[200:203], v[36:39], v[164:167]\n\ts_waitcnt lgkmcnt(8)\n\tv_mfma_f32_16x16x32_bf16 v[144:147], a[204:207], v[40:43], v[144:147]\n\t" \
    "ds_read_b64_tr_b16 v[28:29], v5 offset:12288\n\tds_read_b64_tr_b16 v[30:31], v5 offset:14336\n\tv_mfma_f32_16x16x32_bf16 v[152:155], a[208:211], v[40:43], v[152:155]\n\tv_mfma_f32_16x16x32_bf16 v[160:163], a[212:215], v[40:43], v[160:163]\n\ts_waitcnt lgkmcnt(8)\n\tv_mfma_f32_16x16x32_bf16 v[148:151], a[204:207], v[44:47], v[148:151]\n\tds_read_b64_tr_b16 v[32:33], v4 offset:16384\n\tds_read_b64_tr_b16 v[34:35], v4 offset:18432\n\t" \
    "v_mfma_f32_16x16x32_bf16 v[156:159], a[208:211], v[44:47], v[156:159]\n\tv_mfma_f32_16x16x32_bf16 v[164:167], a[212:215], v[44:47], v[164:167]\n\ts_waitcnt lgkmcnt(8)\n\tv_mfma_f32_16x16x32_bf16 v[144:147], a[216:219], v[16:19], v[144:147]\n\tds_read_b64_tr_b16 v[36:37], v5 offset:16384\n\tds_read_b64_tr_b16 v[38:39], v5 offset:18432\n\tv_mfma_f32_16x16x32_bf16 v[152:155], a[220:223], v[16:19], v[152:155]\n\tv_mfma_f32_16x16x32_bf16 v[160:163], a[224:227], v[16:19], v[160:163]\n\t" \
    "s_waitcnt lgkmcnt(8)\n\tv_mfma_f32_16x16x32_bf16 v[148:151], a[216:219], v[20:23], v[148:151]\n\tds_read_b64_tr_b16 v[40:41], v4 offset:20480\n\tds_read_b64_tr_b16 v[42:43], v4 offset:22528\n\tv_mfma_f32_16x16x32_bf16 v[156:159], a[220:223], v[20:23], v[156:159]\n\tv_mfma_f32_16x16x32_bf16 v[164:167], a[224:227], v[20:23], v[164:167]\n\ts_waitcnt lgkmcnt(8)\n\tv_mfma_f32_16x16x32_bf16 v[168:171], a[156:159], v[24:27], v[168:171]\n\t" \
    "ds_read_b64_tr_b16 v[44:45], v5 offset:20480\n\tds_read_b64_tr_b16 v[46:47], v5 offset:22528\n\ts_xor_b32 s68, s53, 8\n\tv_mfma_f32_16x16x32_bf16 v[176:179], a[160:163], v[24:27], v[176:179]\n\ts_mul_i32 s68, s68, 9984\n\ts_add_u32 s68, s68, 7212288\n\tv_cvt_pk_bf16_f32 v144, v144, v145\n\tv_mfma_f32_16x16x32_bf16 v[184:187], a[164:167], v[24:27], v[184:187]\n\t" \
    "v_cvt_pk_bf16_f32 v145, v146, v147\n\tv_cvt_pk_bf16_f32 v146, v148, v149\n\ts_waitcnt lgkmcnt(8)\n\tv_mfma_f32_16x16x32_bf16 v[172:175], a[156:159], v[28:31], v[172:175]\n\tds_read_b64_tr_b16 v[16:17], v4 offset:24576\n\tds_read_b64_tr_b16 v[18:19], v4 offset:26624\n\tv_cvt_pk_bf16_f32 v147, v150, v151\n\tv_mfma_f32_16x16x32_bf16 v[180:183], a[160:163], v[28:31], v[180:183]\n\t" \
    "buffer_store_dwordx4 v[144:147], v8, s[8:11], s68 offen offset:0 sc1\n\tv_cvt_pk_bf16_f32 v148, v152, v153\n\tv_cvt_pk_bf16_f32 v149, v154, v155\n\tv_mfma_f32_16x16x32_bf16 v[188:191], a[164:167], v[28:31], v[188:191]\n\tv_cvt_pk_bf16_f32 v150, v156, v157\n\tv_cvt_pk_bf16_f32 v151, v158, v159\n\ts_waitcnt lgkmcnt(8)\n\tv_mfma_f32_16x16x32_bf16 v[168:171], a[168:171], v[32:35], v[168:171]\n\t" \
    "ds_read_b64_tr_b16 v[20:21], v5 offset:24576\n\tds_read_b64_tr_b16 v[22:23], v5 offset:26624\n\tv_mfma_f32_16x16x32_bf16 v[176:179], a[172:175], v[32:35], v[176:179]\n\tbuffer_store_dwordx4 v[148:151], v8, s[8:11], s68 offen offset:1024 sc1\n\tv_cvt_pk_bf16_f32 v152, v160, v161\n\tv_cvt_pk_bf16_f32 v153, v162, v163\n\tv_mfma_f32_16x16x32_bf16 v[184:187], a[176:179], v[32:35], v[184:187]\n\tv_cvt_pk_bf16_f32 v154, v164, v165\n\t" \
    "v_cvt_pk_bf16_f32 v155, v166, v167\n\ts_waitcnt lgkmcnt(8)\n\tv_mfma_f32_16x16x32_bf16 v[172:175], a[168:171], v[36:39], v[172:175]\n\tds_read_b64_tr_b16 v[24:25], v4 offset:28672\n\tds_read_b64_tr_b16 v[26:27], v4 offset:30720\n\tv_mfma_f32_16x16x32_bf16 v[180:183], a[172:175], v[36:39], v[180:183]\n\tbuffer_store_dwordx4 v[152:155], v8, s[8:11], s68 offen offset:2048 sc1\n\tv_add3_u32 v156, s36, v144, v145\n\t" \
    "v_add3_u32 v157, v146, v147, v148\n\tv_mfma_f32_16x16x32_bf16 v[188:191], a[176:179], v[36:39], v[188:191]\n\tv_add3_u32 v156, v156, v149, v150\n\tv_add3_u32 v157, v157, v151, v152\n\ts_waitcnt lgkmcnt(8)\n\tv_mfma_f32_16x16x32_bf16 v[168:171], a[180:183], v[40:43], v[168:171]\n\tds_read_b64_tr_b16 v[28:29], v5 offset:28672\n\tds_read_b64_tr_b16 v[30:31], v5 offset:30720\n\t" \
    "v_add3_u32 v156, v156, v153, v154\n\tv_mfma_f32_16x16x32_bf16 v[176:179], a[184:187], v[40:43], v[176:179]\n\tv_add3_u32 v156, v156, v157, v155\n\tv_mfma_f32_16x16x32_bf16 v[184:187], a[188:191], v[40:43], v[184:187]\n\ts_waitcnt lgkmcnt(8)\n\tv_mfma_f32_16x16x32_bf16 v[172:175], a[180:183], v[44:47], v[172:175]\n\tds_read_b64_tr_b16 v[32:33], v4 offset:32768\n\tds_read_b64_tr_b16 v[34:35], v4 offset:34816\n\t" \
    "v_mfma_f32_16x16x32_bf16 v[180:183], a[184:187], v[44:47], v[180:183]\n\tbuffer_store_dword v156, v236, s[8:11], s68 offen offset:3072 sc1\n\tv_mfma_f32_16x16x32_bf16 v[188:191], a[188:191], v[44:47], v[188:191]\n\ts_waitcnt lgkmcnt(8)\n\tv_mfma_f32_16x16x32_bf16 v[168:171], a[192:195], v[16:19], v[168:171]\n\tds_read_b64_tr_b16 v[36:37], v5 offset:32768\n\tds_read_b64_tr_b16 v[38:39], v5 offset:34816\n\tv_mfma_f32_16x16x32_bf16 v[176:179], a[196:199], v[16:19], v[176:179]\n\t" \
    "v_mfma_f32_16x16x32_bf16 v[184:187], a[200:203], v[16:19], v[184:187]\n\ts_waitcnt lgkmcnt(8)\n\tv_mfma_f32_16x16x32_bf16 v[172:175], a[192:195], v[20:23], v[172:175]\n\tds_read_b64_tr_b16 v[40:41], v226 offset:61440\n\tds_read_b64_tr_b16 v[42:43], v226 offset:63488\n\tv_mfma_f32_16x16x32_bf16 v[180:183], a[196:199], v[20:23], v[180:183]\n\tv_mfma_f32_16x16x32_bf16 v[188:191], a[200:203], v[20:23], v[188:191]\n\ts_waitcnt lgkmcnt(8)\n\t" \
    "v_mfma_f32_16x16x32_bf16 v[168:171], a[204:207], v[24:27], v[168:171]\n\tds_read_b64_tr_b16 v[44:45], v227 offset:61440\n\tds_read_b64_tr_b16 v[46:47], v227 offset:63488\n\tv_mfma_f32_16x16x32_bf16 v[176:179], a[208:211], v[24:27], v[176:179]\n\tv_mfma_f32_16x16x32_bf16 v[184:187], a[212:215], v[24:27], v[184:187]\n\ts_waitcnt lgkmcnt(8)\n\tv_mfma_f32_16x16x32_bf16 v[172:175], a[204:207], v[28:31], v[172:175]\n\tds_read_b64_tr_b16 v[16:17], v226 offset:0\n\t" \
    "ds_read_b64_tr_b16 v[18:19], v226 offset:2048\n\tv_mfma_f32_16x16x32_bf16 v[180:183], a[208:211], v[28:31], v[180:183]\n\tv_mfma_f32_16x16x32_bf16 v[188:191], a[212:215], v[28:31], v[188:191]\n\ts_waitcnt vmcnt(8)\n\tv_cmp_ne_u64 vcc, s[36:37], v[250:251]\n\ts_cmp_lg_u64 vcc, 0\n\ts_cselect_b32 s70, 1, 0\n\ts_waitcnt lgkmcnt(8)\n\t" \
    "v_mfma_f32_16x16x32_bf16 v[168:171], a[216:219], v[32:35], v[168:171]\n\tds_read_b64_tr_b16 v[20:21], v227 offset:0\n\tds_read_b64_tr_b16 v[22:23], v227 offset:2048\n\tv_mfma_f32_16x16x32_bf16 v[176:179], a[220:223], v[32:35], v[176:179]\n\tbuffer_load_dwordx4 v[204:207], v8, s[8:11], s56 offen offset:0 sc1\n\tv_mfma_f32_16x16x32_bf16 v[184:187], a[224:227], v[32:35], v[184:187]\n\ts_waitcnt lgkmcnt(8)\n\tv_mfma_f32_16x16x32_bf16 v[172:175], a[216:219], v[36:39], v[172:175]\n\t" \
    "ds_read_b64_tr_b16 v[24:25], v226 offset:4096\n\tds_read_b64_tr_b16 v[26:27], v226 offset:6144\n\tv_mfma_f32_16x16x32_bf16 v[180:183], a[220:223], v[36:39], v[180:183]\n\tbuffer_load_dwordx4 v[208:211], v8, s[8:11], s56 offen offset:1024 sc1\n\tv_mfma_f32_16x16x32_bf16 v[188:191], a[224:227], v[36:39], v[188:191]\n\ts_waitcnt lgkmcnt(8)\n\tv_mfma_f32_16x16x32_bf16 v[48:51], a[32:35], v[40:43], v[48:51]\n\tds_read_b64_tr_b16 v[28:29], v227 offset:4096\n\t" \
    "ds_read_b64_tr_b16 v[30:31], v227 offset:6144\n\tv_mfma_f32_16x16x32_bf16 v[56:59], a[36:39], v[40:43], v[56:59]\n\tbuffer_load_dwordx4 v[212:215], v8, s[8:11], s56 offen offset:2048 sc1\n\ts_xor_b32 s68, s53, 4\n\ts_mul_i32 s68, s68, 9984\n\tv_mfma_f32_16x16x32_bf16 v[64:67], a[40:43], v[40:43], v[64:67]\n\ts_add_u32 s68, s68, 7215616\n\tv_cvt_pk_bf16_f32 v168, v168, v169\n\t" \
    "s_waitcnt lgkmcnt(8)\n\tv_mfma_f32_16x16x32_bf16 v[52:55], a[32:35], v[44:47], v[52:55]\n\tds_read_b64_tr_b16 v[32:33], v226 offset:8192\n\tds_read_b64_tr_b16 v[34:35], v226 offset:10240\n\tv_cvt_pk_bf16_f32 v169, v170, v171\n\tv_mfma_f32_16x16x32_bf16 v[60:63], a[36:39], v[44:47], v[60:63]\n\tv_cvt_pk_bf16_f32 v170, v172, v173\n\tv_cvt_pk_bf16_f32 v171, v174, v175\n\t" \
    "v_mfma_f32_16x16x32_bf16 v[68:71], a[40:43], v[44:47], v[68:71]\n\ts_waitcnt lgkmcnt(8)\n\tv_mfma_f32_16x16x32_bf16 v[48:51], v[84:87], v[16:19], v[48:51]\n\tds_read_b64_tr_b16 v[36:37], v227 offset:8192\n\tds_read_b64_tr_b16 v[38:39], v227 offset:10240\n\tv_mfma_f32_16x16x32_bf16 v[56:59], v[88:91], v[16:19], v[56:59]\n\tbuffer_store_dwordx4 v[168:171], v8, s[8:11], s68 offen offset:0 sc1\n\tv_cvt_pk_bf16_f32 v172, v176, v177\n\t" \
    "v_cvt_pk_bf16_f32 v173, v178, v179\n\tv_mfma_f32_16x16x32_bf16 v[64:67], v[92:95], v[16:19], v[64:67]\n\tv_cvt_pk_bf16_f32 v174, v180, v181\n\tv_cvt_pk_bf16_f32 v175, v182, v183\n\ts_waitcnt lgkmcnt(8)\n\tv_mfma_f32_16x16x32_bf16 v[52:55], v[84:87], v[20:23], v[52:55]\n\tds_read_b64_tr_b16 v[40:41], v226 offset:12288\n\tds_read_b64_tr_b16 v[42:43], v226 offset:14336\n\t" \
    "v_mfma_f32_16x16x32_bf16 v[60:63], v[88:91], v[20:23], v[60:63]\n\tbuffer_store_dwordx4 v[172:175], v8, s[8:11], s68 offen offset:1024 sc1\n\tv_cvt_pk_bf16_f32 v176, v184, v185\n\tv_cvt_pk_bf16_f32 v177, v186, v187\n\tv_mfma_f32_16x16x32_bf16 v[68:71], v[92:95], v[20:23], v[68:71]\n\tv_cvt_pk_bf16_f32 v178, v188, v189\n\tv_cvt_pk_bf16_f32 v179, v190, v191\n\ts_waitcnt lgkmcnt(8)\n\t" \
    "v_mfma_f32_16x16x32_bf16 v[48:51], a[44:47], v[24:27], v[48:51]\n\tds_read_b64_tr_b16 v[44:45], v227 offset:12288\n\tds_read_b64_tr_b16 v[46:47], v227 offset:14336\n\tv_mfma_f32_16x16x32_bf16 v[56:59], a[48:51], v[24:27], v[56:59]\n\tbuffer_store_dwordx4 v[176:179], v8, s[8:11], s68 offen offset:2048 sc1\n\tv_add3_u32 v180, s36, v168, v169\n\tv_add3_u32 v181, v170, v171, v172\n\tv_mfma_f32_16x16x32_bf16 v[64:67], a[52:55], v[24:27], v[64:67]\n\t" \
    "v_add3_u32 v180, v180, v173, v174\n\tv_add3_u32 v181, v181, v175, v176\n\ts_waitcnt lgkmcnt(8)\n\tv_mfma_f32_16x16x32_bf16 v[52:55], a[44:47], v[28:31], v[52:55]\n\tds_read_b64_tr_b16 v[16:17], v226 offset:16384\n\tds_read_b64_tr_b16 v[18:19], v226 offset:18432\n\tv_add3_u32 v180, v180, v177, v178\n\tv_mfma_f32_16x16x32_bf16 v[60:63], a[48:51], v[28:31], v[60:63]\n\t" \
    "v_add3_u32 v180, v180, v181, v179\n\tv_mfma_f32_16x16x32_bf16 v[68:71], a[52:55], v[28:31], v[68:71]\n\ts_waitcnt lgkmcnt(8)\n\tv_mfma_f32_16x16x32_bf16 v[48:51], v[96:99], v[32:35], v[48:51]\n\tds_read_b64_tr_b16 v[20:21], v227 offset:16384\n\tds_read_b64_tr_b16 v[22:23], v227 offset:18432\n\tv_mfma_f32_16x16x32_bf16 v[56:59], v[100:103], v[32:35], v[56:59]\n\tbuffer_store_dword v180, v236, s[8:11], s68 offen offset:3072 sc1\n\t" \
    "v_mfma_f32_16x16x32_bf16 v[64:67], v[104:107], v[32:35], v[64:67]\n\ts_waitcnt lgkmcnt(8)\n\tv_mfma_f32_16x16x32_bf16 v[52:55], v[96:99], v[36:39], v[52:55]\n\tds_read_b64_tr_b16 v[24:25], v226 offset:20480\n\tds_read_b64_tr_b16 v[26:27], v226 offset:22528\n\tv_mfma_f32_16x16x32_bf16 v[60:63], v[100:103], v[36:39], v[60:63]\n\tv_mfma_f32_16x16x32_bf16 v[68:71], v[104:107], v[36:39], v[68:71]\n\ts_barrier\n\t" \
    "s_waitcnt lgkmcnt(8)\n\tv_mfma_f32_16x16x32_bf16 v[48:51], a[56:59], v[40:43], v[48:51]\n\tds_read_b64_tr_b16 v[28:29], v227 offset:20480\n\tds_read_b64_tr_b16 v[30:31], v227 offset:22528\n\tv_mfma_f32_16x16x32_bf16 v[56:59], a[60:63], v[40:43], v[56:59]\n\tv_and_b32 v72, 31, v240\n\tv_mul_u32_u24 v72, 384, v72\n\tv_mfma_f32_16x16x32_bf16 v[64:67], a[64:67], v[40:43], v[64:67]\n\t" \
    "v_lshrrev_b32 v73, 5, v240\n\tv_lshl_add_u32 v72, v73, 4, v72\n\ts_waitcnt lgkmcnt(8)\n\tv_mfma_f32_16x16x32_bf16 v[52:55], a[56:59], v[44:47], v[52:55]\n\tds_read_b64_tr_b16 v[32:33], v226 offset:24576\n\tds_read_b64_tr_b16 v[34:35], v226 offset:26624\n\tv_mfma_f32_16x16x32_bf16 v[60:63], a[60:63], v[44:47], v[60:63]\n\ts_mul_i32 s73, s38, 96\n\t" \
    "s_add_u32 s73, s73, 131072\n\tv_add_u32 v72, s73, v72\n\tv_mfma_f32_16x16x32_bf16 v[68:71], a[64:67], v[44:47], v[68:71]\n\tds_read_b128 v[192:195], v72 offset:0\n\tds_read_b128 v[196:199], v72 offset:32\n\ts_waitcnt lgkmcnt(10)\n\tv_mfma_f32_16x16x32_bf16 v[48:51], a[0:3], v[16:19], v[48:51]\n\tds_read_b64_tr_b16 v[36:37], v227 offset:24576\n\t" \
    "ds_read_b64_tr_b16 v[38:39], v227 offset:26624\n\tv_mfma_f32_16x16x32_bf16 v[56:59], a[4:7], v[16:19], v[56:59]\n\tds_read_b128 v[200:203], v72 offset:64\n\tv_mfma_f32_16x16x32_bf16 v[64:67], a[68:71], v[16:19], v[64:67]\n\ts_waitcnt lgkmcnt(11)\n\tv_mfma_f32_16x16x32_bf16 v[52:55], a[0:3], v[20:23], v[52:55]\n\tds_read_b64_tr_b16 v[40:41], v226 offset:28672\n\tds_read_b64_tr_b16 v[42:43], v226 offset:30720\n\t" \
    "v_mfma_f32_16x16x32_bf16 v[60:63], a[4:7], v[20:23], v[60:63]\n\tv_mfma_f32_16x16x32_bf16 v[68:71], a[68:71], v[20:23], v[68:71]\n\ts_waitcnt lgkmcnt(2)\n\tv_mfma_f32_16x16x32_bf16 v[48:51], a[72:75], v[24:27], v[48:51]\n\tds_read_b64_tr_b16 v[44:45], v227 offset:28672\n\tds_read_b64_tr_b16 v[46:47], v227 offset:30720\n\tv_mfma_f32_16x16x32_bf16 v[56:59], a[76:79], v[24:27], v[56:59]\n\tv_permlane16_swap_b32 v192, v194\n\t" \
    "v_permlane16_swap_b32 v193, v195\n\tv_mfma_f32_16x16x32_bf16 v[64:67], a[80:83], v[24:27], v[64:67]\n\tv_permlane16_swap_b32 v196, v198\n\tv_permlane16_swap_b32 v197, v199\n\tv_mfma_f32_16x16x32_bf16 v[52:55], a[72:75], v[28:31], v[52:55]\n\tds_read_b64_tr_b16 v[16:17], v226 offset:32768\n\tds_read_b64_tr_b16 v[18:19], v226 offset:34816\n\tv_mfma_f32_16x16x32_bf16 v[60:63], a[76:79], v[28:31], v[60:63]\n\t" \
    "v_permlane16_swap_b32 v200, v202\n\tv_permlane16_swap_b32 v201, v203\n\tv_mfma_f32_16x16x32_bf16 v[68:71], a[80:83], v[28:31], v[68:71]\n\tv_lshlrev_b32 v72, 16, v192\n\tv_and_b32 v73, 0xffff0000, v192\n\tv_mfma_f32_16x16x32_bf16 v[48:51], a[8:11], v[32:35], v[48:51]\n\tds_read_b64_tr_b16 v[20:21], v227 offset:32768\n\tds_read_b64_tr_b16 v[22:23], v227 offset:34816\n\t" \
    "v_mfma_f32_16x16x32_bf16 v[56:59], a[12:15], v[32:35], v[56:59]\n\tv_lshlrev_b32 v74, 16, v193\n\tv_and_b32 v75, 0xffff0000, v193\n\tv_mfma_f32_16x16x32_bf16 v[64:67], a[16:19], v[32:35], v[64:67]\n\tv_lshlrev_b32 v76, 16, v194\n\tv_and_b32 v77, 0xffff0000, v194\n\tv_mfma_f32_16x16x32_bf16 v[52:55], a[8:11], v[36:39], v[52:55]\n\tds_read_b64_tr_b16 v[24:25], v226 offset:36864\n\t" \
    "ds_read_b64_tr_b16 v[26:27], v226 offset:38912\n\tv_mfma_f32_16x16x32_bf16 v[60:63], a[12:15], v[36:39], v[60:63]\n\tv_lshlrev_b32 v78, 16, v195\n\tv_and_b32 v79, 0xffff0000, v195\n\tv_mfma_f32_16x16x32_bf16 v[68:71], a[16:19], v[36:39], v[68:71]\n\tv_lshlrev_b32 v80, 16, v196\n\tv_and_b32 v81, 0xffff0000, v196\n\ts_waitcnt lgkmcnt(8)\n\t" \
    "v_mfma_f32_16x16x32_bf16 v[48:51], a[20:23], v[40:43], v[48:51]\n\tds_read_b64_tr_b16 v[28:29], v227 offset:36864\n\tds_read_b64_tr_b16 v[30:31], v227 offset:38912\n\tv_mfma_f32_16x16x32_bf16 v[56:59], a[24:27], v[40:43], v[56:59]\n\tv_lshlrev_b32 v82, 16, v197\n\tv_and_b32 v83, 0xffff0000, v197\n\tv_mfma_f32_16x16x32_bf16 v[64:67], a[28:31], v[40:43], v[64:67]\n\tv_lshlrev_b32 v84, 16, v198\n\t" \
    "v_and_b32 v85, 0xffff0000, v198\n\ts_waitcnt lgkmcnt(8)\n\tv_mfma_f32_16x16x32_bf16 v[52:55], a[20:23], v[44:47], v[52:55]\n\tds_read_b64_tr_b16 v[32:33], v226 offset:40960\n\tds_read_b64_tr_b16 v[34:35], v226 offset:43008\n\tv_mfma_f32_16x16x32_bf16 v[60:63], a[24:27], v[44:47], v[60:63]\n\tv_lshlrev_b32 v86, 16, v199\n\tv_and_b32 v87, 0xffff0000, v199\n\t" \
    "v_mfma_f32_16x16x32_bf16 v[68:71], a[28:31], v[44:47], v[68:71]\n\tv_lshlrev_b32 v88, 16, v200\n\tv_and_b32 v89, 0xffff0000, v200\n\ts_waitcnt lgkmcnt(8)\n\tv_mfma_f32_16x16x32_bf16 v[48:51], a[84:87], v[16:19], v[48:51]\n\tds_read_b64_tr_b16 v[36:37], v227 offset:40960\n\tds_read_b64_tr_b16 v[38:39], v227 offset:43008\n\tv_mfma_f32_16x16x32_bf16 v[56:59], a[88:91], v[16:19], v[56:59]\n\t" \
    "v_lshlrev_b32 v90, 16, v201\n\tv_and_b32 v91, 0xffff0000, v201\n\tv_mfma_f32_16x16x32_bf16 v[64:67], a[92:95], v[16:19], v[64:67]\n\tv_lshlrev_b32 v92, 16, v202\n\tv_and_b32 v93, 0xffff0000, v202\n\ts_waitcnt lgkmcnt(8)\n\tv_mfma_f32_16x16x32_bf16 v[52:55], a[84:87], v[20:23], v[52:55]\n\tds_read_b64_tr_b16 v[40:41], v226 offset:45056\n\t" \
    "ds_read_b64_tr_b16 v[42:43], v226 offset:47104\n\tv_mfma_f32_16x16x32_bf16 v[60:63], a[88:91], v[20:23], v[60:63]\n\tv_lshlrev_b32 v94, 16, v203\n\tv_and_b32 v95, 0xffff0000, v203\n\tv_mfma_f32_16x16x32_bf16 v[68:71], a[92:95], v[20:23], v[68:71]\n\tv_fma_f32 v96, v72, v216, v217\n\tv_fma_f32 v97, v73, v216, v217\n\ts_waitcnt lgkmcnt(8)\n\t" \
    "v_mfma_f32_16x16x32_bf16 v[48:51], a[96:99], v[24:27], v[48:51]\n\tds_read_b64_tr_b16 v[44:45], v227 offset:45056\n\tds_read_b64_tr_b16 v[46:47], v227 offset:47104\n\tv_mfma_f32_16x16x32_bf16 v[56:59], a[100:103], v[24:27], v[56:59]\n\tv_fma_f32 v98, v74, v216, v217\n\tv_fma_f32 v99, v75, v216, v217\n\tv_mfma_f32_16x16x32_bf16 v[64:67], a[104:107], v[24:27], v[64:67]\n\tv_fma_f32 v100, v76, v218, v219\n\t" \
    "v_fma_f32 v101, v77, v218, v219\n\ts_waitcnt lgkmcnt(8)\n\tv_mfma_f32_16x16x32_bf16 v[52:55], a[96:99], v[28:31], v[52:55]\n\tds_read_b64_tr_b16 v[16:17], v226 offset:49152\n\tds_read_b64_tr_b16 v[18:19], v226 offset:51200\n\tv_mfma_f32_16x16x32_bf16 v[60:63], a[100:103], v[28:31], v[60:63]\n\tv_fma_f32 v102, v78, v218, v219\n\tv_fma_f32 v103, v79, v218, v219\n\t" \
    "v_mfma_f32_16x16x32_bf16 v[68:71], a[104:107], v[28:31], v[68:71]\n\tv_fma_f32 v104, v80, v216, v217\n\tv_fma_f32 v105, v81, v216, v217\n\ts_waitcnt lgkmcnt(8)\n\tv_mfma_f32_16x16x32_bf16 v[48:51], a[108:111], v[32:35], v[48:51]\n\tds_read_b64_tr_b16 v[20:21], v227 offset:49152\n\tds_read_b64_tr_b16 v[22:23], v227 offset:51200\n\tv_mfma_f32_16x16x32_bf16 v[56:59], a[112:115], v[32:35], v[56:59]\n\t" \
    "v_fma_f32 v106, v82, v216, v217\n\tv_fma_f32 v107, v83, v216, v217\n\tv_mfma_f32_16x16x32_bf16 v[64:67], a[116:119], v[32:35], v[64:67]\n\tv_fma_f32 v108, v84, v218, v219\n\tv_fma_f32 v109, v85, v218, v219\n\ts_waitcnt lgkmcnt(8)\n\tv_mfma_f32_16x16x32_bf16 v[52:55], a[108:111], v[36:39], v[52:55]\n\tds_read_b64_tr_b16 v[24:25], v226 offset:53248\n\t" \
    "ds_read_b64_tr_b16 v[26:27], v226 offset:55296\n\tv_mfma_f32_16x16x32_bf16 v[60:63], a[112:115], v[36:39], v[60:63]\n\tv_fma_f32 v110, v86, v218, v219\n\tv_fma_f32 v111, v87, v218, v219\n\tv_mfma_f32_16x16x32_bf16 v[68:71], a[116:119], v[36:39], v[68:71]\n\tv_fma_f32 v112, v88, v216, v217\n\tv_fma_f32 v113, v89, v216, v217\n\ts_waitcnt lgkmcnt(8)\n\t" \
    "v_mfma_f32_16x16x32_bf16 v[48:51], a[120:123], v[40:43], v[48:51]\n\tds_read_b64_tr_b16 v[28:29], v227 offset:53248\n\tds_read_b64_tr_b16 v[30:31], v227 offset:55296\n\tv_mfma_f32_16x16x32_bf16 v[56:59], a[124:127], v[40:43], v[56:59]\n\tv_fma_f32 v114, v90, v216, v217\n\tv_fma_f32 v115, v91, v216, v217\n\tv_mfma_f32_16x16x32_bf16 v[64:67], a[128:131], v[40:43], v[64:67]\n\tv_fma_f32 v116, v92, v218, v219\n\t" \
    "v_fma_f32 v117, v93, v218, v219\n\ts_waitcnt lgkmcnt(8)\n\tv_mfma_f32_16x16x32_bf16 v[52:55], a[120:123], v[44:47], v[52:55]\n\tds_read_b64_tr_b16 v[32:33], v6 offset:12288\n\tds_read_b64_tr_b16 v[34:35], v6 offset:14336\n\tv_mfma_f32_16x16x32_bf16 v[60:63], a[124:127], v[44:47], v[60:63]\n\tv_fma_f32 v118, v94, v218, v219\n\tv_fma_f32 v119, v95, v218, v219\n\t" \
    "v_mfma_f32_16x16x32_bf16 v[68:71], a[128:131], v[44:47], v[68:71]\n\ts_waitcnt lgkmcnt(8)\n\tv_mfma_f32_16x16x32_bf16 v[48:51], a[132:135], v[16:19], v[48:51]\n\tds_read_b64_tr_b16 v[36:37], v7 offset:12288\n\tds_read_b64_tr_b16 v[38:39], v7 offset:14336\n\tv_mfma_f32_16x16x32_bf16 v[56:59], a[136:139], v[16:19], v[56:59]\n\tv_mfma_f32_16x16x32_bf16 v[64:67], a[140:143], v[16:19], v[64:67]\n\ts_waitcnt lgkmcnt(8)\n\t" \
    "v_mfma_f32_16x16x32_bf16 v[52:55], a[132:135], v[20:23], v[52:55]\n\tds_read_b64_tr_b16 v[40:41], v6 offset:16384\n\tds_read_b64_tr_b16 v[42:43], v6 offset:18432\n\tv_mfma_f32_16x16x32_bf16 v[60:63], a[136:139], v[20:23], v[60:63]\n\tv_mfma_f32_16x16x32_bf16 v[68:71], a[140:143], v[20:23], v[68:71]\n\ts_waitcnt lgkmcnt(8)\n\tv_mfma_f32_16x16x32_bf16 v[48:51], a[144:147], v[24:27], v[48:51]\n\tds_read_b64_tr_b16 v[44:45], v7 offset:16384\n\t" \
    "ds_read_b64_tr_b16 v[46:47], v7 offset:18432\n\ts_add_u32 s68, s55, 0\n\tv_mfma_f32_16x16x32_bf16 v[56:59], a[148:151], v[24:27], v[56:59]\n\tbuffer_load_dwordx4 v[120:123], v8, s[8:11], s68 offen offset:0 sc1\n\tv_mfma_f32_16x16x32_bf16 v[64:67], a[152:155], v[24:27], v[64:67]\n\ts_waitcnt lgkmcnt(8)\n\tv_mfma_f32_16x16x32_bf16 v[52:55], a[144:147], v[28:31], v[52:55]\n\tds_read_b64_tr_b16 v[16:17], v6 offset:20480\n\t" \
    "ds_read_b64_tr_b16 v[18:19], v6 offset:22528\n\tv_mfma_f32_16x16x32_bf16 v[60:63], a[148:151], v[28:31], v[60:63]\n\tbuffer_load_dwordx4 v[124:127], v8, s[8:11], s68 offen offset:1024 sc1\n\tv_mfma_f32_16x16x32_bf16 v[68:71], a[152:155], v[28:31], v[68:71]\n\ts_waitcnt lgkmcnt(8)\n\tv_mfma_f32_16x16x32_bf16 v[48:51], a[156:159], v[32:35], v[48:51]\n\tds_read_b64_tr_b16 v[20:21], v7 offset:20480\n\tds_read_b64_tr_b16 v[22:23], v7 offset:22528\n\t" \
    "v_mfma_f32_16x16x32_bf16 v[56:59], a[160:163], v[32:35], v[56:59]\n\tbuffer_load_dwordx4 v[128:131], v8, s[8:11], s68 offen offset:2048 sc1\n\tv_mfma_f32_16x16x32_bf16 v[64:67], a[164:167], v[32:35], v[64:67]\n\ts_waitcnt lgkmcnt(8)\n\tv_mfma_f32_16x16x32_bf16 v[52:55], a[156:159], v[36:39], v[52:55]\n\tds_read_b64_tr_b16 v[24:25], v6 offset:24576\n\tds_read_b64_tr_b16 v[26:27], v6 offset:26624\n\tv_mfma_f32_16x16x32_bf16 v[60:63], a[160:163], v[36:39], v[60:63]\n\t" \
    "buffer_load_dword v132, v236, s[8:11], s68 offen offset:3072 sc1\n\tv_mfma_f32_16x16x32_bf16 v[68:71], a[164:167], v[36:39], v[68:71]\n\ts_waitcnt lgkmcnt(8)\n\tv_mfma_f32_16x16x32_bf16 v[48:51], a[168:171], v[40:43], v[48:51]\n\tds_read_b64_tr_b16 v[28:29], v7 offset:24576\n\tds_read_b64_tr_b16 v[30:31], v7 offset:26624\n\tv_mfma_f32_16x16x32_bf16 v[56:59], a[172:175], v[40:43], v[56:59]\n\tv_mfma_f32_16x16x32_bf16 v[64:67], a[176:179], v[40:43], v[64:67]\n\t" \
    "s_waitcnt lgkmcnt(8)\n\tv_mfma_f32_16x16x32_bf16 v[52:55], a[168:171], v[44:47], v[52:55]\n\tds_read_b64_tr_b16 v[32:33], v6 offset:28672\n\tds_read_b64_tr_b16 v[34:35], v6 offset:30720\n\ts_add_u32 s68, s55, 3328\n\tv_mfma_f32_16x16x32_bf16 v[60:63], a[172:175], v[44:47], v[60:63]\n\tbuffer_load_dwordx4 v[144:147], v8, s[8:11], s68 offen offset:0 sc1\n\tv_mfma_f32_16x16x32_bf16 v[68:71], a[176:179], v[44:47], v[68:71]\n\t" \
    "s_waitcnt lgkmcnt(8)\n\tv_mfma_f32_16x16x32_bf16 v[48:51], a[180:183], v[16:19], v[48:51]\n\tds_read_b64_tr_b16 v[36:37], v7 offset:28672\n\tds_read_b64_tr_b16 v[38:39], v7 offset:30720\n\tv_mfma_f32_16x16x32_bf16 v[56:59], a[184:187], v[16:19], v[56:59]\n\tbuffer_load_dwordx4 v[148:151], v8, s[8:11], s68 offen offset:1024 sc1\n\tv_mfma_f32_16x16x32_bf16 v[64:67], a[188:191], v[16:19], v[64:67]\n\ts_waitcnt lgkmcnt(8)\n\t" \
    "v_mfma_f32_16x16x32_bf16 v[52:55], a[180:183], v[20:23], v[52:55]\n\tds_read_b64_tr_b16 v[40:41], v6 offset:32768\n\tds_read_b64_tr_b16 v[42:43], v6 offset:34816\n\tv_mfma_f32_16x16x32_bf16 v[60:63], a[184:187], v[20:23], v[60:63]\n\tbuffer_load_dwordx4 v[152:155], v8, s[8:11], s68 offen offset:2048 sc1\n\tv_mfma_f32_16x16x32_bf16 v[68:71], a[188:191], v[20:23], v[68:71]\n\ts_waitcnt lgkmcnt(8)\n\tv_mfma_f32_16x16x32_bf16 v[48:51], a[192:195], v[24:27], v[48:51]\n\t" \
    "ds_read_b64_tr_b16 v[44:45], v7 offset:32768\n\tds_read_b64_tr_b16 v[46:47], v7 offset:34816\n\tv_mfma_f32_16x16x32_bf16 v[56:59], a[196:199], v[24:27], v[56:59]\n\tbuffer_load_dword v156, v236, s[8:11], s68 offen offset:3072 sc1\n\tv_mfma_f32_16x16x32_bf16 v[64:67], a[200:203], v[24:27], v[64:67]\n\ts_waitcnt lgkmcnt(8)\n\tv_mfma_f32_16x16x32_bf16 v[52:55], a[192:195], v[28:31], v[52:55]\n\tv_mfma_f32_16x16x32_bf16 v[60:63], a[196:199], v[28:31], v[60:63]\n\t" \
    "v_mfma_f32_16x16x32_bf16 v[68:71], a[200:203], v[28:31], v[68:71]\n\ts_waitcnt lgkmcnt(6)\n\tv_mfma_f32_16x16x32_bf16 v[48:51], a[204:207], v[32:35], v[48:51]\n\ts_add_u32 s68, s55, 6656\n\tv_mfma_f32_16x16x32_bf16 v[56:59], a[208:211], v[32:35], v[56:59]\n\tbuffer_load_dwordx4 v[168:171], v8, s[8:11], s68 offen offset:0 sc1\n\tv_mfma_f32_16x16x32_bf16 v[64:67], a[212:215], v[32:35], v[64:67]\n\ts_waitcnt lgkmcnt(4)\n\t" \
    "v_mfma_f32_16x16x32_bf16 v[52:55], a[204:207], v[36:39], v[52:55]\n\tv_mfma_f32_16x16x32_bf16 v[60:63], a[208:211], v[36:39], v[60:63]\n\tbuffer_load_dwordx4 v[172:175], v8, s[8:11], s68 offen offset:1024 sc1\n\tv_mfma_f32_16x16x32_bf16 v[68:71], a[212:215], v[36:39], v[68:71]\n\ts_waitcnt lgkmcnt(2)\n\tv_mfma_f32_16x16x32_bf16 v[48:51], a[216:219], v[40:43], v[48:51]\n\tv_mfma_f32_16x16x32_bf16 v[56:59], a[220:223], v[40:43], v[56:59]\n\tbuffer_load_dwordx4 v[176:179], v8, s[8:11], s68 offen offset:2048 sc1\n\t" \
    "v_mfma_f32_16x16x32_bf16 v[64:67], a[224:227], v[40:43], v[64:67]\n\ts_waitcnt lgkmcnt(0)\n\tv_mfma_f32_16x16x32_bf16 v[52:55], a[216:219], v[44:47], v[52:55]\n\tv_mfma_f32_16x16x32_bf16 v[60:63], a[220:223], v[44:47], v[60:63]\n\tbuffer_load_dword v180, v236, s[8:11], s68 offen offset:3072 sc1\n\tv_mfma_f32_16x16x32_bf16 v[68:71], a[224:227], v[44:47], v[68:71]\n\ts_cmp_eq_u32 s70, 0\n\ts_cbranch_scc1 113f\n\t" \
    "v_cmp_ne_u64 vcc, s[36:37], v[250:251]\n\ts_cbranch_vccz 115f\n\ts_mov_b32 s71, 1048576\n\t114:\n\ts_sleep 1\n\tbuffer_load_dwordx2 v[250:251], v241, s[8:11], s51 offen sc1\n\ts_waitcnt vmcnt(0)\n\tv_cmp_ne_u64 vcc, s[36:37], v[250:251]\n\t" \
    "s_cbranch_vccz 115f\n\ts_sub_u32 s71, s71, 1\n\ts_cbranch_scc0 114b\n\t115:\n\tbuffer_load_dwordx4 v[204:207], v8, s[8:11], s56 offen offset:0 sc1\n\tbuffer_load_dwordx4 v[208:211], v8, s[8:11], s56 offen offset:1024 sc1\n\tbuffer_load_dwordx4 v[212:215], v8, s[8:11], s56 offen offset:2048 sc1\n\ts_waitcnt vmcnt(0)\n\t" \
    "113:\n\ts_waitcnt vmcnt(16)\n\tv_lshlrev_b32 v16, 16, v204\n\tv_and_b32 v17, 0xffff0000, v204\n\tv_lshlrev_b32 v18, 16, v205\n\tv_and_b32 v19, 0xffff0000, v205\n\tv_lshlrev_b32 v20, 16, v206\n\tv_and_b32 v21, 0xffff0000, v206\n\t" \
    "v_lshlrev_b32 v22, 16, v207\n\tv_and_b32 v23, 0xffff0000, v207\n\tv_lshlrev_b32 v24, 16, v208\n\tv_and_b32 v25, 0xffff0000, v208\n\tv_lshlrev_b32 v26, 16, v209\n\tv_and_b32 v27, 0xffff0000, v209\n\tv_lshlrev_b32 v28, 16, v210\n\tv_and_b32 v29, 0xffff0000, v210\n\t" \
    "v_lshlrev_b32 v30, 16, v211\n\tv_and_b32 v31, 0xffff0000, v211\n\tv_lshlrev_b32 v32, 16, v212\n\tv_and_b32 v33, 0xffff0000, v212\n\tv_lshlrev_b32 v34, 16, v213\n\tv_and_b32 v35, 0xffff0000, v213\n\tv_lshlrev_b32 v36, 16, v214\n\tv_and_b32 v37, 0xffff0000, v214\n\t" \
    "v_lshlrev_b32 v38, 16, v215\n\tv_and_b32 v39, 0xffff0000, v215\n\tv_pk_mul_f32 v[96:97], v[96:97], v[16:17]\n\tv_pk_mul_f32 v[98:99], v[98:99], v[18:19]\n\tv_pk_mul_f32 v[100:101], v[100:101], v[20:21]\n\tv_pk_mul_f32 v[102:103], v[102:103], v[22:23]\n\tv_pk_mul_f32 v[104:105], v[104:105], v[24:25]\n\tv_pk_mul_f32 v[106:107], v[106:107], v[26:27]\n\t" \
    "v_pk_mul_f32 v[108:109], v[108:109], v[28:29]\n\tv_pk_mul_f32 v[110:111], v[110:111], v[30:31]\n\tv_pk_mul_f32 v[112:113], v[112:113], v[32:33]\n\tv_pk_mul_f32 v[114:115], v[114:115], v[34:35]\n\tv_pk_mul_f32 v[116:117], v[116:117], v[36:37]\n\tv_pk_mul_f32 v[118:119], v[118:119], v[38:39]\n\ts_mov_b32 s71, 1048576\n\ts_waitcnt vmcnt(8)\n\t" \
    "116:\n\tv_add3_u32 v40, s36, v120, v121\n\tv_add3_u32 v41, v122, v123, v124\n\tv_add3_u32 v40, v40, v125, v126\n\tv_add3_u32 v41, v41, v127, v128\n\tv_add3_u32 v40, v40, v129, v130\n\tv_add3_u32 v40, v40, v41, v131\n\tv_cmp_ne_u32 vcc, v40, v132\n\t" \
    "s_cbranch_vccz 117f\n\ts_sub_u32 s71, s71, 1\n\ts_cbranch_scc1 117f\n\ts_add_u32 s68, s55, 0\n\tbuffer_load_dwordx4 v[120:123], v8, s[8:11], s68 offen offset:0 sc1\n\tbuffer_load_dwordx4 v[124:127], v8, s[8:11], s68 offen offset:1024 sc1\n\tbuffer_load_dwordx4 v[128:131], v8, s[8:11], s68 offen offset:2048 sc1\n\tbuffer_load_dword v132, v236, s[8:11], s68 offen offset:3072 sc1\n\t" \
    "s_waitcnt vmcnt(0)\n\ts_branch 116b\n\t117:\n\tv_lshlrev_b32 v16, 16, v120\n\tv_and_b32 v17, 0xffff0000, v120\n\tv_lshlrev_b32 v18, 16, v121\n\tv_and_b32 v19, 0xffff0000, v121\n\tv_lshlrev_b32 v20, 16, v122\n\t" \
    "v_and_b32 v21, 0xffff0000, v122\n\tv_lshlrev_b32 v22, 16, v123\n\tv_and_b32 v23, 0xffff0000, v123\n\tv_lshlrev_b32 v24, 16, v124\n\tv_and_b32 v25, 0xffff0000, v124\n\tv_lshlrev_b32 v26, 16, v125\n\tv_and_b32 v27, 0xffff0000, v125\n\tv_lshlrev_b32 v28, 16, v126\n\t" \
    "v_and_b32 v29, 0xffff0000, v126\n\tv_lshlrev_b32 v30, 16, v127\n\tv_and_b32 v31, 0xffff0000, v127\n\tv_lshlrev_b32 v32, 16, v128\n\tv_and_b32 v33, 0xffff0000, v128\n\tv_lshlrev_b32 v34, 16, v129\n\tv_and_b32 v35, 0xffff0000, v129\n\tv_lshlrev_b32 v36, 16, v130\n\t" \
    "v_and_b32 v37, 0xffff0000, v130\n\tv_lshlrev_b32 v38, 16, v131\n\tv_and_b32 v39, 0xffff0000, v131\n\tv_pk_add_f32 v[48:49], v[48:49], v[16:17]\n\tv_pk_add_f32 v[50:51], v[50:51], v[18:19]\n\tv_pk_add_f32 v[52:53], v[52:53], v[20:21]\n\tv_pk_add_f32 v[54:55], v[54:55], v[22:23]\n\tv_pk_add_f32 v[56:57], v[56:57], v[24:25]\n\t" \
    "v_pk_add_f32 v[58:59], v[58:59], v[26:27]\n\tv_pk_add_f32 v[60:61], v[60:61], v[28:29]\n\tv_pk_add_f32 v[62:63], v[62:63], v[30:31]\n\tv_pk_add_f32 v[64:65], v[64:65], v[32:33]\n\tv_pk_add_f32 v[66:67], v[66:67], v[34:35]\n\tv_pk_add_f32 v[68:69], v[68:69], v[36:37]\n\tv_pk_add_f32 v[70:71], v[70:71], v[38:39]\n\ts_mov_b32 s71, 1048576\n\t" \
    "s_waitcnt vmcnt(4)\n\t118:\n\tv_add3_u32 v40, s36, v144, v145\n\tv_add3_u32 v41, v146, v147, v148\n\tv_add3_u32 v40, v40, v149, v150\n\tv_add3_u32 v41, v41, v151, v152\n\tv_add3_u32 v40, v40, v153, v154\n\tv_add3_u32 v40, v40, v41, v155\n\t" \
    "v_cmp_ne_u32 vcc, v40, v156\n\ts_cbranch_vccz 119f\n\ts_sub_u32 s71, s71, 1\n\ts_cbranch_scc1 119f\n\ts_add_u32 s68, s55, 3328\n\tbuffer_load_dwordx4 v[144:147], v8, s[8:11], s68 offen offset:0 sc1\n\tbuffer_load_dwordx4 v[148:151], v8, s[8:11], s68 offen offset:1024 sc1\n\tbuffer_load_dwordx4 v[152:155], v8, s[8:11], s68 offen offset:2048 sc1\n\t" \
    "buffer_load_dword v156, v236, s[8:11], s68 offen offset:3072 sc1\n\ts_waitcnt vmcnt(0)\n\ts_branch 118b\n\t119:\n\tv_lshlrev_b32 v16, 16, v144\n\tv_and_b32 v17, 0xffff0000, v144\n\tv_lshlrev_b32 v18, 16, v145\n\tv_and_b32 v19, 0xffff0000, v145\n\t" \
    "v_lshlrev_b32 v20, 16, v146\n\tv_and_b32 v21, 0xffff0000, v146\n\tv_lshlrev_b32 v22, 16, v147\n\tv_and_b32 v23, 0xffff0000, v147\n\tv_lshlrev_b32 v24, 16, v148\n\tv_and_b32 v25, 0xffff0000, v148\n\tv_lshlrev_b32 v26, 16, v149\n\tv_and_b32 v27, 0xffff0000, v149\n\t" \
    "v_lshlrev_b32 v28, 16, v150\n\tv_and_b32 v29, 0xffff0000, v150\n\tv_lshlrev_b32 v30, 16, v151\n\tv_and_b32 v31, 0xffff0000, v151\n\tv_lshlrev_b32 v32, 16, v152\n\tv_and_b32 v33, 0xffff0000, v152\n\tv_lshlrev_b32 v34, 16, v153\n\tv_and_b32 v35, 0xffff0000, v153\n\t" \
    "v_lshlrev_b32 v36, 16, v154\n\tv_and_b32 v37, 0xffff0000, v154\n\tv_lshlrev_b32 v38, 16, v155\n\tv_and_b32 v39, 0xffff0000, v155\n\tv_pk_add_f32 v[48:49], v[48:49], v[16:17]\n\tv_pk_add_f32 v[50:51], v[50:51], v[18:19]\n\tv_pk_add_f32 v[52:53], v[52:53], v[20:21]\n\tv_pk_add_f32 v[54:55], v[54:55], v[22:23]\n\t" \
    "v_pk_add_f32 v[56:57], v[56:57], v[24:25]\n\tv_pk_add_f32 v[58:59], v[58:59], v[26:27]\n\tv_pk_add_f32 v[60:61], v[60:61], v[28:29]\n\tv_pk_add_f32 v[62:63], v[62:63], v[30:31]\n\tv_pk_add_f32 v[64:65], v[64:65], v[32:33]\n\tv_pk_add_f32 v[66:67], v[66:67], v[34:35]\n\tv_pk_add_f32 v[68:69], v[68:69], v[36:37]\n\tv_pk_add_f32 v[70:71], v[70:71], v[38:39]\n\t" \
    "s_mov_b32 s71, 1048576\n\ts_waitcnt vmcnt(0)\n\t120:\n\tv_add3_u32 v40, s36, v168, v169\n\tv_add3_u32 v41, v170, v171, v172\n\tv_add3_u32 v40, v40, v173, v174\n\tv_add3_u32 v41, v41, v175, v176\n\tv_add3_u32 v40, v40, v177, v178\n\t" \
    "v_add3_u32 v40, v40, v41, v179\n\tv_cmp_ne_u32 vcc, v40, v180\n\ts_cbranch_vccz 121f\n\ts_sub_u32 s71, s71, 1\n\ts_cbranch_scc1 121f\n\ts_add_u32 s68, s55, 6656\n\tbuffer_load_dwordx4 v[168:171], v8, s[8:11], s68 offen offset:0 sc1\n\tbuffer_load_dwordx4 v[172:175], v8, s[8:11], s68 offen offset:1024 sc1\n\t" \
    "buffer_load_dwordx4 v[176:179], v8, s[8:11], s68 offen offset:2048 sc1\n\tbuffer_load_dword v180, v236, s[8:11], s68 offen offset:3072 sc1\n\ts_waitcnt vmcnt(0)\n\ts_branch 120b\n\t121:\n\ts_mov_b32 s68, 0xbfb8aa3b\n\ts_mov_b32 s69, 0xbfb8aa3b\n\tv_lshlrev_b32 v16, 16, v168\n\t" \
    "v_and_b32 v17, 0xffff0000, v168\n\tv_lshlrev_b32 v18, 16, v169\n\tv_and_b32 v19, 0xffff0000, v169\n\tv_lshlrev_b32 v20, 16, v170\n\tv_and_b32 v21, 0xffff0000, v170\n\tv_lshlrev_b32 v22, 16, v171\n\tv_and_b32 v23, 0xffff0000, v171\n\tv_pk_add_f32 v[48:49], v[48:49], v[16:17]\n\t" \
    "v_pk_add_f32 v[50:51], v[50:51], v[18:19]\n\tv_pk_add_f32 v[52:53], v[52:53], v[20:21]\n\tv_pk_add_f32 v[54:55], v[54:55], v[22:23]\n\tv_pk_mul_f32 v[16:17], v[48:49], s[68:69]\n\tv_pk_mul_f32 v[18:19], v[50:51], s[68:69]\n\tv_pk_mul_f32 v[20:21], v[52:53], s[68:69]\n\tv_pk_mul_f32 v[22:23], v[54:55], s[68:69]\n\tv_lshlrev_b32 v24, 16, v172\n\t" \
    "v_exp_f32 v16, v16\n\tv_and_b32 v25, 0xffff0000, v172\n\tv_exp_f32 v17, v17\n\tv_lshlrev_b32 v26, 16, v173\n\tv_exp_f32 v18, v18\n\tv_and_b32 v27, 0xffff0000, v173\n\tv_exp_f32 v19, v19\n\tv_lshlrev_b32 v28, 16, v174\n\t" \
    "v_exp_f32 v20, v20\n\tv_and_b32 v29, 0xffff0000, v174\n\tv_exp_f32 v21, v21\n\tv_lshlrev_b32 v30, 16, v175\n\tv_exp_f32 v22, v22\n\tv_and_b32 v31, 0xffff0000, v175\n\tv_exp_f32 v23, v23\n\tv_pk_add_f32 v[56:57], v[56:57], v[24:25]\n\t" \
    "v_pk_add_f32 v[16:17], v[16:17], 1.0 op_sel_hi:[1,0]\n\tv_pk_add_f32 v[18:19], v[18:19], 1.0 op_sel_hi:[1,0]\n\tv_pk_add_f32 v[20:21], v[20:21], 1.0 op_sel_hi:[1,0]\n\tv_pk_add_f32 v[22:23], v[22:23], 1.0 op_sel_hi:[1,0]\n\tv_pk_add_f32 v[58:59], v[58:59], v[26:27]\n\tv_rcp_f32 v16, v16\n\tv_pk_add_f32 v[60:61], v[60:61], v[28:29]\n\tv_rcp_f32 v17, v17\n\t" \
    "v_pk_add_f32 v[62:63], v[62:63], v[30:31]\n\tv_rcp_f32 v18, v18\n\tv_pk_mul_f32 v[24:25], v[56:57], s[68:69]\n\tv_rcp_f32 v19, v19\n\tv_pk_mul_f32 v[26:27], v[58:59], s[68:69]\n\tv_rcp_f32 v20, v20\n\tv_pk_mul_f32 v[28:29], v[60:61], s[68:69]\n\tv_rcp_f32 v21, v21\n\t" \
    "v_pk_mul_f32 v[30:31], v[62:63], s[68:69]\n\tv_rcp_f32 v22, v22\n\tv_lshlrev_b32 v32, 16, v176\n\tv_rcp_f32 v23, v23\n\tv_and_b32 v33, 0xffff0000, v176\n\tv_exp_f32 v24, v24\n\tv_pk_mul_f32 v[48:49], v[48:49], v[96:97]\n\tv_exp_f32 v25, v25\n\t" \
    "v_pk_mul_f32 v[50:51], v[50:51], v[98:99]\n\tv_exp_f32 v26, v26\n\tv_pk_mul_f32 v[52:53], v[52:53], v[100:101]\n\tv_exp_f32 v27, v27\n\tv_pk_mul_f32 v[54:55], v[54:55], v[102:103]\n\tv_exp_f32 v28, v28\n\tv_pk_fma_f32 v[16:17], v[16:17], v[48:49], v[72:73]\n\tv_exp_f32 v29, v29\n\t" \
    "v_pk_fma_f32 v[18:19], v[18:19], v[50:51], v[74:75]\n\tv_exp_f32 v30, v30\n\tv_pk_fma_f32 v[20:21], v[20:21], v[52:53], v[76:77]\n\tv_exp_f32 v31, v31\n\tv_pk_fma_f32 v[22:23], v[22:23], v[54:55], v[78:79]\n\tv_cvt_pk_bf16_f32 v48, v16, v17\n\tv_cvt_pk_bf16_f32 v49, v18, v19\n\tv_cvt_pk_bf16_f32 v50, v20, v21\n\t" \
    "v_cvt_pk_bf16_f32 v51, v22, v23\n\ts_nop 0\n\tv_permlane16_swap_b32 v48, v50\n\tv_permlane16_swap_b32 v49, v51\n\tbuffer_store_dwordx4 v[48:51], v243, s[24:27], 0 offen offset:0\n\tv_pk_add_f32 v[24:25], v[24:25], 1.0 op_sel_hi:[1,0]\n\tv_pk_add_f32 v[26:27], v[26:27], 1.0 op_sel_hi:[1,0]\n\tv_pk_add_f32 v[28:29], v[28:29], 1.0 op_sel_hi:[1,0]\n\t" \
    "v_pk_add_f32 v[30:31], v[30:31], 1.0 op_sel_hi:[1,0]\n\tv_lshlrev_b32 v34, 16, v177\n\tv_rcp_f32 v24, v24\n\tv_and_b32 v35, 0xffff0000, v177\n\tv_rcp_f32 v25, v25\n\tv_lshlrev_b32 v36, 16, v178\n\tv_rcp_f32 v26, v26\n\tv_and_b32 v37, 0xffff0000, v178\n\t" \
    "v_rcp_f32 v27, v27\n\tv_lshlrev_b32 v38, 16, v179\n\tv_rcp_f32 v28, v28\n\tv_and_b32 v39, 0xffff0000, v179\n\tv_rcp_f32 v29, v29\n\tv_pk_add_f32 v[64:65], v[64:65], v[32:33]\n\tv_rcp_f32 v30, v30\n\tv_pk_add_f32 v[66:67], v[66:67], v[34:35]\n\t" \
    "v_rcp_f32 v31, v31\n\tv_pk_add_f32 v[68:69], v[68:69], v[36:37]\n\tv_pk_mul_f32 v[56:57], v[56:57], v[104:105]\n\tv_pk_mul_f32 v[58:59], v[58:59], v[106:107]\n\tv_pk_mul_f32 v[60:61], v[60:61], v[108:109]\n\tv_pk_mul_f32 v[62:63], v[62:63], v[110:111]\n\tv_pk_fma_f32 v[24:25], v[24:25], v[56:57], v[80:81]\n\tv_pk_fma_f32 v[26:27], v[26:27], v[58:59], v[82:83]\n\t" \
    "v_pk_fma_f32 v[28:29], v[28:29], v[60:61], v[84:85]\n\tv_pk_fma_f32 v[30:31], v[30:31], v[62:63], v[86:87]\n\tv_cvt_pk_bf16_f32 v56, v24, v25\n\tv_cvt_pk_bf16_f32 v57, v26, v27\n\tv_cvt_pk_bf16_f32 v58, v28, v29\n\tv_cvt_pk_bf16_f32 v59, v30, v31\n\ts_nop 0\n\tv_permlane16_swap_b32 v56, v58\n\t" \
    "v_permlane16_swap_b32 v57, v59\n\tbuffer_store_dwordx4 v[56:59], v243, s[24:27], 0 offen offset:32\n\tv_pk_add_f32 v[70:71], v[70:71], v[38:39]\n\tv_pk_mul_f32 v[32:33], v[64:65], s[68:69]\n\tv_pk_mul_f32 v[34:35], v[66:67], s[68:69]\n\tv_pk_mul_f32 v[36:37], v[68:69], s[68:69]\n\tv_pk_mul_f32 v[38:39], v[70:71], s[68:69]\n\tv_exp_f32 v32, v32\n\t" \
    "v_exp_f32 v33, v33\n\tv_exp_f32 v34, v34\n\tv_exp_f32 v35, v35\n\tv_exp_f32 v36, v36\n\tv_exp_f32 v37, v37\n\tv_exp_f32 v38, v38\n\tv_exp_f32 v39, v39\n\tv_pk_add_f32 v[32:33], v[32:33], 1.0 op_sel_hi:[1,0]\n\t" \
    "v_pk_add_f32 v[34:35], v[34:35], 1.0 op_sel_hi:[1,0]\n\tv_pk_add_f32 v[36:37], v[36:37], 1.0 op_sel_hi:[1,0]\n\tv_pk_add_f32 v[38:39], v[38:39], 1.0 op_sel_hi:[1,0]\n\tv_rcp_f32 v32, v32\n\tv_rcp_f32 v33, v33\n\tv_rcp_f32 v34, v34\n\tv_rcp_f32 v35, v35\n\tv_rcp_f32 v36, v36\n\t" \
    "v_rcp_f32 v37, v37\n\tv_rcp_f32 v38, v38\n\tv_rcp_f32 v39, v39\n\tv_pk_mul_f32 v[64:65], v[64:65], v[112:113]\n\tv_pk_mul_f32 v[66:67], v[66:67], v[114:115]\n\tv_pk_mul_f32 v[68:69], v[68:69], v[116:117]\n\tv_pk_mul_f32 v[70:71], v[70:71], v[118:119]\n\tv_pk_fma_f32 v[32:33], v[32:33], v[64:65], v[88:89]\n\t" \
    "v_pk_fma_f32 v[34:35], v[34:35], v[66:67], v[90:91]\n\tv_pk_fma_f32 v[36:37], v[36:37], v[68:69], v[92:93]\n\tv_pk_fma_f32 v[38:39], v[38:39], v[70:71], v[94:95]\n\tv_cvt_pk_bf16_f32 v64, v32, v33\n\tv_cvt_pk_bf16_f32 v65, v34, v35\n\tv_cvt_pk_bf16_f32 v66, v36, v37\n\tv_cvt_pk_bf16_f32 v67, v38, v39\n\ts_nop 0\n\t" \
    "v_permlane16_swap_b32 v64, v66\n\tv_permlane16_swap_b32 v65, v67\n\tbuffer_store_dwordx4 v[64:67], v243, s[24:27], 0 offen offset:64\n\ts_branch 103f\n\t102:\n\ts_add_u32 s49, s49, 4096\n\ts_waitcnt vmcnt(0) lgkmcnt(0)\n\tbuffer_store_dwordx2 v[248:249], off, s[8:11], s49 sc1\n\t" \
    "s_load_dwordx8 s[12:19], s[0:1], 16\n\ts_lshr_b32 s67, s40, 3\n\ts_sub_u32 s67, s67, 24\n\ts_lshl_b32 s67, s67, 2\n\ts_add_u32 s67, s67, s38\n\ts_lshl_b32 s65, s67, 6\n\tv_add_u32 v6, s65, v240\n\tv_mul_u32_u24 v7, 52429, v6\n\t" \
    "v_lshrrev_b32 v7, 18, v7\n\tv_mul_u32_u24 v5, 5, v7\n\tv_sub_u32 v6, v6, v5\n\tv_lshrrev_b32 v5, 7, v7\n\tv_and_b32 v7, 127, v7\n\tv_lshl_add_u32 v7, v5, 10, v7\n\tv_mul_u32_u24 v7, 9216, v7\n\tv_lshl_add_u32 v6, v6, 8, v7\n\t" \
    "v_lshlrev_b32 v7, 3, v244\n\tv_lshl_add_u32 v7, v245, 12, v7\n\ts_and_b32 s65, s40, 7\n\ts_mul_i32 s66, s65, 9\n\ts_lshr_b32 s66, s66, 1\n\ts_lshl_b32 s66, s66, 8\n\ts_lshr_b32 s73, s65, 1\n\ts_mul_i32 s73, s73, 4718592\n\t" \
    "s_and_b32 s65, s65, 1\n\ts_lshl_b32 s65, s65, 11\n\ts_add_u32 s65, s65, s73\n\ts_waitcnt lgkmcnt(0)\n\ts_add_u32 s12, s12, s66\n\ts_addc_u32 s13, s13, 0\n\ts_sub_u32 s14, s14, s66\n\ts_add_u32 s16, s16, s65\n\t" \
    "s_addc_u32 s17, s17, 0\n\ts_sub_u32 s18, s18, s65\n\tv_mov_b32 v238, 2146435072\n\tv_mov_b32 v236, 2146435072\n\tv_mul_u32_u24 v237, 12, v240\n\ts_add_u32 s73, s67, 220\n\tv_add_u32 v237, s73, v237\n\tv_lshrrev_b32 v5, 4, v237\n\t" \
    "v_lshlrev_b32 v5, 7, v5\n\tv_cmp_le_u32 vcc, 144, v237\n\tv_mov_b32 v4, 1088\n\tv_xor_b32 v3, 8, v237\n\tv_cndmask_b32 v4, 0, v4, vcc\n\tv_and_b32 v3, 15, v3\n\tv_lshl_add_u32 v5, v3, 2, v5\n\tv_sub_u32 v5, v5, v4\n\t" \
    "v_subrev_u32 v237, 220, v237\n\tv_cmp_gt_u32 vcc, 68, v237\n\tv_lshlrev_b32 v5, 12, v5\n\ts_mov_b32 s43, 0\n\tv_cndmask_b32 v237, v238, v5, vcc\n\ts_nop 0\n\tv_readlane_b32 s41, v236, 0\n\tv_readlane_b32 s42, v237, 0\n\t" \
    "s_nop 4\n\tbuffer_load_dword v15, v7, s[16:19], s42 offen\n\ts_add_u32 s43, s43, 1\n\tv_readlane_b32 s42, v237, s43\n\ts_nop 4\n\tbuffer_load_dword v15, v7, s[16:19], s42 offen\n\ts_add_u32 s43, s43, 1\n\tv_readlane_b32 s42, v237, s43\n\t" \
    "s_nop 4\n\tbuffer_load_dword v15, v7, s[16:19], s42 offen\n\ts_add_u32 s43, s43, 1\n\tv_readlane_b32 s42, v237, s43\n\ts_nop 4\n\tbuffer_load_dword v15, v7, s[16:19], s42 offen\n\ts_add_u32 s43, s43, 1\n\tv_readlane_b32 s42, v237, s43\n\t" \
    "s_nop 4\n\tbuffer_load_dword v15, v7, s[16:19], s42 offen\n\ts_add_u32 s43, s43, 1\n\tv_readlane_b32 s42, v237, s43\n\ts_nop 4\n\tbuffer_load_dword v15, v7, s[16:19], s42 offen\n\ts_add_u32 s43, s43, 1\n\tv_readlane_b32 s42, v237, s43\n\t" \
    "s_nop 4\n\ts_waitcnt vmcnt(0)\n\ts_branch 103f\n\t101:\n\ts_load_dwordx8 s[12:19], s[0:1], 16\n\ts_lshr_b32 s67, s40, 3\n\ts_sub_u32 s67, s67, 27\n\ts_lshl_b32 s67, s67, 2\n\t" \
    "s_add_u32 s67, s67, s38\n\ts_lshl_b32 s65, s67, 6\n\tv_add_u32 v6, s65, v240\n\tv_mul_u32_u24 v7, 52429, v6\n\tv_lshrrev_b32 v7, 18, v7\n\tv_mul_u32_u24 v5, 5, v7\n\tv_sub_u32 v6, v6, v5\n\tv_lshrrev_b32 v5, 7, v7\n\t" \
    "v_and_b32 v7, 127, v7\n\tv_lshl_add_u32 v7, v5, 10, v7\n\tv_mul_u32_u24 v7, 9216, v7\n\tv_lshl_add_u32 v6, v6, 8, v7\n\tv_lshlrev_b32 v7, 3, v244\n\tv_lshl_add_u32 v7, v245, 12, v7\n\ts_and_b32 s65, s40, 7\n\ts_mul_i32 s66, s65, 9\n\t" \
    "s_lshr_b32 s66, s66, 1\n\ts_lshl_b32 s66, s66, 8\n\ts_lshr_b32 s73, s65, 1\n\ts_mul_i32 s73, s73, 4718592\n\ts_and_b32 s65, s65, 1\n\ts_lshl_b32 s65, s65, 11\n\ts_add_u32 s65, s65, s73\n\ts_waitcnt lgkmcnt(0)\n\t" \
    "s_add_u32 s12, s12, s66\n\ts_addc_u32 s13, s13, 0\n\ts_sub_u32 s14, s14, s66\n\ts_add_u32 s16, s16, s65\n\ts_addc_u32 s17, s17, 0\n\ts_sub_u32 s18, s18, s65\n\tv_mov_b32 v238, 2146435072\n\tv_mov_b32 v236, 2146435072\n\t" \
    "s_mov_b32 s73, 4718592\n\tv_writelane_b32 v236, s73, 0\n\ts_mov_b32 s73, 5898240\n\tv_writelane_b32 v236, s73, 1\n\ts_mov_b32 s73, 7077888\n\tv_writelane_b32 v236, s73, 2\n\ts_mov_b32 s73, 8257536\n\tv_writelane_b32 v236, s73, 3\n\t" \
    "s_mov_b32 s73, 4718720\n\tv_writelane_b32 v236, s73, 4\n\ts_mov_b32 s73, 5898368\n\tv_writelane_b32 v236, s73, 5\n\ts_mov_b32 s73, 7078016\n\tv_writelane_b32 v236, s73, 6\n\ts_mov_b32 s73, 8257664\n\tv_writelane_b32 v236, s73, 7\n\t" \
    "v_mul_u32_u24 v237, 20, v240\n\ts_add_u32 s73, s67, -160\n\tv_add_u32 v237, s73, v237\n\tv_cmp_le_i32 vcc, 220, v237\n\tv_add_u32 v5, 68, v237\n\ts_nop 0\n\tv_cndmask_b32 v237, v237, v5, vcc\n\tv_lshrrev_b32 v5, 4, v237\n\t" \
    "v_lshlrev_b32 v5, 7, v5\n\tv_cmp_le_u32 vcc, 144, v237\n\tv_mov_b32 v4, 1088\n\tv_xor_b32 v3, 8, v237\n\tv_cndmask_b32 v4, 0, v4, vcc\n\tv_and_b32 v3, 15, v3\n\tv_lshl_add_u32 v5, v3, 2, v5\n\tv_sub_u32 v5, v5, v4\n\t" \
    "v_subrev_u32 v237, 0, v237\n\tv_cmp_gt_u32 vcc, 288, v237\n\tv_lshlrev_b32 v5, 12, v5\n\ts_mov_b32 s43, 0\n\tv_cndmask_b32 v237, v238, v5, vcc\n\ts_nop 0\n\tv_readlane_b32 s41, v236, 0\n\tv_readlane_b32 s42, v237, 0\n\t" \
    "s_nop 4\n\ts_sub_u32 s65, s40, 216\n\ts_lshl_b32 s44, s65, 4\n\ts_mul_i32 s66, s65, 12\n\ts_add_u32 s66, s66, 96\n\ts_cmp_lt_u32 s65, 24\n\ts_cselect_b32 s44, s44, s66\n\ts_cselect_b32 s45, 0, 2146435072\n\t" \
    "v_cmp_eq_u32 vcc, 0, v240\n\tv_subrev_u32 v13, 8, v8\n\tv_mov_b32 v12, 2146435072\n\tv_cndmask_b32 v13, v13, v12, vcc\n\ts_mov_b32 s4, s2\n\ts_mov_b32 s5, s3\n\ts_mov_b32 s6, 98f-99b\n\ts_mov_b32 s7, 0x20000\n\t" \
    "v_lshlrev_b32 v14, 7, v240\n\ts_mov_b32 s46, 0xffff\n\tv_and_b32 v204, 15, v240\n\tv_lshlrev_b32 v205, 3, v245\n\tv_sub_u32 v206, v205, v204\n\tv_add_u32 v206, 31, v206\n\tv_and_b32 v207, 3, v206\n\tv_mul_u32_u24 v207, 4160, v207\n\t" \
    "v_and_b32 v206, 0xfc, v206\n\tv_lshl_add_u32 v0, v206, 1, v207\n\ts_mul_i32 s65, s38, 16640\n\ts_add_u32 s65, s65, 73984\n\tv_add_u32 v0, s65, v0\n\ts_add_u32 s66, s65, 1040\n\tv_sub_u32 v2, s66, v8\n\tv_bfe_u32 v206, v240, 1, 2\n\t" \
    "v_mul_u32_u24 v206, 4160, v206\n\tv_and_b32 v207, 16, v8\n\tv_add3_u32 v9, v206, v207, s65\n\ts_mul_i32 s66, s38, 4624\n\tv_lshl_add_u32 v3, v204, 5, v205\n\tv_add_u32 v3, s66, v3\n\tv_add3_u32 v1, v3, v205, 32\n\tv_add_u32 v3, s66, v239\n\t" \
    "s_lshl_b32 s73, s38, 4\n\tv_add_u32 v206, s73, v245\n\tv_bfe_u32 v207, v240, 2, 2\n\tv_mul_u32_u24 v4, 4624, v207\n\tv_lshl_add_u32 v4, v245, 4, v4\n\tv_and_b32 v207, 3, v240\n\tv_lshl_add_u32 v4, v207, 7, v4\n\tv_add_u32 v4, s54, v4\n\t" \
    "v_mul_u32_u24 v5, 12288, v206\n\tv_add_u32 v5, v5, v244\n\tv_lshrrev_b32 v206, 5, v240\n\ts_lshl_b32 s73, s38, 2\n\tv_add_u32 v206, s73, v206\n\tv_mul_u32_u24 v206, 4624, v206\n\tv_and_b32 v207, 0x1f0, v8\n\tv_add_u32 v12, v206, v207\n\t" \
    "v_mul_u32_u24 v10, 9216, v245\n\tv_add_u32 v10, v10, v244\n\tv_mul_u32_u24 v11, 4624, v204\n\tv_lshl_add_u32 v11, v245, 4, v11\n\tv_add_u32 v11, s54, v11\n\ts_mul_i32 s62, s38, 589824\n\ts_lshl_b32 s73, s44, 4\n\ts_add_u32 s62, s62, s73\n\t" \
    "s_add_u32 s62, s62, 2490368\n\tv_lshrrev_b32 v206, 3, v204\n\tv_add_u32 v206, 2, v206\n\tv_cmp_eq_u32 vcc, v206, v245\n\tv_and_b32 v207, 1, v204\n\tv_lshlrev_b32 v207, 4, v207\n\tv_lshlrev_b32 v207, v207, s46\n\tv_cndmask_b32 v207, 0, v207, vcc\n\t" \
    "v_bfe_u32 v206, v204, 1, 2\n\tv_cmp_eq_u32 vcc, 0, v206\n\tv_mov_b32 v196, 0\n\ts_nop 0\n\tv_cndmask_b32 v232, 0, v207, vcc\n\tv_cmp_eq_u32 vcc, 1, v206\n\tv_mov_b32 v197, 0\n\ts_nop 0\n\t" \
    "v_cndmask_b32 v233, 0, v207, vcc\n\tv_cmp_eq_u32 vcc, 2, v206\n\tv_mov_b32 v198, 0\n\ts_nop 0\n\tv_cndmask_b32 v234, 0, v207, vcc\n\tv_cmp_eq_u32 vcc, 3, v206\n\tv_mov_b32 v199, 0\n\ts_nop 0\n\t" \
    "v_cndmask_b32 v235, 0, v207, vcc\n\ts_add_u32 s65, s44, 0\n\ts_lshr_b32 s65, s65, 6\n\ts_cmp_eq_u32 s65, 8\n\ts_cselect_b32 s65, 1, s65\n\ts_and_b32 s65, s65, 3\n\ts_lshl_b32 s47, s65, 11\n\ts_lshr_b32 s65, s54, 1\n\t" \
    "s_add_u32 s47, s47, s65\n\ts_add_u32 s47, s47, 17408\n\ts_add_u32 s65, s44, 11\n\ts_lshr_b32 s65, s65, 6\n\ts_cmp_eq_u32 s65, 8\n\ts_cselect_b32 s65, 1, s65\n\ts_and_b32 s65, s65, 3\n\ts_lshl_b32 s50, s65, 11\n\t" \
    "s_lshr_b32 s65, s54, 1\n\ts_add_u32 s50, s50, s65\n\ts_add_u32 s50, s50, 17408\n\tds_write_b128 v9, v[196:199] offset:4128\n\tbuffer_load_dword v15, v6, s[12:15], s41 offen\n\tbuffer_load_dword v15, v7, s[16:19], s42 offen\n\ts_add_u32 s43, s43, 1\n\tv_readlane_b32 s41, v236, s43\n\t" \
    "v_readlane_b32 s42, v237, s43\n\ts_nop 4\n\ts_waitcnt vmcnt(2)\n\tbuffer_store_dwordx2 v[248:249], off, s[8:11], s48 sc1\n\tv_alignbit_b32 v196, v167, v167, 16\n\tv_alignbit_b32 v197, v166, v166, 16\n\tv_alignbit_b32 v198, v165, v165, 16\n\tv_alignbit_b32 v199, v164, v164, 16\n\t" \
    "ds_write_b128 v2, v[196:199] offset:3072\n\tv_bfi_b32 v200, s46, v167, v166\n\tv_bfi_b32 v201, s46, v166, v165\n\tv_bfi_b32 v202, s46, v165, v164\n\tv_bfi_b32 v203, s46, v164, v181\n\tds_write_b128 v2, v[200:203] offset:7232\n\tv_alignbit_b32 v196, v166, v166, 16\n\tv_alignbit_b32 v197, v165, v165, 16\n\t" \
    "v_alignbit_b32 v198, v164, v164, 16\n\tv_alignbit_b32 v199, v181, v181, 16\n\tds_write_b128 v2, v[196:199] offset:11392\n\tv_bfi_b32 v200, s46, v166, v165\n\tv_bfi_b32 v201, s46, v165, v164\n\tv_bfi_b32 v202, s46, v164, v181\n\tv_bfi_b32 v203, s46, v181, v180\n\tds_write_b128 v2, v[200:203] offset:15552\n\t" \
    "v_alignbit_b32 v196, v171, v171, 16\n\tv_alignbit_b32 v197, v170, v170, 16\n\tv_alignbit_b32 v198, v169, v169, 16\n\tv_alignbit_b32 v199, v168, v168, 16\n\tds_write_b128 v2, v[196:199] offset:2048\n\tv_bfi_b32 v200, s46, v171, v170\n\tv_bfi_b32 v201, s46, v170, v169\n\tv_bfi_b32 v202, s46, v169, v168\n\t" \
    "v_bfi_b32 v203, s46, v168, v183\n\tds_write_b128 v2, v[200:203] offset:6208\n\tv_alignbit_b32 v196, v170, v170, 16\n\tv_alignbit_b32 v197, v169, v169, 16\n\tv_alignbit_b32 v198, v168, v168, 16\n\tv_alignbit_b32 v199, v183, v183, 16\n\tds_write_b128 v2, v[196:199] offset:10368\n\tv_bfi_b32 v200, s46, v170, v169\n\t" \
    "v_bfi_b32 v201, s46, v169, v168\n\tv_bfi_b32 v202, s46, v168, v183\n\tv_bfi_b32 v203, s46, v183, v182\n\tds_write_b128 v2, v[200:203] offset:14528\n\tv_alignbit_b32 v196, v175, v175, 16\n\tv_alignbit_b32 v197, v174, v174, 16\n\tv_alignbit_b32 v198, v173, v173, 16\n\tv_alignbit_b32 v199, v172, v172, 16\n\t" \
    "ds_write_b128 v2, v[196:199] offset:1024\n\tv_bfi_b32 v200, s46, v175, v174\n\tv_bfi_b32 v201, s46, v174, v173\n\tv_bfi_b32 v202, s46, v173, v172\n\tv_bfi_b32 v203, s46, v172, v185\n\tds_write_b128 v2, v[200:203] offset:5184\n\tv_alignbit_b32 v196, v174, v174, 16\n\tv_alignbit_b32 v197, v173, v173, 16\n\t" \
    "v_alignbit_b32 v198, v172, v172, 16\n\tv_alignbit_b32 v199, v185, v185, 16\n\tds_write_b128 v2, v[196:199] offset:9344\n\tv_bfi_b32 v200, s46, v174, v173\n\tv_bfi_b32 v201, s46, v173, v172\n\tv_bfi_b32 v202, s46, v172, v185\n\tv_bfi_b32 v203, s46, v185, v184\n\tds_write_b128 v2, v[200:203] offset:13504\n\t" \
    "v_alignbit_b32 v196, v179, v179, 16\n\tv_alignbit_b32 v197, v178, v178, 16\n\tv_alignbit_b32 v198, v177, v177, 16\n\tv_alignbit_b32 v199, v176, v176, 16\n\tds_write_b128 v2, v[196:199] offset:0\n\tv_bfi_b32 v200, s46, v179, v178\n\tv_bfi_b32 v201, s46, v178, v177\n\tv_bfi_b32 v202, s46, v177, v176\n\t" \
    "v_bfi_b32 v203, s46, v176, v187\n\tds_write_b128 v2, v[200:203] offset:4160\n\tv_alignbit_b32 v196, v178, v178, 16\n\tv_alignbit_b32 v197, v177, v177, 16\n\tv_alignbit_b32 v198, v176, v176, 16\n\tv_alignbit_b32 v199, v187, v187, 16\n\tds_write_b128 v2, v[196:199] offset:8320\n\tv_bfi_b32 v200, s46, v178, v177\n\t" \
    "v_bfi_b32 v201, s46, v177, v176\n\tv_bfi_b32 v202, s46, v176, v187\n\tv_bfi_b32 v203, s46, v187, v186\n\tds_write_b128 v2, v[200:203] offset:12480\n\tv_lshl_or_b32 v188, v188, 16, v188\n\tv_and_b32 v228, v188, v232\n\tv_and_b32 v229, v188, v233\n\tv_and_b32 v230, v188, v234\n\t" \
    "v_and_b32 v231, v188, v235\n\tds_read_b64 a[0:1], v0 offset:4032\n\tds_read_b64 a[2:3], v0 offset:4040\n\tds_read_b64 a[4:5], v0 offset:3520\n\tds_read_b64 a[6:7], v0 offset:3528\n\tds_read_b64 a[8:9], v0 offset:3008\n\tds_read_b64 a[10:11], v0 offset:3016\n\tds_read_b64 a[12:13], v0 offset:2496\n\t" \
    "ds_read_b64 a[14:15], v0 offset:2504\n\tds_read_b64 a[16:17], v0 offset:1984\n\tds_read_b64 a[18:19], v0 offset:1992\n\tds_read_b64 a[20:21], v0 offset:1472\n\tds_read_b64 a[22:23], v0 offset:1480\n\tds_read_b64 a[24:25], v0 offset:960\n\tds_read_b64 a[26:27], v0 offset:968\n\tds_read_b64 a[28:29], v0 offset:448\n\t" \
    "ds_read_b64 a[30:31], v0 offset:456\n\ts_barrier\n\tv_mov_b32 v196, 0\n\tv_mov_b32 v197, 0\n\tv_mov_b32 v198, 0\n\tv_mov_b32 v199, 0\n\tds_write_b128 v12, v[196:199]\n\tds_write_b128 v12, v[196:199] offset:9248\n\t" \
    "s_sleep 20\n\ts_mov_b32 s71, 1048576\n\t122:\n\tbuffer_load_dwordx2 v[250:251], v239, s[8:11], s47 offen sc1\n\tbuffer_load_dwordx2 v[252:253], v239, s[8:11], s50 offen sc1\n\ts_waitcnt vmcnt(0)\n\tv_cmp_ne_u64 vcc, s[36:37], v[250:251]\n\tv_cmp_ne_u64 s[74:75], s[36:37], v[252:253]\n\t" \
    "s_or_b64 s[74:75], s[74:75], vcc\n\ts_cbranch_scc0 123f\n\ts_sleep 16\n\ts_sub_u32 s71, s71, 1\n\ts_cbranch_scc0 122b\n\t123:\n\tbuffer_load_dwordx4 v[68:71], v10, s[8:11], s62 offen sc1\n\ts_add_u32 s62, s62, 36864\n\t" \
    "buffer_load_dwordx4 v[72:75], v10, s[8:11], s62 offen sc1\n\ts_add_u32 s62, s62, 36864\n\tbuffer_load_dwordx4 v[76:79], v10, s[8:11], s62 offen sc1\n\ts_add_u32 s62, s62, 36864\n\tbuffer_load_dwordx4 v[80:83], v10, s[8:11], s62 offen sc1\n\ts_add_u32 s62, s62, 36864\n\tbuffer_load_dwordx4 v[84:87], v10, s[8:11], s62 offen sc1\n\ts_add_u32 s62, s62, 36864\n\t" \
    "buffer_load_dwordx4 v[88:91], v10, s[8:11], s62 offen sc1\n\ts_add_u32 s62, s62, 36864\n\tbuffer_load_dwordx4 v[92:95], v10, s[8:11], s62 offen sc1\n\ts_add_u32 s62, s62, 36864\n\tbuffer_load_dwordx4 v[96:99], v10, s[8:11], s62 offen sc1\n\ts_add_u32 s62, s62, 36864\n\tbuffer_load_dwordx4 v[100:103], v10, s[8:11], s62 offen sc1\n\ts_add_u32 s62, s62, 36864\n\t" \
    "buffer_load_dwordx4 v[104:107], v10, s[8:11], s62 offen sc1\n\ts_add_u32 s62, s62, 36864\n\tbuffer_load_dwordx4 v[108:111], v10, s[8:11], s62 offen sc1\n\ts_add_u32 s62, s62, 36864\n\tbuffer_load_dwordx4 v[112:115], v10, s[8:11], s62 offen sc1\n\ts_add_u32 s62, s62, 36864\n\tbuffer_load_dwordx4 v[116:119], v10, s[8:11], s62 offen sc1\n\ts_add_u32 s62, s62, 36864\n\t" \
    "buffer_load_dwordx4 v[120:123], v10, s[8:11], s62 offen sc1\n\ts_add_u32 s62, s62, 36864\n\tbuffer_load_dwordx4 v[124:127], v10, s[8:11], s62 offen sc1\n\ts_add_u32 s62, s62, 36864\n\tbuffer_load_dwordx4 v[128:131], v10, s[8:11], s62 offen sc1\n\ts_add_u32 s62, s62, 36864\n\ts_waitcnt vmcnt(15)\n\tds_write_b128 v11, v[68:71] offset:512\n\t" \
    "s_waitcnt vmcnt(14)\n\tds_write_b128 v11, v[72:75] offset:576\n\ts_waitcnt vmcnt(13)\n\tds_write_b128 v11, v[76:79] offset:640\n\ts_waitcnt vmcnt(12)\n\tds_write_b128 v11, v[80:83] offset:704\n\ts_waitcnt vmcnt(11)\n\tds_write_b128 v11, v[84:87] offset:768\n\t" \
    "s_waitcnt vmcnt(10)\n\tds_write_b128 v11, v[88:91] offset:832\n\ts_waitcnt vmcnt(9)\n\tds_write_b128 v11, v[92:95] offset:896\n\ts_waitcnt vmcnt(8)\n\tds_write_b128 v11, v[96:99] offset:960\n\ts_waitcnt vmcnt(7)\n\tds_write_b128 v11, v[100:103] offset:1024\n\t" \
    "s_waitcnt vmcnt(6)\n\tds_write_b128 v11, v[104:107] offset:1088\n\ts_waitcnt vmcnt(5)\n\tds_write_b128 v11, v[108:111] offset:1152\n\ts_waitcnt vmcnt(4)\n\tds_write_b128 v11, v[112:115] offset:1216\n\ts_waitcnt vmcnt(3)\n\tds_write_b128 v11, v[116:119] offset:1280\n\t" \
    "s_waitcnt vmcnt(2)\n\tds_write_b128 v11, v[120:123] offset:1344\n\ts_waitcnt vmcnt(1)\n\tds_write_b128 v11, v[124:127] offset:1408\n\ts_waitcnt vmcnt(0)\n\tds_write_b128 v11, v[128:131] offset:1472\n\ts_waitcnt lgkmcnt(0)\n\ts_barrier\n\t" \
    "ds_read_b128 v[68:71], v1 offset:448\n\tds_read_b128 v[72:75], v1 offset:960\n\tds_read_b128 v[76:79], v1 offset:1472\n\tds_read_b128 v[80:83], v1 offset:1984\n\tds_read_b128 v[84:87], v1 offset:2496\n\tds_read_b128 v[88:91], v1 offset:3008\n\tds_read_b128 v[92:95], v1 offset:3520\n\tds_read_b128 v[96:99], v1 offset:4032\n\t" \
    "s_add_u32 s68, s44, s38\n\ts_add_u32 s68, s68, 4\n\ts_lshl_b32 s66, s68, 1\n\ts_lshl_b32 s68, s68, 12\n\tbuffer_load_dwordx4 v[42:45], v8, s[76:79], s68 offen offset:0\n\tbuffer_load_dwordx4 v[46:49], v8, s[76:79], s68 offen offset:1024\n\tbuffer_load_dwordx4 v[50:53], v8, s[76:79], s68 offen offset:2048\n\tbuffer_load_dwordx4 v[54:57], v8, s[76:79], s68 offen offset:3072\n\t" \
    "buffer_load_dwordx2 v[58:59], v13, s[76:79], s68 offen\n\tbuffer_load_dwordx2 v[60:61], v8, s[76:79], s68 offen offset:1016\n\tbuffer_load_dwordx2 v[62:63], v8, s[76:79], s68 offen offset:2040\n\tbuffer_load_dwordx2 v[64:65], v8, s[76:79], s68 offen offset:3064\n\tbuffer_load_ushort v66, off, s[80:83], s66\n\tbuffer_load_dword v15, v6, s[12:15], s41 offen\n\tbuffer_load_dword v15, v7, s[16:19], s42 offen\n\ts_add_u32 s43, s43, 1\n\t" \
    "v_readlane_b32 s41, v236, s43\n\tv_readlane_b32 s42, v237, s43\n\ts_nop 4\n\ts_waitcnt lgkmcnt(0)\n\tv_mfma_f32_16x16x32_bf16 v[132:135], v[228:231], v[68:71], 0\n\tds_read_b64 a[32:33], v0 offset:3968\n\tds_read_b64 a[34:35], v0 offset:3976\n\tv_mfma_f32_16x16x32_bf16 v[136:139], v[228:231], v[72:75], 0\n\t" \
    "ds_read_b64 a[36:37], v0 offset:3456\n\tds_read_b64 a[38:39], v0 offset:3464\n\tv_mfma_f32_16x16x32_bf16 v[140:143], v[228:231], v[76:79], 0\n\tds_read_b64 a[40:41], v0 offset:2944\n\tds_read_b64 a[42:43], v0 offset:2952\n\tv_mfma_f32_16x16x32_bf16 v[144:147], v[228:231], v[80:83], 0\n\tds_read_b64 a[44:45], v0 offset:2432\n\tds_read_b64 a[46:47], v0 offset:2440\n\t" \
    "v_mfma_f32_16x16x32_bf16 v[148:151], v[228:231], v[84:87], 0\n\tds_read_b64 a[48:49], v0 offset:1920\n\tds_read_b64 a[50:51], v0 offset:1928\n\tv_mfma_f32_16x16x32_bf16 v[152:155], v[228:231], v[88:91], 0\n\tds_read_b64 a[52:53], v0 offset:1408\n\tv_mfma_f32_16x16x32_bf16 v[156:159], v[228:231], v[92:95], 0\n\tds_read_b64 a[54:55], v0 offset:1416\n\tv_mfma_f32_16x16x32_bf16 v[160:163], v[228:231], v[96:99], 0\n\t" \
    "ds_read_b64 a[56:57], v0 offset:896\n\tv_mfma_f32_16x16x32_bf16 v[132:135], a[0:3], v[68:71], v[132:135]\n\tds_read_b64 a[58:59], v0 offset:904\n\tv_mfma_f32_16x16x32_bf16 v[136:139], a[0:3], v[72:75], v[136:139]\n\tds_read_b64 a[60:61], v0 offset:384\n\tv_mfma_f32_16x16x32_bf16 v[140:143], a[0:3], v[76:79], v[140:143]\n\tds_read_b64 a[62:63], v0 offset:392\n\tv_mfma_f32_16x16x32_bf16 v[144:147], a[0:3], v[80:83], v[144:147]\n\t" \
    "ds_read_b128 v[100:103], v1 offset:384\n\tv_mfma_f32_16x16x32_bf16 v[148:151], a[0:3], v[84:87], v[148:151]\n\tds_read_b128 v[104:107], v1 offset:896\n\tv_mfma_f32_16x16x32_bf16 v[152:155], a[0:3], v[88:91], v[152:155]\n\tds_read_b128 v[108:111], v1 offset:1408\n\tv_mfma_f32_16x16x32_bf16 v[156:159], a[0:3], v[92:95], v[156:159]\n\tds_read_b128 v[112:115], v1 offset:1920\n\tv_mfma_f32_16x16x32_bf16 v[160:163], a[0:3], v[96:99], v[160:163]\n\t" \
    "ds_read_b128 v[116:119], v1 offset:2432\n\tv_mfma_f32_16x16x32_bf16 v[136:139], a[4:7], v[68:71], v[136:139]\n\tds_read_b128 v[120:123], v1 offset:2944\n\tv_mfma_f32_16x16x32_bf16 v[140:143], a[4:7], v[72:75], v[140:143]\n\tbuffer_load_dword v15, v6, s[12:15], s41 offen\n\tds_read_b128 v[124:127], v1 offset:3456\n\tv_mfma_f32_16x16x32_bf16 v[144:147], a[4:7], v[76:79], v[144:147]\n\tbuffer_load_dword v15, v7, s[16:19], s42 offen\n\t" \
    "ds_read_b128 v[128:131], v1 offset:3968\n\tv_mfma_f32_16x16x32_bf16 v[148:151], a[4:7], v[80:83], v[148:151]\n\ts_add_u32 s43, s43, 1\n\tds_read_b64 a[64:65], v0 offset:3904\n\tv_mfma_f32_16x16x32_bf16 v[152:155], a[4:7], v[84:87], v[152:155]\n\tv_readlane_b32 s41, v236, s43\n\tv_readlane_b32 s42, v237, s43\n\ts_nop 4\n\t" \
    "ds_read_b64 a[66:67], v0 offset:3912\n\tv_mfma_f32_16x16x32_bf16 v[156:159], a[4:7], v[88:91], v[156:159]\n\tds_read_b64 a[68:69], v0 offset:3392\n\tv_mfma_f32_16x16x32_bf16 v[160:163], a[4:7], v[92:95], v[160:163]\n\tds_read_b64 a[70:71], v0 offset:3400\n\tv_mfma_f32_16x16x32_bf16 v[140:143], a[8:11], v[68:71], v[140:143]\n\tds_read_b64 a[72:73], v0 offset:2880\n\tv_mfma_f32_16x16x32_bf16 v[144:147], a[8:11], v[72:75], v[144:147]\n\t" \
    "ds_read_b64 a[74:75], v0 offset:2888\n\tv_mfma_f32_16x16x32_bf16 v[148:151], a[8:11], v[76:79], v[148:151]\n\tds_read_b64 a[76:77], v0 offset:2368\n\tv_mfma_f32_16x16x32_bf16 v[152:155], a[8:11], v[80:83], v[152:155]\n\tds_read_b64 a[78:79], v0 offset:2376\n\tv_mfma_f32_16x16x32_bf16 v[156:159], a[8:11], v[84:87], v[156:159]\n\tds_read_b64 a[80:81], v0 offset:1856\n\tv_mfma_f32_16x16x32_bf16 v[160:163], a[8:11], v[88:91], v[160:163]\n\t" \
    "ds_read_b64 a[82:83], v0 offset:1864\n\tv_mfma_f32_16x16x32_bf16 v[144:147], a[12:15], v[68:71], v[144:147]\n\tds_read_b64 a[84:85], v0 offset:1344\n\tv_mfma_f32_16x16x32_bf16 v[148:151], a[12:15], v[72:75], v[148:151]\n\tds_read_b64 a[86:87], v0 offset:1352\n\tv_mfma_f32_16x16x32_bf16 v[152:155], a[12:15], v[76:79], v[152:155]\n\tds_read_b64 a[88:89], v0 offset:832\n\tv_mfma_f32_16x16x32_bf16 v[156:159], a[12:15], v[80:83], v[156:159]\n\t" \
    "ds_read_b64 a[90:91], v0 offset:840\n\tv_mfma_f32_16x16x32_bf16 v[160:163], a[12:15], v[84:87], v[160:163]\n\tds_read_b64 a[92:93], v0 offset:320\n\tv_mfma_f32_16x16x32_bf16 v[148:151], a[16:19], v[68:71], v[148:151]\n\tds_read_b64 a[94:95], v0 offset:328\n\tv_mfma_f32_16x16x32_bf16 v[152:155], a[16:19], v[72:75], v[152:155]\n\tds_read_b64 a[96:97], v0 offset:3840\n\tv_mfma_f32_16x16x32_bf16 v[156:159], a[16:19], v[76:79], v[156:159]\n\t" \
    "ds_read_b64 a[98:99], v0 offset:3848\n\tv_mfma_f32_16x16x32_bf16 v[160:163], a[16:19], v[80:83], v[160:163]\n\tds_read_b64 a[100:101], v0 offset:3328\n\tv_mfma_f32_16x16x32_bf16 v[152:155], a[20:23], v[68:71], v[152:155]\n\tds_read_b64 a[102:103], v0 offset:3336\n\tv_mfma_f32_16x16x32_bf16 v[156:159], a[20:23], v[72:75], v[156:159]\n\tv_mfma_f32_16x16x32_bf16 v[160:163], a[20:23], v[76:79], v[160:163]\n\tv_mfma_f32_16x16x32_bf16 v[156:159], a[24:27], v[68:71], v[156:159]\n\t" \
    "v_mfma_f32_16x16x32_bf16 v[160:163], a[24:27], v[72:75], v[160:163]\n\tv_mfma_f32_16x16x32_bf16 v[160:163], a[28:31], v[68:71], v[160:163]\n\ts_waitcnt lgkmcnt(15)\n\tv_mfma_f32_16x16x32_bf16 v[132:135], a[32:35], v[100:103], v[132:135]\n\tds_read_b64 a[104:105], v0 offset:2816\n\tds_read_b64 a[106:107], v0 offset:2824\n\tv_mfma_f32_16x16x32_bf16 v[136:139], a[32:35], v[104:107], v[136:139]\n\tds_read_b64 a[108:109], v0 offset:2304\n\t" \
    "ds_read_b64 a[110:111], v0 offset:2312\n\tv_mfma_f32_16x16x32_bf16 v[140:143], a[32:35], v[108:111], v[140:143]\n\tds_read_b64 a[112:113], v0 offset:1792\n\tds_read_b64 a[114:115], v0 offset:1800\n\tv_mfma_f32_16x16x32_bf16 v[144:147], a[32:35], v[112:115], v[144:147]\n\tds_read_b64 a[116:117], v0 offset:1280\n\tds_read_b64 a[118:119], v0 offset:1288\n\tv_mfma_f32_16x16x32_bf16 v[148:151], a[32:35], v[116:119], v[148:151]\n\t" \
    "ds_read_b64 a[120:121], v0 offset:768\n\tds_read_b64 a[122:123], v0 offset:776\n\tv_mfma_f32_16x16x32_bf16 v[152:155], a[32:35], v[120:123], v[152:155]\n\tds_read_b64 a[124:125], v0 offset:256\n\tv_mfma_f32_16x16x32_bf16 v[156:159], a[32:35], v[124:127], v[156:159]\n\tds_read_b64 a[126:127], v0 offset:264\n\tv_mfma_f32_16x16x32_bf16 v[160:163], a[32:35], v[128:131], v[160:163]\n\tds_read_b64 a[128:129], v0 offset:3776\n\t" \
    "v_mfma_f32_16x16x32_bf16 v[136:139], a[36:39], v[100:103], v[136:139]\n\tds_read_b64 a[130:131], v0 offset:3784\n\tv_mfma_f32_16x16x32_bf16 v[140:143], a[36:39], v[104:107], v[140:143]\n\tds_read_b64 a[132:133], v0 offset:3264\n\tv_mfma_f32_16x16x32_bf16 v[144:147], a[36:39], v[108:111], v[144:147]\n\tds_read_b64 a[134:135], v0 offset:3272\n\tv_mfma_f32_16x16x32_bf16 v[148:151], a[36:39], v[112:115], v[148:151]\n\tds_read_b128 v[68:71], v1 offset:320\n\t" \
    "v_mfma_f32_16x16x32_bf16 v[152:155], a[36:39], v[116:119], v[152:155]\n\tds_read_b128 v[72:75], v1 offset:832\n\tv_mfma_f32_16x16x32_bf16 v[156:159], a[36:39], v[120:123], v[156:159]\n\tds_read_b128 v[76:79], v1 offset:1344\n\tv_mfma_f32_16x16x32_bf16 v[160:163], a[36:39], v[124:127], v[160:163]\n\tds_read_b128 v[80:83], v1 offset:1856\n\tv_mfma_f32_16x16x32_bf16 v[140:143], a[40:43], v[100:103], v[140:143]\n\tds_read_b128 v[84:87], v1 offset:2368\n\t" \
    "v_mfma_f32_16x16x32_bf16 v[144:147], a[40:43], v[104:107], v[144:147]\n\tds_read_b128 v[88:91], v1 offset:2880\n\tv_mfma_f32_16x16x32_bf16 v[148:151], a[40:43], v[108:111], v[148:151]\n\tds_read_b128 v[92:95], v1 offset:3392\n\tv_mfma_f32_16x16x32_bf16 v[152:155], a[40:43], v[112:115], v[152:155]\n\tds_read_b128 v[96:99], v1 offset:3904\n\tv_mfma_f32_16x16x32_bf16 v[156:159], a[40:43], v[116:119], v[156:159]\n\tds_read_b64 a[136:137], v0 offset:2752\n\t" \
    "v_mfma_f32_16x16x32_bf16 v[160:163], a[40:43], v[120:123], v[160:163]\n\tds_read_b64 a[138:139], v0 offset:2760\n\tv_mfma_f32_16x16x32_bf16 v[144:147], a[44:47], v[100:103], v[144:147]\n\tds_read_b64 a[140:141], v0 offset:2240\n\tv_mfma_f32_16x16x32_bf16 v[148:151], a[44:47], v[104:107], v[148:151]\n\tds_read_b64 a[142:143], v0 offset:2248\n\tv_mfma_f32_16x16x32_bf16 v[152:155], a[44:47], v[108:111], v[152:155]\n\tds_read_b64 a[144:145], v0 offset:1728\n\t" \
    "v_mfma_f32_16x16x32_bf16 v[156:159], a[44:47], v[112:115], v[156:159]\n\tds_read_b64 a[146:147], v0 offset:1736\n\tv_mfma_f32_16x16x32_bf16 v[160:163], a[44:47], v[116:119], v[160:163]\n\tds_read_b64 a[148:149], v0 offset:1216\n\tv_mfma_f32_16x16x32_bf16 v[148:151], a[48:51], v[100:103], v[148:151]\n\tds_read_b64 a[150:151], v0 offset:1224\n\tv_mfma_f32_16x16x32_bf16 v[152:155], a[48:51], v[104:107], v[152:155]\n\tds_read_b64 a[152:153], v0 offset:704\n\t" \
    "v_mfma_f32_16x16x32_bf16 v[156:159], a[48:51], v[108:111], v[156:159]\n\tds_read_b64 a[154:155], v0 offset:712\n\tv_mfma_f32_16x16x32_bf16 v[160:163], a[48:51], v[112:115], v[160:163]\n\tds_read_b64 a[156:157], v0 offset:192\n\tv_mfma_f32_16x16x32_bf16 v[152:155], a[52:55], v[100:103], v[152:155]\n\tds_read_b64 a[158:159], v0 offset:200\n\tv_mfma_f32_16x16x32_bf16 v[156:159], a[52:55], v[104:107], v[156:159]\n\tv_mfma_f32_16x16x32_bf16 v[160:163], a[52:55], v[108:111], v[160:163]\n\t" \
    "buffer_load_dword v15, v6, s[12:15], s41 offen\n\tv_mfma_f32_16x16x32_bf16 v[156:159], a[56:59], v[100:103], v[156:159]\n\tbuffer_load_dword v15, v7, s[16:19], s42 offen\n\tv_mfma_f32_16x16x32_bf16 v[160:163], a[56:59], v[104:107], v[160:163]\n\ts_add_u32 s43, s43, 1\n\tv_mfma_f32_16x16x32_bf16 v[160:163], a[60:63], v[100:103], v[160:163]\n\tv_readlane_b32 s41, v236, s43\n\tv_readlane_b32 s42, v237, s43\n\t" \
    "s_nop 4\n\ts_waitcnt lgkmcnt(12)\n\tv_mfma_f32_16x16x32_bf16 v[132:135], a[64:67], v[68:71], v[132:135]\n\tds_read_b64 a[160:161], v0 offset:3712\n\tds_read_b64 a[162:163], v0 offset:3720\n\tv_mfma_f32_16x16x32_bf16 v[136:139], a[64:67], v[72:75], v[136:139]\n\tds_read_b64 a[164:165], v0 offset:3200\n\tds_read_b64 a[166:167], v0 offset:3208\n\t" \
    "v_mfma_f32_16x16x32_bf16 v[140:143], a[64:67], v[76:79], v[140:143]\n\tds_read_b64 a[168:169], v0 offset:2688\n\tds_read_b64 a[170:171], v0 offset:2696\n\tv_mfma_f32_16x16x32_bf16 v[144:147], a[64:67], v[80:83], v[144:147]\n\tds_read_b64 a[172:173], v0 offset:2176\n\tds_read_b64 a[174:175], v0 offset:2184\n\tv_mfma_f32_16x16x32_bf16 v[148:151], a[64:67], v[84:87], v[148:151]\n\tds_read_b64 a[176:177], v0 offset:1664\n\t" \
    "ds_read_b64 a[178:179], v0 offset:1672\n\tv_mfma_f32_16x16x32_bf16 v[152:155], a[64:67], v[88:91], v[152:155]\n\tds_read_b64 a[180:181], v0 offset:1152\n\tv_mfma_f32_16x16x32_bf16 v[156:159], a[64:67], v[92:95], v[156:159]\n\tds_read_b64 a[182:183], v0 offset:1160\n\tv_mfma_f32_16x16x32_bf16 v[160:163], a[64:67], v[96:99], v[160:163]\n\tds_read_b64 a[184:185], v0 offset:640\n\tv_mfma_f32_16x16x32_bf16 v[136:139], a[68:71], v[68:71], v[136:139]\n\t" \
    "ds_read_b64 a[186:187], v0 offset:648\n\tv_mfma_f32_16x16x32_bf16 v[140:143], a[68:71], v[72:75], v[140:143]\n\tds_read_b64 a[188:189], v0 offset:128\n\tv_mfma_f32_16x16x32_bf16 v[144:147], a[68:71], v[76:79], v[144:147]\n\tds_read_b64 a[190:191], v0 offset:136\n\tv_mfma_f32_16x16x32_bf16 v[148:151], a[68:71], v[80:83], v[148:151]\n\tds_read_b128 v[100:103], v1 offset:256\n\tv_mfma_f32_16x16x32_bf16 v[152:155], a[68:71], v[84:87], v[152:155]\n\t" \
    "ds_read_b128 v[104:107], v1 offset:768\n\tv_mfma_f32_16x16x32_bf16 v[156:159], a[68:71], v[88:91], v[156:159]\n\tds_read_b128 v[108:111], v1 offset:1280\n\tv_mfma_f32_16x16x32_bf16 v[160:163], a[68:71], v[92:95], v[160:163]\n\tds_read_b128 v[112:115], v1 offset:1792\n\tv_mfma_f32_16x16x32_bf16 v[140:143], a[72:75], v[68:71], v[140:143]\n\tds_read_b128 v[116:119], v1 offset:2304\n\tv_mfma_f32_16x16x32_bf16 v[144:147], a[72:75], v[72:75], v[144:147]\n\t" \
    "ds_read_b128 v[120:123], v1 offset:2816\n\tv_mfma_f32_16x16x32_bf16 v[148:151], a[72:75], v[76:79], v[148:151]\n\tds_read_b128 v[124:127], v1 offset:3328\n\tv_mfma_f32_16x16x32_bf16 v[152:155], a[72:75], v[80:83], v[152:155]\n\tds_read_b128 v[128:131], v1 offset:3840\n\tv_mfma_f32_16x16x32_bf16 v[156:159], a[72:75], v[84:87], v[156:159]\n\tds_read_b64 a[192:193], v0 offset:3648\n\tv_mfma_f32_16x16x32_bf16 v[160:163], a[72:75], v[88:91], v[160:163]\n\t" \
    "ds_read_b64 a[194:195], v0 offset:3656\n\tv_mfma_f32_16x16x32_bf16 v[144:147], a[76:79], v[68:71], v[144:147]\n\tds_read_b64 a[196:197], v0 offset:3136\n\tv_mfma_f32_16x16x32_bf16 v[148:151], a[76:79], v[72:75], v[148:151]\n\tds_read_b64 a[198:199], v0 offset:3144\n\tv_mfma_f32_16x16x32_bf16 v[152:155], a[76:79], v[76:79], v[152:155]\n\tds_read_b64 a[200:201], v0 offset:2624\n\tv_mfma_f32_16x16x32_bf16 v[156:159], a[76:79], v[80:83], v[156:159]\n\t" \
    "ds_read_b64 a[202:203], v0 offset:2632\n\tv_mfma_f32_16x16x32_bf16 v[160:163], a[76:79], v[84:87], v[160:163]\n\tds_read_b64 a[204:205], v0 offset:2112\n\tv_mfma_f32_16x16x32_bf16 v[148:151], a[80:83], v[68:71], v[148:151]\n\tds_read_b64 a[206:207], v0 offset:2120\n\tv_mfma_f32_16x16x32_bf16 v[152:155], a[80:83], v[72:75], v[152:155]\n\tds_read_b64 a[208:209], v0 offset:1600\n\tv_mfma_f32_16x16x32_bf16 v[156:159], a[80:83], v[76:79], v[156:159]\n\t" \
    "ds_read_b64 a[210:211], v0 offset:1608\n\tv_mfma_f32_16x16x32_bf16 v[160:163], a[80:83], v[80:83], v[160:163]\n\tds_read_b64 a[212:213], v0 offset:1088\n\tv_mfma_f32_16x16x32_bf16 v[152:155], a[84:87], v[68:71], v[152:155]\n\tds_read_b64 a[214:215], v0 offset:1096\n\tv_mfma_f32_16x16x32_bf16 v[156:159], a[84:87], v[72:75], v[156:159]\n\tv_mfma_f32_16x16x32_bf16 v[160:163], a[84:87], v[76:79], v[160:163]\n\tv_mfma_f32_16x16x32_bf16 v[156:159], a[88:91], v[68:71], v[156:159]\n\t" \
    "v_mfma_f32_16x16x32_bf16 v[160:163], a[88:91], v[72:75], v[160:163]\n\tv_mfma_f32_16x16x32_bf16 v[160:163], a[92:95], v[68:71], v[160:163]\n\ts_waitcnt lgkmcnt(12)\n\tv_mfma_f32_16x16x32_bf16 v[132:135], a[96:99], v[100:103], v[132:135]\n\tds_read_b64 a[216:217], v0 offset:576\n\tv_mfma_f32_16x16x32_bf16 v[136:139], a[96:99], v[104:107], v[136:139]\n\tds_read_b64 a[218:219], v0 offset:584\n\tv_mfma_f32_16x16x32_bf16 v[140:143], a[96:99], v[108:111], v[140:143]\n\t" \
    "ds_read_b64 a[220:221], v0 offset:64\n\tv_mfma_f32_16x16x32_bf16 v[144:147], a[96:99], v[112:115], v[144:147]\n\tds_read_b64 a[222:223], v0 offset:72\n\tv_mfma_f32_16x16x32_bf16 v[148:151], a[96:99], v[116:119], v[148:151]\n\tds_read_b64 a[224:225], v0 offset:3584\n\tv_mfma_f32_16x16x32_bf16 v[152:155], a[96:99], v[120:123], v[152:155]\n\tds_read_b64 a[226:227], v0 offset:3592\n\tv_mfma_f32_16x16x32_bf16 v[156:159], a[96:99], v[124:127], v[156:159]\n\t" \
    "ds_read_b64 a[228:229], v0 offset:3072\n\tv_mfma_f32_16x16x32_bf16 v[160:163], a[96:99], v[128:131], v[160:163]\n\tds_read_b64 a[230:231], v0 offset:3080\n\tv_mfma_f32_16x16x32_bf16 v[136:139], a[100:103], v[100:103], v[136:139]\n\tds_read_b64 a[232:233], v0 offset:2560\n\tv_mfma_f32_16x16x32_bf16 v[140:143], a[100:103], v[104:107], v[140:143]\n\tds_read_b64 a[234:235], v0 offset:2568\n\tv_mfma_f32_16x16x32_bf16 v[144:147], a[100:103], v[108:111], v[144:147]\n\t" \
    "ds_read_b64 a[236:237], v0 offset:2048\n\tv_mfma_f32_16x16x32_bf16 v[148:151], a[100:103], v[112:115], v[148:151]\n\tds_read_b64 a[238:239], v0 offset:2056\n\tv_mfma_f32_16x16x32_bf16 v[152:155], a[100:103], v[116:119], v[152:155]\n\tds_read_b64 a[240:241], v0 offset:1536\n\tv_mfma_f32_16x16x32_bf16 v[156:159], a[100:103], v[120:123], v[156:159]\n\tds_read_b64 a[242:243], v0 offset:1544\n\tv_mfma_f32_16x16x32_bf16 v[160:163], a[100:103], v[124:127], v[160:163]\n\t" \
    "ds_read_b64 a[244:245], v0 offset:1024\n\tv_mfma_f32_16x16x32_bf16 v[140:143], a[104:107], v[100:103], v[140:143]\n\tds_read_b64 a[246:247], v0 offset:1032\n\tv_mfma_f32_16x16x32_bf16 v[144:147], a[104:107], v[104:107], v[144:147]\n\tds_read_b128 v[68:71], v1 offset:192\n\tv_mfma_f32_16x16x32_bf16 v[148:151], a[104:107], v[108:111], v[148:151]\n\tds_read_b128 v[72:75], v1 offset:704\n\tv_mfma_f32_16x16x32_bf16 v[152:155], a[104:107], v[112:115], v[152:155]\n\t" \
    "ds_read_b128 v[76:79], v1 offset:1216\n\tv_mfma_f32_16x16x32_bf16 v[156:159], a[104:107], v[116:119], v[156:159]\n\tds_read_b128 v[80:83], v1 offset:1728\n\tv_mfma_f32_16x16x32_bf16 v[160:163], a[104:107], v[120:123], v[160:163]\n\tbuffer_load_dword v15, v6, s[12:15], s41 offen\n\tds_read_b128 v[84:87], v1 offset:2240\n\tv_mfma_f32_16x16x32_bf16 v[144:147], a[108:111], v[100:103], v[144:147]\n\tbuffer_load_dword v15, v7, s[16:19], s42 offen\n\t" \
    "ds_read_b128 v[88:91], v1 offset:2752\n\tv_mfma_f32_16x16x32_bf16 v[148:151], a[108:111], v[104:107], v[148:151]\n\ts_add_u32 s43, s43, 1\n\tds_read_b128 v[92:95], v1 offset:3264\n\tv_mfma_f32_16x16x32_bf16 v[152:155], a[108:111], v[108:111], v[152:155]\n\tv_readlane_b32 s41, v236, s43\n\tv_readlane_b32 s42, v237, s43\n\ts_nop 4\n\t" \
    "ds_read_b128 v[96:99], v1 offset:3776\n\tv_mfma_f32_16x16x32_bf16 v[156:159], a[108:111], v[112:115], v[156:159]\n\tds_read_b64 a[248:249], v0 offset:512\n\tv_mfma_f32_16x16x32_bf16 v[160:163], a[108:111], v[116:119], v[160:163]\n\tds_read_b64 a[250:251], v0 offset:520\n\tv_mfma_f32_16x16x32_bf16 v[148:151], a[112:115], v[100:103], v[148:151]\n\tds_read_b64 a[252:253], v0 offset:0\n\tv_mfma_f32_16x16x32_bf16 v[152:155], a[112:115], v[104:107], v[152:155]\n\t" \
    "ds_read_b64 a[254:255], v0 offset:8\n\tv_mfma_f32_16x16x32_bf16 v[156:159], a[112:115], v[108:111], v[156:159]\n\tv_mfma_f32_16x16x32_bf16 v[160:163], a[112:115], v[112:115], v[160:163]\n\tv_mfma_f32_16x16x32_bf16 v[152:155], a[116:119], v[100:103], v[152:155]\n\tv_mfma_f32_16x16x32_bf16 v[156:159], a[116:119], v[104:107], v[156:159]\n\tv_mfma_f32_16x16x32_bf16 v[160:163], a[116:119], v[108:111], v[160:163]\n\tv_mfma_f32_16x16x32_bf16 v[156:159], a[120:123], v[100:103], v[156:159]\n\tv_mfma_f32_16x16x32_bf16 v[160:163], a[120:123], v[104:107], v[160:163]\n\t" \
    "v_mfma_f32_16x16x32_bf16 v[160:163], a[124:127], v[100:103], v[160:163]\n\ts_waitcnt lgkmcnt(4)\n\tv_mfma_f32_16x16x32_bf16 v[132:135], a[128:131], v[68:71], v[132:135]\n\tds_read_b128 v[100:103], v1 offset:128\n\ts_waitcnt vmcnt(8)\n\tv_alignbit_b32 v196, v45, v45, 16\n\tv_mfma_f32_16x16x32_bf16 v[136:139], a[128:131], v[72:75], v[136:139]\n\tds_read_b128 v[104:107], v1 offset:640\n\t" \
    "v_alignbit_b32 v197, v44, v44, 16\n\tv_mfma_f32_16x16x32_bf16 v[140:143], a[128:131], v[76:79], v[140:143]\n\tds_read_b128 v[108:111], v1 offset:1152\n\tv_alignbit_b32 v198, v43, v43, 16\n\tv_mfma_f32_16x16x32_bf16 v[144:147], a[128:131], v[80:83], v[144:147]\n\tds_read_b128 v[112:115], v1 offset:1664\n\tv_alignbit_b32 v199, v42, v42, 16\n\tv_mfma_f32_16x16x32_bf16 v[148:151], a[128:131], v[84:87], v[148:151]\n\t" \
    "ds_write_b128 v2, v[196:199] offset:3072\n\tv_mfma_f32_16x16x32_bf16 v[152:155], a[128:131], v[88:91], v[152:155]\n\tds_read_b128 v[116:119], v1 offset:2176\n\tv_bfi_b32 v200, s46, v45, v44\n\tv_mfma_f32_16x16x32_bf16 v[156:159], a[128:131], v[92:95], v[156:159]\n\tds_read_b128 v[120:123], v1 offset:2688\n\tv_bfi_b32 v201, s46, v44, v43\n\tv_mfma_f32_16x16x32_bf16 v[160:163], a[128:131], v[96:99], v[160:163]\n\t" \
    "ds_read_b128 v[124:127], v1 offset:3200\n\tv_bfi_b32 v202, s46, v43, v42\n\tv_mfma_f32_16x16x32_bf16 v[136:139], a[132:135], v[68:71], v[136:139]\n\tds_read_b128 v[128:131], v1 offset:3712\n\tv_bfi_b32 v203, s46, v42, v59\n\tv_mfma_f32_16x16x32_bf16 v[140:143], a[132:135], v[72:75], v[140:143]\n\tds_write_b128 v2, v[200:203] offset:7232\n\tv_mfma_f32_16x16x32_bf16 v[144:147], a[132:135], v[76:79], v[144:147]\n\t" \
    "v_alignbit_b32 v196, v44, v44, 16\n\tv_alignbit_b32 v197, v43, v43, 16\n\tv_mfma_f32_16x16x32_bf16 v[148:151], a[132:135], v[80:83], v[148:151]\n\tv_alignbit_b32 v198, v42, v42, 16\n\tv_alignbit_b32 v199, v59, v59, 16\n\tv_mfma_f32_16x16x32_bf16 v[152:155], a[132:135], v[84:87], v[152:155]\n\tds_write_b128 v2, v[196:199] offset:11392\n\tv_mfma_f32_16x16x32_bf16 v[156:159], a[132:135], v[88:91], v[156:159]\n\t" \
    "v_bfi_b32 v200, s46, v44, v43\n\tv_bfi_b32 v201, s46, v43, v42\n\tv_mfma_f32_16x16x32_bf16 v[160:163], a[132:135], v[92:95], v[160:163]\n\tv_bfi_b32 v202, s46, v42, v59\n\tv_bfi_b32 v203, s46, v59, v58\n\tv_mfma_f32_16x16x32_bf16 v[140:143], a[136:139], v[68:71], v[140:143]\n\tds_write_b128 v2, v[200:203] offset:15552\n\tv_mfma_f32_16x16x32_bf16 v[144:147], a[136:139], v[72:75], v[144:147]\n\t" \
    "v_alignbit_b32 v196, v49, v49, 16\n\tv_alignbit_b32 v197, v48, v48, 16\n\tv_mfma_f32_16x16x32_bf16 v[148:151], a[136:139], v[76:79], v[148:151]\n\tv_alignbit_b32 v198, v47, v47, 16\n\tv_alignbit_b32 v199, v46, v46, 16\n\tv_mfma_f32_16x16x32_bf16 v[152:155], a[136:139], v[80:83], v[152:155]\n\tds_write_b128 v2, v[196:199] offset:2048\n\tv_mfma_f32_16x16x32_bf16 v[156:159], a[136:139], v[84:87], v[156:159]\n\t" \
    "v_bfi_b32 v200, s46, v49, v48\n\tv_bfi_b32 v201, s46, v48, v47\n\tv_mfma_f32_16x16x32_bf16 v[160:163], a[136:139], v[88:91], v[160:163]\n\tv_bfi_b32 v202, s46, v47, v46\n\tv_bfi_b32 v203, s46, v46, v61\n\tv_mfma_f32_16x16x32_bf16 v[144:147], a[140:143], v[68:71], v[144:147]\n\tds_write_b128 v2, v[200:203] offset:6208\n\tv_mfma_f32_16x16x32_bf16 v[148:151], a[140:143], v[72:75], v[148:151]\n\t" \
    "v_alignbit_b32 v196, v48, v48, 16\n\tv_alignbit_b32 v197, v47, v47, 16\n\tv_mfma_f32_16x16x32_bf16 v[152:155], a[140:143], v[76:79], v[152:155]\n\tv_alignbit_b32 v198, v46, v46, 16\n\tv_alignbit_b32 v199, v61, v61, 16\n\tv_mfma_f32_16x16x32_bf16 v[156:159], a[140:143], v[80:83], v[156:159]\n\tds_write_b128 v2, v[196:199] offset:10368\n\tv_mfma_f32_16x16x32_bf16 v[160:163], a[140:143], v[84:87], v[160:163]\n\t" \
    "v_bfi_b32 v200, s46, v48, v47\n\tv_bfi_b32 v201, s46, v47, v46\n\tv_mfma_f32_16x16x32_bf16 v[148:151], a[144:147], v[68:71], v[148:151]\n\tv_bfi_b32 v202, s46, v46, v61\n\tv_bfi_b32 v203, s46, v61, v60\n\tv_mfma_f32_16x16x32_bf16 v[152:155], a[144:147], v[72:75], v[152:155]\n\tds_write_b128 v2, v[200:203] offset:14528\n\tv_mfma_f32_16x16x32_bf16 v[156:159], a[144:147], v[76:79], v[156:159]\n\t" \
    "v_alignbit_b32 v196, v53, v53, 16\n\tv_alignbit_b32 v197, v52, v52, 16\n\tv_mfma_f32_16x16x32_bf16 v[160:163], a[144:147], v[80:83], v[160:163]\n\tv_alignbit_b32 v198, v51, v51, 16\n\tv_alignbit_b32 v199, v50, v50, 16\n\tv_mfma_f32_16x16x32_bf16 v[152:155], a[148:151], v[68:71], v[152:155]\n\tds_write_b128 v2, v[196:199] offset:1024\n\tv_mfma_f32_16x16x32_bf16 v[156:159], a[148:151], v[72:75], v[156:159]\n\t" \
    "v_bfi_b32 v200, s46, v53, v52\n\tv_bfi_b32 v201, s46, v52, v51\n\tv_mfma_f32_16x16x32_bf16 v[160:163], a[148:151], v[76:79], v[160:163]\n\tv_bfi_b32 v202, s46, v51, v50\n\tv_bfi_b32 v203, s46, v50, v63\n\tv_mfma_f32_16x16x32_bf16 v[156:159], a[152:155], v[68:71], v[156:159]\n\tds_write_b128 v2, v[200:203] offset:5184\n\tv_mfma_f32_16x16x32_bf16 v[160:163], a[152:155], v[72:75], v[160:163]\n\t" \
    "v_alignbit_b32 v196, v52, v52, 16\n\tv_alignbit_b32 v197, v51, v51, 16\n\tv_mfma_f32_16x16x32_bf16 v[160:163], a[156:159], v[68:71], v[160:163]\n\tv_alignbit_b32 v198, v50, v50, 16\n\tv_alignbit_b32 v199, v63, v63, 16\n\ts_waitcnt lgkmcnt(9)\n\tv_mfma_f32_16x16x32_bf16 v[132:135], a[160:163], v[100:103], v[132:135]\n\tds_write_b128 v2, v[196:199] offset:9344\n\t" \
    "v_mfma_f32_16x16x32_bf16 v[136:139], a[160:163], v[104:107], v[136:139]\n\tds_read_b128 v[68:71], v1 offset:64\n\tv_bfi_b32 v200, s46, v52, v51\n\tv_mfma_f32_16x16x32_bf16 v[140:143], a[160:163], v[108:111], v[140:143]\n\tds_read_b128 v[72:75], v1 offset:576\n\tv_bfi_b32 v201, s46, v51, v50\n\tv_mfma_f32_16x16x32_bf16 v[144:147], a[160:163], v[112:115], v[144:147]\n\tds_read_b128 v[76:79], v1 offset:1088\n\t" \
    "v_bfi_b32 v202, s46, v50, v63\n\tv_mfma_f32_16x16x32_bf16 v[148:151], a[160:163], v[116:119], v[148:151]\n\tds_read_b128 v[80:83], v1 offset:1600\n\tv_bfi_b32 v203, s46, v63, v62\n\tv_mfma_f32_16x16x32_bf16 v[152:155], a[160:163], v[120:123], v[152:155]\n\tds_write_b128 v2, v[200:203] offset:13504\n\tv_mfma_f32_16x16x32_bf16 v[156:159], a[160:163], v[124:127], v[156:159]\n\tds_read_b128 v[84:87], v1 offset:2112\n\t" \
    "v_alignbit_b32 v196, v57, v57, 16\n\tv_mfma_f32_16x16x32_bf16 v[160:163], a[160:163], v[128:131], v[160:163]\n\tbuffer_load_dword v15, v6, s[12:15], s41 offen\n\tds_read_b128 v[88:91], v1 offset:2624\n\tv_mfma_f32_16x16x32_bf16 v[136:139], a[164:167], v[100:103], v[136:139]\n\tbuffer_load_dword v15, v7, s[16:19], s42 offen\n\tds_read_b128 v[92:95], v1 offset:3136\n\tv_mfma_f32_16x16x32_bf16 v[140:143], a[164:167], v[104:107], v[140:143]\n\t" \
    "s_add_u32 s43, s43, 1\n\tds_read_b128 v[96:99], v1 offset:3648\n\tv_alignbit_b32 v197, v56, v56, 16\n\tv_mfma_f32_16x16x32_bf16 v[144:147], a[164:167], v[108:111], v[144:147]\n\tv_readlane_b32 s41, v236, s43\n\tv_readlane_b32 s42, v237, s43\n\ts_nop 4\n\tv_alignbit_b32 v198, v55, v55, 16\n\t" \
    "v_alignbit_b32 v199, v54, v54, 16\n\tv_mfma_f32_16x16x32_bf16 v[148:151], a[164:167], v[112:115], v[148:151]\n\tds_write_b128 v2, v[196:199] offset:0\n\tv_mfma_f32_16x16x32_bf16 v[152:155], a[164:167], v[116:119], v[152:155]\n\tv_bfi_b32 v200, s46, v57, v56\n\tv_bfi_b32 v201, s46, v56, v55\n\tv_mfma_f32_16x16x32_bf16 v[156:159], a[164:167], v[120:123], v[156:159]\n\tv_bfi_b32 v202, s46, v55, v54\n\t" \
    "v_bfi_b32 v203, s46, v54, v65\n\tv_mfma_f32_16x16x32_bf16 v[160:163], a[164:167], v[124:127], v[160:163]\n\tds_write_b128 v2, v[200:203] offset:4160\n\tv_mfma_f32_16x16x32_bf16 v[140:143], a[168:171], v[100:103], v[140:143]\n\tv_alignbit_b32 v196, v56, v56, 16\n\tv_alignbit_b32 v197, v55, v55, 16\n\tv_mfma_f32_16x16x32_bf16 v[144:147], a[168:171], v[104:107], v[144:147]\n\tv_alignbit_b32 v198, v54, v54, 16\n\t" \
    "v_alignbit_b32 v199, v65, v65, 16\n\tv_mfma_f32_16x16x32_bf16 v[148:151], a[168:171], v[108:111], v[148:151]\n\tds_write_b128 v2, v[196:199] offset:8320\n\tv_mfma_f32_16x16x32_bf16 v[152:155], a[168:171], v[112:115], v[152:155]\n\tv_bfi_b32 v200, s46, v56, v55\n\tv_bfi_b32 v201, s46, v55, v54\n\tv_mfma_f32_16x16x32_bf16 v[156:159], a[168:171], v[116:119], v[156:159]\n\tv_bfi_b32 v202, s46, v54, v65\n\t" \
    "v_bfi_b32 v203, s46, v65, v64\n\tv_mfma_f32_16x16x32_bf16 v[160:163], a[168:171], v[120:123], v[160:163]\n\tds_write_b128 v2, v[200:203] offset:12480\n\tv_mfma_f32_16x16x32_bf16 v[144:147], a[172:175], v[100:103], v[144:147]\n\tv_lshl_or_b32 v66, v66, 16, v66\n\tv_and_b32 v228, v66, v232\n\tv_mfma_f32_16x16x32_bf16 v[148:151], a[172:175], v[104:107], v[148:151]\n\tv_and_b32 v229, v66, v233\n\t" \
    "v_and_b32 v230, v66, v234\n\tv_mfma_f32_16x16x32_bf16 v[152:155], a[172:175], v[108:111], v[152:155]\n\tv_and_b32 v231, v66, v235\n\tv_mfma_f32_16x16x32_bf16 v[156:159], a[172:175], v[112:115], v[156:159]\n\tv_mfma_f32_16x16x32_bf16 v[160:163], a[172:175], v[116:119], v[160:163]\n\tv_mfma_f32_16x16x32_bf16 v[148:151], a[176:179], v[100:103], v[148:151]\n\tv_mfma_f32_16x16x32_bf16 v[152:155], a[176:179], v[104:107], v[152:155]\n\tv_mfma_f32_16x16x32_bf16 v[156:159], a[176:179], v[108:111], v[156:159]\n\t" \
    "v_mfma_f32_16x16x32_bf16 v[160:163], a[176:179], v[112:115], v[160:163]\n\tv_mfma_f32_16x16x32_bf16 v[152:155], a[180:183], v[100:103], v[152:155]\n\tv_mfma_f32_16x16x32_bf16 v[156:159], a[180:183], v[104:107], v[156:159]\n\tv_mfma_f32_16x16x32_bf16 v[160:163], a[180:183], v[108:111], v[160:163]\n\tv_mfma_f32_16x16x32_bf16 v[156:159], a[184:187], v[100:103], v[156:159]\n\tv_mfma_f32_16x16x32_bf16 v[160:163], a[184:187], v[104:107], v[160:163]\n\tv_mfma_f32_16x16x32_bf16 v[160:163], a[188:191], v[100:103], v[160:163]\n\ts_waitcnt lgkmcnt(4)\n\t" \
    "v_mfma_f32_16x16x32_bf16 v[132:135], a[192:195], v[68:71], v[132:135]\n\ts_add_u32 s68, s44, s38\n\tds_read_b128 v[100:103], v1 offset:0\n\tv_mfma_f32_16x16x32_bf16 v[136:139], a[192:195], v[72:75], v[136:139]\n\ts_add_u32 s68, s68, 8\n\tds_read_b128 v[104:107], v1 offset:512\n\tv_mfma_f32_16x16x32_bf16 v[140:143], a[192:195], v[76:79], v[140:143]\n\ts_lshl_b32 s66, s68, 1\n\t" \
    "ds_read_b128 v[108:111], v1 offset:1024\n\tv_mfma_f32_16x16x32_bf16 v[144:147], a[192:195], v[80:83], v[144:147]\n\ts_lshl_b32 s68, s68, 12\n\tbuffer_load_dwordx4 v[16:19], v8, s[76:79], s68 offen offset:0\n\tds_read_b128 v[112:115], v1 offset:1536\n\tv_mfma_f32_16x16x32_bf16 v[148:151], a[192:195], v[84:87], v[148:151]\n\tds_read_b128 v[116:119], v1 offset:2048\n\tv_mfma_f32_16x16x32_bf16 v[152:155], a[192:195], v[88:91], v[152:155]\n\t" \
    "ds_read_b128 v[120:123], v1 offset:2560\n\tv_mfma_f32_16x16x32_bf16 v[156:159], a[192:195], v[92:95], v[156:159]\n\tds_read_b128 v[124:127], v1 offset:3072\n\tv_mfma_f32_16x16x32_bf16 v[160:163], a[192:195], v[96:99], v[160:163]\n\tds_read_b128 v[128:131], v1 offset:3584\n\tv_mfma_f32_16x16x32_bf16 v[136:139], a[196:199], v[68:71], v[136:139]\n\tv_mfma_f32_16x16x32_bf16 v[140:143], a[196:199], v[72:75], v[140:143]\n\tbuffer_load_dwordx4 v[20:23], v8, s[76:79], s68 offen offset:1024\n\t" \
    "v_mfma_f32_16x16x32_bf16 v[144:147], a[196:199], v[76:79], v[144:147]\n\tv_mfma_f32_16x16x32_bf16 v[148:151], a[196:199], v[80:83], v[148:151]\n\tv_mfma_f32_16x16x32_bf16 v[152:155], a[196:199], v[84:87], v[152:155]\n\tv_mfma_f32_16x16x32_bf16 v[156:159], a[196:199], v[88:91], v[156:159]\n\tv_mfma_f32_16x16x32_bf16 v[160:163], a[196:199], v[92:95], v[160:163]\n\tv_mfma_f32_16x16x32_bf16 v[140:143], a[200:203], v[68:71], v[140:143]\n\tbuffer_load_dwordx4 v[24:27], v8, s[76:79], s68 offen offset:2048\n\tv_mfma_f32_16x16x32_bf16 v[144:147], a[200:203], v[72:75], v[144:147]\n\t" \
    "v_mfma_f32_16x16x32_bf16 v[148:151], a[200:203], v[76:79], v[148:151]\n\tv_mfma_f32_16x16x32_bf16 v[152:155], a[200:203], v[80:83], v[152:155]\n\tv_mfma_f32_16x16x32_bf16 v[156:159], a[200:203], v[84:87], v[156:159]\n\tv_mfma_f32_16x16x32_bf16 v[160:163], a[200:203], v[88:91], v[160:163]\n\tv_mfma_f32_16x16x32_bf16 v[144:147], a[204:207], v[68:71], v[144:147]\n\tbuffer_load_dwordx4 v[28:31], v8, s[76:79], s68 offen offset:3072\n\tv_mfma_f32_16x16x32_bf16 v[148:151], a[204:207], v[72:75], v[148:151]\n\tv_mfma_f32_16x16x32_bf16 v[152:155], a[204:207], v[76:79], v[152:155]\n\t" \
    "v_mfma_f32_16x16x32_bf16 v[156:159], a[204:207], v[80:83], v[156:159]\n\tv_mfma_f32_16x16x32_bf16 v[160:163], a[204:207], v[84:87], v[160:163]\n\tv_mfma_f32_16x16x32_bf16 v[148:151], a[208:211], v[68:71], v[148:151]\n\tv_mfma_f32_16x16x32_bf16 v[152:155], a[208:211], v[72:75], v[152:155]\n\tbuffer_load_dwordx2 v[32:33], v13, s[76:79], s68 offen\n\tv_mfma_f32_16x16x32_bf16 v[156:159], a[208:211], v[76:79], v[156:159]\n\tv_mfma_f32_16x16x32_bf16 v[160:163], a[208:211], v[80:83], v[160:163]\n\tv_mfma_f32_16x16x32_bf16 v[152:155], a[212:215], v[68:71], v[152:155]\n\t" \
    "v_mfma_f32_16x16x32_bf16 v[156:159], a[212:215], v[72:75], v[156:159]\n\tv_mfma_f32_16x16x32_bf16 v[160:163], a[212:215], v[76:79], v[160:163]\n\tv_mfma_f32_16x16x32_bf16 v[156:159], a[216:219], v[68:71], v[156:159]\n\tbuffer_load_dwordx2 v[34:35], v8, s[76:79], s68 offen offset:1016\n\tv_mfma_f32_16x16x32_bf16 v[160:163], a[216:219], v[72:75], v[160:163]\n\tv_mfma_f32_16x16x32_bf16 v[160:163], a[220:223], v[68:71], v[160:163]\n\ts_waitcnt lgkmcnt(0)\n\tv_mfma_f32_16x16x32_bf16 v[132:135], a[224:227], v[100:103], v[132:135]\n\t" \
    "ds_read_b64 a[0:1], v0 offset:4032\n\tv_mfma_f32_16x16x32_bf16 v[136:139], a[224:227], v[104:107], v[136:139]\n\tds_read_b64 a[2:3], v0 offset:4040\n\tv_mfma_f32_16x16x32_bf16 v[140:143], a[224:227], v[108:111], v[140:143]\n\tds_read_b64 a[4:5], v0 offset:3520\n\tv_mfma_f32_16x16x32_bf16 v[144:147], a[224:227], v[112:115], v[144:147]\n\tbuffer_load_dwordx2 v[36:37], v8, s[76:79], s68 offen offset:2040\n\tds_read_b64 a[6:7], v0 offset:3528\n\t" \
    "v_mfma_f32_16x16x32_bf16 v[148:151], a[224:227], v[116:119], v[148:151]\n\tds_read_b64 a[8:9], v0 offset:3008\n\tv_mfma_f32_16x16x32_bf16 v[152:155], a[224:227], v[120:123], v[152:155]\n\tds_read_b64 a[10:11], v0 offset:3016\n\tv_mfma_f32_16x16x32_bf16 v[156:159], a[224:227], v[124:127], v[156:159]\n\tds_read_b64 a[12:13], v0 offset:2496\n\tv_mfma_f32_16x16x32_bf16 v[160:163], a[224:227], v[128:131], v[160:163]\n\tds_read_b64 a[14:15], v0 offset:2504\n\t" \
    "v_mfma_f32_16x16x32_bf16 v[136:139], a[228:231], v[100:103], v[136:139]\n\tds_read_b64 a[16:17], v0 offset:1984\n\tv_mfma_f32_16x16x32_bf16 v[140:143], a[228:231], v[104:107], v[140:143]\n\tbuffer_load_dwordx2 v[38:39], v8, s[76:79], s68 offen offset:3064\n\tds_read_b64 a[18:19], v0 offset:1992\n\tv_mfma_f32_16x16x32_bf16 v[144:147], a[228:231], v[108:111], v[144:147]\n\tds_read_b64 a[20:21], v0 offset:1472\n\tv_mfma_f32_16x16x32_bf16 v[148:151], a[228:231], v[112:115], v[148:151]\n\t" \
    "ds_read_b64 a[22:23], v0 offset:1480\n\tv_mfma_f32_16x16x32_bf16 v[152:155], a[228:231], v[116:119], v[152:155]\n\tds_read_b64 a[24:25], v0 offset:960\n\tv_mfma_f32_16x16x32_bf16 v[156:159], a[228:231], v[120:123], v[156:159]\n\tds_read_b64 a[26:27], v0 offset:968\n\tv_mfma_f32_16x16x32_bf16 v[160:163], a[228:231], v[124:127], v[160:163]\n\tds_read_b64 a[28:29], v0 offset:448\n\tv_mfma_f32_16x16x32_bf16 v[140:143], a[232:235], v[100:103], v[140:143]\n\t" \
    "buffer_load_ushort v40, off, s[80:83], s66\n\tds_read_b64 a[30:31], v0 offset:456\n\tv_mfma_f32_16x16x32_bf16 v[144:147], a[232:235], v[104:107], v[144:147]\n\tds_read_b128 v[68:71], v1 offset:18944\n\tv_mfma_f32_16x16x32_bf16 v[148:151], a[232:235], v[108:111], v[148:151]\n\tds_read_b128 v[72:75], v1 offset:19456\n\tv_mfma_f32_16x16x32_bf16 v[152:155], a[232:235], v[112:115], v[152:155]\n\tds_read_b128 v[76:79], v1 offset:19968\n\t" \
    "v_mfma_f32_16x16x32_bf16 v[156:159], a[232:235], v[116:119], v[156:159]\n\tds_read_b128 v[80:83], v1 offset:20480\n\tv_mfma_f32_16x16x32_bf16 v[160:163], a[232:235], v[120:123], v[160:163]\n\tds_read_b128 v[84:87], v1 offset:20992\n\tv_mfma_f32_16x16x32_bf16 v[144:147], a[236:239], v[100:103], v[144:147]\n\tbuffer_load_dword v15, v6, s[12:15], s41 offen\n\tds_read_b128 v[88:91], v1 offset:21504\n\tv_mfma_f32_16x16x32_bf16 v[148:151], a[236:239], v[104:107], v[148:151]\n\t" \
    "buffer_load_dword v15, v7, s[16:19], s42 offen\n\tds_read_b128 v[92:95], v1 offset:22016\n\tv_mfma_f32_16x16x32_bf16 v[152:155], a[236:239], v[108:111], v[152:155]\n\ts_add_u32 s43, s43, 1\n\tds_read_b128 v[96:99], v1 offset:22528\n\tv_mfma_f32_16x16x32_bf16 v[156:159], a[236:239], v[112:115], v[156:159]\n\tv_readlane_b32 s41, v236, s43\n\tv_readlane_b32 s42, v237, s43\n\t" \
    "s_nop 4\n\tv_mfma_f32_16x16x32_bf16 v[160:163], a[236:239], v[116:119], v[160:163]\n\tv_mfma_f32_16x16x32_bf16 v[148:151], a[240:243], v[100:103], v[148:151]\n\tv_mfma_f32_16x16x32_bf16 v[152:155], a[240:243], v[104:107], v[152:155]\n\tv_mfma_f32_16x16x32_bf16 v[156:159], a[240:243], v[108:111], v[156:159]\n\tv_mfma_f32_16x16x32_bf16 v[160:163], a[240:243], v[112:115], v[160:163]\n\tv_mfma_f32_16x16x32_bf16 v[152:155], a[244:247], v[100:103], v[152:155]\n\tv_mfma_f32_16x16x32_bf16 v[156:159], a[244:247], v[104:107], v[156:159]\n\t" \
    "v_mfma_f32_16x16x32_bf16 v[160:163], a[244:247], v[108:111], v[160:163]\n\tv_mfma_f32_16x16x32_bf16 v[156:159], a[248:251], v[100:103], v[156:159]\n\tv_mfma_f32_16x16x32_bf16 v[160:163], a[248:251], v[104:107], v[160:163]\n\tv_mfma_f32_16x16x32_bf16 v[160:163], a[252:255], v[100:103], v[160:163]\n\ts_waitcnt lgkmcnt(0)\n\tv_mfma_f32_16x16x32_bf16 v[164:167], v[228:231], v[68:71], 0\n\tds_read_b64 a[32:33], v0 offset:3968\n\tds_read_b64 a[34:35], v0 offset:3976\n\t" \
    "v_mfma_f32_16x16x32_bf16 v[168:171], v[228:231], v[72:75], 0\n\tds_read_b64 a[36:37], v0 offset:3456\n\tds_read_b64 a[38:39], v0 offset:3464\n\tv_mfma_f32_16x16x32_bf16 v[172:175], v[228:231], v[76:79], 0\n\tds_read_b64 a[40:41], v0 offset:2944\n\tds_read_b64 a[42:43], v0 offset:2952\n\tv_mfma_f32_16x16x32_bf16 v[176:179], v[228:231], v[80:83], 0\n\tds_read_b64 a[44:45], v0 offset:2432\n\t" \
    "ds_read_b64 a[46:47], v0 offset:2440\n\tv_mfma_f32_16x16x32_bf16 v[180:183], v[228:231], v[84:87], 0\n\tds_read_b64 a[48:49], v0 offset:1920\n\tds_read_b64 a[50:51], v0 offset:1928\n\tv_mfma_f32_16x16x32_bf16 v[184:187], v[228:231], v[88:91], 0\n\tds_read_b64 a[52:53], v0 offset:1408\n\tv_cvt_pk_bf16_f32 v132, v132, v133\n\tv_mfma_f32_16x16x32_bf16 v[188:191], v[228:231], v[92:95], 0\n\t" \
    "ds_read_b64 a[54:55], v0 offset:1416\n\tv_cvt_pk_bf16_f32 v133, v134, v135\n\tv_mfma_f32_16x16x32_bf16 v[192:195], v[228:231], v[96:99], 0\n\tds_read_b64 a[56:57], v0 offset:896\n\tds_write_b64 v3, v[132:133] offset:0\n\tv_mfma_f32_16x16x32_bf16 v[164:167], a[0:3], v[68:71], v[164:167]\n\tds_read_b64 a[58:59], v0 offset:904\n\tv_cvt_pk_bf16_f32 v136, v136, v137\n\t" \
    "v_mfma_f32_16x16x32_bf16 v[168:171], a[0:3], v[72:75], v[168:171]\n\tds_read_b64 a[60:61], v0 offset:384\n\tv_cvt_pk_bf16_f32 v137, v138, v139\n\tv_mfma_f32_16x16x32_bf16 v[172:175], a[0:3], v[76:79], v[172:175]\n\tds_read_b64 a[62:63], v0 offset:392\n\tds_write_b64 v3, v[136:137] offset:512\n\tv_mfma_f32_16x16x32_bf16 v[176:179], a[0:3], v[80:83], v[176:179]\n\tds_read_b128 v[100:103], v1 offset:18880\n\t" \
    "v_cvt_pk_bf16_f32 v140, v140, v141\n\tv_mfma_f32_16x16x32_bf16 v[180:183], a[0:3], v[84:87], v[180:183]\n\tds_read_b128 v[104:107], v1 offset:19392\n\tv_cvt_pk_bf16_f32 v141, v142, v143\n\tv_mfma_f32_16x16x32_bf16 v[184:187], a[0:3], v[88:91], v[184:187]\n\tds_read_b128 v[108:111], v1 offset:19904\n\tds_write_b64 v3, v[140:141] offset:1024\n\tv_mfma_f32_16x16x32_bf16 v[188:191], a[0:3], v[92:95], v[188:191]\n\t" \
    "ds_read_b128 v[112:115], v1 offset:20416\n\tv_cvt_pk_bf16_f32 v144, v144, v145\n\tv_mfma_f32_16x16x32_bf16 v[192:195], a[0:3], v[96:99], v[192:195]\n\tds_read_b128 v[116:119], v1 offset:20928\n\tv_cvt_pk_bf16_f32 v145, v146, v147\n\tv_mfma_f32_16x16x32_bf16 v[168:171], a[4:7], v[68:71], v[168:171]\n\tds_read_b128 v[120:123], v1 offset:21440\n\tds_write_b64 v3, v[144:145] offset:1536\n\t" \
    "v_mfma_f32_16x16x32_bf16 v[172:175], a[4:7], v[72:75], v[172:175]\n\tbuffer_load_dword v15, v6, s[12:15], s41 offen\n\tds_read_b128 v[124:127], v1 offset:21952\n\tv_mfma_f32_16x16x32_bf16 v[176:179], a[4:7], v[76:79], v[176:179]\n\tbuffer_load_dword v15, v7, s[16:19], s42 offen\n\tds_read_b128 v[128:131], v1 offset:22464\n\tv_mfma_f32_16x16x32_bf16 v[180:183], a[4:7], v[80:83], v[180:183]\n\ts_add_u32 s43, s43, 1\n\t" \
    "ds_read_b64 a[64:65], v0 offset:3904\n\tv_cvt_pk_bf16_f32 v148, v148, v149\n\tv_mfma_f32_16x16x32_bf16 v[184:187], a[4:7], v[84:87], v[184:187]\n\tv_readlane_b32 s41, v236, s43\n\tv_readlane_b32 s42, v237, s43\n\ts_nop 4\n\tds_read_b64 a[66:67], v0 offset:3912\n\tv_cvt_pk_bf16_f32 v149, v150, v151\n\t" \
    "v_mfma_f32_16x16x32_bf16 v[188:191], a[4:7], v[88:91], v[188:191]\n\tds_read_b64 a[68:69], v0 offset:3392\n\tds_write_b64 v3, v[148:149] offset:2048\n\tv_mfma_f32_16x16x32_bf16 v[192:195], a[4:7], v[92:95], v[192:195]\n\tds_read_b64 a[70:71], v0 offset:3400\n\tv_cvt_pk_bf16_f32 v152, v152, v153\n\tv_mfma_f32_16x16x32_bf16 v[172:175], a[8:11], v[68:71], v[172:175]\n\tds_read_b64 a[72:73], v0 offset:2880\n\t" \
    "v_cvt_pk_bf16_f32 v153, v154, v155\n\tv_mfma_f32_16x16x32_bf16 v[176:179], a[8:11], v[72:75], v[176:179]\n\tds_read_b64 a[74:75], v0 offset:2888\n\tds_write_b64 v3, v[152:153] offset:2560\n\tv_mfma_f32_16x16x32_bf16 v[180:183], a[8:11], v[76:79], v[180:183]\n\tds_read_b64 a[76:77], v0 offset:2368\n\tv_cvt_pk_bf16_f32 v156, v156, v157\n\tv_mfma_f32_16x16x32_bf16 v[184:187], a[8:11], v[80:83], v[184:187]\n\t" \
    "ds_read_b64 a[78:79], v0 offset:2376\n\tv_cvt_pk_bf16_f32 v157, v158, v159\n\tv_mfma_f32_16x16x32_bf16 v[188:191], a[8:11], v[84:87], v[188:191]\n\tds_read_b64 a[80:81], v0 offset:1856\n\tds_write_b64 v3, v[156:157] offset:3072\n\tv_mfma_f32_16x16x32_bf16 v[192:195], a[8:11], v[88:91], v[192:195]\n\tds_read_b64 a[82:83], v0 offset:1864\n\tv_cvt_pk_bf16_f32 v160, v160, v161\n\t" \
    "v_mfma_f32_16x16x32_bf16 v[176:179], a[12:15], v[68:71], v[176:179]\n\tds_read_b64 a[84:85], v0 offset:1344\n\tv_cvt_pk_bf16_f32 v161, v162, v163\n\tv_mfma_f32_16x16x32_bf16 v[180:183], a[12:15], v[72:75], v[180:183]\n\tds_read_b64 a[86:87], v0 offset:1352\n\tds_write_b64 v3, v[160:161] offset:3584\n\tv_mfma_f32_16x16x32_bf16 v[184:187], a[12:15], v[76:79], v[184:187]\n\tds_read_b64 a[88:89], v0 offset:832\n\t" \
    "v_mfma_f32_16x16x32_bf16 v[188:191], a[12:15], v[80:83], v[188:191]\n\tds_read_b64 a[90:91], v0 offset:840\n\tv_mfma_f32_16x16x32_bf16 v[192:195], a[12:15], v[84:87], v[192:195]\n\tds_read_b64 a[92:93], v0 offset:320\n\tv_mfma_f32_16x16x32_bf16 v[180:183], a[16:19], v[68:71], v[180:183]\n\tds_read_b64 a[94:95], v0 offset:328\n\tv_mfma_f32_16x16x32_bf16 v[184:187], a[16:19], v[72:75], v[184:187]\n\tds_read_b64 a[96:97], v0 offset:3840\n\t" \
    "v_mfma_f32_16x16x32_bf16 v[188:191], a[16:19], v[76:79], v[188:191]\n\tds_read_b64 a[98:99], v0 offset:3848\n\tv_mfma_f32_16x16x32_bf16 v[192:195], a[16:19], v[80:83], v[192:195]\n\tds_read_b64 a[100:101], v0 offset:3328\n\tv_mfma_f32_16x16x32_bf16 v[184:187], a[20:23], v[68:71], v[184:187]\n\tds_read_b64 a[102:103], v0 offset:3336\n\tv_mfma_f32_16x16x32_bf16 v[188:191], a[20:23], v[72:75], v[188:191]\n\tv_mfma_f32_16x16x32_bf16 v[192:195], a[20:23], v[76:79], v[192:195]\n\t" \
    "v_mfma_f32_16x16x32_bf16 v[188:191], a[24:27], v[68:71], v[188:191]\n\tv_mfma_f32_16x16x32_bf16 v[192:195], a[24:27], v[72:75], v[192:195]\n\tv_mfma_f32_16x16x32_bf16 v[192:195], a[28:31], v[68:71], v[192:195]\n\ts_waitcnt lgkmcnt(8)\n\ts_barrier\n\tv_mfma_f32_16x16x32_bf16 v[164:167], a[32:35], v[100:103], v[164:167]\n\tds_read_b64 a[104:105], v0 offset:2816\n\tds_read_b64 a[106:107], v0 offset:2824\n\t" \
    "v_mfma_f32_16x16x32_bf16 v[168:171], a[32:35], v[104:107], v[168:171]\n\tds_read_b64 a[108:109], v0 offset:2304\n\tds_read_b64 a[110:111], v0 offset:2312\n\tv_mfma_f32_16x16x32_bf16 v[172:175], a[32:35], v[108:111], v[172:175]\n\tds_read_b64 a[112:113], v0 offset:1792\n\tds_read_b64 a[114:115], v0 offset:1800\n\tv_mfma_f32_16x16x32_bf16 v[176:179], a[32:35], v[112:115], v[176:179]\n\tds_read_b64 a[116:117], v0 offset:1280\n\t" \
    "ds_read_b64 a[118:119], v0 offset:1288\n\tv_mfma_f32_16x16x32_bf16 v[180:183], a[32:35], v[116:119], v[180:183]\n\tds_read_b64 a[120:121], v0 offset:768\n\tds_read_b64 a[122:123], v0 offset:776\n\tv_mfma_f32_16x16x32_bf16 v[184:187], a[32:35], v[120:123], v[184:187]\n\tds_read_b64 a[124:125], v0 offset:256\n\ts_add_u32 s51, s44, 0\n\tv_mfma_f32_16x16x32_bf16 v[188:191], a[32:35], v[124:127], v[188:191]\n\t" \
    "ds_read_b64 a[126:127], v0 offset:264\n\ts_mul_i32 s65, s51, 1366\n\tv_mfma_f32_16x16x32_bf16 v[192:195], a[32:35], v[128:131], v[192:195]\n\tds_read_b64 a[128:129], v0 offset:3776\n\ts_lshr_b32 s65, s65, 16\n\tv_mfma_f32_16x16x32_bf16 v[168:171], a[36:39], v[100:103], v[168:171]\n\tds_read_b64 a[130:131], v0 offset:3784\n\ts_mul_i32 s56, s65, 48\n\t" \
    "v_mfma_f32_16x16x32_bf16 v[172:175], a[36:39], v[104:107], v[172:175]\n\tds_read_b64 a[132:133], v0 offset:3264\n\ts_sub_u32 s56, s51, s56\n\tv_mfma_f32_16x16x32_bf16 v[176:179], a[36:39], v[108:111], v[176:179]\n\tds_read_b64 a[134:135], v0 offset:3272\n\ts_lshl_b32 s56, s56, 6\n\tv_mfma_f32_16x16x32_bf16 v[180:183], a[36:39], v[112:115], v[180:183]\n\tds_read_b128 v[68:71], v1 offset:18816\n\t" \
    "s_lshr_b32 s70, s65, 2\n\tv_mfma_f32_16x16x32_bf16 v[184:187], a[36:39], v[116:119], v[184:187]\n\tds_read_b128 v[72:75], v1 offset:19328\n\ts_mul_i32 s70, s70, 252\n\tv_mfma_f32_16x16x32_bf16 v[188:191], a[36:39], v[120:123], v[188:191]\n\tds_read_b128 v[76:79], v1 offset:19840\n\ts_add_u32 s65, s65, s70\n\tv_mfma_f32_16x16x32_bf16 v[192:195], a[36:39], v[124:127], v[192:195]\n\t" \
    "ds_read_b128 v[80:83], v1 offset:20352\n\ts_mul_i32 s65, s65, 3072\n\tv_mfma_f32_16x16x32_bf16 v[172:175], a[40:43], v[100:103], v[172:175]\n\tds_read_b128 v[84:87], v1 offset:20864\n\ts_add_u32 s56, s56, s65\n\tv_mfma_f32_16x16x32_bf16 v[176:179], a[40:43], v[104:107], v[176:179]\n\tds_read_b128 v[88:91], v1 offset:21376\n\ts_add_u32 s56, s56, 4849664\n\t" \
    "v_mfma_f32_16x16x32_bf16 v[180:183], a[40:43], v[108:111], v[180:183]\n\tds_read_b128 v[92:95], v1 offset:21888\n\ts_add_u32 s51, s51, s38\n\tv_mfma_f32_16x16x32_bf16 v[184:187], a[40:43], v[112:115], v[184:187]\n\tds_read_b128 v[96:99], v1 offset:22400\n\ts_lshl_b32 s51, s51, 3\n\tv_mfma_f32_16x16x32_bf16 v[188:191], a[40:43], v[116:119], v[188:191]\n\tds_read_b64 a[136:137], v0 offset:2752\n\t" \
    "s_add_u32 s51, s51, 50176\n\tds_read_b64_tr_b16 v[204:205], v4 offset:0\n\tv_mfma_f32_16x16x32_bf16 v[192:195], a[40:43], v[120:123], v[192:195]\n\tds_read_b64 a[138:139], v0 offset:2760\n\tds_read_b64_tr_b16 v[206:207], v4 offset:8\n\tv_mfma_f32_16x16x32_bf16 v[176:179], a[44:47], v[100:103], v[176:179]\n\tds_read_b64 a[140:141], v0 offset:2240\n\tds_read_b64_tr_b16 v[208:209], v4 offset:64\n\t" \
    "v_mfma_f32_16x16x32_bf16 v[180:183], a[44:47], v[104:107], v[180:183]\n\tds_read_b64 a[142:143], v0 offset:2248\n\tds_read_b64_tr_b16 v[210:211], v4 offset:72\n\tv_mfma_f32_16x16x32_bf16 v[184:187], a[44:47], v[108:111], v[184:187]\n\tds_read_b64 a[144:145], v0 offset:1728\n\tds_read_b64_tr_b16 v[212:213], v4 offset:512\n\tv_mfma_f32_16x16x32_bf16 v[188:191], a[44:47], v[112:115], v[188:191]\n\tds_read_b64 a[146:147], v0 offset:1736\n\t" \
    "ds_read_b64_tr_b16 v[214:215], v4 offset:520\n\tv_mfma_f32_16x16x32_bf16 v[192:195], a[44:47], v[116:119], v[192:195]\n\tds_read_b64 a[148:149], v0 offset:1216\n\tds_read_b64_tr_b16 v[216:217], v4 offset:576\n\tv_mfma_f32_16x16x32_bf16 v[180:183], a[48:51], v[100:103], v[180:183]\n\tds_read_b64 a[150:151], v0 offset:1224\n\tds_read_b64_tr_b16 v[218:219], v4 offset:584\n\tv_mfma_f32_16x16x32_bf16 v[184:187], a[48:51], v[104:107], v[184:187]\n\t" \
    "ds_read_b64 a[152:153], v0 offset:704\n\tv_mfma_f32_16x16x32_bf16 v[188:191], a[48:51], v[108:111], v[188:191]\n\tds_read_b64 a[154:155], v0 offset:712\n\tv_mfma_f32_16x16x32_bf16 v[192:195], a[48:51], v[112:115], v[192:195]\n\tds_read_b64 a[156:157], v0 offset:192\n\tv_mfma_f32_16x16x32_bf16 v[184:187], a[52:55], v[100:103], v[184:187]\n\tds_read_b64 a[158:159], v0 offset:200\n\tv_mfma_f32_16x16x32_bf16 v[188:191], a[52:55], v[104:107], v[188:191]\n\t" \
    "v_mfma_f32_16x16x32_bf16 v[192:195], a[52:55], v[108:111], v[192:195]\n\tbuffer_load_dword v15, v7, s[16:19], s42 offen\n\ts_waitcnt lgkmcnt(15)\n\tbuffer_store_dwordx4 v[204:207], v5, s[8:11], s56 offen sc1\n\tv_mfma_f32_16x16x32_bf16 v[188:191], a[56:59], v[100:103], v[188:191]\n\ts_add_u32 s43, s43, 1\n\ts_add_u32 s56, s56, 49152\n\tv_mfma_f32_16x16x32_bf16 v[192:195], a[56:59], v[104:107], v[192:195]\n\t" \
    "v_readlane_b32 s42, v237, s43\n\ts_nop 4\n\tv_mfma_f32_16x16x32_bf16 v[192:195], a[60:63], v[100:103], v[192:195]\n\tv_mfma_f32_16x16x32_bf16 v[164:167], a[64:67], v[68:71], v[164:167]\n\tds_read_b64 a[160:161], v0 offset:3712\n\tds_read_b64 a[162:163], v0 offset:3720\n\tv_mfma_f32_16x16x32_bf16 v[168:171], a[64:67], v[72:75], v[168:171]\n\tds_read_b64 a[164:165], v0 offset:3200\n\t" \
    "ds_read_b64 a[166:167], v0 offset:3208\n\tv_mfma_f32_16x16x32_bf16 v[172:175], a[64:67], v[76:79], v[172:175]\n\tds_read_b64 a[168:169], v0 offset:2688\n\tds_read_b64 a[170:171], v0 offset:2696\n\tv_mfma_f32_16x16x32_bf16 v[176:179], a[64:67], v[80:83], v[176:179]\n\tds_read_b64 a[172:173], v0 offset:2176\n\tds_read_b64 a[174:175], v0 offset:2184\n\tv_mfma_f32_16x16x32_bf16 v[180:183], a[64:67], v[84:87], v[180:183]\n\t" \
    "ds_read_b64 a[176:177], v0 offset:1664\n\tds_read_b64 a[178:179], v0 offset:1672\n\tv_mfma_f32_16x16x32_bf16 v[184:187], a[64:67], v[88:91], v[184:187]\n\tds_read_b64 a[180:181], v0 offset:1152\n\tv_mfma_f32_16x16x32_bf16 v[188:191], a[64:67], v[92:95], v[188:191]\n\tds_read_b64 a[182:183], v0 offset:1160\n\tv_mfma_f32_16x16x32_bf16 v[192:195], a[64:67], v[96:99], v[192:195]\n\tds_read_b64 a[184:185], v0 offset:640\n\t" \
    "v_mfma_f32_16x16x32_bf16 v[168:171], a[68:71], v[68:71], v[168:171]\n\tds_read_b64 a[186:187], v0 offset:648\n\tv_mfma_f32_16x16x32_bf16 v[172:175], a[68:71], v[72:75], v[172:175]\n\tds_read_b64 a[188:189], v0 offset:128\n\tv_mfma_f32_16x16x32_bf16 v[176:179], a[68:71], v[76:79], v[176:179]\n\tds_read_b64 a[190:191], v0 offset:136\n\tv_mfma_f32_16x16x32_bf16 v[180:183], a[68:71], v[80:83], v[180:183]\n\tds_read_b128 v[100:103], v1 offset:18752\n\t" \
    "v_mfma_f32_16x16x32_bf16 v[184:187], a[68:71], v[84:87], v[184:187]\n\tds_read_b128 v[104:107], v1 offset:19264\n\tv_mfma_f32_16x16x32_bf16 v[188:191], a[68:71], v[88:91], v[188:191]\n\tds_read_b128 v[108:111], v1 offset:19776\n\tv_mfma_f32_16x16x32_bf16 v[192:195], a[68:71], v[92:95], v[192:195]\n\tds_read_b128 v[112:115], v1 offset:20288\n\tv_mfma_f32_16x16x32_bf16 v[172:175], a[72:75], v[68:71], v[172:175]\n\tds_read_b128 v[116:119], v1 offset:20800\n\t" \
    "s_waitcnt lgkmcnt(15)\n\tbuffer_store_dwordx4 v[208:211], v5, s[8:11], s56 offen sc1\n\tv_mfma_f32_16x16x32_bf16 v[176:179], a[72:75], v[72:75], v[176:179]\n\tds_read_b128 v[120:123], v1 offset:21312\n\ts_add_u32 s56, s56, 49152\n\tv_mfma_f32_16x16x32_bf16 v[180:183], a[72:75], v[76:79], v[180:183]\n\tds_read_b128 v[124:127], v1 offset:21824\n\tv_mfma_f32_16x16x32_bf16 v[184:187], a[72:75], v[80:83], v[184:187]\n\t" \
    "ds_read_b128 v[128:131], v1 offset:22336\n\tv_mfma_f32_16x16x32_bf16 v[188:191], a[72:75], v[84:87], v[188:191]\n\tds_read_b64 a[192:193], v0 offset:3648\n\tv_mfma_f32_16x16x32_bf16 v[192:195], a[72:75], v[88:91], v[192:195]\n\tds_read_b64 a[194:195], v0 offset:3656\n\tv_mfma_f32_16x16x32_bf16 v[176:179], a[76:79], v[68:71], v[176:179]\n\tds_read_b64 a[196:197], v0 offset:3136\n\tv_mfma_f32_16x16x32_bf16 v[180:183], a[76:79], v[72:75], v[180:183]\n\t" \
    "ds_read_b64 a[198:199], v0 offset:3144\n\tv_mfma_f32_16x16x32_bf16 v[184:187], a[76:79], v[76:79], v[184:187]\n\tds_read_b64 a[200:201], v0 offset:2624\n\tv_mfma_f32_16x16x32_bf16 v[188:191], a[76:79], v[80:83], v[188:191]\n\tds_read_b64 a[202:203], v0 offset:2632\n\tv_mfma_f32_16x16x32_bf16 v[192:195], a[76:79], v[84:87], v[192:195]\n\tds_read_b64 a[204:205], v0 offset:2112\n\tv_mfma_f32_16x16x32_bf16 v[180:183], a[80:83], v[68:71], v[180:183]\n\t" \
    "ds_read_b64 a[206:207], v0 offset:2120\n\tv_mfma_f32_16x16x32_bf16 v[184:187], a[80:83], v[72:75], v[184:187]\n\tds_read_b64 a[208:209], v0 offset:1600\n\tv_mfma_f32_16x16x32_bf16 v[188:191], a[80:83], v[76:79], v[188:191]\n\tds_read_b64 a[210:211], v0 offset:1608\n\tv_mfma_f32_16x16x32_bf16 v[192:195], a[80:83], v[80:83], v[192:195]\n\tds_read_b64 a[212:213], v0 offset:1088\n\tv_mfma_f32_16x16x32_bf16 v[184:187], a[84:87], v[68:71], v[184:187]\n\t" \
    "ds_read_b64 a[214:215], v0 offset:1096\n\tv_mfma_f32_16x16x32_bf16 v[188:191], a[84:87], v[72:75], v[188:191]\n\tbuffer_store_dwordx4 v[212:215], v5, s[8:11], s56 offen sc1\n\tv_mfma_f32_16x16x32_bf16 v[192:195], a[84:87], v[76:79], v[192:195]\n\ts_add_u32 s56, s56, 49152\n\tv_mfma_f32_16x16x32_bf16 v[188:191], a[88:91], v[68:71], v[188:191]\n\tv_mfma_f32_16x16x32_bf16 v[192:195], a[88:91], v[72:75], v[192:195]\n\tv_mfma_f32_16x16x32_bf16 v[192:195], a[92:95], v[68:71], v[192:195]\n\t" \
    "s_waitcnt lgkmcnt(12)\n\tv_mfma_f32_16x16x32_bf16 v[164:167], a[96:99], v[100:103], v[164:167]\n\tds_read_b64 a[216:217], v0 offset:576\n\tv_mfma_f32_16x16x32_bf16 v[168:171], a[96:99], v[104:107], v[168:171]\n\tds_read_b64 a[218:219], v0 offset:584\n\tv_mfma_f32_16x16x32_bf16 v[172:175], a[96:99], v[108:111], v[172:175]\n\tds_read_b64 a[220:221], v0 offset:64\n\tv_mfma_f32_16x16x32_bf16 v[176:179], a[96:99], v[112:115], v[176:179]\n\t" \
    "ds_read_b64 a[222:223], v0 offset:72\n\tv_mfma_f32_16x16x32_bf16 v[180:183], a[96:99], v[116:119], v[180:183]\n\tds_read_b64 a[224:225], v0 offset:3584\n\tv_mfma_f32_16x16x32_bf16 v[184:187], a[96:99], v[120:123], v[184:187]\n\tds_read_b64 a[226:227], v0 offset:3592\n\tv_mfma_f32_16x16x32_bf16 v[188:191], a[96:99], v[124:127], v[188:191]\n\tds_read_b64 a[228:229], v0 offset:3072\n\tv_mfma_f32_16x16x32_bf16 v[192:195], a[96:99], v[128:131], v[192:195]\n\t" \
    "ds_read_b64 a[230:231], v0 offset:3080\n\tv_mfma_f32_16x16x32_bf16 v[168:171], a[100:103], v[100:103], v[168:171]\n\tds_read_b64 a[232:233], v0 offset:2560\n\tbuffer_store_dwordx4 v[216:219], v5, s[8:11], s56 offen sc1\n\tv_mfma_f32_16x16x32_bf16 v[172:175], a[100:103], v[104:107], v[172:175]\n\tds_read_b64 a[234:235], v0 offset:2568\n\ts_add_u32 s56, s56, 49152\n\tv_mfma_f32_16x16x32_bf16 v[176:179], a[100:103], v[108:111], v[176:179]\n\t" \
    "ds_read_b64 a[236:237], v0 offset:2048\n\tv_mfma_f32_16x16x32_bf16 v[180:183], a[100:103], v[112:115], v[180:183]\n\tds_read_b64 a[238:239], v0 offset:2056\n\tv_mfma_f32_16x16x32_bf16 v[184:187], a[100:103], v[116:119], v[184:187]\n\tds_read_b64 a[240:241], v0 offset:1536\n\tv_mfma_f32_16x16x32_bf16 v[188:191], a[100:103], v[120:123], v[188:191]\n\tds_read_b64 a[242:243], v0 offset:1544\n\tv_mfma_f32_16x16x32_bf16 v[192:195], a[100:103], v[124:127], v[192:195]\n\t" \
    "ds_read_b64 a[244:245], v0 offset:1024\n\tv_mfma_f32_16x16x32_bf16 v[172:175], a[104:107], v[100:103], v[172:175]\n\tds_read_b64 a[246:247], v0 offset:1032\n\tv_mfma_f32_16x16x32_bf16 v[176:179], a[104:107], v[104:107], v[176:179]\n\tds_read_b128 v[68:71], v1 offset:18688\n\tv_mfma_f32_16x16x32_bf16 v[180:183], a[104:107], v[108:111], v[180:183]\n\tds_read_b128 v[72:75], v1 offset:19200\n\tv_mfma_f32_16x16x32_bf16 v[184:187], a[104:107], v[112:115], v[184:187]\n\t" \
    "ds_read_b128 v[76:79], v1 offset:19712\n\tv_mfma_f32_16x16x32_bf16 v[188:191], a[104:107], v[116:119], v[188:191]\n\tds_read_b128 v[80:83], v1 offset:20224\n\tv_mfma_f32_16x16x32_bf16 v[192:195], a[104:107], v[120:123], v[192:195]\n\ts_lshl_b32 s69, s67, 13\n\tbuffer_load_dword v15, v14, s[4:7], s69 offen\n\tds_read_b128 v[84:87], v1 offset:20736\n\tv_mfma_f32_16x16x32_bf16 v[176:179], a[108:111], v[100:103], v[176:179]\n\t" \
    "ds_read_b128 v[88:91], v1 offset:21248\n\tv_mfma_f32_16x16x32_bf16 v[180:183], a[108:111], v[104:107], v[180:183]\n\tds_read_b128 v[92:95], v1 offset:21760\n\tv_mfma_f32_16x16x32_bf16 v[184:187], a[108:111], v[108:111], v[184:187]\n\tds_read_b128 v[96:99], v1 offset:22272\n\tv_mfma_f32_16x16x32_bf16 v[188:191], a[108:111], v[112:115], v[188:191]\n\tds_read_b64 a[248:249], v0 offset:512\n\tv_mfma_f32_16x16x32_bf16 v[192:195], a[108:111], v[116:119], v[192:195]\n\t" \
    "ds_read_b64 a[250:251], v0 offset:520\n\tv_mfma_f32_16x16x32_bf16 v[180:183], a[112:115], v[100:103], v[180:183]\n\tds_read_b64 a[252:253], v0 offset:0\n\tv_mfma_f32_16x16x32_bf16 v[184:187], a[112:115], v[104:107], v[184:187]\n\tds_read_b64 a[254:255], v0 offset:8\n\tv_mfma_f32_16x16x32_bf16 v[188:191], a[112:115], v[108:111], v[188:191]\n\tv_mfma_f32_16x16x32_bf16 v[192:195], a[112:115], v[112:115], v[192:195]\n\tv_mfma_f32_16x16x32_bf16 v[184:187], a[116:119], v[100:103], v[184:187]\n\t" \
    "v_mfma_f32_16x16x32_bf16 v[188:191], a[116:119], v[104:107], v[188:191]\n\tv_mfma_f32_16x16x32_bf16 v[192:195], a[116:119], v[108:111], v[192:195]\n\tv_mfma_f32_16x16x32_bf16 v[188:191], a[120:123], v[100:103], v[188:191]\n\tv_mfma_f32_16x16x32_bf16 v[192:195], a[120:123], v[104:107], v[192:195]\n\tv_mfma_f32_16x16x32_bf16 v[192:195], a[124:127], v[100:103], v[192:195]\n\ts_waitcnt lgkmcnt(4)\n\tv_mfma_f32_16x16x32_bf16 v[164:167], a[128:131], v[68:71], v[164:167]\n\tds_read_b128 v[100:103], v1 offset:18624\n\t" \
    "s_waitcnt vmcnt(10)\n\tv_alignbit_b32 v196, v19, v19, 16\n\tv_mfma_f32_16x16x32_bf16 v[168:171], a[128:131], v[72:75], v[168:171]\n\tds_read_b128 v[104:107], v1 offset:19136\n\tv_alignbit_b32 v197, v18, v18, 16\n\tv_mfma_f32_16x16x32_bf16 v[172:175], a[128:131], v[76:79], v[172:175]\n\tds_read_b128 v[108:111], v1 offset:19648\n\tv_alignbit_b32 v198, v17, v17, 16\n\t" \
    "v_mfma_f32_16x16x32_bf16 v[176:179], a[128:131], v[80:83], v[176:179]\n\tds_read_b128 v[112:115], v1 offset:20160\n\tv_alignbit_b32 v199, v16, v16, 16\n\tv_mfma_f32_16x16x32_bf16 v[180:183], a[128:131], v[84:87], v[180:183]\n\tds_write_b128 v2, v[196:199] offset:3072\n\tv_mfma_f32_16x16x32_bf16 v[184:187], a[128:131], v[88:91], v[184:187]\n\tds_read_b128 v[116:119], v1 offset:20672\n\tv_bfi_b32 v200, s46, v19, v18\n\t" \
    "v_mfma_f32_16x16x32_bf16 v[188:191], a[128:131], v[92:95], v[188:191]\n\tds_read_b128 v[120:123], v1 offset:21184\n\tv_bfi_b32 v201, s46, v18, v17\n\tv_mfma_f32_16x16x32_bf16 v[192:195], a[128:131], v[96:99], v[192:195]\n\tds_read_b128 v[124:127], v1 offset:21696\n\tv_bfi_b32 v202, s46, v17, v16\n\tv_mfma_f32_16x16x32_bf16 v[168:171], a[132:135], v[68:71], v[168:171]\n\tds_read_b128 v[128:131], v1 offset:22208\n\t" \
    "v_bfi_b32 v203, s46, v16, v33\n\tv_mfma_f32_16x16x32_bf16 v[172:175], a[132:135], v[72:75], v[172:175]\n\tds_write_b128 v2, v[200:203] offset:7232\n\tv_mfma_f32_16x16x32_bf16 v[176:179], a[132:135], v[76:79], v[176:179]\n\tv_alignbit_b32 v196, v18, v18, 16\n\tv_alignbit_b32 v197, v17, v17, 16\n\tv_mfma_f32_16x16x32_bf16 v[180:183], a[132:135], v[80:83], v[180:183]\n\tv_alignbit_b32 v198, v16, v16, 16\n\t" \
    "v_alignbit_b32 v199, v33, v33, 16\n\tv_mfma_f32_16x16x32_bf16 v[184:187], a[132:135], v[84:87], v[184:187]\n\tds_write_b128 v2, v[196:199] offset:11392\n\tv_mfma_f32_16x16x32_bf16 v[188:191], a[132:135], v[88:91], v[188:191]\n\tv_bfi_b32 v200, s46, v18, v17\n\tv_bfi_b32 v201, s46, v17, v16\n\tv_mfma_f32_16x16x32_bf16 v[192:195], a[132:135], v[92:95], v[192:195]\n\tv_bfi_b32 v202, s46, v16, v33\n\t" \
    "v_bfi_b32 v203, s46, v33, v32\n\tv_mfma_f32_16x16x32_bf16 v[172:175], a[136:139], v[68:71], v[172:175]\n\tds_write_b128 v2, v[200:203] offset:15552\n\tv_mfma_f32_16x16x32_bf16 v[176:179], a[136:139], v[72:75], v[176:179]\n\tv_alignbit_b32 v196, v23, v23, 16\n\tv_alignbit_b32 v197, v22, v22, 16\n\tv_mfma_f32_16x16x32_bf16 v[180:183], a[136:139], v[76:79], v[180:183]\n\tv_alignbit_b32 v198, v21, v21, 16\n\t" \
    "v_alignbit_b32 v199, v20, v20, 16\n\tv_mfma_f32_16x16x32_bf16 v[184:187], a[136:139], v[80:83], v[184:187]\n\tds_write_b128 v2, v[196:199] offset:2048\n\tv_mfma_f32_16x16x32_bf16 v[188:191], a[136:139], v[84:87], v[188:191]\n\tv_bfi_b32 v200, s46, v23, v22\n\tv_bfi_b32 v201, s46, v22, v21\n\tv_mfma_f32_16x16x32_bf16 v[192:195], a[136:139], v[88:91], v[192:195]\n\tv_bfi_b32 v202, s46, v21, v20\n\t" \
    "v_bfi_b32 v203, s46, v20, v35\n\tv_mfma_f32_16x16x32_bf16 v[176:179], a[140:143], v[68:71], v[176:179]\n\tds_write_b128 v2, v[200:203] offset:6208\n\tv_mfma_f32_16x16x32_bf16 v[180:183], a[140:143], v[72:75], v[180:183]\n\tv_alignbit_b32 v196, v22, v22, 16\n\tv_alignbit_b32 v197, v21, v21, 16\n\tv_mfma_f32_16x16x32_bf16 v[184:187], a[140:143], v[76:79], v[184:187]\n\tv_alignbit_b32 v198, v20, v20, 16\n\t" \
    "v_alignbit_b32 v199, v35, v35, 16\n\tv_mfma_f32_16x16x32_bf16 v[188:191], a[140:143], v[80:83], v[188:191]\n\tds_write_b128 v2, v[196:199] offset:10368\n\tv_mfma_f32_16x16x32_bf16 v[192:195], a[140:143], v[84:87], v[192:195]\n\tv_bfi_b32 v200, s46, v22, v21\n\tv_bfi_b32 v201, s46, v21, v20\n\tv_mfma_f32_16x16x32_bf16 v[180:183], a[144:147], v[68:71], v[180:183]\n\tv_bfi_b32 v202, s46, v20, v35\n\t" \
    "v_bfi_b32 v203, s46, v35, v34\n\tv_mfma_f32_16x16x32_bf16 v[184:187], a[144:147], v[72:75], v[184:187]\n\tds_write_b128 v2, v[200:203] offset:14528\n\tv_mfma_f32_16x16x32_bf16 v[188:191], a[144:147], v[76:79], v[188:191]\n\tv_alignbit_b32 v196, v27, v27, 16\n\tv_alignbit_b32 v197, v26, v26, 16\n\tv_mfma_f32_16x16x32_bf16 v[192:195], a[144:147], v[80:83], v[192:195]\n\tv_alignbit_b32 v198, v25, v25, 16\n\t" \
    "v_alignbit_b32 v199, v24, v24, 16\n\tv_mfma_f32_16x16x32_bf16 v[184:187], a[148:151], v[68:71], v[184:187]\n\tds_write_b128 v2, v[196:199] offset:1024\n\tv_mfma_f32_16x16x32_bf16 v[188:191], a[148:151], v[72:75], v[188:191]\n\tv_bfi_b32 v200, s46, v27, v26\n\tv_bfi_b32 v201, s46, v26, v25\n\tv_mfma_f32_16x16x32_bf16 v[192:195], a[148:151], v[76:79], v[192:195]\n\tv_bfi_b32 v202, s46, v25, v24\n\t" \
    "v_bfi_b32 v203, s46, v24, v37\n\tv_mfma_f32_16x16x32_bf16 v[188:191], a[152:155], v[68:71], v[188:191]\n\tds_write_b128 v2, v[200:203] offset:5184\n\tv_mfma_f32_16x16x32_bf16 v[192:195], a[152:155], v[72:75], v[192:195]\n\tv_alignbit_b32 v196, v26, v26, 16\n\tv_alignbit_b32 v197, v25, v25, 16\n\tv_mfma_f32_16x16x32_bf16 v[192:195], a[156:159], v[68:71], v[192:195]\n\tv_alignbit_b32 v198, v24, v24, 16\n\t" \
    "v_alignbit_b32 v199, v37, v37, 16\n\ts_waitcnt lgkmcnt(9)\n\tv_mfma_f32_16x16x32_bf16 v[164:167], a[160:163], v[100:103], v[164:167]\n\tds_write_b128 v2, v[196:199] offset:9344\n\tv_mfma_f32_16x16x32_bf16 v[168:171], a[160:163], v[104:107], v[168:171]\n\tds_read_b128 v[68:71], v1 offset:18560\n\tv_bfi_b32 v200, s46, v26, v25\n\tv_mfma_f32_16x16x32_bf16 v[172:175], a[160:163], v[108:111], v[172:175]\n\t" \
    "ds_read_b128 v[72:75], v1 offset:19072\n\tv_bfi_b32 v201, s46, v25, v24\n\tv_mfma_f32_16x16x32_bf16 v[176:179], a[160:163], v[112:115], v[176:179]\n\tds_read_b128 v[76:79], v1 offset:19584\n\tv_bfi_b32 v202, s46, v24, v37\n\tv_mfma_f32_16x16x32_bf16 v[180:183], a[160:163], v[116:119], v[180:183]\n\tds_read_b128 v[80:83], v1 offset:20096\n\tv_bfi_b32 v203, s46, v37, v36\n\t" \
    "v_mfma_f32_16x16x32_bf16 v[184:187], a[160:163], v[120:123], v[184:187]\n\tds_write_b128 v2, v[200:203] offset:13504\n\tv_mfma_f32_16x16x32_bf16 v[188:191], a[160:163], v[124:127], v[188:191]\n\tds_read_b128 v[84:87], v1 offset:20608\n\tv_alignbit_b32 v196, v31, v31, 16\n\tv_mfma_f32_16x16x32_bf16 v[192:195], a[160:163], v[128:131], v[192:195]\n\tbuffer_load_dword v15, v7, s[16:19], s42 offen\n\tds_read_b128 v[88:91], v1 offset:21120\n\t" \
    "v_mfma_f32_16x16x32_bf16 v[168:171], a[164:167], v[100:103], v[168:171]\n\ts_add_u32 s43, s43, 1\n\tds_read_b128 v[92:95], v1 offset:21632\n\tv_alignbit_b32 v197, v30, v30, 16\n\tv_mfma_f32_16x16x32_bf16 v[172:175], a[164:167], v[104:107], v[172:175]\n\tv_readlane_b32 s42, v237, s43\n\ts_nop 4\n\tds_read_b128 v[96:99], v1 offset:22144\n\t" \
    "v_alignbit_b32 v198, v29, v29, 16\n\tv_mfma_f32_16x16x32_bf16 v[176:179], a[164:167], v[108:111], v[176:179]\n\tv_alignbit_b32 v199, v28, v28, 16\n\tv_mfma_f32_16x16x32_bf16 v[180:183], a[164:167], v[112:115], v[180:183]\n\tds_write_b128 v2, v[196:199] offset:0\n\tv_mfma_f32_16x16x32_bf16 v[184:187], a[164:167], v[116:119], v[184:187]\n\tv_bfi_b32 v200, s46, v31, v30\n\tv_bfi_b32 v201, s46, v30, v29\n\t" \
    "v_mfma_f32_16x16x32_bf16 v[188:191], a[164:167], v[120:123], v[188:191]\n\tv_bfi_b32 v202, s46, v29, v28\n\tv_bfi_b32 v203, s46, v28, v39\n\tv_mfma_f32_16x16x32_bf16 v[192:195], a[164:167], v[124:127], v[192:195]\n\tds_write_b128 v2, v[200:203] offset:4160\n\tv_mfma_f32_16x16x32_bf16 v[172:175], a[168:171], v[100:103], v[172:175]\n\tv_alignbit_b32 v196, v30, v30, 16\n\tv_alignbit_b32 v197, v29, v29, 16\n\t" \
    "v_mfma_f32_16x16x32_bf16 v[176:179], a[168:171], v[104:107], v[176:179]\n\tv_alignbit_b32 v198, v28, v28, 16\n\tv_alignbit_b32 v199, v39, v39, 16\n\tv_mfma_f32_16x16x32_bf16 v[180:183], a[168:171], v[108:111], v[180:183]\n\tds_write_b128 v2, v[196:199] offset:8320\n\tv_mfma_f32_16x16x32_bf16 v[184:187], a[168:171], v[112:115], v[184:187]\n\tv_bfi_b32 v200, s46, v30, v29\n\tv_bfi_b32 v201, s46, v29, v28\n\t" \
    "v_mfma_f32_16x16x32_bf16 v[188:191], a[168:171], v[116:119], v[188:191]\n\tv_bfi_b32 v202, s46, v28, v39\n\tv_bfi_b32 v203, s46, v39, v38\n\tv_mfma_f32_16x16x32_bf16 v[192:195], a[168:171], v[120:123], v[192:195]\n\tds_write_b128 v2, v[200:203] offset:12480\n\tv_mfma_f32_16x16x32_bf16 v[176:179], a[172:175], v[100:103], v[176:179]\n\tv_lshl_or_b32 v40, v40, 16, v40\n\tv_and_b32 v228, v40, v232\n\t" \
    "v_mfma_f32_16x16x32_bf16 v[180:183], a[172:175], v[104:107], v[180:183]\n\tv_and_b32 v229, v40, v233\n\tv_and_b32 v230, v40, v234\n\tv_mfma_f32_16x16x32_bf16 v[184:187], a[172:175], v[108:111], v[184:187]\n\tv_and_b32 v231, v40, v235\n\tv_mfma_f32_16x16x32_bf16 v[188:191], a[172:175], v[112:115], v[188:191]\n\tv_mfma_f32_16x16x32_bf16 v[192:195], a[172:175], v[116:119], v[192:195]\n\tv_mfma_f32_16x16x32_bf16 v[180:183], a[176:179], v[100:103], v[180:183]\n\t" \
    "v_mfma_f32_16x16x32_bf16 v[184:187], a[176:179], v[104:107], v[184:187]\n\tv_mfma_f32_16x16x32_bf16 v[188:191], a[176:179], v[108:111], v[188:191]\n\tv_mfma_f32_16x16x32_bf16 v[192:195], a[176:179], v[112:115], v[192:195]\n\tv_mfma_f32_16x16x32_bf16 v[184:187], a[180:183], v[100:103], v[184:187]\n\tv_mfma_f32_16x16x32_bf16 v[188:191], a[180:183], v[104:107], v[188:191]\n\tv_mfma_f32_16x16x32_bf16 v[192:195], a[180:183], v[108:111], v[192:195]\n\tv_mfma_f32_16x16x32_bf16 v[188:191], a[184:187], v[100:103], v[188:191]\n\tv_mfma_f32_16x16x32_bf16 v[192:195], a[184:187], v[104:107], v[192:195]\n\t" \
    "v_mfma_f32_16x16x32_bf16 v[192:195], a[188:191], v[100:103], v[192:195]\n\ts_waitcnt lgkmcnt(4)\n\tv_mfma_f32_16x16x32_bf16 v[164:167], a[192:195], v[68:71], v[164:167]\n\ts_add_u32 s68, s44, s38\n\tds_read_b128 v[100:103], v1 offset:18496\n\tv_mfma_f32_16x16x32_bf16 v[168:171], a[192:195], v[72:75], v[168:171]\n\ts_add_u32 s68, s68, 12\n\tds_read_b128 v[104:107], v1 offset:19008\n\t" \
    "v_mfma_f32_16x16x32_bf16 v[172:175], a[192:195], v[76:79], v[172:175]\n\ts_lshl_b32 s66, s68, 1\n\tds_read_b128 v[108:111], v1 offset:19520\n\tv_mfma_f32_16x16x32_bf16 v[176:179], a[192:195], v[80:83], v[176:179]\n\ts_lshl_b32 s68, s68, 12\n\tds_read_b128 v[112:115], v1 offset:20032\n\tv_mfma_f32_16x16x32_bf16 v[180:183], a[192:195], v[84:87], v[180:183]\n\ts_or_b32 s68, s68, s45\n\t" \
    "ds_read_b128 v[116:119], v1 offset:20544\n\tv_mfma_f32_16x16x32_bf16 v[184:187], a[192:195], v[88:91], v[184:187]\n\ts_or_b32 s66, s66, s45\n\tbuffer_load_dwordx4 v[42:45], v8, s[76:79], s68 offen offset:0\n\tds_read_b128 v[120:123], v1 offset:21056\n\tv_mfma_f32_16x16x32_bf16 v[188:191], a[192:195], v[92:95], v[188:191]\n\tds_read_b128 v[124:127], v1 offset:21568\n\tv_mfma_f32_16x16x32_bf16 v[192:195], a[192:195], v[96:99], v[192:195]\n\t" \
    "ds_read_b128 v[128:131], v1 offset:22080\n\tv_mfma_f32_16x16x32_bf16 v[168:171], a[196:199], v[68:71], v[168:171]\n\tv_mfma_f32_16x16x32_bf16 v[172:175], a[196:199], v[72:75], v[172:175]\n\tv_mfma_f32_16x16x32_bf16 v[176:179], a[196:199], v[76:79], v[176:179]\n\tv_mfma_f32_16x16x32_bf16 v[180:183], a[196:199], v[80:83], v[180:183]\n\tbuffer_load_dwordx4 v[46:49], v8, s[76:79], s68 offen offset:1024\n\tv_mfma_f32_16x16x32_bf16 v[184:187], a[196:199], v[84:87], v[184:187]\n\tv_mfma_f32_16x16x32_bf16 v[188:191], a[196:199], v[88:91], v[188:191]\n\t" \
    "v_mfma_f32_16x16x32_bf16 v[192:195], a[196:199], v[92:95], v[192:195]\n\tv_mfma_f32_16x16x32_bf16 v[172:175], a[200:203], v[68:71], v[172:175]\n\tv_mfma_f32_16x16x32_bf16 v[176:179], a[200:203], v[72:75], v[176:179]\n\tv_mfma_f32_16x16x32_bf16 v[180:183], a[200:203], v[76:79], v[180:183]\n\tbuffer_load_dwordx4 v[50:53], v8, s[76:79], s68 offen offset:2048\n\tv_mfma_f32_16x16x32_bf16 v[184:187], a[200:203], v[80:83], v[184:187]\n\tv_mfma_f32_16x16x32_bf16 v[188:191], a[200:203], v[84:87], v[188:191]\n\tv_mfma_f32_16x16x32_bf16 v[192:195], a[200:203], v[88:91], v[192:195]\n\t" \
    "v_mfma_f32_16x16x32_bf16 v[176:179], a[204:207], v[68:71], v[176:179]\n\tv_mfma_f32_16x16x32_bf16 v[180:183], a[204:207], v[72:75], v[180:183]\n\tv_mfma_f32_16x16x32_bf16 v[184:187], a[204:207], v[76:79], v[184:187]\n\tbuffer_load_dwordx4 v[54:57], v8, s[76:79], s68 offen offset:3072\n\tv_mfma_f32_16x16x32_bf16 v[188:191], a[204:207], v[80:83], v[188:191]\n\tv_mfma_f32_16x16x32_bf16 v[192:195], a[204:207], v[84:87], v[192:195]\n\tv_mfma_f32_16x16x32_bf16 v[180:183], a[208:211], v[68:71], v[180:183]\n\tv_mfma_f32_16x16x32_bf16 v[184:187], a[208:211], v[72:75], v[184:187]\n\t" \
    "v_mfma_f32_16x16x32_bf16 v[188:191], a[208:211], v[76:79], v[188:191]\n\tv_mfma_f32_16x16x32_bf16 v[192:195], a[208:211], v[80:83], v[192:195]\n\tbuffer_load_dwordx2 v[58:59], v13, s[76:79], s68 offen\n\tv_mfma_f32_16x16x32_bf16 v[184:187], a[212:215], v[68:71], v[184:187]\n\tv_mfma_f32_16x16x32_bf16 v[188:191], a[212:215], v[72:75], v[188:191]\n\tv_mfma_f32_16x16x32_bf16 v[192:195], a[212:215], v[76:79], v[192:195]\n\tv_mfma_f32_16x16x32_bf16 v[188:191], a[216:219], v[68:71], v[188:191]\n\tv_mfma_f32_16x16x32_bf16 v[192:195], a[216:219], v[72:75], v[192:195]\n\t" \
    "v_mfma_f32_16x16x32_bf16 v[192:195], a[220:223], v[68:71], v[192:195]\n\tbuffer_load_dwordx2 v[60:61], v8, s[76:79], s68 offen offset:1016\n\ts_waitcnt lgkmcnt(0)\n\tv_mfma_f32_16x16x32_bf16 v[164:167], a[224:227], v[100:103], v[164:167]\n\tds_read_b64 a[0:1], v0 offset:4032\n\tv_mfma_f32_16x16x32_bf16 v[168:171], a[224:227], v[104:107], v[168:171]\n\tds_read_b64 a[2:3], v0 offset:4040\n\tv_mfma_f32_16x16x32_bf16 v[172:175], a[224:227], v[108:111], v[172:175]\n\t" \
    "ds_read_b64 a[4:5], v0 offset:3520\n\tv_mfma_f32_16x16x32_bf16 v[176:179], a[224:227], v[112:115], v[176:179]\n\tds_read_b64 a[6:7], v0 offset:3528\n\tv_mfma_f32_16x16x32_bf16 v[180:183], a[224:227], v[116:119], v[180:183]\n\tds_read_b64 a[8:9], v0 offset:3008\n\tv_mfma_f32_16x16x32_bf16 v[184:187], a[224:227], v[120:123], v[184:187]\n\tbuffer_load_dwordx2 v[62:63], v8, s[76:79], s68 offen offset:2040\n\tds_read_b64 a[10:11], v0 offset:3016\n\t" \
    "v_mfma_f32_16x16x32_bf16 v[188:191], a[224:227], v[124:127], v[188:191]\n\tds_read_b64 a[12:13], v0 offset:2496\n\tv_mfma_f32_16x16x32_bf16 v[192:195], a[224:227], v[128:131], v[192:195]\n\tds_read_b64 a[14:15], v0 offset:2504\n\tv_mfma_f32_16x16x32_bf16 v[168:171], a[228:231], v[100:103], v[168:171]\n\tds_read_b64 a[16:17], v0 offset:1984\n\tv_mfma_f32_16x16x32_bf16 v[172:175], a[228:231], v[104:107], v[172:175]\n\tds_read_b64 a[18:19], v0 offset:1992\n\t" \
    "v_mfma_f32_16x16x32_bf16 v[176:179], a[228:231], v[108:111], v[176:179]\n\tds_read_b64 a[20:21], v0 offset:1472\n\tv_mfma_f32_16x16x32_bf16 v[180:183], a[228:231], v[112:115], v[180:183]\n\tbuffer_load_dwordx2 v[64:65], v8, s[76:79], s68 offen offset:3064\n\tds_read_b64 a[22:23], v0 offset:1480\n\tv_mfma_f32_16x16x32_bf16 v[184:187], a[228:231], v[116:119], v[184:187]\n\tds_read_b64 a[24:25], v0 offset:960\n\tv_mfma_f32_16x16x32_bf16 v[188:191], a[228:231], v[120:123], v[188:191]\n\t" \
    "ds_read_b64 a[26:27], v0 offset:968\n\tv_mfma_f32_16x16x32_bf16 v[192:195], a[228:231], v[124:127], v[192:195]\n\tds_read_b64 a[28:29], v0 offset:448\n\tv_mfma_f32_16x16x32_bf16 v[172:175], a[232:235], v[100:103], v[172:175]\n\tds_read_b64 a[30:31], v0 offset:456\n\tv_mfma_f32_16x16x32_bf16 v[176:179], a[232:235], v[104:107], v[176:179]\n\tds_read_b128 v[68:71], v1 offset:37440\n\tv_mfma_f32_16x16x32_bf16 v[180:183], a[232:235], v[108:111], v[180:183]\n\t" \
    "buffer_load_ushort v66, off, s[80:83], s66\n\tds_read_b128 v[72:75], v1 offset:37952\n\tv_mfma_f32_16x16x32_bf16 v[184:187], a[232:235], v[112:115], v[184:187]\n\tds_read_b128 v[76:79], v1 offset:38464\n\tv_mfma_f32_16x16x32_bf16 v[188:191], a[232:235], v[116:119], v[188:191]\n\tds_read_b128 v[80:83], v1 offset:38976\n\tv_mfma_f32_16x16x32_bf16 v[192:195], a[232:235], v[120:123], v[192:195]\n\tds_read_b128 v[84:87], v1 offset:39488\n\t" \
    "v_mfma_f32_16x16x32_bf16 v[176:179], a[236:239], v[100:103], v[176:179]\n\tds_read_b128 v[88:91], v1 offset:40000\n\tv_mfma_f32_16x16x32_bf16 v[180:183], a[236:239], v[104:107], v[180:183]\n\tds_read_b128 v[92:95], v1 offset:40512\n\tv_mfma_f32_16x16x32_bf16 v[184:187], a[236:239], v[108:111], v[184:187]\n\tbuffer_load_dword v15, v7, s[16:19], s42 offen\n\tds_read_b128 v[96:99], v1 offset:41024\n\tv_mfma_f32_16x16x32_bf16 v[188:191], a[236:239], v[112:115], v[188:191]\n\t" \
    "s_add_u32 s43, s43, 1\n\tv_mfma_f32_16x16x32_bf16 v[192:195], a[236:239], v[116:119], v[192:195]\n\tv_readlane_b32 s42, v237, s43\n\ts_nop 4\n\ts_waitcnt vmcnt(12)\n\tbuffer_store_dwordx2 v[248:249], off, s[8:11], s51 sc1\n\tv_mfma_f32_16x16x32_bf16 v[180:183], a[240:243], v[100:103], v[180:183]\n\tv_mfma_f32_16x16x32_bf16 v[184:187], a[240:243], v[104:107], v[184:187]\n\t" \
    "v_mfma_f32_16x16x32_bf16 v[188:191], a[240:243], v[108:111], v[188:191]\n\tv_mfma_f32_16x16x32_bf16 v[192:195], a[240:243], v[112:115], v[192:195]\n\tv_mfma_f32_16x16x32_bf16 v[184:187], a[244:247], v[100:103], v[184:187]\n\tv_mfma_f32_16x16x32_bf16 v[188:191], a[244:247], v[104:107], v[188:191]\n\tv_mfma_f32_16x16x32_bf16 v[192:195], a[244:247], v[108:111], v[192:195]\n\tv_mfma_f32_16x16x32_bf16 v[188:191], a[248:251], v[100:103], v[188:191]\n\tv_mfma_f32_16x16x32_bf16 v[192:195], a[248:251], v[104:107], v[192:195]\n\tv_mfma_f32_16x16x32_bf16 v[192:195], a[252:255], v[100:103], v[192:195]\n\t" \
    "s_waitcnt lgkmcnt(0)\n\tv_mfma_f32_16x16x32_bf16 v[132:135], v[228:231], v[68:71], 0\n\tds_read_b64 a[32:33], v0 offset:3968\n\tds_read_b64 a[34:35], v0 offset:3976\n\tv_mfma_f32_16x16x32_bf16 v[136:139], v[228:231], v[72:75], 0\n\tds_read_b64 a[36:37], v0 offset:3456\n\tds_read_b64 a[38:39], v0 offset:3464\n\tv_mfma_f32_16x16x32_bf16 v[140:143], v[228:231], v[76:79], 0\n\t" \
    "ds_read_b64 a[40:41], v0 offset:2944\n\tds_read_b64 a[42:43], v0 offset:2952\n\tv_mfma_f32_16x16x32_bf16 v[144:147], v[228:231], v[80:83], 0\n\tds_read_b64 a[44:45], v0 offset:2432\n\tds_read_b64 a[46:47], v0 offset:2440\n\tv_mfma_f32_16x16x32_bf16 v[148:151], v[228:231], v[84:87], 0\n\tds_read_b64 a[48:49], v0 offset:1920\n\tds_read_b64 a[50:51], v0 offset:1928\n\t" \
    "v_mfma_f32_16x16x32_bf16 v[152:155], v[228:231], v[88:91], 0\n\tds_read_b64 a[52:53], v0 offset:1408\n\tv_cvt_pk_bf16_f32 v164, v164, v165\n\tv_mfma_f32_16x16x32_bf16 v[156:159], v[228:231], v[92:95], 0\n\tds_read_b64 a[54:55], v0 offset:1416\n\tv_cvt_pk_bf16_f32 v165, v166, v167\n\tv_mfma_f32_16x16x32_bf16 v[160:163], v[228:231], v[96:99], 0\n\tds_read_b64 a[56:57], v0 offset:896\n\t" \
    "ds_write_b64 v3, v[164:165] offset:18496\n\tv_mfma_f32_16x16x32_bf16 v[132:135], a[0:3], v[68:71], v[132:135]\n\tds_read_b64 a[58:59], v0 offset:904\n\tv_cvt_pk_bf16_f32 v168, v168, v169\n\tv_mfma_f32_16x16x32_bf16 v[136:139], a[0:3], v[72:75], v[136:139]\n\tds_read_b64 a[60:61], v0 offset:384\n\tv_cvt_pk_bf16_f32 v169, v170, v171\n\tv_mfma_f32_16x16x32_bf16 v[140:143], a[0:3], v[76:79], v[140:143]\n\t" \
    "ds_read_b64 a[62:63], v0 offset:392\n\tds_write_b64 v3, v[168:169] offset:19008\n\tv_mfma_f32_16x16x32_bf16 v[144:147], a[0:3], v[80:83], v[144:147]\n\tds_read_b128 v[100:103], v1 offset:37376\n\tv_cvt_pk_bf16_f32 v172, v172, v173\n\tv_mfma_f32_16x16x32_bf16 v[148:151], a[0:3], v[84:87], v[148:151]\n\tds_read_b128 v[104:107], v1 offset:37888\n\tv_cvt_pk_bf16_f32 v173, v174, v175\n\t" \
    "v_mfma_f32_16x16x32_bf16 v[152:155], a[0:3], v[88:91], v[152:155]\n\tds_read_b128 v[108:111], v1 offset:38400\n\tds_write_b64 v3, v[172:173] offset:19520\n\tv_mfma_f32_16x16x32_bf16 v[156:159], a[0:3], v[92:95], v[156:159]\n\tds_read_b128 v[112:115], v1 offset:38912\n\tv_cvt_pk_bf16_f32 v176, v176, v177\n\tv_mfma_f32_16x16x32_bf16 v[160:163], a[0:3], v[96:99], v[160:163]\n\tds_read_b128 v[116:119], v1 offset:39424\n\t" \
    "v_cvt_pk_bf16_f32 v177, v178, v179\n\tv_mfma_f32_16x16x32_bf16 v[136:139], a[4:7], v[68:71], v[136:139]\n\tds_read_b128 v[120:123], v1 offset:39936\n\tds_write_b64 v3, v[176:177] offset:20032\n\tv_mfma_f32_16x16x32_bf16 v[140:143], a[4:7], v[72:75], v[140:143]\n\tbuffer_load_dword v15, v7, s[16:19], s42 offen\n\tds_read_b128 v[124:127], v1 offset:40448\n\tv_mfma_f32_16x16x32_bf16 v[144:147], a[4:7], v[76:79], v[144:147]\n\t" \
    "s_add_u32 s43, s43, 1\n\tds_read_b128 v[128:131], v1 offset:40960\n\tv_cvt_pk_bf16_f32 v180, v180, v181\n\tv_mfma_f32_16x16x32_bf16 v[148:151], a[4:7], v[80:83], v[148:151]\n\tv_readlane_b32 s42, v237, s43\n\ts_nop 4\n\tds_read_b64 a[64:65], v0 offset:3904\n\tv_cvt_pk_bf16_f32 v181, v182, v183\n\t" \
    "v_mfma_f32_16x16x32_bf16 v[152:155], a[4:7], v[84:87], v[152:155]\n\tds_read_b64 a[66:67], v0 offset:3912\n\tds_write_b64 v3, v[180:181] offset:20544\n\tv_mfma_f32_16x16x32_bf16 v[156:159], a[4:7], v[88:91], v[156:159]\n\tds_read_b64 a[68:69], v0 offset:3392\n\tv_cvt_pk_bf16_f32 v184, v184, v185\n\tv_mfma_f32_16x16x32_bf16 v[160:163], a[4:7], v[92:95], v[160:163]\n\tds_read_b64 a[70:71], v0 offset:3400\n\t" \
    "v_cvt_pk_bf16_f32 v185, v186, v187\n\tv_mfma_f32_16x16x32_bf16 v[140:143], a[8:11], v[68:71], v[140:143]\n\tds_read_b64 a[72:73], v0 offset:2880\n\tds_write_b64 v3, v[184:185] offset:21056\n\tv_mfma_f32_16x16x32_bf16 v[144:147], a[8:11], v[72:75], v[144:147]\n\tds_read_b64 a[74:75], v0 offset:2888\n\tv_cvt_pk_bf16_f32 v188, v188, v189\n\tv_mfma_f32_16x16x32_bf16 v[148:151], a[8:11], v[76:79], v[148:151]\n\t" \
    "ds_read_b64 a[76:77], v0 offset:2368\n\tv_cvt_pk_bf16_f32 v189, v190, v191\n\tv_mfma_f32_16x16x32_bf16 v[152:155], a[8:11], v[80:83], v[152:155]\n\tds_read_b64 a[78:79], v0 offset:2376\n\tds_write_b64 v3, v[188:189] offset:21568\n\tv_mfma_f32_16x16x32_bf16 v[156:159], a[8:11], v[84:87], v[156:159]\n\tds_read_b64 a[80:81], v0 offset:1856\n\tv_cvt_pk_bf16_f32 v192, v192, v193\n\t" \
    "v_mfma_f32_16x16x32_bf16 v[160:163], a[8:11], v[88:91], v[160:163]\n\tds_read_b64 a[82:83], v0 offset:1864\n\tv_cvt_pk_bf16_f32 v193, v194, v195\n\tv_mfma_f32_16x16x32_bf16 v[144:147], a[12:15], v[68:71], v[144:147]\n\tds_read_b64 a[84:85], v0 offset:1344\n\tds_write_b64 v3, v[192:193] offset:22080\n\tv_mfma_f32_16x16x32_bf16 v[148:151], a[12:15], v[72:75], v[148:151]\n\tds_read_b64 a[86:87], v0 offset:1352\n\t" \
    "v_mfma_f32_16x16x32_bf16 v[152:155], a[12:15], v[76:79], v[152:155]\n\tds_read_b64 a[88:89], v0 offset:832\n\tv_mfma_f32_16x16x32_bf16 v[156:159], a[12:15], v[80:83], v[156:159]\n\tds_read_b64 a[90:91], v0 offset:840\n\tv_mfma_f32_16x16x32_bf16 v[160:163], a[12:15], v[84:87], v[160:163]\n\tds_read_b64 a[92:93], v0 offset:320\n\tv_mfma_f32_16x16x32_bf16 v[148:151], a[16:19], v[68:71], v[148:151]\n\tds_read_b64 a[94:95], v0 offset:328\n\t" \
    "v_mfma_f32_16x16x32_bf16 v[152:155], a[16:19], v[72:75], v[152:155]\n\tds_read_b64 a[96:97], v0 offset:3840\n\tv_mfma_f32_16x16x32_bf16 v[156:159], a[16:19], v[76:79], v[156:159]\n\tds_read_b64 a[98:99], v0 offset:3848\n\tv_mfma_f32_16x16x32_bf16 v[160:163], a[16:19], v[80:83], v[160:163]\n\tds_read_b64 a[100:101], v0 offset:3328\n\tv_mfma_f32_16x16x32_bf16 v[152:155], a[20:23], v[68:71], v[152:155]\n\tds_read_b64 a[102:103], v0 offset:3336\n\t" \
    "v_mfma_f32_16x16x32_bf16 v[156:159], a[20:23], v[72:75], v[156:159]\n\tv_mfma_f32_16x16x32_bf16 v[160:163], a[20:23], v[76:79], v[160:163]\n\tv_mfma_f32_16x16x32_bf16 v[156:159], a[24:27], v[68:71], v[156:159]\n\tv_mfma_f32_16x16x32_bf16 v[160:163], a[24:27], v[72:75], v[160:163]\n\tv_mfma_f32_16x16x32_bf16 v[160:163], a[28:31], v[68:71], v[160:163]\n\ts_waitcnt lgkmcnt(9)\n\ts_barrier\n\tv_mfma_f32_16x16x32_bf16 v[132:135], a[32:35], v[100:103], v[132:135]\n\t" \
    "ds_read_b64 a[104:105], v0 offset:2816\n\tds_read_b64 a[106:107], v0 offset:2824\n\tv_mfma_f32_16x16x32_bf16 v[136:139], a[32:35], v[104:107], v[136:139]\n\tds_read_b64 a[108:109], v0 offset:2304\n\tds_read_b64 a[110:111], v0 offset:2312\n\tv_mfma_f32_16x16x32_bf16 v[140:143], a[32:35], v[108:111], v[140:143]\n\tds_read_b64 a[112:113], v0 offset:1792\n\tds_read_b64 a[114:115], v0 offset:1800\n\t" \
    "v_mfma_f32_16x16x32_bf16 v[144:147], a[32:35], v[112:115], v[144:147]\n\tds_read_b64 a[116:117], v0 offset:1280\n\tds_read_b64 a[118:119], v0 offset:1288\n\tv_mfma_f32_16x16x32_bf16 v[148:151], a[32:35], v[116:119], v[148:151]\n\tds_read_b64 a[120:121], v0 offset:768\n\tds_read_b64 a[122:123], v0 offset:776\n\tv_mfma_f32_16x16x32_bf16 v[152:155], a[32:35], v[120:123], v[152:155]\n\tds_read_b64 a[124:125], v0 offset:256\n\t" \
    "s_add_u32 s51, s44, 4\n\tv_mfma_f32_16x16x32_bf16 v[156:159], a[32:35], v[124:127], v[156:159]\n\tds_read_b64 a[126:127], v0 offset:264\n\ts_mul_i32 s65, s51, 1366\n\tv_mfma_f32_16x16x32_bf16 v[160:163], a[32:35], v[128:131], v[160:163]\n\tds_read_b64 a[128:129], v0 offset:3776\n\ts_lshr_b32 s65, s65, 16\n\tv_mfma_f32_16x16x32_bf16 v[136:139], a[36:39], v[100:103], v[136:139]\n\t" \
    "ds_read_b64 a[130:131], v0 offset:3784\n\ts_mul_i32 s56, s65, 48\n\tv_mfma_f32_16x16x32_bf16 v[140:143], a[36:39], v[104:107], v[140:143]\n\tds_read_b64 a[132:133], v0 offset:3264\n\ts_sub_u32 s56, s51, s56\n\tv_mfma_f32_16x16x32_bf16 v[144:147], a[36:39], v[108:111], v[144:147]\n\tds_read_b64 a[134:135], v0 offset:3272\n\ts_lshl_b32 s56, s56, 6\n\t" \
    "v_mfma_f32_16x16x32_bf16 v[148:151], a[36:39], v[112:115], v[148:151]\n\tds_read_b128 v[68:71], v1 offset:37312\n\ts_lshr_b32 s70, s65, 2\n\tv_mfma_f32_16x16x32_bf16 v[152:155], a[36:39], v[116:119], v[152:155]\n\tds_read_b128 v[72:75], v1 offset:37824\n\ts_mul_i32 s70, s70, 252\n\tv_mfma_f32_16x16x32_bf16 v[156:159], a[36:39], v[120:123], v[156:159]\n\tds_read_b128 v[76:79], v1 offset:38336\n\t" \
    "s_add_u32 s65, s65, s70\n\tv_mfma_f32_16x16x32_bf16 v[160:163], a[36:39], v[124:127], v[160:163]\n\tds_read_b128 v[80:83], v1 offset:38848\n\ts_mul_i32 s65, s65, 3072\n\tv_mfma_f32_16x16x32_bf16 v[140:143], a[40:43], v[100:103], v[140:143]\n\tds_read_b128 v[84:87], v1 offset:39360\n\ts_add_u32 s56, s56, s65\n\tv_mfma_f32_16x16x32_bf16 v[144:147], a[40:43], v[104:107], v[144:147]\n\t" \
    "ds_read_b128 v[88:91], v1 offset:39872\n\ts_add_u32 s56, s56, 4849664\n\tv_mfma_f32_16x16x32_bf16 v[148:151], a[40:43], v[108:111], v[148:151]\n\tds_read_b128 v[92:95], v1 offset:40384\n\ts_add_u32 s51, s51, s38\n\tv_mfma_f32_16x16x32_bf16 v[152:155], a[40:43], v[112:115], v[152:155]\n\tds_read_b128 v[96:99], v1 offset:40896\n\ts_lshl_b32 s51, s51, 3\n\t" \
    "v_mfma_f32_16x16x32_bf16 v[156:159], a[40:43], v[116:119], v[156:159]\n\tds_read_b64 a[136:137], v0 offset:2752\n\ts_add_u32 s51, s51, 50176\n\tds_read_b64_tr_b16 v[204:205], v4 offset:18496\n\tv_mfma_f32_16x16x32_bf16 v[160:163], a[40:43], v[120:123], v[160:163]\n\tds_read_b64 a[138:139], v0 offset:2760\n\tds_read_b64_tr_b16 v[206:207], v4 offset:18504\n\tv_mfma_f32_16x16x32_bf16 v[144:147], a[44:47], v[100:103], v[144:147]\n\t" \
    "ds_read_b64 a[140:141], v0 offset:2240\n\tds_read_b64_tr_b16 v[208:209], v4 offset:18560\n\tv_mfma_f32_16x16x32_bf16 v[148:151], a[44:47], v[104:107], v[148:151]\n\tds_read_b64 a[142:143], v0 offset:2248\n\tds_read_b64_tr_b16 v[210:211], v4 offset:18568\n\tv_mfma_f32_16x16x32_bf16 v[152:155], a[44:47], v[108:111], v[152:155]\n\tds_read_b64 a[144:145], v0 offset:1728\n\tds_read_b64_tr_b16 v[212:213], v4 offset:19008\n\t" \
    "v_mfma_f32_16x16x32_bf16 v[156:159], a[44:47], v[112:115], v[156:159]\n\tds_read_b64 a[146:147], v0 offset:1736\n\tds_read_b64_tr_b16 v[214:215], v4 offset:19016\n\tv_mfma_f32_16x16x32_bf16 v[160:163], a[44:47], v[116:119], v[160:163]\n\tds_read_b64 a[148:149], v0 offset:1216\n\tds_read_b64_tr_b16 v[216:217], v4 offset:19072\n\tv_mfma_f32_16x16x32_bf16 v[148:151], a[48:51], v[100:103], v[148:151]\n\tds_read_b64 a[150:151], v0 offset:1224\n\t" \
    "ds_read_b64_tr_b16 v[218:219], v4 offset:19080\n\tv_mfma_f32_16x16x32_bf16 v[152:155], a[48:51], v[104:107], v[152:155]\n\tds_read_b64 a[152:153], v0 offset:704\n\tv_mfma_f32_16x16x32_bf16 v[156:159], a[48:51], v[108:111], v[156:159]\n\tds_read_b64 a[154:155], v0 offset:712\n\tv_mfma_f32_16x16x32_bf16 v[160:163], a[48:51], v[112:115], v[160:163]\n\tds_read_b64 a[156:157], v0 offset:192\n\tv_mfma_f32_16x16x32_bf16 v[152:155], a[52:55], v[100:103], v[152:155]\n\t" \
    "ds_read_b64 a[158:159], v0 offset:200\n\tv_mfma_f32_16x16x32_bf16 v[156:159], a[52:55], v[104:107], v[156:159]\n\tv_mfma_f32_16x16x32_bf16 v[160:163], a[52:55], v[108:111], v[160:163]\n\tbuffer_load_dword v15, v7, s[16:19], s42 offen\n\ts_waitcnt lgkmcnt(15)\n\tbuffer_store_dwordx4 v[204:207], v5, s[8:11], s56 offen sc1\n\tv_mfma_f32_16x16x32_bf16 v[156:159], a[56:59], v[100:103], v[156:159]\n\ts_add_u32 s43, s43, 1\n\t" \
    "s_add_u32 s56, s56, 49152\n\tv_mfma_f32_16x16x32_bf16 v[160:163], a[56:59], v[104:107], v[160:163]\n\tv_readlane_b32 s42, v237, s43\n\ts_nop 4\n\tv_mfma_f32_16x16x32_bf16 v[160:163], a[60:63], v[100:103], v[160:163]\n\tv_mfma_f32_16x16x32_bf16 v[132:135], a[64:67], v[68:71], v[132:135]\n\tds_read_b64 a[160:161], v0 offset:3712\n\tds_read_b64 a[162:163], v0 offset:3720\n\t" \
    "v_mfma_f32_16x16x32_bf16 v[136:139], a[64:67], v[72:75], v[136:139]\n\tds_read_b64 a[164:165], v0 offset:3200\n\tds_read_b64 a[166:167], v0 offset:3208\n\tv_mfma_f32_16x16x32_bf16 v[140:143], a[64:67], v[76:79], v[140:143]\n\tds_read_b64 a[168:169], v0 offset:2688\n\tds_read_b64 a[170:171], v0 offset:2696\n\tv_mfma_f32_16x16x32_bf16 v[144:147], a[64:67], v[80:83], v[144:147]\n\tds_read_b64 a[172:173], v0 offset:2176\n\t" \
    "ds_read_b64 a[174:175], v0 offset:2184\n\tv_mfma_f32_16x16x32_bf16 v[148:151], a[64:67], v[84:87], v[148:151]\n\tds_read_b64 a[176:177], v0 offset:1664\n\tds_read_b64 a[178:179], v0 offset:1672\n\tv_mfma_f32_16x16x32_bf16 v[152:155], a[64:67], v[88:91], v[152:155]\n\tds_read_b64 a[180:181], v0 offset:1152\n\tv_mfma_f32_16x16x32_bf16 v[156:159], a[64:67], v[92:95], v[156:159]\n\tds_read_b64 a[182:183], v0 offset:1160\n\t" \
    "v_mfma_f32_16x16x32_bf16 v[160:163], a[64:67], v[96:99], v[160:163]\n\tds_read_b64 a[184:185], v0 offset:640\n\tv_mfma_f32_16x16x32_bf16 v[136:139], a[68:71], v[68:71], v[136:139]\n\tds_read_b64 a[186:187], v0 offset:648\n\tv_mfma_f32_16x16x32_bf16 v[140:143], a[68:71], v[72:75], v[140:143]\n\tds_read_b64 a[188:189], v0 offset:128\n\tv_mfma_f32_16x16x32_bf16 v[144:147], a[68:71], v[76:79], v[144:147]\n\tds_read_b64 a[190:191], v0 offset:136\n\t" \
    "v_mfma_f32_16x16x32_bf16 v[148:151], a[68:71], v[80:83], v[148:151]\n\tds_read_b128 v[100:103], v1 offset:37248\n\tv_mfma_f32_16x16x32_bf16 v[152:155], a[68:71], v[84:87], v[152:155]\n\tds_read_b128 v[104:107], v1 offset:37760\n\tv_mfma_f32_16x16x32_bf16 v[156:159], a[68:71], v[88:91], v[156:159]\n\tds_read_b128 v[108:111], v1 offset:38272\n\tv_mfma_f32_16x16x32_bf16 v[160:163], a[68:71], v[92:95], v[160:163]\n\tds_read_b128 v[112:115], v1 offset:38784\n\t" \
    "v_mfma_f32_16x16x32_bf16 v[140:143], a[72:75], v[68:71], v[140:143]\n\tds_read_b128 v[116:119], v1 offset:39296\n\ts_waitcnt lgkmcnt(15)\n\tbuffer_store_dwordx4 v[208:211], v5, s[8:11], s56 offen sc1\n\tv_mfma_f32_16x16x32_bf16 v[144:147], a[72:75], v[72:75], v[144:147]\n\tds_read_b128 v[120:123], v1 offset:39808\n\ts_add_u32 s56, s56, 49152\n\tv_mfma_f32_16x16x32_bf16 v[148:151], a[72:75], v[76:79], v[148:151]\n\t" \
    "ds_read_b128 v[124:127], v1 offset:40320\n\tv_mfma_f32_16x16x32_bf16 v[152:155], a[72:75], v[80:83], v[152:155]\n\tds_read_b128 v[128:131], v1 offset:40832\n\tv_mfma_f32_16x16x32_bf16 v[156:159], a[72:75], v[84:87], v[156:159]\n\tds_read_b64 a[192:193], v0 offset:3648\n\tv_mfma_f32_16x16x32_bf16 v[160:163], a[72:75], v[88:91], v[160:163]\n\tds_read_b64 a[194:195], v0 offset:3656\n\tv_mfma_f32_16x16x32_bf16 v[144:147], a[76:79], v[68:71], v[144:147]\n\t" \
    "ds_read_b64 a[196:197], v0 offset:3136\n\tv_mfma_f32_16x16x32_bf16 v[148:151], a[76:79], v[72:75], v[148:151]\n\tds_read_b64 a[198:199], v0 offset:3144\n\tv_mfma_f32_16x16x32_bf16 v[152:155], a[76:79], v[76:79], v[152:155]\n\tds_read_b64 a[200:201], v0 offset:2624\n\tv_mfma_f32_16x16x32_bf16 v[156:159], a[76:79], v[80:83], v[156:159]\n\tds_read_b64 a[202:203], v0 offset:2632\n\tv_mfma_f32_16x16x32_bf16 v[160:163], a[76:79], v[84:87], v[160:163]\n\t" \
    "ds_read_b64 a[204:205], v0 offset:2112\n\tv_mfma_f32_16x16x32_bf16 v[148:151], a[80:83], v[68:71], v[148:151]\n\tds_read_b64 a[206:207], v0 offset:2120\n\tv_mfma_f32_16x16x32_bf16 v[152:155], a[80:83], v[72:75], v[152:155]\n\tds_read_b64 a[208:209], v0 offset:1600\n\tv_mfma_f32_16x16x32_bf16 v[156:159], a[80:83], v[76:79], v[156:159]\n\tds_read_b64 a[210:211], v0 offset:1608\n\tv_mfma_f32_16x16x32_bf16 v[160:163], a[80:83], v[80:83], v[160:163]\n\t" \
    "ds_read_b64 a[212:213], v0 offset:1088\n\tv_mfma_f32_16x16x32_bf16 v[152:155], a[84:87], v[68:71], v[152:155]\n\tds_read_b64 a[214:215], v0 offset:1096\n\tv_mfma_f32_16x16x32_bf16 v[156:159], a[84:87], v[72:75], v[156:159]\n\tbuffer_store_dwordx4 v[212:215], v5, s[8:11], s56 offen sc1\n\tv_mfma_f32_16x16x32_bf16 v[160:163], a[84:87], v[76:79], v[160:163]\n\ts_add_u32 s56, s56, 49152\n\tv_mfma_f32_16x16x32_bf16 v[156:159], a[88:91], v[68:71], v[156:159]\n\t" \
    "v_mfma_f32_16x16x32_bf16 v[160:163], a[88:91], v[72:75], v[160:163]\n\tv_mfma_f32_16x16x32_bf16 v[160:163], a[92:95], v[68:71], v[160:163]\n\ts_waitcnt lgkmcnt(12)\n\tv_mfma_f32_16x16x32_bf16 v[132:135], a[96:99], v[100:103], v[132:135]\n\tds_read_b64 a[216:217], v0 offset:576\n\tv_mfma_f32_16x16x32_bf16 v[136:139], a[96:99], v[104:107], v[136:139]\n\tds_read_b64 a[218:219], v0 offset:584\n\tv_mfma_f32_16x16x32_bf16 v[140:143], a[96:99], v[108:111], v[140:143]\n\t" \
    "ds_read_b64 a[220:221], v0 offset:64\n\tv_mfma_f32_16x16x32_bf16 v[144:147], a[96:99], v[112:115], v[144:147]\n\tds_read_b64 a[222:223], v0 offset:72\n\tv_mfma_f32_16x16x32_bf16 v[148:151], a[96:99], v[116:119], v[148:151]\n\tds_read_b64 a[224:225], v0 offset:3584\n\tv_mfma_f32_16x16x32_bf16 v[152:155], a[96:99], v[120:123], v[152:155]\n\tds_read_b64 a[226:227], v0 offset:3592\n\tv_mfma_f32_16x16x32_bf16 v[156:159], a[96:99], v[124:127], v[156:159]\n\t" \
    "ds_read_b64 a[228:229], v0 offset:3072\n\tv_mfma_f32_16x16x32_bf16 v[160:163], a[96:99], v[128:131], v[160:163]\n\tds_read_b64 a[230:231], v0 offset:3080\n\tv_mfma_f32_16x16x32_bf16 v[136:139], a[100:103], v[100:103], v[136:139]\n\tds_read_b64 a[232:233], v0 offset:2560\n\tbuffer_store_dwordx4 v[216:219], v5, s[8:11], s56 offen sc1\n\tv_mfma_f32_16x16x32_bf16 v[140:143], a[100:103], v[104:107], v[140:143]\n\tds_read_b64 a[234:235], v0 offset:2568\n\t" \
    "s_add_u32 s56, s56, 49152\n\tv_mfma_f32_16x16x32_bf16 v[144:147], a[100:103], v[108:111], v[144:147]\n\tds_read_b64 a[236:237], v0 offset:2048\n\tv_mfma_f32_16x16x32_bf16 v[148:151], a[100:103], v[112:115], v[148:151]\n\tds_read_b64 a[238:239], v0 offset:2056\n\tv_mfma_f32_16x16x32_bf16 v[152:155], a[100:103], v[116:119], v[152:155]\n\tds_read_b64 a[240:241], v0 offset:1536\n\tv_mfma_f32_16x16x32_bf16 v[156:159], a[100:103], v[120:123], v[156:159]\n\t" \
    "ds_read_b64 a[242:243], v0 offset:1544\n\tv_mfma_f32_16x16x32_bf16 v[160:163], a[100:103], v[124:127], v[160:163]\n\tds_read_b64 a[244:245], v0 offset:1024\n\tv_mfma_f32_16x16x32_bf16 v[140:143], a[104:107], v[100:103], v[140:143]\n\tds_read_b64 a[246:247], v0 offset:1032\n\tv_mfma_f32_16x16x32_bf16 v[144:147], a[104:107], v[104:107], v[144:147]\n\tds_read_b128 v[68:71], v1 offset:37184\n\tv_mfma_f32_16x16x32_bf16 v[148:151], a[104:107], v[108:111], v[148:151]\n\t" \
    "ds_read_b128 v[72:75], v1 offset:37696\n\tv_mfma_f32_16x16x32_bf16 v[152:155], a[104:107], v[112:115], v[152:155]\n\tds_read_b128 v[76:79], v1 offset:38208\n\tv_mfma_f32_16x16x32_bf16 v[156:159], a[104:107], v[116:119], v[156:159]\n\tds_read_b128 v[80:83], v1 offset:38720\n\tv_mfma_f32_16x16x32_bf16 v[160:163], a[104:107], v[120:123], v[160:163]\n\tbuffer_load_dword v15, v7, s[16:19], s42 offen\n\tds_read_b128 v[84:87], v1 offset:39232\n\t" \
    "v_mfma_f32_16x16x32_bf16 v[144:147], a[108:111], v[100:103], v[144:147]\n\ts_add_u32 s43, s43, 1\n\tds_read_b128 v[88:91], v1 offset:39744\n\tv_mfma_f32_16x16x32_bf16 v[148:151], a[108:111], v[104:107], v[148:151]\n\tv_readlane_b32 s42, v237, s43\n\ts_nop 4\n\tds_read_b128 v[92:95], v1 offset:40256\n\tv_mfma_f32_16x16x32_bf16 v[152:155], a[108:111], v[108:111], v[152:155]\n\t" \
    "ds_read_b128 v[96:99], v1 offset:40768\n\tv_mfma_f32_16x16x32_bf16 v[156:159], a[108:111], v[112:115], v[156:159]\n\tds_read_b64 a[248:249], v0 offset:512\n\tv_mfma_f32_16x16x32_bf16 v[160:163], a[108:111], v[116:119], v[160:163]\n\tds_read_b64 a[250:251], v0 offset:520\n\tv_mfma_f32_16x16x32_bf16 v[148:151], a[112:115], v[100:103], v[148:151]\n\tds_read_b64 a[252:253], v0 offset:0\n\tv_mfma_f32_16x16x32_bf16 v[152:155], a[112:115], v[104:107], v[152:155]\n\t" \
    "ds_read_b64 a[254:255], v0 offset:8\n\tv_mfma_f32_16x16x32_bf16 v[156:159], a[112:115], v[108:111], v[156:159]\n\tv_mfma_f32_16x16x32_bf16 v[160:163], a[112:115], v[112:115], v[160:163]\n\tv_mfma_f32_16x16x32_bf16 v[152:155], a[116:119], v[100:103], v[152:155]\n\tv_mfma_f32_16x16x32_bf16 v[156:159], a[116:119], v[104:107], v[156:159]\n\tv_mfma_f32_16x16x32_bf16 v[160:163], a[116:119], v[108:111], v[160:163]\n\tv_mfma_f32_16x16x32_bf16 v[156:159], a[120:123], v[100:103], v[156:159]\n\tv_mfma_f32_16x16x32_bf16 v[160:163], a[120:123], v[104:107], v[160:163]\n\t" \
    "v_mfma_f32_16x16x32_bf16 v[160:163], a[124:127], v[100:103], v[160:163]\n\ts_waitcnt lgkmcnt(4)\n\tv_mfma_f32_16x16x32_bf16 v[132:135], a[128:131], v[68:71], v[132:135]\n\tds_read_b128 v[100:103], v1 offset:37120\n\ts_waitcnt vmcnt(9)\n\tv_alignbit_b32 v196, v45, v45, 16\n\tv_mfma_f32_16x16x32_bf16 v[136:139], a[128:131], v[72:75], v[136:139]\n\tds_read_b128 v[104:107], v1 offset:37632\n\t" \
    "v_alignbit_b32 v197, v44, v44, 16\n\tv_mfma_f32_16x16x32_bf16 v[140:143], a[128:131], v[76:79], v[140:143]\n\tds_read_b128 v[108:111], v1 offset:38144\n\tv_alignbit_b32 v198, v43, v43, 16\n\tv_mfma_f32_16x16x32_bf16 v[144:147], a[128:131], v[80:83], v[144:147]\n\tds_read_b128 v[112:115], v1 offset:38656\n\tv_alignbit_b32 v199, v42, v42, 16\n\tv_mfma_f32_16x16x32_bf16 v[148:151], a[128:131], v[84:87], v[148:151]\n\t" \
    "ds_write_b128 v2, v[196:199] offset:3072\n\tv_mfma_f32_16x16x32_bf16 v[152:155], a[128:131], v[88:91], v[152:155]\n\tds_read_b128 v[116:119], v1 offset:39168\n\tv_bfi_b32 v200, s46, v45, v44\n\tv_mfma_f32_16x16x32_bf16 v[156:159], a[128:131], v[92:95], v[156:159]\n\tds_read_b128 v[120:123], v1 offset:39680\n\tv_bfi_b32 v201, s46, v44, v43\n\tv_mfma_f32_16x16x32_bf16 v[160:163], a[128:131], v[96:99], v[160:163]\n\t" \
    "ds_read_b128 v[124:127], v1 offset:40192\n\tv_bfi_b32 v202, s46, v43, v42\n\tv_mfma_f32_16x16x32_bf16 v[136:139], a[132:135], v[68:71], v[136:139]\n\tds_read_b128 v[128:131], v1 offset:40704\n\tv_bfi_b32 v203, s46, v42, v59\n\tv_mfma_f32_16x16x32_bf16 v[140:143], a[132:135], v[72:75], v[140:143]\n\tds_write_b128 v2, v[200:203] offset:7232\n\tv_mfma_f32_16x16x32_bf16 v[144:147], a[132:135], v[76:79], v[144:147]\n\t" \
    "v_alignbit_b32 v196, v44, v44, 16\n\tv_alignbit_b32 v197, v43, v43, 16\n\tv_mfma_f32_16x16x32_bf16 v[148:151], a[132:135], v[80:83], v[148:151]\n\tv_alignbit_b32 v198, v42, v42, 16\n\tv_alignbit_b32 v199, v59, v59, 16\n\tv_mfma_f32_16x16x32_bf16 v[152:155], a[132:135], v[84:87], v[152:155]\n\tds_write_b128 v2, v[196:199] offset:11392\n\tv_mfma_f32_16x16x32_bf16 v[156:159], a[132:135], v[88:91], v[156:159]\n\t" \
    "v_bfi_b32 v200, s46, v44, v43\n\tv_bfi_b32 v201, s46, v43, v42\n\tv_mfma_f32_16x16x32_bf16 v[160:163], a[132:135], v[92:95], v[160:163]\n\tv_bfi_b32 v202, s46, v42, v59\n\tv_bfi_b32 v203, s46, v59, v58\n\tv_mfma_f32_16x16x32_bf16 v[140:143], a[136:139], v[68:71], v[140:143]\n\tds_write_b128 v2, v[200:203] offset:15552\n\tv_mfma_f32_16x16x32_bf16 v[144:147], a[136:139], v[72:75], v[144:147]\n\t" \
    "v_alignbit_b32 v196, v49, v49, 16\n\tv_alignbit_b32 v197, v48, v48, 16\n\tv_mfma_f32_16x16x32_bf16 v[148:151], a[136:139], v[76:79], v[148:151]\n\tv_alignbit_b32 v198, v47, v47, 16\n\tv_alignbit_b32 v199, v46, v46, 16\n\tv_mfma_f32_16x16x32_bf16 v[152:155], a[136:139], v[80:83], v[152:155]\n\tds_write_b128 v2, v[196:199] offset:2048\n\tv_mfma_f32_16x16x32_bf16 v[156:159], a[136:139], v[84:87], v[156:159]\n\t" \
    "v_bfi_b32 v200, s46, v49, v48\n\tv_bfi_b32 v201, s46, v48, v47\n\tv_mfma_f32_16x16x32_bf16 v[160:163], a[136:139], v[88:91], v[160:163]\n\tv_bfi_b32 v202, s46, v47, v46\n\tv_bfi_b32 v203, s46, v46, v61\n\tv_mfma_f32_16x16x32_bf16 v[144:147], a[140:143], v[68:71], v[144:147]\n\tds_write_b128 v2, v[200:203] offset:6208\n\tv_mfma_f32_16x16x32_bf16 v[148:151], a[140:143], v[72:75], v[148:151]\n\t" \
    "v_alignbit_b32 v196, v48, v48, 16\n\tv_alignbit_b32 v197, v47, v47, 16\n\tv_mfma_f32_16x16x32_bf16 v[152:155], a[140:143], v[76:79], v[152:155]\n\tv_alignbit_b32 v198, v46, v46, 16\n\tv_alignbit_b32 v199, v61, v61, 16\n\tv_mfma_f32_16x16x32_bf16 v[156:159], a[140:143], v[80:83], v[156:159]\n\tds_write_b128 v2, v[196:199] offset:10368\n\tv_mfma_f32_16x16x32_bf16 v[160:163], a[140:143], v[84:87], v[160:163]\n\t" \
    "v_bfi_b32 v200, s46, v48, v47\n\tv_bfi_b32 v201, s46, v47, v46\n\tv_mfma_f32_16x16x32_bf16 v[148:151], a[144:147], v[68:71], v[148:151]\n\tv_bfi_b32 v202, s46, v46, v61\n\tv_bfi_b32 v203, s46, v61, v60\n\tv_mfma_f32_16x16x32_bf16 v[152:155], a[144:147], v[72:75], v[152:155]\n\tds_write_b128 v2, v[200:203] offset:14528\n\tv_mfma_f32_16x16x32_bf16 v[156:159], a[144:147], v[76:79], v[156:159]\n\t" \
    "v_alignbit_b32 v196, v53, v53, 16\n\tv_alignbit_b32 v197, v52, v52, 16\n\tv_mfma_f32_16x16x32_bf16 v[160:163], a[144:147], v[80:83], v[160:163]\n\tv_alignbit_b32 v198, v51, v51, 16\n\tv_alignbit_b32 v199, v50, v50, 16\n\tv_mfma_f32_16x16x32_bf16 v[152:155], a[148:151], v[68:71], v[152:155]\n\tds_write_b128 v2, v[196:199] offset:1024\n\tv_mfma_f32_16x16x32_bf16 v[156:159], a[148:151], v[72:75], v[156:159]\n\t" \
    "v_bfi_b32 v200, s46, v53, v52\n\tv_bfi_b32 v201, s46, v52, v51\n\tv_mfma_f32_16x16x32_bf16 v[160:163], a[148:151], v[76:79], v[160:163]\n\tv_bfi_b32 v202, s46, v51, v50\n\tv_bfi_b32 v203, s46, v50, v63\n\tv_mfma_f32_16x16x32_bf16 v[156:159], a[152:155], v[68:71], v[156:159]\n\tds_write_b128 v2, v[200:203] offset:5184\n\tv_mfma_f32_16x16x32_bf16 v[160:163], a[152:155], v[72:75], v[160:163]\n\t" \
    "v_alignbit_b32 v196, v52, v52, 16\n\tv_alignbit_b32 v197, v51, v51, 16\n\tv_mfma_f32_16x16x32_bf16 v[160:163], a[156:159], v[68:71], v[160:163]\n\tv_alignbit_b32 v198, v50, v50, 16\n\tv_alignbit_b32 v199, v63, v63, 16\n\ts_waitcnt lgkmcnt(9)\n\tv_mfma_f32_16x16x32_bf16 v[132:135], a[160:163], v[100:103], v[132:135]\n\tds_write_b128 v2, v[196:199] offset:9344\n\t" \
    "v_mfma_f32_16x16x32_bf16 v[136:139], a[160:163], v[104:107], v[136:139]\n\tds_read_b128 v[68:71], v1 offset:37056\n\tv_bfi_b32 v200, s46, v52, v51\n\tv_mfma_f32_16x16x32_bf16 v[140:143], a[160:163], v[108:111], v[140:143]\n\tds_read_b128 v[72:75], v1 offset:37568\n\tv_bfi_b32 v201, s46, v51, v50\n\tv_mfma_f32_16x16x32_bf16 v[144:147], a[160:163], v[112:115], v[144:147]\n\tds_read_b128 v[76:79], v1 offset:38080\n\t" \
    "v_bfi_b32 v202, s46, v50, v63\n\tv_mfma_f32_16x16x32_bf16 v[148:151], a[160:163], v[116:119], v[148:151]\n\tds_read_b128 v[80:83], v1 offset:38592\n\tv_bfi_b32 v203, s46, v63, v62\n\tv_mfma_f32_16x16x32_bf16 v[152:155], a[160:163], v[120:123], v[152:155]\n\tds_write_b128 v2, v[200:203] offset:13504\n\tv_mfma_f32_16x16x32_bf16 v[156:159], a[160:163], v[124:127], v[156:159]\n\tds_read_b128 v[84:87], v1 offset:39104\n\t" \
    "v_alignbit_b32 v196, v57, v57, 16\n\tv_mfma_f32_16x16x32_bf16 v[160:163], a[160:163], v[128:131], v[160:163]\n\tbuffer_load_dword v15, v7, s[16:19], s42 offen\n\tds_read_b128 v[88:91], v1 offset:39616\n\tv_mfma_f32_16x16x32_bf16 v[136:139], a[164:167], v[100:103], v[136:139]\n\ts_add_u32 s43, s43, 1\n\tds_read_b128 v[92:95], v1 offset:40128\n\tv_alignbit_b32 v197, v56, v56, 16\n\t" \
    "v_mfma_f32_16x16x32_bf16 v[140:143], a[164:167], v[104:107], v[140:143]\n\tv_readlane_b32 s42, v237, s43\n\ts_nop 4\n\tds_read_b128 v[96:99], v1 offset:40640\n\tv_alignbit_b32 v198, v55, v55, 16\n\tv_mfma_f32_16x16x32_bf16 v[144:147], a[164:167], v[108:111], v[144:147]\n\tv_alignbit_b32 v199, v54, v54, 16\n\tv_mfma_f32_16x16x32_bf16 v[148:151], a[164:167], v[112:115], v[148:151]\n\t" \
    "ds_write_b128 v2, v[196:199] offset:0\n\tv_mfma_f32_16x16x32_bf16 v[152:155], a[164:167], v[116:119], v[152:155]\n\tv_bfi_b32 v200, s46, v57, v56\n\tv_bfi_b32 v201, s46, v56, v55\n\tv_mfma_f32_16x16x32_bf16 v[156:159], a[164:167], v[120:123], v[156:159]\n\tv_bfi_b32 v202, s46, v55, v54\n\tv_bfi_b32 v203, s46, v54, v65\n\tv_mfma_f32_16x16x32_bf16 v[160:163], a[164:167], v[124:127], v[160:163]\n\t" \
    "ds_write_b128 v2, v[200:203] offset:4160\n\tv_mfma_f32_16x16x32_bf16 v[140:143], a[168:171], v[100:103], v[140:143]\n\tv_alignbit_b32 v196, v56, v56, 16\n\tv_alignbit_b32 v197, v55, v55, 16\n\tv_mfma_f32_16x16x32_bf16 v[144:147], a[168:171], v[104:107], v[144:147]\n\tv_alignbit_b32 v198, v54, v54, 16\n\tv_alignbit_b32 v199, v65, v65, 16\n\tv_mfma_f32_16x16x32_bf16 v[148:151], a[168:171], v[108:111], v[148:151]\n\t" \
    "ds_write_b128 v2, v[196:199] offset:8320\n\tv_mfma_f32_16x16x32_bf16 v[152:155], a[168:171], v[112:115], v[152:155]\n\tv_bfi_b32 v200, s46, v56, v55\n\tv_bfi_b32 v201, s46, v55, v54\n\tv_mfma_f32_16x16x32_bf16 v[156:159], a[168:171], v[116:119], v[156:159]\n\tv_bfi_b32 v202, s46, v54, v65\n\tv_bfi_b32 v203, s46, v65, v64\n\tv_mfma_f32_16x16x32_bf16 v[160:163], a[168:171], v[120:123], v[160:163]\n\t" \
    "ds_write_b128 v2, v[200:203] offset:12480\n\tv_mfma_f32_16x16x32_bf16 v[144:147], a[172:175], v[100:103], v[144:147]\n\tv_lshl_or_b32 v66, v66, 16, v66\n\tv_and_b32 v228, v66, v232\n\tv_mfma_f32_16x16x32_bf16 v[148:151], a[172:175], v[104:107], v[148:151]\n\tv_and_b32 v229, v66, v233\n\tv_and_b32 v230, v66, v234\n\tv_mfma_f32_16x16x32_bf16 v[152:155], a[172:175], v[108:111], v[152:155]\n\t" \
    "v_and_b32 v231, v66, v235\n\tv_mfma_f32_16x16x32_bf16 v[156:159], a[172:175], v[112:115], v[156:159]\n\tv_mfma_f32_16x16x32_bf16 v[160:163], a[172:175], v[116:119], v[160:163]\n\tv_mfma_f32_16x16x32_bf16 v[148:151], a[176:179], v[100:103], v[148:151]\n\tv_mfma_f32_16x16x32_bf16 v[152:155], a[176:179], v[104:107], v[152:155]\n\tv_mfma_f32_16x16x32_bf16 v[156:159], a[176:179], v[108:111], v[156:159]\n\tv_mfma_f32_16x16x32_bf16 v[160:163], a[176:179], v[112:115], v[160:163]\n\tv_mfma_f32_16x16x32_bf16 v[152:155], a[180:183], v[100:103], v[152:155]\n\t" \
    "v_mfma_f32_16x16x32_bf16 v[156:159], a[180:183], v[104:107], v[156:159]\n\tv_mfma_f32_16x16x32_bf16 v[160:163], a[180:183], v[108:111], v[160:163]\n\tv_mfma_f32_16x16x32_bf16 v[156:159], a[184:187], v[100:103], v[156:159]\n\tv_mfma_f32_16x16x32_bf16 v[160:163], a[184:187], v[104:107], v[160:163]\n\tv_mfma_f32_16x16x32_bf16 v[160:163], a[188:191], v[100:103], v[160:163]\n\ts_waitcnt lgkmcnt(4)\n\tv_mfma_f32_16x16x32_bf16 v[132:135], a[192:195], v[68:71], v[132:135]\n\tds_read_b128 v[100:103], v1 offset:36992\n\t" \
    "v_mfma_f32_16x16x32_bf16 v[136:139], a[192:195], v[72:75], v[136:139]\n\tds_read_b128 v[104:107], v1 offset:37504\n\tv_mfma_f32_16x16x32_bf16 v[140:143], a[192:195], v[76:79], v[140:143]\n\tds_read_b128 v[108:111], v1 offset:38016\n\tv_mfma_f32_16x16x32_bf16 v[144:147], a[192:195], v[80:83], v[144:147]\n\tds_read_b128 v[112:115], v1 offset:38528\n\tv_mfma_f32_16x16x32_bf16 v[148:151], a[192:195], v[84:87], v[148:151]\n\tds_read_b128 v[116:119], v1 offset:39040\n\t" \
    "v_mfma_f32_16x16x32_bf16 v[152:155], a[192:195], v[88:91], v[152:155]\n\tds_read_b128 v[120:123], v1 offset:39552\n\tv_mfma_f32_16x16x32_bf16 v[156:159], a[192:195], v[92:95], v[156:159]\n\tds_read_b128 v[124:127], v1 offset:40064\n\tv_mfma_f32_16x16x32_bf16 v[160:163], a[192:195], v[96:99], v[160:163]\n\tds_read_b128 v[128:131], v1 offset:40576\n\tv_mfma_f32_16x16x32_bf16 v[136:139], a[196:199], v[68:71], v[136:139]\n\tv_mfma_f32_16x16x32_bf16 v[140:143], a[196:199], v[72:75], v[140:143]\n\t" \
    "v_mfma_f32_16x16x32_bf16 v[144:147], a[196:199], v[76:79], v[144:147]\n\tv_mfma_f32_16x16x32_bf16 v[148:151], a[196:199], v[80:83], v[148:151]\n\tv_mfma_f32_16x16x32_bf16 v[152:155], a[196:199], v[84:87], v[152:155]\n\tv_mfma_f32_16x16x32_bf16 v[156:159], a[196:199], v[88:91], v[156:159]\n\tv_mfma_f32_16x16x32_bf16 v[160:163], a[196:199], v[92:95], v[160:163]\n\tv_mfma_f32_16x16x32_bf16 v[140:143], a[200:203], v[68:71], v[140:143]\n\tv_mfma_f32_16x16x32_bf16 v[144:147], a[200:203], v[72:75], v[144:147]\n\tv_mfma_f32_16x16x32_bf16 v[148:151], a[200:203], v[76:79], v[148:151]\n\t" \
    "v_mfma_f32_16x16x32_bf16 v[152:155], a[200:203], v[80:83], v[152:155]\n\tv_mfma_f32_16x16x32_bf16 v[156:159], a[200:203], v[84:87], v[156:159]\n\tv_mfma_f32_16x16x32_bf16 v[160:163], a[200:203], v[88:91], v[160:163]\n\tv_mfma_f32_16x16x32_bf16 v[144:147], a[204:207], v[68:71], v[144:147]\n\tv_mfma_f32_16x16x32_bf16 v[148:151], a[204:207], v[72:75], v[148:151]\n\tv_mfma_f32_16x16x32_bf16 v[152:155], a[204:207], v[76:79], v[152:155]\n\tv_mfma_f32_16x16x32_bf16 v[156:159], a[204:207], v[80:83], v[156:159]\n\tv_mfma_f32_16x16x32_bf16 v[160:163], a[204:207], v[84:87], v[160:163]\n\t" \
    "v_mfma_f32_16x16x32_bf16 v[148:151], a[208:211], v[68:71], v[148:151]\n\tv_mfma_f32_16x16x32_bf16 v[152:155], a[208:211], v[72:75], v[152:155]\n\tv_mfma_f32_16x16x32_bf16 v[156:159], a[208:211], v[76:79], v[156:159]\n\tv_mfma_f32_16x16x32_bf16 v[160:163], a[208:211], v[80:83], v[160:163]\n\tv_mfma_f32_16x16x32_bf16 v[152:155], a[212:215], v[68:71], v[152:155]\n\tbuffer_load_dword v15, v7, s[16:19], s42 offen\n\tv_mfma_f32_16x16x32_bf16 v[156:159], a[212:215], v[72:75], v[156:159]\n\ts_add_u32 s43, s43, 1\n\t" \
    "v_mfma_f32_16x16x32_bf16 v[160:163], a[212:215], v[76:79], v[160:163]\n\tv_readlane_b32 s42, v237, s43\n\ts_nop 4\n\tv_mfma_f32_16x16x32_bf16 v[156:159], a[216:219], v[68:71], v[156:159]\n\tv_mfma_f32_16x16x32_bf16 v[160:163], a[216:219], v[72:75], v[160:163]\n\tv_mfma_f32_16x16x32_bf16 v[160:163], a[220:223], v[68:71], v[160:163]\n\ts_waitcnt lgkmcnt(0)\n\tv_mfma_f32_16x16x32_bf16 v[132:135], a[224:227], v[100:103], v[132:135]\n\t" \
    "s_waitcnt vmcnt(3)\n\tbuffer_store_dwordx2 v[248:249], off, s[8:11], s51 sc1\n\tds_read_b64 a[0:1], v0 offset:4032\n\tv_mfma_f32_16x16x32_bf16 v[136:139], a[224:227], v[104:107], v[136:139]\n\tds_read_b64 a[2:3], v0 offset:4040\n\tv_mfma_f32_16x16x32_bf16 v[140:143], a[224:227], v[108:111], v[140:143]\n\tds_read_b64 a[4:5], v0 offset:3520\n\tv_mfma_f32_16x16x32_bf16 v[144:147], a[224:227], v[112:115], v[144:147]\n\t" \
    "ds_read_b64 a[6:7], v0 offset:3528\n\tv_mfma_f32_16x16x32_bf16 v[148:151], a[224:227], v[116:119], v[148:151]\n\tds_read_b64 a[8:9], v0 offset:3008\n\tv_mfma_f32_16x16x32_bf16 v[152:155], a[224:227], v[120:123], v[152:155]\n\tds_read_b64 a[10:11], v0 offset:3016\n\tv_mfma_f32_16x16x32_bf16 v[156:159], a[224:227], v[124:127], v[156:159]\n\tds_read_b64 a[12:13], v0 offset:2496\n\tv_mfma_f32_16x16x32_bf16 v[160:163], a[224:227], v[128:131], v[160:163]\n\t" \
    "ds_read_b64 a[14:15], v0 offset:2504\n\tv_mfma_f32_16x16x32_bf16 v[136:139], a[228:231], v[100:103], v[136:139]\n\tds_read_b64 a[16:17], v0 offset:1984\n\tv_mfma_f32_16x16x32_bf16 v[140:143], a[228:231], v[104:107], v[140:143]\n\tds_read_b64 a[18:19], v0 offset:1992\n\tv_mfma_f32_16x16x32_bf16 v[144:147], a[228:231], v[108:111], v[144:147]\n\tds_read_b64 a[20:21], v0 offset:1472\n\tv_mfma_f32_16x16x32_bf16 v[148:151], a[228:231], v[112:115], v[148:151]\n\t" \
    "ds_read_b64 a[22:23], v0 offset:1480\n\tv_mfma_f32_16x16x32_bf16 v[152:155], a[228:231], v[116:119], v[152:155]\n\tds_read_b64 a[24:25], v0 offset:960\n\tv_mfma_f32_16x16x32_bf16 v[156:159], a[228:231], v[120:123], v[156:159]\n\tds_read_b64 a[26:27], v0 offset:968\n\tv_mfma_f32_16x16x32_bf16 v[160:163], a[228:231], v[124:127], v[160:163]\n\tds_read_b64 a[28:29], v0 offset:448\n\tv_mfma_f32_16x16x32_bf16 v[140:143], a[232:235], v[100:103], v[140:143]\n\t" \
    "ds_read_b64 a[30:31], v0 offset:456\n\tv_mfma_f32_16x16x32_bf16 v[144:147], a[232:235], v[104:107], v[144:147]\n\tds_read_b128 v[68:71], v1 offset:55936\n\tv_mfma_f32_16x16x32_bf16 v[148:151], a[232:235], v[108:111], v[148:151]\n\tds_read_b128 v[72:75], v1 offset:56448\n\tv_mfma_f32_16x16x32_bf16 v[152:155], a[232:235], v[112:115], v[152:155]\n\tds_read_b128 v[76:79], v1 offset:56960\n\tv_mfma_f32_16x16x32_bf16 v[156:159], a[232:235], v[116:119], v[156:159]\n\t" \
    "ds_read_b128 v[80:83], v1 offset:57472\n\tv_mfma_f32_16x16x32_bf16 v[160:163], a[232:235], v[120:123], v[160:163]\n\tds_read_b128 v[84:87], v1 offset:57984\n\tv_mfma_f32_16x16x32_bf16 v[144:147], a[236:239], v[100:103], v[144:147]\n\tds_read_b128 v[88:91], v1 offset:58496\n\tv_mfma_f32_16x16x32_bf16 v[148:151], a[236:239], v[104:107], v[148:151]\n\tds_read_b128 v[92:95], v1 offset:59008\n\tv_mfma_f32_16x16x32_bf16 v[152:155], a[236:239], v[108:111], v[152:155]\n\t" \
    "ds_read_b128 v[96:99], v1 offset:59520\n\tv_mfma_f32_16x16x32_bf16 v[156:159], a[236:239], v[112:115], v[156:159]\n\tv_mfma_f32_16x16x32_bf16 v[160:163], a[236:239], v[116:119], v[160:163]\n\tv_mfma_f32_16x16x32_bf16 v[148:151], a[240:243], v[100:103], v[148:151]\n\tv_mfma_f32_16x16x32_bf16 v[152:155], a[240:243], v[104:107], v[152:155]\n\tv_mfma_f32_16x16x32_bf16 v[156:159], a[240:243], v[108:111], v[156:159]\n\tv_mfma_f32_16x16x32_bf16 v[160:163], a[240:243], v[112:115], v[160:163]\n\tv_mfma_f32_16x16x32_bf16 v[152:155], a[244:247], v[100:103], v[152:155]\n\t" \
    "v_mfma_f32_16x16x32_bf16 v[156:159], a[244:247], v[104:107], v[156:159]\n\tv_mfma_f32_16x16x32_bf16 v[160:163], a[244:247], v[108:111], v[160:163]\n\tv_mfma_f32_16x16x32_bf16 v[156:159], a[248:251], v[100:103], v[156:159]\n\tv_mfma_f32_16x16x32_bf16 v[160:163], a[248:251], v[104:107], v[160:163]\n\tv_mfma_f32_16x16x32_bf16 v[160:163], a[252:255], v[100:103], v[160:163]\n\ts_waitcnt lgkmcnt(0)\n\tv_mfma_f32_16x16x32_bf16 v[164:167], v[228:231], v[68:71], 0\n\tds_read_b64 a[32:33], v0 offset:3968\n\t" \
    "ds_read_b64 a[34:35], v0 offset:3976\n\tv_mfma_f32_16x16x32_bf16 v[168:171], v[228:231], v[72:75], 0\n\tds_read_b64 a[36:37], v0 offset:3456\n\tds_read_b64 a[38:39], v0 offset:3464\n\tv_mfma_f32_16x16x32_bf16 v[172:175], v[228:231], v[76:79], 0\n\tds_read_b64 a[40:41], v0 offset:2944\n\tds_read_b64 a[42:43], v0 offset:2952\n\tv_mfma_f32_16x16x32_bf16 v[176:179], v[228:231], v[80:83], 0\n\t" \
    "ds_read_b64 a[44:45], v0 offset:2432\n\tds_read_b64 a[46:47], v0 offset:2440\n\tv_mfma_f32_16x16x32_bf16 v[180:183], v[228:231], v[84:87], 0\n\tds_read_b64 a[48:49], v0 offset:1920\n\tds_read_b64 a[50:51], v0 offset:1928\n\tv_mfma_f32_16x16x32_bf16 v[184:187], v[228:231], v[88:91], 0\n\tds_read_b64 a[52:53], v0 offset:1408\n\tv_cvt_pk_bf16_f32 v132, v132, v133\n\t" \
    "v_mfma_f32_16x16x32_bf16 v[188:191], v[228:231], v[92:95], 0\n\tds_read_b64 a[54:55], v0 offset:1416\n\tv_cvt_pk_bf16_f32 v133, v134, v135\n\tv_mfma_f32_16x16x32_bf16 v[192:195], v[228:231], v[96:99], 0\n\tds_read_b64 a[56:57], v0 offset:896\n\tds_write_b64 v3, v[132:133] offset:36992\n\tv_mfma_f32_16x16x32_bf16 v[164:167], a[0:3], v[68:71], v[164:167]\n\tds_read_b64 a[58:59], v0 offset:904\n\t" \
    "v_cvt_pk_bf16_f32 v136, v136, v137\n\tv_mfma_f32_16x16x32_bf16 v[168:171], a[0:3], v[72:75], v[168:171]\n\tds_read_b64 a[60:61], v0 offset:384\n\tv_cvt_pk_bf16_f32 v137, v138, v139\n\tv_mfma_f32_16x16x32_bf16 v[172:175], a[0:3], v[76:79], v[172:175]\n\tds_read_b64 a[62:63], v0 offset:392\n\tds_write_b64 v3, v[136:137] offset:37504\n\tv_mfma_f32_16x16x32_bf16 v[176:179], a[0:3], v[80:83], v[176:179]\n\t" \
    "ds_read_b128 v[100:103], v1 offset:55872\n\tv_cvt_pk_bf16_f32 v140, v140, v141\n\tv_mfma_f32_16x16x32_bf16 v[180:183], a[0:3], v[84:87], v[180:183]\n\tds_read_b128 v[104:107], v1 offset:56384\n\tv_cvt_pk_bf16_f32 v141, v142, v143\n\tv_mfma_f32_16x16x32_bf16 v[184:187], a[0:3], v[88:91], v[184:187]\n\tds_read_b128 v[108:111], v1 offset:56896\n\tds_write_b64 v3, v[140:141] offset:38016\n\t" \
    "v_mfma_f32_16x16x32_bf16 v[188:191], a[0:3], v[92:95], v[188:191]\n\tds_read_b128 v[112:115], v1 offset:57408\n\tv_cvt_pk_bf16_f32 v144, v144, v145\n\tv_mfma_f32_16x16x32_bf16 v[192:195], a[0:3], v[96:99], v[192:195]\n\tds_read_b128 v[116:119], v1 offset:57920\n\tv_cvt_pk_bf16_f32 v145, v146, v147\n\tv_mfma_f32_16x16x32_bf16 v[168:171], a[4:7], v[68:71], v[168:171]\n\tds_read_b128 v[120:123], v1 offset:58432\n\t" \
    "ds_write_b64 v3, v[144:145] offset:38528\n\tv_mfma_f32_16x16x32_bf16 v[172:175], a[4:7], v[72:75], v[172:175]\n\tbuffer_load_dword v15, v7, s[16:19], s42 offen\n\tds_read_b128 v[124:127], v1 offset:58944\n\tv_mfma_f32_16x16x32_bf16 v[176:179], a[4:7], v[76:79], v[176:179]\n\ts_add_u32 s43, s43, 1\n\tds_read_b128 v[128:131], v1 offset:59456\n\tv_cvt_pk_bf16_f32 v148, v148, v149\n\t" \
    "v_mfma_f32_16x16x32_bf16 v[180:183], a[4:7], v[80:83], v[180:183]\n\tv_readlane_b32 s42, v237, s43\n\ts_nop 4\n\tds_read_b64 a[64:65], v0 offset:3904\n\tv_cvt_pk_bf16_f32 v149, v150, v151\n\tv_mfma_f32_16x16x32_bf16 v[184:187], a[4:7], v[84:87], v[184:187]\n\tds_read_b64 a[66:67], v0 offset:3912\n\tds_write_b64 v3, v[148:149] offset:39040\n\t" \
    "v_mfma_f32_16x16x32_bf16 v[188:191], a[4:7], v[88:91], v[188:191]\n\tds_read_b64 a[68:69], v0 offset:3392\n\tv_cvt_pk_bf16_f32 v152, v152, v153\n\tv_mfma_f32_16x16x32_bf16 v[192:195], a[4:7], v[92:95], v[192:195]\n\tds_read_b64 a[70:71], v0 offset:3400\n\tv_cvt_pk_bf16_f32 v153, v154, v155\n\tv_mfma_f32_16x16x32_bf16 v[172:175], a[8:11], v[68:71], v[172:175]\n\tds_read_b64 a[72:73], v0 offset:2880\n\t" \
    "ds_write_b64 v3, v[152:153] offset:39552\n\tv_mfma_f32_16x16x32_bf16 v[176:179], a[8:11], v[72:75], v[176:179]\n\tds_read_b64 a[74:75], v0 offset:2888\n\tv_cvt_pk_bf16_f32 v156, v156, v157\n\tv_mfma_f32_16x16x32_bf16 v[180:183], a[8:11], v[76:79], v[180:183]\n\tds_read_b64 a[76:77], v0 offset:2368\n\tv_cvt_pk_bf16_f32 v157, v158, v159\n\tv_mfma_f32_16x16x32_bf16 v[184:187], a[8:11], v[80:83], v[184:187]\n\t" \
    "ds_read_b64 a[78:79], v0 offset:2376\n\tds_write_b64 v3, v[156:157] offset:40064\n\tv_mfma_f32_16x16x32_bf16 v[188:191], a[8:11], v[84:87], v[188:191]\n\tds_read_b64 a[80:81], v0 offset:1856\n\tv_cvt_pk_bf16_f32 v160, v160, v161\n\tv_mfma_f32_16x16x32_bf16 v[192:195], a[8:11], v[88:91], v[192:195]\n\tds_read_b64 a[82:83], v0 offset:1864\n\tv_cvt_pk_bf16_f32 v161, v162, v163\n\t" \
    "v_mfma_f32_16x16x32_bf16 v[176:179], a[12:15], v[68:71], v[176:179]\n\tds_read_b64 a[84:85], v0 offset:1344\n\tds_write_b64 v3, v[160:161] offset:40576\n\tv_mfma_f32_16x16x32_bf16 v[180:183], a[12:15], v[72:75], v[180:183]\n\tds_read_b64 a[86:87], v0 offset:1352\n\tv_mfma_f32_16x16x32_bf16 v[184:187], a[12:15], v[76:79], v[184:187]\n\tds_read_b64 a[88:89], v0 offset:832\n\tv_mfma_f32_16x16x32_bf16 v[188:191], a[12:15], v[80:83], v[188:191]\n\t" \
    "ds_read_b64 a[90:91], v0 offset:840\n\tv_mfma_f32_16x16x32_bf16 v[192:195], a[12:15], v[84:87], v[192:195]\n\tds_read_b64 a[92:93], v0 offset:320\n\tv_mfma_f32_16x16x32_bf16 v[180:183], a[16:19], v[68:71], v[180:183]\n\tds_read_b64 a[94:95], v0 offset:328\n\tv_mfma_f32_16x16x32_bf16 v[184:187], a[16:19], v[72:75], v[184:187]\n\tds_read_b64 a[96:97], v0 offset:3840\n\tv_mfma_f32_16x16x32_bf16 v[188:191], a[16:19], v[76:79], v[188:191]\n\t" \
    "ds_read_b64 a[98:99], v0 offset:3848\n\tv_mfma_f32_16x16x32_bf16 v[192:195], a[16:19], v[80:83], v[192:195]\n\tds_read_b64 a[100:101], v0 offset:3328\n\tv_mfma_f32_16x16x32_bf16 v[184:187], a[20:23], v[68:71], v[184:187]\n\tds_read_b64 a[102:103], v0 offset:3336\n\tv_mfma_f32_16x16x32_bf16 v[188:191], a[20:23], v[72:75], v[188:191]\n\tv_mfma_f32_16x16x32_bf16 v[192:195], a[20:23], v[76:79], v[192:195]\n\tv_mfma_f32_16x16x32_bf16 v[188:191], a[24:27], v[68:71], v[188:191]\n\t" \
    "v_mfma_f32_16x16x32_bf16 v[192:195], a[24:27], v[72:75], v[192:195]\n\tv_mfma_f32_16x16x32_bf16 v[192:195], a[28:31], v[68:71], v[192:195]\n\ts_waitcnt lgkmcnt(9)\n\ts_barrier\n\tv_mfma_f32_16x16x32_bf16 v[164:167], a[32:35], v[100:103], v[164:167]\n\tds_read_b64 a[104:105], v0 offset:2816\n\tds_read_b64 a[106:107], v0 offset:2824\n\tv_mfma_f32_16x16x32_bf16 v[168:171], a[32:35], v[104:107], v[168:171]\n\t" \
    "ds_read_b64 a[108:109], v0 offset:2304\n\tds_read_b64 a[110:111], v0 offset:2312\n\tv_mfma_f32_16x16x32_bf16 v[172:175], a[32:35], v[108:111], v[172:175]\n\tds_read_b64 a[112:113], v0 offset:1792\n\tds_read_b64 a[114:115], v0 offset:1800\n\tv_mfma_f32_16x16x32_bf16 v[176:179], a[32:35], v[112:115], v[176:179]\n\tds_read_b64 a[116:117], v0 offset:1280\n\tds_read_b64 a[118:119], v0 offset:1288\n\t" \
    "v_mfma_f32_16x16x32_bf16 v[180:183], a[32:35], v[116:119], v[180:183]\n\tds_read_b64 a[120:121], v0 offset:768\n\tds_read_b64 a[122:123], v0 offset:776\n\tv_mfma_f32_16x16x32_bf16 v[184:187], a[32:35], v[120:123], v[184:187]\n\tds_read_b64 a[124:125], v0 offset:256\n\ts_add_u32 s51, s44, 8\n\tv_mfma_f32_16x16x32_bf16 v[188:191], a[32:35], v[124:127], v[188:191]\n\tds_read_b64 a[126:127], v0 offset:264\n\t" \
    "s_mul_i32 s65, s51, 1366\n\tv_mfma_f32_16x16x32_bf16 v[192:195], a[32:35], v[128:131], v[192:195]\n\tds_read_b64 a[128:129], v0 offset:3776\n\ts_lshr_b32 s65, s65, 16\n\tv_mfma_f32_16x16x32_bf16 v[168:171], a[36:39], v[100:103], v[168:171]\n\tds_read_b64 a[130:131], v0 offset:3784\n\ts_mul_i32 s56, s65, 48\n\tv_mfma_f32_16x16x32_bf16 v[172:175], a[36:39], v[104:107], v[172:175]\n\t" \
    "ds_read_b64 a[132:133], v0 offset:3264\n\ts_sub_u32 s56, s51, s56\n\tv_mfma_f32_16x16x32_bf16 v[176:179], a[36:39], v[108:111], v[176:179]\n\tds_read_b64 a[134:135], v0 offset:3272\n\ts_lshl_b32 s56, s56, 6\n\tv_mfma_f32_16x16x32_bf16 v[180:183], a[36:39], v[112:115], v[180:183]\n\tds_read_b128 v[68:71], v1 offset:55808\n\ts_lshr_b32 s70, s65, 2\n\t" \
    "v_mfma_f32_16x16x32_bf16 v[184:187], a[36:39], v[116:119], v[184:187]\n\tds_read_b128 v[72:75], v1 offset:56320\n\ts_mul_i32 s70, s70, 252\n\tv_mfma_f32_16x16x32_bf16 v[188:191], a[36:39], v[120:123], v[188:191]\n\tds_read_b128 v[76:79], v1 offset:56832\n\ts_add_u32 s65, s65, s70\n\tv_mfma_f32_16x16x32_bf16 v[192:195], a[36:39], v[124:127], v[192:195]\n\tds_read_b128 v[80:83], v1 offset:57344\n\t" \
    "s_mul_i32 s65, s65, 3072\n\tv_mfma_f32_16x16x32_bf16 v[172:175], a[40:43], v[100:103], v[172:175]\n\tds_read_b128 v[84:87], v1 offset:57856\n\ts_add_u32 s56, s56, s65\n\tv_mfma_f32_16x16x32_bf16 v[176:179], a[40:43], v[104:107], v[176:179]\n\tds_read_b128 v[88:91], v1 offset:58368\n\ts_add_u32 s56, s56, 4849664\n\tv_mfma_f32_16x16x32_bf16 v[180:183], a[40:43], v[108:111], v[180:183]\n\t" \
    "ds_read_b128 v[92:95], v1 offset:58880\n\ts_add_u32 s51, s51, s38\n\tv_mfma_f32_16x16x32_bf16 v[184:187], a[40:43], v[112:115], v[184:187]\n\tds_read_b128 v[96:99], v1 offset:59392\n\ts_lshl_b32 s51, s51, 3\n\tv_mfma_f32_16x16x32_bf16 v[188:191], a[40:43], v[116:119], v[188:191]\n\tds_read_b64 a[136:137], v0 offset:2752\n\ts_add_u32 s51, s51, 50176\n\t" \
    "ds_read_b64_tr_b16 v[204:205], v4 offset:36992\n\tv_mfma_f32_16x16x32_bf16 v[192:195], a[40:43], v[120:123], v[192:195]\n\tds_read_b64 a[138:139], v0 offset:2760\n\tds_read_b64_tr_b16 v[206:207], v4 offset:37000\n\tv_mfma_f32_16x16x32_bf16 v[176:179], a[44:47], v[100:103], v[176:179]\n\tds_read_b64 a[140:141], v0 offset:2240\n\tds_read_b64_tr_b16 v[208:209], v4 offset:37056\n\tv_mfma_f32_16x16x32_bf16 v[180:183], a[44:47], v[104:107], v[180:183]\n\t" \
    "ds_read_b64 a[142:143], v0 offset:2248\n\tds_read_b64_tr_b16 v[210:211], v4 offset:37064\n\tv_mfma_f32_16x16x32_bf16 v[184:187], a[44:47], v[108:111], v[184:187]\n\tds_read_b64 a[144:145], v0 offset:1728\n\tds_read_b64_tr_b16 v[212:213], v4 offset:37504\n\tv_mfma_f32_16x16x32_bf16 v[188:191], a[44:47], v[112:115], v[188:191]\n\tds_read_b64 a[146:147], v0 offset:1736\n\tds_read_b64_tr_b16 v[214:215], v4 offset:37512\n\t" \
    "v_mfma_f32_16x16x32_bf16 v[192:195], a[44:47], v[116:119], v[192:195]\n\tds_read_b64 a[148:149], v0 offset:1216\n\tds_read_b64_tr_b16 v[216:217], v4 offset:37568\n\tv_mfma_f32_16x16x32_bf16 v[180:183], a[48:51], v[100:103], v[180:183]\n\tds_read_b64 a[150:151], v0 offset:1224\n\tds_read_b64_tr_b16 v[218:219], v4 offset:37576\n\tv_mfma_f32_16x16x32_bf16 v[184:187], a[48:51], v[104:107], v[184:187]\n\tds_read_b64 a[152:153], v0 offset:704\n\t" \
    "v_mfma_f32_16x16x32_bf16 v[188:191], a[48:51], v[108:111], v[188:191]\n\tds_read_b64 a[154:155], v0 offset:712\n\tv_mfma_f32_16x16x32_bf16 v[192:195], a[48:51], v[112:115], v[192:195]\n\tds_read_b64 a[156:157], v0 offset:192\n\tv_mfma_f32_16x16x32_bf16 v[184:187], a[52:55], v[100:103], v[184:187]\n\tds_read_b64 a[158:159], v0 offset:200\n\tv_mfma_f32_16x16x32_bf16 v[188:191], a[52:55], v[104:107], v[188:191]\n\tv_mfma_f32_16x16x32_bf16 v[192:195], a[52:55], v[108:111], v[192:195]\n\t" \
    "buffer_load_dword v15, v7, s[16:19], s42 offen\n\ts_waitcnt lgkmcnt(15)\n\tbuffer_store_dwordx4 v[204:207], v5, s[8:11], s56 offen sc1\n\tv_mfma_f32_16x16x32_bf16 v[188:191], a[56:59], v[100:103], v[188:191]\n\ts_add_u32 s43, s43, 1\n\ts_add_u32 s56, s56, 49152\n\tv_mfma_f32_16x16x32_bf16 v[192:195], a[56:59], v[104:107], v[192:195]\n\tv_readlane_b32 s42, v237, s43\n\t" \
    "s_nop 4\n\tv_mfma_f32_16x16x32_bf16 v[192:195], a[60:63], v[100:103], v[192:195]\n\tv_mfma_f32_16x16x32_bf16 v[164:167], a[64:67], v[68:71], v[164:167]\n\tds_read_b64 a[160:161], v0 offset:3712\n\tds_read_b64 a[162:163], v0 offset:3720\n\tv_mfma_f32_16x16x32_bf16 v[168:171], a[64:67], v[72:75], v[168:171]\n\tds_read_b64 a[164:165], v0 offset:3200\n\tds_read_b64 a[166:167], v0 offset:3208\n\t" \
    "v_mfma_f32_16x16x32_bf16 v[172:175], a[64:67], v[76:79], v[172:175]\n\tds_read_b64 a[168:169], v0 offset:2688\n\tds_read_b64 a[170:171], v0 offset:2696\n\tv_mfma_f32_16x16x32_bf16 v[176:179], a[64:67], v[80:83], v[176:179]\n\tds_read_b64 a[172:173], v0 offset:2176\n\tds_read_b64 a[174:175], v0 offset:2184\n\tv_mfma_f32_16x16x32_bf16 v[180:183], a[64:67], v[84:87], v[180:183]\n\tds_read_b64 a[176:177], v0 offset:1664\n\t" \
    "ds_read_b64 a[178:179], v0 offset:1672\n\tv_mfma_f32_16x16x32_bf16 v[184:187], a[64:67], v[88:91], v[184:187]\n\tds_read_b64 a[180:181], v0 offset:1152\n\tv_mfma_f32_16x16x32_bf16 v[188:191], a[64:67], v[92:95], v[188:191]\n\tds_read_b64 a[182:183], v0 offset:1160\n\tv_mfma_f32_16x16x32_bf16 v[192:195], a[64:67], v[96:99], v[192:195]\n\tds_read_b64 a[184:185], v0 offset:640\n\tv_mfma_f32_16x16x32_bf16 v[168:171], a[68:71], v[68:71], v[168:171]\n\t" \
    "ds_read_b64 a[186:187], v0 offset:648\n\tv_mfma_f32_16x16x32_bf16 v[172:175], a[68:71], v[72:75], v[172:175]\n\tds_read_b64 a[188:189], v0 offset:128\n\tv_mfma_f32_16x16x32_bf16 v[176:179], a[68:71], v[76:79], v[176:179]\n\tds_read_b64 a[190:191], v0 offset:136\n\tv_mfma_f32_16x16x32_bf16 v[180:183], a[68:71], v[80:83], v[180:183]\n\tds_read_b128 v[100:103], v1 offset:55744\n\tv_mfma_f32_16x16x32_bf16 v[184:187], a[68:71], v[84:87], v[184:187]\n\t" \
    "ds_read_b128 v[104:107], v1 offset:56256\n\tv_mfma_f32_16x16x32_bf16 v[188:191], a[68:71], v[88:91], v[188:191]\n\tds_read_b128 v[108:111], v1 offset:56768\n\tv_mfma_f32_16x16x32_bf16 v[192:195], a[68:71], v[92:95], v[192:195]\n\tds_read_b128 v[112:115], v1 offset:57280\n\tv_mfma_f32_16x16x32_bf16 v[172:175], a[72:75], v[68:71], v[172:175]\n\tds_read_b128 v[116:119], v1 offset:57792\n\ts_waitcnt lgkmcnt(15)\n\t" \
    "buffer_store_dwordx4 v[208:211], v5, s[8:11], s56 offen sc1\n\tv_mfma_f32_16x16x32_bf16 v[176:179], a[72:75], v[72:75], v[176:179]\n\tds_read_b128 v[120:123], v1 offset:58304\n\ts_add_u32 s56, s56, 49152\n\tv_mfma_f32_16x16x32_bf16 v[180:183], a[72:75], v[76:79], v[180:183]\n\tds_read_b128 v[124:127], v1 offset:58816\n\tv_mfma_f32_16x16x32_bf16 v[184:187], a[72:75], v[80:83], v[184:187]\n\tds_read_b128 v[128:131], v1 offset:59328\n\t" \
    "v_mfma_f32_16x16x32_bf16 v[188:191], a[72:75], v[84:87], v[188:191]\n\tds_read_b64 a[192:193], v0 offset:3648\n\tv_mfma_f32_16x16x32_bf16 v[192:195], a[72:75], v[88:91], v[192:195]\n\tds_read_b64 a[194:195], v0 offset:3656\n\tv_mfma_f32_16x16x32_bf16 v[176:179], a[76:79], v[68:71], v[176:179]\n\tds_read_b64 a[196:197], v0 offset:3136\n\tv_mfma_f32_16x16x32_bf16 v[180:183], a[76:79], v[72:75], v[180:183]\n\tds_read_b64 a[198:199], v0 offset:3144\n\t" \
    "v_mfma_f32_16x16x32_bf16 v[184:187], a[76:79], v[76:79], v[184:187]\n\tds_read_b64 a[200:201], v0 offset:2624\n\tv_mfma_f32_16x16x32_bf16 v[188:191], a[76:79], v[80:83], v[188:191]\n\tds_read_b64 a[202:203], v0 offset:2632\n\tv_mfma_f32_16x16x32_bf16 v[192:195], a[76:79], v[84:87], v[192:195]\n\tds_read_b64 a[204:205], v0 offset:2112\n\tv_mfma_f32_16x16x32_bf16 v[180:183], a[80:83], v[68:71], v[180:183]\n\tds_read_b64 a[206:207], v0 offset:2120\n\t" \
    "v_mfma_f32_16x16x32_bf16 v[184:187], a[80:83], v[72:75], v[184:187]\n\tds_read_b64 a[208:209], v0 offset:1600\n\tv_mfma_f32_16x16x32_bf16 v[188:191], a[80:83], v[76:79], v[188:191]\n\tds_read_b64 a[210:211], v0 offset:1608\n\tv_mfma_f32_16x16x32_bf16 v[192:195], a[80:83], v[80:83], v[192:195]\n\tds_read_b64 a[212:213], v0 offset:1088\n\tv_mfma_f32_16x16x32_bf16 v[184:187], a[84:87], v[68:71], v[184:187]\n\tds_read_b64 a[214:215], v0 offset:1096\n\t" \
    "v_mfma_f32_16x16x32_bf16 v[188:191], a[84:87], v[72:75], v[188:191]\n\tbuffer_store_dwordx4 v[212:215], v5, s[8:11], s56 offen sc1\n\tv_mfma_f32_16x16x32_bf16 v[192:195], a[84:87], v[76:79], v[192:195]\n\ts_add_u32 s56, s56, 49152\n\tv_mfma_f32_16x16x32_bf16 v[188:191], a[88:91], v[68:71], v[188:191]\n\tv_mfma_f32_16x16x32_bf16 v[192:195], a[88:91], v[72:75], v[192:195]\n\tv_mfma_f32_16x16x32_bf16 v[192:195], a[92:95], v[68:71], v[192:195]\n\ts_waitcnt lgkmcnt(12)\n\t" \
    "v_mfma_f32_16x16x32_bf16 v[164:167], a[96:99], v[100:103], v[164:167]\n\tds_read_b64 a[216:217], v0 offset:576\n\tv_mfma_f32_16x16x32_bf16 v[168:171], a[96:99], v[104:107], v[168:171]\n\tds_read_b64 a[218:219], v0 offset:584\n\tv_mfma_f32_16x16x32_bf16 v[172:175], a[96:99], v[108:111], v[172:175]\n\tds_read_b64 a[220:221], v0 offset:64\n\tv_mfma_f32_16x16x32_bf16 v[176:179], a[96:99], v[112:115], v[176:179]\n\tds_read_b64 a[222:223], v0 offset:72\n\t" \
    "v_mfma_f32_16x16x32_bf16 v[180:183], a[96:99], v[116:119], v[180:183]\n\tds_read_b64 a[224:225], v0 offset:3584\n\tv_mfma_f32_16x16x32_bf16 v[184:187], a[96:99], v[120:123], v[184:187]\n\tds_read_b64 a[226:227], v0 offset:3592\n\tv_mfma_f32_16x16x32_bf16 v[188:191], a[96:99], v[124:127], v[188:191]\n\tds_read_b64 a[228:229], v0 offset:3072\n\tv_mfma_f32_16x16x32_bf16 v[192:195], a[96:99], v[128:131], v[192:195]\n\tds_read_b64 a[230:231], v0 offset:3080\n\t" \
    "v_mfma_f32_16x16x32_bf16 v[168:171], a[100:103], v[100:103], v[168:171]\n\tds_read_b64 a[232:233], v0 offset:2560\n\tbuffer_store_dwordx4 v[216:219], v5, s[8:11], s56 offen sc1\n\tv_mfma_f32_16x16x32_bf16 v[172:175], a[100:103], v[104:107], v[172:175]\n\tds_read_b64 a[234:235], v0 offset:2568\n\ts_add_u32 s56, s56, 49152\n\tv_mfma_f32_16x16x32_bf16 v[176:179], a[100:103], v[108:111], v[176:179]\n\tds_read_b64 a[236:237], v0 offset:2048\n\t" \
    "v_mfma_f32_16x16x32_bf16 v[180:183], a[100:103], v[112:115], v[180:183]\n\tds_read_b64 a[238:239], v0 offset:2056\n\tv_mfma_f32_16x16x32_bf16 v[184:187], a[100:103], v[116:119], v[184:187]\n\tds_read_b64 a[240:241], v0 offset:1536\n\tv_mfma_f32_16x16x32_bf16 v[188:191], a[100:103], v[120:123], v[188:191]\n\tds_read_b64 a[242:243], v0 offset:1544\n\tv_mfma_f32_16x16x32_bf16 v[192:195], a[100:103], v[124:127], v[192:195]\n\tds_read_b64 a[244:245], v0 offset:1024\n\t" \
    "v_mfma_f32_16x16x32_bf16 v[172:175], a[104:107], v[100:103], v[172:175]\n\tds_read_b64 a[246:247], v0 offset:1032\n\tv_mfma_f32_16x16x32_bf16 v[176:179], a[104:107], v[104:107], v[176:179]\n\tds_read_b128 v[68:71], v1 offset:55680\n\tv_mfma_f32_16x16x32_bf16 v[180:183], a[104:107], v[108:111], v[180:183]\n\tds_read_b128 v[72:75], v1 offset:56192\n\tv_mfma_f32_16x16x32_bf16 v[184:187], a[104:107], v[112:115], v[184:187]\n\tds_read_b128 v[76:79], v1 offset:56704\n\t" \
    "v_mfma_f32_16x16x32_bf16 v[188:191], a[104:107], v[116:119], v[188:191]\n\tds_read_b128 v[80:83], v1 offset:57216\n\tv_mfma_f32_16x16x32_bf16 v[192:195], a[104:107], v[120:123], v[192:195]\n\tbuffer_load_dword v15, v7, s[16:19], s42 offen\n\tds_read_b128 v[84:87], v1 offset:57728\n\tv_mfma_f32_16x16x32_bf16 v[176:179], a[108:111], v[100:103], v[176:179]\n\ts_add_u32 s43, s43, 1\n\tds_read_b128 v[88:91], v1 offset:58240\n\t" \
    "v_mfma_f32_16x16x32_bf16 v[180:183], a[108:111], v[104:107], v[180:183]\n\tv_readlane_b32 s42, v237, s43\n\ts_nop 4\n\tds_read_b128 v[92:95], v1 offset:58752\n\tv_mfma_f32_16x16x32_bf16 v[184:187], a[108:111], v[108:111], v[184:187]\n\tds_read_b128 v[96:99], v1 offset:59264\n\tv_mfma_f32_16x16x32_bf16 v[188:191], a[108:111], v[112:115], v[188:191]\n\tds_read_b64 a[248:249], v0 offset:512\n\t" \
    "v_mfma_f32_16x16x32_bf16 v[192:195], a[108:111], v[116:119], v[192:195]\n\tds_read_b64 a[250:251], v0 offset:520\n\tv_mfma_f32_16x16x32_bf16 v[180:183], a[112:115], v[100:103], v[180:183]\n\tds_read_b64 a[252:253], v0 offset:0\n\tv_mfma_f32_16x16x32_bf16 v[184:187], a[112:115], v[104:107], v[184:187]\n\tds_read_b64 a[254:255], v0 offset:8\n\tv_mfma_f32_16x16x32_bf16 v[188:191], a[112:115], v[108:111], v[188:191]\n\tv_mfma_f32_16x16x32_bf16 v[192:195], a[112:115], v[112:115], v[192:195]\n\t" \
    "v_mfma_f32_16x16x32_bf16 v[184:187], a[116:119], v[100:103], v[184:187]\n\tv_mfma_f32_16x16x32_bf16 v[188:191], a[116:119], v[104:107], v[188:191]\n\tv_mfma_f32_16x16x32_bf16 v[192:195], a[116:119], v[108:111], v[192:195]\n\tv_mfma_f32_16x16x32_bf16 v[188:191], a[120:123], v[100:103], v[188:191]\n\tv_mfma_f32_16x16x32_bf16 v[192:195], a[120:123], v[104:107], v[192:195]\n\tv_mfma_f32_16x16x32_bf16 v[192:195], a[124:127], v[100:103], v[192:195]\n\ts_waitcnt lgkmcnt(4)\n\tv_mfma_f32_16x16x32_bf16 v[164:167], a[128:131], v[68:71], v[164:167]\n\t" \
    "ds_read_b128 v[100:103], v1 offset:55616\n\tv_mfma_f32_16x16x32_bf16 v[168:171], a[128:131], v[72:75], v[168:171]\n\tds_read_b128 v[104:107], v1 offset:56128\n\tv_mfma_f32_16x16x32_bf16 v[172:175], a[128:131], v[76:79], v[172:175]\n\tds_read_b128 v[108:111], v1 offset:56640\n\tv_mfma_f32_16x16x32_bf16 v[176:179], a[128:131], v[80:83], v[176:179]\n\tds_read_b128 v[112:115], v1 offset:57152\n\tv_mfma_f32_16x16x32_bf16 v[180:183], a[128:131], v[84:87], v[180:183]\n\t" \
    "ds_read_b128 v[116:119], v1 offset:57664\n\tv_mfma_f32_16x16x32_bf16 v[184:187], a[128:131], v[88:91], v[184:187]\n\tds_read_b128 v[120:123], v1 offset:58176\n\tv_mfma_f32_16x16x32_bf16 v[188:191], a[128:131], v[92:95], v[188:191]\n\tds_read_b128 v[124:127], v1 offset:58688\n\tv_mfma_f32_16x16x32_bf16 v[192:195], a[128:131], v[96:99], v[192:195]\n\tds_read_b128 v[128:131], v1 offset:59200\n\tv_mfma_f32_16x16x32_bf16 v[168:171], a[132:135], v[68:71], v[168:171]\n\t" \
    "v_mfma_f32_16x16x32_bf16 v[172:175], a[132:135], v[72:75], v[172:175]\n\tv_mfma_f32_16x16x32_bf16 v[176:179], a[132:135], v[76:79], v[176:179]\n\tv_mfma_f32_16x16x32_bf16 v[180:183], a[132:135], v[80:83], v[180:183]\n\tv_mfma_f32_16x16x32_bf16 v[184:187], a[132:135], v[84:87], v[184:187]\n\tv_mfma_f32_16x16x32_bf16 v[188:191], a[132:135], v[88:91], v[188:191]\n\tv_mfma_f32_16x16x32_bf16 v[192:195], a[132:135], v[92:95], v[192:195]\n\tv_mfma_f32_16x16x32_bf16 v[172:175], a[136:139], v[68:71], v[172:175]\n\tv_mfma_f32_16x16x32_bf16 v[176:179], a[136:139], v[72:75], v[176:179]\n\t" \
    "v_mfma_f32_16x16x32_bf16 v[180:183], a[136:139], v[76:79], v[180:183]\n\tv_mfma_f32_16x16x32_bf16 v[184:187], a[136:139], v[80:83], v[184:187]\n\tv_mfma_f32_16x16x32_bf16 v[188:191], a[136:139], v[84:87], v[188:191]\n\tv_mfma_f32_16x16x32_bf16 v[192:195], a[136:139], v[88:91], v[192:195]\n\tv_mfma_f32_16x16x32_bf16 v[176:179], a[140:143], v[68:71], v[176:179]\n\tv_mfma_f32_16x16x32_bf16 v[180:183], a[140:143], v[72:75], v[180:183]\n\tv_mfma_f32_16x16x32_bf16 v[184:187], a[140:143], v[76:79], v[184:187]\n\tv_mfma_f32_16x16x32_bf16 v[188:191], a[140:143], v[80:83], v[188:191]\n\t" \
    "v_mfma_f32_16x16x32_bf16 v[192:195], a[140:143], v[84:87], v[192:195]\n\tv_mfma_f32_16x16x32_bf16 v[180:183], a[144:147], v[68:71], v[180:183]\n\tv_mfma_f32_16x16x32_bf16 v[184:187], a[144:147], v[72:75], v[184:187]\n\tv_mfma_f32_16x16x32_bf16 v[188:191], a[144:147], v[76:79], v[188:191]\n\tv_mfma_f32_16x16x32_bf16 v[192:195], a[144:147], v[80:83], v[192:195]\n\tv_mfma_f32_16x16x32_bf16 v[184:187], a[148:151], v[68:71], v[184:187]\n\tv_mfma_f32_16x16x32_bf16 v[188:191], a[148:151], v[72:75], v[188:191]\n\tv_mfma_f32_16x16x32_bf16 v[192:195], a[148:151], v[76:79], v[192:195]\n\t" \
    "v_mfma_f32_16x16x32_bf16 v[188:191], a[152:155], v[68:71], v[188:191]\n\tv_mfma_f32_16x16x32_bf16 v[192:195], a[152:155], v[72:75], v[192:195]\n\tv_mfma_f32_16x16x32_bf16 v[192:195], a[156:159], v[68:71], v[192:195]\n\ts_waitcnt lgkmcnt(0)\n\tv_mfma_f32_16x16x32_bf16 v[164:167], a[160:163], v[100:103], v[164:167]\n\tds_read_b128 v[68:71], v1 offset:55552\n\tv_mfma_f32_16x16x32_bf16 v[168:171], a[160:163], v[104:107], v[168:171]\n\tds_read_b128 v[72:75], v1 offset:56064\n\t" \
    "v_mfma_f32_16x16x32_bf16 v[172:175], a[160:163], v[108:111], v[172:175]\n\tds_read_b128 v[76:79], v1 offset:56576\n\tv_mfma_f32_16x16x32_bf16 v[176:179], a[160:163], v[112:115], v[176:179]\n\tds_read_b128 v[80:83], v1 offset:57088\n\tv_mfma_f32_16x16x32_bf16 v[180:183], a[160:163], v[116:119], v[180:183]\n\tds_read_b128 v[84:87], v1 offset:57600\n\tv_mfma_f32_16x16x32_bf16 v[184:187], a[160:163], v[120:123], v[184:187]\n\tds_read_b128 v[88:91], v1 offset:58112\n\t" \
    "v_mfma_f32_16x16x32_bf16 v[188:191], a[160:163], v[124:127], v[188:191]\n\tds_read_b128 v[92:95], v1 offset:58624\n\tv_mfma_f32_16x16x32_bf16 v[192:195], a[160:163], v[128:131], v[192:195]\n\tds_read_b128 v[96:99], v1 offset:59136\n\tv_mfma_f32_16x16x32_bf16 v[168:171], a[164:167], v[100:103], v[168:171]\n\tv_mfma_f32_16x16x32_bf16 v[172:175], a[164:167], v[104:107], v[172:175]\n\tv_mfma_f32_16x16x32_bf16 v[176:179], a[164:167], v[108:111], v[176:179]\n\tv_mfma_f32_16x16x32_bf16 v[180:183], a[164:167], v[112:115], v[180:183]\n\t" \
    "v_mfma_f32_16x16x32_bf16 v[184:187], a[164:167], v[116:119], v[184:187]\n\tv_mfma_f32_16x16x32_bf16 v[188:191], a[164:167], v[120:123], v[188:191]\n\tv_mfma_f32_16x16x32_bf16 v[192:195], a[164:167], v[124:127], v[192:195]\n\tv_mfma_f32_16x16x32_bf16 v[172:175], a[168:171], v[100:103], v[172:175]\n\tv_mfma_f32_16x16x32_bf16 v[176:179], a[168:171], v[104:107], v[176:179]\n\tv_mfma_f32_16x16x32_bf16 v[180:183], a[168:171], v[108:111], v[180:183]\n\tv_mfma_f32_16x16x32_bf16 v[184:187], a[168:171], v[112:115], v[184:187]\n\tv_mfma_f32_16x16x32_bf16 v[188:191], a[168:171], v[116:119], v[188:191]\n\t" \
    "v_mfma_f32_16x16x32_bf16 v[192:195], a[168:171], v[120:123], v[192:195]\n\tv_mfma_f32_16x16x32_bf16 v[176:179], a[172:175], v[100:103], v[176:179]\n\tv_mfma_f32_16x16x32_bf16 v[180:183], a[172:175], v[104:107], v[180:183]\n\tv_mfma_f32_16x16x32_bf16 v[184:187], a[172:175], v[108:111], v[184:187]\n\tv_mfma_f32_16x16x32_bf16 v[188:191], a[172:175], v[112:115], v[188:191]\n\tv_mfma_f32_16x16x32_bf16 v[192:195], a[172:175], v[116:119], v[192:195]\n\tv_mfma_f32_16x16x32_bf16 v[180:183], a[176:179], v[100:103], v[180:183]\n\tv_mfma_f32_16x16x32_bf16 v[184:187], a[176:179], v[104:107], v[184:187]\n\t" \
    "v_mfma_f32_16x16x32_bf16 v[188:191], a[176:179], v[108:111], v[188:191]\n\tv_mfma_f32_16x16x32_bf16 v[192:195], a[176:179], v[112:115], v[192:195]\n\tv_mfma_f32_16x16x32_bf16 v[184:187], a[180:183], v[100:103], v[184:187]\n\tv_mfma_f32_16x16x32_bf16 v[188:191], a[180:183], v[104:107], v[188:191]\n\tv_mfma_f32_16x16x32_bf16 v[192:195], a[180:183], v[108:111], v[192:195]\n\tv_mfma_f32_16x16x32_bf16 v[188:191], a[184:187], v[100:103], v[188:191]\n\tv_mfma_f32_16x16x32_bf16 v[192:195], a[184:187], v[104:107], v[192:195]\n\tv_mfma_f32_16x16x32_bf16 v[192:195], a[188:191], v[100:103], v[192:195]\n\t" \
    "s_waitcnt lgkmcnt(0)\n\tv_mfma_f32_16x16x32_bf16 v[164:167], a[192:195], v[68:71], v[164:167]\n\tds_read_b128 v[100:103], v1 offset:55488\n\tv_mfma_f32_16x16x32_bf16 v[168:171], a[192:195], v[72:75], v[168:171]\n\tds_read_b128 v[104:107], v1 offset:56000\n\tv_mfma_f32_16x16x32_bf16 v[172:175], a[192:195], v[76:79], v[172:175]\n\tds_read_b128 v[108:111], v1 offset:56512\n\tv_mfma_f32_16x16x32_bf16 v[176:179], a[192:195], v[80:83], v[176:179]\n\t" \
    "ds_read_b128 v[112:115], v1 offset:57024\n\tv_mfma_f32_16x16x32_bf16 v[180:183], a[192:195], v[84:87], v[180:183]\n\tds_read_b128 v[116:119], v1 offset:57536\n\tv_mfma_f32_16x16x32_bf16 v[184:187], a[192:195], v[88:91], v[184:187]\n\tds_read_b128 v[120:123], v1 offset:58048\n\tv_mfma_f32_16x16x32_bf16 v[188:191], a[192:195], v[92:95], v[188:191]\n\tds_read_b128 v[124:127], v1 offset:58560\n\tv_mfma_f32_16x16x32_bf16 v[192:195], a[192:195], v[96:99], v[192:195]\n\t" \
    "ds_read_b128 v[128:131], v1 offset:59072\n\tv_mfma_f32_16x16x32_bf16 v[168:171], a[196:199], v[68:71], v[168:171]\n\tv_mfma_f32_16x16x32_bf16 v[172:175], a[196:199], v[72:75], v[172:175]\n\tv_mfma_f32_16x16x32_bf16 v[176:179], a[196:199], v[76:79], v[176:179]\n\tv_mfma_f32_16x16x32_bf16 v[180:183], a[196:199], v[80:83], v[180:183]\n\tv_mfma_f32_16x16x32_bf16 v[184:187], a[196:199], v[84:87], v[184:187]\n\tv_mfma_f32_16x16x32_bf16 v[188:191], a[196:199], v[88:91], v[188:191]\n\tv_mfma_f32_16x16x32_bf16 v[192:195], a[196:199], v[92:95], v[192:195]\n\t" \
    "v_mfma_f32_16x16x32_bf16 v[172:175], a[200:203], v[68:71], v[172:175]\n\tv_mfma_f32_16x16x32_bf16 v[176:179], a[200:203], v[72:75], v[176:179]\n\tv_mfma_f32_16x16x32_bf16 v[180:183], a[200:203], v[76:79], v[180:183]\n\tv_mfma_f32_16x16x32_bf16 v[184:187], a[200:203], v[80:83], v[184:187]\n\tv_mfma_f32_16x16x32_bf16 v[188:191], a[200:203], v[84:87], v[188:191]\n\tv_mfma_f32_16x16x32_bf16 v[192:195], a[200:203], v[88:91], v[192:195]\n\tv_mfma_f32_16x16x32_bf16 v[176:179], a[204:207], v[68:71], v[176:179]\n\tv_mfma_f32_16x16x32_bf16 v[180:183], a[204:207], v[72:75], v[180:183]\n\t" \
    "v_mfma_f32_16x16x32_bf16 v[184:187], a[204:207], v[76:79], v[184:187]\n\tv_mfma_f32_16x16x32_bf16 v[188:191], a[204:207], v[80:83], v[188:191]\n\tv_mfma_f32_16x16x32_bf16 v[192:195], a[204:207], v[84:87], v[192:195]\n\tv_mfma_f32_16x16x32_bf16 v[180:183], a[208:211], v[68:71], v[180:183]\n\tv_mfma_f32_16x16x32_bf16 v[184:187], a[208:211], v[72:75], v[184:187]\n\tv_mfma_f32_16x16x32_bf16 v[188:191], a[208:211], v[76:79], v[188:191]\n\tv_mfma_f32_16x16x32_bf16 v[192:195], a[208:211], v[80:83], v[192:195]\n\tv_mfma_f32_16x16x32_bf16 v[184:187], a[212:215], v[68:71], v[184:187]\n\t" \
    "v_mfma_f32_16x16x32_bf16 v[188:191], a[212:215], v[72:75], v[188:191]\n\tv_mfma_f32_16x16x32_bf16 v[192:195], a[212:215], v[76:79], v[192:195]\n\tv_mfma_f32_16x16x32_bf16 v[188:191], a[216:219], v[68:71], v[188:191]\n\tv_mfma_f32_16x16x32_bf16 v[192:195], a[216:219], v[72:75], v[192:195]\n\tv_mfma_f32_16x16x32_bf16 v[192:195], a[220:223], v[68:71], v[192:195]\n\ts_waitcnt lgkmcnt(0)\n\tv_mfma_f32_16x16x32_bf16 v[164:167], a[224:227], v[100:103], v[164:167]\n\ts_waitcnt vmcnt(1)\n\t" \
    "buffer_store_dwordx2 v[248:249], off, s[8:11], s51 sc1\n\tv_mfma_f32_16x16x32_bf16 v[168:171], a[224:227], v[104:107], v[168:171]\n\tv_mfma_f32_16x16x32_bf16 v[172:175], a[224:227], v[108:111], v[172:175]\n\tv_mfma_f32_16x16x32_bf16 v[176:179], a[224:227], v[112:115], v[176:179]\n\tv_mfma_f32_16x16x32_bf16 v[180:183], a[224:227], v[116:119], v[180:183]\n\tv_mfma_f32_16x16x32_bf16 v[184:187], a[224:227], v[120:123], v[184:187]\n\tv_mfma_f32_16x16x32_bf16 v[188:191], a[224:227], v[124:127], v[188:191]\n\tv_mfma_f32_16x16x32_bf16 v[192:195], a[224:227], v[128:131], v[192:195]\n\t" \
    "v_mfma_f32_16x16x32_bf16 v[168:171], a[228:231], v[100:103], v[168:171]\n\tv_mfma_f32_16x16x32_bf16 v[172:175], a[228:231], v[104:107], v[172:175]\n\tv_mfma_f32_16x16x32_bf16 v[176:179], a[228:231], v[108:111], v[176:179]\n\tv_mfma_f32_16x16x32_bf16 v[180:183], a[228:231], v[112:115], v[180:183]\n\tv_mfma_f32_16x16x32_bf16 v[184:187], a[228:231], v[116:119], v[184:187]\n\tv_mfma_f32_16x16x32_bf16 v[188:191], a[228:231], v[120:123], v[188:191]\n\tv_mfma_f32_16x16x32_bf16 v[192:195], a[228:231], v[124:127], v[192:195]\n\tv_mfma_f32_16x16x32_bf16 v[172:175], a[232:235], v[100:103], v[172:175]\n\t" \
    "v_mfma_f32_16x16x32_bf16 v[176:179], a[232:235], v[104:107], v[176:179]\n\tv_mfma_f32_16x16x32_bf16 v[180:183], a[232:235], v[108:111], v[180:183]\n\tv_mfma_f32_16x16x32_bf16 v[184:187], a[232:235], v[112:115], v[184:187]\n\tv_mfma_f32_16x16x32_bf16 v[188:191], a[232:235], v[116:119], v[188:191]\n\tv_mfma_f32_16x16x32_bf16 v[192:195], a[232:235], v[120:123], v[192:195]\n\tv_mfma_f32_16x16x32_bf16 v[176:179], a[236:239], v[100:103], v[176:179]\n\tv_mfma_f32_16x16x32_bf16 v[180:183], a[236:239], v[104:107], v[180:183]\n\tv_mfma_f32_16x16x32_bf16 v[184:187], a[236:239], v[108:111], v[184:187]\n\t" \
    "v_mfma_f32_16x16x32_bf16 v[188:191], a[236:239], v[112:115], v[188:191]\n\tv_mfma_f32_16x16x32_bf16 v[192:195], a[236:239], v[116:119], v[192:195]\n\tv_mfma_f32_16x16x32_bf16 v[180:183], a[240:243], v[100:103], v[180:183]\n\tv_mfma_f32_16x16x32_bf16 v[184:187], a[240:243], v[104:107], v[184:187]\n\tv_mfma_f32_16x16x32_bf16 v[188:191], a[240:243], v[108:111], v[188:191]\n\tv_mfma_f32_16x16x32_bf16 v[192:195], a[240:243], v[112:115], v[192:195]\n\tv_mfma_f32_16x16x32_bf16 v[184:187], a[244:247], v[100:103], v[184:187]\n\tv_mfma_f32_16x16x32_bf16 v[188:191], a[244:247], v[104:107], v[188:191]\n\t" \
    "v_mfma_f32_16x16x32_bf16 v[192:195], a[244:247], v[108:111], v[192:195]\n\tv_mfma_f32_16x16x32_bf16 v[188:191], a[248:251], v[100:103], v[188:191]\n\tv_mfma_f32_16x16x32_bf16 v[192:195], a[248:251], v[104:107], v[192:195]\n\tv_mfma_f32_16x16x32_bf16 v[192:195], a[252:255], v[100:103], v[192:195]\n\tv_cvt_pk_bf16_f32 v164, v164, v165\n\tv_cvt_pk_bf16_f32 v165, v166, v167\n\tds_write_b64 v3, v[164:165] offset:55488\n\tv_cvt_pk_bf16_f32 v168, v168, v169\n\t" \
    "v_cvt_pk_bf16_f32 v169, v170, v171\n\tds_write_b64 v3, v[168:169] offset:56000\n\tv_cvt_pk_bf16_f32 v172, v172, v173\n\tv_cvt_pk_bf16_f32 v173, v174, v175\n\tds_write_b64 v3, v[172:173] offset:56512\n\tv_cvt_pk_bf16_f32 v176, v176, v177\n\tv_cvt_pk_bf16_f32 v177, v178, v179\n\tds_write_b64 v3, v[176:177] offset:57024\n\t" \
    "v_cvt_pk_bf16_f32 v180, v180, v181\n\tv_cvt_pk_bf16_f32 v181, v182, v183\n\tds_write_b64 v3, v[180:181] offset:57536\n\tv_cvt_pk_bf16_f32 v184, v184, v185\n\tv_cvt_pk_bf16_f32 v185, v186, v187\n\tds_write_b64 v3, v[184:185] offset:58048\n\tv_cvt_pk_bf16_f32 v188, v188, v189\n\tv_cvt_pk_bf16_f32 v189, v190, v191\n\t" \
    "ds_write_b64 v3, v[188:189] offset:58560\n\tv_cvt_pk_bf16_f32 v192, v192, v193\n\tv_cvt_pk_bf16_f32 v193, v194, v195\n\tds_write_b64 v3, v[192:193] offset:59072\n\ts_waitcnt lgkmcnt(0)\n\ts_barrier\n\ts_add_u32 s51, s44, 12\n\ts_mul_i32 s65, s51, 1366\n\t" \
    "s_lshr_b32 s65, s65, 16\n\ts_mul_i32 s56, s65, 48\n\ts_sub_u32 s56, s51, s56\n\ts_lshl_b32 s56, s56, 6\n\ts_lshr_b32 s70, s65, 2\n\ts_mul_i32 s70, s70, 252\n\ts_add_u32 s65, s65, s70\n\ts_mul_i32 s65, s65, 3072\n\t" \
    "s_add_u32 s56, s56, s65\n\ts_add_u32 s56, s56, 4849664\n\ts_add_u32 s51, s51, s38\n\ts_lshl_b32 s51, s51, 3\n\ts_add_u32 s51, s51, 50176\n\ts_or_b32 s56, s56, s45\n\ts_or_b32 s51, s51, s45\n\tds_read_b64_tr_b16 v[204:205], v4 offset:55488\n\t" \
    "ds_read_b64_tr_b16 v[206:207], v4 offset:55496\n\tds_read_b64_tr_b16 v[208:209], v4 offset:55552\n\tds_read_b64_tr_b16 v[210:211], v4 offset:55560\n\tds_read_b64_tr_b16 v[212:213], v4 offset:56000\n\tds_read_b64_tr_b16 v[214:215], v4 offset:56008\n\tds_read_b64_tr_b16 v[216:217], v4 offset:56064\n\tds_read_b64_tr_b16 v[218:219], v4 offset:56072\n\ts_waitcnt lgkmcnt(6)\n\t" \
    "buffer_store_dwordx4 v[204:207], v5, s[8:11], s56 offen sc1\n\ts_add_u32 s56, s56, 49152\n\ts_waitcnt lgkmcnt(4)\n\tbuffer_store_dwordx4 v[208:211], v5, s[8:11], s56 offen sc1\n\ts_add_u32 s56, s56, 49152\n\ts_waitcnt lgkmcnt(2)\n\tbuffer_store_dwordx4 v[212:215], v5, s[8:11], s56 offen sc1\n\ts_add_u32 s56, s56, 49152\n\t" \
    "s_waitcnt lgkmcnt(0)\n\tbuffer_store_dwordx4 v[216:219], v5, s[8:11], s56 offen sc1\n\ts_add_u32 s56, s56, 49152\n\ts_waitcnt vmcnt(0)\n\tbuffer_store_dwordx2 v[248:249], off, s[8:11], s51 sc1\n\ts_waitcnt vmcnt(0)\n\t103:\n\t98:\n\t" \
    "s_nop 1\n\t" \
    ""
#define MAIN_CLOB "s2", "s3", "s4", "s5", "s6", "s7", "s8", "s9", "s10", "s11", "s12", "s13", "s14", "s15", "s16", "s17", "s18", "s19", "s20", "s21", "s22", "s23", "s24", "s25", "s26", "s27", "s28", "s29", "s30", "s31", "s33", "s34", "s35", "s36", "s37", "s39", "s41", "s42", "s43", "s44", "s45", "s46", "s47", "s48", "s49", "s50", "s51", "s52", "s53", "s54", "s55", "s56", "s57", "s58", "s59", "s60", "s61", "s62", "s63", "s64", "s65", "s66", "s67", "s68", "s69", "s70", "s71", "s72", "s73", "s74", "s75", "s76", "s77", "s78", "s79", "s80", "s81", "s82", "s83", "s84", "s85", "s86", "s87", "v0", "v1", "v2", "v3", "v4", "v5", "v6", "v7", "v8", "v9", "v10", "v11", "v12", "v13", "v14", "v15", "v16", "v17", "v18", "v19", "v20", "v21", "v22", "v23", "v24", "v25", "v26", "v27", "v28", "v29", "v30", "v31", "v32", "v33", "v34", "v35", "v36", "v37", "v38", "v39", "v40", "v41", "v42", "v43", "v44", "v45", "v46", "v47", "v48", "v49", "v50", "v51", "v52", "v53", "v54", "v55", "v56", "v57", "v58", "v59", "v60", "v61", "v62", "v63", "v64", "v65", "v66", "v67", "v68", "v69", "v70", "v71", "v72", "v73", "v74", "v75", "v76", "v77", "v78", "v79", "v80", "v81", "v82", "v83", "v84", "v85", "v86", "v87", "v88", "v89", "v90", "v91", "v92", "v93", "v94", "v95", "v96", "v97", "v98", "v99", "v100", "v101", "v102", "v103", "v104", "v105", "v106", "v107", "v108", "v109", "v110", "v111", "v112", "v113", "v114", "v115", "v116", "v117", "v118", "v119", "v120", "v121", "v122", "v123", "v124", "v125", "v126", "v127", "v128", "v129", "v130", "v131", "v132", "v133", "v134", "v135", "v136", "v137", "v138", "v139", "v140", "v141", "v142", "v143", "v144", "v145", "v146", "v147", "v148", "v149", "v150", "v151", "v152", "v153", "v154", "v155", "v156", "v157", "v158", "v159", "v160", "v161", "v162", "v163", "v164", "v165", "v166", "v167", "v168", "v169", "v170", "v171", "v172", "v173", "v174", "v175", "v176", "v177", "v178", "v179", "v180", "v181", "v182", "v183", "v184", "v185", "v186", "v187", "v188", "v189", "v190", "v191", "v192", "v193", "v194", "v195", "v196", "v197", "v198", "v199", "v200", "v201", "v202", "v203", "v204", "v205", "v206", "v207", "v208", "v209", "v210", "v211", "v212", "v213", "v214", "v215", "v216", "v217", "v218", "v219", "v220", "v221", "v222", "v223", "v224", "v225", "v226", "v227", "v228", "v229", "v230", "v231", "v232", "v233", "v234", "v235", "v236", "v237", "v238", "v239", "v241", "v242", "v243", "v244", "v245", "v246", "v247", "v248", "v249", "v250", "v251", "v252", "v253", "a0", "a1", "a2", "a3", "a4", "a5", "a6", "a7", "a8", "a9", "a10", "a11", "a12", "a13", "a14", "a15", "a16", "a17", "a18", "a19", "a20", "a21", "a22", "a23", "a24", "a25", "a26", "a27", "a28", "a29", "a30", "a31", "a32", "a33", "a34", "a35", "a36", "a37", "a38", "a39", "a40", "a41", "a42", "a43", "a44", "a45", "a46", "a47", "a48", "a49", "a50", "a51", "a52", "a53", "a54", "a55", "a56", "a57", "a58", "a59", "a60", "a61", "a62", "a63", "a64", "a65", "a66", "a67", "a68", "a69", "a70", "a71", "a72", "a73", "a74", "a75", "a76", "a77", "a78", "a79", "a80", "a81", "a82", "a83", "a84", "a85", "a86", "a87", "a88", "a89", "a90", "a91", "a92", "a93", "a94", "a95", "a96", "a97", "a98", "a99", "a100", "a101", "a102", "a103", "a104", "a105", "a106", "a107", "a108", "a109", "a110", "a111", "a112", "a113", "a114", "a115", "a116", "a117", "a118", "a119", "a120", "a121", "a122", "a123", "a124", "a125", "a126", "a127", "a128", "a129", "a130", "a131", "a132", "a133", "a134", "a135", "a136", "a137", "a138", "a139", "a140", "a141", "a142", "a143", "a144", "a145", "a146", "a147", "a148", "a149", "a150", "a151", "a152", "a153", "a154", "a155", "a156", "a157", "a158", "a159", "a160", "a161", "a162", "a163", "a164", "a165", "a166", "a167", "a168", "a169", "a170", "a171", "a172", "a173", "a174", "a175", "a176", "a177", "a178", "a179", "a180", "a181", "a182", "a183", "a184", "a185", "a186", "a187", "a188", "a189", "a190", "a191", "a192", "a193", "a194", "a195", "a196", "a197", "a198", "a199", "a200", "a201", "a202", "a203", "a204", "a205", "a206", "a207", "a208", "a209", "a210", "a211", "a212", "a213", "a214", "a215", "a216", "a217", "a218", "a219", "a220", "a221", "a222", "a223", "a224", "a225", "a226", "a227", "a228", "a229", "a230", "a231", "a232", "a233", "a234", "a235", "a236", "a237", "a238", "a239", "a240", "a241", "a242", "a243", "a244", "a245", "a246", "a247", "a248", "a249", "a250", "a251", "a252", "a253", "a254", "a255"

#define ARG(i) const void* p##i, unsigned n##i, unsigned f##i
__global__ __launch_bounds__(256, 1) void fused_kernel(ARG(0), ARG(1), ARG(2), ARG(3), ARG(4), ARG(5), ARG(6), ARG(7), ARG(8), ARG(9), ARG(10),
                                                       ARG(11)) {
#define MAIN_OUT
    const int b = blockIdx.x;
    unsigned tid = threadIdx.x;
    u64 ka = (u64)__builtin_amdgcn_kernarg_segment_ptr();
    int w;
    asm volatile(MAIN_ASM
                 : "=&{s38}"(w), "+{s[0:1]}"(ka), "+{v240}"(tid) MAIN_OUT
                 : "{s40}"(b)
                 : "memory", "scc", "vcc", MAIN_CLOB);
}

extern "C" void kernel_launch(void* const* d_in, const int* in_sizes, int n_in, void* d_out, int out_size,
                              void* d_ws, size_t ws_size, hipStream_t stream) {
    (void)hipFuncSetAttribute((const void*)fused_kernel, hipFuncAttributeMaxDynamicSharedMemorySize, LDS_BYTES);
    const unsigned F = 0x20000u, NX = S_LEN * D_DIM * 2, NW = S_LEN * F_DIM * 2;
    fused_kernel<<<256, 256, LDS_BYTES, stream>>>(d_ws, WS_END, F, d_in[7], NW, F, d_in[9], NW, F, d_in[0], NX, F, d_out, NX, F, d_in[8], F_DIM * 2, F,
                                                  d_in[10], S_LEN * 2, F, d_in[1], NX, F, d_in[2], NX, F, d_in[3], D_DIM * 8, F, d_in[4], NX, F,
                                                  d_in[5], D_DIM * 2, F);
}
```

```cpp
#include <hip/hip_runtime.h>

typedef unsigned long long u64;

#define S_LEN 2048
#define D_DIM 576
#define F_DIM 4608
#define LDS_BYTES 163840

#define WS_END 29163520u
#define MAIN_ASM \
    "s_load_dwordx16 s[8:23], s[0:1], 0\n\ts_getpc_b64 s[2:3]\n\t99:\n\tv_lshrrev_b32 v20, 5, v240\n\tv_mul_u32_u24 v20, 1152, v20\n\tv_and_b32 v21, 31, v240\n\tv_lshl_add_u32 v22, v21, 2, v20\n\tv_lshl_add_u32 v21, v21, 4, v20\n\t" \
    "s_mul_i32 s68, s40, 9216\n\tv_lshrrev_b32 v245, 6, v240\n\tv_and_b32 v240, 63, v240\n\ts_and_b32 s2, s2, 0xffffff80\n\ts_mov_b32 s4, s2\n\ts_mov_b32 s5, s3\n\ts_mov_b32 s6, 98f-99b\n\ts_mov_b32 s7, 0x20000\n\t" \
    "v_lshl_add_u32 v16, v245, 16, v240\n\tv_lshlrev_b32 v16, 7, v16\n\ts_lshr_b32 s65, s40, 3\n\ts_lshl_b32 s73, s65, 13\n\tbuffer_load_dword v57, v16, s[4:7], s73 offen\n\ts_waitcnt lgkmcnt(0)\n\tbuffer_load_dwordx4 v[48:51], v21, s[20:23], s68 offen\n\tbuffer_load_dwordx4 v[52:55], v21, s[20:23], s68 offen offset:512\n\t" \
    "buffer_load_dword v56, v22, s[20:23], s68 offen offset:1024\n\tbuffer_load_dwordx2 v[58:59], off, s[8:11], 0 sc1\n\ts_load_dwordx8 s[76:83], s[0:1], 112\n\ts_load_dwordx4 s[84:87], s[0:1], 144\n\ts_load_dwordx8 s[24:31], s[0:1], 64\n\ts_load_dwordx4 s[32:35], s[0:1], 96\n\tv_readfirstlane_b32 s38, v245\n\tv_lshlrev_b32 v8, 4, v240\n\t" \
    "v_and_b32 v244, 0xf0, v8\n\tv_lshrrev_b32 v245, 4, v240\n\ts_and_b32 s57, s40, 1\n\ts_and_b32 s58, s38, 1\n\ts_cmp_lt_u32 s65, 27\n\ts_cselect_b32 s39, 1, 0\n\ts_cmp_lt_u32 s65, 24\n\ts_cselect_b32 s39, 2, s39\n\t" \
    "s_min_u32 s65, s65, 26\n\ts_mul_i32 s42, s65, 43\n\ts_lshr_b32 s42, s42, 7\n\ts_mul_i32 s66, s42, 3\n\ts_sub_u32 s41, s65, s66\n\ts_lshl_b32 s69, s42, 8\n\ts_bfe_u32 s66, s40, 0x20001\n\ts_mul_i32 s66, s66, 9\n\t" \
    "s_add_u32 s42, s42, s66\n\ts_mul_i32 s43, s41, 903\n\ts_lshr_b32 s43, s43, 7\n\ts_sub_u32 s70, s43, 21\n\ts_mul_i32 s67, s57, 9437184\n\ts_lshl_b32 s66, s66, 8\n\ts_add_u32 s67, s67, s66\n\ts_lshl_b32 s65, s65, 2\n\t" \
    "s_add_u32 s65, s65, s38\n\ts_lshl_b32 s65, s65, 6\n\tv_add_u32 v252, s65, v240\n\tv_mul_u32_u24 v253, 3641, v252\n\tv_lshrrev_b32 v253, 16, v253\n\tv_mul_u32_u24 v253, 54, v253\n\tv_add_lshl_u32 v252, v252, v253, 7\n\tv_lshrrev_b32 v16, 3, v240\n\t" \
    "s_lshl_b32 s73, s38, 3\n\tv_add_u32 v16, s73, v16\n\tv_mul_u32_u24 v16, 9216, v16\n\tv_and_b32 v17, 0x70, v8\n\tv_and_b32 v18, 16, v240\n\tv_lshlrev_b32 v18, 1, v18\n\tv_xor_b32 v17, v17, v18\n\ts_lshl_b32 s73, s57, 1\n\t" \
    "s_add_u32 s73, s73, s58\n\ts_lshl_b32 s73, s73, 6\n\tv_xor_b32 v17, s73, v17\n\tv_add3_u32 v11, v16, v17, s69\n\tv_xor_b32 v10, 128, v11\n\ts_lshl_b32 s54, s38, 10\n\tv_add_u32 v9, 4096, v8\n\tv_lshlrev_b32 v239, 3, v240\n\t" \
    "s_mul_i32 s73, s40, 9216\n\ts_add_u32 s73, s73, s54\n\tv_add_u32 v23, s73, v8\n\tv_subrev_u32 v19, 3456, v23\n\ts_add_u32 s12, s12, s67\n\ts_addc_u32 s13, s13, 0\n\ts_sub_u32 s14, s14, s67\n\ts_cmp_eq_u32 s39, 0\n\t" \
    "s_cselect_b32 s14, 0, s14\n\tv_and_b32 v88, 31, v240\n\tv_lshrrev_b32 v89, 5, v240\n\ts_lshl_b32 s73, s38, 1\n\tv_add_u32 v89, s73, v89\n\tv_mul_u32_u24 v90, 1168, v89\n\tv_add_u32 v90, 153600, v90\n\tv_lshl_add_u32 v91, v88, 2, v90\n\t" \
    "v_lshl_add_u32 v90, v88, 4, v90\n\tv_cmp_eq_u32 vcc, 0, v88\n\tv_lshlrev_b32 v89, 3, v89\n\tv_mov_b32 v92, 0x7ff00000\n\tv_cndmask_b32 v89, v92, v89, vcc\n\tv_bfe_u32 v88, v240, 2, 2\n\tv_and_b32 v92, 3, v240\n\tv_lshlrev_b32 v92, 3, v92\n\t" \
    "v_lshl_add_u32 v92, v245, 5, v92\n\ts_lshl_b32 s73, s38, 7\n\tv_add_u32 v93, s73, v92\n\tv_mul_u32_u24 v94, 1168, v88\n\tv_add_u32 v94, 153600, v94\n\tv_add_u32 v95, v94, v92\n\tv_add_u32 v94, v94, v93\n\tv_mul_u32_u24 v88, 1152, v88\n\t" \
    "v_add_u32 v88, 131072, v88\n\tv_add_u32 v92, v88, v92\n\tv_add_u32 v93, v88, v93\n\tv_lshl_add_u32 v88, v245, 10, v244\n\ts_waitcnt lgkmcnt(0)\n\ts_lshr_b32 s61, s40, 2\n\ts_mul_i32 s61, s61, 36864\n\ts_and_b32 s65, s40, 3\n\t" \
    "s_lshl_b32 s65, s65, 8\n\ts_add_u32 s61, s61, s65\n\ts_add_u32 s61, s61, 131072\n\ts_mul_i32 s62, s40, 9216\n\ts_add_u32 s62, s62, 2490368\n\ts_lshl_b32 s63, s40, 3\n\ts_lshl_b32 s48, s38, 11\n\ts_add_u32 s48, s48, s63\n\t" \
    "s_add_u32 s48, s48, 17408\n\ts_add_u32 s63, s63, 1024\n\ts_cmp_eq_u32 s38, 0\n\ts_cselect_b32 s63, s63, 2146435072\n\ts_lshl_b32 s64, s40, 6\n\ts_add_u32 s64, s64, 58368\n\ts_add_u32 m0, s54, 131072\n\ts_nop 0\n\t" \
    "buffer_load_dwordx4 v19, s[76:79], 0 offen lds\n\ts_and_b32 s4, s40, 7\n\ts_lshl_b32 s47, s4, 11\n\ts_lshl_b32 s65, s57, 10\n\ts_add_u32 s47, s47, s65\n\tv_add_u32 v19, 4096, v19\n\ts_add_u32 m0, s54, 135168\n\ts_nop 0\n\t" \
    "buffer_load_dwordx4 v19, s[76:79], 0 offen lds\n\ts_add_u32 s47, s47, 1024\n\ts_mul_i32 s44, s57, 1179648\n\ts_mul_i32 s65, s41, 12288\n\ts_add_u32 s44, s44, s65\n\tv_add_u32 v19, 4096, v19\n\ts_add_u32 m0, s54, 139264\n\ts_nop 0\n\t" \
    "buffer_load_dwordx4 v19, s[76:79], 0 offen lds\n\ts_mul_i32 s65, s38, 3072\n\ts_add_u32 s44, s44, s65\n\ts_add_u32 s44, s44, 131072\n\ts_lshr_b32 s5, s4, 1\n\tv_add_u32 v19, 4096, v19\n\ts_add_u32 m0, s54, 144384\n\ts_nop 0\n\t" \
    "buffer_load_dwordx4 v23, s[80:83], 0 offen lds\n\tv_bfe_u32 v16, v240, 2, 2\n\tv_and_b32 v17, 3, v240\n\tv_lshlrev_b32 v17, 3, v17\n\tv_lshl_add_u32 v16, v16, 7, v17\n\tv_add_u32 v23, 4096, v23\n\ts_add_u32 m0, s54, 148480\n\ts_nop 0\n\t" \
    "buffer_load_dwordx4 v23, s[80:83], 0 offen lds\n\tv_bfe_u32 v17, v240, 3, 2\n\tv_lshl_add_u32 v16, v17, 5, v16\n\tv_lshl_add_u32 v0, v245, 10, v16\n\ts_and_b32 s65, s5, 1\n\tv_add_u32 v23, 4096, v23\n\tv_subrev_u32 v19, s54, v19\n\tv_subrev_u32 v23, s54, v23\n\t" \
    "s_cmp_eq_u32 s58, 1\n\ts_cselect_b64 vcc, -1, 0\n\ts_cselect_b64 s[76:77], s[80:81], s[76:77]\n\ts_mov_b32 m0, 143360\n\ts_cselect_b32 m0, 152576, m0\n\tv_cndmask_b32 v19, v19, v23, vcc\n\tbuffer_load_dwordx4 v19, s[76:79], 0 offen lds\n\ts_xor_b32 s65, s65, 1\n\t" \
    "s_lshl_b32 s65, s65, 6\n\tv_xor_b32 v17, s65, v16\n\tv_lshl_add_u32 v4, v245, 9, v17\n\ts_lshr_b32 s71, s54, 1\n\tbuffer_load_dwordx2 v[60:61], v239, s[84:87], s71 offen\n\tv_xor_b32 v1, 32, v0\n\tv_xor_b32 v5, 32, v4\n\tv_xor_b32 v2, 64, v0\n\t" \
    "v_xor_b32 v6, 64, v4\n\tbuffer_load_dwordx2 v[62:63], v239, s[84:87], s71 offen offset:2048\n\tv_xor_b32 v3, 96, v0\n\tv_xor_b32 v7, 96, v4\n\tv_add_u32 v224, 0x10000, v4\n\tv_add_u32 v14, 0x10000, v0\n\ts_mov_b32 s71, 4096\n\tbuffer_load_dwordx2 v[64:65], v239, s[84:87], s71 offen\n\t" \
    "v_add_u32 v220, 0x20000, v4\n\tv_add_u32 v225, 0x10000, v5\n\tv_add_u32 v15, 0x10000, v1\n\tv_add_u32 v221, 0x20000, v5\n\ts_cmp_ge_u32 s43, 21\n\ts_cselect_b32 s69, s70, s43\n\ts_mul_i32 s69, s69, 294912\n\ts_add_u32 s69, s69, 0\n\t" \
    "s_add_u32 m0, s54, 0\n\ts_nop 0\n\tbuffer_load_dwordx4 v10, s[12:15], s69 offen lds\n\tv_add_u32 v226, 0x10000, v6\n\tv_add_u32 v237, 0x10000, v2\n\tv_add_u32 v222, 0x20000, v6\n\tv_add_u32 v227, 0x10000, v7\n\ts_cmp_ge_u32 s43, 21\n\t" \
    "s_cselect_b32 s69, s70, s43\n\ts_mul_i32 s69, s69, 294912\n\ts_add_u32 s69, s69, 0\n\ts_add_u32 m0, s54, 4096\n\ts_nop 0\n\tbuffer_load_dwordx4 v11, s[12:15], s69 offen lds\n\tv_add_u32 v238, 0x10000, v3\n\tv_add_u32 v223, 0x20000, v7\n\t" \
    "s_lshl_b32 s72, s40, 2\n\ts_add_u32 s72, s72, s38\n\ts_waitcnt vmcnt(12)\n\ts_load_dwordx8 s[76:83], s[0:1], 160\n\tv_lshlrev_b32 v24, 16, v48\n\tv_and_b32 v25, 0xffff0000, v48\n\tv_lshlrev_b32 v26, 16, v49\n\tv_and_b32 v27, 0xffff0000, v49\n\t" \
    "v_lshlrev_b32 v28, 16, v50\n\tv_and_b32 v29, 0xffff0000, v50\n\tv_lshlrev_b32 v30, 16, v51\n\tv_and_b32 v31, 0xffff0000, v51\n\tv_lshlrev_b32 v32, 16, v52\n\tv_and_b32 v33, 0xffff0000, v52\n\tv_lshlrev_b32 v34, 16, v53\n\tv_and_b32 v35, 0xffff0000, v53\n\t" \
    "v_lshlrev_b32 v36, 16, v54\n\tv_and_b32 v37, 0xffff0000, v54\n\tv_lshlrev_b32 v38, 16, v55\n\tv_and_b32 v39, 0xffff0000, v55\n\tv_lshlrev_b32 v40, 16, v56\n\tv_and_b32 v41, 0xffff0000, v56\n\tv_pk_add_f32 v[70:71], v[24:25], v[26:27]\n\tv_pk_add_f32 v[72:73], v[28:29], v[30:31]\n\t" \
    "v_pk_add_f32 v[74:75], v[32:33], v[34:35]\n\tv_pk_add_f32 v[76:77], v[36:37], v[38:39]\n\tv_pk_add_f32 v[70:71], v[70:71], v[72:73]\n\tv_pk_add_f32 v[74:75], v[74:75], v[76:77]\n\tv_pk_add_f32 v[70:71], v[70:71], v[40:41]\n\tv_pk_add_f32 v[70:71], v[70:71], v[74:75]\n\tv_add_f32 v66, v70, v71\n\ts_nop 1\n\t" \
    "v_add_f32_dpp v66, v66, v66 row_ror:8 row_mask:0xf bank_mask:0xf\n\ts_nop 1\n\tv_add_f32_dpp v66, v66, v66 row_ror:4 row_mask:0xf bank_mask:0xf\n\ts_nop 1\n\tv_add_f32_dpp v66, v66, v66 row_ror:2 row_mask:0xf bank_mask:0xf\n\ts_nop 1\n\tv_add_f32_dpp v66, v66, v66 row_ror:1 row_mask:0xf bank_mask:0xf\n\tv_mov_b32 v67, v66\n\t" \
    "s_nop 1\n\tv_permlane16_swap_b32 v66, v67\n\tv_add_f32 v66, v66, v67\n\tv_mul_f32 v66, 0x3ae38e39, v66\n\tv_pk_add_f32 v[70:71], v[24:25], v[66:67] op_sel_hi:[1,0] neg_lo:[0,1] neg_hi:[0,1]\n\tv_pk_add_f32 v[72:73], v[26:27], v[66:67] op_sel_hi:[1,0] neg_lo:[0,1] neg_hi:[0,1]\n\tv_pk_add_f32 v[74:75], v[28:29], v[66:67] op_sel_hi:[1,0] neg_lo:[0,1] neg_hi:[0,1]\n\tv_pk_add_f32 v[76:77], v[30:31], v[66:67] op_sel_hi:[1,0] neg_lo:[0,1] neg_hi:[0,1]\n\t" \
    "v_pk_add_f32 v[78:79], v[32:33], v[66:67] op_sel_hi:[1,0] neg_lo:[0,1] neg_hi:[0,1]\n\tv_pk_add_f32 v[80:81], v[34:35], v[66:67] op_sel_hi:[1,0] neg_lo:[0,1] neg_hi:[0,1]\n\tv_pk_add_f32 v[82:83], v[36:37], v[66:67] op_sel_hi:[1,0] neg_lo:[0,1] neg_hi:[0,1]\n\tv_pk_add_f32 v[84:85], v[38:39], v[66:67] op_sel_hi:[1,0] neg_lo:[0,1] neg_hi:[0,1]\n\tv_pk_add_f32 v[86:87], v[40:41], v[66:67] op_sel_hi:[1,0] neg_lo:[0,1] neg_hi:[0,1]\n\tv_pk_mul_f32 v[44:45], v[70:71], v[70:71]\n\tv_pk_mul_f32 v[46:47], v[72:73], v[72:73]\n\tv_pk_fma_f32 v[44:45], v[74:75], v[74:75], v[44:45]\n\t" \
    "v_pk_fma_f32 v[46:47], v[76:77], v[76:77], v[46:47]\n\tv_pk_fma_f32 v[44:45], v[78:79], v[78:79], v[44:45]\n\tv_pk_fma_f32 v[46:47], v[80:81], v[80:81], v[46:47]\n\tv_pk_fma_f32 v[44:45], v[82:83], v[82:83], v[44:45]\n\tv_pk_fma_f32 v[46:47], v[84:85], v[84:85], v[46:47]\n\tv_pk_fma_f32 v[44:45], v[86:87], v[86:87], v[44:45]\n\tv_pk_add_f32 v[44:45], v[44:45], v[46:47]\n\tv_add_f32 v68, v44, v45\n\t" \
    "s_nop 1\n\tv_add_f32_dpp v68, v68, v68 row_ror:8 row_mask:0xf bank_mask:0xf\n\ts_nop 1\n\tv_add_f32_dpp v68, v68, v68 row_ror:4 row_mask:0xf bank_mask:0xf\n\ts_nop 1\n\tv_add_f32_dpp v68, v68, v68 row_ror:2 row_mask:0xf bank_mask:0xf\n\ts_nop 1\n\tv_add_f32_dpp v68, v68, v68 row_ror:1 row_mask:0xf bank_mask:0xf\n\t" \
    "v_mov_b32 v69, v68\n\ts_nop 1\n\tv_permlane16_swap_b32 v68, v69\n\tv_add_f32 v68, v68, v69\n\tv_mul_f32 v68, 0x3ae38e39, v68\n\tv_cvt_pk_bf16_f32 v68, v68, v68\n\tv_lshlrev_b32 v68, 16, v68\n\tv_add_f32 v68, 0x3727c5ac, v68\n\t" \
    "v_cvt_pk_bf16_f32 v68, v68, v68\n\tv_lshlrev_b32 v68, 16, v68\n\tv_sqrt_f32 v68, v68\n\tv_cvt_pk_bf16_f32 v66, v66, v66\n\tv_lshlrev_b32 v66, 16, v66\n\tv_cvt_pk_bf16_f32 v68, v68, v68\n\tv_lshlrev_b32 v68, 16, v68\n\tv_rcp_f32 v68, v68\n\t" \
    "s_nop 0\n\tv_mul_f32 v69, v66, v68\n\tv_xor_b32 v69, 0x80000000, v69\n\tv_pk_fma_f32 v[24:25], v[24:25], v[68:69], v[68:69] op_sel:[0,0,1] op_sel_hi:[1,0,1]\n\tv_pk_fma_f32 v[26:27], v[26:27], v[68:69], v[68:69] op_sel:[0,0,1] op_sel_hi:[1,0,1]\n\tv_pk_fma_f32 v[28:29], v[28:29], v[68:69], v[68:69] op_sel:[0,0,1] op_sel_hi:[1,0,1]\n\tv_pk_fma_f32 v[30:31], v[30:31], v[68:69], v[68:69] op_sel:[0,0,1] op_sel_hi:[1,0,1]\n\tv_pk_fma_f32 v[32:33], v[32:33], v[68:69], v[68:69] op_sel:[0,0,1] op_sel_hi:[1,0,1]\n\t" \
    "v_pk_fma_f32 v[34:35], v[34:35], v[68:69], v[68:69] op_sel:[0,0,1] op_sel_hi:[1,0,1]\n\tv_pk_fma_f32 v[36:37], v[36:37], v[68:69], v[68:69] op_sel:[0,0,1] op_sel_hi:[1,0,1]\n\tv_pk_fma_f32 v[38:39], v[38:39], v[68:69], v[68:69] op_sel:[0,0,1] op_sel_hi:[1,0,1]\n\tv_pk_fma_f32 v[40:41], v[40:41], v[68:69], v[68:69] op_sel:[0,0,1] op_sel_hi:[1,0,1]\n\tv_cvt_pk_bf16_f32 v48, v24, v25\n\tv_cvt_pk_bf16_f32 v49, v26, v27\n\tv_cvt_pk_bf16_f32 v50, v28, v29\n\tv_cvt_pk_bf16_f32 v51, v30, v31\n\t" \
    "v_cvt_pk_bf16_f32 v52, v32, v33\n\tv_cvt_pk_bf16_f32 v53, v34, v35\n\tv_cvt_pk_bf16_f32 v54, v36, v37\n\tv_cvt_pk_bf16_f32 v55, v38, v39\n\tv_cvt_pk_bf16_f32 v56, v40, v41\n\tv_lshlrev_b32 v24, 16, v48\n\tv_and_b32 v25, 0xffff0000, v48\n\tv_lshlrev_b32 v26, 16, v49\n\t" \
    "v_and_b32 v27, 0xffff0000, v49\n\tv_lshlrev_b32 v28, 16, v50\n\tv_and_b32 v29, 0xffff0000, v50\n\tv_lshlrev_b32 v30, 16, v51\n\tv_and_b32 v31, 0xffff0000, v51\n\tv_lshlrev_b32 v32, 16, v52\n\tv_and_b32 v33, 0xffff0000, v52\n\tv_lshlrev_b32 v34, 16, v53\n\t" \
    "v_and_b32 v35, 0xffff0000, v53\n\tv_lshlrev_b32 v36, 16, v54\n\tv_and_b32 v37, 0xffff0000, v54\n\tv_lshlrev_b32 v38, 16, v55\n\tv_and_b32 v39, 0xffff0000, v55\n\tv_lshlrev_b32 v40, 16, v56\n\tv_and_b32 v41, 0xffff0000, v56\n\tv_pk_add_f32 v[70:71], v[24:25], v[26:27]\n\t" \
    "v_pk_add_f32 v[72:73], v[28:29], v[30:31]\n\tv_pk_add_f32 v[74:75], v[32:33], v[34:35]\n\tv_pk_add_f32 v[76:77], v[36:37], v[38:39]\n\tv_pk_mul_f32 v[44:45], v[24:25], v[24:25]\n\tv_pk_mul_f32 v[46:47], v[26:27], v[26:27]\n\tv_pk_add_f32 v[70:71], v[70:71], v[72:73]\n\tv_pk_add_f32 v[74:75], v[74:75], v[76:77]\n\tv_pk_fma_f32 v[44:45], v[28:29], v[28:29], v[44:45]\n\t" \
    "v_pk_fma_f32 v[46:47], v[30:31], v[30:31], v[46:47]\n\tv_pk_add_f32 v[70:71], v[70:71], v[40:41]\n\tv_pk_fma_f32 v[44:45], v[32:33], v[32:33], v[44:45]\n\tv_pk_fma_f32 v[46:47], v[34:35], v[34:35], v[46:47]\n\tv_pk_add_f32 v[70:71], v[70:71], v[74:75]\n\tv_pk_fma_f32 v[44:45], v[36:37], v[36:37], v[44:45]\n\tv_pk_fma_f32 v[46:47], v[38:39], v[38:39], v[46:47]\n\tv_pk_fma_f32 v[44:45], v[40:41], v[40:41], v[44:45]\n\t" \
    "v_pk_add_f32 v[44:45], v[44:45], v[46:47]\n\tv_add_f32 v66, v70, v71\n\tv_add_f32 v42, v44, v45\n\ts_nop 0\n\tv_add_f32_dpp v66, v66, v66 row_ror:8 row_mask:0xf bank_mask:0xf\n\tv_add_f32_dpp v42, v42, v42 row_ror:8 row_mask:0xf bank_mask:0xf\n\ts_nop 0\n\tv_add_f32_dpp v66, v66, v66 row_ror:4 row_mask:0xf bank_mask:0xf\n\t" \
    "v_add_f32_dpp v42, v42, v42 row_ror:4 row_mask:0xf bank_mask:0xf\n\ts_nop 0\n\tv_add_f32_dpp v66, v66, v66 row_ror:2 row_mask:0xf bank_mask:0xf\n\tv_add_f32_dpp v42, v42, v42 row_ror:2 row_mask:0xf bank_mask:0xf\n\ts_nop 0\n\tv_add_f32_dpp v66, v66, v66 row_ror:1 row_mask:0xf bank_mask:0xf\n\tv_add_f32_dpp v42, v42, v42 row_ror:1 row_mask:0xf bank_mask:0xf\n\tv_mov_b32 v67, v66\n\t" \
    "v_mov_b32 v43, v42\n\ts_nop 0\n\tv_permlane16_swap_b32 v66, v67\n\tv_permlane16_swap_b32 v42, v43\n\tv_add_f32 v66, v66, v67\n\tv_add_f32 v42, v42, v43\n\tv_mul_f32 v66, 0x3ae38e39, v66\n\tv_mul_f32 v42, 0x3ae38e39, v42\n\t" \
    "v_fma_f32 v42, -v66, v66, v42\n\tv_max_f32 v42, 0, v42\n\tv_cvt_pk_bf16_f32 v42, v42, v42\n\tv_lshlrev_b32 v42, 16, v42\n\tv_add_f32 v42, 0x3727c5ac, v42\n\tv_cvt_pk_bf16_f32 v42, v42, v42\n\tv_lshlrev_b32 v42, 16, v42\n\tv_sqrt_f32 v42, v42\n\t" \
    "v_cvt_pk_bf16_f32 v66, v66, v66\n\tv_lshlrev_b32 v66, 16, v66\n\tv_cvt_pk_bf16_f32 v42, v42, v42\n\tv_lshlrev_b32 v42, 16, v42\n\tv_rcp_f32 v42, v42\n\ts_nop 0\n\tv_mul_f32 v43, v66, v42\n\tv_xor_b32 v43, 0x80000000, v43\n\t" \
    "v_pk_fma_f32 v[24:25], v[24:25], v[42:43], v[42:43] op_sel:[0,0,1] op_sel_hi:[1,0,1]\n\tv_pk_fma_f32 v[26:27], v[26:27], v[42:43], v[42:43] op_sel:[0,0,1] op_sel_hi:[1,0,1]\n\tv_pk_fma_f32 v[28:29], v[28:29], v[42:43], v[42:43] op_sel:[0,0,1] op_sel_hi:[1,0,1]\n\tv_pk_fma_f32 v[30:31], v[30:31], v[42:43], v[42:43] op_sel:[0,0,1] op_sel_hi:[1,0,1]\n\tv_pk_fma_f32 v[32:33], v[32:33], v[42:43], v[42:43] op_sel:[0,0,1] op_sel_hi:[1,0,1]\n\tv_pk_fma_f32 v[34:35], v[34:35], v[42:43], v[42:43] op_sel:[0,0,1] op_sel_hi:[1,0,1]\n\tv_pk_fma_f32 v[36:37], v[36:37], v[42:43], v[42:43] op_sel:[0,0,1] op_sel_hi:[1,0,1]\n\tv_pk_fma_f32 v[38:39], v[38:39], v[42:43], v[42:43] op_sel:[0,0,1] op_sel_hi:[1,0,1]\n\t" \
    "v_pk_fma_f32 v[40:41], v[40:41], v[42:43], v[42:43] op_sel:[0,0,1] op_sel_hi:[1,0,1]\n\tv_cvt_pk_bf16_f32 v48, v24, v25\n\tv_cvt_pk_bf16_f32 v49, v26, v27\n\tv_cvt_pk_bf16_f32 v50, v28, v29\n\tv_cvt_pk_bf16_f32 v51, v30, v31\n\tv_cvt_pk_bf16_f32 v52, v32, v33\n\tv_cvt_pk_bf16_f32 v53, v34, v35\n\tv_cvt_pk_bf16_f32 v54, v36, v37\n\t" \
    "v_cvt_pk_bf16_f32 v55, v38, v39\n\tv_cvt_pk_bf16_f32 v56, v40, v41\n\tds_write_b128 v90, v[48:51]\n\tds_write_b128 v90, v[52:55] offset:512\n\tds_write_b32 v91, v56 offset:1024\n\ts_waitcnt vmcnt(11) lgkmcnt(0)\n\ts_barrier\n\tds_read_b64_tr_b16 v[24:25], v94 offset:0\n\t" \
    "ds_read_b64_tr_b16 v[26:27], v94 offset:4672\n\tds_read_b64_tr_b16 v[28:29], v94 offset:512\n\tds_read_b64_tr_b16 v[30:31], v94 offset:5184\n\tds_read_b64_tr_b16 v[32:33], v95 offset:1024\n\tds_read_b64_tr_b16 v[34:35], v95 offset:5696\n\tbuffer_store_dwordx2 v[68:69], v89, s[8:11], s64 offen sc1\n\tv_readfirstlane_b32 s36, v58\n\tv_readfirstlane_b32 s37, v59\n\t" \
    "s_lshl_b32 s73, s38, 12\n\ts_add_u32 s68, s61, s73\n\ts_add_u32 s36, s36, 0x7f4a7c15\n\ts_addc_u32 s37, s37, 0x9e3779b9\n\ts_waitcnt lgkmcnt(4)\n\tbuffer_store_dwordx4 v[24:27], v88, s[8:11], s68 offen sc1\n\ts_add_u32 s68, s68, 16384\n\ts_waitcnt lgkmcnt(2)\n\t" \
    "buffer_store_dwordx4 v[28:31], v88, s[8:11], s68 offen sc1\n\ts_add_u32 s68, s61, 32768\n\ts_cmp_eq_u32 s38, 0\n\ts_cselect_b32 s68, s68, 0x7ff00000\n\ts_waitcnt lgkmcnt(0)\n\tbuffer_store_dwordx4 v[32:35], v88, s[8:11], s68 offen sc1\n\tv_mov_b64 v[248:249], s[36:37]\n\tv_and_b32 v89, 7, v240\n\t" \
    "v_lshlrev_b32 v89, 11, v89\n\ts_cmp_ge_u32 s43, 20\n\ts_cselect_b32 s69, s70, s43\n\ts_mul_i32 s69, s69, 294912\n\ts_add_u32 s69, s69, 294912\n\ts_add_u32 m0, s54, 8192\n\ts_nop 0\n\tbuffer_load_dwordx4 v10, s[12:15], s69 offen lds\n\t" \
    "s_xor_b32 s52, s72, 4\n\ts_mul_i32 s72, s72, 6656\n\ts_add_u32 s72, s72, 22347776\n\ts_mul_i32 s52, s52, 6656\n\ts_add_u32 s52, s52, 22347776\n\tv_mov_b32 v246, 0xbdd2d3e8\n\tv_mov_b32 v247, 0xc0135761\n\ts_lshr_b32 s65, s40, 3\n\t" \
    "s_mul_i32 s66, s65, 43\n\ts_lshr_b32 s66, s66, 7\n\ts_mul_i32 s6, s66, 3\n\ts_sub_u32 s6, s65, s6\n\ts_and_b32 s66, s66, 7\n\ts_and_b32 s7, s4, 1\n\ts_lshl_b32 s7, s7, 3\n\ts_add_u32 s7, s7, s66\n\t" \
    "s_mul_i32 s65, s5, 4718592\n\ts_lshl_b32 s67, s7, 8\n\ts_add_u32 s65, s65, s67\n\ts_add_u32 s16, s16, s65\n\ts_addc_u32 s17, s17, 0\n\ts_sub_u32 s18, s18, s65\n\ts_cmp_eq_u32 s39, 2\n\ts_cselect_b32 s18, s18, 0\n\t" \
    "v_lshrrev_b32 v16, 3, v240\n\ts_lshl_b32 s65, s38, 3\n\tv_add_u32 v16, s65, v16\n\tv_and_b32 v17, 0x70, v8\n\tv_lshlrev_b32 v18, 5, v245\n\tv_xor_b32 v17, v17, v18\n\tv_lshl_add_u32 v16, v16, 12, v17\n\ts_lshr_b32 s65, s5, 1\n\t" \
    "s_cmp_ge_u32 s43, 20\n\ts_cselect_b32 s69, s70, s43\n\ts_mul_i32 s69, s69, 294912\n\ts_add_u32 s69, s69, 294912\n\ts_add_u32 m0, s54, 12288\n\ts_nop 0\n\tbuffer_load_dwordx4 v11, s[12:15], s69 offen lds\n\ts_lshl_b32 s65, s65, 7\n\t" \
    "v_add_u32 v13, s65, v16\n\tv_xor_b32 v12, 128, v13\n\ts_lshl_b32 s65, s7, 7\n\ts_lshl_b32 s66, s5, 5\n\ts_add_u32 s65, s65, s66\n\ts_lshl_b32 s59, s65, 3\n\ts_add_u32 s59, s59, 58368\n\ts_lshl_b32 s66, s65, 1\n\t" \
    "s_add_u32 s32, s32, s66\n\ts_addc_u32 s33, s33, 0\n\ts_sub_u32 s34, s34, s66\n\ts_lshl_b32 s66, s6, 2\n\ts_add_u32 s66, s66, s38\n\ts_mul_i32 s67, s66, 96\n\ts_mul_i32 s65, s65, 1152\n\ts_add_u32 s65, s65, s67\n\t" \
    "s_add_u32 s24, s24, s65\n\ts_addc_u32 s25, s25, 0\n\ts_sub_u32 s26, s26, s65\n\ts_add_u32 s20, s20, s65\n\ts_addc_u32 s21, s21, 0\n\ts_sub_u32 s22, s22, s65\n\ts_lshl_b32 s51, s67, 2\n\ts_add_u32 s51, s51, 50176\n\t" \
    "s_lshl_b32 s66, s66, 2\n\ts_add_u32 s66, s66, s5\n\ts_mul_i32 s46, s66, 110592\n\ts_add_u32 s46, s46, 16646144\n\ts_lshl_b32 s50, s66, 13\n\ts_add_u32 s50, s50, 21954560\n\ts_mul_i32 s65, s5, 9\n\ts_sub_u32 s65, s42, s65\n\t" \
    "s_cmp_ge_u32 s43, 19\n\ts_cselect_b32 s69, s70, s43\n\ts_mul_i32 s69, s69, 294912\n\ts_add_u32 s69, s69, 589824\n\ts_add_u32 m0, s54, 16384\n\ts_nop 0\n\tbuffer_load_dwordx4 v10, s[12:15], s69 offen lds\n\ts_lshl_b32 s65, s65, 1\n\t" \
    "s_add_u32 s65, s65, s57\n\ts_mov_b32 s4, s65\n\ts_lshl_b32 s49, s65, 7\n\ts_add_u32 s49, s49, s50\n\ts_mul_i32 s45, s65, 3072\n\ts_add_u32 s45, s45, s46\n\ts_cmp_eq_u32 s39, 2\n\ts_cselect_b32 s46, s46, 2146435072\n\t" \
    "v_and_b32 v16, 31, v240\n\tv_mul_u32_u24 v243, 1152, v16\n\tv_lshrrev_b32 v16, 5, v240\n\tv_lshl_add_u32 v243, v16, 4, v243\n\tv_lshrrev_b32 v242, 1, v244\n\tv_lshlrev_b32 v236, 2, v240\n\tv_min_u32 v241, 17, v240\n\tv_lshlrev_b32 v241, 7, v241\n\t" \
    "s_lshl_b32 s65, s6, 4\n\ts_add_u32 s65, s65, s7\n\ts_lshl_b32 s65, s65, 2\n\ts_add_u32 s65, s65, s5\n\ts_lshl_b32 s65, s65, 2\n\ts_add_u32 s53, s65, s38\n\ts_mul_i32 s55, s53, 9984\n\ts_add_u32 s55, s55, 7208960\n\t" \
    "s_cmp_eq_u32 s39, 2\n\ts_cselect_b32 s55, s55, 2146435072\n\ts_mul_i32 s56, s53, 3072\n\ts_add_u32 s56, s56, 4849664\n\ts_or_b32 s65, s40, s38\n\ts_cselect_b32 s60, 2146435072, 0\n\ts_cmp_ge_u32 s43, 19\n\ts_cselect_b32 s69, s70, s43\n\t" \
    "s_mul_i32 s69, s69, 294912\n\ts_add_u32 s69, s69, 589824\n\ts_add_u32 m0, s54, 20480\n\ts_nop 0\n\tbuffer_load_dwordx4 v11, s[12:15], s69 offen lds\n\ts_cmp_ge_u32 s43, 18\n\ts_cselect_b32 s69, s70, s43\n\ts_mul_i32 s69, s69, 294912\n\t" \
    "s_add_u32 s69, s69, 884736\n\ts_add_u32 m0, s54, 24576\n\ts_nop 0\n\tbuffer_load_dwordx4 v10, s[12:15], s69 offen lds\n\ts_cmp_ge_u32 s43, 18\n\ts_cselect_b32 s69, s70, s43\n\ts_mul_i32 s69, s69, 294912\n\ts_add_u32 s69, s69, 884736\n\t" \
    "s_add_u32 m0, s54, 28672\n\ts_nop 0\n\tbuffer_load_dwordx4 v11, s[12:15], s69 offen lds\n\ts_cmp_ge_u32 s43, 17\n\ts_cselect_b32 s69, s70, s43\n\ts_mul_i32 s69, s69, 294912\n\ts_add_u32 s69, s69, 1179648\n\ts_add_u32 m0, s54, 32768\n\t" \
    "s_nop 0\n\tbuffer_load_dwordx4 v10, s[12:15], s69 offen lds\n\ts_cmp_ge_u32 s43, 17\n\ts_cselect_b32 s69, s70, s43\n\ts_mul_i32 s69, s69, 294912\n\ts_add_u32 s69, s69, 1179648\n\ts_add_u32 m0, s54, 36864\n\ts_nop 0\n\t" \
    "buffer_load_dwordx4 v11, s[12:15], s69 offen lds\n\ts_waitcnt vmcnt(14)\n\ts_barrier\n\tds_read_b64_tr_b16 v[36:37], v93 offset:0\n\tds_read_b64_tr_b16 v[38:39], v93 offset:4608\n\tds_read_b64_tr_b16 v[40:41], v93 offset:9216\n\tds_read_b64_tr_b16 v[42:43], v93 offset:13312\n\tds_read_b64_tr_b16 v[44:45], v93 offset:17920\n\t" \
    "ds_read_b64_tr_b16 v[46:47], v93 offset:512\n\tds_read_b64_tr_b16 v[48:49], v93 offset:5120\n\tds_read_b64_tr_b16 v[50:51], v93 offset:9728\n\tds_read_b64_tr_b16 v[52:53], v93 offset:13824\n\tds_read_b64_tr_b16 v[54:55], v93 offset:18432\n\tds_read_b64_tr_b16 v[120:121], v92 offset:1024\n\tds_read_b64_tr_b16 v[122:123], v92 offset:5632\n\tds_read_b64_tr_b16 v[124:125], v92 offset:10240\n\t" \
    "ds_read_b64_tr_b16 v[126:127], v92 offset:14336\n\tds_read_b64_tr_b16 v[128:129], v92 offset:18944\n\tv_lshlrev_b32 v78, 16, v60\n\tv_and_b32 v79, 0xffff0000, v60\n\tv_lshlrev_b32 v80, 16, v61\n\tv_and_b32 v81, 0xffff0000, v61\n\ts_waitcnt lgkmcnt(10)\n\tv_lshlrev_b32 v66, 16, v36\n\t" \
    "v_and_b32 v67, 0xffff0000, v36\n\tv_lshlrev_b32 v68, 16, v37\n\tv_and_b32 v69, 0xffff0000, v37\n\tv_lshlrev_b32 v70, 16, v38\n\tv_and_b32 v71, 0xffff0000, v38\n\tv_lshlrev_b32 v72, 16, v39\n\tv_and_b32 v73, 0xffff0000, v39\n\tv_lshlrev_b32 v74, 16, v40\n\t" \
    "v_and_b32 v75, 0xffff0000, v40\n\tv_lshlrev_b32 v76, 16, v41\n\tv_mul_f32 v16, v66, v81\n\tv_mul_f32 v17, v67, v81\n\tv_mul_f32 v18, v68, v81\n\tv_mul_f32 v19, v69, v81\n\tv_mul_f32 v20, v70, v81\n\tv_mul_f32 v21, v71, v81\n\t" \
    "v_mul_f32 v22, v72, v81\n\tv_mul_f32 v23, v73, v81\n\tv_fmac_f32 v16, v67, v80\n\tv_fmac_f32 v17, v68, v80\n\tv_fmac_f32 v18, v69, v80\n\tv_fmac_f32 v19, v70, v80\n\tv_fmac_f32 v20, v71, v80\n\tv_fmac_f32 v21, v72, v80\n\t" \
    "v_fmac_f32 v22, v73, v80\n\tv_fmac_f32 v23, v74, v80\n\tv_fmac_f32 v16, v68, v79\n\tv_fmac_f32 v17, v69, v79\n\tv_fmac_f32 v18, v70, v79\n\tv_fmac_f32 v19, v71, v79\n\tv_fmac_f32 v20, v72, v79\n\tv_fmac_f32 v21, v73, v79\n\t" \
    "v_fmac_f32 v22, v74, v79\n\tv_fmac_f32 v23, v75, v79\n\tv_fmac_f32 v16, v69, v78\n\tv_fmac_f32 v17, v70, v78\n\tv_fmac_f32 v18, v71, v78\n\tv_fmac_f32 v19, v72, v78\n\tv_fmac_f32 v20, v73, v78\n\tv_fmac_f32 v21, v74, v78\n\t" \
    "v_fmac_f32 v22, v75, v78\n\tv_fmac_f32 v23, v76, v78\n\tv_cvt_pk_bf16_f32 v66, v16, v17\n\tv_cvt_pk_bf16_f32 v67, v18, v19\n\tv_cvt_pk_bf16_f32 v68, v20, v21\n\tv_cvt_pk_bf16_f32 v69, v22, v23\n\tv_lshlrev_b32 v16, 16, v66\n\tv_and_b32 v17, 0xffff0000, v66\n\t" \
    "v_lshlrev_b32 v18, 16, v67\n\tv_and_b32 v19, 0xffff0000, v67\n\tv_lshlrev_b32 v20, 16, v68\n\tv_and_b32 v21, 0xffff0000, v68\n\tv_lshlrev_b32 v22, 16, v69\n\tv_and_b32 v23, 0xffff0000, v69\n\tv_lshlrev_b32 v66, 16, v42\n\tv_and_b32 v67, 0xffff0000, v42\n\t" \
    "v_lshlrev_b32 v68, 16, v43\n\tv_and_b32 v69, 0xffff0000, v43\n\tv_lshlrev_b32 v70, 16, v44\n\tv_and_b32 v71, 0xffff0000, v44\n\tv_lshlrev_b32 v72, 16, v45\n\tv_and_b32 v73, 0xffff0000, v45\n\tv_pk_mul_f32 v[16:17], v[16:17], v[66:67]\n\tv_pk_mul_f32 v[18:19], v[18:19], v[68:69]\n\t" \
    "v_pk_mul_f32 v[20:21], v[20:21], v[70:71]\n\tv_pk_mul_f32 v[22:23], v[22:23], v[72:73]\n\tv_cvt_pk_bf16_f32 v24, v16, v17\n\tv_cvt_pk_bf16_f32 v25, v18, v19\n\tv_cvt_pk_bf16_f32 v26, v20, v21\n\tv_cvt_pk_bf16_f32 v27, v22, v23\n\ts_add_u32 s68, s62, s54\n\tbuffer_store_dwordx4 v[24:27], v8, s[8:11], s68 offen sc1\n\t" \
    "s_waitcnt vmcnt(9) lgkmcnt(0)\n\ts_barrier\n\tbuffer_store_dwordx2 v[248:249], v89, s[8:11], s63 offen sc1\n\ts_cmp_lg_u32 s39, 0\n\ts_cbranch_scc1 104f\n\ts_sub_u32 s73, s40, 216\n\ts_lshl_b32 s71, s73, 4\n\ts_mul_i32 s68, s73, 12\n\t" \
    "s_add_u32 s68, s68, 96\n\ts_cmp_lt_u32 s73, 24\n\ts_cselect_b32 s71, s71, s68\n\ts_cselect_b32 s73, 0, 2146435072\n\tv_cmp_eq_u32 vcc, 0, v240\n\tv_subrev_u32 v190, 8, v8\n\tv_mov_b32 v191, 2146435072\n\tv_cndmask_b32 v190, v190, v191, vcc\n\t" \
    "s_add_u32 s68, s71, s38\n\ts_add_u32 s68, s68, 0\n\ts_lshl_b32 s73, s68, 1\n\ts_lshl_b32 s68, s68, 12\n\tbuffer_load_dwordx4 v[164:167], v8, s[76:79], s68 offen offset:0\n\tbuffer_load_dwordx4 v[168:171], v8, s[76:79], s68 offen offset:1024\n\tbuffer_load_dwordx4 v[172:175], v8, s[76:79], s68 offen offset:2048\n\tbuffer_load_dwordx4 v[176:179], v8, s[76:79], s68 offen offset:3072\n\t" \
    "buffer_load_dwordx2 v[180:181], v190, s[76:79], s68 offen\n\tbuffer_load_dwordx2 v[182:183], v8, s[76:79], s68 offen offset:1016\n\tbuffer_load_dwordx2 v[184:185], v8, s[76:79], s68 offen offset:2040\n\tbuffer_load_dwordx2 v[186:187], v8, s[76:79], s68 offen offset:3064\n\tbuffer_load_ushort v188, off, s[80:83], s73\n\t104:\n\tv_lshlrev_b32 v78, 16, v62\n\ts_cmp_ge_u32 s43, 16\n\t" \
    "s_cselect_b32 s69, s70, s43\n\ts_mul_i32 s69, s69, 294912\n\ts_add_u32 s69, s69, 1474560\n\ts_add_u32 m0, s54, 40960\n\tv_and_b32 v79, 0xffff0000, v62\n\tv_lshlrev_b32 v80, 16, v63\n\tv_and_b32 v81, 0xffff0000, v63\n\tv_lshlrev_b32 v66, 16, v46\n\t" \
    "v_and_b32 v67, 0xffff0000, v46\n\tv_lshlrev_b32 v68, 16, v47\n\tv_and_b32 v69, 0xffff0000, v47\n\tv_lshlrev_b32 v70, 16, v48\n\tv_and_b32 v71, 0xffff0000, v48\n\tv_lshlrev_b32 v72, 16, v49\n\tv_and_b32 v73, 0xffff0000, v49\n\tv_lshlrev_b32 v74, 16, v50\n\t" \
    "v_and_b32 v75, 0xffff0000, v50\n\tv_lshlrev_b32 v76, 16, v51\n\tv_mul_f32 v16, v66, v81\n\tv_mul_f32 v17, v67, v81\n\tv_mul_f32 v18, v68, v81\n\tv_mul_f32 v19, v69, v81\n\tv_mul_f32 v20, v70, v81\n\tv_mul_f32 v21, v71, v81\n\t" \
    "v_mul_f32 v22, v72, v81\n\tv_mul_f32 v23, v73, v81\n\tv_fmac_f32 v16, v67, v80\n\tv_fmac_f32 v17, v68, v80\n\tv_fmac_f32 v18, v69, v80\n\tv_fmac_f32 v19, v70, v80\n\tv_fmac_f32 v20, v71, v80\n\tv_fmac_f32 v21, v72, v80\n\t" \
    "v_fmac_f32 v22, v73, v80\n\tv_fmac_f32 v23, v74, v80\n\tv_fmac_f32 v16, v68, v79\n\tv_fmac_f32 v17, v69, v79\n\tv_fmac_f32 v18, v70, v79\n\tv_fmac_f32 v19, v71, v79\n\tv_fmac_f32 v20, v72, v79\n\tv_fmac_f32 v21, v73, v79\n\t" \
    "v_fmac_f32 v22, v74, v79\n\tv_fmac_f32 v23, v75, v79\n\tv_fmac_f32 v16, v69, v78\n\tv_fmac_f32 v17, v70, v78\n\tv_fmac_f32 v18, v71, v78\n\tv_fmac_f32 v19, v72, v78\n\tv_fmac_f32 v20, v73, v78\n\tv_fmac_f32 v21, v74, v78\n\t" \
    "v_fmac_f32 v22, v75, v78\n\tv_fmac_f32 v23, v76, v78\n\tv_cvt_pk_bf16_f32 v66, v16, v17\n\tv_cvt_pk_bf16_f32 v67, v18, v19\n\tv_cvt_pk_bf16_f32 v68, v20, v21\n\tv_cvt_pk_bf16_f32 v69, v22, v23\n\tv_lshlrev_b32 v16, 16, v66\n\tv_and_b32 v17, 0xffff0000, v66\n\t" \
    "v_lshlrev_b32 v18, 16, v67\n\tv_and_b32 v19, 0xffff0000, v67\n\tv_lshlrev_b32 v20, 16, v68\n\tv_and_b32 v21, 0xffff0000, v68\n\tv_lshlrev_b32 v22, 16, v69\n\tv_and_b32 v23, 0xffff0000, v69\n\tv_lshlrev_b32 v66, 16, v52\n\tv_and_b32 v67, 0xffff0000, v52\n\t" \
    "v_lshlrev_b32 v68, 16, v53\n\tv_and_b32 v69, 0xffff0000, v53\n\tv_lshlrev_b32 v70, 16, v54\n\tv_and_b32 v71, 0xffff0000, v54\n\tv_lshlrev_b32 v72, 16, v55\n\tv_and_b32 v73, 0xffff0000, v55\n\tv_pk_mul_f32 v[16:17], v[16:17], v[66:67]\n\tv_pk_mul_f32 v[18:19], v[18:19], v[68:69]\n\t" \
    "v_pk_mul_f32 v[20:21], v[20:21], v[70:71]\n\tv_pk_mul_f32 v[22:23], v[22:23], v[72:73]\n\tv_cvt_pk_bf16_f32 v28, v16, v17\n\tv_cvt_pk_bf16_f32 v29, v18, v19\n\tv_cvt_pk_bf16_f32 v30, v20, v21\n\tv_cvt_pk_bf16_f32 v31, v22, v23\n\ts_add_u32 s68, s62, s54\n\tbuffer_store_dwordx4 v[28:31], v9, s[8:11], s68 offen sc1\n\t" \
    "v_lshlrev_b32 v78, 16, v64\n\tv_and_b32 v79, 0xffff0000, v64\n\tv_lshlrev_b32 v80, 16, v65\n\tv_and_b32 v81, 0xffff0000, v65\n\tv_lshlrev_b32 v66, 16, v120\n\tv_and_b32 v67, 0xffff0000, v120\n\tv_lshlrev_b32 v68, 16, v121\n\tv_and_b32 v69, 0xffff0000, v121\n\t" \
    "v_lshlrev_b32 v70, 16, v122\n\tv_and_b32 v71, 0xffff0000, v122\n\tv_lshlrev_b32 v72, 16, v123\n\tv_and_b32 v73, 0xffff0000, v123\n\tv_lshlrev_b32 v74, 16, v124\n\tv_and_b32 v75, 0xffff0000, v124\n\tv_lshlrev_b32 v76, 16, v125\n\tv_mul_f32 v16, v66, v81\n\t" \
    "v_mul_f32 v17, v67, v81\n\tv_mul_f32 v18, v68, v81\n\tv_mul_f32 v19, v69, v81\n\tv_mul_f32 v20, v70, v81\n\tv_mul_f32 v21, v71, v81\n\tv_mul_f32 v22, v72, v81\n\tv_mul_f32 v23, v73, v81\n\tv_fmac_f32 v16, v67, v80\n\t" \
    "v_fmac_f32 v17, v68, v80\n\tv_fmac_f32 v18, v69, v80\n\tv_fmac_f32 v19, v70, v80\n\tv_fmac_f32 v20, v71, v80\n\tv_fmac_f32 v21, v72, v80\n\tv_fmac_f32 v22, v73, v80\n\tv_fmac_f32 v23, v74, v80\n\tv_fmac_f32 v16, v68, v79\n\t" \
    "v_fmac_f32 v17, v69, v79\n\tv_fmac_f32 v18, v70, v79\n\tv_fmac_f32 v19, v71, v79\n\tv_fmac_f32 v20, v72, v79\n\tv_fmac_f32 v21, v73, v79\n\tv_fmac_f32 v22, v74, v79\n\tv_fmac_f32 v23, v75, v79\n\tv_fmac_f32 v16, v69, v78\n\t" \
    "v_fmac_f32 v17, v70, v78\n\tv_fmac_f32 v18, v71, v78\n\tv_fmac_f32 v19, v72, v78\n\tv_fmac_f32 v20, v73, v78\n\tv_fmac_f32 v21, v74, v78\n\tv_fmac_f32 v22, v75, v78\n\tv_fmac_f32 v23, v76, v78\n\tv_cvt_pk_bf16_f32 v66, v16, v17\n\t" \
    "v_cvt_pk_bf16_f32 v67, v18, v19\n\tv_cvt_pk_bf16_f32 v68, v20, v21\n\tv_cvt_pk_bf16_f32 v69, v22, v23\n\tv_lshlrev_b32 v16, 16, v66\n\tv_and_b32 v17, 0xffff0000, v66\n\tv_lshlrev_b32 v18, 16, v67\n\tv_and_b32 v19, 0xffff0000, v67\n\tv_lshlrev_b32 v20, 16, v68\n\t" \
    "v_and_b32 v21, 0xffff0000, v68\n\tv_lshlrev_b32 v22, 16, v69\n\tv_and_b32 v23, 0xffff0000, v69\n\tv_lshlrev_b32 v66, 16, v126\n\tv_and_b32 v67, 0xffff0000, v126\n\tv_lshlrev_b32 v68, 16, v127\n\tv_and_b32 v69, 0xffff0000, v127\n\tv_lshlrev_b32 v70, 16, v128\n\t" \
    "v_and_b32 v71, 0xffff0000, v128\n\tv_lshlrev_b32 v72, 16, v129\n\tv_and_b32 v73, 0xffff0000, v129\n\tv_pk_mul_f32 v[16:17], v[16:17], v[66:67]\n\tv_pk_mul_f32 v[18:19], v[18:19], v[68:69]\n\tv_pk_mul_f32 v[20:21], v[20:21], v[70:71]\n\tv_pk_mul_f32 v[22:23], v[22:23], v[72:73]\n\tv_cvt_pk_bf16_f32 v32, v16, v17\n\t" \
    "v_cvt_pk_bf16_f32 v33, v18, v19\n\tv_cvt_pk_bf16_f32 v34, v20, v21\n\tv_cvt_pk_bf16_f32 v35, v22, v23\n\ts_add_u32 s68, s62, 8192\n\ts_cmp_eq_u32 s38, 1\n\ts_cselect_b32 s68, s68, 0x7ff00000\n\tbuffer_store_dwordx4 v[32:35], v8, s[8:11], s68 offen sc1\n\ts_cmp_eq_u32 s39, 0\n\t" \
    "s_cbranch_scc1 101f\n\tbuffer_load_dwordx4 v10, s[12:15], s69 offen lds\n\ts_cmp_ge_u32 s43, 16\n\ts_cselect_b32 s69, s70, s43\n\ts_mul_i32 s69, s69, 294912\n\ts_add_u32 s69, s69, 1474560\n\ts_add_u32 m0, s54, 45056\n\ts_nop 0\n\t" \
    "buffer_load_dwordx4 v11, s[12:15], s69 offen lds\n\ts_cmp_ge_u32 s43, 15\n\ts_cselect_b32 s69, s70, s43\n\ts_mul_i32 s69, s69, 294912\n\ts_add_u32 s69, s69, 1769472\n\ts_add_u32 m0, s54, 49152\n\ts_nop 0\n\tbuffer_load_dwordx4 v10, s[12:15], s69 offen lds\n\t" \
    "s_waitcnt vmcnt(3)\n\tbuffer_store_dwordx2 v[248:249], off, s[8:11], s48 sc1\n\ts_cmp_ge_u32 s43, 15\n\ts_cselect_b32 s69, s70, s43\n\ts_mul_i32 s69, s69, 294912\n\ts_add_u32 s69, s69, 1769472\n\ts_add_u32 m0, s54, 53248\n\ts_nop 0\n\t" \
    "buffer_load_dwordx4 v11, s[12:15], s69 offen lds\n\ts_cmp_ge_u32 s43, 14\n\ts_cselect_b32 s69, s70, s43\n\ts_mul_i32 s69, s69, 294912\n\ts_add_u32 s69, s69, 2064384\n\ts_add_u32 m0, s54, 57344\n\ts_nop 0\n\tbuffer_load_dwordx4 v10, s[12:15], s69 offen lds\n\t" \
    "s_cmp_ge_u32 s43, 14\n\ts_cselect_b32 s69, s70, s43\n\ts_mul_i32 s69, s69, 294912\n\ts_add_u32 s69, s69, 2064384\n\ts_add_u32 m0, s54, 61440\n\ts_nop 0\n\tbuffer_load_dwordx4 v11, s[12:15], s69 offen lds\n\ts_cmp_ge_u32 s43, 13\n\t" \
    "s_cselect_b32 s69, s70, s43\n\ts_mul_i32 s69, s69, 294912\n\ts_add_u32 s69, s69, 2359296\n\ts_add_u32 m0, s54, 65536\n\ts_nop 0\n\tbuffer_load_dwordx4 v10, s[12:15], s69 offen lds\n\ts_cmp_ge_u32 s43, 13\n\ts_cselect_b32 s69, s70, s43\n\t" \
    "s_mul_i32 s69, s69, 294912\n\ts_add_u32 s69, s69, 2359296\n\ts_add_u32 m0, s54, 69632\n\ts_nop 0\n\tbuffer_load_dwordx4 v11, s[12:15], s69 offen lds\n\ts_cmp_ge_u32 s43, 12\n\ts_cselect_b32 s69, s70, s43\n\ts_mul_i32 s69, s69, 294912\n\t" \
    "s_add_u32 s69, s69, 2654208\n\ts_add_u32 m0, s54, 73728\n\ts_nop 0\n\tbuffer_load_dwordx4 v10, s[12:15], s69 offen lds\n\ts_cmp_ge_u32 s43, 12\n\ts_cselect_b32 s69, s70, s43\n\ts_mul_i32 s69, s69, 294912\n\ts_add_u32 s69, s69, 2654208\n\t" \
    "s_add_u32 m0, s54, 77824\n\ts_nop 0\n\tbuffer_load_dwordx4 v11, s[12:15], s69 offen lds\n\ts_cmp_ge_u32 s43, 11\n\ts_cselect_b32 s69, s70, s43\n\ts_mul_i32 s69, s69, 294912\n\ts_add_u32 s69, s69, 2949120\n\ts_add_u32 m0, s54, 81920\n\t" \
    "s_nop 0\n\tbuffer_load_dwordx4 v10, s[12:15], s69 offen lds\n\ts_lshl_b32 s71, s57, 7\n\ts_lshl_b32 s69, s42, 8\n\ts_add_u32 s71, s71, s69\n\tv_lshlrev_b32 v96, 3, v245\n\tbuffer_load_dwordx2 v[112:113], v96, s[28:31], s71 offen offset:0\n\tbuffer_load_dwordx2 v[114:115], v96, s[28:31], s71 offen offset:32\n\t" \
    "buffer_load_dwordx2 v[116:117], v96, s[28:31], s71 offen offset:64\n\tbuffer_load_dwordx2 v[118:119], v96, s[28:31], s71 offen offset:96\n\tv_lshrrev_b32 v229, 3, v244\n\tbuffer_load_ushort v228, v229, s[32:35], 0 offen offset:0\n\tbuffer_load_ushort v232, v229, s[32:35], 0 offen offset:32\n\ts_cmp_ge_u32 s43, 11\n\ts_cselect_b32 s69, s70, s43\n\ts_mul_i32 s69, s69, 294912\n\t" \
    "s_add_u32 s69, s69, 2949120\n\ts_add_u32 m0, s54, 86016\n\ts_nop 0\n\tbuffer_load_dwordx4 v11, s[12:15], s69 offen lds\n\tbuffer_load_dwordx4 v[250:253], v8, s[8:11], s47 offen sc1\n\ts_cmp_ge_u32 s43, 10\n\ts_cselect_b32 s69, s70, s43\n\ts_mul_i32 s69, s69, 294912\n\t" \
    "s_add_u32 s69, s69, 3244032\n\ts_add_u32 m0, s54, 90112\n\ts_nop 0\n\tbuffer_load_dwordx4 v10, s[12:15], s69 offen lds\n\ts_cmp_ge_u32 s43, 10\n\ts_cselect_b32 s69, s70, s43\n\ts_mul_i32 s69, s69, 294912\n\ts_add_u32 s69, s69, 3244032\n\t" \
    "s_add_u32 m0, s54, 94208\n\ts_nop 0\n\tbuffer_load_dwordx4 v11, s[12:15], s69 offen lds\n\ts_cmp_ge_u32 s43, 9\n\ts_cselect_b32 s69, s70, s43\n\ts_mul_i32 s69, s69, 294912\n\ts_add_u32 s69, s69, 3538944\n\ts_add_u32 m0, s54, 98304\n\t" \
    "s_nop 0\n\tbuffer_load_dwordx4 v10, s[12:15], s69 offen lds\n\ts_cmp_ge_u32 s43, 8\n\ts_cselect_b32 s69, s70, s43\n\ts_mul_i32 s69, s69, 294912\n\ts_add_u32 s69, s69, 3833856\n\ts_add_u32 m0, s54, 102400\n\ts_nop 0\n\t" \
    "buffer_load_dwordx4 v10, s[12:15], s69 offen lds\n\ts_cmp_ge_u32 s43, 7\n\ts_cselect_b32 s69, s70, s43\n\ts_mul_i32 s69, s69, 294912\n\ts_add_u32 s69, s69, 4128768\n\ts_add_u32 m0, s54, 106496\n\ts_nop 0\n\tbuffer_load_dwordx4 v10, s[12:15], s69 offen lds\n\t" \
    "s_cmp_ge_u32 s43, 6\n\ts_cselect_b32 s69, s70, s43\n\ts_mul_i32 s69, s69, 294912\n\ts_add_u32 s69, s69, 4423680\n\ts_add_u32 m0, s54, 110592\n\ts_nop 0\n\tbuffer_load_dwordx4 v10, s[12:15], s69 offen lds\n\ts_cmp_ge_u32 s43, 5\n\t" \
    "s_cselect_b32 s69, s70, s43\n\ts_mul_i32 s69, s69, 294912\n\ts_add_u32 s69, s69, 4718592\n\ts_add_u32 m0, s54, 114688\n\ts_nop 0\n\tbuffer_load_dwordx4 v10, s[12:15], s69 offen lds\n\ts_cmp_ge_u32 s43, 4\n\ts_cselect_b32 s69, s70, s43\n\t" \
    "s_mul_i32 s69, s69, 294912\n\ts_add_u32 s69, s69, 5013504\n\ts_add_u32 m0, s54, 118784\n\ts_nop 0\n\tbuffer_load_dwordx4 v10, s[12:15], s69 offen lds\n\ts_cmp_ge_u32 s43, 3\n\ts_cselect_b32 s69, s70, s43\n\ts_mul_i32 s69, s69, 294912\n\t" \
    "s_add_u32 s69, s69, 5308416\n\ts_add_u32 m0, s54, 122880\n\ts_nop 0\n\tbuffer_load_dwordx4 v10, s[12:15], s69 offen lds\n\ts_cmp_ge_u32 s43, 2\n\ts_cselect_b32 s69, s70, s43\n\ts_mul_i32 s69, s69, 294912\n\ts_add_u32 s69, s69, 5603328\n\t" \
    "s_add_u32 m0, s54, 126976\n\ts_nop 0\n\tbuffer_load_dwordx4 v10, s[12:15], s69 offen lds\n\ts_waitcnt vmcnt(10)\n\tv_cmp_ne_u64 vcc, s[36:37], v[250:251]\n\tv_cmp_ne_u64 s[74:75], s[36:37], v[252:253]\n\ts_or_b64 s[74:75], s[74:75], vcc\n\ts_cbranch_scc0 106f\n\t" \
    "s_mov_b32 s71, 1048576\n\t105:\n\ts_sleep 1\n\tbuffer_load_dwordx4 v[250:253], v8, s[8:11], s47 offen sc1\n\ts_waitcnt vmcnt(0)\n\tv_cmp_ne_u64 vcc, s[36:37], v[250:251]\n\tv_cmp_ne_u64 s[74:75], s[36:37], v[252:253]\n\ts_or_b64 s[74:75], s[74:75], vcc\n\t" \
    "s_cbranch_scc0 106f\n\ts_sub_u32 s71, s71, 1\n\ts_cbranch_scc0 105b\n\t106:\n\ts_cmp_ge_u32 s43, 21\n\ts_cselect_b32 s68, s70, s43\n\ts_mul_i32 s68, s68, 36864\n\ts_add_u32 s68, s68, s44\n\t" \
    "s_add_u32 s68, s68, 0\n\tbuffer_load_dwordx4 v[192:195], v8, s[8:11], s68 offen offset:0 sc1\n\tbuffer_load_dwordx4 v[196:199], v8, s[8:11], s68 offen offset:1024 sc1\n\tbuffer_load_dwordx4 v[200:203], v8, s[8:11], s68 offen offset:2048 sc1\n\ts_cmp_ge_u32 s43, 20\n\ts_cselect_b32 s68, s70, s43\n\ts_mul_i32 s68, s68, 36864\n\ts_add_u32 s68, s68, s44\n\t" \
    "s_add_u32 s68, s68, 36864\n\tbuffer_load_dwordx4 v[168:171], v8, s[8:11], s68 offen offset:0 sc1\n\tbuffer_load_dwordx4 v[172:175], v8, s[8:11], s68 offen offset:1024 sc1\n\tbuffer_load_dwordx4 v[176:179], v8, s[8:11], s68 offen offset:2048 sc1\n\ts_cmp_ge_u32 s43, 19\n\ts_cselect_b32 s68, s70, s43\n\ts_mul_i32 s68, s68, 36864\n\ts_add_u32 s68, s68, s44\n\t" \
    "s_add_u32 s68, s68, 73728\n\tbuffer_load_dwordx4 v[180:183], v8, s[8:11], s68 offen offset:0 sc1\n\tbuffer_load_dwordx4 v[184:187], v8, s[8:11], s68 offen offset:1024 sc1\n\tbuffer_load_dwordx4 v[188:191], v8, s[8:11], s68 offen offset:2048 sc1\n\ts_cmp_ge_u32 s43, 18\n\ts_cselect_b32 s68, s70, s43\n\ts_mul_i32 s68, s68, 36864\n\ts_add_u32 s68, s68, s44\n\t" \
    "s_add_u32 s68, s68, 110592\n\tbuffer_load_dwordx4 v[204:207], v8, s[8:11], s68 offen offset:0 sc1\n\tbuffer_load_dwordx4 v[208:211], v8, s[8:11], s68 offen offset:1024 sc1\n\tbuffer_load_dwordx4 v[212:215], v8, s[8:11], s68 offen offset:2048 sc1\n\tv_lshlrev_b32 v96, 16, v112\n\tv_and_b32 v97, 0xffff0000, v112\n\tv_lshlrev_b32 v98, 16, v113\n\tv_and_b32 v99, 0xffff0000, v113\n\t" \
    "v_lshlrev_b32 v100, 16, v114\n\tv_and_b32 v101, 0xffff0000, v114\n\tv_lshlrev_b32 v102, 16, v115\n\tv_and_b32 v103, 0xffff0000, v115\n\tv_lshlrev_b32 v104, 16, v116\n\tv_and_b32 v105, 0xffff0000, v116\n\tv_lshlrev_b32 v106, 16, v117\n\tv_and_b32 v107, 0xffff0000, v117\n\t" \
    "v_lshlrev_b32 v108, 16, v118\n\tv_and_b32 v109, 0xffff0000, v118\n\tv_lshlrev_b32 v110, 16, v119\n\tv_and_b32 v111, 0xffff0000, v119\n\tv_lshlrev_b32 v228, 16, v228\n\tv_mov_b32 v229, v228\n\tv_mov_b32 v230, v228\n\tv_mov_b32 v231, v228\n\t" \
    "v_lshlrev_b32 v232, 16, v232\n\tv_mov_b32 v233, v232\n\tv_mov_b32 v234, v232\n\tv_mov_b32 v235, v232\n\ts_barrier\n\tds_read_b64_tr_b16 v[16:17], v0 offset:0\n\tds_read_b64_tr_b16 v[18:19], v0 offset:512\n\tds_read_b64_tr_b16 v[20:21], v1 offset:0\n\t" \
    "ds_read_b64_tr_b16 v[22:23], v1 offset:512\n\tds_read_b64_tr_b16 v[24:25], v2 offset:0\n\tds_read_b64_tr_b16 v[26:27], v2 offset:512\n\tds_read_b64_tr_b16 v[28:29], v3 offset:0\n\tds_read_b64_tr_b16 v[30:31], v3 offset:512\n\tds_read_b64_tr_b16 v[32:33], v0 offset:4096\n\tds_read_b64_tr_b16 v[34:35], v0 offset:4608\n\ts_waitcnt vmcnt(9) lgkmcnt(8)\n\t" \
    "v_mfma_f32_16x16x32_bf16 v[120:123], v[16:19], v[192:195], 0\n\tds_read_b64_tr_b16 v[36:37], v1 offset:4096\n\tds_read_b64_tr_b16 v[38:39], v1 offset:4608\n\ts_cmp_ge_u32 s43, 17\n\ts_cselect_b32 s68, s70, s43\n\tv_mfma_f32_16x16x32_bf16 v[128:131], v[16:19], v[196:199], 0\n\ts_mul_i32 s68, s68, 36864\n\ts_add_u32 s68, s68, s44\n\t" \
    "s_add_u32 s68, s68, 147456\n\tv_mfma_f32_16x16x32_bf16 v[136:139], v[16:19], v[200:203], 0\n\ts_waitcnt lgkmcnt(8)\n\tv_mfma_f32_16x16x32_bf16 v[124:127], v[20:23], v[192:195], 0\n\tds_read_b64_tr_b16 v[40:41], v2 offset:4096\n\tds_read_b64_tr_b16 v[42:43], v2 offset:4608\n\tv_mfma_f32_16x16x32_bf16 v[132:135], v[20:23], v[196:199], 0\n\tbuffer_load_dwordx4 v[216:219], v8, s[8:11], s68 offen offset:0 sc1\n\t" \
    "v_mfma_f32_16x16x32_bf16 v[140:143], v[20:23], v[200:203], 0\n\ts_waitcnt lgkmcnt(8)\n\tv_mfma_f32_16x16x32_bf16 v[144:147], v[24:27], v[192:195], 0\n\tds_read_b64_tr_b16 v[44:45], v3 offset:4096\n\tds_read_b64_tr_b16 v[46:47], v3 offset:4608\n\tv_mfma_f32_16x16x32_bf16 v[152:155], v[24:27], v[196:199], 0\n\tbuffer_load_dwordx4 a[0:3], v8, s[8:11], s68 offen offset:1024 sc1\n\tv_mfma_f32_16x16x32_bf16 v[160:163], v[24:27], v[200:203], 0\n\t" \
    "s_waitcnt lgkmcnt(8)\n\tv_mfma_f32_16x16x32_bf16 v[148:151], v[28:31], v[192:195], 0\n\tds_read_b64_tr_b16 v[16:17], v0 offset:8192\n\tds_read_b64_tr_b16 v[18:19], v0 offset:8704\n\tv_mfma_f32_16x16x32_bf16 v[156:159], v[28:31], v[196:199], 0\n\tbuffer_load_dwordx4 a[4:7], v8, s[8:11], s68 offen offset:2048 sc1\n\ts_cmp_ge_u32 s43, 16\n\ts_cselect_b32 s68, s70, s43\n\t" \
    "s_mul_i32 s68, s68, 36864\n\tv_mfma_f32_16x16x32_bf16 v[164:167], v[28:31], v[200:203], 0\n\ts_add_u32 s68, s68, s44\n\ts_add_u32 s68, s68, 184320\n\ts_waitcnt lgkmcnt(8)\n\tv_mfma_f32_16x16x32_bf16 v[48:51], v[32:35], v[192:195], v[96:99]\n\tds_read_b64_tr_b16 v[20:21], v1 offset:8192\n\tds_read_b64_tr_b16 v[22:23], v1 offset:8704\n\t" \
    "v_mfma_f32_16x16x32_bf16 v[56:59], v[32:35], v[196:199], v[96:99]\n\tbuffer_load_dwordx4 a[8:11], v8, s[8:11], s68 offen offset:0 sc1\n\tv_mfma_f32_16x16x32_bf16 v[64:67], v[32:35], v[200:203], v[96:99]\n\ts_waitcnt lgkmcnt(8)\n\tv_mfma_f32_16x16x32_bf16 v[52:55], v[36:39], v[192:195], v[100:103]\n\tds_read_b64_tr_b16 v[24:25], v2 offset:8192\n\tds_read_b64_tr_b16 v[26:27], v2 offset:8704\n\tv_mfma_f32_16x16x32_bf16 v[60:63], v[36:39], v[196:199], v[100:103]\n\t" \
    "buffer_load_dwordx4 a[12:15], v8, s[8:11], s68 offen offset:1024 sc1\n\tv_mfma_f32_16x16x32_bf16 v[68:71], v[36:39], v[200:203], v[100:103]\n\ts_waitcnt lgkmcnt(8)\n\tv_mfma_f32_16x16x32_bf16 v[72:75], v[40:43], v[192:195], v[104:107]\n\tds_read_b64_tr_b16 v[28:29], v3 offset:8192\n\tds_read_b64_tr_b16 v[30:31], v3 offset:8704\n\tv_mfma_f32_16x16x32_bf16 v[80:83], v[40:43], v[196:199], v[104:107]\n\tbuffer_load_dwordx4 a[16:19], v8, s[8:11], s68 offen offset:2048 sc1\n\t" \
    "s_cmp_ge_u32 s43, 15\n\ts_cselect_b32 s68, s70, s43\n\ts_mul_i32 s68, s68, 36864\n\tv_mfma_f32_16x16x32_bf16 v[88:91], v[40:43], v[200:203], v[104:107]\n\ts_add_u32 s68, s68, s44\n\ts_add_u32 s68, s68, 221184\n\ts_waitcnt lgkmcnt(8)\n\tv_mfma_f32_16x16x32_bf16 v[76:79], v[44:47], v[192:195], v[108:111]\n\t" \
    "ds_read_b64_tr_b16 v[32:33], v0 offset:12288\n\tds_read_b64_tr_b16 v[34:35], v0 offset:12800\n\tv_mfma_f32_16x16x32_bf16 v[84:87], v[44:47], v[196:199], v[108:111]\n\tbuffer_load_dwordx4 a[20:23], v8, s[8:11], s68 offen offset:0 sc1\n\tv_mfma_f32_16x16x32_bf16 v[92:95], v[44:47], v[200:203], v[108:111]\n\ts_waitcnt vmcnt(13) lgkmcnt(8)\n\tv_mfma_f32_16x16x32_bf16 v[120:123], v[16:19], v[168:171], v[120:123]\n\tds_read_b64_tr_b16 v[36:37], v1 offset:12288\n\t" \
    "ds_read_b64_tr_b16 v[38:39], v1 offset:12800\n\tv_mfma_f32_16x16x32_bf16 v[128:131], v[16:19], v[172:175], v[128:131]\n\tv_mfma_f32_16x16x32_bf16 v[136:139], v[16:19], v[176:179], v[136:139]\n\ts_waitcnt lgkmcnt(8)\n\tv_mfma_f32_16x16x32_bf16 v[124:127], v[20:23], v[168:171], v[124:127]\n\tds_read_b64_tr_b16 v[40:41], v2 offset:12288\n\tds_read_b64_tr_b16 v[42:43], v2 offset:12800\n\tv_mfma_f32_16x16x32_bf16 v[132:135], v[20:23], v[172:175], v[132:135]\n\t" \
    "buffer_load_dwordx4 a[24:27], v8, s[8:11], s68 offen offset:1024 sc1\n\tv_mfma_f32_16x16x32_bf16 v[140:143], v[20:23], v[176:179], v[140:143]\n\ts_waitcnt lgkmcnt(8)\n\tv_mfma_f32_16x16x32_bf16 v[144:147], v[24:27], v[168:171], v[144:147]\n\tds_read_b64_tr_b16 v[44:45], v3 offset:12288\n\tds_read_b64_tr_b16 v[46:47], v3 offset:12800\n\tv_mfma_f32_16x16x32_bf16 v[152:155], v[24:27], v[172:175], v[152:155]\n\tbuffer_load_dwordx4 a[28:31], v8, s[8:11], s68 offen offset:2048 sc1\n\t" \
    "s_cmp_ge_u32 s43, 14\n\ts_cselect_b32 s68, s70, s43\n\ts_mul_i32 s68, s68, 36864\n\tv_mfma_f32_16x16x32_bf16 v[160:163], v[24:27], v[176:179], v[160:163]\n\ts_add_u32 s68, s68, s44\n\ts_add_u32 s68, s68, 258048\n\ts_waitcnt lgkmcnt(8)\n\tv_mfma_f32_16x16x32_bf16 v[148:151], v[28:31], v[168:171], v[148:151]\n\t" \
    "ds_read_b64_tr_b16 v[16:17], v0 offset:16384\n\tds_read_b64_tr_b16 v[18:19], v0 offset:16896\n\tv_mfma_f32_16x16x32_bf16 v[156:159], v[28:31], v[172:175], v[156:159]\n\tv_mfma_f32_16x16x32_bf16 v[164:167], v[28:31], v[176:179], v[164:167]\n\ts_waitcnt lgkmcnt(8)\n\tv_mfma_f32_16x16x32_bf16 v[48:51], v[32:35], v[168:171], v[48:51]\n\tds_read_b64_tr_b16 v[20:21], v1 offset:16384\n\tds_read_b64_tr_b16 v[22:23], v1 offset:16896\n\t" \
    "v_mfma_f32_16x16x32_bf16 v[56:59], v[32:35], v[172:175], v[56:59]\n\tbuffer_load_dwordx4 v[192:195], v8, s[8:11], s68 offen offset:0 sc1\n\tv_mfma_f32_16x16x32_bf16 v[64:67], v[32:35], v[176:179], v[64:67]\n\ts_waitcnt lgkmcnt(8)\n\tv_mfma_f32_16x16x32_bf16 v[52:55], v[36:39], v[168:171], v[52:55]\n\tds_read_b64_tr_b16 v[24:25], v2 offset:16384\n\tds_read_b64_tr_b16 v[26:27], v2 offset:16896\n\tv_mfma_f32_16x16x32_bf16 v[60:63], v[36:39], v[172:175], v[60:63]\n\t" \
    "buffer_load_dwordx4 v[196:199], v8, s[8:11], s68 offen offset:1024 sc1\n\tv_mfma_f32_16x16x32_bf16 v[68:71], v[36:39], v[176:179], v[68:71]\n\ts_waitcnt lgkmcnt(8)\n\tv_mfma_f32_16x16x32_bf16 v[72:75], v[40:43], v[168:171], v[72:75]\n\tds_read_b64_tr_b16 v[28:29], v3 offset:16384\n\tds_read_b64_tr_b16 v[30:31], v3 offset:16896\n\tv_mfma_f32_16x16x32_bf16 v[80:83], v[40:43], v[172:175], v[80:83]\n\tbuffer_load_dwordx4 v[200:203], v8, s[8:11], s68 offen offset:2048 sc1\n\t" \
    "s_cmp_ge_u32 s43, 13\n\ts_cselect_b32 s68, s70, s43\n\ts_mul_i32 s68, s68, 36864\n\tv_mfma_f32_16x16x32_bf16 v[88:91], v[40:43], v[176:179], v[88:91]\n\ts_add_u32 s68, s68, s44\n\ts_add_u32 s68, s68, 294912\n\ts_waitcnt lgkmcnt(8)\n\tv_mfma_f32_16x16x32_bf16 v[76:79], v[44:47], v[168:171], v[76:79]\n\t" \
    "ds_read_b64_tr_b16 v[32:33], v0 offset:20480\n\tds_read_b64_tr_b16 v[34:35], v0 offset:20992\n\tv_mfma_f32_16x16x32_bf16 v[84:87], v[44:47], v[172:175], v[84:87]\n\tv_mfma_f32_16x16x32_bf16 v[92:95], v[44:47], v[176:179], v[92:95]\n\ts_barrier\n\ts_waitcnt vmcnt(15) lgkmcnt(8)\n\tv_mfma_f32_16x16x32_bf16 v[120:123], v[16:19], v[180:183], v[120:123]\n\tds_read_b64_tr_b16 v[36:37], v1 offset:20480\n\t" \
    "ds_read_b64_tr_b16 v[38:39], v1 offset:20992\n\tv_mfma_f32_16x16x32_bf16 v[128:131], v[16:19], v[184:187], v[128:131]\n\tbuffer_load_dwordx4 a[32:35], v8, s[8:11], s68 offen offset:0 sc1\n\tv_mfma_f32_16x16x32_bf16 v[136:139], v[16:19], v[188:191], v[136:139]\n\ts_waitcnt lgkmcnt(8)\n\tv_mfma_f32_16x16x32_bf16 v[124:127], v[20:23], v[180:183], v[124:127]\n\tds_read_b64_tr_b16 v[40:41], v2 offset:20480\n\tds_read_b64_tr_b16 v[42:43], v2 offset:20992\n\t" \
    "v_mfma_f32_16x16x32_bf16 v[132:135], v[20:23], v[184:187], v[132:135]\n\tbuffer_load_dwordx4 a[36:39], v8, s[8:11], s68 offen offset:1024 sc1\n\tv_mfma_f32_16x16x32_bf16 v[140:143], v[20:23], v[188:191], v[140:143]\n\ts_waitcnt lgkmcnt(8)\n\tv_mfma_f32_16x16x32_bf16 v[144:147], v[24:27], v[180:183], v[144:147]\n\tds_read_b64_tr_b16 v[44:45], v3 offset:20480\n\tds_read_b64_tr_b16 v[46:47], v3 offset:20992\n\tv_mfma_f32_16x16x32_bf16 v[152:155], v[24:27], v[184:187], v[152:155]\n\t" \
    "buffer_load_dwordx4 a[40:43], v8, s[8:11], s68 offen offset:2048 sc1\n\ts_cmp_ge_u32 s43, 12\n\ts_cselect_b32 s68, s70, s43\n\ts_mul_i32 s68, s68, 36864\n\tv_mfma_f32_16x16x32_bf16 v[160:163], v[24:27], v[188:191], v[160:163]\n\ts_add_u32 s68, s68, s44\n\ts_add_u32 s68, s68, 331776\n\ts_waitcnt lgkmcnt(8)\n\t" \
    "v_mfma_f32_16x16x32_bf16 v[148:151], v[28:31], v[180:183], v[148:151]\n\tds_read_b64_tr_b16 v[16:17], v0 offset:24576\n\tds_read_b64_tr_b16 v[18:19], v0 offset:25088\n\tv_mfma_f32_16x16x32_bf16 v[156:159], v[28:31], v[184:187], v[156:159]\n\tbuffer_load_dwordx4 v[168:171], v8, s[8:11], s68 offen offset:0 sc1\n\tv_mfma_f32_16x16x32_bf16 v[164:167], v[28:31], v[188:191], v[164:167]\n\ts_waitcnt lgkmcnt(8)\n\tv_mfma_f32_16x16x32_bf16 v[48:51], v[32:35], v[180:183], v[48:51]\n\t" \
    "ds_read_b64_tr_b16 v[20:21], v1 offset:24576\n\tds_read_b64_tr_b16 v[22:23], v1 offset:25088\n\tv_mfma_f32_16x16x32_bf16 v[56:59], v[32:35], v[184:187], v[56:59]\n\tbuffer_load_dwordx4 v[172:175], v8, s[8:11], s68 offen offset:1024 sc1\n\tv_mfma_f32_16x16x32_bf16 v[64:67], v[32:35], v[188:191], v[64:67]\n\ts_waitcnt lgkmcnt(8)\n\tv_mfma_f32_16x16x32_bf16 v[52:55], v[36:39], v[180:183], v[52:55]\n\tds_read_b64_tr_b16 v[24:25], v2 offset:24576\n\t" \
    "ds_read_b64_tr_b16 v[26:27], v2 offset:25088\n\tv_mfma_f32_16x16x32_bf16 v[60:63], v[36:39], v[184:187], v[60:63]\n\tv_mfma_f32_16x16x32_bf16 v[68:71], v[36:39], v[188:191], v[68:71]\n\ts_waitcnt lgkmcnt(8)\n\tv_mfma_f32_16x16x32_bf16 v[72:75], v[40:43], v[180:183], v[72:75]\n\tds_read_b64_tr_b16 v[28:29], v3 offset:24576\n\tds_read_b64_tr_b16 v[30:31], v3 offset:25088\n\tv_mfma_f32_16x16x32_bf16 v[80:83], v[40:43], v[184:187], v[80:83]\n\t" \
    "buffer_load_dwordx4 v[176:179], v8, s[8:11], s68 offen offset:2048 sc1\n\ts_cmp_ge_u32 s43, 11\n\ts_cselect_b32 s68, s70, s43\n\ts_mul_i32 s68, s68, 36864\n\tv_mfma_f32_16x16x32_bf16 v[88:91], v[40:43], v[188:191], v[88:91]\n\ts_add_u32 s68, s68, s44\n\ts_add_u32 s68, s68, 368640\n\ts_waitcnt lgkmcnt(8)\n\t" \
    "v_mfma_f32_16x16x32_bf16 v[76:79], v[44:47], v[180:183], v[76:79]\n\tds_read_b64_tr_b16 v[32:33], v0 offset:28672\n\tds_read_b64_tr_b16 v[34:35], v0 offset:29184\n\tv_mfma_f32_16x16x32_bf16 v[84:87], v[44:47], v[184:187], v[84:87]\n\tbuffer_load_dwordx4 a[44:47], v8, s[8:11], s68 offen offset:0 sc1\n\tv_mfma_f32_16x16x32_bf16 v[92:95], v[44:47], v[188:191], v[92:95]\n\ts_waitcnt vmcnt(19) lgkmcnt(8)\n\tv_mfma_f32_16x16x32_bf16 v[120:123], v[16:19], v[204:207], v[120:123]\n\t" \
    "ds_read_b64_tr_b16 v[36:37], v1 offset:28672\n\tds_read_b64_tr_b16 v[38:39], v1 offset:29184\n\tv_mfma_f32_16x16x32_bf16 v[128:131], v[16:19], v[208:211], v[128:131]\n\tbuffer_load_dwordx4 a[48:51], v8, s[8:11], s68 offen offset:1024 sc1\n\tv_mfma_f32_16x16x32_bf16 v[136:139], v[16:19], v[212:215], v[136:139]\n\ts_waitcnt lgkmcnt(8)\n\tv_mfma_f32_16x16x32_bf16 v[124:127], v[20:23], v[204:207], v[124:127]\n\tds_read_b64_tr_b16 v[40:41], v2 offset:28672\n\t" \
    "ds_read_b64_tr_b16 v[42:43], v2 offset:29184\n\tv_mfma_f32_16x16x32_bf16 v[132:135], v[20:23], v[208:211], v[132:135]\n\tv_mfma_f32_16x16x32_bf16 v[140:143], v[20:23], v[212:215], v[140:143]\n\ts_waitcnt lgkmcnt(8)\n\tv_mfma_f32_16x16x32_bf16 v[144:147], v[24:27], v[204:207], v[144:147]\n\tds_read_b64_tr_b16 v[44:45], v3 offset:28672\n\tds_read_b64_tr_b16 v[46:47], v3 offset:29184\n\tv_mfma_f32_16x16x32_bf16 v[152:155], v[24:27], v[208:211], v[152:155]\n\t" \
    "buffer_load_dwordx4 a[52:55], v8, s[8:11], s68 offen offset:2048 sc1\n\ts_cmp_ge_u32 s43, 10\n\ts_cselect_b32 s68, s70, s43\n\ts_mul_i32 s68, s68, 36864\n\tv_mfma_f32_16x16x32_bf16 v[160:163], v[24:27], v[212:215], v[160:163]\n\ts_add_u32 s68, s68, s44\n\ts_add_u32 s68, s68, 405504\n\ts_waitcnt lgkmcnt(8)\n\t" \
    "v_mfma_f32_16x16x32_bf16 v[148:151], v[28:31], v[204:207], v[148:151]\n\tds_read_b64_tr_b16 v[16:17], v0 offset:32768\n\tds_read_b64_tr_b16 v[18:19], v0 offset:33280\n\tv_mfma_f32_16x16x32_bf16 v[156:159], v[28:31], v[208:211], v[156:159]\n\tbuffer_load_dwordx4 v[180:183], v8, s[8:11], s68 offen offset:0 sc1\n\tv_mfma_f32_16x16x32_bf16 v[164:167], v[28:31], v[212:215], v[164:167]\n\ts_waitcnt lgkmcnt(8)\n\tv_mfma_f32_16x16x32_bf16 v[48:51], v[32:35], v[204:207], v[48:51]\n\t" \
    "ds_read_b64_tr_b16 v[20:21], v1 offset:32768\n\tds_read_b64_tr_b16 v[22:23], v1 offset:33280\n\tv_mfma_f32_16x16x32_bf16 v[56:59], v[32:35], v[208:211], v[56:59]\n\tbuffer_load_dwordx4 v[184:187], v8, s[8:11], s68 offen offset:1024 sc1\n\tv_mfma_f32_16x16x32_bf16 v[64:67], v[32:35], v[212:215], v[64:67]\n\ts_waitcnt lgkmcnt(8)\n\tv_mfma_f32_16x16x32_bf16 v[52:55], v[36:39], v[204:207], v[52:55]\n\tds_read_b64_tr_b16 v[24:25], v2 offset:32768\n\t" \
    "ds_read_b64_tr_b16 v[26:27], v2 offset:33280\n\tv_mfma_f32_16x16x32_bf16 v[60:63], v[36:39], v[208:211], v[60:63]\n\tv_mfma_f32_16x16x32_bf16 v[68:71], v[36:39], v[212:215], v[68:71]\n\ts_waitcnt lgkmcnt(8)\n\tv_mfma_f32_16x16x32_bf16 v[72:75], v[40:43], v[204:207], v[72:75]\n\tds_read_b64_tr_b16 v[28:29], v3 offset:32768\n\tds_read_b64_tr_b16 v[30:31], v3 offset:33280\n\tv_mfma_f32_16x16x32_bf16 v[80:83], v[40:43], v[208:211], v[80:83]\n\t" \
    "buffer_load_dwordx4 v[188:191], v8, s[8:11], s68 offen offset:2048 sc1\n\ts_cmp_ge_u32 s43, 9\n\ts_cselect_b32 s68, s70, s43\n\ts_mul_i32 s68, s68, 36864\n\tv_mfma_f32_16x16x32_bf16 v[88:91], v[40:43], v[212:215], v[88:91]\n\ts_add_u32 s68, s68, s44\n\ts_add_u32 s68, s68, 442368\n\ts_waitcnt lgkmcnt(8)\n\t" \
    "v_mfma_f32_16x16x32_bf16 v[76:79], v[44:47], v[204:207], v[76:79]\n\tds_read_b64_tr_b16 v[32:33], v0 offset:36864\n\tds_read_b64_tr_b16 v[34:35], v0 offset:37376\n\tv_mfma_f32_16x16x32_bf16 v[84:87], v[44:47], v[208:211], v[84:87]\n\tbuffer_load_dwordx4 a[56:59], v8, s[8:11], s68 offen offset:0 sc1\n\tv_mfma_f32_16x16x32_bf16 v[92:95], v[44:47], v[212:215], v[92:95]\n\ts_barrier\n\ts_waitcnt vmcnt(22) lgkmcnt(8)\n\t" \
    "v_mfma_f32_16x16x32_bf16 v[120:123], v[16:19], v[216:219], v[120:123]\n\tds_read_b64_tr_b16 v[36:37], v1 offset:36864\n\tds_read_b64_tr_b16 v[38:39], v1 offset:37376\n\tv_mfma_f32_16x16x32_bf16 v[128:131], v[16:19], a[0:3], v[128:131]\n\tbuffer_load_dwordx4 a[60:63], v8, s[8:11], s68 offen offset:1024 sc1\n\tv_mfma_f32_16x16x32_bf16 v[136:139], v[16:19], a[4:7], v[136:139]\n\ts_waitcnt lgkmcnt(8)\n\tv_mfma_f32_16x16x32_bf16 v[124:127], v[20:23], v[216:219], v[124:127]\n\t" \
    "ds_read_b64_tr_b16 v[40:41], v2 offset:36864\n\tds_read_b64_tr_b16 v[42:43], v2 offset:37376\n\tv_mfma_f32_16x16x32_bf16 v[132:135], v[20:23], a[0:3], v[132:135]\n\tbuffer_load_dwordx4 a[64:67], v8, s[8:11], s68 offen offset:2048 sc1\n\ts_cmp_ge_u32 s43, 8\n\ts_cselect_b32 s68, s70, s43\n\ts_mul_i32 s68, s68, 36864\n\tv_mfma_f32_16x16x32_bf16 v[140:143], v[20:23], a[4:7], v[140:143]\n\t" \
    "s_add_u32 s68, s68, s44\n\ts_add_u32 s68, s68, 479232\n\ts_waitcnt lgkmcnt(8)\n\tv_mfma_f32_16x16x32_bf16 v[144:147], v[24:27], v[216:219], v[144:147]\n\tds_read_b64_tr_b16 v[44:45], v3 offset:36864\n\tds_read_b64_tr_b16 v[46:47], v3 offset:37376\n\tv_mfma_f32_16x16x32_bf16 v[152:155], v[24:27], a[0:3], v[152:155]\n\tbuffer_load_dwordx4 v[204:207], v8, s[8:11], s68 offen offset:0 sc1\n\t" \
    "v_mfma_f32_16x16x32_bf16 v[160:163], v[24:27], a[4:7], v[160:163]\n\ts_waitcnt lgkmcnt(8)\n\tv_mfma_f32_16x16x32_bf16 v[148:151], v[28:31], v[216:219], v[148:151]\n\tds_read_b64_tr_b16 v[16:17], v0 offset:40960\n\tds_read_b64_tr_b16 v[18:19], v0 offset:41472\n\tv_mfma_f32_16x16x32_bf16 v[156:159], v[28:31], a[0:3], v[156:159]\n\tv_mfma_f32_16x16x32_bf16 v[164:167], v[28:31], a[4:7], v[164:167]\n\ts_waitcnt lgkmcnt(8)\n\t" \
    "v_mfma_f32_16x16x32_bf16 v[48:51], v[32:35], v[216:219], v[48:51]\n\tds_read_b64_tr_b16 v[20:21], v1 offset:40960\n\tds_read_b64_tr_b16 v[22:23], v1 offset:41472\n\tv_mfma_f32_16x16x32_bf16 v[56:59], v[32:35], a[0:3], v[56:59]\n\tbuffer_load_dwordx4 v[208:211], v8, s[8:11], s68 offen offset:1024 sc1\n\tv_mfma_f32_16x16x32_bf16 v[64:67], v[32:35], a[4:7], v[64:67]\n\ts_waitcnt lgkmcnt(8)\n\tv_mfma_f32_16x16x32_bf16 v[52:55], v[36:39], v[216:219], v[52:55]\n\t" \
    "ds_read_b64_tr_b16 v[24:25], v2 offset:40960\n\tds_read_b64_tr_b16 v[26:27], v2 offset:41472\n\tv_mfma_f32_16x16x32_bf16 v[60:63], v[36:39], a[0:3], v[60:63]\n\tbuffer_load_dwordx4 v[212:215], v8, s[8:11], s68 offen offset:2048 sc1\n\ts_cmp_ge_u32 s43, 7\n\ts_cselect_b32 s68, s70, s43\n\ts_mul_i32 s68, s68, 36864\n\tv_mfma_f32_16x16x32_bf16 v[68:71], v[36:39], a[4:7], v[68:71]\n\t" \
    "s_add_u32 s68, s68, s44\n\ts_add_u32 s68, s68, 516096\n\ts_waitcnt lgkmcnt(8)\n\tv_mfma_f32_16x16x32_bf16 v[72:75], v[40:43], v[216:219], v[72:75]\n\tds_read_b64_tr_b16 v[28:29], v3 offset:40960\n\tds_read_b64_tr_b16 v[30:31], v3 offset:41472\n\tv_mfma_f32_16x16x32_bf16 v[80:83], v[40:43], a[0:3], v[80:83]\n\tbuffer_load_dwordx4 a[68:71], v8, s[8:11], s68 offen offset:0 sc1\n\t" \
    "v_mfma_f32_16x16x32_bf16 v[88:91], v[40:43], a[4:7], v[88:91]\n\ts_waitcnt lgkmcnt(8)\n\tv_mfma_f32_16x16x32_bf16 v[76:79], v[44:47], v[216:219], v[76:79]\n\tds_read_b64_tr_b16 v[32:33], v0 offset:45056\n\tds_read_b64_tr_b16 v[34:35], v0 offset:45568\n\tv_mfma_f32_16x16x32_bf16 v[84:87], v[44:47], a[0:3], v[84:87]\n\tv_mfma_f32_16x16x32_bf16 v[92:95], v[44:47], a[4:7], v[92:95]\n\ts_waitcnt vmcnt(25) lgkmcnt(8)\n\t" \
    "v_mfma_f32_16x16x32_bf16 v[120:123], v[16:19], a[8:11], v[120:123]\n\tds_read_b64_tr_b16 v[36:37], v1 offset:45056\n\tds_read_b64_tr_b16 v[38:39], v1 offset:45568\n\tv_mfma_f32_16x16x32_bf16 v[128:131], v[16:19], a[12:15], v[128:131]\n\tbuffer_load_dwordx4 a[72:75], v8, s[8:11], s68 offen offset:1024 sc1\n\tv_mfma_f32_16x16x32_bf16 v[136:139], v[16:19], a[16:19], v[136:139]\n\ts_waitcnt lgkmcnt(8)\n\tv_mfma_f32_16x16x32_bf16 v[124:127], v[20:23], a[8:11], v[124:127]\n\t" \
    "ds_read_b64_tr_b16 v[40:41], v2 offset:45056\n\tds_read_b64_tr_b16 v[42:43], v2 offset:45568\n\tv_mfma_f32_16x16x32_bf16 v[132:135], v[20:23], a[12:15], v[132:135]\n\tbuffer_load_dwordx4 a[76:79], v8, s[8:11], s68 offen offset:2048 sc1\n\ts_cmp_ge_u32 s43, 6\n\ts_cselect_b32 s68, s70, s43\n\ts_mul_i32 s68, s68, 36864\n\tv_mfma_f32_16x16x32_bf16 v[140:143], v[20:23], a[16:19], v[140:143]\n\t" \
    "s_add_u32 s68, s68, s44\n\ts_add_u32 s68, s68, 552960\n\ts_waitcnt lgkmcnt(8)\n\tv_mfma_f32_16x16x32_bf16 v[144:147], v[24:27], a[8:11], v[144:147]\n\tds_read_b64_tr_b16 v[44:45], v3 offset:45056\n\tds_read_b64_tr_b16 v[46:47], v3 offset:45568\n\tv_mfma_f32_16x16x32_bf16 v[152:155], v[24:27], a[12:15], v[152:155]\n\tbuffer_load_dwordx4 v[216:219], v8, s[8:11], s68 offen offset:0 sc1\n\t" \
    "v_mfma_f32_16x16x32_bf16 v[160:163], v[24:27], a[16:19], v[160:163]\n\ts_waitcnt lgkmcnt(8)\n\tv_mfma_f32_16x16x32_bf16 v[148:151], v[28:31], a[8:11], v[148:151]\n\tds_read_b64_tr_b16 v[16:17], v0 offset:49152\n\tds_read_b64_tr_b16 v[18:19], v0 offset:49664\n\tv_mfma_f32_16x16x32_bf16 v[156:159], v[28:31], a[12:15], v[156:159]\n\tv_mfma_f32_16x16x32_bf16 v[164:167], v[28:31], a[16:19], v[164:167]\n\ts_waitcnt lgkmcnt(8)\n\t" \
    "v_mfma_f32_16x16x32_bf16 v[48:51], v[32:35], a[8:11], v[48:51]\n\tds_read_b64_tr_b16 v[20:21], v1 offset:49152\n\tds_read_b64_tr_b16 v[22:23], v1 offset:49664\n\tv_mfma_f32_16x16x32_bf16 v[56:59], v[32:35], a[12:15], v[56:59]\n\tbuffer_load_dwordx4 a[0:3], v8, s[8:11], s68 offen offset:1024 sc1\n\tv_mfma_f32_16x16x32_bf16 v[64:67], v[32:35], a[16:19], v[64:67]\n\ts_waitcnt lgkmcnt(8)\n\tv_mfma_f32_16x16x32_bf16 v[52:55], v[36:39], a[8:11], v[52:55]\n\t" \
    "ds_read_b64_tr_b16 v[24:25], v2 offset:49152\n\tds_read_b64_tr_b16 v[26:27], v2 offset:49664\n\tv_mfma_f32_16x16x32_bf16 v[60:63], v[36:39], a[12:15], v[60:63]\n\tbuffer_load_dwordx4 a[4:7], v8, s[8:11], s68 offen offset:2048 sc1\n\ts_cmp_ge_u32 s43, 5\n\ts_cselect_b32 s68, s70, s43\n\ts_mul_i32 s68, s68, 36864\n\tv_mfma_f32_16x16x32_bf16 v[68:71], v[36:39], a[16:19], v[68:71]\n\t" \
    "s_add_u32 s68, s68, s44\n\ts_add_u32 s68, s68, 589824\n\ts_waitcnt lgkmcnt(8)\n\tv_mfma_f32_16x16x32_bf16 v[72:75], v[40:43], a[8:11], v[72:75]\n\tds_read_b64_tr_b16 v[28:29], v3 offset:49152\n\tds_read_b64_tr_b16 v[30:31], v3 offset:49664\n\tv_mfma_f32_16x16x32_bf16 v[80:83], v[40:43], a[12:15], v[80:83]\n\tbuffer_load_dwordx4 a[80:83], v8, s[8:11], s68 offen offset:0 sc1\n\t" \
    "v_mfma_f32_16x16x32_bf16 v[88:91], v[40:43], a[16:19], v[88:91]\n\ts_waitcnt lgkmcnt(8)\n\tv_mfma_f32_16x16x32_bf16 v[76:79], v[44:47], a[8:11], v[76:79]\n\tds_read_b64_tr_b16 v[32:33], v0 offset:53248\n\tds_read_b64_tr_b16 v[34:35], v0 offset:53760\n\tv_mfma_f32_16x16x32_bf16 v[84:87], v[44:47], a[12:15], v[84:87]\n\tv_mfma_f32_16x16x32_bf16 v[92:95], v[44:47], a[16:19], v[92:95]\n\ts_barrier\n\t" \
    "s_waitcnt vmcnt(28) lgkmcnt(8)\n\tv_mfma_f32_16x16x32_bf16 v[120:123], v[16:19], a[20:23], v[120:123]\n\tds_read_b64_tr_b16 v[36:37], v1 offset:53248\n\tds_read_b64_tr_b16 v[38:39], v1 offset:53760\n\tv_mfma_f32_16x16x32_bf16 v[128:131], v[16:19], a[24:27], v[128:131]\n\tbuffer_load_dwordx4 a[84:87], v8, s[8:11], s68 offen offset:1024 sc1\n\tv_mfma_f32_16x16x32_bf16 v[136:139], v[16:19], a[28:31], v[136:139]\n\ts_waitcnt lgkmcnt(8)\n\t" \
    "v_mfma_f32_16x16x32_bf16 v[124:127], v[20:23], a[20:23], v[124:127]\n\tds_read_b64_tr_b16 v[40:41], v2 offset:53248\n\tds_read_b64_tr_b16 v[42:43], v2 offset:53760\n\tv_mfma_f32_16x16x32_bf16 v[132:135], v[20:23], a[24:27], v[132:135]\n\tbuffer_load_dwordx4 a[88:91], v8, s[8:11], s68 offen offset:2048 sc1\n\ts_cmp_ge_u32 s43, 1\n\ts_cselect_b32 s69, s70, s43\n\ts_mul_i32 s69, s69, 294912\n\t" \
    "v_mfma_f32_16x16x32_bf16 v[140:143], v[20:23], a[28:31], v[140:143]\n\ts_add_u32 s69, s69, 5898240\n\ts_add_u32 m0, s54, 0\n\ts_waitcnt lgkmcnt(8)\n\tv_mfma_f32_16x16x32_bf16 v[144:147], v[24:27], a[20:23], v[144:147]\n\tds_read_b64_tr_b16 v[44:45], v3 offset:53248\n\tds_read_b64_tr_b16 v[46:47], v3 offset:53760\n\tv_mfma_f32_16x16x32_bf16 v[152:155], v[24:27], a[24:27], v[152:155]\n\t" \
    "buffer_load_dwordx4 v10, s[12:15], s69 offen lds\n\ts_mov_b32 s69, 6193152\n\ts_add_u32 m0, s54, 4096\n\tv_mfma_f32_16x16x32_bf16 v[160:163], v[24:27], a[28:31], v[160:163]\n\ts_waitcnt lgkmcnt(8)\n\tv_mfma_f32_16x16x32_bf16 v[148:151], v[28:31], a[20:23], v[148:151]\n\tds_read_b64_tr_b16 v[16:17], v0 offset:57344\n\tds_read_b64_tr_b16 v[18:19], v0 offset:57856\n\t" \
    "v_mfma_f32_16x16x32_bf16 v[156:159], v[28:31], a[24:27], v[156:159]\n\tbuffer_load_dwordx4 v10, s[12:15], s69 offen lds\n\ts_cmp_ge_u32 s43, 4\n\ts_cselect_b32 s68, s70, s43\n\ts_mul_i32 s68, s68, 36864\n\tv_mfma_f32_16x16x32_bf16 v[164:167], v[28:31], a[28:31], v[164:167]\n\ts_add_u32 s68, s68, s44\n\ts_add_u32 s68, s68, 626688\n\t" \
    "s_waitcnt lgkmcnt(8)\n\tv_mfma_f32_16x16x32_bf16 v[48:51], v[32:35], a[20:23], v[48:51]\n\tds_read_b64_tr_b16 v[20:21], v1 offset:57344\n\tds_read_b64_tr_b16 v[22:23], v1 offset:57856\n\tv_mfma_f32_16x16x32_bf16 v[56:59], v[32:35], a[24:27], v[56:59]\n\tbuffer_load_dwordx4 a[8:11], v8, s[8:11], s68 offen offset:0 sc1\n\tv_mfma_f32_16x16x32_bf16 v[64:67], v[32:35], a[28:31], v[64:67]\n\ts_waitcnt lgkmcnt(8)\n\t" \
    "v_mfma_f32_16x16x32_bf16 v[52:55], v[36:39], a[20:23], v[52:55]\n\tds_read_b64_tr_b16 v[24:25], v2 offset:57344\n\tds_read_b64_tr_b16 v[26:27], v2 offset:57856\n\tv_mfma_f32_16x16x32_bf16 v[60:63], v[36:39], a[24:27], v[60:63]\n\tv_mfma_f32_16x16x32_bf16 v[68:71], v[36:39], a[28:31], v[68:71]\n\ts_waitcnt lgkmcnt(8)\n\tv_mfma_f32_16x16x32_bf16 v[72:75], v[40:43], a[20:23], v[72:75]\n\tds_read_b64_tr_b16 v[28:29], v3 offset:57344\n\t" \
    "ds_read_b64_tr_b16 v[30:31], v3 offset:57856\n\tv_mfma_f32_16x16x32_bf16 v[80:83], v[40:43], a[24:27], v[80:83]\n\tbuffer_load_dwordx4 a[12:15], v8, s[8:11], s68 offen offset:1024 sc1\n\tv_mfma_f32_16x16x32_bf16 v[88:91], v[40:43], a[28:31], v[88:91]\n\ts_waitcnt lgkmcnt(8)\n\tv_mfma_f32_16x16x32_bf16 v[76:79], v[44:47], a[20:23], v[76:79]\n\tds_read_b64_tr_b16 v[32:33], v0 offset:61440\n\tds_read_b64_tr_b16 v[34:35], v0 offset:61952\n\t" \
    "v_mfma_f32_16x16x32_bf16 v[84:87], v[44:47], a[24:27], v[84:87]\n\tbuffer_load_dwordx4 a[16:19], v8, s[8:11], s68 offen offset:2048 sc1\n\ts_cmp_ge_u32 s43, 3\n\ts_cselect_b32 s68, s70, s43\n\ts_mul_i32 s68, s68, 36864\n\tv_mfma_f32_16x16x32_bf16 v[92:95], v[44:47], a[28:31], v[92:95]\n\ts_add_u32 s68, s68, s44\n\ts_add_u32 s68, s68, 663552\n\t" \
    "s_waitcnt vmcnt(32) lgkmcnt(8)\n\tv_mfma_f32_16x16x32_bf16 v[120:123], v[16:19], v[192:195], v[120:123]\n\tds_read_b64_tr_b16 v[36:37], v1 offset:61440\n\tds_read_b64_tr_b16 v[38:39], v1 offset:61952\n\tv_mfma_f32_16x16x32_bf16 v[128:131], v[16:19], v[196:199], v[128:131]\n\tbuffer_load_dwordx4 a[92:95], v8, s[8:11], s68 offen offset:0 sc1\n\tv_mfma_f32_16x16x32_bf16 v[136:139], v[16:19], v[200:203], v[136:139]\n\ts_waitcnt lgkmcnt(8)\n\t" \
    "v_mfma_f32_16x16x32_bf16 v[124:127], v[20:23], v[192:195], v[124:127]\n\tds_read_b64_tr_b16 v[40:41], v2 offset:61440\n\tds_read_b64_tr_b16 v[42:43], v2 offset:61952\n\tv_mfma_f32_16x16x32_bf16 v[132:135], v[20:23], v[196:199], v[132:135]\n\tv_mfma_f32_16x16x32_bf16 v[140:143], v[20:23], v[200:203], v[140:143]\n\ts_waitcnt lgkmcnt(8)\n\tv_mfma_f32_16x16x32_bf16 v[144:147], v[24:27], v[192:195], v[144:147]\n\tds_read_b64_tr_b16 v[44:45], v3 offset:61440\n\t" \
    "ds_read_b64_tr_b16 v[46:47], v3 offset:61952\n\tv_mfma_f32_16x16x32_bf16 v[152:155], v[24:27], v[196:199], v[152:155]\n\tbuffer_load_dwordx4 a[96:99], v8, s[8:11], s68 offen offset:1024 sc1\n\tv_mfma_f32_16x16x32_bf16 v[160:163], v[24:27], v[200:203], v[160:163]\n\ts_waitcnt lgkmcnt(8)\n\tv_mfma_f32_16x16x32_bf16 v[148:151], v[28:31], v[192:195], v[148:151]\n\tds_read_b64_tr_b16 v[16:17], v14 offset:0\n\tds_read_b64_tr_b16 v[18:19], v14 offset:512\n\t" \
    "v_mfma_f32_16x16x32_bf16 v[156:159], v[28:31], v[196:199], v[156:159]\n\tbuffer_load_dwordx4 a[100:103], v8, s[8:11], s68 offen offset:2048 sc1\n\ts_cmp_ge_u32 s43, 2\n\ts_cselect_b32 s68, s70, s43\n\ts_mul_i32 s68, s68, 36864\n\tv_mfma_f32_16x16x32_bf16 v[164:167], v[28:31], v[200:203], v[164:167]\n\ts_add_u32 s68, s68, s44\n\ts_add_u32 s68, s68, 700416\n\t" \
    "s_waitcnt lgkmcnt(8)\n\tv_mfma_f32_16x16x32_bf16 v[48:51], v[32:35], v[192:195], v[48:51]\n\tds_read_b64_tr_b16 v[20:21], v15 offset:0\n\tds_read_b64_tr_b16 v[22:23], v15 offset:512\n\tv_mfma_f32_16x16x32_bf16 v[56:59], v[32:35], v[196:199], v[56:59]\n\tbuffer_load_dwordx4 a[20:23], v8, s[8:11], s68 offen offset:0 sc1\n\tv_mfma_f32_16x16x32_bf16 v[64:67], v[32:35], v[200:203], v[64:67]\n\ts_waitcnt lgkmcnt(8)\n\t" \
    "v_mfma_f32_16x16x32_bf16 v[52:55], v[36:39], v[192:195], v[52:55]\n\tds_read_b64_tr_b16 v[24:25], v237 offset:0\n\tds_read_b64_tr_b16 v[26:27], v237 offset:512\n\tv_mfma_f32_16x16x32_bf16 v[60:63], v[36:39], v[196:199], v[60:63]\n\tv_mfma_f32_16x16x32_bf16 v[68:71], v[36:39], v[200:203], v[68:71]\n\ts_waitcnt lgkmcnt(8)\n\tv_mfma_f32_16x16x32_bf16 v[72:75], v[40:43], v[192:195], v[72:75]\n\tds_read_b64_tr_b16 v[28:29], v238 offset:0\n\t" \
    "ds_read_b64_tr_b16 v[30:31], v238 offset:512\n\tv_mfma_f32_16x16x32_bf16 v[80:83], v[40:43], v[196:199], v[80:83]\n\tbuffer_load_dwordx4 a[24:27], v8, s[8:11], s68 offen offset:1024 sc1\n\tv_mfma_f32_16x16x32_bf16 v[88:91], v[40:43], v[200:203], v[88:91]\n\ts_waitcnt lgkmcnt(8)\n\tv_mfma_f32_16x16x32_bf16 v[76:79], v[44:47], v[192:195], v[76:79]\n\tds_read_b64_tr_b16 v[32:33], v14 offset:4096\n\tds_read_b64_tr_b16 v[34:35], v14 offset:4608\n\t" \
    "v_mfma_f32_16x16x32_bf16 v[84:87], v[44:47], v[196:199], v[84:87]\n\tbuffer_load_dwordx4 a[28:31], v8, s[8:11], s68 offen offset:2048 sc1\n\ts_cmp_ge_u32 s43, 1\n\ts_cselect_b32 s68, s70, s43\n\ts_mul_i32 s68, s68, 36864\n\tv_mfma_f32_16x16x32_bf16 v[92:95], v[44:47], v[200:203], v[92:95]\n\ts_add_u32 s68, s68, s44\n\ts_add_u32 s68, s68, 737280\n\t" \
    "s_barrier\n\ts_waitcnt vmcnt(35) lgkmcnt(8)\n\tv_mfma_f32_16x16x32_bf16 v[120:123], v[16:19], a[32:35], v[120:123]\n\tds_read_b64_tr_b16 v[36:37], v15 offset:4096\n\tds_read_b64_tr_b16 v[38:39], v15 offset:4608\n\tv_mfma_f32_16x16x32_bf16 v[128:131], v[16:19], a[36:39], v[128:131]\n\tbuffer_load_dwordx4 a[104:107], v8, s[8:11], s68 offen offset:0 sc1\n\tv_mfma_f32_16x16x32_bf16 v[136:139], v[16:19], a[40:43], v[136:139]\n\t" \
    "s_waitcnt lgkmcnt(8)\n\tv_mfma_f32_16x16x32_bf16 v[124:127], v[20:23], a[32:35], v[124:127]\n\tds_read_b64_tr_b16 v[40:41], v237 offset:4096\n\tds_read_b64_tr_b16 v[42:43], v237 offset:4608\n\tv_mfma_f32_16x16x32_bf16 v[132:135], v[20:23], a[36:39], v[132:135]\n\tbuffer_load_dwordx4 a[108:111], v8, s[8:11], s68 offen offset:1024 sc1\n\tv_mfma_f32_16x16x32_bf16 v[140:143], v[20:23], a[40:43], v[140:143]\n\ts_waitcnt lgkmcnt(8)\n\t" \
    "v_mfma_f32_16x16x32_bf16 v[144:147], v[24:27], a[32:35], v[144:147]\n\tds_read_b64_tr_b16 v[44:45], v238 offset:4096\n\tds_read_b64_tr_b16 v[46:47], v238 offset:4608\n\tv_mfma_f32_16x16x32_bf16 v[152:155], v[24:27], a[36:39], v[152:155]\n\tbuffer_load_dwordx4 a[112:115], v8, s[8:11], s68 offen offset:2048 sc1\n\ts_mov_b32 s69, 6488064\n\ts_add_u32 m0, s54, 8192\n\tv_mfma_f32_16x16x32_bf16 v[160:163], v[24:27], a[40:43], v[160:163]\n\t" \
    "s_waitcnt lgkmcnt(8)\n\tv_mfma_f32_16x16x32_bf16 v[148:151], v[28:31], a[32:35], v[148:151]\n\tds_read_b64_tr_b16 v[16:17], v14 offset:8192\n\tds_read_b64_tr_b16 v[18:19], v14 offset:8704\n\tv_mfma_f32_16x16x32_bf16 v[156:159], v[28:31], a[36:39], v[156:159]\n\tv_mfma_f32_16x16x32_bf16 v[164:167], v[28:31], a[40:43], v[164:167]\n\ts_waitcnt lgkmcnt(8)\n\tv_mfma_f32_16x16x32_bf16 v[48:51], v[32:35], a[32:35], v[48:51]\n\t" \
    "ds_read_b64_tr_b16 v[20:21], v15 offset:8192\n\tds_read_b64_tr_b16 v[22:23], v15 offset:8704\n\tv_mfma_f32_16x16x32_bf16 v[56:59], v[32:35], a[36:39], v[56:59]\n\tbuffer_load_dwordx4 v10, s[12:15], s69 offen lds\n\ts_mov_b32 s69, 6782976\n\ts_add_u32 m0, s54, 12288\n\tv_mfma_f32_16x16x32_bf16 v[64:67], v[32:35], a[40:43], v[64:67]\n\ts_waitcnt lgkmcnt(8)\n\t" \
    "v_mfma_f32_16x16x32_bf16 v[52:55], v[36:39], a[32:35], v[52:55]\n\tds_read_b64_tr_b16 v[24:25], v237 offset:8192\n\tds_read_b64_tr_b16 v[26:27], v237 offset:8704\n\tv_mfma_f32_16x16x32_bf16 v[60:63], v[36:39], a[36:39], v[60:63]\n\tbuffer_load_dwordx4 v10, s[12:15], s69 offen lds\n\ts_mov_b32 s69, 7077888\n\ts_add_u32 m0, s54, 16384\n\tv_mfma_f32_16x16x32_bf16 v[68:71], v[36:39], a[40:43], v[68:71]\n\t" \
    "s_waitcnt lgkmcnt(8)\n\tv_mfma_f32_16x16x32_bf16 v[72:75], v[40:43], a[32:35], v[72:75]\n\tds_read_b64_tr_b16 v[28:29], v238 offset:8192\n\tds_read_b64_tr_b16 v[30:31], v238 offset:8704\n\tv_mfma_f32_16x16x32_bf16 v[80:83], v[40:43], a[36:39], v[80:83]\n\tbuffer_load_dwordx4 v10, s[12:15], s69 offen lds\n\ts_mov_b32 s69, 7372800\n\ts_add_u32 m0, s54, 20480\n\t" \
    "v_mfma_f32_16x16x32_bf16 v[88:91], v[40:43], a[40:43], v[88:91]\n\ts_waitcnt lgkmcnt(8)\n\tv_mfma_f32_16x16x32_bf16 v[76:79], v[44:47], a[32:35], v[76:79]\n\tds_read_b64_tr_b16 v[32:33], v14 offset:12288\n\tds_read_b64_tr_b16 v[34:35], v14 offset:12800\n\tv_mfma_f32_16x16x32_bf16 v[84:87], v[44:47], a[36:39], v[84:87]\n\tv_mfma_f32_16x16x32_bf16 v[92:95], v[44:47], a[40:43], v[92:95]\n\ts_waitcnt vmcnt(38) lgkmcnt(8)\n\t" \
    "v_mfma_f32_16x16x32_bf16 v[120:123], v[16:19], v[168:171], v[120:123]\n\tds_read_b64_tr_b16 v[36:37], v15 offset:12288\n\tds_read_b64_tr_b16 v[38:39], v15 offset:12800\n\tv_mfma_f32_16x16x32_bf16 v[128:131], v[16:19], v[172:175], v[128:131]\n\tbuffer_load_dwordx4 v10, s[12:15], s69 offen lds\n\ts_mov_b32 s69, 7667712\n\ts_add_u32 m0, s54, 24576\n\tv_mfma_f32_16x16x32_bf16 v[136:139], v[16:19], v[176:179], v[136:139]\n\t" \
    "s_waitcnt lgkmcnt(8)\n\tv_mfma_f32_16x16x32_bf16 v[124:127], v[20:23], v[168:171], v[124:127]\n\tds_read_b64_tr_b16 v[40:41], v237 offset:12288\n\tds_read_b64_tr_b16 v[42:43], v237 offset:12800\n\tv_mfma_f32_16x16x32_bf16 v[132:135], v[20:23], v[172:175], v[132:135]\n\tbuffer_load_dwordx4 v10, s[12:15], s69 offen lds\n\ts_mov_b32 s69, 7962624\n\ts_add_u32 m0, s54, 28672\n\t" \
    "v_mfma_f32_16x16x32_bf16 v[140:143], v[20:23], v[176:179], v[140:143]\n\ts_waitcnt lgkmcnt(8)\n\tv_mfma_f32_16x16x32_bf16 v[144:147], v[24:27], v[168:171], v[144:147]\n\tds_read_b64_tr_b16 v[44:45], v238 offset:12288\n\tds_read_b64_tr_b16 v[46:47], v238 offset:12800\n\tv_mfma_f32_16x16x32_bf16 v[152:155], v[24:27], v[172:175], v[152:155]\n\tbuffer_load_dwordx4 v10, s[12:15], s69 offen lds\n\ts_mov_b32 s69, 8257536\n\t" \
    "s_add_u32 m0, s54, 32768\n\tv_mfma_f32_16x16x32_bf16 v[160:163], v[24:27], v[176:179], v[160:163]\n\ts_waitcnt lgkmcnt(8)\n\tv_mfma_f32_16x16x32_bf16 v[148:151], v[28:31], v[168:171], v[148:151]\n\tds_read_b64_tr_b16 v[16:17], v14 offset:16384\n\tds_read_b64_tr_b16 v[18:19], v14 offset:16896\n\tv_mfma_f32_16x16x32_bf16 v[156:159], v[28:31], v[172:175], v[156:159]\n\tv_mfma_f32_16x16x32_bf16 v[164:167], v[28:31], v[176:179], v[164:167]\n\t" \
    "s_waitcnt lgkmcnt(8)\n\tv_mfma_f32_16x16x32_bf16 v[48:51], v[32:35], v[168:171], v[48:51]\n\tds_read_b64_tr_b16 v[20:21], v15 offset:16384\n\tds_read_b64_tr_b16 v[22:23], v15 offset:16896\n\tv_mfma_f32_16x16x32_bf16 v[56:59], v[32:35], v[172:175], v[56:59]\n\tbuffer_load_dwordx4 v10, s[12:15], s69 offen lds\n\ts_mov_b32 s69, 8552448\n\ts_add_u32 m0, s54, 36864\n\t" \
    "v_mfma_f32_16x16x32_bf16 v[64:67], v[32:35], v[176:179], v[64:67]\n\ts_waitcnt lgkmcnt(8)\n\tv_mfma_f32_16x16x32_bf16 v[52:55], v[36:39], v[168:171], v[52:55]\n\tds_read_b64_tr_b16 v[24:25], v237 offset:16384\n\tds_read_b64_tr_b16 v[26:27], v237 offset:16896\n\tv_mfma_f32_16x16x32_bf16 v[60:63], v[36:39], v[172:175], v[60:63]\n\tbuffer_load_dwordx4 v10, s[12:15], s69 offen lds\n\ts_add_u32 s68, s44, 774144\n\t" \
    "v_mfma_f32_16x16x32_bf16 v[68:71], v[36:39], v[176:179], v[68:71]\n\ts_waitcnt lgkmcnt(8)\n\tv_mfma_f32_16x16x32_bf16 v[72:75], v[40:43], v[168:171], v[72:75]\n\tds_read_b64_tr_b16 v[28:29], v238 offset:16384\n\tds_read_b64_tr_b16 v[30:31], v238 offset:16896\n\tv_mfma_f32_16x16x32_bf16 v[80:83], v[40:43], v[172:175], v[80:83]\n\tv_mfma_f32_16x16x32_bf16 v[88:91], v[40:43], v[176:179], v[88:91]\n\ts_waitcnt lgkmcnt(8)\n\t" \
    "v_mfma_f32_16x16x32_bf16 v[76:79], v[44:47], v[168:171], v[76:79]\n\tds_read_b64_tr_b16 v[32:33], v14 offset:20480\n\tds_read_b64_tr_b16 v[34:35], v14 offset:20992\n\tv_mfma_f32_16x16x32_bf16 v[84:87], v[44:47], v[172:175], v[84:87]\n\tbuffer_load_dwordx4 a[32:35], v8, s[8:11], s68 offen offset:0 sc1\n\tv_mfma_f32_16x16x32_bf16 v[92:95], v[44:47], v[176:179], v[92:95]\n\ts_barrier\n\ts_waitcnt vmcnt(41) lgkmcnt(8)\n\t" \
    "v_mfma_f32_16x16x32_bf16 v[120:123], v[16:19], a[44:47], v[120:123]\n\tds_read_b64_tr_b16 v[36:37], v15 offset:20480\n\tds_read_b64_tr_b16 v[38:39], v15 offset:20992\n\tv_mfma_f32_16x16x32_bf16 v[128:131], v[16:19], a[48:51], v[128:131]\n\tbuffer_load_dwordx4 a[36:39], v8, s[8:11], s68 offen offset:1024 sc1\n\tv_mfma_f32_16x16x32_bf16 v[136:139], v[16:19], a[52:55], v[136:139]\n\ts_waitcnt lgkmcnt(8)\n\tv_mfma_f32_16x16x32_bf16 v[124:127], v[20:23], a[44:47], v[124:127]\n\t" \
    "ds_read_b64_tr_b16 v[40:41], v237 offset:20480\n\tds_read_b64_tr_b16 v[42:43], v237 offset:20992\n\tv_mfma_f32_16x16x32_bf16 v[132:135], v[20:23], a[48:51], v[132:135]\n\tbuffer_load_dwordx4 a[40:43], v8, s[8:11], s68 offen offset:2048 sc1\n\ts_add_u32 s68, s44, 811008\n\tv_mfma_f32_16x16x32_bf16 v[140:143], v[20:23], a[52:55], v[140:143]\n\ts_waitcnt lgkmcnt(8)\n\tv_mfma_f32_16x16x32_bf16 v[144:147], v[24:27], a[44:47], v[144:147]\n\t" \
    "ds_read_b64_tr_b16 v[44:45], v238 offset:20480\n\tds_read_b64_tr_b16 v[46:47], v238 offset:20992\n\tv_mfma_f32_16x16x32_bf16 v[152:155], v[24:27], a[48:51], v[152:155]\n\tbuffer_load_dwordx4 v[168:171], v8, s[8:11], s68 offen offset:0 sc1\n\tv_mfma_f32_16x16x32_bf16 v[160:163], v[24:27], a[52:55], v[160:163]\n\ts_waitcnt lgkmcnt(8)\n\tv_mfma_f32_16x16x32_bf16 v[148:151], v[28:31], a[44:47], v[148:151]\n\tds_read_b64_tr_b16 v[16:17], v14 offset:24576\n\t" \
    "ds_read_b64_tr_b16 v[18:19], v14 offset:25088\n\tv_mfma_f32_16x16x32_bf16 v[156:159], v[28:31], a[48:51], v[156:159]\n\tbuffer_load_dwordx4 v[172:175], v8, s[8:11], s68 offen offset:1024 sc1\n\tv_mfma_f32_16x16x32_bf16 v[164:167], v[28:31], a[52:55], v[164:167]\n\ts_waitcnt lgkmcnt(8)\n\tv_mfma_f32_16x16x32_bf16 v[48:51], v[32:35], a[44:47], v[48:51]\n\tds_read_b64_tr_b16 v[20:21], v15 offset:24576\n\tds_read_b64_tr_b16 v[22:23], v15 offset:25088\n\t" \
    "v_mfma_f32_16x16x32_bf16 v[56:59], v[32:35], a[48:51], v[56:59]\n\tv_mfma_f32_16x16x32_bf16 v[64:67], v[32:35], a[52:55], v[64:67]\n\ts_waitcnt lgkmcnt(8)\n\tv_mfma_f32_16x16x32_bf16 v[52:55], v[36:39], a[44:47], v[52:55]\n\tds_read_b64_tr_b16 v[24:25], v237 offset:24576\n\tds_read_b64_tr_b16 v[26:27], v237 offset:25088\n\tv_mfma_f32_16x16x32_bf16 v[60:63], v[36:39], a[48:51], v[60:63]\n\tbuffer_load_dwordx4 v[176:179], v8, s[8:11], s68 offen offset:2048 sc1\n\t" \
    "s_add_u32 s68, s44, 847872\n\tv_mfma_f32_16x16x32_bf16 v[68:71], v[36:39], a[52:55], v[68:71]\n\ts_waitcnt lgkmcnt(8)\n\tv_mfma_f32_16x16x32_bf16 v[72:75], v[40:43], a[44:47], v[72:75]\n\tds_read_b64_tr_b16 v[28:29], v238 offset:24576\n\tds_read_b64_tr_b16 v[30:31], v238 offset:25088\n\tv_mfma_f32_16x16x32_bf16 v[80:83], v[40:43], a[48:51], v[80:83]\n\tbuffer_load_dwordx4 a[116:119], v8, s[8:11], s68 offen offset:0 sc1\n\t" \
    "v_mfma_f32_16x16x32_bf16 v[88:91], v[40:43], a[52:55], v[88:91]\n\ts_waitcnt lgkmcnt(8)\n\tv_mfma_f32_16x16x32_bf16 v[76:79], v[44:47], a[44:47], v[76:79]\n\tds_read_b64_tr_b16 v[32:33], v14 offset:28672\n\tds_read_b64_tr_b16 v[34:35], v14 offset:29184\n\tv_mfma_f32_16x16x32_bf16 v[84:87], v[44:47], a[48:51], v[84:87]\n\tbuffer_load_dwordx4 a[120:123], v8, s[8:11], s68 offen offset:1024 sc1\n\tv_mfma_f32_16x16x32_bf16 v[92:95], v[44:47], a[52:55], v[92:95]\n\t" \
    "s_waitcnt vmcnt(45) lgkmcnt(8)\n\tv_mfma_f32_16x16x32_bf16 v[120:123], v[16:19], v[180:183], v[120:123]\n\tds_read_b64_tr_b16 v[36:37], v15 offset:28672\n\tds_read_b64_tr_b16 v[38:39], v15 offset:29184\n\tv_mfma_f32_16x16x32_bf16 v[128:131], v[16:19], v[184:187], v[128:131]\n\tv_mfma_f32_16x16x32_bf16 v[136:139], v[16:19], v[188:191], v[136:139]\n\ts_waitcnt lgkmcnt(8)\n\tv_mfma_f32_16x16x32_bf16 v[124:127], v[20:23], v[180:183], v[124:127]\n\t" \
    "ds_read_b64_tr_b16 v[40:41], v14 offset:32768\n\tds_read_b64_tr_b16 v[42:43], v14 offset:33280\n\tv_mfma_f32_16x16x32_bf16 v[132:135], v[20:23], v[184:187], v[132:135]\n\tbuffer_load_dwordx4 a[124:127], v8, s[8:11], s68 offen offset:2048 sc1\n\ts_add_u32 s68, s44, 884736\n\tv_mfma_f32_16x16x32_bf16 v[140:143], v[20:23], v[188:191], v[140:143]\n\ts_waitcnt lgkmcnt(8)\n\tv_mfma_f32_16x16x32_bf16 v[144:147], v[24:27], v[180:183], v[144:147]\n\t" \
    "ds_read_b64_tr_b16 v[44:45], v15 offset:32768\n\tds_read_b64_tr_b16 v[46:47], v15 offset:33280\n\tv_mfma_f32_16x16x32_bf16 v[152:155], v[24:27], v[184:187], v[152:155]\n\tbuffer_load_dwordx4 a[44:47], v8, s[8:11], s68 offen offset:0 sc1\n\tv_mfma_f32_16x16x32_bf16 v[160:163], v[24:27], v[188:191], v[160:163]\n\ts_waitcnt lgkmcnt(8)\n\tv_mfma_f32_16x16x32_bf16 v[148:151], v[28:31], v[180:183], v[148:151]\n\tds_read_b64_tr_b16 v[16:17], v237 offset:32768\n\t" \
    "ds_read_b64_tr_b16 v[18:19], v237 offset:33280\n\tv_mfma_f32_16x16x32_bf16 v[156:159], v[28:31], v[184:187], v[156:159]\n\tv_mfma_f32_16x16x32_bf16 v[164:167], v[28:31], v[188:191], v[164:167]\n\ts_waitcnt lgkmcnt(8)\n\tv_mfma_f32_16x16x32_bf16 v[48:51], v[32:35], v[180:183], v[48:51]\n\tds_read_b64_tr_b16 v[20:21], v238 offset:32768\n\tds_read_b64_tr_b16 v[22:23], v238 offset:33280\n\tv_mfma_f32_16x16x32_bf16 v[56:59], v[32:35], v[184:187], v[56:59]\n\t" \
    "buffer_load_dwordx4 a[48:51], v8, s[8:11], s68 offen offset:1024 sc1\n\tv_mfma_f32_16x16x32_bf16 v[64:67], v[32:35], v[188:191], v[64:67]\n\ts_waitcnt lgkmcnt(8)\n\tv_mfma_f32_16x16x32_bf16 v[52:55], v[36:39], v[180:183], v[52:55]\n\tds_read_b64_tr_b16 v[24:25], v14 offset:36864\n\tds_read_b64_tr_b16 v[26:27], v14 offset:37376\n\tv_mfma_f32_16x16x32_bf16 v[60:63], v[36:39], v[184:187], v[60:63]\n\tbuffer_load_dwordx4 a[52:55], v8, s[8:11], s68 offen offset:2048 sc1\n\t" \
    "s_add_u32 s68, s44, 921600\n\tv_mfma_f32_16x16x32_bf16 v[68:71], v[36:39], v[188:191], v[68:71]\n\ts_waitcnt vmcnt(46) lgkmcnt(8)\n\tv_mfma_f32_16x16x32_bf16 v[120:123], v[40:43], a[56:59], v[120:123]\n\tds_read_b64_tr_b16 v[28:29], v15 offset:36864\n\tds_read_b64_tr_b16 v[30:31], v15 offset:37376\n\tv_mfma_f32_16x16x32_bf16 v[128:131], v[40:43], a[60:63], v[128:131]\n\tbuffer_load_dwordx4 a[128:131], v8, s[8:11], s68 offen offset:0 sc1\n\t" \
    "v_mfma_f32_16x16x32_bf16 v[136:139], v[40:43], a[64:67], v[136:139]\n\ts_waitcnt lgkmcnt(8)\n\tv_mfma_f32_16x16x32_bf16 v[124:127], v[44:47], a[56:59], v[124:127]\n\tds_read_b64_tr_b16 v[32:33], v237 offset:36864\n\tds_read_b64_tr_b16 v[34:35], v237 offset:37376\n\tv_mfma_f32_16x16x32_bf16 v[132:135], v[44:47], a[60:63], v[132:135]\n\tv_mfma_f32_16x16x32_bf16 v[140:143], v[44:47], a[64:67], v[140:143]\n\ts_barrier\n\t" \
    "s_waitcnt lgkmcnt(8)\n\tv_mfma_f32_16x16x32_bf16 v[144:147], v[16:19], a[56:59], v[144:147]\n\tds_read_b64_tr_b16 v[36:37], v238 offset:36864\n\tds_read_b64_tr_b16 v[38:39], v238 offset:37376\n\tv_mfma_f32_16x16x32_bf16 v[152:155], v[16:19], a[60:63], v[152:155]\n\tbuffer_load_dwordx4 a[132:135], v8, s[8:11], s68 offen offset:1024 sc1\n\tv_mfma_f32_16x16x32_bf16 v[160:163], v[16:19], a[64:67], v[160:163]\n\ts_waitcnt lgkmcnt(8)\n\t" \
    "v_mfma_f32_16x16x32_bf16 v[148:151], v[20:23], a[56:59], v[148:151]\n\tds_read_b64_tr_b16 v[40:41], v14 offset:40960\n\tds_read_b64_tr_b16 v[42:43], v14 offset:41472\n\tv_mfma_f32_16x16x32_bf16 v[156:159], v[20:23], a[60:63], v[156:159]\n\tbuffer_load_dwordx4 a[136:139], v8, s[8:11], s68 offen offset:2048 sc1\n\ts_add_u32 s68, s44, 958464\n\tv_mfma_f32_16x16x32_bf16 v[164:167], v[20:23], a[64:67], v[164:167]\n\ts_waitcnt vmcnt(46) lgkmcnt(8)\n\t" \
    "v_mfma_f32_16x16x32_bf16 v[120:123], v[24:27], v[204:207], v[120:123]\n\tds_read_b64_tr_b16 v[44:45], v15 offset:40960\n\tds_read_b64_tr_b16 v[46:47], v15 offset:41472\n\tv_mfma_f32_16x16x32_bf16 v[128:131], v[24:27], v[208:211], v[128:131]\n\tbuffer_load_dwordx4 a[140:143], v8, s[8:11], s68 offen offset:0 sc1\n\tv_mfma_f32_16x16x32_bf16 v[136:139], v[24:27], v[212:215], v[136:139]\n\ts_waitcnt lgkmcnt(8)\n\tv_mfma_f32_16x16x32_bf16 v[124:127], v[28:31], v[204:207], v[124:127]\n\t" \
    "ds_read_b64_tr_b16 v[16:17], v237 offset:40960\n\tds_read_b64_tr_b16 v[18:19], v237 offset:41472\n\tv_mfma_f32_16x16x32_bf16 v[132:135], v[28:31], v[208:211], v[132:135]\n\tbuffer_load_dwordx4 a[144:147], v8, s[8:11], s68 offen offset:1024 sc1\n\tv_mfma_f32_16x16x32_bf16 v[140:143], v[28:31], v[212:215], v[140:143]\n\ts_waitcnt lgkmcnt(8)\n\tv_mfma_f32_16x16x32_bf16 v[144:147], v[32:35], v[204:207], v[144:147]\n\tds_read_b64_tr_b16 v[20:21], v238 offset:40960\n\t" \
    "ds_read_b64_tr_b16 v[22:23], v238 offset:41472\n\tv_mfma_f32_16x16x32_bf16 v[152:155], v[32:35], v[208:211], v[152:155]\n\tbuffer_load_dwordx4 a[148:151], v8, s[8:11], s68 offen offset:2048 sc1\n\ts_mov_b32 s69, 8847360\n\ts_add_u32 m0, s54, 40960\n\tv_mfma_f32_16x16x32_bf16 v[160:163], v[32:35], v[212:215], v[160:163]\n\ts_waitcnt lgkmcnt(8)\n\tv_mfma_f32_16x16x32_bf16 v[148:151], v[36:39], v[204:207], v[148:151]\n\t" \
    "ds_read_b64_tr_b16 v[24:25], v14 offset:45056\n\tds_read_b64_tr_b16 v[26:27], v14 offset:45568\n\tv_mfma_f32_16x16x32_bf16 v[156:159], v[36:39], v[208:211], v[156:159]\n\tv_mfma_f32_16x16x32_bf16 v[164:167], v[36:39], v[212:215], v[164:167]\n\ts_waitcnt vmcnt(46) lgkmcnt(8)\n\tv_mfma_f32_16x16x32_bf16 v[120:123], v[40:43], a[68:71], v[120:123]\n\tds_read_b64_tr_b16 v[28:29], v15 offset:45056\n\tds_read_b64_tr_b16 v[30:31], v15 offset:45568\n\t" \
    "v_mfma_f32_16x16x32_bf16 v[128:131], v[40:43], a[72:75], v[128:131]\n\tbuffer_load_dwordx4 v10, s[12:15], s69 offen lds\n\ts_mov_b32 s69, 9142272\n\ts_add_u32 m0, s54, 45056\n\tv_mfma_f32_16x16x32_bf16 v[136:139], v[40:43], a[76:79], v[136:139]\n\ts_waitcnt lgkmcnt(8)\n\tv_mfma_f32_16x16x32_bf16 v[124:127], v[44:47], a[68:71], v[124:127]\n\tds_read_b64_tr_b16 v[32:33], v237 offset:45056\n\t" \
    "ds_read_b64_tr_b16 v[34:35], v237 offset:45568\n\tv_mfma_f32_16x16x32_bf16 v[132:135], v[44:47], a[72:75], v[132:135]\n\tbuffer_load_dwordx4 v10, s[12:15], s69 offen lds\n\ts_cmp_ge_u32 s43, 9\n\ts_cselect_b32 s69, s70, s43\n\ts_mul_i32 s69, s69, 294912\n\tv_mfma_f32_16x16x32_bf16 v[140:143], v[44:47], a[76:79], v[140:143]\n\ts_add_u32 s69, s69, 3538944\n\t" \
    "s_add_u32 m0, s54, 49152\n\ts_waitcnt lgkmcnt(8)\n\tv_mfma_f32_16x16x32_bf16 v[144:147], v[16:19], a[68:71], v[144:147]\n\tds_read_b64_tr_b16 v[36:37], v238 offset:45056\n\tds_read_b64_tr_b16 v[38:39], v238 offset:45568\n\tv_mfma_f32_16x16x32_bf16 v[152:155], v[16:19], a[72:75], v[152:155]\n\tbuffer_load_dwordx4 v11, s[12:15], s69 offen lds\n\ts_cmp_ge_u32 s43, 8\n\t" \
    "s_cselect_b32 s69, s70, s43\n\ts_mul_i32 s69, s69, 294912\n\tv_mfma_f32_16x16x32_bf16 v[160:163], v[16:19], a[76:79], v[160:163]\n\ts_add_u32 s69, s69, 3833856\n\ts_add_u32 m0, s54, 53248\n\ts_waitcnt lgkmcnt(8)\n\tv_mfma_f32_16x16x32_bf16 v[148:151], v[20:23], a[68:71], v[148:151]\n\tds_read_b64_tr_b16 v[40:41], v14 offset:49152\n\t" \
    "ds_read_b64_tr_b16 v[42:43], v14 offset:49664\n\tv_mfma_f32_16x16x32_bf16 v[156:159], v[20:23], a[72:75], v[156:159]\n\tv_mfma_f32_16x16x32_bf16 v[164:167], v[20:23], a[76:79], v[164:167]\n\ts_waitcnt vmcnt(46) lgkmcnt(8)\n\tv_mfma_f32_16x16x32_bf16 v[120:123], v[24:27], v[216:219], v[120:123]\n\tds_read_b64_tr_b16 v[44:45], v15 offset:49152\n\tds_read_b64_tr_b16 v[46:47], v15 offset:49664\n\tv_mfma_f32_16x16x32_bf16 v[128:131], v[24:27], a[0:3], v[128:131]\n\t" \
    "buffer_load_dwordx4 v11, s[12:15], s69 offen lds\n\ts_cmp_ge_u32 s43, 7\n\ts_cselect_b32 s69, s70, s43\n\ts_mul_i32 s69, s69, 294912\n\tv_mfma_f32_16x16x32_bf16 v[136:139], v[24:27], a[4:7], v[136:139]\n\ts_add_u32 s69, s69, 4128768\n\ts_add_u32 m0, s54, 57344\n\ts_waitcnt lgkmcnt(8)\n\t" \
    "v_mfma_f32_16x16x32_bf16 v[124:127], v[28:31], v[216:219], v[124:127]\n\tds_read_b64_tr_b16 v[16:17], v237 offset:49152\n\tds_read_b64_tr_b16 v[18:19], v237 offset:49664\n\tv_mfma_f32_16x16x32_bf16 v[132:135], v[28:31], a[0:3], v[132:135]\n\tbuffer_load_dwordx4 v11, s[12:15], s69 offen lds\n\ts_cmp_ge_u32 s43, 6\n\ts_cselect_b32 s69, s70, s43\n\ts_mul_i32 s69, s69, 294912\n\t" \
    "v_mfma_f32_16x16x32_bf16 v[140:143], v[28:31], a[4:7], v[140:143]\n\ts_add_u32 s69, s69, 4423680\n\ts_add_u32 m0, s54, 61440\n\ts_waitcnt lgkmcnt(8)\n\tv_mfma_f32_16x16x32_bf16 v[144:147], v[32:35], v[216:219], v[144:147]\n\tds_read_b64_tr_b16 v[20:21], v238 offset:49152\n\tds_read_b64_tr_b16 v[22:23], v238 offset:49664\n\tv_mfma_f32_16x16x32_bf16 v[152:155], v[32:35], a[0:3], v[152:155]\n\t" \
    "buffer_load_dwordx4 v11, s[12:15], s69 offen lds\n\ts_cmp_ge_u32 s43, 5\n\ts_cselect_b32 s69, s70, s43\n\ts_mul_i32 s69, s69, 294912\n\tv_mfma_f32_16x16x32_bf16 v[160:163], v[32:35], a[4:7], v[160:163]\n\ts_add_u32 s69, s69, 4718592\n\ts_add_u32 m0, s54, 65536\n\ts_waitcnt lgkmcnt(8)\n\t" \
    "v_mfma_f32_16x16x32_bf16 v[148:151], v[36:39], v[216:219], v[148:151]\n\tds_read_b64_tr_b16 v[24:25], v14 offset:53248\n\tds_read_b64_tr_b16 v[26:27], v14 offset:53760\n\tv_mfma_f32_16x16x32_bf16 v[156:159], v[36:39], a[0:3], v[156:159]\n\tv_mfma_f32_16x16x32_bf16 v[164:167], v[36:39], a[4:7], v[164:167]\n\ts_waitcnt vmcnt(46) lgkmcnt(8)\n\tv_mfma_f32_16x16x32_bf16 v[120:123], v[40:43], a[80:83], v[120:123]\n\tds_read_b64_tr_b16 v[28:29], v15 offset:53248\n\t" \
    "ds_read_b64_tr_b16 v[30:31], v15 offset:53760\n\tv_mfma_f32_16x16x32_bf16 v[128:131], v[40:43], a[84:87], v[128:131]\n\tbuffer_load_dwordx4 v11, s[12:15], s69 offen lds\n\ts_cmp_ge_u32 s43, 4\n\ts_cselect_b32 s69, s70, s43\n\ts_mul_i32 s69, s69, 294912\n\tv_mfma_f32_16x16x32_bf16 v[136:139], v[40:43], a[88:91], v[136:139]\n\ts_add_u32 s69, s69, 5013504\n\t" \
    "s_add_u32 m0, s54, 69632\n\ts_waitcnt lgkmcnt(8)\n\tv_mfma_f32_16x16x32_bf16 v[124:127], v[44:47], a[80:83], v[124:127]\n\tds_read_b64_tr_b16 v[32:33], v237 offset:53248\n\tds_read_b64_tr_b16 v[34:35], v237 offset:53760\n\tv_mfma_f32_16x16x32_bf16 v[132:135], v[44:47], a[84:87], v[132:135]\n\tbuffer_load_dwordx4 v11, s[12:15], s69 offen lds\n\ts_add_u32 s68, s44, 995328\n\t" \
    "v_mfma_f32_16x16x32_bf16 v[140:143], v[44:47], a[88:91], v[140:143]\n\ts_waitcnt vmcnt(46)\n\ts_barrier\n\ts_waitcnt lgkmcnt(8)\n\tv_mfma_f32_16x16x32_bf16 v[144:147], v[16:19], a[80:83], v[144:147]\n\tds_read_b64_tr_b16 v[36:37], v238 offset:53248\n\tds_read_b64_tr_b16 v[38:39], v238 offset:53760\n\tv_mfma_f32_16x16x32_bf16 v[152:155], v[16:19], a[84:87], v[152:155]\n\t" \
    "buffer_load_dwordx4 a[152:155], v8, s[8:11], s68 offen offset:0 sc1\n\tv_mfma_f32_16x16x32_bf16 v[160:163], v[16:19], a[88:91], v[160:163]\n\ts_waitcnt lgkmcnt(8)\n\tv_mfma_f32_16x16x32_bf16 v[148:151], v[20:23], a[80:83], v[148:151]\n\tds_read_b64_tr_b16 v[40:41], v14 offset:57344\n\tds_read_b64_tr_b16 v[42:43], v14 offset:57856\n\tv_mfma_f32_16x16x32_bf16 v[156:159], v[20:23], a[84:87], v[156:159]\n\tbuffer_load_dwordx4 a[156:159], v8, s[8:11], s68 offen offset:1024 sc1\n\t" \
    "v_mfma_f32_16x16x32_bf16 v[164:167], v[20:23], a[88:91], v[164:167]\n\ts_waitcnt vmcnt(45) lgkmcnt(8)\n\tv_mfma_f32_16x16x32_bf16 v[120:123], v[24:27], a[8:11], v[120:123]\n\tds_read_b64_tr_b16 v[44:45], v15 offset:57344\n\tds_read_b64_tr_b16 v[46:47], v15 offset:57856\n\tv_mfma_f32_16x16x32_bf16 v[128:131], v[24:27], a[12:15], v[128:131]\n\tbuffer_load_dwordx4 a[160:163], v8, s[8:11], s68 offen offset:2048 sc1\n\ts_add_u32 s68, s44, 1032192\n\t" \
    "v_mfma_f32_16x16x32_bf16 v[136:139], v[24:27], a[16:19], v[136:139]\n\ts_waitcnt lgkmcnt(8)\n\tv_mfma_f32_16x16x32_bf16 v[124:127], v[28:31], a[8:11], v[124:127]\n\tds_read_b64_tr_b16 v[16:17], v237 offset:57344\n\tds_read_b64_tr_b16 v[18:19], v237 offset:57856\n\tv_mfma_f32_16x16x32_bf16 v[132:135], v[28:31], a[12:15], v[132:135]\n\tbuffer_load_dwordx4 a[164:167], v8, s[8:11], s68 offen offset:0 sc1\n\tv_mfma_f32_16x16x32_bf16 v[140:143], v[28:31], a[16:19], v[140:143]\n\t" \
    "s_waitcnt lgkmcnt(8)\n\tv_mfma_f32_16x16x32_bf16 v[144:147], v[32:35], a[8:11], v[144:147]\n\tds_read_b64_tr_b16 v[20:21], v238 offset:57344\n\tds_read_b64_tr_b16 v[22:23], v238 offset:57856\n\tv_mfma_f32_16x16x32_bf16 v[152:155], v[32:35], a[12:15], v[152:155]\n\tv_mfma_f32_16x16x32_bf16 v[160:163], v[32:35], a[16:19], v[160:163]\n\ts_waitcnt lgkmcnt(8)\n\tv_mfma_f32_16x16x32_bf16 v[148:151], v[36:39], a[8:11], v[148:151]\n\t" \
    "ds_read_b64_tr_b16 v[24:25], v14 offset:61440\n\tds_read_b64_tr_b16 v[26:27], v14 offset:61952\n\tv_mfma_f32_16x16x32_bf16 v[156:159], v[36:39], a[12:15], v[156:159]\n\tbuffer_load_dwordx4 a[168:171], v8, s[8:11], s68 offen offset:1024 sc1\n\tv_mfma_f32_16x16x32_bf16 v[164:167], v[36:39], a[16:19], v[164:167]\n\ts_waitcnt vmcnt(45) lgkmcnt(8)\n\tv_mfma_f32_16x16x32_bf16 v[120:123], v[40:43], a[92:95], v[120:123]\n\tds_read_b64_tr_b16 v[28:29], v15 offset:61440\n\t" \
    "ds_read_b64_tr_b16 v[30:31], v15 offset:61952\n\tv_mfma_f32_16x16x32_bf16 v[128:131], v[40:43], a[96:99], v[128:131]\n\tbuffer_load_dwordx4 a[172:175], v8, s[8:11], s68 offen offset:2048 sc1\n\ts_add_u32 s68, s44, 1069056\n\tv_mfma_f32_16x16x32_bf16 v[136:139], v[40:43], a[100:103], v[136:139]\n\ts_waitcnt lgkmcnt(8)\n\tv_mfma_f32_16x16x32_bf16 v[124:127], v[44:47], a[92:95], v[124:127]\n\tds_read_b64_tr_b16 v[32:33], v237 offset:61440\n\t" \
    "ds_read_b64_tr_b16 v[34:35], v237 offset:61952\n\tv_mfma_f32_16x16x32_bf16 v[132:135], v[44:47], a[96:99], v[132:135]\n\tv_mfma_f32_16x16x32_bf16 v[140:143], v[44:47], a[100:103], v[140:143]\n\ts_waitcnt lgkmcnt(8)\n\tv_mfma_f32_16x16x32_bf16 v[144:147], v[16:19], a[92:95], v[144:147]\n\tds_read_b64_tr_b16 v[36:37], v238 offset:61440\n\tds_read_b64_tr_b16 v[38:39], v238 offset:61952\n\tv_mfma_f32_16x16x32_bf16 v[152:155], v[16:19], a[96:99], v[152:155]\n\t" \
    "buffer_load_dwordx4 a[176:179], v8, s[8:11], s68 offen offset:0 sc1\n\tv_mfma_f32_16x16x32_bf16 v[160:163], v[16:19], a[100:103], v[160:163]\n\ts_waitcnt lgkmcnt(8)\n\tv_mfma_f32_16x16x32_bf16 v[148:151], v[20:23], a[92:95], v[148:151]\n\tds_read_b64_tr_b16 v[40:41], v0 offset:0\n\tds_read_b64_tr_b16 v[42:43], v0 offset:512\n\tv_mfma_f32_16x16x32_bf16 v[156:159], v[20:23], a[96:99], v[156:159]\n\tbuffer_load_dwordx4 a[180:183], v8, s[8:11], s68 offen offset:1024 sc1\n\t" \
    "v_mfma_f32_16x16x32_bf16 v[164:167], v[20:23], a[100:103], v[164:167]\n\ts_waitcnt vmcnt(45) lgkmcnt(8)\n\tv_mfma_f32_16x16x32_bf16 v[120:123], v[24:27], a[20:23], v[120:123]\n\tds_read_b64_tr_b16 v[44:45], v1 offset:0\n\tds_read_b64_tr_b16 v[46:47], v1 offset:512\n\tv_mfma_f32_16x16x32_bf16 v[128:131], v[24:27], a[24:27], v[128:131]\n\tbuffer_load_dwordx4 a[184:187], v8, s[8:11], s68 offen offset:2048 sc1\n\ts_add_u32 s68, s44, 1105920\n\t" \
    "v_mfma_f32_16x16x32_bf16 v[136:139], v[24:27], a[28:31], v[136:139]\n\ts_waitcnt lgkmcnt(8)\n\tv_mfma_f32_16x16x32_bf16 v[124:127], v[28:31], a[20:23], v[124:127]\n\tds_read_b64_tr_b16 v[16:17], v2 offset:0\n\tds_read_b64_tr_b16 v[18:19], v2 offset:512\n\tv_mfma_f32_16x16x32_bf16 v[132:135], v[28:31], a[24:27], v[132:135]\n\tv_mfma_f32_16x16x32_bf16 v[140:143], v[28:31], a[28:31], v[140:143]\n\ts_waitcnt lgkmcnt(8)\n\t" \
    "v_mfma_f32_16x16x32_bf16 v[144:147], v[32:35], a[20:23], v[144:147]\n\tds_read_b64_tr_b16 v[20:21], v3 offset:0\n\tds_read_b64_tr_b16 v[22:23], v3 offset:512\n\tv_mfma_f32_16x16x32_bf16 v[152:155], v[32:35], a[24:27], v[152:155]\n\tbuffer_load_dwordx4 a[188:191], v8, s[8:11], s68 offen offset:0 sc1\n\tv_mfma_f32_16x16x32_bf16 v[160:163], v[32:35], a[28:31], v[160:163]\n\ts_waitcnt lgkmcnt(8)\n\tv_mfma_f32_16x16x32_bf16 v[148:151], v[36:39], a[20:23], v[148:151]\n\t" \
    "ds_read_b64_tr_b16 v[24:25], v0 offset:4096\n\tds_read_b64_tr_b16 v[26:27], v0 offset:4608\n\tv_mfma_f32_16x16x32_bf16 v[156:159], v[36:39], a[24:27], v[156:159]\n\tbuffer_load_dwordx4 a[192:195], v8, s[8:11], s68 offen offset:1024 sc1\n\tv_mfma_f32_16x16x32_bf16 v[164:167], v[36:39], a[28:31], v[164:167]\n\ts_waitcnt vmcnt(45) lgkmcnt(8)\n\tv_mfma_f32_16x16x32_bf16 v[120:123], v[40:43], a[104:107], v[120:123]\n\tds_read_b64_tr_b16 v[28:29], v1 offset:4096\n\t" \
    "ds_read_b64_tr_b16 v[30:31], v1 offset:4608\n\tv_mfma_f32_16x16x32_bf16 v[128:131], v[40:43], a[108:111], v[128:131]\n\tbuffer_load_dwordx4 a[196:199], v8, s[8:11], s68 offen offset:2048 sc1\n\ts_add_u32 s68, s44, 1142784\n\tv_mfma_f32_16x16x32_bf16 v[136:139], v[40:43], a[112:115], v[136:139]\n\ts_waitcnt lgkmcnt(8)\n\tv_mfma_f32_16x16x32_bf16 v[124:127], v[44:47], a[104:107], v[124:127]\n\tds_read_b64_tr_b16 v[32:33], v2 offset:4096\n\t" \
    "ds_read_b64_tr_b16 v[34:35], v2 offset:4608\n\tv_mfma_f32_16x16x32_bf16 v[132:135], v[44:47], a[108:111], v[132:135]\n\tv_mfma_f32_16x16x32_bf16 v[140:143], v[44:47], a[112:115], v[140:143]\n\ts_waitcnt vmcnt(38)\n\ts_barrier\n\ts_waitcnt lgkmcnt(8)\n\tv_mfma_f32_16x16x32_bf16 v[144:147], v[16:19], a[104:107], v[144:147]\n\tds_read_b64_tr_b16 v[36:37], v3 offset:4096\n\t" \
    "ds_read_b64_tr_b16 v[38:39], v3 offset:4608\n\tv_mfma_f32_16x16x32_bf16 v[152:155], v[16:19], a[108:111], v[152:155]\n\tbuffer_load_dwordx4 a[200:203], v8, s[8:11], s68 offen offset:0 sc1\n\tv_mfma_f32_16x16x32_bf16 v[160:163], v[16:19], a[112:115], v[160:163]\n\ts_waitcnt lgkmcnt(8)\n\tv_mfma_f32_16x16x32_bf16 v[148:151], v[20:23], a[104:107], v[148:151]\n\tds_read_b64_tr_b16 v[40:41], v0 offset:8192\n\tds_read_b64_tr_b16 v[42:43], v0 offset:8704\n\t" \
    "v_mfma_f32_16x16x32_bf16 v[156:159], v[20:23], a[108:111], v[156:159]\n\tbuffer_load_dwordx4 a[204:207], v8, s[8:11], s68 offen offset:1024 sc1\n\tv_mfma_f32_16x16x32_bf16 v[164:167], v[20:23], a[112:115], v[164:167]\n\ts_waitcnt vmcnt(37) lgkmcnt(8)\n\tv_mfma_f32_16x16x32_bf16 v[120:123], v[24:27], a[32:35], v[120:123]\n\tds_read_b64_tr_b16 v[44:45], v1 offset:8192\n\tds_read_b64_tr_b16 v[46:47], v1 offset:8704\n\tv_mfma_f32_16x16x32_bf16 v[128:131], v[24:27], a[36:39], v[128:131]\n\t" \
    "buffer_load_dwordx4 a[208:211], v8, s[8:11], s68 offen offset:2048 sc1\n\ts_cmp_ge_u32 s43, 3\n\ts_cselect_b32 s69, s70, s43\n\ts_mul_i32 s69, s69, 294912\n\tv_mfma_f32_16x16x32_bf16 v[136:139], v[24:27], a[40:43], v[136:139]\n\ts_add_u32 s69, s69, 5308416\n\ts_add_u32 m0, s54, 73728\n\ts_waitcnt lgkmcnt(8)\n\t" \
    "v_mfma_f32_16x16x32_bf16 v[124:127], v[28:31], a[32:35], v[124:127]\n\tds_read_b64_tr_b16 v[16:17], v0 offset:12288\n\tds_read_b64_tr_b16 v[18:19], v0 offset:12800\n\tv_mfma_f32_16x16x32_bf16 v[132:135], v[28:31], a[36:39], v[132:135]\n\tbuffer_load_dwordx4 v11, s[12:15], s69 offen lds\n\ts_cmp_ge_u32 s43, 2\n\ts_cselect_b32 s69, s70, s43\n\ts_mul_i32 s69, s69, 294912\n\t" \
    "v_mfma_f32_16x16x32_bf16 v[140:143], v[28:31], a[40:43], v[140:143]\n\ts_add_u32 s69, s69, 5603328\n\ts_add_u32 m0, s54, 77824\n\ts_waitcnt lgkmcnt(8)\n\tv_mfma_f32_16x16x32_bf16 v[144:147], v[32:35], a[32:35], v[144:147]\n\tds_read_b64_tr_b16 v[20:21], v1 offset:12288\n\tds_read_b64_tr_b16 v[22:23], v1 offset:12800\n\tv_mfma_f32_16x16x32_bf16 v[152:155], v[32:35], a[36:39], v[152:155]\n\t" \
    "buffer_load_dwordx4 v11, s[12:15], s69 offen lds\n\ts_cmp_ge_u32 s43, 1\n\ts_cselect_b32 s69, s70, s43\n\ts_mul_i32 s69, s69, 294912\n\tv_mfma_f32_16x16x32_bf16 v[160:163], v[32:35], a[40:43], v[160:163]\n\ts_add_u32 s69, s69, 5898240\n\ts_add_u32 m0, s54, 81920\n\ts_waitcnt lgkmcnt(8)\n\t" \
    "v_mfma_f32_16x16x32_bf16 v[148:151], v[36:39], a[32:35], v[148:151]\n\tds_read_b64_tr_b16 v[24:25], v0 offset:16384\n\tds_read_b64_tr_b16 v[26:27], v0 offset:16896\n\tv_mfma_f32_16x16x32_bf16 v[156:159], v[36:39], a[36:39], v[156:159]\n\tv_mfma_f32_16x16x32_bf16 v[164:167], v[36:39], a[40:43], v[164:167]\n\ts_waitcnt vmcnt(37) lgkmcnt(8)\n\tv_mfma_f32_16x16x32_bf16 v[120:123], v[40:43], v[168:171], v[120:123]\n\tds_read_b64_tr_b16 v[28:29], v1 offset:16384\n\t" \
    "ds_read_b64_tr_b16 v[30:31], v1 offset:16896\n\tv_mfma_f32_16x16x32_bf16 v[128:131], v[40:43], v[172:175], v[128:131]\n\tbuffer_load_dwordx4 v11, s[12:15], s69 offen lds\n\ts_mov_b32 s69, 6193152\n\ts_add_u32 m0, s54, 86016\n\tv_mfma_f32_16x16x32_bf16 v[136:139], v[40:43], v[176:179], v[136:139]\n\ts_waitcnt lgkmcnt(8)\n\tv_mfma_f32_16x16x32_bf16 v[124:127], v[44:47], v[168:171], v[124:127]\n\t" \
    "ds_read_b64_tr_b16 v[32:33], v0 offset:20480\n\tds_read_b64_tr_b16 v[34:35], v0 offset:20992\n\tv_mfma_f32_16x16x32_bf16 v[132:135], v[44:47], v[172:175], v[132:135]\n\tbuffer_load_dwordx4 v11, s[12:15], s69 offen lds\n\ts_mov_b32 s69, 6488064\n\ts_add_u32 m0, s54, 90112\n\tv_mfma_f32_16x16x32_bf16 v[140:143], v[44:47], v[176:179], v[140:143]\n\ts_waitcnt vmcnt(36) lgkmcnt(8)\n\t" \
    "v_mfma_f32_16x16x32_bf16 v[120:123], v[16:19], a[116:119], v[120:123]\n\tds_read_b64_tr_b16 v[36:37], v1 offset:20480\n\tds_read_b64_tr_b16 v[38:39], v1 offset:20992\n\tv_mfma_f32_16x16x32_bf16 v[128:131], v[16:19], a[120:123], v[128:131]\n\tbuffer_load_dwordx4 v11, s[12:15], s69 offen lds\n\ts_mov_b32 s69, 6782976\n\ts_add_u32 m0, s54, 98304\n\tv_mfma_f32_16x16x32_bf16 v[136:139], v[16:19], a[124:127], v[136:139]\n\t" \
    "s_waitcnt lgkmcnt(8)\n\tv_mfma_f32_16x16x32_bf16 v[124:127], v[20:23], a[116:119], v[124:127]\n\tds_read_b64_tr_b16 v[40:41], v0 offset:24576\n\tds_read_b64_tr_b16 v[42:43], v0 offset:25088\n\tv_mfma_f32_16x16x32_bf16 v[132:135], v[20:23], a[120:123], v[132:135]\n\tv_mfma_f32_16x16x32_bf16 v[140:143], v[20:23], a[124:127], v[140:143]\n\ts_waitcnt vmcnt(34) lgkmcnt(8)\n\tv_mfma_f32_16x16x32_bf16 v[120:123], v[24:27], a[44:47], v[120:123]\n\t" \
    "ds_read_b64_tr_b16 v[44:45], v1 offset:24576\n\tds_read_b64_tr_b16 v[46:47], v1 offset:25088\n\tv_mfma_f32_16x16x32_bf16 v[128:131], v[24:27], a[48:51], v[128:131]\n\tbuffer_load_dwordx4 v11, s[12:15], s69 offen lds\n\ts_mov_b32 s69, 7077888\n\ts_add_u32 m0, s54, 102400\n\tv_mfma_f32_16x16x32_bf16 v[136:139], v[24:27], a[52:55], v[136:139]\n\ts_waitcnt lgkmcnt(8)\n\t" \
    "v_mfma_f32_16x16x32_bf16 v[124:127], v[28:31], a[44:47], v[124:127]\n\tds_read_b64_tr_b16 v[16:17], v0 offset:28672\n\tds_read_b64_tr_b16 v[18:19], v0 offset:29184\n\tv_mfma_f32_16x16x32_bf16 v[132:135], v[28:31], a[48:51], v[132:135]\n\tbuffer_load_dwordx4 v11, s[12:15], s69 offen lds\n\ts_mov_b32 s69, 7372800\n\ts_add_u32 m0, s54, 106496\n\tv_mfma_f32_16x16x32_bf16 v[140:143], v[28:31], a[52:55], v[140:143]\n\t" \
    "s_waitcnt vmcnt(33) lgkmcnt(8)\n\tv_mfma_f32_16x16x32_bf16 v[120:123], v[32:35], a[128:131], v[120:123]\n\tds_read_b64_tr_b16 v[20:21], v1 offset:28672\n\tds_read_b64_tr_b16 v[22:23], v1 offset:29184\n\tv_mfma_f32_16x16x32_bf16 v[128:131], v[32:35], a[132:135], v[128:131]\n\tv_mfma_f32_16x16x32_bf16 v[136:139], v[32:35], a[136:139], v[136:139]\n\ts_waitcnt lgkmcnt(8)\n\tv_mfma_f32_16x16x32_bf16 v[124:127], v[36:39], a[128:131], v[124:127]\n\t" \
    "ds_read_b64_tr_b16 v[24:25], v0 offset:32768\n\tds_read_b64_tr_b16 v[26:27], v0 offset:33280\n\tv_mfma_f32_16x16x32_bf16 v[132:135], v[36:39], a[132:135], v[132:135]\n\tbuffer_load_dwordx4 v11, s[12:15], s69 offen lds\n\ts_mov_b32 s69, 7667712\n\ts_add_u32 m0, s54, 110592\n\tv_mfma_f32_16x16x32_bf16 v[140:143], v[36:39], a[136:139], v[140:143]\n\ts_waitcnt vmcnt(31) lgkmcnt(8)\n\t" \
    "v_mfma_f32_16x16x32_bf16 v[120:123], v[40:43], a[140:143], v[120:123]\n\tds_read_b64_tr_b16 v[28:29], v1 offset:32768\n\tds_read_b64_tr_b16 v[30:31], v1 offset:33280\n\tv_mfma_f32_16x16x32_bf16 v[128:131], v[40:43], a[144:147], v[128:131]\n\tbuffer_load_dwordx4 v11, s[12:15], s69 offen lds\n\ts_mov_b32 s69, 7962624\n\ts_add_u32 m0, s54, 114688\n\tv_mfma_f32_16x16x32_bf16 v[136:139], v[40:43], a[148:151], v[136:139]\n\t" \
    "s_waitcnt lgkmcnt(8)\n\tv_mfma_f32_16x16x32_bf16 v[124:127], v[44:47], a[140:143], v[124:127]\n\tds_read_b64_tr_b16 v[32:33], v0 offset:36864\n\tds_read_b64_tr_b16 v[34:35], v0 offset:37376\n\tv_mfma_f32_16x16x32_bf16 v[132:135], v[44:47], a[144:147], v[132:135]\n\tbuffer_load_dwordx4 v11, s[12:15], s69 offen lds\n\ts_mov_b32 s69, 8257536\n\ts_add_u32 m0, s54, 118784\n\t" \
    "v_mfma_f32_16x16x32_bf16 v[140:143], v[44:47], a[148:151], v[140:143]\n\ts_waitcnt vmcnt(25)\n\ts_barrier\n\ts_waitcnt vmcnt(22) lgkmcnt(8)\n\tv_mfma_f32_16x16x32_bf16 v[120:123], v[16:19], a[152:155], v[120:123]\n\tds_read_b64_tr_b16 v[36:37], v1 offset:36864\n\tds_read_b64_tr_b16 v[38:39], v1 offset:37376\n\tv_mfma_f32_16x16x32_bf16 v[128:131], v[16:19], a[156:159], v[128:131]\n\t" \
    "buffer_load_dwordx4 v11, s[12:15], s69 offen lds\n\ts_mov_b32 s69, 8552448\n\ts_add_u32 m0, s54, 122880\n\tv_mfma_f32_16x16x32_bf16 v[136:139], v[16:19], a[160:163], v[136:139]\n\ts_waitcnt lgkmcnt(8)\n\tv_mfma_f32_16x16x32_bf16 v[124:127], v[20:23], a[152:155], v[124:127]\n\tds_read_b64_tr_b16 v[40:41], v0 offset:40960\n\tds_read_b64_tr_b16 v[42:43], v0 offset:41472\n\t" \
    "v_mfma_f32_16x16x32_bf16 v[132:135], v[20:23], a[156:159], v[132:135]\n\tbuffer_load_dwordx4 v11, s[12:15], s69 offen lds\n\ts_mov_b32 s69, 8847360\n\ts_add_u32 m0, s54, 126976\n\tv_mfma_f32_16x16x32_bf16 v[140:143], v[20:23], a[160:163], v[140:143]\n\ts_waitcnt vmcnt(21) lgkmcnt(8)\n\tv_mfma_f32_16x16x32_bf16 v[120:123], v[24:27], a[164:167], v[120:123]\n\tds_read_b64_tr_b16 v[44:45], v1 offset:40960\n\t" \
    "ds_read_b64_tr_b16 v[46:47], v1 offset:41472\n\tv_mfma_f32_16x16x32_bf16 v[128:131], v[24:27], a[168:171], v[128:131]\n\tv_mfma_f32_16x16x32_bf16 v[136:139], v[24:27], a[172:175], v[136:139]\n\ts_waitcnt lgkmcnt(8)\n\tv_mfma_f32_16x16x32_bf16 v[124:127], v[28:31], a[164:167], v[124:127]\n\tds_read_b64_tr_b16 v[16:17], v0 offset:45056\n\tds_read_b64_tr_b16 v[18:19], v0 offset:45568\n\tv_mfma_f32_16x16x32_bf16 v[132:135], v[28:31], a[168:171], v[132:135]\n\t" \
    "buffer_load_dwordx4 v11, s[12:15], s69 offen lds\n\ts_mov_b32 s69, 9142272\n\ts_add_u32 m0, s54, 0\n\tv_mfma_f32_16x16x32_bf16 v[140:143], v[28:31], a[172:175], v[140:143]\n\ts_waitcnt vmcnt(19) lgkmcnt(8)\n\tv_mfma_f32_16x16x32_bf16 v[120:123], v[32:35], a[176:179], v[120:123]\n\tds_read_b64_tr_b16 v[20:21], v1 offset:45056\n\tds_read_b64_tr_b16 v[22:23], v1 offset:45568\n\t" \
    "v_mfma_f32_16x16x32_bf16 v[128:131], v[32:35], a[180:183], v[128:131]\n\tbuffer_load_dwordx4 v11, s[12:15], s69 offen lds\n\tv_mfma_f32_16x16x32_bf16 v[136:139], v[32:35], a[184:187], v[136:139]\n\ts_waitcnt lgkmcnt(8)\n\tv_mfma_f32_16x16x32_bf16 v[124:127], v[36:39], a[176:179], v[124:127]\n\tds_read_b64_tr_b16 v[24:25], v0 offset:49152\n\tds_read_b64_tr_b16 v[26:27], v0 offset:49664\n\tv_mfma_f32_16x16x32_bf16 v[132:135], v[36:39], a[180:183], v[132:135]\n\t" \
    "v_mfma_f32_16x16x32_bf16 v[140:143], v[36:39], a[184:187], v[140:143]\n\ts_waitcnt vmcnt(17) lgkmcnt(8)\n\tv_mfma_f32_16x16x32_bf16 v[120:123], v[40:43], a[188:191], v[120:123]\n\tds_read_b64_tr_b16 v[28:29], v1 offset:49152\n\tds_read_b64_tr_b16 v[30:31], v1 offset:49664\n\tv_mfma_f32_16x16x32_bf16 v[128:131], v[40:43], a[192:195], v[128:131]\n\tv_mfma_f32_16x16x32_bf16 v[136:139], v[40:43], a[196:199], v[136:139]\n\ts_waitcnt lgkmcnt(8)\n\t" \
    "v_mfma_f32_16x16x32_bf16 v[124:127], v[44:47], a[188:191], v[124:127]\n\tds_read_b64_tr_b16 v[32:33], v0 offset:53248\n\tds_read_b64_tr_b16 v[34:35], v0 offset:53760\n\tv_mfma_f32_16x16x32_bf16 v[132:135], v[44:47], a[192:195], v[132:135]\n\tv_mfma_f32_16x16x32_bf16 v[140:143], v[44:47], a[196:199], v[140:143]\n\ts_waitcnt vmcnt(14) lgkmcnt(8)\n\tv_mfma_f32_16x16x32_bf16 v[120:123], v[16:19], a[200:203], v[120:123]\n\tds_read_b64_tr_b16 v[36:37], v1 offset:53248\n\t" \
    "ds_read_b64_tr_b16 v[38:39], v1 offset:53760\n\tv_mfma_f32_16x16x32_bf16 v[128:131], v[16:19], a[204:207], v[128:131]\n\tv_mfma_f32_16x16x32_bf16 v[136:139], v[16:19], a[208:211], v[136:139]\n\ts_waitcnt lgkmcnt(8)\n\tv_mfma_f32_16x16x32_bf16 v[124:127], v[20:23], a[200:203], v[124:127]\n\tds_read_b64_tr_b16 v[40:41], v0 offset:57344\n\tds_read_b64_tr_b16 v[42:43], v0 offset:57856\n\tv_mfma_f32_16x16x32_bf16 v[132:135], v[20:23], a[204:207], v[132:135]\n\t" \
    "v_mfma_f32_16x16x32_bf16 v[140:143], v[20:23], a[208:211], v[140:143]\n\ts_waitcnt lgkmcnt(8)\n\tv_mfma_f32_16x16x32_bf16 v[48:51], v[24:27], a[56:59], v[48:51]\n\tds_read_b64_tr_b16 v[44:45], v1 offset:57344\n\tds_read_b64_tr_b16 v[46:47], v1 offset:57856\n\tv_mfma_f32_16x16x32_bf16 v[56:59], v[24:27], a[60:63], v[56:59]\n\tv_mfma_f32_16x16x32_bf16 v[64:67], v[24:27], a[64:67], v[64:67]\n\ts_add_u32 s68, s52, 0\n\t" \
    "v_cvt_pk_bf16_f32 v120, v120, v121\n\tv_cvt_pk_bf16_f32 v121, v122, v123\n\tv_cvt_pk_bf16_f32 v122, v124, v125\n\tv_cvt_pk_bf16_f32 v123, v126, v127\n\tbuffer_store_dwordx4 v[120:123], v8, s[8:11], s68 offen offset:0 sc1\n\tv_cvt_pk_bf16_f32 v124, v128, v129\n\tv_cvt_pk_bf16_f32 v125, v130, v131\n\tv_cvt_pk_bf16_f32 v126, v132, v133\n\t" \
    "v_cvt_pk_bf16_f32 v127, v134, v135\n\tbuffer_store_dwordx4 v[124:127], v8, s[8:11], s68 offen offset:1024 sc1\n\tv_cvt_pk_bf16_f32 v128, v136, v137\n\tv_cvt_pk_bf16_f32 v129, v138, v139\n\tv_cvt_pk_bf16_f32 v130, v140, v141\n\tv_cvt_pk_bf16_f32 v131, v142, v143\n\tbuffer_store_dwordx4 v[128:131], v8, s[8:11], s68 offen offset:2048 sc1\n\tv_add3_u32 v132, s36, v120, v121\n\t" \
    "v_add3_u32 v133, v122, v123, v124\n\tv_add3_u32 v132, v132, v125, v126\n\tv_add3_u32 v133, v133, v127, v128\n\tv_add3_u32 v132, v132, v129, v130\n\tv_add3_u32 v132, v132, v133, v131\n\tbuffer_store_dword v132, v236, s[8:11], s68 offen offset:3072 sc1\n\ts_waitcnt lgkmcnt(8)\n\tv_mfma_f32_16x16x32_bf16 v[52:55], v[28:31], a[56:59], v[52:55]\n\t" \
    "ds_read_b64_tr_b16 v[16:17], v0 offset:61440\n\tds_read_b64_tr_b16 v[18:19], v0 offset:61952\n\tv_mfma_f32_16x16x32_bf16 v[60:63], v[28:31], a[60:63], v[60:63]\n\tv_mfma_f32_16x16x32_bf16 v[68:71], v[28:31], a[64:67], v[68:71]\n\ts_waitcnt lgkmcnt(8)\n\tv_mfma_f32_16x16x32_bf16 v[48:51], v[32:35], v[204:207], v[48:51]\n\tds_read_b64_tr_b16 v[20:21], v1 offset:61440\n\tds_read_b64_tr_b16 v[22:23], v1 offset:61952\n\t" \
    "v_mfma_f32_16x16x32_bf16 v[56:59], v[32:35], v[208:211], v[56:59]\n\tv_mfma_f32_16x16x32_bf16 v[64:67], v[32:35], v[212:215], v[64:67]\n\ts_waitcnt lgkmcnt(8)\n\tv_mfma_f32_16x16x32_bf16 v[52:55], v[36:39], v[204:207], v[52:55]\n\tds_read_b64_tr_b16 v[24:25], v14 offset:0\n\tds_read_b64_tr_b16 v[26:27], v14 offset:512\n\ts_lshl_b32 s69, s4, 18\n\tv_mfma_f32_16x16x32_bf16 v[60:63], v[36:39], v[208:211], v[60:63]\n\t" \
    "s_add_u32 s69, s69, 0\n\ts_add_u32 m0, s54, 131072\n\tv_mfma_f32_16x16x32_bf16 v[68:71], v[36:39], v[212:215], v[68:71]\n\ts_waitcnt lgkmcnt(8)\n\tv_mfma_f32_16x16x32_bf16 v[48:51], v[40:43], a[68:71], v[48:51]\n\tds_read_b64_tr_b16 v[28:29], v15 offset:0\n\tds_read_b64_tr_b16 v[30:31], v15 offset:512\n\tv_mfma_f32_16x16x32_bf16 v[56:59], v[40:43], a[72:75], v[56:59]\n\t" \
    "buffer_load_dwordx4 v12, s[16:19], s69 offen lds\n\ts_lshl_b32 s69, s4, 18\n\ts_add_u32 s69, s69, 0\n\tv_mfma_f32_16x16x32_bf16 v[64:67], v[40:43], a[76:79], v[64:67]\n\ts_add_u32 m0, s54, 135168\n\ts_waitcnt lgkmcnt(8)\n\tv_mfma_f32_16x16x32_bf16 v[52:55], v[44:47], a[68:71], v[52:55]\n\tds_read_b64_tr_b16 v[32:33], v14 offset:4096\n\t" \
    "ds_read_b64_tr_b16 v[34:35], v14 offset:4608\n\tv_mfma_f32_16x16x32_bf16 v[60:63], v[44:47], a[72:75], v[60:63]\n\tbuffer_load_dwordx4 v13, s[16:19], s69 offen lds\n\ts_lshl_b32 s69, s4, 18\n\ts_add_u32 s69, s69, 131072\n\tv_mfma_f32_16x16x32_bf16 v[68:71], v[44:47], a[76:79], v[68:71]\n\ts_add_u32 m0, s54, 139264\n\ts_waitcnt vmcnt(12)\n\t" \
    "s_barrier\n\ts_waitcnt lgkmcnt(8)\n\tv_mfma_f32_16x16x32_bf16 v[48:51], v[16:19], v[216:219], v[48:51]\n\tds_read_b64_tr_b16 v[36:37], v15 offset:4096\n\tds_read_b64_tr_b16 v[38:39], v15 offset:4608\n\tv_mfma_f32_16x16x32_bf16 v[56:59], v[16:19], a[0:3], v[56:59]\n\tbuffer_load_dwordx4 v12, s[16:19], s69 offen lds\n\ts_lshl_b32 s69, s4, 18\n\t" \
    "s_add_u32 s69, s69, 131072\n\tv_mfma_f32_16x16x32_bf16 v[64:67], v[16:19], a[4:7], v[64:67]\n\ts_add_u32 m0, s54, 143360\n\ts_waitcnt lgkmcnt(8)\n\tv_mfma_f32_16x16x32_bf16 v[52:55], v[20:23], v[216:219], v[52:55]\n\tds_read_b64_tr_b16 v[40:41], v14 offset:8192\n\tds_read_b64_tr_b16 v[42:43], v14 offset:8704\n\tv_mfma_f32_16x16x32_bf16 v[60:63], v[20:23], a[0:3], v[60:63]\n\t" \
    "buffer_load_dwordx4 v13, s[16:19], s69 offen lds\n\tv_mfma_f32_16x16x32_bf16 v[68:71], v[20:23], a[4:7], v[68:71]\n\ts_waitcnt lgkmcnt(8)\n\tv_mfma_f32_16x16x32_bf16 v[48:51], v[24:27], a[80:83], v[48:51]\n\tds_read_b64_tr_b16 v[44:45], v15 offset:8192\n\tds_read_b64_tr_b16 v[46:47], v15 offset:8704\n\tv_mfma_f32_16x16x32_bf16 v[56:59], v[24:27], a[84:87], v[56:59]\n\tv_mfma_f32_16x16x32_bf16 v[64:67], v[24:27], a[88:91], v[64:67]\n\t" \
    "s_waitcnt lgkmcnt(8)\n\tv_mfma_f32_16x16x32_bf16 v[52:55], v[28:31], a[80:83], v[52:55]\n\tds_read_b64_tr_b16 v[16:17], v14 offset:12288\n\tds_read_b64_tr_b16 v[18:19], v14 offset:12800\n\tv_mfma_f32_16x16x32_bf16 v[60:63], v[28:31], a[84:87], v[60:63]\n\tv_mfma_f32_16x16x32_bf16 v[68:71], v[28:31], a[88:91], v[68:71]\n\ts_waitcnt lgkmcnt(8)\n\tv_mfma_f32_16x16x32_bf16 v[48:51], v[32:35], a[8:11], v[48:51]\n\t" \
    "ds_read_b64_tr_b16 v[20:21], v15 offset:12288\n\tds_read_b64_tr_b16 v[22:23], v15 offset:12800\n\tv_mfma_f32_16x16x32_bf16 v[56:59], v[32:35], a[12:15], v[56:59]\n\tv_mfma_f32_16x16x32_bf16 v[64:67], v[32:35], a[16:19], v[64:67]\n\ts_waitcnt lgkmcnt(8)\n\tv_mfma_f32_16x16x32_bf16 v[52:55], v[36:39], a[8:11], v[52:55]\n\tds_read_b64_tr_b16 v[24:25], v14 offset:16384\n\tds_read_b64_tr_b16 v[26:27], v14 offset:16896\n\t" \
    "v_mfma_f32_16x16x32_bf16 v[60:63], v[36:39], a[12:15], v[60:63]\n\tv_mfma_f32_16x16x32_bf16 v[68:71], v[36:39], a[16:19], v[68:71]\n\ts_waitcnt lgkmcnt(8)\n\tv_mfma_f32_16x16x32_bf16 v[48:51], v[40:43], a[92:95], v[48:51]\n\tds_read_b64_tr_b16 v[28:29], v15 offset:16384\n\tds_read_b64_tr_b16 v[30:31], v15 offset:16896\n\tv_mfma_f32_16x16x32_bf16 v[56:59], v[40:43], a[96:99], v[56:59]\n\tv_mfma_f32_16x16x32_bf16 v[64:67], v[40:43], a[100:103], v[64:67]\n\t" \
    "s_waitcnt lgkmcnt(8)\n\tv_mfma_f32_16x16x32_bf16 v[52:55], v[44:47], a[92:95], v[52:55]\n\tds_read_b64_tr_b16 v[32:33], v14 offset:20480\n\tds_read_b64_tr_b16 v[34:35], v14 offset:20992\n\tv_mfma_f32_16x16x32_bf16 v[60:63], v[44:47], a[96:99], v[60:63]\n\tv_mfma_f32_16x16x32_bf16 v[68:71], v[44:47], a[100:103], v[68:71]\n\ts_waitcnt lgkmcnt(8)\n\tv_mfma_f32_16x16x32_bf16 v[48:51], v[16:19], a[20:23], v[48:51]\n\t" \
    "ds_read_b64_tr_b16 v[36:37], v15 offset:20480\n\tds_read_b64_tr_b16 v[38:39], v15 offset:20992\n\tv_mfma_f32_16x16x32_bf16 v[56:59], v[16:19], a[24:27], v[56:59]\n\tv_mfma_f32_16x16x32_bf16 v[64:67], v[16:19], a[28:31], v[64:67]\n\ts_waitcnt lgkmcnt(8)\n\tv_mfma_f32_16x16x32_bf16 v[52:55], v[20:23], a[20:23], v[52:55]\n\tds_read_b64_tr_b16 v[40:41], v14 offset:24576\n\tds_read_b64_tr_b16 v[42:43], v14 offset:25088\n\t" \
    "v_mfma_f32_16x16x32_bf16 v[60:63], v[20:23], a[24:27], v[60:63]\n\tv_mfma_f32_16x16x32_bf16 v[68:71], v[20:23], a[28:31], v[68:71]\n\ts_waitcnt lgkmcnt(8)\n\tv_mfma_f32_16x16x32_bf16 v[48:51], v[24:27], a[104:107], v[48:51]\n\tds_read_b64_tr_b16 v[44:45], v15 offset:24576\n\tds_read_b64_tr_b16 v[46:47], v15 offset:25088\n\tv_mfma_f32_16x16x32_bf16 v[56:59], v[24:27], a[108:111], v[56:59]\n\tv_mfma_f32_16x16x32_bf16 v[64:67], v[24:27], a[112:115], v[64:67]\n\t" \
    "s_waitcnt lgkmcnt(8)\n\tv_mfma_f32_16x16x32_bf16 v[52:55], v[28:31], a[104:107], v[52:55]\n\tds_read_b64_tr_b16 v[16:17], v14 offset:32768\n\tds_read_b64_tr_b16 v[18:19], v14 offset:33280\n\tv_mfma_f32_16x16x32_bf16 v[60:63], v[28:31], a[108:111], v[60:63]\n\tv_mfma_f32_16x16x32_bf16 v[68:71], v[28:31], a[112:115], v[68:71]\n\ts_waitcnt lgkmcnt(8)\n\tv_mfma_f32_16x16x32_bf16 v[48:51], v[32:35], a[32:35], v[48:51]\n\t" \
    "ds_read_b64_tr_b16 v[20:21], v15 offset:32768\n\tds_read_b64_tr_b16 v[22:23], v15 offset:33280\n\ts_add_u32 s68, s72, 0\n\tv_mfma_f32_16x16x32_bf16 v[56:59], v[32:35], a[36:39], v[56:59]\n\tbuffer_load_dwordx4 v[120:123], v8, s[8:11], s68 offen offset:0 sc1\n\tv_mfma_f32_16x16x32_bf16 v[64:67], v[32:35], a[40:43], v[64:67]\n\ts_waitcnt lgkmcnt(8)\n\tv_mfma_f32_16x16x32_bf16 v[52:55], v[36:39], a[32:35], v[52:55]\n\t" \
    "ds_read_b64_tr_b16 v[24:25], v14 offset:36864\n\tds_read_b64_tr_b16 v[26:27], v14 offset:37376\n\tv_mfma_f32_16x16x32_bf16 v[60:63], v[36:39], a[36:39], v[60:63]\n\tbuffer_load_dwordx4 v[124:127], v8, s[8:11], s68 offen offset:1024 sc1\n\tv_mfma_f32_16x16x32_bf16 v[68:71], v[36:39], a[40:43], v[68:71]\n\ts_waitcnt lgkmcnt(8)\n\tv_mfma_f32_16x16x32_bf16 v[48:51], v[40:43], v[168:171], v[48:51]\n\tds_read_b64_tr_b16 v[28:29], v15 offset:36864\n\t" \
    "ds_read_b64_tr_b16 v[30:31], v15 offset:37376\n\tv_mfma_f32_16x16x32_bf16 v[56:59], v[40:43], v[172:175], v[56:59]\n\tbuffer_load_dwordx4 v[128:131], v8, s[8:11], s68 offen offset:2048 sc1\n\tv_mfma_f32_16x16x32_bf16 v[64:67], v[40:43], v[176:179], v[64:67]\n\ts_waitcnt lgkmcnt(8)\n\tv_mfma_f32_16x16x32_bf16 v[52:55], v[44:47], v[168:171], v[52:55]\n\tds_read_b64_tr_b16 v[32:33], v14 offset:40960\n\tds_read_b64_tr_b16 v[34:35], v14 offset:41472\n\t" \
    "v_mfma_f32_16x16x32_bf16 v[60:63], v[44:47], v[172:175], v[60:63]\n\tbuffer_load_dword v132, v236, s[8:11], s68 offen offset:3072 sc1\n\tv_mfma_f32_16x16x32_bf16 v[68:71], v[44:47], v[176:179], v[68:71]\n\ts_waitcnt vmcnt(12)\n\ts_barrier\n\ts_waitcnt lgkmcnt(8)\n\tv_mfma_f32_16x16x32_bf16 v[48:51], v[16:19], a[116:119], v[48:51]\n\tds_read_b64_tr_b16 v[36:37], v15 offset:40960\n\t" \
    "ds_read_b64_tr_b16 v[38:39], v15 offset:41472\n\tv_mfma_f32_16x16x32_bf16 v[56:59], v[16:19], a[120:123], v[56:59]\n\tv_mfma_f32_16x16x32_bf16 v[64:67], v[16:19], a[124:127], v[64:67]\n\ts_waitcnt lgkmcnt(8)\n\tv_mfma_f32_16x16x32_bf16 v[52:55], v[20:23], a[116:119], v[52:55]\n\tds_read_b64_tr_b16 v[40:41], v14 offset:45056\n\tds_read_b64_tr_b16 v[42:43], v14 offset:45568\n\tv_mfma_f32_16x16x32_bf16 v[60:63], v[20:23], a[120:123], v[60:63]\n\t" \
    "v_mfma_f32_16x16x32_bf16 v[68:71], v[20:23], a[124:127], v[68:71]\n\ts_waitcnt lgkmcnt(8)\n\tv_mfma_f32_16x16x32_bf16 v[48:51], v[24:27], a[44:47], v[48:51]\n\tds_read_b64_tr_b16 v[44:45], v15 offset:45056\n\tds_read_b64_tr_b16 v[46:47], v15 offset:45568\n\tv_mfma_f32_16x16x32_bf16 v[56:59], v[24:27], a[48:51], v[56:59]\n\tv_mfma_f32_16x16x32_bf16 v[64:67], v[24:27], a[52:55], v[64:67]\n\ts_waitcnt lgkmcnt(8)\n\t" \
    "v_mfma_f32_16x16x32_bf16 v[52:55], v[28:31], a[44:47], v[52:55]\n\tds_read_b64_tr_b16 v[16:17], v14 offset:49152\n\tds_read_b64_tr_b16 v[18:19], v14 offset:49664\n\tv_mfma_f32_16x16x32_bf16 v[60:63], v[28:31], a[48:51], v[60:63]\n\tv_mfma_f32_16x16x32_bf16 v[68:71], v[28:31], a[52:55], v[68:71]\n\ts_waitcnt lgkmcnt(8)\n\tv_mfma_f32_16x16x32_bf16 v[48:51], v[32:35], a[128:131], v[48:51]\n\tds_read_b64_tr_b16 v[20:21], v15 offset:49152\n\t" \
    "ds_read_b64_tr_b16 v[22:23], v15 offset:49664\n\tv_mfma_f32_16x16x32_bf16 v[56:59], v[32:35], a[132:135], v[56:59]\n\tv_mfma_f32_16x16x32_bf16 v[64:67], v[32:35], a[136:139], v[64:67]\n\ts_waitcnt lgkmcnt(8)\n\tv_mfma_f32_16x16x32_bf16 v[52:55], v[36:39], a[128:131], v[52:55]\n\tds_read_b64_tr_b16 v[24:25], v14 offset:53248\n\tds_read_b64_tr_b16 v[26:27], v14 offset:53760\n\tv_mfma_f32_16x16x32_bf16 v[60:63], v[36:39], a[132:135], v[60:63]\n\t" \
    "v_mfma_f32_16x16x32_bf16 v[68:71], v[36:39], a[136:139], v[68:71]\n\ts_waitcnt lgkmcnt(8)\n\tv_mfma_f32_16x16x32_bf16 v[48:51], v[40:43], a[140:143], v[48:51]\n\tds_read_b64_tr_b16 v[28:29], v15 offset:53248\n\tds_read_b64_tr_b16 v[30:31], v15 offset:53760\n\tv_mfma_f32_16x16x32_bf16 v[56:59], v[40:43], a[144:147], v[56:59]\n\tv_mfma_f32_16x16x32_bf16 v[64:67], v[40:43], a[148:151], v[64:67]\n\ts_waitcnt lgkmcnt(8)\n\t" \
    "v_mfma_f32_16x16x32_bf16 v[52:55], v[44:47], a[140:143], v[52:55]\n\tds_read_b64_tr_b16 v[32:33], v14 offset:57344\n\tds_read_b64_tr_b16 v[34:35], v14 offset:57856\n\tv_mfma_f32_16x16x32_bf16 v[60:63], v[44:47], a[144:147], v[60:63]\n\tv_mfma_f32_16x16x32_bf16 v[68:71], v[44:47], a[148:151], v[68:71]\n\ts_waitcnt lgkmcnt(8)\n\tv_mfma_f32_16x16x32_bf16 v[48:51], v[16:19], a[152:155], v[48:51]\n\tds_read_b64_tr_b16 v[36:37], v15 offset:57344\n\t" \
    "ds_read_b64_tr_b16 v[38:39], v15 offset:57856\n\tv_mfma_f32_16x16x32_bf16 v[56:59], v[16:19], a[156:159], v[56:59]\n\tv_mfma_f32_16x16x32_bf16 v[64:67], v[16:19], a[160:163], v[64:67]\n\ts_waitcnt lgkmcnt(8)\n\tv_mfma_f32_16x16x32_bf16 v[52:55], v[20:23], a[152:155], v[52:55]\n\tds_read_b64_tr_b16 v[40:41], v14 offset:61440\n\tds_read_b64_tr_b16 v[42:43], v14 offset:61952\n\tv_mfma_f32_16x16x32_bf16 v[60:63], v[20:23], a[156:159], v[60:63]\n\t" \
    "v_mfma_f32_16x16x32_bf16 v[68:71], v[20:23], a[160:163], v[68:71]\n\ts_waitcnt lgkmcnt(8)\n\tv_mfma_f32_16x16x32_bf16 v[48:51], v[24:27], a[164:167], v[48:51]\n\tds_read_b64_tr_b16 v[44:45], v15 offset:61440\n\tds_read_b64_tr_b16 v[46:47], v15 offset:61952\n\tv_mfma_f32_16x16x32_bf16 v[56:59], v[24:27], a[168:171], v[56:59]\n\tv_mfma_f32_16x16x32_bf16 v[64:67], v[24:27], a[172:175], v[64:67]\n\ts_waitcnt lgkmcnt(8)\n\t" \
    "v_mfma_f32_16x16x32_bf16 v[52:55], v[28:31], a[164:167], v[52:55]\n\tds_read_b64_tr_b16 v[16:17], v0 offset:0\n\tds_read_b64_tr_b16 v[18:19], v0 offset:512\n\tv_mfma_f32_16x16x32_bf16 v[60:63], v[28:31], a[168:171], v[60:63]\n\tv_mfma_f32_16x16x32_bf16 v[68:71], v[28:31], a[172:175], v[68:71]\n\ts_waitcnt lgkmcnt(8)\n\tv_mfma_f32_16x16x32_bf16 v[48:51], v[32:35], a[176:179], v[48:51]\n\tds_read_b64_tr_b16 v[20:21], v1 offset:0\n\t" \
    "ds_read_b64_tr_b16 v[22:23], v1 offset:512\n\tv_mfma_f32_16x16x32_bf16 v[56:59], v[32:35], a[180:183], v[56:59]\n\tv_mfma_f32_16x16x32_bf16 v[64:67], v[32:35], a[184:187], v[64:67]\n\ts_waitcnt lgkmcnt(8)\n\tv_mfma_f32_16x16x32_bf16 v[52:55], v[36:39], a[176:179], v[52:55]\n\tds_read_b64_tr_b16 v[24:25], v2 offset:8192\n\tds_read_b64_tr_b16 v[26:27], v2 offset:8704\n\tv_mfma_f32_16x16x32_bf16 v[60:63], v[36:39], a[180:183], v[60:63]\n\t" \
    "v_mfma_f32_16x16x32_bf16 v[68:71], v[36:39], a[184:187], v[68:71]\n\ts_waitcnt lgkmcnt(8)\n\tv_mfma_f32_16x16x32_bf16 v[48:51], v[40:43], a[188:191], v[48:51]\n\tds_read_b64_tr_b16 v[28:29], v3 offset:8192\n\tds_read_b64_tr_b16 v[30:31], v3 offset:8704\n\tv_mfma_f32_16x16x32_bf16 v[56:59], v[40:43], a[192:195], v[56:59]\n\tv_mfma_f32_16x16x32_bf16 v[64:67], v[40:43], a[196:199], v[64:67]\n\ts_waitcnt lgkmcnt(8)\n\t" \
    "v_mfma_f32_16x16x32_bf16 v[52:55], v[44:47], a[188:191], v[52:55]\n\tds_read_b64_tr_b16 v[32:33], v2 offset:12288\n\tds_read_b64_tr_b16 v[34:35], v2 offset:12800\n\tv_mfma_f32_16x16x32_bf16 v[60:63], v[44:47], a[192:195], v[60:63]\n\tv_mfma_f32_16x16x32_bf16 v[68:71], v[44:47], a[196:199], v[68:71]\n\ts_barrier\n\ts_waitcnt lgkmcnt(8)\n\tv_mfma_f32_16x16x32_bf16 v[48:51], v[16:19], a[200:203], v[48:51]\n\t" \
    "ds_read_b64_tr_b16 v[36:37], v3 offset:12288\n\tds_read_b64_tr_b16 v[38:39], v3 offset:12800\n\tv_mfma_f32_16x16x32_bf16 v[56:59], v[16:19], a[204:207], v[56:59]\n\tv_mfma_f32_16x16x32_bf16 v[64:67], v[16:19], a[208:211], v[64:67]\n\ts_waitcnt lgkmcnt(8)\n\tv_mfma_f32_16x16x32_bf16 v[52:55], v[20:23], a[200:203], v[52:55]\n\tds_read_b64_tr_b16 v[40:41], v2 offset:16384\n\tds_read_b64_tr_b16 v[42:43], v2 offset:16896\n\t" \
    "v_mfma_f32_16x16x32_bf16 v[60:63], v[20:23], a[204:207], v[60:63]\n\tv_mfma_f32_16x16x32_bf16 v[68:71], v[20:23], a[208:211], v[68:71]\n\ts_mov_b32 s71, 1048576\n\ts_waitcnt vmcnt(0)\n\t107:\n\tv_add3_u32 v134, s36, v120, v121\n\tv_add3_u32 v135, v122, v123, v124\n\tv_add3_u32 v134, v134, v125, v126\n\t" \
    "v_add3_u32 v135, v135, v127, v128\n\tv_add3_u32 v134, v134, v129, v130\n\tv_add3_u32 v134, v134, v135, v131\n\tv_cmp_ne_u32 vcc, v134, v132\n\ts_cbranch_vccz 108f\n\ts_sub_u32 s71, s71, 1\n\ts_cbranch_scc1 108f\n\ts_sleep 6\n\t" \
    "s_add_u32 s73, s72, 0\n\tbuffer_load_dwordx4 v[120:123], v8, s[8:11], s73 offen offset:0 sc1\n\tbuffer_load_dwordx4 v[124:127], v8, s[8:11], s73 offen offset:1024 sc1\n\tbuffer_load_dwordx4 v[128:131], v8, s[8:11], s73 offen offset:2048 sc1\n\tbuffer_load_dword v132, v236, s[8:11], s73 offen offset:3072 sc1\n\ts_waitcnt vmcnt(0)\n\ts_branch 107b\n\t108:\n\t" \
    "v_lshlrev_b32 v96, 16, v120\n\tv_and_b32 v97, 0xffff0000, v120\n\tv_lshlrev_b32 v98, 16, v121\n\tv_and_b32 v99, 0xffff0000, v121\n\tv_lshlrev_b32 v100, 16, v122\n\tv_and_b32 v101, 0xffff0000, v122\n\tv_lshlrev_b32 v102, 16, v123\n\tv_and_b32 v103, 0xffff0000, v123\n\t" \
    "v_pk_add_f32 v[48:49], v[48:49], v[96:97]\n\tv_pk_add_f32 v[50:51], v[50:51], v[98:99]\n\tv_pk_add_f32 v[52:53], v[52:53], v[100:101]\n\tv_pk_add_f32 v[54:55], v[54:55], v[102:103]\n\tv_pk_mul_f32 v[96:97], v[48:49], v[48:49]\n\tv_pk_mul_f32 v[98:99], v[50:51], v[50:51]\n\tv_pk_mul_f32 v[100:101], v[52:53], v[52:53]\n\tv_pk_mul_f32 v[102:103], v[54:55], v[54:55]\n\t" \
    "v_pk_fma_f32 v[96:97], v[96:97], v[246:247], v[246:247] op_sel:[0,0,1] op_sel_hi:[1,0,1]\n\tv_pk_fma_f32 v[98:99], v[98:99], v[246:247], v[246:247] op_sel:[0,0,1] op_sel_hi:[1,0,1]\n\tv_pk_fma_f32 v[100:101], v[100:101], v[246:247], v[246:247] op_sel:[0,0,1] op_sel_hi:[1,0,1]\n\tv_pk_fma_f32 v[102:103], v[102:103], v[246:247], v[246:247] op_sel:[0,0,1] op_sel_hi:[1,0,1]\n\tv_pk_mul_f32 v[96:97], v[96:97], v[48:49]\n\tv_pk_mul_f32 v[98:99], v[98:99], v[50:51]\n\tv_pk_mul_f32 v[100:101], v[100:101], v[52:53]\n\tv_pk_mul_f32 v[102:103], v[102:103], v[54:55]\n\t" \
    "v_lshlrev_b32 v104, 16, v124\n\tv_exp_f32 v96, v96\n\tv_and_b32 v105, 0xffff0000, v124\n\tv_exp_f32 v97, v97\n\tv_lshlrev_b32 v106, 16, v125\n\tv_exp_f32 v98, v98\n\tv_and_b32 v107, 0xffff0000, v125\n\tv_exp_f32 v99, v99\n\t" \
    "v_lshlrev_b32 v108, 16, v126\n\tv_exp_f32 v100, v100\n\tv_and_b32 v109, 0xffff0000, v126\n\tv_exp_f32 v101, v101\n\tv_lshlrev_b32 v110, 16, v127\n\tv_exp_f32 v102, v102\n\tv_and_b32 v111, 0xffff0000, v127\n\tv_exp_f32 v103, v103\n\t" \
    "v_pk_add_f32 v[56:57], v[56:57], v[104:105]\n\tv_pk_add_f32 v[96:97], v[96:97], 1.0 op_sel_hi:[1,0]\n\tv_pk_add_f32 v[98:99], v[98:99], 1.0 op_sel_hi:[1,0]\n\tv_pk_add_f32 v[100:101], v[100:101], 1.0 op_sel_hi:[1,0]\n\tv_pk_add_f32 v[102:103], v[102:103], 1.0 op_sel_hi:[1,0]\n\tv_pk_add_f32 v[58:59], v[58:59], v[106:107]\n\tv_rcp_f32 v96, v96\n\tv_pk_add_f32 v[60:61], v[60:61], v[108:109]\n\t" \
    "v_rcp_f32 v97, v97\n\tv_pk_add_f32 v[62:63], v[62:63], v[110:111]\n\tv_rcp_f32 v98, v98\n\tv_pk_mul_f32 v[104:105], v[56:57], v[56:57]\n\tv_rcp_f32 v99, v99\n\tv_pk_mul_f32 v[106:107], v[58:59], v[58:59]\n\tv_rcp_f32 v100, v100\n\tv_pk_mul_f32 v[108:109], v[60:61], v[60:61]\n\t" \
    "v_rcp_f32 v101, v101\n\tv_pk_mul_f32 v[110:111], v[62:63], v[62:63]\n\tv_rcp_f32 v102, v102\n\tv_pk_fma_f32 v[104:105], v[104:105], v[246:247], v[246:247] op_sel:[0,0,1] op_sel_hi:[1,0,1]\n\tv_rcp_f32 v103, v103\n\tv_pk_fma_f32 v[106:107], v[106:107], v[246:247], v[246:247] op_sel:[0,0,1] op_sel_hi:[1,0,1]\n\tv_pk_mul_f32 v[96:97], v[96:97], v[48:49]\n\tv_pk_mul_f32 v[98:99], v[98:99], v[50:51]\n\t" \
    "v_pk_mul_f32 v[100:101], v[100:101], v[52:53]\n\tv_pk_mul_f32 v[102:103], v[102:103], v[54:55]\n\tv_cvt_pk_bf16_f32 v192, v96, v97\n\tv_cvt_pk_bf16_f32 v193, v98, v99\n\tv_cvt_pk_bf16_f32 v194, v100, v101\n\tv_cvt_pk_bf16_f32 v195, v102, v103\n\tbuffer_store_dwordx4 v[192:195], v8, s[8:11], s45 offen offset:0 sc1\n\tv_pk_fma_f32 v[108:109], v[108:109], v[246:247], v[246:247] op_sel:[0,0,1] op_sel_hi:[1,0,1]\n\t" \
    "v_pk_fma_f32 v[110:111], v[110:111], v[246:247], v[246:247] op_sel:[0,0,1] op_sel_hi:[1,0,1]\n\tv_pk_mul_f32 v[104:105], v[104:105], v[56:57]\n\tv_pk_mul_f32 v[106:107], v[106:107], v[58:59]\n\tv_pk_mul_f32 v[108:109], v[108:109], v[60:61]\n\tv_pk_mul_f32 v[110:111], v[110:111], v[62:63]\n\tv_lshlrev_b32 v112, 16, v128\n\tv_exp_f32 v104, v104\n\tv_and_b32 v113, 0xffff0000, v128\n\t" \
    "v_exp_f32 v105, v105\n\tv_lshlrev_b32 v114, 16, v129\n\tv_exp_f32 v106, v106\n\tv_and_b32 v115, 0xffff0000, v129\n\tv_exp_f32 v107, v107\n\tv_lshlrev_b32 v116, 16, v130\n\tv_exp_f32 v108, v108\n\tv_and_b32 v117, 0xffff0000, v130\n\t" \
    "v_exp_f32 v109, v109\n\tv_lshlrev_b32 v118, 16, v131\n\tv_exp_f32 v110, v110\n\tv_and_b32 v119, 0xffff0000, v131\n\tv_exp_f32 v111, v111\n\tv_pk_add_f32 v[64:65], v[64:65], v[112:113]\n\tv_pk_add_f32 v[104:105], v[104:105], 1.0 op_sel_hi:[1,0]\n\tv_pk_add_f32 v[106:107], v[106:107], 1.0 op_sel_hi:[1,0]\n\t" \
    "v_pk_add_f32 v[108:109], v[108:109], 1.0 op_sel_hi:[1,0]\n\tv_pk_add_f32 v[110:111], v[110:111], 1.0 op_sel_hi:[1,0]\n\tv_pk_add_f32 v[66:67], v[66:67], v[114:115]\n\tv_rcp_f32 v104, v104\n\tv_pk_add_f32 v[68:69], v[68:69], v[116:117]\n\tv_rcp_f32 v105, v105\n\tv_pk_add_f32 v[70:71], v[70:71], v[118:119]\n\tv_rcp_f32 v106, v106\n\t" \
    "v_pk_mul_f32 v[112:113], v[64:65], v[64:65]\n\tv_rcp_f32 v107, v107\n\tv_pk_mul_f32 v[114:115], v[66:67], v[66:67]\n\tv_rcp_f32 v108, v108\n\tv_pk_mul_f32 v[116:117], v[68:69], v[68:69]\n\tv_rcp_f32 v109, v109\n\tv_pk_mul_f32 v[118:119], v[70:71], v[70:71]\n\tv_rcp_f32 v110, v110\n\t" \
    "v_pk_fma_f32 v[112:113], v[112:113], v[246:247], v[246:247] op_sel:[0,0,1] op_sel_hi:[1,0,1]\n\tv_rcp_f32 v111, v111\n\tv_pk_fma_f32 v[114:115], v[114:115], v[246:247], v[246:247] op_sel:[0,0,1] op_sel_hi:[1,0,1]\n\tv_pk_mul_f32 v[104:105], v[104:105], v[56:57]\n\tv_pk_mul_f32 v[106:107], v[106:107], v[58:59]\n\tv_pk_mul_f32 v[108:109], v[108:109], v[60:61]\n\tv_pk_mul_f32 v[110:111], v[110:111], v[62:63]\n\tv_cvt_pk_bf16_f32 v196, v104, v105\n\t" \
    "v_cvt_pk_bf16_f32 v197, v106, v107\n\tv_cvt_pk_bf16_f32 v198, v108, v109\n\tv_cvt_pk_bf16_f32 v199, v110, v111\n\tbuffer_store_dwordx4 v[196:199], v8, s[8:11], s45 offen offset:1024 sc1\n\tv_pk_fma_f32 v[116:117], v[116:117], v[246:247], v[246:247] op_sel:[0,0,1] op_sel_hi:[1,0,1]\n\tv_pk_fma_f32 v[118:119], v[118:119], v[246:247], v[246:247] op_sel:[0,0,1] op_sel_hi:[1,0,1]\n\tv_pk_mul_f32 v[112:113], v[112:113], v[64:65]\n\tv_pk_mul_f32 v[114:115], v[114:115], v[66:67]\n\t" \
    "v_pk_mul_f32 v[116:117], v[116:117], v[68:69]\n\tv_pk_mul_f32 v[118:119], v[118:119], v[70:71]\n\tv_exp_f32 v112, v112\n\tv_exp_f32 v113, v113\n\tv_exp_f32 v114, v114\n\tv_exp_f32 v115, v115\n\tv_exp_f32 v116, v116\n\tv_exp_f32 v117, v117\n\t" \
    "v_exp_f32 v118, v118\n\tv_exp_f32 v119, v119\n\tv_pk_add_f32 v[112:113], v[112:113], 1.0 op_sel_hi:[1,0]\n\tv_pk_add_f32 v[114:115], v[114:115], 1.0 op_sel_hi:[1,0]\n\tv_pk_add_f32 v[116:117], v[116:117], 1.0 op_sel_hi:[1,0]\n\tv_pk_add_f32 v[118:119], v[118:119], 1.0 op_sel_hi:[1,0]\n\tv_rcp_f32 v112, v112\n\tv_rcp_f32 v113, v113\n\t" \
    "v_rcp_f32 v114, v114\n\tv_rcp_f32 v115, v115\n\tv_rcp_f32 v116, v116\n\tv_rcp_f32 v117, v117\n\tv_rcp_f32 v118, v118\n\tv_rcp_f32 v119, v119\n\tv_pk_mul_f32 v[112:113], v[112:113], v[64:65]\n\tv_pk_mul_f32 v[114:115], v[114:115], v[66:67]\n\t" \
    "v_pk_mul_f32 v[116:117], v[116:117], v[68:69]\n\tv_pk_mul_f32 v[118:119], v[118:119], v[70:71]\n\tv_cvt_pk_bf16_f32 v200, v112, v113\n\tv_cvt_pk_bf16_f32 v201, v114, v115\n\tv_cvt_pk_bf16_f32 v202, v116, v117\n\tv_cvt_pk_bf16_f32 v203, v118, v119\n\tbuffer_store_dwordx4 v[200:203], v8, s[8:11], s45 offen offset:2048 sc1\n\ts_waitcnt lgkmcnt(8)\n\t" \
    "v_mfma_f32_16x16x32_bf16 v[144:147], v[24:27], v[168:171], v[144:147]\n\tds_read_b64_tr_b16 v[44:45], v3 offset:16384\n\tds_read_b64_tr_b16 v[46:47], v3 offset:16896\n\tv_mfma_f32_16x16x32_bf16 v[152:155], v[24:27], v[172:175], v[152:155]\n\tbuffer_store_dwordx2 v[248:249], off, s[8:11], s60 sc1\n\tv_mfma_f32_16x16x32_bf16 v[160:163], v[24:27], v[176:179], v[160:163]\n\ts_waitcnt lgkmcnt(8)\n\tv_mfma_f32_16x16x32_bf16 v[148:151], v[28:31], v[168:171], v[148:151]\n\t" \
    "ds_read_b64_tr_b16 v[16:17], v2 offset:20480\n\tds_read_b64_tr_b16 v[18:19], v2 offset:20992\n\tv_mfma_f32_16x16x32_bf16 v[156:159], v[28:31], v[172:175], v[156:159]\n\tv_mfma_f32_16x16x32_bf16 v[164:167], v[28:31], v[176:179], v[164:167]\n\ts_waitcnt lgkmcnt(8)\n\tv_mfma_f32_16x16x32_bf16 v[144:147], v[32:35], a[116:119], v[144:147]\n\tds_read_b64_tr_b16 v[20:21], v3 offset:20480\n\tds_read_b64_tr_b16 v[22:23], v3 offset:20992\n\t" \
    "v_mfma_f32_16x16x32_bf16 v[152:155], v[32:35], a[120:123], v[152:155]\n\tv_mfma_f32_16x16x32_bf16 v[160:163], v[32:35], a[124:127], v[160:163]\n\ts_waitcnt lgkmcnt(8)\n\tv_mfma_f32_16x16x32_bf16 v[148:151], v[36:39], a[116:119], v[148:151]\n\tds_read_b64_tr_b16 v[24:25], v2 offset:24576\n\tds_read_b64_tr_b16 v[26:27], v2 offset:25088\n\tv_mfma_f32_16x16x32_bf16 v[156:159], v[36:39], a[120:123], v[156:159]\n\tv_mfma_f32_16x16x32_bf16 v[164:167], v[36:39], a[124:127], v[164:167]\n\t" \
    "s_waitcnt lgkmcnt(8)\n\tv_mfma_f32_16x16x32_bf16 v[144:147], v[40:43], a[44:47], v[144:147]\n\tds_read_b64_tr_b16 v[28:29], v3 offset:24576\n\tds_read_b64_tr_b16 v[30:31], v3 offset:25088\n\tv_mfma_f32_16x16x32_bf16 v[152:155], v[40:43], a[48:51], v[152:155]\n\tv_mfma_f32_16x16x32_bf16 v[160:163], v[40:43], a[52:55], v[160:163]\n\ts_waitcnt lgkmcnt(8)\n\tv_mfma_f32_16x16x32_bf16 v[148:151], v[44:47], a[44:47], v[148:151]\n\t" \
    "ds_read_b64_tr_b16 v[32:33], v2 offset:28672\n\tds_read_b64_tr_b16 v[34:35], v2 offset:29184\n\tv_mfma_f32_16x16x32_bf16 v[156:159], v[44:47], a[48:51], v[156:159]\n\tv_mfma_f32_16x16x32_bf16 v[164:167], v[44:47], a[52:55], v[164:167]\n\ts_waitcnt lgkmcnt(8)\n\tv_mfma_f32_16x16x32_bf16 v[144:147], v[16:19], a[128:131], v[144:147]\n\tds_read_b64_tr_b16 v[36:37], v3 offset:28672\n\tds_read_b64_tr_b16 v[38:39], v3 offset:29184\n\t" \
    "v_mfma_f32_16x16x32_bf16 v[152:155], v[16:19], a[132:135], v[152:155]\n\tv_mfma_f32_16x16x32_bf16 v[160:163], v[16:19], a[136:139], v[160:163]\n\ts_waitcnt lgkmcnt(8)\n\tv_mfma_f32_16x16x32_bf16 v[148:151], v[20:23], a[128:131], v[148:151]\n\tds_read_b64_tr_b16 v[40:41], v2 offset:32768\n\tds_read_b64_tr_b16 v[42:43], v2 offset:33280\n\tv_mfma_f32_16x16x32_bf16 v[156:159], v[20:23], a[132:135], v[156:159]\n\tv_mfma_f32_16x16x32_bf16 v[164:167], v[20:23], a[136:139], v[164:167]\n\t" \
    "s_waitcnt lgkmcnt(8)\n\tv_mfma_f32_16x16x32_bf16 v[144:147], v[24:27], a[140:143], v[144:147]\n\tds_read_b64_tr_b16 v[44:45], v3 offset:32768\n\tds_read_b64_tr_b16 v[46:47], v3 offset:33280\n\tv_mfma_f32_16x16x32_bf16 v[152:155], v[24:27], a[144:147], v[152:155]\n\tv_mfma_f32_16x16x32_bf16 v[160:163], v[24:27], a[148:151], v[160:163]\n\ts_waitcnt lgkmcnt(8)\n\tv_mfma_f32_16x16x32_bf16 v[148:151], v[28:31], a[140:143], v[148:151]\n\t" \
    "ds_read_b64_tr_b16 v[16:17], v2 offset:36864\n\tds_read_b64_tr_b16 v[18:19], v2 offset:37376\n\tv_mfma_f32_16x16x32_bf16 v[156:159], v[28:31], a[144:147], v[156:159]\n\tv_mfma_f32_16x16x32_bf16 v[164:167], v[28:31], a[148:151], v[164:167]\n\ts_waitcnt lgkmcnt(8)\n\tv_mfma_f32_16x16x32_bf16 v[144:147], v[32:35], a[152:155], v[144:147]\n\tds_read_b64_tr_b16 v[20:21], v3 offset:36864\n\tds_read_b64_tr_b16 v[22:23], v3 offset:37376\n\t" \
    "v_mfma_f32_16x16x32_bf16 v[152:155], v[32:35], a[156:159], v[152:155]\n\tv_mfma_f32_16x16x32_bf16 v[160:163], v[32:35], a[160:163], v[160:163]\n\ts_waitcnt lgkmcnt(8)\n\tv_mfma_f32_16x16x32_bf16 v[148:151], v[36:39], a[152:155], v[148:151]\n\tds_read_b64_tr_b16 v[24:25], v2 offset:40960\n\tds_read_b64_tr_b16 v[26:27], v2 offset:41472\n\tv_mfma_f32_16x16x32_bf16 v[156:159], v[36:39], a[156:159], v[156:159]\n\tv_mfma_f32_16x16x32_bf16 v[164:167], v[36:39], a[160:163], v[164:167]\n\t" \
    "s_waitcnt lgkmcnt(8)\n\tv_mfma_f32_16x16x32_bf16 v[144:147], v[40:43], a[164:167], v[144:147]\n\tds_read_b64_tr_b16 v[28:29], v3 offset:40960\n\tds_read_b64_tr_b16 v[30:31], v3 offset:41472\n\tv_mfma_f32_16x16x32_bf16 v[152:155], v[40:43], a[168:171], v[152:155]\n\tv_mfma_f32_16x16x32_bf16 v[160:163], v[40:43], a[172:175], v[160:163]\n\ts_waitcnt lgkmcnt(8)\n\tv_mfma_f32_16x16x32_bf16 v[148:151], v[44:47], a[164:167], v[148:151]\n\t" \
    "ds_read_b64_tr_b16 v[32:33], v2 offset:45056\n\tds_read_b64_tr_b16 v[34:35], v2 offset:45568\n\tv_mfma_f32_16x16x32_bf16 v[156:159], v[44:47], a[168:171], v[156:159]\n\tv_mfma_f32_16x16x32_bf16 v[164:167], v[44:47], a[172:175], v[164:167]\n\ts_barrier\n\ts_waitcnt lgkmcnt(8)\n\tv_mfma_f32_16x16x32_bf16 v[144:147], v[16:19], a[176:179], v[144:147]\n\tds_read_b64_tr_b16 v[36:37], v3 offset:45056\n\t" \
    "ds_read_b64_tr_b16 v[38:39], v3 offset:45568\n\tv_mfma_f32_16x16x32_bf16 v[152:155], v[16:19], a[180:183], v[152:155]\n\tv_mfma_f32_16x16x32_bf16 v[160:163], v[16:19], a[184:187], v[160:163]\n\ts_waitcnt lgkmcnt(8)\n\tv_mfma_f32_16x16x32_bf16 v[148:151], v[20:23], a[176:179], v[148:151]\n\tds_read_b64_tr_b16 v[40:41], v237 offset:28672\n\tds_read_b64_tr_b16 v[42:43], v237 offset:29184\n\tv_mfma_f32_16x16x32_bf16 v[156:159], v[20:23], a[180:183], v[156:159]\n\t" \
    "v_mfma_f32_16x16x32_bf16 v[164:167], v[20:23], a[184:187], v[164:167]\n\ts_waitcnt lgkmcnt(8)\n\tv_mfma_f32_16x16x32_bf16 v[144:147], v[24:27], a[188:191], v[144:147]\n\tds_read_b64_tr_b16 v[44:45], v238 offset:28672\n\tds_read_b64_tr_b16 v[46:47], v238 offset:29184\n\tv_mfma_f32_16x16x32_bf16 v[152:155], v[24:27], a[192:195], v[152:155]\n\tv_mfma_f32_16x16x32_bf16 v[160:163], v[24:27], a[196:199], v[160:163]\n\ts_waitcnt lgkmcnt(8)\n\t" \
    "v_mfma_f32_16x16x32_bf16 v[148:151], v[28:31], a[188:191], v[148:151]\n\tds_read_b64_tr_b16 v[16:17], v2 offset:49152\n\tds_read_b64_tr_b16 v[18:19], v2 offset:49664\n\tv_mfma_f32_16x16x32_bf16 v[156:159], v[28:31], a[192:195], v[156:159]\n\tv_mfma_f32_16x16x32_bf16 v[164:167], v[28:31], a[196:199], v[164:167]\n\ts_waitcnt lgkmcnt(8)\n\tv_mfma_f32_16x16x32_bf16 v[144:147], v[32:35], a[200:203], v[144:147]\n\tds_read_b64_tr_b16 v[20:21], v3 offset:49152\n\t" \
    "ds_read_b64_tr_b16 v[22:23], v3 offset:49664\n\tv_mfma_f32_16x16x32_bf16 v[152:155], v[32:35], a[204:207], v[152:155]\n\tv_mfma_f32_16x16x32_bf16 v[160:163], v[32:35], a[208:211], v[160:163]\n\ts_waitcnt vmcnt(1)\n\tbuffer_store_dwordx2 v[248:249], off, s[8:11], s49 sc1\n\ts_waitcnt lgkmcnt(8)\n\tv_mfma_f32_16x16x32_bf16 v[148:151], v[36:39], a[200:203], v[148:151]\n\tds_read_b64_tr_b16 v[24:25], v2 offset:53248\n\t" \
    "ds_read_b64_tr_b16 v[26:27], v2 offset:53760\n\tv_mfma_f32_16x16x32_bf16 v[156:159], v[36:39], a[204:207], v[156:159]\n\tv_mfma_f32_16x16x32_bf16 v[164:167], v[36:39], a[208:211], v[164:167]\n\ts_waitcnt lgkmcnt(8)\n\tv_mfma_f32_16x16x32_bf16 v[72:75], v[40:43], v[180:183], v[72:75]\n\tds_read_b64_tr_b16 v[28:29], v3 offset:53248\n\tds_read_b64_tr_b16 v[30:31], v3 offset:53760\n\ts_add_u32 s68, s52, 3328\n\t" \
    "v_mfma_f32_16x16x32_bf16 v[80:83], v[40:43], v[184:187], v[80:83]\n\tv_cvt_pk_bf16_f32 v144, v144, v145\n\tv_cvt_pk_bf16_f32 v145, v146, v147\n\tv_cvt_pk_bf16_f32 v146, v148, v149\n\tv_mfma_f32_16x16x32_bf16 v[88:91], v[40:43], v[188:191], v[88:91]\n\tv_cvt_pk_bf16_f32 v147, v150, v151\n\ts_waitcnt lgkmcnt(8)\n\tv_mfma_f32_16x16x32_bf16 v[76:79], v[44:47], v[180:183], v[76:79]\n\t" \
    "ds_read_b64_tr_b16 v[32:33], v2 offset:57344\n\tds_read_b64_tr_b16 v[34:35], v2 offset:57856\n\tv_mfma_f32_16x16x32_bf16 v[84:87], v[44:47], v[184:187], v[84:87]\n\tbuffer_store_dwordx4 v[144:147], v8, s[8:11], s68 offen offset:0 sc1\n\tv_cvt_pk_bf16_f32 v148, v152, v153\n\tv_cvt_pk_bf16_f32 v149, v154, v155\n\tv_mfma_f32_16x16x32_bf16 v[92:95], v[44:47], v[188:191], v[92:95]\n\tv_cvt_pk_bf16_f32 v150, v156, v157\n\t" \
    "v_cvt_pk_bf16_f32 v151, v158, v159\n\ts_waitcnt lgkmcnt(8)\n\tv_mfma_f32_16x16x32_bf16 v[72:75], v[16:19], a[56:59], v[72:75]\n\tds_read_b64_tr_b16 v[36:37], v3 offset:57344\n\tds_read_b64_tr_b16 v[38:39], v3 offset:57856\n\tv_mfma_f32_16x16x32_bf16 v[80:83], v[16:19], a[60:63], v[80:83]\n\tbuffer_store_dwordx4 v[148:151], v8, s[8:11], s68 offen offset:1024 sc1\n\tv_cvt_pk_bf16_f32 v152, v160, v161\n\t" \
    "v_cvt_pk_bf16_f32 v153, v162, v163\n\tv_mfma_f32_16x16x32_bf16 v[88:91], v[16:19], a[64:67], v[88:91]\n\tv_cvt_pk_bf16_f32 v154, v164, v165\n\tv_cvt_pk_bf16_f32 v155, v166, v167\n\ts_waitcnt lgkmcnt(8)\n\tv_mfma_f32_16x16x32_bf16 v[76:79], v[20:23], a[56:59], v[76:79]\n\tds_read_b64_tr_b16 v[40:41], v2 offset:61440\n\tds_read_b64_tr_b16 v[42:43], v2 offset:61952\n\t" \
    "v_mfma_f32_16x16x32_bf16 v[84:87], v[20:23], a[60:63], v[84:87]\n\tbuffer_store_dwordx4 v[152:155], v8, s[8:11], s68 offen offset:2048 sc1\n\tv_add3_u32 v156, s36, v144, v145\n\tv_add3_u32 v157, v146, v147, v148\n\tv_mfma_f32_16x16x32_bf16 v[92:95], v[20:23], a[64:67], v[92:95]\n\tv_add3_u32 v156, v156, v149, v150\n\tv_add3_u32 v157, v157, v151, v152\n\ts_waitcnt lgkmcnt(8)\n\t" \
    "v_mfma_f32_16x16x32_bf16 v[72:75], v[24:27], v[204:207], v[72:75]\n\tds_read_b64_tr_b16 v[44:45], v3 offset:61440\n\tds_read_b64_tr_b16 v[46:47], v3 offset:61952\n\tv_add3_u32 v156, v156, v153, v154\n\tv_mfma_f32_16x16x32_bf16 v[80:83], v[24:27], v[208:211], v[80:83]\n\tv_add3_u32 v156, v156, v157, v155\n\tv_mfma_f32_16x16x32_bf16 v[88:91], v[24:27], v[212:215], v[88:91]\n\ts_waitcnt lgkmcnt(8)\n\t" \
    "v_mfma_f32_16x16x32_bf16 v[76:79], v[28:31], v[204:207], v[76:79]\n\tds_read_b64_tr_b16 v[16:17], v237 offset:0\n\tds_read_b64_tr_b16 v[18:19], v237 offset:512\n\tv_mfma_f32_16x16x32_bf16 v[84:87], v[28:31], v[208:211], v[84:87]\n\tbuffer_store_dword v156, v236, s[8:11], s68 offen offset:3072 sc1\n\tv_mfma_f32_16x16x32_bf16 v[92:95], v[28:31], v[212:215], v[92:95]\n\ts_waitcnt lgkmcnt(8)\n\tv_mfma_f32_16x16x32_bf16 v[72:75], v[32:35], a[68:71], v[72:75]\n\t" \
    "ds_read_b64_tr_b16 v[20:21], v238 offset:0\n\tds_read_b64_tr_b16 v[22:23], v238 offset:512\n\tv_mfma_f32_16x16x32_bf16 v[80:83], v[32:35], a[72:75], v[80:83]\n\tv_mfma_f32_16x16x32_bf16 v[88:91], v[32:35], a[76:79], v[88:91]\n\ts_waitcnt lgkmcnt(8)\n\tv_mfma_f32_16x16x32_bf16 v[76:79], v[36:39], a[68:71], v[76:79]\n\tds_read_b64_tr_b16 v[24:25], v237 offset:4096\n\tds_read_b64_tr_b16 v[26:27], v237 offset:4608\n\t" \
    "v_mfma_f32_16x16x32_bf16 v[84:87], v[36:39], a[72:75], v[84:87]\n\tv_mfma_f32_16x16x32_bf16 v[92:95], v[36:39], a[76:79], v[92:95]\n\ts_waitcnt lgkmcnt(8)\n\tv_mfma_f32_16x16x32_bf16 v[72:75], v[40:43], v[216:219], v[72:75]\n\tds_read_b64_tr_b16 v[28:29], v238 offset:4096\n\tds_read_b64_tr_b16 v[30:31], v238 offset:4608\n\tv_mfma_f32_16x16x32_bf16 v[80:83], v[40:43], a[0:3], v[80:83]\n\tv_mfma_f32_16x16x32_bf16 v[88:91], v[40:43], a[4:7], v[88:91]\n\t" \
    "s_waitcnt lgkmcnt(8)\n\tv_mfma_f32_16x16x32_bf16 v[76:79], v[44:47], v[216:219], v[76:79]\n\tds_read_b64_tr_b16 v[32:33], v237 offset:8192\n\tds_read_b64_tr_b16 v[34:35], v237 offset:8704\n\tv_mfma_f32_16x16x32_bf16 v[84:87], v[44:47], a[0:3], v[84:87]\n\tv_mfma_f32_16x16x32_bf16 v[92:95], v[44:47], a[4:7], v[92:95]\n\ts_barrier\n\ts_waitcnt lgkmcnt(8)\n\t" \
    "v_mfma_f32_16x16x32_bf16 v[72:75], v[16:19], a[80:83], v[72:75]\n\tds_read_b64_tr_b16 v[36:37], v238 offset:8192\n\tds_read_b64_tr_b16 v[38:39], v238 offset:8704\n\ts_cmp_le_u32 s4, 0\n\ts_cselect_b32 s69, 262144, 0\n\tv_mfma_f32_16x16x32_bf16 v[80:83], v[16:19], a[84:87], v[80:83]\n\ts_add_u32 s69, s69, 0\n\ts_add_u32 m0, s54, 147456\n\t" \
    "v_mfma_f32_16x16x32_bf16 v[88:91], v[16:19], a[88:91], v[88:91]\n\ts_waitcnt lgkmcnt(8)\n\tv_mfma_f32_16x16x32_bf16 v[76:79], v[20:23], a[80:83], v[76:79]\n\tds_read_b64_tr_b16 v[40:41], v237 offset:12288\n\tds_read_b64_tr_b16 v[42:43], v237 offset:12800\n\tv_mfma_f32_16x16x32_bf16 v[84:87], v[20:23], a[84:87], v[84:87]\n\tbuffer_load_dwordx4 v12, s[16:19], s69 offen lds\n\ts_cmp_le_u32 s4, 0\n\t" \
    "s_cselect_b32 s69, 262144, 0\n\ts_add_u32 s69, s69, 0\n\tv_mfma_f32_16x16x32_bf16 v[92:95], v[20:23], a[88:91], v[92:95]\n\ts_add_u32 m0, s54, 151552\n\ts_waitcnt lgkmcnt(8)\n\tv_mfma_f32_16x16x32_bf16 v[72:75], v[24:27], a[8:11], v[72:75]\n\tds_read_b64_tr_b16 v[44:45], v238 offset:12288\n\tds_read_b64_tr_b16 v[46:47], v238 offset:12800\n\t" \
    "v_mfma_f32_16x16x32_bf16 v[80:83], v[24:27], a[12:15], v[80:83]\n\tbuffer_load_dwordx4 v13, s[16:19], s69 offen lds\n\ts_cmp_le_u32 s4, 1\n\ts_cselect_b32 s69, 262144, 0\n\ts_add_u32 s69, s69, 262144\n\tv_mfma_f32_16x16x32_bf16 v[88:91], v[24:27], a[16:19], v[88:91]\n\ts_add_u32 m0, s54, 155648\n\ts_waitcnt lgkmcnt(8)\n\t" \
    "v_mfma_f32_16x16x32_bf16 v[76:79], v[28:31], a[8:11], v[76:79]\n\tds_read_b64_tr_b16 v[16:17], v237 offset:16384\n\tds_read_b64_tr_b16 v[18:19], v237 offset:16896\n\tv_mfma_f32_16x16x32_bf16 v[84:87], v[28:31], a[12:15], v[84:87]\n\tbuffer_load_dwordx4 v12, s[16:19], s69 offen lds\n\ts_cmp_le_u32 s4, 1\n\ts_cselect_b32 s69, 262144, 0\n\ts_add_u32 s69, s69, 262144\n\t" \
    "v_mfma_f32_16x16x32_bf16 v[92:95], v[28:31], a[16:19], v[92:95]\n\ts_add_u32 m0, s54, 159744\n\ts_waitcnt lgkmcnt(8)\n\tv_mfma_f32_16x16x32_bf16 v[72:75], v[32:35], a[92:95], v[72:75]\n\tds_read_b64_tr_b16 v[20:21], v238 offset:16384\n\tds_read_b64_tr_b16 v[22:23], v238 offset:16896\n\tv_mfma_f32_16x16x32_bf16 v[80:83], v[32:35], a[96:99], v[80:83]\n\tbuffer_load_dwordx4 v13, s[16:19], s69 offen lds\n\t" \
    "s_cmp_le_u32 s4, 2\n\ts_cselect_b32 s69, 262144, 0\n\ts_add_u32 s69, s69, 524288\n\tv_mfma_f32_16x16x32_bf16 v[88:91], v[32:35], a[100:103], v[88:91]\n\ts_add_u32 m0, s54, 4096\n\ts_waitcnt lgkmcnt(8)\n\tv_mfma_f32_16x16x32_bf16 v[76:79], v[36:39], a[92:95], v[76:79]\n\tds_read_b64_tr_b16 v[24:25], v237 offset:20480\n\t" \
    "ds_read_b64_tr_b16 v[26:27], v237 offset:20992\n\tv_mfma_f32_16x16x32_bf16 v[84:87], v[36:39], a[96:99], v[84:87]\n\tbuffer_load_dwordx4 v12, s[16:19], s69 offen lds\n\ts_cmp_le_u32 s4, 2\n\ts_cselect_b32 s69, 262144, 0\n\ts_add_u32 s69, s69, 524288\n\tv_mfma_f32_16x16x32_bf16 v[92:95], v[36:39], a[100:103], v[92:95]\n\ts_add_u32 m0, s54, 8192\n\t" \
    "s_waitcnt lgkmcnt(8)\n\tv_mfma_f32_16x16x32_bf16 v[72:75], v[40:43], a[20:23], v[72:75]\n\tds_read_b64_tr_b16 v[28:29], v238 offset:20480\n\tds_read_b64_tr_b16 v[30:31], v238 offset:20992\n\tv_mfma_f32_16x16x32_bf16 v[80:83], v[40:43], a[24:27], v[80:83]\n\tbuffer_load_dwordx4 v13, s[16:19], s69 offen lds\n\ts_cmp_le_u32 s4, 3\n\ts_cselect_b32 s69, 262144, 0\n\t" \
    "s_add_u32 s69, s69, 786432\n\tv_mfma_f32_16x16x32_bf16 v[88:91], v[40:43], a[28:31], v[88:91]\n\ts_add_u32 m0, s54, 12288\n\ts_waitcnt lgkmcnt(8)\n\tv_mfma_f32_16x16x32_bf16 v[76:79], v[44:47], a[20:23], v[76:79]\n\tds_read_b64_tr_b16 v[32:33], v237 offset:24576\n\tds_read_b64_tr_b16 v[34:35], v237 offset:25088\n\tv_mfma_f32_16x16x32_bf16 v[84:87], v[44:47], a[24:27], v[84:87]\n\t" \
    "buffer_load_dwordx4 v12, s[16:19], s69 offen lds\n\ts_cmp_le_u32 s4, 3\n\ts_cselect_b32 s69, 262144, 0\n\ts_add_u32 s69, s69, 786432\n\tv_mfma_f32_16x16x32_bf16 v[92:95], v[44:47], a[28:31], v[92:95]\n\ts_add_u32 m0, s54, 16384\n\ts_waitcnt lgkmcnt(8)\n\tv_mfma_f32_16x16x32_bf16 v[72:75], v[16:19], a[104:107], v[72:75]\n\t" \
    "ds_read_b64_tr_b16 v[36:37], v238 offset:24576\n\tds_read_b64_tr_b16 v[38:39], v238 offset:25088\n\tv_mfma_f32_16x16x32_bf16 v[80:83], v[16:19], a[108:111], v[80:83]\n\tbuffer_load_dwordx4 v13, s[16:19], s69 offen lds\n\ts_cmp_le_u32 s4, 4\n\ts_cselect_b32 s69, 262144, 0\n\ts_add_u32 s69, s69, 1048576\n\tv_mfma_f32_16x16x32_bf16 v[88:91], v[16:19], a[112:115], v[88:91]\n\t" \
    "s_add_u32 m0, s54, 20480\n\ts_waitcnt lgkmcnt(8)\n\tv_mfma_f32_16x16x32_bf16 v[76:79], v[20:23], a[104:107], v[76:79]\n\tds_read_b64_tr_b16 v[40:41], v237 offset:32768\n\tds_read_b64_tr_b16 v[42:43], v237 offset:33280\n\tv_mfma_f32_16x16x32_bf16 v[84:87], v[20:23], a[108:111], v[84:87]\n\tv_mfma_f32_16x16x32_bf16 v[92:95], v[20:23], a[112:115], v[92:95]\n\ts_waitcnt lgkmcnt(8)\n\t" \
    "v_mfma_f32_16x16x32_bf16 v[72:75], v[24:27], a[32:35], v[72:75]\n\tds_read_b64_tr_b16 v[44:45], v238 offset:32768\n\tds_read_b64_tr_b16 v[46:47], v238 offset:33280\n\tv_mfma_f32_16x16x32_bf16 v[80:83], v[24:27], a[36:39], v[80:83]\n\tbuffer_load_dwordx4 v12, s[16:19], s69 offen lds\n\ts_cmp_le_u32 s4, 4\n\ts_cselect_b32 s69, 262144, 0\n\ts_add_u32 s69, s69, 1048576\n\t" \
    "v_mfma_f32_16x16x32_bf16 v[88:91], v[24:27], a[40:43], v[88:91]\n\ts_add_u32 m0, s54, 24576\n\ts_waitcnt lgkmcnt(8)\n\tv_mfma_f32_16x16x32_bf16 v[76:79], v[28:31], a[32:35], v[76:79]\n\tds_read_b64_tr_b16 v[16:17], v237 offset:36864\n\tds_read_b64_tr_b16 v[18:19], v237 offset:37376\n\tv_mfma_f32_16x16x32_bf16 v[84:87], v[28:31], a[36:39], v[84:87]\n\tbuffer_load_dwordx4 v13, s[16:19], s69 offen lds\n\t" \
    "s_cmp_le_u32 s4, 5\n\ts_cselect_b32 s69, 262144, 0\n\ts_add_u32 s69, s69, 1310720\n\tv_mfma_f32_16x16x32_bf16 v[92:95], v[28:31], a[40:43], v[92:95]\n\ts_add_u32 m0, s54, 28672\n\ts_waitcnt lgkmcnt(8)\n\tv_mfma_f32_16x16x32_bf16 v[72:75], v[32:35], v[168:171], v[72:75]\n\tds_read_b64_tr_b16 v[20:21], v238 offset:36864\n\t" \
    "ds_read_b64_tr_b16 v[22:23], v238 offset:37376\n\tv_mfma_f32_16x16x32_bf16 v[80:83], v[32:35], v[172:175], v[80:83]\n\tbuffer_load_dwordx4 v12, s[16:19], s69 offen lds\n\ts_add_u32 s68, s72, 3328\n\tv_mfma_f32_16x16x32_bf16 v[88:91], v[32:35], v[176:179], v[88:91]\n\ts_waitcnt lgkmcnt(8)\n\tv_mfma_f32_16x16x32_bf16 v[76:79], v[36:39], v[168:171], v[76:79]\n\tds_read_b64_tr_b16 v[24:25], v237 offset:40960\n\t" \
    "ds_read_b64_tr_b16 v[26:27], v237 offset:41472\n\tv_mfma_f32_16x16x32_bf16 v[84:87], v[36:39], v[172:175], v[84:87]\n\tv_mfma_f32_16x16x32_bf16 v[92:95], v[36:39], v[176:179], v[92:95]\n\ts_waitcnt lgkmcnt(8)\n\tv_mfma_f32_16x16x32_bf16 v[72:75], v[40:43], a[116:119], v[72:75]\n\tds_read_b64_tr_b16 v[28:29], v238 offset:40960\n\tds_read_b64_tr_b16 v[30:31], v238 offset:41472\n\tv_mfma_f32_16x16x32_bf16 v[80:83], v[40:43], a[120:123], v[80:83]\n\t" \
    "buffer_load_dwordx4 v[144:147], v8, s[8:11], s68 offen offset:0 sc1\n\tv_mfma_f32_16x16x32_bf16 v[88:91], v[40:43], a[124:127], v[88:91]\n\ts_waitcnt lgkmcnt(8)\n\tv_mfma_f32_16x16x32_bf16 v[76:79], v[44:47], a[116:119], v[76:79]\n\tds_read_b64_tr_b16 v[32:33], v237 offset:45056\n\tds_read_b64_tr_b16 v[34:35], v237 offset:45568\n\tv_mfma_f32_16x16x32_bf16 v[84:87], v[44:47], a[120:123], v[84:87]\n\tbuffer_load_dwordx4 v[148:151], v8, s[8:11], s68 offen offset:1024 sc1\n\t" \
    "v_mfma_f32_16x16x32_bf16 v[92:95], v[44:47], a[124:127], v[92:95]\n\ts_barrier\n\ts_waitcnt lgkmcnt(8)\n\tv_mfma_f32_16x16x32_bf16 v[72:75], v[16:19], a[44:47], v[72:75]\n\tds_read_b64_tr_b16 v[36:37], v238 offset:45056\n\tds_read_b64_tr_b16 v[38:39], v238 offset:45568\n\tv_mfma_f32_16x16x32_bf16 v[80:83], v[16:19], a[48:51], v[80:83]\n\tbuffer_load_dwordx4 v[152:155], v8, s[8:11], s68 offen offset:2048 sc1\n\t" \
    "v_mfma_f32_16x16x32_bf16 v[88:91], v[16:19], a[52:55], v[88:91]\n\ts_waitcnt lgkmcnt(8)\n\tv_mfma_f32_16x16x32_bf16 v[76:79], v[20:23], a[44:47], v[76:79]\n\tds_read_b64_tr_b16 v[40:41], v237 offset:49152\n\tds_read_b64_tr_b16 v[42:43], v237 offset:49664\n\tv_mfma_f32_16x16x32_bf16 v[84:87], v[20:23], a[48:51], v[84:87]\n\tbuffer_load_dword v156, v236, s[8:11], s68 offen offset:3072 sc1\n\ts_cmp_le_u32 s4, 5\n\t" \
    "s_cselect_b32 s69, 262144, 0\n\ts_add_u32 s69, s69, 1310720\n\tv_mfma_f32_16x16x32_bf16 v[92:95], v[20:23], a[52:55], v[92:95]\n\ts_add_u32 m0, s54, 32768\n\ts_waitcnt lgkmcnt(8)\n\tv_mfma_f32_16x16x32_bf16 v[72:75], v[24:27], a[128:131], v[72:75]\n\tds_read_b64_tr_b16 v[44:45], v238 offset:49152\n\tds_read_b64_tr_b16 v[46:47], v238 offset:49664\n\t" \
    "v_mfma_f32_16x16x32_bf16 v[80:83], v[24:27], a[132:135], v[80:83]\n\tbuffer_load_dwordx4 v13, s[16:19], s69 offen lds\n\ts_cmp_le_u32 s4, 6\n\ts_cselect_b32 s69, 262144, 0\n\ts_add_u32 s69, s69, 1572864\n\tv_mfma_f32_16x16x32_bf16 v[88:91], v[24:27], a[136:139], v[88:91]\n\ts_add_u32 m0, s54, 94208\n\ts_waitcnt lgkmcnt(8)\n\t" \
    "v_mfma_f32_16x16x32_bf16 v[76:79], v[28:31], a[128:131], v[76:79]\n\tds_read_b64_tr_b16 v[16:17], v237 offset:53248\n\tds_read_b64_tr_b16 v[18:19], v237 offset:53760\n\tv_mfma_f32_16x16x32_bf16 v[84:87], v[28:31], a[132:135], v[84:87]\n\tv_mfma_f32_16x16x32_bf16 v[92:95], v[28:31], a[136:139], v[92:95]\n\ts_waitcnt lgkmcnt(8)\n\tv_mfma_f32_16x16x32_bf16 v[72:75], v[32:35], a[140:143], v[72:75]\n\tds_read_b64_tr_b16 v[20:21], v238 offset:53248\n\t" \
    "ds_read_b64_tr_b16 v[22:23], v238 offset:53760\n\tv_mfma_f32_16x16x32_bf16 v[80:83], v[32:35], a[144:147], v[80:83]\n\tbuffer_load_dwordx4 v12, s[16:19], s69 offen lds\n\ts_cmp_le_u32 s4, 6\n\ts_cselect_b32 s69, 262144, 0\n\ts_add_u32 s69, s69, 1572864\n\tv_mfma_f32_16x16x32_bf16 v[88:91], v[32:35], a[148:151], v[88:91]\n\ts_add_u32 m0, s54, 36864\n\t" \
    "s_waitcnt lgkmcnt(8)\n\tv_mfma_f32_16x16x32_bf16 v[76:79], v[36:39], a[140:143], v[76:79]\n\tds_read_b64_tr_b16 v[24:25], v237 offset:57344\n\tds_read_b64_tr_b16 v[26:27], v237 offset:57856\n\tv_mfma_f32_16x16x32_bf16 v[84:87], v[36:39], a[144:147], v[84:87]\n\tbuffer_load_dwordx4 v13, s[16:19], s69 offen lds\n\ts_cmp_le_u32 s4, 7\n\ts_cselect_b32 s69, 262144, 0\n\t" \
    "s_add_u32 s69, s69, 1835008\n\tv_mfma_f32_16x16x32_bf16 v[92:95], v[36:39], a[148:151], v[92:95]\n\ts_add_u32 m0, s54, 40960\n\ts_waitcnt lgkmcnt(8)\n\tv_mfma_f32_16x16x32_bf16 v[72:75], v[40:43], a[152:155], v[72:75]\n\tds_read_b64_tr_b16 v[28:29], v238 offset:57344\n\tds_read_b64_tr_b16 v[30:31], v238 offset:57856\n\tv_mfma_f32_16x16x32_bf16 v[80:83], v[40:43], a[156:159], v[80:83]\n\t" \
    "buffer_load_dwordx4 v12, s[16:19], s69 offen lds\n\ts_cmp_le_u32 s4, 7\n\ts_cselect_b32 s69, 262144, 0\n\ts_add_u32 s69, s69, 1835008\n\tv_mfma_f32_16x16x32_bf16 v[88:91], v[40:43], a[160:163], v[88:91]\n\ts_add_u32 m0, s54, 45056\n\ts_waitcnt lgkmcnt(8)\n\tv_mfma_f32_16x16x32_bf16 v[76:79], v[44:47], a[152:155], v[76:79]\n\t" \
    "ds_read_b64_tr_b16 v[32:33], v237 offset:61440\n\tds_read_b64_tr_b16 v[34:35], v237 offset:61952\n\tv_mfma_f32_16x16x32_bf16 v[84:87], v[44:47], a[156:159], v[84:87]\n\tv_mfma_f32_16x16x32_bf16 v[92:95], v[44:47], a[160:163], v[92:95]\n\ts_waitcnt lgkmcnt(8)\n\tv_mfma_f32_16x16x32_bf16 v[72:75], v[16:19], a[164:167], v[72:75]\n\tds_read_b64_tr_b16 v[36:37], v238 offset:61440\n\tds_read_b64_tr_b16 v[38:39], v238 offset:61952\n\t" \
    "v_mfma_f32_16x16x32_bf16 v[80:83], v[16:19], a[168:171], v[80:83]\n\tbuffer_load_dwordx4 v13, s[16:19], s69 offen lds\n\ts_cmp_le_u32 s4, 8\n\ts_cselect_b32 s69, 262144, 0\n\ts_add_u32 s69, s69, 2097152\n\tv_mfma_f32_16x16x32_bf16 v[88:91], v[16:19], a[172:175], v[88:91]\n\ts_add_u32 m0, s54, 49152\n\ts_waitcnt lgkmcnt(8)\n\t" \
    "v_mfma_f32_16x16x32_bf16 v[76:79], v[20:23], a[164:167], v[76:79]\n\tds_read_b64_tr_b16 v[40:41], v2 offset:0\n\tds_read_b64_tr_b16 v[42:43], v2 offset:512\n\tv_mfma_f32_16x16x32_bf16 v[84:87], v[20:23], a[168:171], v[84:87]\n\tbuffer_load_dwordx4 v12, s[16:19], s69 offen lds\n\ts_cmp_le_u32 s4, 8\n\ts_cselect_b32 s69, 262144, 0\n\ts_add_u32 s69, s69, 2097152\n\t" \
    "v_mfma_f32_16x16x32_bf16 v[92:95], v[20:23], a[172:175], v[92:95]\n\ts_add_u32 m0, s54, 53248\n\ts_waitcnt lgkmcnt(8)\n\tv_mfma_f32_16x16x32_bf16 v[72:75], v[24:27], a[176:179], v[72:75]\n\tds_read_b64_tr_b16 v[44:45], v3 offset:0\n\tds_read_b64_tr_b16 v[46:47], v3 offset:512\n\tv_mfma_f32_16x16x32_bf16 v[80:83], v[24:27], a[180:183], v[80:83]\n\tbuffer_load_dwordx4 v13, s[16:19], s69 offen lds\n\t" \
    "s_cmp_le_u32 s4, 9\n\ts_cselect_b32 s69, 262144, 0\n\ts_add_u32 s69, s69, 2359296\n\tv_mfma_f32_16x16x32_bf16 v[88:91], v[24:27], a[184:187], v[88:91]\n\ts_add_u32 m0, s54, 57344\n\ts_waitcnt lgkmcnt(8)\n\tv_mfma_f32_16x16x32_bf16 v[76:79], v[28:31], a[176:179], v[76:79]\n\tds_read_b64_tr_b16 v[16:17], v220 offset:0\n\t" \
    "ds_read_b64_tr_b16 v[18:19], v220 offset:2048\n\tv_mfma_f32_16x16x32_bf16 v[84:87], v[28:31], a[180:183], v[84:87]\n\tbuffer_load_dwordx4 v12, s[16:19], s69 offen lds\n\ts_cmp_le_u32 s4, 9\n\ts_cselect_b32 s69, 262144, 0\n\ts_add_u32 s69, s69, 2359296\n\tv_mfma_f32_16x16x32_bf16 v[92:95], v[28:31], a[184:187], v[92:95]\n\ts_add_u32 m0, s54, 61440\n\t" \
    "s_waitcnt lgkmcnt(8)\n\tv_mfma_f32_16x16x32_bf16 v[72:75], v[32:35], a[188:191], v[72:75]\n\tds_read_b64_tr_b16 v[20:21], v221 offset:0\n\tds_read_b64_tr_b16 v[22:23], v221 offset:2048\n\tv_mfma_f32_16x16x32_bf16 v[80:83], v[32:35], a[192:195], v[80:83]\n\tv_mfma_f32_16x16x32_bf16 v[88:91], v[32:35], a[196:199], v[88:91]\n\ts_waitcnt lgkmcnt(8)\n\tv_mfma_f32_16x16x32_bf16 v[76:79], v[36:39], a[188:191], v[76:79]\n\t" \
    "ds_read_b64_tr_b16 v[24:25], v222 offset:0\n\tds_read_b64_tr_b16 v[26:27], v222 offset:2048\n\tv_mfma_f32_16x16x32_bf16 v[84:87], v[36:39], a[192:195], v[84:87]\n\tbuffer_load_dwordx4 v13, s[16:19], s69 offen lds\n\tv_mfma_f32_16x16x32_bf16 v[92:95], v[36:39], a[196:199], v[92:95]\n\ts_waitcnt lgkmcnt(8)\n\tv_mfma_f32_16x16x32_bf16 v[72:75], v[40:43], a[200:203], v[72:75]\n\tds_read_b64_tr_b16 v[28:29], v223 offset:0\n\t" \
    "ds_read_b64_tr_b16 v[30:31], v223 offset:2048\n\tv_mfma_f32_16x16x32_bf16 v[80:83], v[40:43], a[204:207], v[80:83]\n\tv_mfma_f32_16x16x32_bf16 v[88:91], v[40:43], a[208:211], v[88:91]\n\ts_waitcnt lgkmcnt(8)\n\tv_mfma_f32_16x16x32_bf16 v[76:79], v[44:47], a[200:203], v[76:79]\n\tds_read_b64_tr_b16 v[32:33], v220 offset:4096\n\tds_read_b64_tr_b16 v[34:35], v220 offset:6144\n\tv_mfma_f32_16x16x32_bf16 v[84:87], v[44:47], a[204:207], v[84:87]\n\t" \
    "v_mfma_f32_16x16x32_bf16 v[92:95], v[44:47], a[208:211], v[92:95]\n\ts_mov_b32 s71, 1048576\n\ts_waitcnt vmcnt(9)\n\t109:\n\tv_add3_u32 v158, s36, v144, v145\n\tv_add3_u32 v159, v146, v147, v148\n\tv_add3_u32 v158, v158, v149, v150\n\tv_add3_u32 v159, v159, v151, v152\n\t" \
    "v_add3_u32 v158, v158, v153, v154\n\tv_add3_u32 v158, v158, v159, v155\n\tv_cmp_ne_u32 vcc, v158, v156\n\ts_cbranch_vccz 110f\n\ts_sub_u32 s71, s71, 1\n\ts_cbranch_scc1 110f\n\ts_sleep 6\n\ts_add_u32 s73, s72, 3328\n\t" \
    "buffer_load_dwordx4 v[144:147], v8, s[8:11], s73 offen offset:0 sc1\n\tbuffer_load_dwordx4 v[148:151], v8, s[8:11], s73 offen offset:1024 sc1\n\tbuffer_load_dwordx4 v[152:155], v8, s[8:11], s73 offen offset:2048 sc1\n\tbuffer_load_dword v156, v236, s[8:11], s73 offen offset:3072 sc1\n\ts_waitcnt vmcnt(0)\n\ts_branch 109b\n\t110:\n\ts_add_u32 s45, s45, 55296\n\t" \
    "v_lshlrev_b32 v96, 16, v144\n\tv_and_b32 v97, 0xffff0000, v144\n\tv_lshlrev_b32 v98, 16, v145\n\tv_and_b32 v99, 0xffff0000, v145\n\tv_lshlrev_b32 v100, 16, v146\n\tv_and_b32 v101, 0xffff0000, v146\n\tv_lshlrev_b32 v102, 16, v147\n\tv_and_b32 v103, 0xffff0000, v147\n\t" \
    "v_pk_add_f32 v[72:73], v[72:73], v[96:97]\n\tv_pk_add_f32 v[74:75], v[74:75], v[98:99]\n\tv_pk_add_f32 v[76:77], v[76:77], v[100:101]\n\tv_pk_add_f32 v[78:79], v[78:79], v[102:103]\n\tv_pk_mul_f32 v[96:97], v[72:73], v[72:73]\n\tv_pk_mul_f32 v[98:99], v[74:75], v[74:75]\n\tv_pk_mul_f32 v[100:101], v[76:77], v[76:77]\n\tv_pk_mul_f32 v[102:103], v[78:79], v[78:79]\n\t" \
    "v_pk_fma_f32 v[96:97], v[96:97], v[246:247], v[246:247] op_sel:[0,0,1] op_sel_hi:[1,0,1]\n\tv_pk_fma_f32 v[98:99], v[98:99], v[246:247], v[246:247] op_sel:[0,0,1] op_sel_hi:[1,0,1]\n\tv_pk_fma_f32 v[100:101], v[100:101], v[246:247], v[246:247] op_sel:[0,0,1] op_sel_hi:[1,0,1]\n\tv_pk_fma_f32 v[102:103], v[102:103], v[246:247], v[246:247] op_sel:[0,0,1] op_sel_hi:[1,0,1]\n\tv_pk_mul_f32 v[96:97], v[96:97], v[72:73]\n\tv_pk_mul_f32 v[98:99], v[98:99], v[74:75]\n\tv_pk_mul_f32 v[100:101], v[100:101], v[76:77]\n\tv_pk_mul_f32 v[102:103], v[102:103], v[78:79]\n\t" \
    "v_lshlrev_b32 v104, 16, v148\n\tv_exp_f32 v96, v96\n\tv_and_b32 v105, 0xffff0000, v148\n\tv_exp_f32 v97, v97\n\tv_lshlrev_b32 v106, 16, v149\n\tv_exp_f32 v98, v98\n\tv_and_b32 v107, 0xffff0000, v149\n\tv_exp_f32 v99, v99\n\t" \
    "v_lshlrev_b32 v108, 16, v150\n\tv_exp_f32 v100, v100\n\tv_and_b32 v109, 0xffff0000, v150\n\tv_exp_f32 v101, v101\n\tv_lshlrev_b32 v110, 16, v151\n\tv_exp_f32 v102, v102\n\tv_and_b32 v111, 0xffff0000, v151\n\tv_exp_f32 v103, v103\n\t" \
    "v_pk_add_f32 v[80:81], v[80:81], v[104:105]\n\tv_pk_add_f32 v[96:97], v[96:97], 1.0 op_sel_hi:[1,0]\n\tv_pk_add_f32 v[98:99], v[98:99], 1.0 op_sel_hi:[1,0]\n\tv_pk_add_f32 v[100:101], v[100:101], 1.0 op_sel_hi:[1,0]\n\tv_pk_add_f32 v[102:103], v[102:103], 1.0 op_sel_hi:[1,0]\n\tv_pk_add_f32 v[82:83], v[82:83], v[106:107]\n\tv_rcp_f32 v96, v96\n\tv_pk_add_f32 v[84:85], v[84:85], v[108:109]\n\t" \
    "v_rcp_f32 v97, v97\n\tv_pk_add_f32 v[86:87], v[86:87], v[110:111]\n\tv_rcp_f32 v98, v98\n\tv_pk_mul_f32 v[104:105], v[80:81], v[80:81]\n\tv_rcp_f32 v99, v99\n\tv_pk_mul_f32 v[106:107], v[82:83], v[82:83]\n\tv_rcp_f32 v100, v100\n\tv_pk_mul_f32 v[108:109], v[84:85], v[84:85]\n\t" \
    "v_rcp_f32 v101, v101\n\tv_pk_mul_f32 v[110:111], v[86:87], v[86:87]\n\tv_rcp_f32 v102, v102\n\tv_pk_fma_f32 v[104:105], v[104:105], v[246:247], v[246:247] op_sel:[0,0,1] op_sel_hi:[1,0,1]\n\tv_rcp_f32 v103, v103\n\tv_pk_fma_f32 v[106:107], v[106:107], v[246:247], v[246:247] op_sel:[0,0,1] op_sel_hi:[1,0,1]\n\tv_pk_mul_f32 v[96:97], v[96:97], v[72:73]\n\tv_pk_mul_f32 v[98:99], v[98:99], v[74:75]\n\t" \
    "v_pk_mul_f32 v[100:101], v[100:101], v[76:77]\n\tv_pk_mul_f32 v[102:103], v[102:103], v[78:79]\n\tv_cvt_pk_bf16_f32 v72, v96, v97\n\tv_cvt_pk_bf16_f32 v73, v98, v99\n\tv_cvt_pk_bf16_f32 v74, v100, v101\n\tv_cvt_pk_bf16_f32 v75, v102, v103\n\tv_pk_fma_f32 v[108:109], v[108:109], v[246:247], v[246:247] op_sel:[0,0,1] op_sel_hi:[1,0,1]\n\tv_pk_fma_f32 v[110:111], v[110:111], v[246:247], v[246:247] op_sel:[0,0,1] op_sel_hi:[1,0,1]\n\t" \
    "v_pk_mul_f32 v[104:105], v[104:105], v[80:81]\n\tv_pk_mul_f32 v[106:107], v[106:107], v[82:83]\n\tv_pk_mul_f32 v[108:109], v[108:109], v[84:85]\n\tv_pk_mul_f32 v[110:111], v[110:111], v[86:87]\n\tv_lshlrev_b32 v112, 16, v152\n\tv_exp_f32 v104, v104\n\tv_and_b32 v113, 0xffff0000, v152\n\tv_exp_f32 v105, v105\n\t" \
    "v_lshlrev_b32 v114, 16, v153\n\tv_exp_f32 v106, v106\n\tv_and_b32 v115, 0xffff0000, v153\n\tv_exp_f32 v107, v107\n\tv_lshlrev_b32 v116, 16, v154\n\tv_exp_f32 v108, v108\n\tv_and_b32 v117, 0xffff0000, v154\n\tv_exp_f32 v109, v109\n\t" \
    "v_lshlrev_b32 v118, 16, v155\n\tv_exp_f32 v110, v110\n\tv_and_b32 v119, 0xffff0000, v155\n\tv_exp_f32 v111, v111\n\tv_pk_add_f32 v[88:89], v[88:89], v[112:113]\n\tv_pk_add_f32 v[104:105], v[104:105], 1.0 op_sel_hi:[1,0]\n\tv_pk_add_f32 v[106:107], v[106:107], 1.0 op_sel_hi:[1,0]\n\tv_pk_add_f32 v[108:109], v[108:109], 1.0 op_sel_hi:[1,0]\n\t" \
    "v_pk_add_f32 v[110:111], v[110:111], 1.0 op_sel_hi:[1,0]\n\tv_pk_add_f32 v[90:91], v[90:91], v[114:115]\n\tv_rcp_f32 v104, v104\n\tv_pk_add_f32 v[92:93], v[92:93], v[116:117]\n\tv_rcp_f32 v105, v105\n\tv_pk_add_f32 v[94:95], v[94:95], v[118:119]\n\tv_rcp_f32 v106, v106\n\tv_pk_mul_f32 v[112:113], v[88:89], v[88:89]\n\t" \
    "v_rcp_f32 v107, v107\n\tv_pk_mul_f32 v[114:115], v[90:91], v[90:91]\n\tv_rcp_f32 v108, v108\n\tv_pk_mul_f32 v[116:117], v[92:93], v[92:93]\n\tv_rcp_f32 v109, v109\n\tv_pk_mul_f32 v[118:119], v[94:95], v[94:95]\n\tv_rcp_f32 v110, v110\n\tv_pk_fma_f32 v[112:113], v[112:113], v[246:247], v[246:247] op_sel:[0,0,1] op_sel_hi:[1,0,1]\n\t" \
    "v_rcp_f32 v111, v111\n\tv_pk_fma_f32 v[114:115], v[114:115], v[246:247], v[246:247] op_sel:[0,0,1] op_sel_hi:[1,0,1]\n\tv_pk_mul_f32 v[104:105], v[104:105], v[80:81]\n\tv_pk_mul_f32 v[106:107], v[106:107], v[82:83]\n\tv_pk_mul_f32 v[108:109], v[108:109], v[84:85]\n\tv_pk_mul_f32 v[110:111], v[110:111], v[86:87]\n\tv_cvt_pk_bf16_f32 v80, v104, v105\n\tv_cvt_pk_bf16_f32 v81, v106, v107\n\t" \
    "v_cvt_pk_bf16_f32 v82, v108, v109\n\tv_cvt_pk_bf16_f32 v83, v110, v111\n\tbuffer_load_dwordx2 v[250:251], v241, s[8:11], s50 offen sc1\n\tv_pk_fma_f32 v[116:117], v[116:117], v[246:247], v[246:247] op_sel:[0,0,1] op_sel_hi:[1,0,1]\n\tv_pk_fma_f32 v[118:119], v[118:119], v[246:247], v[246:247] op_sel:[0,0,1] op_sel_hi:[1,0,1]\n\tv_pk_mul_f32 v[112:113], v[112:113], v[88:89]\n\tv_pk_mul_f32 v[114:115], v[114:115], v[90:91]\n\tv_pk_mul_f32 v[116:117], v[116:117], v[92:93]\n\t" \
    "v_pk_mul_f32 v[118:119], v[118:119], v[94:95]\n\tv_exp_f32 v112, v112\n\tv_exp_f32 v113, v113\n\tv_exp_f32 v114, v114\n\tv_exp_f32 v115, v115\n\tv_exp_f32 v116, v116\n\tv_exp_f32 v117, v117\n\tv_exp_f32 v118, v118\n\t" \
    "v_exp_f32 v119, v119\n\tv_pk_add_f32 v[112:113], v[112:113], 1.0 op_sel_hi:[1,0]\n\tv_pk_add_f32 v[114:115], v[114:115], 1.0 op_sel_hi:[1,0]\n\tv_pk_add_f32 v[116:117], v[116:117], 1.0 op_sel_hi:[1,0]\n\tv_pk_add_f32 v[118:119], v[118:119], 1.0 op_sel_hi:[1,0]\n\tv_rcp_f32 v112, v112\n\tv_rcp_f32 v113, v113\n\tv_rcp_f32 v114, v114\n\t" \
    "v_rcp_f32 v115, v115\n\tv_rcp_f32 v116, v116\n\tv_rcp_f32 v117, v117\n\tv_rcp_f32 v118, v118\n\tv_rcp_f32 v119, v119\n\tv_pk_mul_f32 v[112:113], v[112:113], v[88:89]\n\tv_pk_mul_f32 v[114:115], v[114:115], v[90:91]\n\tv_pk_mul_f32 v[116:117], v[116:117], v[92:93]\n\t" \
    "v_pk_mul_f32 v[118:119], v[118:119], v[94:95]\n\tv_cvt_pk_bf16_f32 v88, v112, v113\n\tv_cvt_pk_bf16_f32 v89, v114, v115\n\tv_cvt_pk_bf16_f32 v90, v116, v117\n\tv_cvt_pk_bf16_f32 v91, v118, v119\n\ts_cmp_lg_u32 s39, 2\n\ts_cbranch_scc1 102f\n\ts_barrier\n\t" \
    "s_waitcnt lgkmcnt(8)\n\tv_mfma_f32_16x16x32_bf16 v[120:123], v[192:195], v[16:19], 0\n\tds_read_b64_tr_b16 v[36:37], v221 offset:4096\n\tds_read_b64_tr_b16 v[38:39], v221 offset:6144\n\tv_mfma_f32_16x16x32_bf16 v[128:131], v[196:199], v[16:19], 0\n\tv_mfma_f32_16x16x32_bf16 v[136:139], v[200:203], v[16:19], 0\n\ts_waitcnt lgkmcnt(8)\n\tv_mfma_f32_16x16x32_bf16 v[124:127], v[192:195], v[20:23], 0\n\t" \
    "ds_read_b64_tr_b16 v[40:41], v222 offset:4096\n\tds_read_b64_tr_b16 v[42:43], v222 offset:6144\n\tv_mfma_f32_16x16x32_bf16 v[132:135], v[196:199], v[20:23], 0\n\tv_mfma_f32_16x16x32_bf16 v[140:143], v[200:203], v[20:23], 0\n\ts_waitcnt lgkmcnt(8)\n\tv_mfma_f32_16x16x32_bf16 v[144:147], v[192:195], v[24:27], 0\n\tds_read_b64_tr_b16 v[44:45], v223 offset:4096\n\tds_read_b64_tr_b16 v[46:47], v223 offset:6144\n\t" \
    "v_mfma_f32_16x16x32_bf16 v[152:155], v[196:199], v[24:27], 0\n\tv_mfma_f32_16x16x32_bf16 v[160:163], v[200:203], v[24:27], 0\n\ts_waitcnt lgkmcnt(8)\n\tv_mfma_f32_16x16x32_bf16 v[148:151], v[192:195], v[28:31], 0\n\tds_read_b64_tr_b16 v[16:17], v220 offset:8192\n\tds_read_b64_tr_b16 v[18:19], v220 offset:10240\n\tv_mfma_f32_16x16x32_bf16 v[156:159], v[196:199], v[28:31], 0\n\tv_mfma_f32_16x16x32_bf16 v[164:167], v[200:203], v[28:31], 0\n\t" \
    "s_waitcnt vmcnt(0)\n\tv_cmp_ne_u64 vcc, s[36:37], v[250:251]\n\ts_cbranch_vccz 112f\n\ts_mov_b32 s71, 1048576\n\t111:\n\ts_sleep 1\n\tbuffer_load_dwordx2 v[250:251], v241, s[8:11], s50 offen sc1\n\ts_waitcnt vmcnt(0)\n\t" \
    "v_cmp_ne_u64 vcc, s[36:37], v[250:251]\n\ts_cbranch_vccz 112f\n\ts_sub_u32 s71, s71, 1\n\ts_cbranch_scc0 111b\n\t112:\n\ts_cmp_le_u32 s4, 0\n\ts_cselect_b32 s68, 3072, 0\n\ts_add_u32 s68, s68, s46\n\t" \
    "s_add_u32 s68, s68, 0\n\tbuffer_load_dwordx4 v[216:219], v8, s[8:11], s68 offen offset:0 sc1\n\tbuffer_load_dwordx4 v[204:207], v8, s[8:11], s68 offen offset:1024 sc1\n\tbuffer_load_dwordx4 v[208:211], v8, s[8:11], s68 offen offset:2048 sc1\n\ts_cmp_le_u32 s4, 1\n\ts_cselect_b32 s68, 3072, 0\n\ts_add_u32 s68, s68, s46\n\ts_add_u32 s68, s68, 3072\n\t" \
    "buffer_load_dwordx4 v[212:215], v8, s[8:11], s68 offen offset:0 sc1\n\tbuffer_load_dwordx4 a[0:3], v8, s[8:11], s68 offen offset:1024 sc1\n\tbuffer_load_dwordx4 a[4:7], v8, s[8:11], s68 offen offset:2048 sc1\n\ts_cmp_le_u32 s4, 2\n\ts_cselect_b32 s68, 3072, 0\n\ts_add_u32 s68, s68, s46\n\ts_add_u32 s68, s68, 6144\n\tbuffer_load_dwordx4 a[8:11], v8, s[8:11], s68 offen offset:0 sc1\n\t" \
    "buffer_load_dwordx4 a[12:15], v8, s[8:11], s68 offen offset:1024 sc1\n\tbuffer_load_dwordx4 a[16:19], v8, s[8:11], s68 offen offset:2048 sc1\n\ts_waitcnt lgkmcnt(8)\n\tv_mfma_f32_16x16x32_bf16 v[168:171], v[192:195], v[32:35], 0\n\tds_read_b64_tr_b16 v[20:21], v221 offset:8192\n\tds_read_b64_tr_b16 v[22:23], v221 offset:10240\n\ts_cmp_le_u32 s4, 3\n\ts_cselect_b32 s68, 3072, 0\n\t" \
    "v_mfma_f32_16x16x32_bf16 v[176:179], v[196:199], v[32:35], 0\n\ts_add_u32 s68, s68, s46\n\ts_add_u32 s68, s68, 9216\n\tv_mfma_f32_16x16x32_bf16 v[184:187], v[200:203], v[32:35], 0\n\ts_waitcnt lgkmcnt(8)\n\tv_mfma_f32_16x16x32_bf16 v[172:175], v[192:195], v[36:39], 0\n\tds_read_b64_tr_b16 v[24:25], v222 offset:8192\n\tds_read_b64_tr_b16 v[26:27], v222 offset:10240\n\t" \
    "v_mfma_f32_16x16x32_bf16 v[180:183], v[196:199], v[36:39], 0\n\tv_mfma_f32_16x16x32_bf16 v[188:191], v[200:203], v[36:39], 0\n\ts_waitcnt lgkmcnt(8)\n\tv_mfma_f32_16x16x32_bf16 v[48:51], v[192:195], v[40:43], v[228:231]\n\tds_read_b64_tr_b16 v[28:29], v223 offset:8192\n\tds_read_b64_tr_b16 v[30:31], v223 offset:10240\n\tv_mfma_f32_16x16x32_bf16 v[56:59], v[196:199], v[40:43], v[228:231]\n\tv_mfma_f32_16x16x32_bf16 v[64:67], v[200:203], v[40:43], v[228:231]\n\t" \
    "s_waitcnt lgkmcnt(8)\n\tv_mfma_f32_16x16x32_bf16 v[52:55], v[192:195], v[44:47], v[232:235]\n\tds_read_b64_tr_b16 v[32:33], v220 offset:12288\n\tds_read_b64_tr_b16 v[34:35], v220 offset:14336\n\tv_mfma_f32_16x16x32_bf16 v[60:63], v[196:199], v[44:47], v[232:235]\n\tv_mfma_f32_16x16x32_bf16 v[68:71], v[200:203], v[44:47], v[232:235]\n\ts_waitcnt lgkmcnt(8)\n\tv_mfma_f32_16x16x32_bf16 v[120:123], v[72:75], v[16:19], v[120:123]\n\t" \
    "ds_read_b64_tr_b16 v[36:37], v221 offset:12288\n\tds_read_b64_tr_b16 v[38:39], v221 offset:14336\n\tv_mfma_f32_16x16x32_bf16 v[128:131], v[80:83], v[16:19], v[128:131]\n\tv_mfma_f32_16x16x32_bf16 v[136:139], v[88:91], v[16:19], v[136:139]\n\ts_waitcnt lgkmcnt(8)\n\tv_mfma_f32_16x16x32_bf16 v[124:127], v[72:75], v[20:23], v[124:127]\n\tds_read_b64_tr_b16 v[40:41], v222 offset:12288\n\tds_read_b64_tr_b16 v[42:43], v222 offset:14336\n\t" \
    "v_mfma_f32_16x16x32_bf16 v[132:135], v[80:83], v[20:23], v[132:135]\n\tv_mfma_f32_16x16x32_bf16 v[140:143], v[88:91], v[20:23], v[140:143]\n\ts_waitcnt lgkmcnt(8)\n\tv_mfma_f32_16x16x32_bf16 v[144:147], v[72:75], v[24:27], v[144:147]\n\tds_read_b64_tr_b16 v[44:45], v223 offset:12288\n\tds_read_b64_tr_b16 v[46:47], v223 offset:14336\n\tv_mfma_f32_16x16x32_bf16 v[152:155], v[80:83], v[24:27], v[152:155]\n\tv_mfma_f32_16x16x32_bf16 v[160:163], v[88:91], v[24:27], v[160:163]\n\t" \
    "s_waitcnt lgkmcnt(8)\n\tv_mfma_f32_16x16x32_bf16 v[148:151], v[72:75], v[28:31], v[148:151]\n\tds_read_b64_tr_b16 v[16:17], v220 offset:16384\n\tds_read_b64_tr_b16 v[18:19], v220 offset:18432\n\tv_mfma_f32_16x16x32_bf16 v[156:159], v[80:83], v[28:31], v[156:159]\n\tv_mfma_f32_16x16x32_bf16 v[164:167], v[88:91], v[28:31], v[164:167]\n\ts_waitcnt lgkmcnt(8)\n\tv_mfma_f32_16x16x32_bf16 v[168:171], v[72:75], v[32:35], v[168:171]\n\t" \
    "ds_read_b64_tr_b16 v[20:21], v221 offset:16384\n\tds_read_b64_tr_b16 v[22:23], v221 offset:18432\n\tv_mfma_f32_16x16x32_bf16 v[176:179], v[80:83], v[32:35], v[176:179]\n\tv_mfma_f32_16x16x32_bf16 v[184:187], v[88:91], v[32:35], v[184:187]\n\ts_waitcnt lgkmcnt(8)\n\tv_mfma_f32_16x16x32_bf16 v[172:175], v[72:75], v[36:39], v[172:175]\n\tds_read_b64_tr_b16 v[24:25], v222 offset:16384\n\tds_read_b64_tr_b16 v[26:27], v222 offset:18432\n\t" \
    "v_mfma_f32_16x16x32_bf16 v[180:183], v[80:83], v[36:39], v[180:183]\n\tbuffer_load_dwordx4 a[20:23], v8, s[8:11], s68 offen offset:0 sc1\n\tv_mfma_f32_16x16x32_bf16 v[188:191], v[88:91], v[36:39], v[188:191]\n\ts_waitcnt lgkmcnt(8)\n\tv_mfma_f32_16x16x32_bf16 v[48:51], v[72:75], v[40:43], v[48:51]\n\tds_read_b64_tr_b16 v[28:29], v223 offset:16384\n\tds_read_b64_tr_b16 v[30:31], v223 offset:18432\n\tv_mfma_f32_16x16x32_bf16 v[56:59], v[80:83], v[40:43], v[56:59]\n\t" \
    "buffer_load_dwordx4 a[24:27], v8, s[8:11], s68 offen offset:1024 sc1\n\tv_mfma_f32_16x16x32_bf16 v[64:67], v[88:91], v[40:43], v[64:67]\n\ts_waitcnt lgkmcnt(8)\n\tv_mfma_f32_16x16x32_bf16 v[52:55], v[72:75], v[44:47], v[52:55]\n\tds_read_b64_tr_b16 v[32:33], v220 offset:20480\n\tds_read_b64_tr_b16 v[34:35], v220 offset:22528\n\tv_mfma_f32_16x16x32_bf16 v[60:63], v[80:83], v[44:47], v[60:63]\n\tbuffer_load_dwordx4 a[28:31], v8, s[8:11], s68 offen offset:2048 sc1\n\t" \
    "v_mfma_f32_16x16x32_bf16 v[68:71], v[88:91], v[44:47], v[68:71]\n\ts_barrier\n\tbuffer_store_dwordx4 v[72:75], v8, s[8:11], s45 offen offset:0 sc1\n\tbuffer_store_dwordx4 v[80:83], v8, s[8:11], s45 offen offset:1024 sc1\n\ts_waitcnt vmcnt(11) lgkmcnt(8)\n\tv_mfma_f32_16x16x32_bf16 v[120:123], v[216:219], v[16:19], v[120:123]\n\tds_read_b64_tr_b16 v[36:37], v221 offset:20480\n\tds_read_b64_tr_b16 v[38:39], v221 offset:22528\n\t" \
    "v_mfma_f32_16x16x32_bf16 v[128:131], v[204:207], v[16:19], v[128:131]\n\tbuffer_store_dwordx4 v[88:91], v8, s[8:11], s45 offen offset:2048 sc1\n\ts_cmp_le_u32 s4, 4\n\ts_cselect_b32 s68, 3072, 0\n\ts_add_u32 s68, s68, s46\n\tv_mfma_f32_16x16x32_bf16 v[136:139], v[208:211], v[16:19], v[136:139]\n\ts_add_u32 s68, s68, 12288\n\ts_waitcnt lgkmcnt(8)\n\t" \
    "v_mfma_f32_16x16x32_bf16 v[124:127], v[216:219], v[20:23], v[124:127]\n\tds_read_b64_tr_b16 v[40:41], v222 offset:20480\n\tds_read_b64_tr_b16 v[42:43], v222 offset:22528\n\tv_mfma_f32_16x16x32_bf16 v[132:135], v[204:207], v[20:23], v[132:135]\n\tbuffer_load_dwordx4 a[32:35], v8, s[8:11], s68 offen offset:0 sc1\n\tv_mfma_f32_16x16x32_bf16 v[140:143], v[208:211], v[20:23], v[140:143]\n\ts_waitcnt lgkmcnt(8)\n\tv_mfma_f32_16x16x32_bf16 v[144:147], v[216:219], v[24:27], v[144:147]\n\t" \
    "ds_read_b64_tr_b16 v[44:45], v223 offset:20480\n\tds_read_b64_tr_b16 v[46:47], v223 offset:22528\n\tv_mfma_f32_16x16x32_bf16 v[152:155], v[204:207], v[24:27], v[152:155]\n\tv_mfma_f32_16x16x32_bf16 v[160:163], v[208:211], v[24:27], v[160:163]\n\ts_waitcnt lgkmcnt(8)\n\tv_mfma_f32_16x16x32_bf16 v[148:151], v[216:219], v[28:31], v[148:151]\n\tds_read_b64_tr_b16 v[16:17], v220 offset:24576\n\tds_read_b64_tr_b16 v[18:19], v220 offset:26624\n\t" \
    "v_mfma_f32_16x16x32_bf16 v[156:159], v[204:207], v[28:31], v[156:159]\n\tbuffer_load_dwordx4 a[36:39], v8, s[8:11], s68 offen offset:1024 sc1\n\tv_mfma_f32_16x16x32_bf16 v[164:167], v[208:211], v[28:31], v[164:167]\n\ts_waitcnt lgkmcnt(8)\n\tv_mfma_f32_16x16x32_bf16 v[168:171], v[216:219], v[32:35], v[168:171]\n\tds_read_b64_tr_b16 v[20:21], v221 offset:24576\n\tds_read_b64_tr_b16 v[22:23], v221 offset:26624\n\tv_mfma_f32_16x16x32_bf16 v[176:179], v[204:207], v[32:35], v[176:179]\n\t" \
    "buffer_load_dwordx4 a[40:43], v8, s[8:11], s68 offen offset:2048 sc1\n\ts_cmp_le_u32 s4, 5\n\ts_cselect_b32 s68, 3072, 0\n\ts_add_u32 s68, s68, s46\n\tv_mfma_f32_16x16x32_bf16 v[184:187], v[208:211], v[32:35], v[184:187]\n\ts_add_u32 s68, s68, 15360\n\ts_waitcnt lgkmcnt(8)\n\tv_mfma_f32_16x16x32_bf16 v[172:175], v[216:219], v[36:39], v[172:175]\n\t" \
    "ds_read_b64_tr_b16 v[24:25], v222 offset:24576\n\tds_read_b64_tr_b16 v[26:27], v222 offset:26624\n\tv_mfma_f32_16x16x32_bf16 v[180:183], v[204:207], v[36:39], v[180:183]\n\tv_mfma_f32_16x16x32_bf16 v[188:191], v[208:211], v[36:39], v[188:191]\n\ts_waitcnt lgkmcnt(8)\n\tv_mfma_f32_16x16x32_bf16 v[48:51], v[216:219], v[40:43], v[48:51]\n\tds_read_b64_tr_b16 v[28:29], v223 offset:24576\n\tds_read_b64_tr_b16 v[30:31], v223 offset:26624\n\t" \
    "v_mfma_f32_16x16x32_bf16 v[56:59], v[204:207], v[40:43], v[56:59]\n\tbuffer_load_dwordx4 a[44:47], v8, s[8:11], s68 offen offset:0 sc1\n\tv_mfma_f32_16x16x32_bf16 v[64:67], v[208:211], v[40:43], v[64:67]\n\ts_waitcnt lgkmcnt(8)\n\tv_mfma_f32_16x16x32_bf16 v[52:55], v[216:219], v[44:47], v[52:55]\n\tds_read_b64_tr_b16 v[32:33], v220 offset:28672\n\tds_read_b64_tr_b16 v[34:35], v220 offset:30720\n\tv_mfma_f32_16x16x32_bf16 v[60:63], v[204:207], v[44:47], v[60:63]\n\t" \
    "buffer_load_dwordx4 a[48:51], v8, s[8:11], s68 offen offset:1024 sc1\n\tv_mfma_f32_16x16x32_bf16 v[68:71], v[208:211], v[44:47], v[68:71]\n\ts_waitcnt vmcnt(14) lgkmcnt(8)\n\tv_mfma_f32_16x16x32_bf16 v[120:123], v[212:215], v[16:19], v[120:123]\n\tds_read_b64_tr_b16 v[36:37], v221 offset:28672\n\tds_read_b64_tr_b16 v[38:39], v221 offset:30720\n\tv_mfma_f32_16x16x32_bf16 v[128:131], a[0:3], v[16:19], v[128:131]\n\tv_mfma_f32_16x16x32_bf16 v[136:139], a[4:7], v[16:19], v[136:139]\n\t" \
    "s_waitcnt lgkmcnt(8)\n\tv_mfma_f32_16x16x32_bf16 v[124:127], v[212:215], v[20:23], v[124:127]\n\tds_read_b64_tr_b16 v[40:41], v222 offset:28672\n\tds_read_b64_tr_b16 v[42:43], v222 offset:30720\n\tv_mfma_f32_16x16x32_bf16 v[132:135], a[0:3], v[20:23], v[132:135]\n\tbuffer_load_dwordx4 a[52:55], v8, s[8:11], s68 offen offset:2048 sc1\n\ts_cmp_le_u32 s4, 6\n\ts_cselect_b32 s68, 3072, 0\n\t" \
    "s_add_u32 s68, s68, s46\n\tv_mfma_f32_16x16x32_bf16 v[140:143], a[4:7], v[20:23], v[140:143]\n\ts_add_u32 s68, s68, 18432\n\ts_waitcnt lgkmcnt(8)\n\tv_mfma_f32_16x16x32_bf16 v[144:147], v[212:215], v[24:27], v[144:147]\n\tds_read_b64_tr_b16 v[44:45], v223 offset:28672\n\tds_read_b64_tr_b16 v[46:47], v223 offset:30720\n\tv_mfma_f32_16x16x32_bf16 v[152:155], a[0:3], v[24:27], v[152:155]\n\t" \
    "buffer_load_dwordx4 v[216:219], v8, s[8:11], s68 offen offset:0 sc1\n\tv_mfma_f32_16x16x32_bf16 v[160:163], a[4:7], v[24:27], v[160:163]\n\ts_waitcnt lgkmcnt(8)\n\tv_mfma_f32_16x16x32_bf16 v[148:151], v[212:215], v[28:31], v[148:151]\n\tds_read_b64_tr_b16 v[16:17], v4 offset:4096\n\tds_read_b64_tr_b16 v[18:19], v4 offset:6144\n\tv_mfma_f32_16x16x32_bf16 v[156:159], a[0:3], v[28:31], v[156:159]\n\tv_mfma_f32_16x16x32_bf16 v[164:167], a[4:7], v[28:31], v[164:167]\n\t" \
    "s_waitcnt lgkmcnt(8)\n\tv_mfma_f32_16x16x32_bf16 v[168:171], v[212:215], v[32:35], v[168:171]\n\tds_read_b64_tr_b16 v[20:21], v5 offset:4096\n\tds_read_b64_tr_b16 v[22:23], v5 offset:6144\n\tv_mfma_f32_16x16x32_bf16 v[176:179], a[0:3], v[32:35], v[176:179]\n\tbuffer_load_dwordx4 v[204:207], v8, s[8:11], s68 offen offset:1024 sc1\n\tv_mfma_f32_16x16x32_bf16 v[184:187], a[4:7], v[32:35], v[184:187]\n\ts_waitcnt lgkmcnt(8)\n\t" \
    "v_mfma_f32_16x16x32_bf16 v[172:175], v[212:215], v[36:39], v[172:175]\n\tds_read_b64_tr_b16 v[24:25], v6 offset:4096\n\tds_read_b64_tr_b16 v[26:27], v6 offset:6144\n\tv_mfma_f32_16x16x32_bf16 v[180:183], a[0:3], v[36:39], v[180:183]\n\tbuffer_load_dwordx4 v[208:211], v8, s[8:11], s68 offen offset:2048 sc1\n\ts_cmp_le_u32 s4, 7\n\ts_cselect_b32 s68, 3072, 0\n\ts_add_u32 s68, s68, s46\n\t" \
    "v_mfma_f32_16x16x32_bf16 v[188:191], a[4:7], v[36:39], v[188:191]\n\ts_add_u32 s68, s68, 21504\n\ts_waitcnt lgkmcnt(8)\n\tv_mfma_f32_16x16x32_bf16 v[48:51], v[212:215], v[40:43], v[48:51]\n\tds_read_b64_tr_b16 v[28:29], v7 offset:4096\n\tds_read_b64_tr_b16 v[30:31], v7 offset:6144\n\tv_mfma_f32_16x16x32_bf16 v[56:59], a[0:3], v[40:43], v[56:59]\n\tv_mfma_f32_16x16x32_bf16 v[64:67], a[4:7], v[40:43], v[64:67]\n\t" \
    "s_waitcnt lgkmcnt(8)\n\tv_mfma_f32_16x16x32_bf16 v[52:55], v[212:215], v[44:47], v[52:55]\n\tds_read_b64_tr_b16 v[32:33], v4 offset:8192\n\tds_read_b64_tr_b16 v[34:35], v4 offset:10240\n\tv_mfma_f32_16x16x32_bf16 v[60:63], a[0:3], v[44:47], v[60:63]\n\tbuffer_load_dwordx4 a[56:59], v8, s[8:11], s68 offen offset:0 sc1\n\tv_mfma_f32_16x16x32_bf16 v[68:71], a[4:7], v[44:47], v[68:71]\n\ts_barrier\n\t" \
    "s_waitcnt vmcnt(16) lgkmcnt(8)\n\tv_mfma_f32_16x16x32_bf16 v[120:123], a[8:11], v[16:19], v[120:123]\n\tds_read_b64_tr_b16 v[36:37], v5 offset:8192\n\tds_read_b64_tr_b16 v[38:39], v5 offset:10240\n\tv_mfma_f32_16x16x32_bf16 v[128:131], a[12:15], v[16:19], v[128:131]\n\tbuffer_load_dwordx4 a[60:63], v8, s[8:11], s68 offen offset:1024 sc1\n\tv_mfma_f32_16x16x32_bf16 v[136:139], a[16:19], v[16:19], v[136:139]\n\ts_waitcnt lgkmcnt(8)\n\t" \
    "v_mfma_f32_16x16x32_bf16 v[124:127], a[8:11], v[20:23], v[124:127]\n\tds_read_b64_tr_b16 v[40:41], v6 offset:8192\n\tds_read_b64_tr_b16 v[42:43], v6 offset:10240\n\tv_mfma_f32_16x16x32_bf16 v[132:135], a[12:15], v[20:23], v[132:135]\n\tbuffer_load_dwordx4 a[64:67], v8, s[8:11], s68 offen offset:2048 sc1\n\ts_cmp_le_u32 s4, 10\n\ts_cselect_b32 s69, 262144, 0\n\ts_add_u32 s69, s69, 2621440\n\t" \
    "v_mfma_f32_16x16x32_bf16 v[140:143], a[16:19], v[20:23], v[140:143]\n\ts_add_u32 m0, s54, 131072\n\ts_waitcnt lgkmcnt(8)\n\tv_mfma_f32_16x16x32_bf16 v[144:147], a[8:11], v[24:27], v[144:147]\n\tds_read_b64_tr_b16 v[44:45], v7 offset:8192\n\tds_read_b64_tr_b16 v[46:47], v7 offset:10240\n\tv_mfma_f32_16x16x32_bf16 v[152:155], a[12:15], v[24:27], v[152:155]\n\tv_mfma_f32_16x16x32_bf16 v[160:163], a[16:19], v[24:27], v[160:163]\n\t" \
    "s_waitcnt lgkmcnt(8)\n\tv_mfma_f32_16x16x32_bf16 v[148:151], a[8:11], v[28:31], v[148:151]\n\tds_read_b64_tr_b16 v[16:17], v4 offset:12288\n\tds_read_b64_tr_b16 v[18:19], v4 offset:14336\n\tv_mfma_f32_16x16x32_bf16 v[156:159], a[12:15], v[28:31], v[156:159]\n\tbuffer_load_dwordx4 v12, s[16:19], s69 offen lds\n\ts_cmp_le_u32 s4, 10\n\ts_cselect_b32 s69, 262144, 0\n\t" \
    "s_add_u32 s69, s69, 2621440\n\tv_mfma_f32_16x16x32_bf16 v[164:167], a[16:19], v[28:31], v[164:167]\n\ts_add_u32 m0, s54, 135168\n\ts_waitcnt lgkmcnt(8)\n\tv_mfma_f32_16x16x32_bf16 v[168:171], a[8:11], v[32:35], v[168:171]\n\tds_read_b64_tr_b16 v[20:21], v5 offset:12288\n\tds_read_b64_tr_b16 v[22:23], v5 offset:14336\n\tv_mfma_f32_16x16x32_bf16 v[176:179], a[12:15], v[32:35], v[176:179]\n\t" \
    "buffer_load_dwordx4 v13, s[16:19], s69 offen lds\n\ts_cmp_le_u32 s4, 8\n\ts_cselect_b32 s68, 3072, 0\n\ts_add_u32 s68, s68, s46\n\tv_mfma_f32_16x16x32_bf16 v[184:187], a[16:19], v[32:35], v[184:187]\n\ts_add_u32 s68, s68, 24576\n\ts_waitcnt lgkmcnt(8)\n\tv_mfma_f32_16x16x32_bf16 v[172:175], a[8:11], v[36:39], v[172:175]\n\t" \
    "ds_read_b64_tr_b16 v[24:25], v6 offset:12288\n\tds_read_b64_tr_b16 v[26:27], v6 offset:14336\n\tv_mfma_f32_16x16x32_bf16 v[180:183], a[12:15], v[36:39], v[180:183]\n\tv_mfma_f32_16x16x32_bf16 v[188:191], a[16:19], v[36:39], v[188:191]\n\ts_waitcnt vmcnt(14)\n\ts_add_u32 s73, s49, 4096\n\tbuffer_store_dwordx2 v[248:249], off, s[8:11], s73 sc1\n\ts_waitcnt lgkmcnt(8)\n\t" \
    "v_mfma_f32_16x16x32_bf16 v[48:51], a[8:11], v[40:43], v[48:51]\n\tds_read_b64_tr_b16 v[28:29], v7 offset:12288\n\tds_read_b64_tr_b16 v[30:31], v7 offset:14336\n\tv_mfma_f32_16x16x32_bf16 v[56:59], a[12:15], v[40:43], v[56:59]\n\tbuffer_load_dwordx4 v[212:215], v8, s[8:11], s68 offen offset:0 sc1\n\tv_mfma_f32_16x16x32_bf16 v[64:67], a[16:19], v[40:43], v[64:67]\n\ts_waitcnt lgkmcnt(8)\n\tv_mfma_f32_16x16x32_bf16 v[52:55], a[8:11], v[44:47], v[52:55]\n\t" \
    "ds_read_b64_tr_b16 v[32:33], v4 offset:16384\n\tds_read_b64_tr_b16 v[34:35], v4 offset:18432\n\tv_mfma_f32_16x16x32_bf16 v[60:63], a[12:15], v[44:47], v[60:63]\n\tbuffer_load_dwordx4 a[0:3], v8, s[8:11], s68 offen offset:1024 sc1\n\tv_mfma_f32_16x16x32_bf16 v[68:71], a[16:19], v[44:47], v[68:71]\n\ts_waitcnt lgkmcnt(8)\n\tv_mfma_f32_16x16x32_bf16 v[120:123], a[20:23], v[16:19], v[120:123]\n\tds_read_b64_tr_b16 v[36:37], v5 offset:16384\n\t" \
    "ds_read_b64_tr_b16 v[38:39], v5 offset:18432\n\tv_mfma_f32_16x16x32_bf16 v[128:131], a[24:27], v[16:19], v[128:131]\n\tbuffer_load_dwordx4 a[4:7], v8, s[8:11], s68 offen offset:2048 sc1\n\ts_cmp_le_u32 s4, 9\n\ts_cselect_b32 s68, 3072, 0\n\ts_add_u32 s68, s68, s46\n\tv_mfma_f32_16x16x32_bf16 v[136:139], a[28:31], v[16:19], v[136:139]\n\ts_add_u32 s68, s68, 27648\n\t" \
    "s_waitcnt lgkmcnt(8)\n\tv_mfma_f32_16x16x32_bf16 v[124:127], a[20:23], v[20:23], v[124:127]\n\tds_read_b64_tr_b16 v[40:41], v6 offset:16384\n\tds_read_b64_tr_b16 v[42:43], v6 offset:18432\n\tv_mfma_f32_16x16x32_bf16 v[132:135], a[24:27], v[20:23], v[132:135]\n\tbuffer_load_dwordx4 v[192:195], v8, s[8:11], s68 offen offset:0 sc1\n\tv_mfma_f32_16x16x32_bf16 v[140:143], a[28:31], v[20:23], v[140:143]\n\ts_waitcnt lgkmcnt(8)\n\t" \
    "v_mfma_f32_16x16x32_bf16 v[144:147], a[20:23], v[24:27], v[144:147]\n\tds_read_b64_tr_b16 v[44:45], v7 offset:16384\n\tds_read_b64_tr_b16 v[46:47], v7 offset:18432\n\tv_mfma_f32_16x16x32_bf16 v[152:155], a[24:27], v[24:27], v[152:155]\n\tbuffer_load_dwordx4 v[196:199], v8, s[8:11], s68 offen offset:1024 sc1\n\tv_mfma_f32_16x16x32_bf16 v[160:163], a[28:31], v[24:27], v[160:163]\n\ts_waitcnt lgkmcnt(8)\n\tv_mfma_f32_16x16x32_bf16 v[148:151], a[20:23], v[28:31], v[148:151]\n\t" \
    "ds_read_b64_tr_b16 v[16:17], v4 offset:20480\n\tds_read_b64_tr_b16 v[18:19], v4 offset:22528\n\tv_mfma_f32_16x16x32_bf16 v[156:159], a[24:27], v[28:31], v[156:159]\n\tbuffer_load_dwordx4 v[200:203], v8, s[8:11], s68 offen offset:2048 sc1\n\ts_cmp_le_u32 s4, 11\n\ts_cselect_b32 s69, 262144, 0\n\ts_add_u32 s69, s69, 2883584\n\tv_mfma_f32_16x16x32_bf16 v[164:167], a[28:31], v[28:31], v[164:167]\n\t" \
    "s_add_u32 m0, s54, 139264\n\ts_waitcnt lgkmcnt(8)\n\tv_mfma_f32_16x16x32_bf16 v[168:171], a[20:23], v[32:35], v[168:171]\n\tds_read_b64_tr_b16 v[20:21], v5 offset:20480\n\tds_read_b64_tr_b16 v[22:23], v5 offset:22528\n\tv_mfma_f32_16x16x32_bf16 v[176:179], a[24:27], v[32:35], v[176:179]\n\tbuffer_load_dwordx4 v12, s[16:19], s69 offen lds\n\ts_cmp_le_u32 s4, 11\n\t" \
    "s_cselect_b32 s69, 262144, 0\n\ts_add_u32 s69, s69, 2883584\n\tv_mfma_f32_16x16x32_bf16 v[184:187], a[28:31], v[32:35], v[184:187]\n\ts_add_u32 m0, s54, 143360\n\ts_waitcnt lgkmcnt(8)\n\tv_mfma_f32_16x16x32_bf16 v[172:175], a[20:23], v[36:39], v[172:175]\n\tds_read_b64_tr_b16 v[24:25], v6 offset:20480\n\tds_read_b64_tr_b16 v[26:27], v6 offset:22528\n\t" \
    "v_mfma_f32_16x16x32_bf16 v[180:183], a[24:27], v[36:39], v[180:183]\n\tv_mfma_f32_16x16x32_bf16 v[188:191], a[28:31], v[36:39], v[188:191]\n\ts_waitcnt lgkmcnt(8)\n\tv_mfma_f32_16x16x32_bf16 v[48:51], a[20:23], v[40:43], v[48:51]\n\tds_read_b64_tr_b16 v[28:29], v7 offset:20480\n\tds_read_b64_tr_b16 v[30:31], v7 offset:22528\n\tv_mfma_f32_16x16x32_bf16 v[56:59], a[24:27], v[40:43], v[56:59]\n\tbuffer_load_dwordx4 v13, s[16:19], s69 offen lds\n\t" \
    "s_cmp_le_u32 s4, 12\n\ts_cselect_b32 s69, 262144, 0\n\ts_add_u32 s69, s69, 3145728\n\tv_mfma_f32_16x16x32_bf16 v[64:67], a[28:31], v[40:43], v[64:67]\n\ts_add_u32 m0, s54, 147456\n\ts_waitcnt lgkmcnt(8)\n\tv_mfma_f32_16x16x32_bf16 v[52:55], a[20:23], v[44:47], v[52:55]\n\tds_read_b64_tr_b16 v[32:33], v4 offset:24576\n\t" \
    "ds_read_b64_tr_b16 v[34:35], v4 offset:26624\n\tv_mfma_f32_16x16x32_bf16 v[60:63], a[24:27], v[44:47], v[60:63]\n\tbuffer_load_dwordx4 v12, s[16:19], s69 offen lds\n\ts_cmp_le_u32 s4, 12\n\ts_cselect_b32 s69, 262144, 0\n\ts_add_u32 s69, s69, 3145728\n\tv_mfma_f32_16x16x32_bf16 v[68:71], a[28:31], v[44:47], v[68:71]\n\ts_add_u32 m0, s54, 151552\n\t" \
    "s_barrier\n\ts_waitcnt vmcnt(21) lgkmcnt(8)\n\tv_mfma_f32_16x16x32_bf16 v[120:123], a[32:35], v[16:19], v[120:123]\n\tds_read_b64_tr_b16 v[36:37], v5 offset:24576\n\tds_read_b64_tr_b16 v[38:39], v5 offset:26624\n\tv_mfma_f32_16x16x32_bf16 v[128:131], a[36:39], v[16:19], v[128:131]\n\tbuffer_load_dwordx4 v13, s[16:19], s69 offen lds\n\ts_cmp_le_u32 s4, 10\n\t" \
    "s_cselect_b32 s68, 3072, 0\n\ts_add_u32 s68, s68, s46\n\tv_mfma_f32_16x16x32_bf16 v[136:139], a[40:43], v[16:19], v[136:139]\n\ts_add_u32 s68, s68, 30720\n\ts_waitcnt lgkmcnt(8)\n\tv_mfma_f32_16x16x32_bf16 v[124:127], a[32:35], v[20:23], v[124:127]\n\tds_read_b64_tr_b16 v[40:41], v6 offset:24576\n\tds_read_b64_tr_b16 v[42:43], v6 offset:26624\n\t" \
    "v_mfma_f32_16x16x32_bf16 v[132:135], a[36:39], v[20:23], v[132:135]\n\tbuffer_load_dwordx4 v[72:75], v8, s[8:11], s68 offen offset:0 sc1\n\tv_mfma_f32_16x16x32_bf16 v[140:143], a[40:43], v[20:23], v[140:143]\n\ts_waitcnt lgkmcnt(8)\n\tv_mfma_f32_16x16x32_bf16 v[144:147], a[32:35], v[24:27], v[144:147]\n\tds_read_b64_tr_b16 v[44:45], v7 offset:24576\n\tds_read_b64_tr_b16 v[46:47], v7 offset:26624\n\tv_mfma_f32_16x16x32_bf16 v[152:155], a[36:39], v[24:27], v[152:155]\n\t" \
    "v_mfma_f32_16x16x32_bf16 v[160:163], a[40:43], v[24:27], v[160:163]\n\ts_waitcnt lgkmcnt(8)\n\tv_mfma_f32_16x16x32_bf16 v[148:151], a[32:35], v[28:31], v[148:151]\n\tds_read_b64_tr_b16 v[16:17], v4 offset:28672\n\tds_read_b64_tr_b16 v[18:19], v4 offset:30720\n\tv_mfma_f32_16x16x32_bf16 v[156:159], a[36:39], v[28:31], v[156:159]\n\tbuffer_load_dwordx4 v[76:79], v8, s[8:11], s68 offen offset:1024 sc1\n\tv_mfma_f32_16x16x32_bf16 v[164:167], a[40:43], v[28:31], v[164:167]\n\t" \
    "s_waitcnt lgkmcnt(8)\n\tv_mfma_f32_16x16x32_bf16 v[168:171], a[32:35], v[32:35], v[168:171]\n\tds_read_b64_tr_b16 v[20:21], v5 offset:28672\n\tds_read_b64_tr_b16 v[22:23], v5 offset:30720\n\tv_mfma_f32_16x16x32_bf16 v[176:179], a[36:39], v[32:35], v[176:179]\n\tv_mfma_f32_16x16x32_bf16 v[184:187], a[40:43], v[32:35], v[184:187]\n\ts_waitcnt lgkmcnt(8)\n\tv_mfma_f32_16x16x32_bf16 v[172:175], a[32:35], v[36:39], v[172:175]\n\t" \
    "ds_read_b64_tr_b16 v[24:25], v6 offset:28672\n\tds_read_b64_tr_b16 v[26:27], v6 offset:30720\n\tv_mfma_f32_16x16x32_bf16 v[180:183], a[36:39], v[36:39], v[180:183]\n\tbuffer_load_dwordx4 v[80:83], v8, s[8:11], s68 offen offset:2048 sc1\n\ts_cmp_le_u32 s4, 11\n\ts_cselect_b32 s68, 3072, 0\n\ts_add_u32 s68, s68, s46\n\tv_mfma_f32_16x16x32_bf16 v[188:191], a[40:43], v[36:39], v[188:191]\n\t" \
    "s_add_u32 s68, s68, 33792\n\ts_waitcnt lgkmcnt(8)\n\tv_mfma_f32_16x16x32_bf16 v[48:51], a[32:35], v[40:43], v[48:51]\n\tds_read_b64_tr_b16 v[28:29], v7 offset:28672\n\tds_read_b64_tr_b16 v[30:31], v7 offset:30720\n\tv_mfma_f32_16x16x32_bf16 v[56:59], a[36:39], v[40:43], v[56:59]\n\tbuffer_load_dwordx4 v[84:87], v8, s[8:11], s68 offen offset:0 sc1\n\tv_mfma_f32_16x16x32_bf16 v[64:67], a[40:43], v[40:43], v[64:67]\n\t" \
    "s_waitcnt lgkmcnt(8)\n\tv_mfma_f32_16x16x32_bf16 v[52:55], a[32:35], v[44:47], v[52:55]\n\tds_read_b64_tr_b16 v[32:33], v4 offset:32768\n\tds_read_b64_tr_b16 v[34:35], v4 offset:34816\n\tv_mfma_f32_16x16x32_bf16 v[60:63], a[36:39], v[44:47], v[60:63]\n\tv_mfma_f32_16x16x32_bf16 v[68:71], a[40:43], v[44:47], v[68:71]\n\ts_waitcnt vmcnt(23) lgkmcnt(8)\n\tv_mfma_f32_16x16x32_bf16 v[120:123], a[44:47], v[16:19], v[120:123]\n\t" \
    "ds_read_b64_tr_b16 v[36:37], v5 offset:32768\n\tds_read_b64_tr_b16 v[38:39], v5 offset:34816\n\tv_mfma_f32_16x16x32_bf16 v[128:131], a[48:51], v[16:19], v[128:131]\n\tbuffer_load_dwordx4 v[88:91], v8, s[8:11], s68 offen offset:1024 sc1\n\tv_mfma_f32_16x16x32_bf16 v[136:139], a[52:55], v[16:19], v[136:139]\n\ts_waitcnt lgkmcnt(8)\n\tv_mfma_f32_16x16x32_bf16 v[124:127], a[44:47], v[20:23], v[124:127]\n\tds_read_b64_tr_b16 v[40:41], v6 offset:32768\n\t" \
    "ds_read_b64_tr_b16 v[42:43], v6 offset:34816\n\tv_mfma_f32_16x16x32_bf16 v[132:135], a[48:51], v[20:23], v[132:135]\n\tbuffer_load_dwordx4 v[92:95], v8, s[8:11], s68 offen offset:2048 sc1\n\ts_cmp_le_u32 s4, 13\n\ts_cselect_b32 s69, 262144, 0\n\ts_add_u32 s69, s69, 3407872\n\tv_mfma_f32_16x16x32_bf16 v[140:143], a[52:55], v[20:23], v[140:143]\n\ts_add_u32 m0, s54, 155648\n\t" \
    "s_waitcnt lgkmcnt(8)\n\tv_mfma_f32_16x16x32_bf16 v[144:147], a[44:47], v[24:27], v[144:147]\n\tds_read_b64_tr_b16 v[44:45], v7 offset:32768\n\tds_read_b64_tr_b16 v[46:47], v7 offset:34816\n\tv_mfma_f32_16x16x32_bf16 v[152:155], a[48:51], v[24:27], v[152:155]\n\tv_mfma_f32_16x16x32_bf16 v[160:163], a[52:55], v[24:27], v[160:163]\n\ts_waitcnt lgkmcnt(8)\n\tv_mfma_f32_16x16x32_bf16 v[148:151], a[44:47], v[28:31], v[148:151]\n\t" \
    "ds_read_b64_tr_b16 v[16:17], v224 offset:28672\n\tds_read_b64_tr_b16 v[18:19], v224 offset:30720\n\tv_mfma_f32_16x16x32_bf16 v[156:159], a[48:51], v[28:31], v[156:159]\n\tbuffer_load_dwordx4 v12, s[16:19], s69 offen lds\n\ts_cmp_le_u32 s4, 13\n\ts_cselect_b32 s69, 262144, 0\n\ts_add_u32 s69, s69, 3407872\n\tv_mfma_f32_16x16x32_bf16 v[164:167], a[52:55], v[28:31], v[164:167]\n\t" \
    "s_add_u32 m0, s54, 159744\n\ts_waitcnt lgkmcnt(8)\n\tv_mfma_f32_16x16x32_bf16 v[168:171], a[44:47], v[32:35], v[168:171]\n\tds_read_b64_tr_b16 v[20:21], v225 offset:28672\n\tds_read_b64_tr_b16 v[22:23], v225 offset:30720\n\tv_mfma_f32_16x16x32_bf16 v[176:179], a[48:51], v[32:35], v[176:179]\n\tbuffer_load_dwordx4 v13, s[16:19], s69 offen lds\n\ts_cmp_le_u32 s4, 14\n\t" \
    "s_cselect_b32 s69, 262144, 0\n\ts_add_u32 s69, s69, 3670016\n\tv_mfma_f32_16x16x32_bf16 v[184:187], a[52:55], v[32:35], v[184:187]\n\ts_add_u32 m0, s54, 65536\n\ts_waitcnt lgkmcnt(8)\n\tv_mfma_f32_16x16x32_bf16 v[172:175], a[44:47], v[36:39], v[172:175]\n\tds_read_b64_tr_b16 v[24:25], v226 offset:28672\n\tds_read_b64_tr_b16 v[26:27], v226 offset:30720\n\t" \
    "v_mfma_f32_16x16x32_bf16 v[180:183], a[48:51], v[36:39], v[180:183]\n\tbuffer_load_dwordx4 v12, s[16:19], s69 offen lds\n\ts_cmp_le_u32 s4, 14\n\ts_cselect_b32 s69, 262144, 0\n\ts_add_u32 s69, s69, 3670016\n\tv_mfma_f32_16x16x32_bf16 v[188:191], a[52:55], v[36:39], v[188:191]\n\ts_add_u32 m0, s54, 69632\n\ts_waitcnt lgkmcnt(8)\n\t" \
    "v_mfma_f32_16x16x32_bf16 v[48:51], a[44:47], v[40:43], v[48:51]\n\tds_read_b64_tr_b16 v[28:29], v227 offset:28672\n\tds_read_b64_tr_b16 v[30:31], v227 offset:30720\n\tv_mfma_f32_16x16x32_bf16 v[56:59], a[48:51], v[40:43], v[56:59]\n\tv_mfma_f32_16x16x32_bf16 v[64:67], a[52:55], v[40:43], v[64:67]\n\ts_waitcnt lgkmcnt(8)\n\tv_mfma_f32_16x16x32_bf16 v[52:55], a[44:47], v[44:47], v[52:55]\n\tds_read_b64_tr_b16 v[32:33], v4 offset:36864\n\t" \
    "ds_read_b64_tr_b16 v[34:35], v4 offset:38912\n\tv_mfma_f32_16x16x32_bf16 v[60:63], a[48:51], v[44:47], v[60:63]\n\tbuffer_load_dwordx4 v13, s[16:19], s69 offen lds\n\ts_cmp_le_u32 s4, 15\n\ts_cselect_b32 s69, 262144, 0\n\ts_add_u32 s69, s69, 3932160\n\tv_mfma_f32_16x16x32_bf16 v[68:71], a[52:55], v[44:47], v[68:71]\n\ts_add_u32 m0, s54, 73728\n\t" \
    "s_barrier\n\ts_waitcnt vmcnt(26) lgkmcnt(8)\n\tv_mfma_f32_16x16x32_bf16 v[120:123], v[216:219], v[16:19], v[120:123]\n\tds_read_b64_tr_b16 v[36:37], v5 offset:36864\n\tds_read_b64_tr_b16 v[38:39], v5 offset:38912\n\tv_mfma_f32_16x16x32_bf16 v[128:131], v[204:207], v[16:19], v[128:131]\n\tbuffer_load_dwordx4 v12, s[16:19], s69 offen lds\n\ts_cmp_le_u32 s4, 12\n\t" \
    "s_cselect_b32 s68, 3072, 0\n\ts_add_u32 s68, s68, s46\n\tv_mfma_f32_16x16x32_bf16 v[136:139], v[208:211], v[16:19], v[136:139]\n\ts_add_u32 s68, s68, 36864\n\ts_waitcnt lgkmcnt(8)\n\tv_mfma_f32_16x16x32_bf16 v[124:127], v[216:219], v[20:23], v[124:127]\n\tds_read_b64_tr_b16 v[40:41], v6 offset:36864\n\tds_read_b64_tr_b16 v[42:43], v6 offset:38912\n\t" \
    "v_mfma_f32_16x16x32_bf16 v[132:135], v[204:207], v[20:23], v[132:135]\n\tbuffer_load_dwordx4 v[96:99], v8, s[8:11], s68 offen offset:0 sc1\n\tv_mfma_f32_16x16x32_bf16 v[140:143], v[208:211], v[20:23], v[140:143]\n\ts_waitcnt lgkmcnt(8)\n\tv_mfma_f32_16x16x32_bf16 v[144:147], v[216:219], v[24:27], v[144:147]\n\tds_read_b64_tr_b16 v[44:45], v7 offset:36864\n\tds_read_b64_tr_b16 v[46:47], v7 offset:38912\n\tv_mfma_f32_16x16x32_bf16 v[152:155], v[204:207], v[24:27], v[152:155]\n\t" \
    "v_mfma_f32_16x16x32_bf16 v[160:163], v[208:211], v[24:27], v[160:163]\n\ts_waitcnt lgkmcnt(8)\n\tv_mfma_f32_16x16x32_bf16 v[148:151], v[216:219], v[28:31], v[148:151]\n\tds_read_b64_tr_b16 v[16:17], v4 offset:40960\n\tds_read_b64_tr_b16 v[18:19], v4 offset:43008\n\tv_mfma_f32_16x16x32_bf16 v[156:159], v[204:207], v[28:31], v[156:159]\n\tbuffer_load_dwordx4 v[100:103], v8, s[8:11], s68 offen offset:1024 sc1\n\tv_mfma_f32_16x16x32_bf16 v[164:167], v[208:211], v[28:31], v[164:167]\n\t" \
    "s_waitcnt lgkmcnt(8)\n\tv_mfma_f32_16x16x32_bf16 v[168:171], v[216:219], v[32:35], v[168:171]\n\tds_read_b64_tr_b16 v[20:21], v5 offset:40960\n\tds_read_b64_tr_b16 v[22:23], v5 offset:43008\n\tv_mfma_f32_16x16x32_bf16 v[176:179], v[204:207], v[32:35], v[176:179]\n\tbuffer_load_dwordx4 v[104:107], v8, s[8:11], s68 offen offset:2048 sc1\n\ts_cmp_le_u32 s4, 13\n\ts_cselect_b32 s68, 3072, 0\n\t" \
    "s_add_u32 s68, s68, s46\n\tv_mfma_f32_16x16x32_bf16 v[184:187], v[208:211], v[32:35], v[184:187]\n\ts_add_u32 s68, s68, 39936\n\ts_waitcnt lgkmcnt(8)\n\tv_mfma_f32_16x16x32_bf16 v[172:175], v[216:219], v[36:39], v[172:175]\n\tds_read_b64_tr_b16 v[24:25], v6 offset:40960\n\tds_read_b64_tr_b16 v[26:27], v6 offset:43008\n\tv_mfma_f32_16x16x32_bf16 v[180:183], v[204:207], v[36:39], v[180:183]\n\t" \
    "v_mfma_f32_16x16x32_bf16 v[188:191], v[208:211], v[36:39], v[188:191]\n\ts_waitcnt lgkmcnt(8)\n\tv_mfma_f32_16x16x32_bf16 v[48:51], v[216:219], v[40:43], v[48:51]\n\tds_read_b64_tr_b16 v[28:29], v7 offset:40960\n\tds_read_b64_tr_b16 v[30:31], v7 offset:43008\n\tv_mfma_f32_16x16x32_bf16 v[56:59], v[204:207], v[40:43], v[56:59]\n\tbuffer_load_dwordx4 v[108:111], v8, s[8:11], s68 offen offset:0 sc1\n\tv_mfma_f32_16x16x32_bf16 v[64:67], v[208:211], v[40:43], v[64:67]\n\t" \
    "s_waitcnt lgkmcnt(8)\n\tv_mfma_f32_16x16x32_bf16 v[52:55], v[216:219], v[44:47], v[52:55]\n\tds_read_b64_tr_b16 v[32:33], v4 offset:45056\n\tds_read_b64_tr_b16 v[34:35], v4 offset:47104\n\tv_mfma_f32_16x16x32_bf16 v[60:63], v[204:207], v[44:47], v[60:63]\n\tbuffer_load_dwordx4 v[112:115], v8, s[8:11], s68 offen offset:1024 sc1\n\tv_mfma_f32_16x16x32_bf16 v[68:71], v[208:211], v[44:47], v[68:71]\n\ts_waitcnt vmcnt(29) lgkmcnt(8)\n\t" \
    "v_mfma_f32_16x16x32_bf16 v[120:123], a[56:59], v[16:19], v[120:123]\n\tds_read_b64_tr_b16 v[36:37], v5 offset:45056\n\tds_read_b64_tr_b16 v[38:39], v5 offset:47104\n\tv_mfma_f32_16x16x32_bf16 v[128:131], a[60:63], v[16:19], v[128:131]\n\tv_mfma_f32_16x16x32_bf16 v[136:139], a[64:67], v[16:19], v[136:139]\n\ts_waitcnt lgkmcnt(8)\n\tv_mfma_f32_16x16x32_bf16 v[124:127], a[56:59], v[20:23], v[124:127]\n\tds_read_b64_tr_b16 v[40:41], v6 offset:45056\n\t" \
    "ds_read_b64_tr_b16 v[42:43], v6 offset:47104\n\tv_mfma_f32_16x16x32_bf16 v[132:135], a[60:63], v[20:23], v[132:135]\n\tbuffer_load_dwordx4 v[116:119], v8, s[8:11], s68 offen offset:2048 sc1\n\ts_cmp_le_u32 s4, 14\n\ts_cselect_b32 s68, 3072, 0\n\ts_add_u32 s68, s68, s46\n\tv_mfma_f32_16x16x32_bf16 v[140:143], a[64:67], v[20:23], v[140:143]\n\ts_add_u32 s68, s68, 43008\n\t" \
    "s_waitcnt lgkmcnt(8)\n\tv_mfma_f32_16x16x32_bf16 v[144:147], a[56:59], v[24:27], v[144:147]\n\tds_read_b64_tr_b16 v[44:45], v7 offset:45056\n\tds_read_b64_tr_b16 v[46:47], v7 offset:47104\n\tv_mfma_f32_16x16x32_bf16 v[152:155], a[60:63], v[24:27], v[152:155]\n\tbuffer_load_dwordx4 a[8:11], v8, s[8:11], s68 offen offset:0 sc1\n\tv_mfma_f32_16x16x32_bf16 v[160:163], a[64:67], v[24:27], v[160:163]\n\ts_waitcnt lgkmcnt(8)\n\t" \
    "v_mfma_f32_16x16x32_bf16 v[148:151], a[56:59], v[28:31], v[148:151]\n\tds_read_b64_tr_b16 v[16:17], v4 offset:49152\n\tds_read_b64_tr_b16 v[18:19], v4 offset:51200\n\tv_mfma_f32_16x16x32_bf16 v[156:159], a[60:63], v[28:31], v[156:159]\n\tv_mfma_f32_16x16x32_bf16 v[164:167], a[64:67], v[28:31], v[164:167]\n\ts_waitcnt lgkmcnt(8)\n\tv_mfma_f32_16x16x32_bf16 v[168:171], a[56:59], v[32:35], v[168:171]\n\tds_read_b64_tr_b16 v[20:21], v5 offset:49152\n\t" \
    "ds_read_b64_tr_b16 v[22:23], v5 offset:51200\n\tv_mfma_f32_16x16x32_bf16 v[176:179], a[60:63], v[32:35], v[176:179]\n\tbuffer_load_dwordx4 a[12:15], v8, s[8:11], s68 offen offset:1024 sc1\n\tv_mfma_f32_16x16x32_bf16 v[184:187], a[64:67], v[32:35], v[184:187]\n\ts_waitcnt lgkmcnt(8)\n\tv_mfma_f32_16x16x32_bf16 v[172:175], a[56:59], v[36:39], v[172:175]\n\tds_read_b64_tr_b16 v[24:25], v6 offset:49152\n\tds_read_b64_tr_b16 v[26:27], v6 offset:51200\n\t" \
    "v_mfma_f32_16x16x32_bf16 v[180:183], a[60:63], v[36:39], v[180:183]\n\tbuffer_load_dwordx4 a[16:19], v8, s[8:11], s68 offen offset:2048 sc1\n\ts_cmp_le_u32 s4, 15\n\ts_cselect_b32 s69, 262144, 0\n\ts_add_u32 s69, s69, 3932160\n\tv_mfma_f32_16x16x32_bf16 v[188:191], a[64:67], v[36:39], v[188:191]\n\ts_add_u32 m0, s54, 77824\n\ts_add_u32 s50, s50, 4096\n\t" \
    "buffer_load_dwordx2 v[250:251], v241, s[8:11], s50 offen sc1\n\ts_waitcnt lgkmcnt(8)\n\tv_mfma_f32_16x16x32_bf16 v[48:51], a[56:59], v[40:43], v[48:51]\n\tds_read_b64_tr_b16 v[28:29], v7 offset:49152\n\tds_read_b64_tr_b16 v[30:31], v7 offset:51200\n\tv_mfma_f32_16x16x32_bf16 v[56:59], a[60:63], v[40:43], v[56:59]\n\tv_mfma_f32_16x16x32_bf16 v[64:67], a[64:67], v[40:43], v[64:67]\n\ts_waitcnt lgkmcnt(8)\n\t" \
    "v_mfma_f32_16x16x32_bf16 v[52:55], a[56:59], v[44:47], v[52:55]\n\tds_read_b64_tr_b16 v[32:33], v4 offset:53248\n\tds_read_b64_tr_b16 v[34:35], v4 offset:55296\n\tv_mfma_f32_16x16x32_bf16 v[60:63], a[60:63], v[44:47], v[60:63]\n\tv_mfma_f32_16x16x32_bf16 v[68:71], a[64:67], v[44:47], v[68:71]\n\ts_waitcnt vmcnt(32)\n\ts_barrier\n\ts_waitcnt vmcnt(28) lgkmcnt(8)\n\t" \
    "v_mfma_f32_16x16x32_bf16 v[120:123], v[212:215], v[16:19], v[120:123]\n\tds_read_b64_tr_b16 v[36:37], v5 offset:53248\n\tds_read_b64_tr_b16 v[38:39], v5 offset:55296\n\tv_mfma_f32_16x16x32_bf16 v[128:131], a[0:3], v[16:19], v[128:131]\n\tbuffer_load_dwordx4 v13, s[16:19], s69 offen lds\n\ts_cmp_le_u32 s4, 16\n\ts_cselect_b32 s69, 262144, 0\n\ts_add_u32 s69, s69, 4194304\n\t" \
    "v_mfma_f32_16x16x32_bf16 v[136:139], a[4:7], v[16:19], v[136:139]\n\ts_add_u32 m0, s54, 81920\n\ts_waitcnt lgkmcnt(8)\n\tv_mfma_f32_16x16x32_bf16 v[124:127], v[212:215], v[20:23], v[124:127]\n\tds_read_b64_tr_b16 v[40:41], v6 offset:53248\n\tds_read_b64_tr_b16 v[42:43], v6 offset:55296\n\tv_mfma_f32_16x16x32_bf16 v[132:135], a[0:3], v[20:23], v[132:135]\n\tbuffer_load_dwordx4 v12, s[16:19], s69 offen lds\n\t" \
    "s_cmp_le_u32 s4, 16\n\ts_cselect_b32 s69, 262144, 0\n\ts_add_u32 s69, s69, 4194304\n\tv_mfma_f32_16x16x32_bf16 v[140:143], a[4:7], v[20:23], v[140:143]\n\ts_add_u32 m0, s54, 86016\n\ts_waitcnt lgkmcnt(8)\n\tv_mfma_f32_16x16x32_bf16 v[144:147], v[212:215], v[24:27], v[144:147]\n\tds_read_b64_tr_b16 v[44:45], v7 offset:53248\n\t" \
    "ds_read_b64_tr_b16 v[46:47], v7 offset:55296\n\tv_mfma_f32_16x16x32_bf16 v[152:155], a[0:3], v[24:27], v[152:155]\n\tbuffer_load_dwordx4 v13, s[16:19], s69 offen lds\n\ts_cmp_le_u32 s4, 0\n\ts_cselect_b32 s69, 262144, 0\n\ts_add_u32 s69, s69, 131072\n\tv_mfma_f32_16x16x32_bf16 v[160:163], a[4:7], v[24:27], v[160:163]\n\ts_add_u32 m0, s54, 90112\n\t" \
    "s_waitcnt lgkmcnt(8)\n\tv_mfma_f32_16x16x32_bf16 v[148:151], v[212:215], v[28:31], v[148:151]\n\tds_read_b64_tr_b16 v[16:17], v4 offset:57344\n\tds_read_b64_tr_b16 v[18:19], v4 offset:59392\n\tv_mfma_f32_16x16x32_bf16 v[156:159], a[0:3], v[28:31], v[156:159]\n\tv_mfma_f32_16x16x32_bf16 v[164:167], a[4:7], v[28:31], v[164:167]\n\ts_waitcnt lgkmcnt(8)\n\tv_mfma_f32_16x16x32_bf16 v[168:171], v[212:215], v[32:35], v[168:171]\n\t" \
    "ds_read_b64_tr_b16 v[20:21], v5 offset:57344\n\tds_read_b64_tr_b16 v[22:23], v5 offset:59392\n\tv_mfma_f32_16x16x32_bf16 v[176:179], a[0:3], v[32:35], v[176:179]\n\tbuffer_load_dwordx4 v12, s[16:19], s69 offen lds\n\ts_cmp_le_u32 s4, 0\n\ts_cselect_b32 s69, 262144, 0\n\ts_add_u32 s69, s69, 131072\n\tv_mfma_f32_16x16x32_bf16 v[184:187], a[4:7], v[32:35], v[184:187]\n\t" \
    "s_add_u32 m0, s54, 98304\n\ts_waitcnt lgkmcnt(8)\n\tv_mfma_f32_16x16x32_bf16 v[172:175], v[212:215], v[36:39], v[172:175]\n\tds_read_b64_tr_b16 v[24:25], v6 offset:57344\n\tds_read_b64_tr_b16 v[26:27], v6 offset:59392\n\tv_mfma_f32_16x16x32_bf16 v[180:183], a[0:3], v[36:39], v[180:183]\n\tbuffer_load_dwordx4 v13, s[16:19], s69 offen lds\n\ts_cmp_le_u32 s4, 15\n\t" \
    "s_cselect_b32 s68, 3072, 0\n\ts_add_u32 s68, s68, s46\n\tv_mfma_f32_16x16x32_bf16 v[188:191], a[4:7], v[36:39], v[188:191]\n\ts_add_u32 s68, s68, 46080\n\ts_waitcnt lgkmcnt(8)\n\tv_mfma_f32_16x16x32_bf16 v[48:51], v[212:215], v[40:43], v[48:51]\n\tds_read_b64_tr_b16 v[28:29], v7 offset:57344\n\tds_read_b64_tr_b16 v[30:31], v7 offset:59392\n\t" \
    "v_mfma_f32_16x16x32_bf16 v[56:59], a[0:3], v[40:43], v[56:59]\n\tv_mfma_f32_16x16x32_bf16 v[64:67], a[4:7], v[40:43], v[64:67]\n\ts_waitcnt lgkmcnt(8)\n\tv_mfma_f32_16x16x32_bf16 v[52:55], v[212:215], v[44:47], v[52:55]\n\tds_read_b64_tr_b16 v[32:33], v4 offset:61440\n\tds_read_b64_tr_b16 v[34:35], v4 offset:63488\n\tv_mfma_f32_16x16x32_bf16 v[60:63], a[0:3], v[44:47], v[60:63]\n\tbuffer_load_dwordx4 a[20:23], v8, s[8:11], s68 offen offset:0 sc1\n\t" \
    "v_mfma_f32_16x16x32_bf16 v[68:71], a[4:7], v[44:47], v[68:71]\n\ts_waitcnt vmcnt(31) lgkmcnt(8)\n\tv_mfma_f32_16x16x32_bf16 v[120:123], v[192:195], v[16:19], v[120:123]\n\tds_read_b64_tr_b16 v[36:37], v5 offset:61440\n\tds_read_b64_tr_b16 v[38:39], v5 offset:63488\n\tv_mfma_f32_16x16x32_bf16 v[128:131], v[196:199], v[16:19], v[128:131]\n\tbuffer_load_dwordx4 a[24:27], v8, s[8:11], s68 offen offset:1024 sc1\n\tv_mfma_f32_16x16x32_bf16 v[136:139], v[200:203], v[16:19], v[136:139]\n\t" \
    "s_waitcnt lgkmcnt(8)\n\tv_mfma_f32_16x16x32_bf16 v[124:127], v[192:195], v[20:23], v[124:127]\n\tds_read_b64_tr_b16 v[40:41], v6 offset:61440\n\tds_read_b64_tr_b16 v[42:43], v6 offset:63488\n\tv_mfma_f32_16x16x32_bf16 v[132:135], v[196:199], v[20:23], v[132:135]\n\tv_mfma_f32_16x16x32_bf16 v[140:143], v[200:203], v[20:23], v[140:143]\n\ts_waitcnt lgkmcnt(8)\n\tv_mfma_f32_16x16x32_bf16 v[144:147], v[192:195], v[24:27], v[144:147]\n\t" \
    "ds_read_b64_tr_b16 v[44:45], v7 offset:61440\n\tds_read_b64_tr_b16 v[46:47], v7 offset:63488\n\tv_mfma_f32_16x16x32_bf16 v[152:155], v[196:199], v[24:27], v[152:155]\n\tbuffer_load_dwordx4 a[28:31], v8, s[8:11], s68 offen offset:2048 sc1\n\ts_cmp_le_u32 s4, 16\n\ts_cselect_b32 s68, 3072, 0\n\ts_add_u32 s68, s68, s46\n\tv_mfma_f32_16x16x32_bf16 v[160:163], v[200:203], v[24:27], v[160:163]\n\t" \
    "s_add_u32 s68, s68, 49152\n\ts_waitcnt lgkmcnt(8)\n\tv_mfma_f32_16x16x32_bf16 v[148:151], v[192:195], v[28:31], v[148:151]\n\tds_read_b64_tr_b16 v[16:17], v220 offset:0\n\tds_read_b64_tr_b16 v[18:19], v220 offset:2048\n\tv_mfma_f32_16x16x32_bf16 v[156:159], v[196:199], v[28:31], v[156:159]\n\tbuffer_load_dwordx4 a[0:3], v8, s[8:11], s68 offen offset:0 sc1\n\tv_mfma_f32_16x16x32_bf16 v[164:167], v[200:203], v[28:31], v[164:167]\n\t" \
    "s_waitcnt lgkmcnt(8)\n\tv_mfma_f32_16x16x32_bf16 v[168:171], v[192:195], v[32:35], v[168:171]\n\tds_read_b64_tr_b16 v[20:21], v221 offset:0\n\tds_read_b64_tr_b16 v[22:23], v221 offset:2048\n\tv_mfma_f32_16x16x32_bf16 v[176:179], v[196:199], v[32:35], v[176:179]\n\tv_mfma_f32_16x16x32_bf16 v[184:187], v[200:203], v[32:35], v[184:187]\n\ts_waitcnt lgkmcnt(8)\n\tv_mfma_f32_16x16x32_bf16 v[172:175], v[192:195], v[36:39], v[172:175]\n\t" \
    "ds_read_b64_tr_b16 v[24:25], v222 offset:0\n\tds_read_b64_tr_b16 v[26:27], v222 offset:2048\n\tv_mfma_f32_16x16x32_bf16 v[180:183], v[196:199], v[36:39], v[180:183]\n\tbuffer_load_dwordx4 a[4:7], v8, s[8:11], s68 offen offset:1024 sc1\n\tv_mfma_f32_16x16x32_bf16 v[188:191], v[200:203], v[36:39], v[188:191]\n\ts_waitcnt lgkmcnt(8)\n\tv_mfma_f32_16x16x32_bf16 v[48:51], v[192:195], v[40:43], v[48:51]\n\tds_read_b64_tr_b16 v[28:29], v223 offset:0\n\t" \
    "ds_read_b64_tr_b16 v[30:31], v223 offset:2048\n\tv_mfma_f32_16x16x32_bf16 v[56:59], v[196:199], v[40:43], v[56:59]\n\tbuffer_load_dwordx4 a[32:35], v8, s[8:11], s68 offen offset:2048 sc1\n\ts_cmp_le_u32 s4, 1\n\ts_cselect_b32 s69, 262144, 0\n\ts_add_u32 s69, s69, 393216\n\tv_mfma_f32_16x16x32_bf16 v[64:67], v[200:203], v[40:43], v[64:67]\n\ts_add_u32 m0, s54, 102400\n\t" \
    "s_waitcnt lgkmcnt(8)\n\tv_mfma_f32_16x16x32_bf16 v[52:55], v[192:195], v[44:47], v[52:55]\n\tds_read_b64_tr_b16 v[32:33], v220 offset:4096\n\tds_read_b64_tr_b16 v[34:35], v220 offset:6144\n\tv_mfma_f32_16x16x32_bf16 v[60:63], v[196:199], v[44:47], v[60:63]\n\tbuffer_load_dwordx4 v12, s[16:19], s69 offen lds\n\ts_cmp_le_u32 s4, 1\n\ts_cselect_b32 s69, 262144, 0\n\t" \
    "s_add_u32 s69, s69, 393216\n\tv_mfma_f32_16x16x32_bf16 v[68:71], v[200:203], v[44:47], v[68:71]\n\ts_add_u32 m0, s54, 106496\n\ts_waitcnt vmcnt(33)\n\ts_barrier\n\ts_waitcnt vmcnt(30) lgkmcnt(8)\n\tv_mfma_f32_16x16x32_bf16 v[120:123], v[72:75], v[16:19], v[120:123]\n\tds_read_b64_tr_b16 v[36:37], v221 offset:4096\n\t" \
    "ds_read_b64_tr_b16 v[38:39], v221 offset:6144\n\tv_mfma_f32_16x16x32_bf16 v[128:131], v[76:79], v[16:19], v[128:131]\n\tbuffer_load_dwordx4 v13, s[16:19], s69 offen lds\n\ts_cmp_le_u32 s4, 2\n\ts_cselect_b32 s69, 262144, 0\n\ts_add_u32 s69, s69, 655360\n\tv_mfma_f32_16x16x32_bf16 v[136:139], v[80:83], v[16:19], v[136:139]\n\ts_add_u32 m0, s54, 110592\n\t" \
    "s_waitcnt lgkmcnt(8)\n\tv_mfma_f32_16x16x32_bf16 v[124:127], v[72:75], v[20:23], v[124:127]\n\tds_read_b64_tr_b16 v[40:41], v222 offset:4096\n\tds_read_b64_tr_b16 v[42:43], v222 offset:6144\n\tv_mfma_f32_16x16x32_bf16 v[132:135], v[76:79], v[20:23], v[132:135]\n\tbuffer_load_dwordx4 v12, s[16:19], s69 offen lds\n\ts_cmp_le_u32 s4, 2\n\ts_cselect_b32 s69, 262144, 0\n\t" \
    "s_add_u32 s69, s69, 655360\n\tv_mfma_f32_16x16x32_bf16 v[140:143], v[80:83], v[20:23], v[140:143]\n\ts_add_u32 m0, s54, 114688\n\ts_waitcnt lgkmcnt(8)\n\tv_mfma_f32_16x16x32_bf16 v[144:147], v[72:75], v[24:27], v[144:147]\n\tds_read_b64_tr_b16 v[44:45], v223 offset:4096\n\tds_read_b64_tr_b16 v[46:47], v223 offset:6144\n\tv_mfma_f32_16x16x32_bf16 v[152:155], v[76:79], v[24:27], v[152:155]\n\t" \
    "buffer_load_dwordx4 v13, s[16:19], s69 offen lds\n\ts_cmp_le_u32 s4, 3\n\ts_cselect_b32 s69, 262144, 0\n\ts_add_u32 s69, s69, 917504\n\tv_mfma_f32_16x16x32_bf16 v[160:163], v[80:83], v[24:27], v[160:163]\n\ts_add_u32 m0, s54, 118784\n\ts_waitcnt lgkmcnt(8)\n\tv_mfma_f32_16x16x32_bf16 v[148:151], v[72:75], v[28:31], v[148:151]\n\t" \
    "ds_read_b64_tr_b16 v[16:17], v220 offset:8192\n\tds_read_b64_tr_b16 v[18:19], v220 offset:10240\n\tv_mfma_f32_16x16x32_bf16 v[156:159], v[76:79], v[28:31], v[156:159]\n\tbuffer_load_dwordx4 v12, s[16:19], s69 offen lds\n\ts_cmp_le_u32 s4, 3\n\ts_cselect_b32 s69, 262144, 0\n\ts_add_u32 s69, s69, 917504\n\tv_mfma_f32_16x16x32_bf16 v[164:167], v[80:83], v[28:31], v[164:167]\n\t" \
    "s_add_u32 m0, s54, 122880\n\ts_waitcnt vmcnt(16)\n\tv_cmp_ne_u64 vcc, s[36:37], v[250:251]\n\ts_cbranch_vccz 114f\n\ts_mov_b32 s71, 1048576\n\t113:\n\ts_sleep 1\n\tbuffer_load_dwordx2 v[250:251], v241, s[8:11], s50 offen sc1\n\t" \
    "s_waitcnt vmcnt(0)\n\tv_cmp_ne_u64 vcc, s[36:37], v[250:251]\n\ts_cbranch_vccz 114f\n\ts_sub_u32 s71, s71, 1\n\ts_cbranch_scc0 113b\n\t114:\n\ts_waitcnt lgkmcnt(8)\n\tv_mfma_f32_16x16x32_bf16 v[168:171], v[72:75], v[32:35], v[168:171]\n\t" \
    "ds_read_b64_tr_b16 v[20:21], v221 offset:8192\n\tds_read_b64_tr_b16 v[22:23], v221 offset:10240\n\tv_mfma_f32_16x16x32_bf16 v[176:179], v[76:79], v[32:35], v[176:179]\n\tv_mfma_f32_16x16x32_bf16 v[184:187], v[80:83], v[32:35], v[184:187]\n\ts_waitcnt lgkmcnt(8)\n\tv_mfma_f32_16x16x32_bf16 v[172:175], v[72:75], v[36:39], v[172:175]\n\tds_read_b64_tr_b16 v[24:25], v222 offset:8192\n\tds_read_b64_tr_b16 v[26:27], v222 offset:10240\n\t" \
    "v_mfma_f32_16x16x32_bf16 v[180:183], v[76:79], v[36:39], v[180:183]\n\tbuffer_load_dwordx4 v13, s[16:19], s69 offen lds\n\ts_cmp_le_u32 s4, 0\n\ts_cselect_b32 s68, 3072, 0\n\ts_add_u32 s68, s68, s46\n\tv_mfma_f32_16x16x32_bf16 v[188:191], v[80:83], v[36:39], v[188:191]\n\ts_add_u32 s68, s68, 55296\n\ts_waitcnt lgkmcnt(8)\n\t" \
    "v_mfma_f32_16x16x32_bf16 v[48:51], v[72:75], v[40:43], v[48:51]\n\tds_read_b64_tr_b16 v[28:29], v223 offset:8192\n\tds_read_b64_tr_b16 v[30:31], v223 offset:10240\n\tv_mfma_f32_16x16x32_bf16 v[56:59], v[76:79], v[40:43], v[56:59]\n\tbuffer_load_dwordx4 a[36:39], v8, s[8:11], s68 offen offset:0 sc1\n\tv_mfma_f32_16x16x32_bf16 v[64:67], v[80:83], v[40:43], v[64:67]\n\ts_waitcnt lgkmcnt(8)\n\tv_mfma_f32_16x16x32_bf16 v[52:55], v[72:75], v[44:47], v[52:55]\n\t" \
    "ds_read_b64_tr_b16 v[32:33], v220 offset:12288\n\tds_read_b64_tr_b16 v[34:35], v220 offset:14336\n\tv_mfma_f32_16x16x32_bf16 v[60:63], v[76:79], v[44:47], v[60:63]\n\tv_mfma_f32_16x16x32_bf16 v[68:71], v[80:83], v[44:47], v[68:71]\n\ts_waitcnt lgkmcnt(8)\n\tv_mfma_f32_16x16x32_bf16 v[120:123], v[84:87], v[16:19], v[120:123]\n\tds_read_b64_tr_b16 v[36:37], v221 offset:12288\n\tds_read_b64_tr_b16 v[38:39], v221 offset:14336\n\t" \
    "v_mfma_f32_16x16x32_bf16 v[128:131], v[88:91], v[16:19], v[128:131]\n\tbuffer_load_dwordx4 a[40:43], v8, s[8:11], s68 offen offset:1024 sc1\n\tv_mfma_f32_16x16x32_bf16 v[136:139], v[92:95], v[16:19], v[136:139]\n\ts_waitcnt lgkmcnt(8)\n\tv_mfma_f32_16x16x32_bf16 v[124:127], v[84:87], v[20:23], v[124:127]\n\tds_read_b64_tr_b16 v[40:41], v222 offset:12288\n\tds_read_b64_tr_b16 v[42:43], v222 offset:14336\n\tv_mfma_f32_16x16x32_bf16 v[132:135], v[88:91], v[20:23], v[132:135]\n\t" \
    "buffer_load_dwordx4 a[44:47], v8, s[8:11], s68 offen offset:2048 sc1\n\ts_cmp_le_u32 s4, 1\n\ts_cselect_b32 s68, 3072, 0\n\ts_add_u32 s68, s68, s46\n\tv_mfma_f32_16x16x32_bf16 v[140:143], v[92:95], v[20:23], v[140:143]\n\ts_add_u32 s68, s68, 58368\n\ts_waitcnt lgkmcnt(8)\n\tv_mfma_f32_16x16x32_bf16 v[144:147], v[84:87], v[24:27], v[144:147]\n\t" \
    "ds_read_b64_tr_b16 v[44:45], v223 offset:12288\n\tds_read_b64_tr_b16 v[46:47], v223 offset:14336\n\tv_mfma_f32_16x16x32_bf16 v[152:155], v[88:91], v[24:27], v[152:155]\n\tv_mfma_f32_16x16x32_bf16 v[160:163], v[92:95], v[24:27], v[160:163]\n\ts_waitcnt lgkmcnt(8)\n\tv_mfma_f32_16x16x32_bf16 v[148:151], v[84:87], v[28:31], v[148:151]\n\tds_read_b64_tr_b16 v[16:17], v220 offset:16384\n\tds_read_b64_tr_b16 v[18:19], v220 offset:18432\n\t" \
    "v_mfma_f32_16x16x32_bf16 v[156:159], v[88:91], v[28:31], v[156:159]\n\tbuffer_load_dwordx4 a[48:51], v8, s[8:11], s68 offen offset:0 sc1\n\tv_mfma_f32_16x16x32_bf16 v[164:167], v[92:95], v[28:31], v[164:167]\n\ts_waitcnt lgkmcnt(8)\n\tv_mfma_f32_16x16x32_bf16 v[168:171], v[84:87], v[32:35], v[168:171]\n\tds_read_b64_tr_b16 v[20:21], v221 offset:16384\n\tds_read_b64_tr_b16 v[22:23], v221 offset:18432\n\tv_mfma_f32_16x16x32_bf16 v[176:179], v[88:91], v[32:35], v[176:179]\n\t" \
    "buffer_load_dwordx4 a[52:55], v8, s[8:11], s68 offen offset:1024 sc1\n\tv_mfma_f32_16x16x32_bf16 v[184:187], v[92:95], v[32:35], v[184:187]\n\ts_waitcnt lgkmcnt(8)\n\tv_mfma_f32_16x16x32_bf16 v[172:175], v[84:87], v[36:39], v[172:175]\n\tds_read_b64_tr_b16 v[24:25], v222 offset:16384\n\tds_read_b64_tr_b16 v[26:27], v222 offset:18432\n\tv_mfma_f32_16x16x32_bf16 v[180:183], v[88:91], v[36:39], v[180:183]\n\tv_mfma_f32_16x16x32_bf16 v[188:191], v[92:95], v[36:39], v[188:191]\n\t" \
    "s_waitcnt lgkmcnt(8)\n\tv_mfma_f32_16x16x32_bf16 v[48:51], v[84:87], v[40:43], v[48:51]\n\tds_read_b64_tr_b16 v[28:29], v223 offset:16384\n\tds_read_b64_tr_b16 v[30:31], v223 offset:18432\n\tv_mfma_f32_16x16x32_bf16 v[56:59], v[88:91], v[40:43], v[56:59]\n\tbuffer_load_dwordx4 a[56:59], v8, s[8:11], s68 offen offset:2048 sc1\n\ts_cmp_le_u32 s4, 2\n\ts_cselect_b32 s68, 3072, 0\n\t" \
    "s_add_u32 s68, s68, s46\n\tv_mfma_f32_16x16x32_bf16 v[64:67], v[92:95], v[40:43], v[64:67]\n\ts_add_u32 s68, s68, 61440\n\ts_waitcnt lgkmcnt(8)\n\tv_mfma_f32_16x16x32_bf16 v[52:55], v[84:87], v[44:47], v[52:55]\n\tds_read_b64_tr_b16 v[32:33], v220 offset:20480\n\tds_read_b64_tr_b16 v[34:35], v220 offset:22528\n\tv_mfma_f32_16x16x32_bf16 v[60:63], v[88:91], v[44:47], v[60:63]\n\t" \
    "buffer_load_dwordx4 a[60:63], v8, s[8:11], s68 offen offset:0 sc1\n\tv_mfma_f32_16x16x32_bf16 v[68:71], v[92:95], v[44:47], v[68:71]\n\ts_barrier\n\ts_waitcnt lgkmcnt(8)\n\tv_mfma_f32_16x16x32_bf16 v[120:123], v[96:99], v[16:19], v[120:123]\n\tds_read_b64_tr_b16 v[36:37], v221 offset:20480\n\tds_read_b64_tr_b16 v[38:39], v221 offset:22528\n\tv_mfma_f32_16x16x32_bf16 v[128:131], v[100:103], v[16:19], v[128:131]\n\t" \
    "buffer_load_dwordx4 a[64:67], v8, s[8:11], s68 offen offset:1024 sc1\n\tv_mfma_f32_16x16x32_bf16 v[136:139], v[104:107], v[16:19], v[136:139]\n\ts_waitcnt lgkmcnt(8)\n\tv_mfma_f32_16x16x32_bf16 v[124:127], v[96:99], v[20:23], v[124:127]\n\tds_read_b64_tr_b16 v[40:41], v222 offset:20480\n\tds_read_b64_tr_b16 v[42:43], v222 offset:22528\n\tv_mfma_f32_16x16x32_bf16 v[132:135], v[100:103], v[20:23], v[132:135]\n\tbuffer_load_dwordx4 a[68:71], v8, s[8:11], s68 offen offset:2048 sc1\n\t" \
    "s_cmp_le_u32 s4, 4\n\ts_cselect_b32 s69, 262144, 0\n\ts_add_u32 s69, s69, 1179648\n\tv_mfma_f32_16x16x32_bf16 v[140:143], v[104:107], v[20:23], v[140:143]\n\ts_add_u32 m0, s54, 131072\n\ts_waitcnt lgkmcnt(8)\n\tv_mfma_f32_16x16x32_bf16 v[144:147], v[96:99], v[24:27], v[144:147]\n\tds_read_b64_tr_b16 v[44:45], v223 offset:20480\n\t" \
    "ds_read_b64_tr_b16 v[46:47], v223 offset:22528\n\tv_mfma_f32_16x16x32_bf16 v[152:155], v[100:103], v[24:27], v[152:155]\n\tv_mfma_f32_16x16x32_bf16 v[160:163], v[104:107], v[24:27], v[160:163]\n\ts_waitcnt lgkmcnt(8)\n\tv_mfma_f32_16x16x32_bf16 v[148:151], v[96:99], v[28:31], v[148:151]\n\tds_read_b64_tr_b16 v[16:17], v220 offset:24576\n\tds_read_b64_tr_b16 v[18:19], v220 offset:26624\n\tv_mfma_f32_16x16x32_bf16 v[156:159], v[100:103], v[28:31], v[156:159]\n\t" \
    "buffer_load_dwordx4 v12, s[16:19], s69 offen lds\n\ts_cmp_le_u32 s4, 4\n\ts_cselect_b32 s69, 262144, 0\n\ts_add_u32 s69, s69, 1179648\n\tv_mfma_f32_16x16x32_bf16 v[164:167], v[104:107], v[28:31], v[164:167]\n\ts_add_u32 m0, s54, 135168\n\ts_waitcnt lgkmcnt(8)\n\tv_mfma_f32_16x16x32_bf16 v[168:171], v[96:99], v[32:35], v[168:171]\n\t" \
    "ds_read_b64_tr_b16 v[20:21], v221 offset:24576\n\tds_read_b64_tr_b16 v[22:23], v221 offset:26624\n\tv_mfma_f32_16x16x32_bf16 v[176:179], v[100:103], v[32:35], v[176:179]\n\tbuffer_load_dwordx4 v13, s[16:19], s69 offen lds\n\ts_cmp_le_u32 s4, 5\n\ts_cselect_b32 s69, 262144, 0\n\ts_add_u32 s69, s69, 1441792\n\tv_mfma_f32_16x16x32_bf16 v[184:187], v[104:107], v[32:35], v[184:187]\n\t" \
    "s_add_u32 m0, s54, 139264\n\ts_waitcnt lgkmcnt(8)\n\tv_mfma_f32_16x16x32_bf16 v[172:175], v[96:99], v[36:39], v[172:175]\n\tds_read_b64_tr_b16 v[24:25], v222 offset:24576\n\tds_read_b64_tr_b16 v[26:27], v222 offset:26624\n\tv_mfma_f32_16x16x32_bf16 v[180:183], v[100:103], v[36:39], v[180:183]\n\tv_mfma_f32_16x16x32_bf16 v[188:191], v[104:107], v[36:39], v[188:191]\n\ts_waitcnt lgkmcnt(8)\n\t" \
    "v_mfma_f32_16x16x32_bf16 v[48:51], v[96:99], v[40:43], v[48:51]\n\tds_read_b64_tr_b16 v[28:29], v223 offset:24576\n\tds_read_b64_tr_b16 v[30:31], v223 offset:26624\n\tv_mfma_f32_16x16x32_bf16 v[56:59], v[100:103], v[40:43], v[56:59]\n\tbuffer_load_dwordx4 v12, s[16:19], s69 offen lds\n\ts_cmp_le_u32 s4, 5\n\ts_cselect_b32 s69, 262144, 0\n\ts_add_u32 s69, s69, 1441792\n\t" \
    "v_mfma_f32_16x16x32_bf16 v[64:67], v[104:107], v[40:43], v[64:67]\n\ts_add_u32 m0, s54, 143360\n\ts_waitcnt lgkmcnt(8)\n\tv_mfma_f32_16x16x32_bf16 v[52:55], v[96:99], v[44:47], v[52:55]\n\tds_read_b64_tr_b16 v[32:33], v220 offset:28672\n\tds_read_b64_tr_b16 v[34:35], v220 offset:30720\n\tv_mfma_f32_16x16x32_bf16 v[60:63], v[100:103], v[44:47], v[60:63]\n\tbuffer_load_dwordx4 v13, s[16:19], s69 offen lds\n\t" \
    "s_cmp_le_u32 s4, 6\n\ts_cselect_b32 s69, 262144, 0\n\ts_add_u32 s69, s69, 1703936\n\tv_mfma_f32_16x16x32_bf16 v[68:71], v[104:107], v[44:47], v[68:71]\n\ts_add_u32 m0, s54, 126976\n\ts_waitcnt lgkmcnt(8)\n\tv_mfma_f32_16x16x32_bf16 v[120:123], v[108:111], v[16:19], v[120:123]\n\tds_read_b64_tr_b16 v[36:37], v221 offset:28672\n\t" \
    "ds_read_b64_tr_b16 v[38:39], v221 offset:30720\n\tv_mfma_f32_16x16x32_bf16 v[128:131], v[112:115], v[16:19], v[128:131]\n\tv_mfma_f32_16x16x32_bf16 v[136:139], v[116:119], v[16:19], v[136:139]\n\ts_waitcnt lgkmcnt(8)\n\tv_mfma_f32_16x16x32_bf16 v[124:127], v[108:111], v[20:23], v[124:127]\n\tds_read_b64_tr_b16 v[40:41], v224 offset:0\n\tds_read_b64_tr_b16 v[42:43], v224 offset:2048\n\tv_mfma_f32_16x16x32_bf16 v[132:135], v[112:115], v[20:23], v[132:135]\n\t" \
    "buffer_load_dwordx4 v12, s[16:19], s69 offen lds\n\ts_cmp_le_u32 s4, 3\n\ts_cselect_b32 s68, 3072, 0\n\ts_add_u32 s68, s68, s46\n\tv_mfma_f32_16x16x32_bf16 v[140:143], v[116:119], v[20:23], v[140:143]\n\ts_add_u32 s68, s68, 64512\n\ts_waitcnt lgkmcnt(8)\n\tv_mfma_f32_16x16x32_bf16 v[144:147], v[108:111], v[24:27], v[144:147]\n\t" \
    "ds_read_b64_tr_b16 v[44:45], v225 offset:0\n\tds_read_b64_tr_b16 v[46:47], v225 offset:2048\n\tv_mfma_f32_16x16x32_bf16 v[152:155], v[112:115], v[24:27], v[152:155]\n\tbuffer_load_dwordx4 a[72:75], v8, s[8:11], s68 offen offset:0 sc1\n\tv_mfma_f32_16x16x32_bf16 v[160:163], v[116:119], v[24:27], v[160:163]\n\ts_waitcnt lgkmcnt(8)\n\tv_mfma_f32_16x16x32_bf16 v[148:151], v[108:111], v[28:31], v[148:151]\n\tds_read_b64_tr_b16 v[16:17], v226 offset:0\n\t" \
    "ds_read_b64_tr_b16 v[18:19], v226 offset:2048\n\tv_mfma_f32_16x16x32_bf16 v[156:159], v[112:115], v[28:31], v[156:159]\n\tv_mfma_f32_16x16x32_bf16 v[164:167], v[116:119], v[28:31], v[164:167]\n\ts_waitcnt lgkmcnt(8)\n\tv_mfma_f32_16x16x32_bf16 v[168:171], v[108:111], v[32:35], v[168:171]\n\tds_read_b64_tr_b16 v[20:21], v227 offset:0\n\tds_read_b64_tr_b16 v[22:23], v227 offset:2048\n\tv_mfma_f32_16x16x32_bf16 v[176:179], v[112:115], v[32:35], v[176:179]\n\t" \
    "buffer_load_dwordx4 a[76:79], v8, s[8:11], s68 offen offset:1024 sc1\n\tv_mfma_f32_16x16x32_bf16 v[184:187], v[116:119], v[32:35], v[184:187]\n\ts_waitcnt lgkmcnt(8)\n\tv_mfma_f32_16x16x32_bf16 v[172:175], v[108:111], v[36:39], v[172:175]\n\tds_read_b64_tr_b16 v[24:25], v224 offset:4096\n\tds_read_b64_tr_b16 v[26:27], v224 offset:6144\n\tv_mfma_f32_16x16x32_bf16 v[180:183], v[112:115], v[36:39], v[180:183]\n\tbuffer_load_dwordx4 a[80:83], v8, s[8:11], s68 offen offset:2048 sc1\n\t" \
    "s_cmp_le_u32 s4, 4\n\ts_cselect_b32 s68, 3072, 0\n\ts_add_u32 s68, s68, s46\n\tv_mfma_f32_16x16x32_bf16 v[188:191], v[116:119], v[36:39], v[188:191]\n\ts_add_u32 s68, s68, 67584\n\ts_waitcnt lgkmcnt(8)\n\tv_mfma_f32_16x16x32_bf16 v[120:123], a[8:11], v[40:43], v[120:123]\n\tds_read_b64_tr_b16 v[28:29], v225 offset:4096\n\t" \
    "ds_read_b64_tr_b16 v[30:31], v225 offset:6144\n\tv_mfma_f32_16x16x32_bf16 v[128:131], a[12:15], v[40:43], v[128:131]\n\tv_mfma_f32_16x16x32_bf16 v[136:139], a[16:19], v[40:43], v[136:139]\n\ts_waitcnt lgkmcnt(8)\n\tv_mfma_f32_16x16x32_bf16 v[124:127], a[8:11], v[44:47], v[124:127]\n\tds_read_b64_tr_b16 v[32:33], v224 offset:8192\n\tds_read_b64_tr_b16 v[34:35], v224 offset:10240\n\tv_mfma_f32_16x16x32_bf16 v[132:135], a[12:15], v[44:47], v[132:135]\n\t" \
    "buffer_load_dwordx4 a[84:87], v8, s[8:11], s68 offen offset:0 sc1\n\tv_mfma_f32_16x16x32_bf16 v[140:143], a[16:19], v[44:47], v[140:143]\n\ts_waitcnt vmcnt(30)\n\ts_barrier\n\ts_waitcnt lgkmcnt(8)\n\tv_mfma_f32_16x16x32_bf16 v[144:147], a[8:11], v[16:19], v[144:147]\n\tds_read_b64_tr_b16 v[36:37], v225 offset:8192\n\tds_read_b64_tr_b16 v[38:39], v225 offset:10240\n\t" \
    "v_mfma_f32_16x16x32_bf16 v[152:155], a[12:15], v[16:19], v[152:155]\n\tbuffer_load_dwordx4 a[88:91], v8, s[8:11], s68 offen offset:1024 sc1\n\tv_mfma_f32_16x16x32_bf16 v[160:163], a[16:19], v[16:19], v[160:163]\n\ts_waitcnt lgkmcnt(8)\n\tv_mfma_f32_16x16x32_bf16 v[148:151], a[8:11], v[20:23], v[148:151]\n\tds_read_b64_tr_b16 v[40:41], v226 offset:8192\n\tds_read_b64_tr_b16 v[42:43], v226 offset:10240\n\tv_mfma_f32_16x16x32_bf16 v[156:159], a[12:15], v[20:23], v[156:159]\n\t" \
    "buffer_load_dwordx4 a[92:95], v8, s[8:11], s68 offen offset:2048 sc1\n\ts_cmp_le_u32 s4, 5\n\ts_cselect_b32 s68, 3072, 0\n\ts_add_u32 s68, s68, s46\n\tv_mfma_f32_16x16x32_bf16 v[164:167], a[16:19], v[20:23], v[164:167]\n\ts_add_u32 s68, s68, 70656\n\ts_waitcnt lgkmcnt(8)\n\tv_mfma_f32_16x16x32_bf16 v[168:171], a[8:11], v[24:27], v[168:171]\n\t" \
    "ds_read_b64_tr_b16 v[44:45], v227 offset:8192\n\tds_read_b64_tr_b16 v[46:47], v227 offset:10240\n\tv_mfma_f32_16x16x32_bf16 v[176:179], a[12:15], v[24:27], v[176:179]\n\tbuffer_load_dwordx4 a[96:99], v8, s[8:11], s68 offen offset:0 sc1\n\tv_mfma_f32_16x16x32_bf16 v[184:187], a[16:19], v[24:27], v[184:187]\n\ts_waitcnt lgkmcnt(8)\n\tv_mfma_f32_16x16x32_bf16 v[172:175], a[8:11], v[28:31], v[172:175]\n\tds_read_b64_tr_b16 v[16:17], v224 offset:12288\n\t" \
    "ds_read_b64_tr_b16 v[18:19], v224 offset:14336\n\tv_mfma_f32_16x16x32_bf16 v[180:183], a[12:15], v[28:31], v[180:183]\n\tv_mfma_f32_16x16x32_bf16 v[188:191], a[16:19], v[28:31], v[188:191]\n\ts_waitcnt vmcnt(30) lgkmcnt(8)\n\tv_mfma_f32_16x16x32_bf16 v[120:123], a[20:23], v[32:35], v[120:123]\n\tds_read_b64_tr_b16 v[20:21], v225 offset:12288\n\tds_read_b64_tr_b16 v[22:23], v225 offset:14336\n\tv_mfma_f32_16x16x32_bf16 v[128:131], a[24:27], v[32:35], v[128:131]\n\t" \
    "buffer_load_dwordx4 a[100:103], v8, s[8:11], s68 offen offset:1024 sc1\n\tv_mfma_f32_16x16x32_bf16 v[136:139], a[28:31], v[32:35], v[136:139]\n\ts_waitcnt lgkmcnt(8)\n\tv_mfma_f32_16x16x32_bf16 v[124:127], a[20:23], v[36:39], v[124:127]\n\tds_read_b64_tr_b16 v[24:25], v224 offset:16384\n\tds_read_b64_tr_b16 v[26:27], v224 offset:18432\n\tv_mfma_f32_16x16x32_bf16 v[132:135], a[24:27], v[36:39], v[132:135]\n\tbuffer_load_dwordx4 a[104:107], v8, s[8:11], s68 offen offset:2048 sc1\n\t" \
    "s_cmp_le_u32 s4, 6\n\ts_cselect_b32 s69, 262144, 0\n\ts_add_u32 s69, s69, 1703936\n\tv_mfma_f32_16x16x32_bf16 v[140:143], a[28:31], v[36:39], v[140:143]\n\ts_add_u32 m0, s54, 147456\n\ts_waitcnt lgkmcnt(8)\n\tv_mfma_f32_16x16x32_bf16 v[144:147], a[20:23], v[40:43], v[144:147]\n\tds_read_b64_tr_b16 v[28:29], v225 offset:16384\n\t" \
    "ds_read_b64_tr_b16 v[30:31], v225 offset:18432\n\tv_mfma_f32_16x16x32_bf16 v[152:155], a[24:27], v[40:43], v[152:155]\n\tv_mfma_f32_16x16x32_bf16 v[160:163], a[28:31], v[40:43], v[160:163]\n\ts_waitcnt lgkmcnt(8)\n\tv_mfma_f32_16x16x32_bf16 v[148:151], a[20:23], v[44:47], v[148:151]\n\tds_read_b64_tr_b16 v[32:33], v226 offset:16384\n\tds_read_b64_tr_b16 v[34:35], v226 offset:18432\n\tv_mfma_f32_16x16x32_bf16 v[156:159], a[24:27], v[44:47], v[156:159]\n\t" \
    "buffer_load_dwordx4 v13, s[16:19], s69 offen lds\n\ts_cmp_le_u32 s4, 7\n\ts_cselect_b32 s69, 262144, 0\n\ts_add_u32 s69, s69, 1966080\n\tv_mfma_f32_16x16x32_bf16 v[164:167], a[28:31], v[44:47], v[164:167]\n\ts_add_u32 m0, s54, 151552\n\ts_waitcnt lgkmcnt(8)\n\tv_mfma_f32_16x16x32_bf16 v[168:171], a[20:23], v[16:19], v[168:171]\n\t" \
    "ds_read_b64_tr_b16 v[36:37], v227 offset:16384\n\tds_read_b64_tr_b16 v[38:39], v227 offset:18432\n\tv_mfma_f32_16x16x32_bf16 v[176:179], a[24:27], v[16:19], v[176:179]\n\tbuffer_load_dwordx4 v12, s[16:19], s69 offen lds\n\ts_cmp_le_u32 s4, 7\n\ts_cselect_b32 s69, 262144, 0\n\ts_add_u32 s69, s69, 1966080\n\tv_mfma_f32_16x16x32_bf16 v[184:187], a[28:31], v[16:19], v[184:187]\n\t" \
    "s_add_u32 m0, s54, 155648\n\ts_waitcnt lgkmcnt(8)\n\tv_mfma_f32_16x16x32_bf16 v[172:175], a[20:23], v[20:23], v[172:175]\n\tds_read_b64_tr_b16 v[40:41], v224 offset:20480\n\tds_read_b64_tr_b16 v[42:43], v224 offset:22528\n\tv_mfma_f32_16x16x32_bf16 v[180:183], a[24:27], v[20:23], v[180:183]\n\tv_mfma_f32_16x16x32_bf16 v[188:191], a[28:31], v[20:23], v[188:191]\n\ts_waitcnt vmcnt(31) lgkmcnt(8)\n\t" \
    "v_mfma_f32_16x16x32_bf16 v[120:123], a[0:3], v[24:27], v[120:123]\n\tds_read_b64_tr_b16 v[44:45], v225 offset:20480\n\tds_read_b64_tr_b16 v[46:47], v225 offset:22528\n\tv_mfma_f32_16x16x32_bf16 v[128:131], a[4:7], v[24:27], v[128:131]\n\tbuffer_load_dwordx4 v13, s[16:19], s69 offen lds\n\ts_cmp_le_u32 s4, 8\n\ts_cselect_b32 s69, 262144, 0\n\ts_add_u32 s69, s69, 2228224\n\t" \
    "v_mfma_f32_16x16x32_bf16 v[136:139], a[32:35], v[24:27], v[136:139]\n\ts_add_u32 m0, s54, 0\n\ts_waitcnt lgkmcnt(8)\n\tv_mfma_f32_16x16x32_bf16 v[124:127], a[0:3], v[28:31], v[124:127]\n\tds_read_b64_tr_b16 v[16:17], v224 offset:24576\n\tds_read_b64_tr_b16 v[18:19], v224 offset:26624\n\tv_mfma_f32_16x16x32_bf16 v[132:135], a[4:7], v[28:31], v[132:135]\n\tbuffer_load_dwordx4 v12, s[16:19], s69 offen lds\n\t" \
    "s_cmp_le_u32 s4, 8\n\ts_cselect_b32 s69, 262144, 0\n\ts_add_u32 s69, s69, 2228224\n\tv_mfma_f32_16x16x32_bf16 v[140:143], a[32:35], v[28:31], v[140:143]\n\ts_add_u32 m0, s54, 4096\n\ts_waitcnt lgkmcnt(8)\n\tv_mfma_f32_16x16x32_bf16 v[144:147], a[0:3], v[32:35], v[144:147]\n\tds_read_b64_tr_b16 v[20:21], v225 offset:24576\n\t" \
    "ds_read_b64_tr_b16 v[22:23], v225 offset:26624\n\tv_mfma_f32_16x16x32_bf16 v[152:155], a[4:7], v[32:35], v[152:155]\n\tv_mfma_f32_16x16x32_bf16 v[160:163], a[32:35], v[32:35], v[160:163]\n\ts_waitcnt lgkmcnt(8)\n\tv_mfma_f32_16x16x32_bf16 v[148:151], a[0:3], v[36:39], v[148:151]\n\tds_read_b64_tr_b16 v[24:25], v226 offset:24576\n\tds_read_b64_tr_b16 v[26:27], v226 offset:26624\n\tv_mfma_f32_16x16x32_bf16 v[156:159], a[4:7], v[36:39], v[156:159]\n\t" \
    "buffer_load_dwordx4 v13, s[16:19], s69 offen lds\n\ts_cmp_le_u32 s4, 6\n\ts_cselect_b32 s68, 3072, 0\n\ts_add_u32 s68, s68, s46\n\tv_mfma_f32_16x16x32_bf16 v[164:167], a[32:35], v[36:39], v[164:167]\n\ts_add_u32 s68, s68, 73728\n\ts_waitcnt lgkmcnt(8)\n\tv_mfma_f32_16x16x32_bf16 v[168:171], a[0:3], v[40:43], v[168:171]\n\t" \
    "ds_read_b64_tr_b16 v[28:29], v227 offset:24576\n\tds_read_b64_tr_b16 v[30:31], v227 offset:26624\n\tv_mfma_f32_16x16x32_bf16 v[176:179], a[4:7], v[40:43], v[176:179]\n\tbuffer_load_dwordx4 a[108:111], v8, s[8:11], s68 offen offset:0 sc1\n\tv_mfma_f32_16x16x32_bf16 v[184:187], a[32:35], v[40:43], v[184:187]\n\ts_waitcnt lgkmcnt(8)\n\tv_mfma_f32_16x16x32_bf16 v[172:175], a[0:3], v[44:47], v[172:175]\n\tds_read_b64_tr_b16 v[32:33], v224 offset:32768\n\t" \
    "ds_read_b64_tr_b16 v[34:35], v224 offset:34816\n\tv_mfma_f32_16x16x32_bf16 v[180:183], a[4:7], v[44:47], v[180:183]\n\tv_mfma_f32_16x16x32_bf16 v[188:191], a[32:35], v[44:47], v[188:191]\n\ts_waitcnt vmcnt(30)\n\ts_barrier\n\ts_waitcnt vmcnt(26) lgkmcnt(8)\n\tv_mfma_f32_16x16x32_bf16 v[120:123], a[36:39], v[16:19], v[120:123]\n\tds_read_b64_tr_b16 v[36:37], v225 offset:32768\n\t" \
    "ds_read_b64_tr_b16 v[38:39], v225 offset:34816\n\tv_mfma_f32_16x16x32_bf16 v[128:131], a[40:43], v[16:19], v[128:131]\n\tbuffer_load_dwordx4 a[112:115], v8, s[8:11], s68 offen offset:1024 sc1\n\tv_mfma_f32_16x16x32_bf16 v[136:139], a[44:47], v[16:19], v[136:139]\n\ts_waitcnt lgkmcnt(8)\n\tv_mfma_f32_16x16x32_bf16 v[124:127], a[36:39], v[20:23], v[124:127]\n\tds_read_b64_tr_b16 v[40:41], v224 offset:36864\n\tds_read_b64_tr_b16 v[42:43], v224 offset:38912\n\t" \
    "v_mfma_f32_16x16x32_bf16 v[132:135], a[40:43], v[20:23], v[132:135]\n\tbuffer_load_dwordx4 a[116:119], v8, s[8:11], s68 offen offset:2048 sc1\n\ts_cmp_le_u32 s4, 7\n\ts_cselect_b32 s68, 3072, 0\n\ts_add_u32 s68, s68, s46\n\tv_mfma_f32_16x16x32_bf16 v[140:143], a[44:47], v[20:23], v[140:143]\n\ts_add_u32 s68, s68, 76800\n\ts_waitcnt lgkmcnt(8)\n\t" \
    "v_mfma_f32_16x16x32_bf16 v[144:147], a[36:39], v[24:27], v[144:147]\n\tds_read_b64_tr_b16 v[44:45], v225 offset:36864\n\tds_read_b64_tr_b16 v[46:47], v225 offset:38912\n\tv_mfma_f32_16x16x32_bf16 v[152:155], a[40:43], v[24:27], v[152:155]\n\tbuffer_load_dwordx4 a[120:123], v8, s[8:11], s68 offen offset:0 sc1\n\tv_mfma_f32_16x16x32_bf16 v[160:163], a[44:47], v[24:27], v[160:163]\n\ts_waitcnt lgkmcnt(8)\n\tv_mfma_f32_16x16x32_bf16 v[148:151], a[36:39], v[28:31], v[148:151]\n\t" \
    "ds_read_b64_tr_b16 v[16:17], v226 offset:36864\n\tds_read_b64_tr_b16 v[18:19], v226 offset:38912\n\tv_mfma_f32_16x16x32_bf16 v[156:159], a[40:43], v[28:31], v[156:159]\n\tbuffer_load_dwordx4 a[124:127], v8, s[8:11], s68 offen offset:1024 sc1\n\tv_mfma_f32_16x16x32_bf16 v[164:167], a[44:47], v[28:31], v[164:167]\n\ts_waitcnt lgkmcnt(8)\n\tv_mfma_f32_16x16x32_bf16 v[168:171], a[36:39], v[32:35], v[168:171]\n\tds_read_b64_tr_b16 v[20:21], v227 offset:36864\n\t" \
    "ds_read_b64_tr_b16 v[22:23], v227 offset:38912\n\tv_mfma_f32_16x16x32_bf16 v[176:179], a[40:43], v[32:35], v[176:179]\n\tv_mfma_f32_16x16x32_bf16 v[184:187], a[44:47], v[32:35], v[184:187]\n\ts_waitcnt lgkmcnt(8)\n\tv_mfma_f32_16x16x32_bf16 v[172:175], a[36:39], v[36:39], v[172:175]\n\tds_read_b64_tr_b16 v[24:25], v224 offset:40960\n\tds_read_b64_tr_b16 v[26:27], v224 offset:43008\n\tv_mfma_f32_16x16x32_bf16 v[180:183], a[40:43], v[36:39], v[180:183]\n\t" \
    "buffer_load_dwordx4 a[128:131], v8, s[8:11], s68 offen offset:2048 sc1\n\ts_cmp_le_u32 s4, 9\n\ts_cselect_b32 s69, 262144, 0\n\ts_add_u32 s69, s69, 2490368\n\tv_mfma_f32_16x16x32_bf16 v[188:191], a[44:47], v[36:39], v[188:191]\n\ts_add_u32 m0, s54, 8192\n\ts_waitcnt vmcnt(28) lgkmcnt(8)\n\tv_mfma_f32_16x16x32_bf16 v[120:123], a[48:51], v[40:43], v[120:123]\n\t" \
    "ds_read_b64_tr_b16 v[28:29], v225 offset:40960\n\tds_read_b64_tr_b16 v[30:31], v225 offset:43008\n\tv_mfma_f32_16x16x32_bf16 v[128:131], a[52:55], v[40:43], v[128:131]\n\tbuffer_load_dwordx4 v12, s[16:19], s69 offen lds\n\ts_cmp_le_u32 s4, 9\n\ts_cselect_b32 s69, 262144, 0\n\ts_add_u32 s69, s69, 2490368\n\tv_mfma_f32_16x16x32_bf16 v[136:139], a[56:59], v[40:43], v[136:139]\n\t" \
    "s_add_u32 m0, s54, 12288\n\ts_waitcnt lgkmcnt(8)\n\tv_mfma_f32_16x16x32_bf16 v[124:127], a[48:51], v[44:47], v[124:127]\n\tds_read_b64_tr_b16 v[32:33], v224 offset:45056\n\tds_read_b64_tr_b16 v[34:35], v224 offset:47104\n\tv_mfma_f32_16x16x32_bf16 v[132:135], a[52:55], v[44:47], v[132:135]\n\tv_mfma_f32_16x16x32_bf16 v[140:143], a[56:59], v[44:47], v[140:143]\n\ts_waitcnt lgkmcnt(8)\n\t" \
    "v_mfma_f32_16x16x32_bf16 v[144:147], a[48:51], v[16:19], v[144:147]\n\tds_read_b64_tr_b16 v[36:37], v225 offset:45056\n\tds_read_b64_tr_b16 v[38:39], v225 offset:47104\n\tv_mfma_f32_16x16x32_bf16 v[152:155], a[52:55], v[16:19], v[152:155]\n\tbuffer_load_dwordx4 v13, s[16:19], s69 offen lds\n\ts_cmp_le_u32 s4, 10\n\ts_cselect_b32 s69, 262144, 0\n\ts_add_u32 s69, s69, 2752512\n\t" \
    "v_mfma_f32_16x16x32_bf16 v[160:163], a[56:59], v[16:19], v[160:163]\n\ts_add_u32 m0, s54, 16384\n\ts_waitcnt lgkmcnt(8)\n\tv_mfma_f32_16x16x32_bf16 v[148:151], a[48:51], v[20:23], v[148:151]\n\tds_read_b64_tr_b16 v[40:41], v226 offset:45056\n\tds_read_b64_tr_b16 v[42:43], v226 offset:47104\n\tv_mfma_f32_16x16x32_bf16 v[156:159], a[52:55], v[20:23], v[156:159]\n\tbuffer_load_dwordx4 v12, s[16:19], s69 offen lds\n\t" \
    "s_cmp_le_u32 s4, 10\n\ts_cselect_b32 s69, 262144, 0\n\ts_add_u32 s69, s69, 2752512\n\tv_mfma_f32_16x16x32_bf16 v[164:167], a[56:59], v[20:23], v[164:167]\n\ts_add_u32 m0, s54, 20480\n\ts_waitcnt lgkmcnt(8)\n\tv_mfma_f32_16x16x32_bf16 v[168:171], a[48:51], v[24:27], v[168:171]\n\tds_read_b64_tr_b16 v[44:45], v227 offset:45056\n\t" \
    "ds_read_b64_tr_b16 v[46:47], v227 offset:47104\n\tv_mfma_f32_16x16x32_bf16 v[176:179], a[52:55], v[24:27], v[176:179]\n\tv_mfma_f32_16x16x32_bf16 v[184:187], a[56:59], v[24:27], v[184:187]\n\ts_waitcnt lgkmcnt(8)\n\tv_mfma_f32_16x16x32_bf16 v[172:175], a[48:51], v[28:31], v[172:175]\n\tds_read_b64_tr_b16 v[16:17], v224 offset:49152\n\tds_read_b64_tr_b16 v[18:19], v224 offset:51200\n\tv_mfma_f32_16x16x32_bf16 v[180:183], a[52:55], v[28:31], v[180:183]\n\t" \
    "buffer_load_dwordx4 v13, s[16:19], s69 offen lds\n\ts_cmp_le_u32 s4, 11\n\ts_cselect_b32 s69, 262144, 0\n\ts_add_u32 s69, s69, 3014656\n\tv_mfma_f32_16x16x32_bf16 v[188:191], a[56:59], v[28:31], v[188:191]\n\ts_add_u32 m0, s54, 24576\n\ts_waitcnt vmcnt(29) lgkmcnt(8)\n\tv_mfma_f32_16x16x32_bf16 v[120:123], a[60:63], v[32:35], v[120:123]\n\t" \
    "ds_read_b64_tr_b16 v[20:21], v225 offset:49152\n\tds_read_b64_tr_b16 v[22:23], v225 offset:51200\n\tv_mfma_f32_16x16x32_bf16 v[128:131], a[64:67], v[32:35], v[128:131]\n\tbuffer_load_dwordx4 v12, s[16:19], s69 offen lds\n\ts_cmp_le_u32 s4, 12\n\ts_cselect_b32 s69, 262144, 0\n\ts_add_u32 s69, s69, 3276800\n\tv_mfma_f32_16x16x32_bf16 v[136:139], a[68:71], v[32:35], v[136:139]\n\t" \
    "s_add_u32 m0, s54, 28672\n\ts_waitcnt lgkmcnt(8)\n\tv_mfma_f32_16x16x32_bf16 v[124:127], a[60:63], v[36:39], v[124:127]\n\tds_read_b64_tr_b16 v[24:25], v224 offset:53248\n\tds_read_b64_tr_b16 v[26:27], v224 offset:55296\n\tv_mfma_f32_16x16x32_bf16 v[132:135], a[64:67], v[36:39], v[132:135]\n\tv_mfma_f32_16x16x32_bf16 v[140:143], a[68:71], v[36:39], v[140:143]\n\ts_waitcnt lgkmcnt(8)\n\t" \
    "v_mfma_f32_16x16x32_bf16 v[144:147], a[60:63], v[40:43], v[144:147]\n\tds_read_b64_tr_b16 v[28:29], v225 offset:53248\n\tds_read_b64_tr_b16 v[30:31], v225 offset:55296\n\tv_mfma_f32_16x16x32_bf16 v[152:155], a[64:67], v[40:43], v[152:155]\n\tbuffer_load_dwordx4 v12, s[16:19], s69 offen lds\n\ts_cmp_le_u32 s4, 8\n\ts_cselect_b32 s68, 3072, 0\n\ts_add_u32 s68, s68, s46\n\t" \
    "v_mfma_f32_16x16x32_bf16 v[160:163], a[68:71], v[40:43], v[160:163]\n\ts_add_u32 s68, s68, 79872\n\ts_waitcnt lgkmcnt(8)\n\tv_mfma_f32_16x16x32_bf16 v[148:151], a[60:63], v[44:47], v[148:151]\n\tds_read_b64_tr_b16 v[32:33], v226 offset:53248\n\tds_read_b64_tr_b16 v[34:35], v226 offset:55296\n\tv_mfma_f32_16x16x32_bf16 v[156:159], a[64:67], v[44:47], v[156:159]\n\tbuffer_load_dwordx4 a[132:135], v8, s[8:11], s68 offen offset:0 sc1\n\t" \
    "v_mfma_f32_16x16x32_bf16 v[164:167], a[68:71], v[44:47], v[164:167]\n\ts_waitcnt vmcnt(27)\n\ts_barrier\n\ts_waitcnt lgkmcnt(8)\n\tv_mfma_f32_16x16x32_bf16 v[168:171], a[60:63], v[16:19], v[168:171]\n\tds_read_b64_tr_b16 v[36:37], v227 offset:53248\n\tds_read_b64_tr_b16 v[38:39], v227 offset:55296\n\tv_mfma_f32_16x16x32_bf16 v[176:179], a[64:67], v[16:19], v[176:179]\n\t" \
    "buffer_load_dwordx4 a[136:139], v8, s[8:11], s68 offen offset:1024 sc1\n\tv_mfma_f32_16x16x32_bf16 v[184:187], a[68:71], v[16:19], v[184:187]\n\ts_waitcnt lgkmcnt(8)\n\tv_mfma_f32_16x16x32_bf16 v[172:175], a[60:63], v[20:23], v[172:175]\n\tds_read_b64_tr_b16 v[40:41], v224 offset:57344\n\tds_read_b64_tr_b16 v[42:43], v224 offset:59392\n\tv_mfma_f32_16x16x32_bf16 v[180:183], a[64:67], v[20:23], v[180:183]\n\tbuffer_load_dwordx4 a[140:143], v8, s[8:11], s68 offen offset:2048 sc1\n\t" \
    "s_cmp_le_u32 s4, 9\n\ts_cselect_b32 s68, 3072, 0\n\ts_add_u32 s68, s68, s46\n\tv_mfma_f32_16x16x32_bf16 v[188:191], a[68:71], v[20:23], v[188:191]\n\ts_add_u32 s68, s68, 82944\n\ts_waitcnt vmcnt(26) lgkmcnt(8)\n\tv_mfma_f32_16x16x32_bf16 v[120:123], a[72:75], v[24:27], v[120:123]\n\tds_read_b64_tr_b16 v[44:45], v225 offset:57344\n\t" \
    "ds_read_b64_tr_b16 v[46:47], v225 offset:59392\n\tv_mfma_f32_16x16x32_bf16 v[128:131], a[76:79], v[24:27], v[128:131]\n\tv_mfma_f32_16x16x32_bf16 v[136:139], a[80:83], v[24:27], v[136:139]\n\ts_waitcnt lgkmcnt(8)\n\tv_mfma_f32_16x16x32_bf16 v[124:127], a[72:75], v[28:31], v[124:127]\n\tds_read_b64_tr_b16 v[16:17], v220 offset:0\n\tds_read_b64_tr_b16 v[18:19], v220 offset:2048\n\tv_mfma_f32_16x16x32_bf16 v[132:135], a[76:79], v[28:31], v[132:135]\n\t" \
    "buffer_load_dwordx4 a[144:147], v8, s[8:11], s68 offen offset:0 sc1\n\tv_mfma_f32_16x16x32_bf16 v[140:143], a[80:83], v[28:31], v[140:143]\n\ts_waitcnt lgkmcnt(8)\n\tv_mfma_f32_16x16x32_bf16 v[144:147], a[72:75], v[32:35], v[144:147]\n\tds_read_b64_tr_b16 v[20:21], v221 offset:0\n\tds_read_b64_tr_b16 v[22:23], v221 offset:2048\n\tv_mfma_f32_16x16x32_bf16 v[152:155], a[76:79], v[32:35], v[152:155]\n\tbuffer_load_dwordx4 a[148:151], v8, s[8:11], s68 offen offset:1024 sc1\n\t" \
    "v_mfma_f32_16x16x32_bf16 v[160:163], a[80:83], v[32:35], v[160:163]\n\ts_waitcnt lgkmcnt(8)\n\tv_mfma_f32_16x16x32_bf16 v[148:151], a[72:75], v[36:39], v[148:151]\n\tds_read_b64_tr_b16 v[24:25], v222 offset:0\n\tds_read_b64_tr_b16 v[26:27], v222 offset:2048\n\tv_mfma_f32_16x16x32_bf16 v[156:159], a[76:79], v[36:39], v[156:159]\n\tv_mfma_f32_16x16x32_bf16 v[164:167], a[80:83], v[36:39], v[164:167]\n\ts_waitcnt lgkmcnt(8)\n\t" \
    "v_mfma_f32_16x16x32_bf16 v[168:171], a[72:75], v[40:43], v[168:171]\n\tds_read_b64_tr_b16 v[28:29], v223 offset:0\n\tds_read_b64_tr_b16 v[30:31], v223 offset:2048\n\tv_mfma_f32_16x16x32_bf16 v[176:179], a[76:79], v[40:43], v[176:179]\n\tbuffer_load_dwordx4 a[152:155], v8, s[8:11], s68 offen offset:2048 sc1\n\ts_cmp_le_u32 s4, 10\n\ts_cselect_b32 s68, 3072, 0\n\ts_add_u32 s68, s68, s46\n\t" \
    "v_mfma_f32_16x16x32_bf16 v[184:187], a[80:83], v[40:43], v[184:187]\n\ts_add_u32 s68, s68, 86016\n\ts_waitcnt lgkmcnt(8)\n\tv_mfma_f32_16x16x32_bf16 v[172:175], a[72:75], v[44:47], v[172:175]\n\tds_read_b64_tr_b16 v[32:33], v220 offset:4096\n\tds_read_b64_tr_b16 v[34:35], v220 offset:6144\n\tv_mfma_f32_16x16x32_bf16 v[180:183], a[76:79], v[44:47], v[180:183]\n\tbuffer_load_dwordx4 a[156:159], v8, s[8:11], s68 offen offset:0 sc1\n\t" \
    "v_mfma_f32_16x16x32_bf16 v[188:191], a[80:83], v[44:47], v[188:191]\n\ts_waitcnt vmcnt(27) lgkmcnt(8)\n\tv_mfma_f32_16x16x32_bf16 v[120:123], a[84:87], v[16:19], v[120:123]\n\tds_read_b64_tr_b16 v[36:37], v221 offset:4096\n\tds_read_b64_tr_b16 v[38:39], v221 offset:6144\n\tv_mfma_f32_16x16x32_bf16 v[128:131], a[88:91], v[16:19], v[128:131]\n\tv_mfma_f32_16x16x32_bf16 v[136:139], a[92:95], v[16:19], v[136:139]\n\ts_waitcnt lgkmcnt(8)\n\t" \
    "v_mfma_f32_16x16x32_bf16 v[124:127], a[84:87], v[20:23], v[124:127]\n\tds_read_b64_tr_b16 v[40:41], v220 offset:8192\n\tds_read_b64_tr_b16 v[42:43], v220 offset:10240\n\tv_mfma_f32_16x16x32_bf16 v[132:135], a[88:91], v[20:23], v[132:135]\n\tbuffer_load_dwordx4 a[160:163], v8, s[8:11], s68 offen offset:1024 sc1\n\tv_mfma_f32_16x16x32_bf16 v[140:143], a[92:95], v[20:23], v[140:143]\n\ts_waitcnt lgkmcnt(8)\n\tv_mfma_f32_16x16x32_bf16 v[144:147], a[84:87], v[24:27], v[144:147]\n\t" \
    "ds_read_b64_tr_b16 v[44:45], v221 offset:8192\n\tds_read_b64_tr_b16 v[46:47], v221 offset:10240\n\tv_mfma_f32_16x16x32_bf16 v[152:155], a[88:91], v[24:27], v[152:155]\n\tv_mfma_f32_16x16x32_bf16 v[160:163], a[92:95], v[24:27], v[160:163]\n\ts_waitcnt lgkmcnt(8)\n\tv_mfma_f32_16x16x32_bf16 v[148:151], a[84:87], v[28:31], v[148:151]\n\tds_read_b64_tr_b16 v[16:17], v222 offset:8192\n\tds_read_b64_tr_b16 v[18:19], v222 offset:10240\n\t" \
    "v_mfma_f32_16x16x32_bf16 v[156:159], a[88:91], v[28:31], v[156:159]\n\tbuffer_load_dwordx4 a[164:167], v8, s[8:11], s68 offen offset:2048 sc1\n\ts_cmp_le_u32 s4, 13\n\ts_cselect_b32 s69, 262144, 0\n\ts_add_u32 s69, s69, 3538944\n\tv_mfma_f32_16x16x32_bf16 v[164:167], a[92:95], v[28:31], v[164:167]\n\ts_add_u32 m0, s54, 32768\n\ts_waitcnt lgkmcnt(8)\n\t" \
    "v_mfma_f32_16x16x32_bf16 v[168:171], a[84:87], v[32:35], v[168:171]\n\tds_read_b64_tr_b16 v[20:21], v223 offset:8192\n\tds_read_b64_tr_b16 v[22:23], v223 offset:10240\n\tv_mfma_f32_16x16x32_bf16 v[176:179], a[88:91], v[32:35], v[176:179]\n\tbuffer_load_dwordx4 v12, s[16:19], s69 offen lds\n\ts_cmp_le_u32 s4, 14\n\ts_cselect_b32 s69, 262144, 0\n\ts_add_u32 s69, s69, 3801088\n\t" \
    "v_mfma_f32_16x16x32_bf16 v[184:187], a[92:95], v[32:35], v[184:187]\n\ts_add_u32 m0, s54, 94208\n\ts_waitcnt lgkmcnt(8)\n\tv_mfma_f32_16x16x32_bf16 v[172:175], a[84:87], v[36:39], v[172:175]\n\tds_read_b64_tr_b16 v[24:25], v220 offset:12288\n\tds_read_b64_tr_b16 v[26:27], v220 offset:14336\n\tv_mfma_f32_16x16x32_bf16 v[180:183], a[88:91], v[36:39], v[180:183]\n\tbuffer_load_dwordx4 v12, s[16:19], s69 offen lds\n\t" \
    "s_cmp_le_u32 s4, 15\n\ts_cselect_b32 s69, 262144, 0\n\ts_add_u32 s69, s69, 4063232\n\tv_mfma_f32_16x16x32_bf16 v[188:191], a[92:95], v[36:39], v[188:191]\n\ts_add_u32 m0, s54, 36864\n\ts_waitcnt vmcnt(28) lgkmcnt(8)\n\tv_mfma_f32_16x16x32_bf16 v[120:123], a[96:99], v[40:43], v[120:123]\n\tds_read_b64_tr_b16 v[28:29], v221 offset:12288\n\t" \
    "ds_read_b64_tr_b16 v[30:31], v221 offset:14336\n\tv_mfma_f32_16x16x32_bf16 v[128:131], a[100:103], v[40:43], v[128:131]\n\tv_mfma_f32_16x16x32_bf16 v[136:139], a[104:107], v[40:43], v[136:139]\n\ts_waitcnt lgkmcnt(8)\n\tv_mfma_f32_16x16x32_bf16 v[124:127], a[96:99], v[44:47], v[124:127]\n\tds_read_b64_tr_b16 v[32:33], v224 offset:61440\n\tds_read_b64_tr_b16 v[34:35], v224 offset:63488\n\tv_mfma_f32_16x16x32_bf16 v[132:135], a[100:103], v[44:47], v[132:135]\n\t" \
    "buffer_load_dwordx4 v12, s[16:19], s69 offen lds\n\ts_cmp_le_u32 s4, 16\n\ts_cselect_b32 s69, 262144, 0\n\ts_add_u32 s69, s69, 4325376\n\tv_mfma_f32_16x16x32_bf16 v[140:143], a[104:107], v[44:47], v[140:143]\n\ts_add_u32 m0, s54, 40960\n\ts_waitcnt vmcnt(24)\n\ts_barrier\n\t" \
    "s_waitcnt lgkmcnt(8)\n\tv_mfma_f32_16x16x32_bf16 v[144:147], a[96:99], v[16:19], v[144:147]\n\tds_read_b64_tr_b16 v[36:37], v225 offset:61440\n\tds_read_b64_tr_b16 v[38:39], v225 offset:63488\n\tv_mfma_f32_16x16x32_bf16 v[152:155], a[100:103], v[16:19], v[152:155]\n\tbuffer_load_dwordx4 v12, s[16:19], s69 offen lds\n\ts_cmp_le_u32 s4, 11\n\ts_cselect_b32 s68, 3072, 0\n\t" \
    "s_add_u32 s68, s68, s46\n\tv_mfma_f32_16x16x32_bf16 v[160:163], a[104:107], v[16:19], v[160:163]\n\ts_add_u32 s68, s68, 89088\n\ts_waitcnt lgkmcnt(8)\n\tv_mfma_f32_16x16x32_bf16 v[148:151], a[96:99], v[20:23], v[148:151]\n\tds_read_b64_tr_b16 v[40:41], v226 offset:61440\n\tds_read_b64_tr_b16 v[42:43], v226 offset:63488\n\tv_mfma_f32_16x16x32_bf16 v[156:159], a[100:103], v[20:23], v[156:159]\n\t" \
    "buffer_load_dwordx4 a[168:171], v8, s[8:11], s68 offen offset:0 sc1\n\tv_mfma_f32_16x16x32_bf16 v[164:167], a[104:107], v[20:23], v[164:167]\n\ts_waitcnt lgkmcnt(8)\n\tv_mfma_f32_16x16x32_bf16 v[168:171], a[96:99], v[24:27], v[168:171]\n\tds_read_b64_tr_b16 v[44:45], v227 offset:61440\n\tds_read_b64_tr_b16 v[46:47], v227 offset:63488\n\tv_mfma_f32_16x16x32_bf16 v[176:179], a[100:103], v[24:27], v[176:179]\n\tbuffer_load_dwordx4 a[172:175], v8, s[8:11], s68 offen offset:1024 sc1\n\t" \
    "v_mfma_f32_16x16x32_bf16 v[184:187], a[104:107], v[24:27], v[184:187]\n\ts_waitcnt lgkmcnt(8)\n\tv_mfma_f32_16x16x32_bf16 v[172:175], a[96:99], v[28:31], v[172:175]\n\tds_read_b64_tr_b16 v[16:17], v220 offset:16384\n\tds_read_b64_tr_b16 v[18:19], v220 offset:18432\n\tv_mfma_f32_16x16x32_bf16 v[180:183], a[100:103], v[28:31], v[180:183]\n\tv_mfma_f32_16x16x32_bf16 v[188:191], a[104:107], v[28:31], v[188:191]\n\ts_waitcnt vmcnt(24) lgkmcnt(8)\n\t" \
    "v_mfma_f32_16x16x32_bf16 v[120:123], a[108:111], v[32:35], v[120:123]\n\tds_read_b64_tr_b16 v[20:21], v221 offset:16384\n\tds_read_b64_tr_b16 v[22:23], v221 offset:18432\n\tv_mfma_f32_16x16x32_bf16 v[128:131], a[112:115], v[32:35], v[128:131]\n\tbuffer_load_dwordx4 a[176:179], v8, s[8:11], s68 offen offset:2048 sc1\n\ts_cmp_le_u32 s4, 12\n\ts_cselect_b32 s68, 3072, 0\n\ts_add_u32 s68, s68, s46\n\t" \
    "v_mfma_f32_16x16x32_bf16 v[136:139], a[116:119], v[32:35], v[136:139]\n\ts_add_u32 s68, s68, 92160\n\ts_waitcnt lgkmcnt(8)\n\tv_mfma_f32_16x16x32_bf16 v[124:127], a[108:111], v[36:39], v[124:127]\n\tds_read_b64_tr_b16 v[24:25], v220 offset:20480\n\tds_read_b64_tr_b16 v[26:27], v220 offset:22528\n\tv_mfma_f32_16x16x32_bf16 v[132:135], a[112:115], v[36:39], v[132:135]\n\tbuffer_load_dwordx4 a[180:183], v8, s[8:11], s68 offen offset:0 sc1\n\t" \
    "v_mfma_f32_16x16x32_bf16 v[140:143], a[116:119], v[36:39], v[140:143]\n\ts_waitcnt lgkmcnt(8)\n\tv_mfma_f32_16x16x32_bf16 v[144:147], a[108:111], v[40:43], v[144:147]\n\tds_read_b64_tr_b16 v[28:29], v221 offset:20480\n\tds_read_b64_tr_b16 v[30:31], v221 offset:22528\n\tv_mfma_f32_16x16x32_bf16 v[152:155], a[112:115], v[40:43], v[152:155]\n\tv_mfma_f32_16x16x32_bf16 v[160:163], a[116:119], v[40:43], v[160:163]\n\ts_waitcnt lgkmcnt(8)\n\t" \
    "v_mfma_f32_16x16x32_bf16 v[148:151], a[108:111], v[44:47], v[148:151]\n\tds_read_b64_tr_b16 v[32:33], v222 offset:20480\n\tds_read_b64_tr_b16 v[34:35], v222 offset:22528\n\tv_mfma_f32_16x16x32_bf16 v[156:159], a[112:115], v[44:47], v[156:159]\n\tbuffer_load_dwordx4 a[184:187], v8, s[8:11], s68 offen offset:1024 sc1\n\tv_mfma_f32_16x16x32_bf16 v[164:167], a[116:119], v[44:47], v[164:167]\n\ts_waitcnt lgkmcnt(8)\n\tv_mfma_f32_16x16x32_bf16 v[168:171], a[108:111], v[16:19], v[168:171]\n\t" \
    "ds_read_b64_tr_b16 v[36:37], v223 offset:20480\n\tds_read_b64_tr_b16 v[38:39], v223 offset:22528\n\tv_mfma_f32_16x16x32_bf16 v[176:179], a[112:115], v[16:19], v[176:179]\n\tv_mfma_f32_16x16x32_bf16 v[184:187], a[116:119], v[16:19], v[184:187]\n\ts_waitcnt lgkmcnt(8)\n\tv_mfma_f32_16x16x32_bf16 v[172:175], a[108:111], v[20:23], v[172:175]\n\tds_read_b64_tr_b16 v[40:41], v220 offset:24576\n\tds_read_b64_tr_b16 v[42:43], v220 offset:26624\n\t" \
    "v_mfma_f32_16x16x32_bf16 v[180:183], a[112:115], v[20:23], v[180:183]\n\tbuffer_load_dwordx4 a[188:191], v8, s[8:11], s68 offen offset:2048 sc1\n\ts_cmp_le_u32 s4, 13\n\ts_cselect_b32 s68, 3072, 0\n\ts_add_u32 s68, s68, s46\n\tv_mfma_f32_16x16x32_bf16 v[188:191], a[116:119], v[20:23], v[188:191]\n\ts_add_u32 s68, s68, 95232\n\ts_waitcnt vmcnt(25) lgkmcnt(8)\n\t" \
    "v_mfma_f32_16x16x32_bf16 v[120:123], a[120:123], v[24:27], v[120:123]\n\tds_read_b64_tr_b16 v[44:45], v221 offset:24576\n\tds_read_b64_tr_b16 v[46:47], v221 offset:26624\n\tv_mfma_f32_16x16x32_bf16 v[128:131], a[124:127], v[24:27], v[128:131]\n\tbuffer_load_dwordx4 a[192:195], v8, s[8:11], s68 offen offset:0 sc1\n\tv_mfma_f32_16x16x32_bf16 v[136:139], a[128:131], v[24:27], v[136:139]\n\ts_waitcnt lgkmcnt(8)\n\tv_mfma_f32_16x16x32_bf16 v[124:127], a[120:123], v[28:31], v[124:127]\n\t" \
    "ds_read_b64_tr_b16 v[16:17], v4 offset:0\n\tds_read_b64_tr_b16 v[18:19], v4 offset:2048\n\tv_mfma_f32_16x16x32_bf16 v[132:135], a[124:127], v[28:31], v[132:135]\n\tv_mfma_f32_16x16x32_bf16 v[140:143], a[128:131], v[28:31], v[140:143]\n\ts_waitcnt lgkmcnt(8)\n\tv_mfma_f32_16x16x32_bf16 v[144:147], a[120:123], v[32:35], v[144:147]\n\tds_read_b64_tr_b16 v[20:21], v5 offset:0\n\tds_read_b64_tr_b16 v[22:23], v5 offset:2048\n\t" \
    "v_mfma_f32_16x16x32_bf16 v[152:155], a[124:127], v[32:35], v[152:155]\n\tbuffer_load_dwordx4 a[196:199], v8, s[8:11], s68 offen offset:1024 sc1\n\tv_mfma_f32_16x16x32_bf16 v[160:163], a[128:131], v[32:35], v[160:163]\n\ts_waitcnt lgkmcnt(8)\n\tv_mfma_f32_16x16x32_bf16 v[148:151], a[120:123], v[36:39], v[148:151]\n\tds_read_b64_tr_b16 v[24:25], v6 offset:0\n\tds_read_b64_tr_b16 v[26:27], v6 offset:2048\n\tv_mfma_f32_16x16x32_bf16 v[156:159], a[124:127], v[36:39], v[156:159]\n\t" \
    "buffer_load_dwordx4 a[200:203], v8, s[8:11], s68 offen offset:2048 sc1\n\ts_cmp_le_u32 s4, 14\n\ts_cselect_b32 s68, 3072, 0\n\ts_add_u32 s68, s68, s46\n\tv_mfma_f32_16x16x32_bf16 v[164:167], a[128:131], v[36:39], v[164:167]\n\ts_add_u32 s68, s68, 98304\n\ts_waitcnt lgkmcnt(8)\n\tv_mfma_f32_16x16x32_bf16 v[168:171], a[120:123], v[40:43], v[168:171]\n\t" \
    "ds_read_b64_tr_b16 v[28:29], v7 offset:0\n\tds_read_b64_tr_b16 v[30:31], v7 offset:2048\n\tv_mfma_f32_16x16x32_bf16 v[176:179], a[124:127], v[40:43], v[176:179]\n\tv_mfma_f32_16x16x32_bf16 v[184:187], a[128:131], v[40:43], v[184:187]\n\ts_waitcnt lgkmcnt(8)\n\tv_mfma_f32_16x16x32_bf16 v[172:175], a[120:123], v[44:47], v[172:175]\n\tds_read_b64_tr_b16 v[32:33], v4 offset:4096\n\tds_read_b64_tr_b16 v[34:35], v4 offset:6144\n\t" \
    "v_mfma_f32_16x16x32_bf16 v[180:183], a[124:127], v[44:47], v[180:183]\n\tbuffer_load_dwordx4 a[204:207], v8, s[8:11], s68 offen offset:0 sc1\n\tv_mfma_f32_16x16x32_bf16 v[188:191], a[128:131], v[44:47], v[188:191]\n\ts_waitcnt vmcnt(23)\n\ts_barrier\n\ts_waitcnt vmcnt(20) lgkmcnt(8)\n\tv_mfma_f32_16x16x32_bf16 v[120:123], a[132:135], v[16:19], v[120:123]\n\tds_read_b64_tr_b16 v[36:37], v5 offset:4096\n\t" \
    "ds_read_b64_tr_b16 v[38:39], v5 offset:6144\n\tv_mfma_f32_16x16x32_bf16 v[128:131], a[136:139], v[16:19], v[128:131]\n\tbuffer_load_dwordx4 a[208:211], v8, s[8:11], s68 offen offset:1024 sc1\n\tv_mfma_f32_16x16x32_bf16 v[136:139], a[140:143], v[16:19], v[136:139]\n\ts_waitcnt lgkmcnt(8)\n\tv_mfma_f32_16x16x32_bf16 v[124:127], a[132:135], v[20:23], v[124:127]\n\tds_read_b64_tr_b16 v[40:41], v4 offset:8192\n\tds_read_b64_tr_b16 v[42:43], v4 offset:10240\n\t" \
    "v_mfma_f32_16x16x32_bf16 v[132:135], a[136:139], v[20:23], v[132:135]\n\tbuffer_load_dwordx4 a[212:215], v8, s[8:11], s68 offen offset:2048 sc1\n\ts_cmp_le_u32 s4, 15\n\ts_cselect_b32 s68, 3072, 0\n\ts_add_u32 s68, s68, s46\n\tv_mfma_f32_16x16x32_bf16 v[140:143], a[140:143], v[20:23], v[140:143]\n\ts_add_u32 s68, s68, 101376\n\ts_waitcnt lgkmcnt(8)\n\t" \
    "v_mfma_f32_16x16x32_bf16 v[144:147], a[132:135], v[24:27], v[144:147]\n\tds_read_b64_tr_b16 v[44:45], v5 offset:8192\n\tds_read_b64_tr_b16 v[46:47], v5 offset:10240\n\tv_mfma_f32_16x16x32_bf16 v[152:155], a[136:139], v[24:27], v[152:155]\n\tbuffer_load_dwordx4 a[216:219], v8, s[8:11], s68 offen offset:0 sc1\n\tv_mfma_f32_16x16x32_bf16 v[160:163], a[140:143], v[24:27], v[160:163]\n\ts_waitcnt lgkmcnt(8)\n\tv_mfma_f32_16x16x32_bf16 v[148:151], a[132:135], v[28:31], v[148:151]\n\t" \
    "ds_read_b64_tr_b16 v[16:17], v6 offset:8192\n\tds_read_b64_tr_b16 v[18:19], v6 offset:10240\n\tv_mfma_f32_16x16x32_bf16 v[156:159], a[136:139], v[28:31], v[156:159]\n\tv_mfma_f32_16x16x32_bf16 v[164:167], a[140:143], v[28:31], v[164:167]\n\ts_waitcnt lgkmcnt(8)\n\tv_mfma_f32_16x16x32_bf16 v[168:171], a[132:135], v[32:35], v[168:171]\n\tds_read_b64_tr_b16 v[20:21], v7 offset:8192\n\tds_read_b64_tr_b16 v[22:23], v7 offset:10240\n\t" \
    "v_mfma_f32_16x16x32_bf16 v[176:179], a[136:139], v[32:35], v[176:179]\n\tbuffer_load_dwordx4 a[220:223], v8, s[8:11], s68 offen offset:1024 sc1\n\tv_mfma_f32_16x16x32_bf16 v[184:187], a[140:143], v[32:35], v[184:187]\n\ts_waitcnt lgkmcnt(8)\n\tv_mfma_f32_16x16x32_bf16 v[172:175], a[132:135], v[36:39], v[172:175]\n\tds_read_b64_tr_b16 v[24:25], v4 offset:12288\n\tds_read_b64_tr_b16 v[26:27], v4 offset:14336\n\tv_mfma_f32_16x16x32_bf16 v[180:183], a[136:139], v[36:39], v[180:183]\n\t" \
    "buffer_load_dwordx4 a[224:227], v8, s[8:11], s68 offen offset:2048 sc1\n\ts_cmp_le_u32 s4, 16\n\ts_cselect_b32 s68, 3072, 0\n\ts_add_u32 s68, s68, s46\n\tv_mfma_f32_16x16x32_bf16 v[188:191], a[140:143], v[36:39], v[188:191]\n\ts_add_u32 s68, s68, 104448\n\ts_waitcnt vmcnt(22) lgkmcnt(8)\n\tv_mfma_f32_16x16x32_bf16 v[120:123], a[144:147], v[40:43], v[120:123]\n\t" \
    "ds_read_b64_tr_b16 v[28:29], v5 offset:12288\n\tds_read_b64_tr_b16 v[30:31], v5 offset:14336\n\tv_mfma_f32_16x16x32_bf16 v[128:131], a[148:151], v[40:43], v[128:131]\n\tv_mfma_f32_16x16x32_bf16 v[136:139], a[152:155], v[40:43], v[136:139]\n\ts_waitcnt lgkmcnt(8)\n\tv_mfma_f32_16x16x32_bf16 v[124:127], a[144:147], v[44:47], v[124:127]\n\tds_read_b64_tr_b16 v[32:33], v4 offset:16384\n\tds_read_b64_tr_b16 v[34:35], v4 offset:18432\n\t" \
    "v_mfma_f32_16x16x32_bf16 v[132:135], a[148:151], v[44:47], v[132:135]\n\tbuffer_load_dwordx4 a[228:231], v8, s[8:11], s68 offen offset:0 sc1\n\tv_mfma_f32_16x16x32_bf16 v[140:143], a[152:155], v[44:47], v[140:143]\n\ts_waitcnt lgkmcnt(8)\n\tv_mfma_f32_16x16x32_bf16 v[144:147], a[144:147], v[16:19], v[144:147]\n\tds_read_b64_tr_b16 v[36:37], v5 offset:16384\n\tds_read_b64_tr_b16 v[38:39], v5 offset:18432\n\tv_mfma_f32_16x16x32_bf16 v[152:155], a[148:151], v[16:19], v[152:155]\n\t" \
    "buffer_load_dwordx4 a[232:235], v8, s[8:11], s68 offen offset:1024 sc1\n\tv_mfma_f32_16x16x32_bf16 v[160:163], a[152:155], v[16:19], v[160:163]\n\ts_waitcnt lgkmcnt(8)\n\tv_mfma_f32_16x16x32_bf16 v[148:151], a[144:147], v[20:23], v[148:151]\n\tds_read_b64_tr_b16 v[40:41], v6 offset:16384\n\tds_read_b64_tr_b16 v[42:43], v6 offset:18432\n\tv_mfma_f32_16x16x32_bf16 v[156:159], a[148:151], v[20:23], v[156:159]\n\tv_mfma_f32_16x16x32_bf16 v[164:167], a[152:155], v[20:23], v[164:167]\n\t" \
    "s_waitcnt lgkmcnt(8)\n\tv_mfma_f32_16x16x32_bf16 v[168:171], a[144:147], v[24:27], v[168:171]\n\tds_read_b64_tr_b16 v[44:45], v7 offset:16384\n\tds_read_b64_tr_b16 v[46:47], v7 offset:18432\n\tv_mfma_f32_16x16x32_bf16 v[176:179], a[148:151], v[24:27], v[176:179]\n\tbuffer_load_dwordx4 a[236:239], v8, s[8:11], s68 offen offset:2048 sc1\n\ts_cmp_le_u32 s4, 11\n\ts_cselect_b32 s69, 262144, 0\n\t" \
    "s_add_u32 s69, s69, 3014656\n\tv_mfma_f32_16x16x32_bf16 v[184:187], a[152:155], v[24:27], v[184:187]\n\ts_add_u32 m0, s54, 131072\n\ts_waitcnt lgkmcnt(8)\n\tv_mfma_f32_16x16x32_bf16 v[172:175], a[144:147], v[28:31], v[172:175]\n\tds_read_b64_tr_b16 v[16:17], v4 offset:20480\n\tds_read_b64_tr_b16 v[18:19], v4 offset:22528\n\tv_mfma_f32_16x16x32_bf16 v[180:183], a[148:151], v[28:31], v[180:183]\n\t" \
    "buffer_load_dwordx4 v13, s[16:19], s69 offen lds\n\ts_cmp_le_u32 s4, 12\n\ts_cselect_b32 s69, 262144, 0\n\ts_add_u32 s69, s69, 3276800\n\tv_mfma_f32_16x16x32_bf16 v[188:191], a[152:155], v[28:31], v[188:191]\n\ts_add_u32 m0, s54, 139264\n\ts_waitcnt vmcnt(23) lgkmcnt(8)\n\tv_mfma_f32_16x16x32_bf16 v[120:123], a[156:159], v[32:35], v[120:123]\n\t" \
    "ds_read_b64_tr_b16 v[20:21], v5 offset:20480\n\tds_read_b64_tr_b16 v[22:23], v5 offset:22528\n\tv_mfma_f32_16x16x32_bf16 v[128:131], a[160:163], v[32:35], v[128:131]\n\tv_mfma_f32_16x16x32_bf16 v[136:139], a[164:167], v[32:35], v[136:139]\n\ts_waitcnt lgkmcnt(8)\n\tv_mfma_f32_16x16x32_bf16 v[124:127], a[156:159], v[36:39], v[124:127]\n\tds_read_b64_tr_b16 v[24:25], v4 offset:24576\n\tds_read_b64_tr_b16 v[26:27], v4 offset:26624\n\t" \
    "v_mfma_f32_16x16x32_bf16 v[132:135], a[160:163], v[36:39], v[132:135]\n\tbuffer_load_dwordx4 v13, s[16:19], s69 offen lds\n\ts_cmp_le_u32 s4, 13\n\ts_cselect_b32 s69, 262144, 0\n\ts_add_u32 s69, s69, 3538944\n\tv_mfma_f32_16x16x32_bf16 v[140:143], a[164:167], v[36:39], v[140:143]\n\ts_add_u32 m0, s54, 151552\n\ts_waitcnt lgkmcnt(8)\n\t" \
    "v_mfma_f32_16x16x32_bf16 v[144:147], a[156:159], v[40:43], v[144:147]\n\tds_read_b64_tr_b16 v[28:29], v5 offset:24576\n\tds_read_b64_tr_b16 v[30:31], v5 offset:26624\n\tv_mfma_f32_16x16x32_bf16 v[152:155], a[160:163], v[40:43], v[152:155]\n\tbuffer_load_dwordx4 v13, s[16:19], s69 offen lds\n\ts_cmp_le_u32 s4, 14\n\ts_cselect_b32 s69, 262144, 0\n\ts_add_u32 s69, s69, 3801088\n\t" \
    "v_mfma_f32_16x16x32_bf16 v[160:163], a[164:167], v[40:43], v[160:163]\n\ts_add_u32 m0, s54, 45056\n\ts_waitcnt lgkmcnt(8)\n\tv_mfma_f32_16x16x32_bf16 v[148:151], a[156:159], v[44:47], v[148:151]\n\tds_read_b64_tr_b16 v[32:33], v4 offset:28672\n\tds_read_b64_tr_b16 v[34:35], v4 offset:30720\n\tv_mfma_f32_16x16x32_bf16 v[156:159], a[160:163], v[44:47], v[156:159]\n\tv_mfma_f32_16x16x32_bf16 v[164:167], a[164:167], v[44:47], v[164:167]\n\t" \
    "s_waitcnt vmcnt(21)\n\ts_barrier\n\ts_waitcnt lgkmcnt(8)\n\tv_mfma_f32_16x16x32_bf16 v[168:171], a[156:159], v[16:19], v[168:171]\n\tds_read_b64_tr_b16 v[36:37], v5 offset:28672\n\tds_read_b64_tr_b16 v[38:39], v5 offset:30720\n\tv_mfma_f32_16x16x32_bf16 v[176:179], a[160:163], v[16:19], v[176:179]\n\tbuffer_load_dwordx4 v13, s[16:19], s69 offen lds\n\t" \
    "s_cmp_le_u32 s4, 15\n\ts_cselect_b32 s69, 262144, 0\n\ts_add_u32 s69, s69, 4063232\n\tv_mfma_f32_16x16x32_bf16 v[184:187], a[164:167], v[16:19], v[184:187]\n\ts_add_u32 m0, s54, 49152\n\ts_waitcnt lgkmcnt(8)\n\tv_mfma_f32_16x16x32_bf16 v[172:175], a[156:159], v[20:23], v[172:175]\n\tds_read_b64_tr_b16 v[40:41], v4 offset:32768\n\t" \
    "ds_read_b64_tr_b16 v[42:43], v4 offset:34816\n\tv_mfma_f32_16x16x32_bf16 v[180:183], a[160:163], v[20:23], v[180:183]\n\tbuffer_load_dwordx4 v13, s[16:19], s69 offen lds\n\ts_cmp_le_u32 s4, 16\n\ts_cselect_b32 s69, 262144, 0\n\ts_add_u32 s69, s69, 4325376\n\tv_mfma_f32_16x16x32_bf16 v[188:191], a[164:167], v[20:23], v[188:191]\n\ts_add_u32 m0, s54, 53248\n\t" \
    "s_waitcnt vmcnt(20) lgkmcnt(8)\n\tv_mfma_f32_16x16x32_bf16 v[120:123], a[168:171], v[24:27], v[120:123]\n\tds_read_b64_tr_b16 v[44:45], v5 offset:32768\n\tds_read_b64_tr_b16 v[46:47], v5 offset:34816\n\tv_mfma_f32_16x16x32_bf16 v[128:131], a[172:175], v[24:27], v[128:131]\n\tbuffer_load_dwordx4 v13, s[16:19], s69 offen lds\n\tv_mfma_f32_16x16x32_bf16 v[136:139], a[176:179], v[24:27], v[136:139]\n\ts_waitcnt lgkmcnt(8)\n\t" \
    "v_mfma_f32_16x16x32_bf16 v[124:127], a[168:171], v[28:31], v[124:127]\n\tds_read_b64_tr_b16 v[16:17], v224 offset:28672\n\tds_read_b64_tr_b16 v[18:19], v224 offset:30720\n\tv_mfma_f32_16x16x32_bf16 v[132:135], a[172:175], v[28:31], v[132:135]\n\tbuffer_load_dwordx4 v[192:195], v243, s[20:23], 0 offen offset:0\n\tv_mfma_f32_16x16x32_bf16 v[140:143], a[176:179], v[28:31], v[140:143]\n\ts_waitcnt vmcnt(19) lgkmcnt(8)\n\tv_mfma_f32_16x16x32_bf16 v[120:123], a[180:183], v[32:35], v[120:123]\n\t" \
    "ds_read_b64_tr_b16 v[20:21], v225 offset:28672\n\tds_read_b64_tr_b16 v[22:23], v225 offset:30720\n\tv_mfma_f32_16x16x32_bf16 v[128:131], a[184:187], v[32:35], v[128:131]\n\tv_mfma_f32_16x16x32_bf16 v[136:139], a[188:191], v[32:35], v[136:139]\n\ts_waitcnt lgkmcnt(8)\n\tv_mfma_f32_16x16x32_bf16 v[124:127], a[180:183], v[36:39], v[124:127]\n\tds_read_b64_tr_b16 v[24:25], v4 offset:36864\n\tds_read_b64_tr_b16 v[26:27], v4 offset:38912\n\t" \
    "v_mfma_f32_16x16x32_bf16 v[132:135], a[184:187], v[36:39], v[132:135]\n\tbuffer_load_dwordx4 v[196:199], v243, s[20:23], 0 offen offset:32\n\tv_mfma_f32_16x16x32_bf16 v[140:143], a[188:191], v[36:39], v[140:143]\n\ts_waitcnt vmcnt(17) lgkmcnt(8)\n\tv_mfma_f32_16x16x32_bf16 v[120:123], a[192:195], v[40:43], v[120:123]\n\tds_read_b64_tr_b16 v[28:29], v5 offset:36864\n\tds_read_b64_tr_b16 v[30:31], v5 offset:38912\n\tv_mfma_f32_16x16x32_bf16 v[128:131], a[196:199], v[40:43], v[128:131]\n\t" \
    "buffer_load_dwordx4 v[200:203], v243, s[20:23], 0 offen offset:64\n\tv_mfma_f32_16x16x32_bf16 v[136:139], a[200:203], v[40:43], v[136:139]\n\ts_waitcnt lgkmcnt(8)\n\tv_mfma_f32_16x16x32_bf16 v[124:127], a[192:195], v[44:47], v[124:127]\n\tds_read_b64_tr_b16 v[32:33], v4 offset:40960\n\tds_read_b64_tr_b16 v[34:35], v4 offset:43008\n\tv_mfma_f32_16x16x32_bf16 v[132:135], a[196:199], v[44:47], v[132:135]\n\tv_mfma_f32_16x16x32_bf16 v[140:143], a[200:203], v[44:47], v[140:143]\n\t" \
    "s_waitcnt vmcnt(15) lgkmcnt(8)\n\tv_mfma_f32_16x16x32_bf16 v[120:123], a[204:207], v[16:19], v[120:123]\n\tds_read_b64_tr_b16 v[36:37], v5 offset:40960\n\tds_read_b64_tr_b16 v[38:39], v5 offset:43008\n\tv_mfma_f32_16x16x32_bf16 v[128:131], a[208:211], v[16:19], v[128:131]\n\tbuffer_load_dwordx2 v[216:217], v242, s[8:11], s59 offen offset:0 sc1\n\tv_mfma_f32_16x16x32_bf16 v[136:139], a[212:215], v[16:19], v[136:139]\n\ts_waitcnt lgkmcnt(8)\n\t" \
    "v_mfma_f32_16x16x32_bf16 v[124:127], a[204:207], v[20:23], v[124:127]\n\tds_read_b64_tr_b16 v[40:41], v6 offset:24576\n\tds_read_b64_tr_b16 v[42:43], v6 offset:26624\n\tv_mfma_f32_16x16x32_bf16 v[132:135], a[208:211], v[20:23], v[132:135]\n\tv_mfma_f32_16x16x32_bf16 v[140:143], a[212:215], v[20:23], v[140:143]\n\ts_waitcnt vmcnt(13) lgkmcnt(8)\n\tv_mfma_f32_16x16x32_bf16 v[120:123], a[216:219], v[24:27], v[120:123]\n\tds_read_b64_tr_b16 v[44:45], v7 offset:24576\n\t" \
    "ds_read_b64_tr_b16 v[46:47], v7 offset:26624\n\tv_mfma_f32_16x16x32_bf16 v[128:131], a[220:223], v[24:27], v[128:131]\n\tbuffer_load_dwordx2 v[218:219], v242, s[8:11], s59 offen offset:128 sc1\n\tv_mfma_f32_16x16x32_bf16 v[136:139], a[224:227], v[24:27], v[136:139]\n\ts_waitcnt lgkmcnt(8)\n\tv_mfma_f32_16x16x32_bf16 v[124:127], a[216:219], v[28:31], v[124:127]\n\tds_read_b64_tr_b16 v[16:17], v6 offset:28672\n\tds_read_b64_tr_b16 v[18:19], v6 offset:30720\n\t" \
    "v_mfma_f32_16x16x32_bf16 v[132:135], a[220:223], v[28:31], v[132:135]\n\tv_mfma_f32_16x16x32_bf16 v[140:143], a[224:227], v[28:31], v[140:143]\n\ts_waitcnt vmcnt(11) lgkmcnt(8)\n\tv_mfma_f32_16x16x32_bf16 v[120:123], a[228:231], v[32:35], v[120:123]\n\tds_read_b64_tr_b16 v[20:21], v7 offset:28672\n\tds_read_b64_tr_b16 v[22:23], v7 offset:30720\n\tv_mfma_f32_16x16x32_bf16 v[128:131], a[232:235], v[32:35], v[128:131]\n\tv_mfma_f32_16x16x32_bf16 v[136:139], a[236:239], v[32:35], v[136:139]\n\t" \
    "v_min_u32 v241, 376, v239\n\tbuffer_load_dwordx2 v[250:251], v241, s[8:11], s51 offen sc1\n\ts_waitcnt lgkmcnt(8)\n\tv_mfma_f32_16x16x32_bf16 v[124:127], a[228:231], v[36:39], v[124:127]\n\tds_read_b64_tr_b16 v[24:25], v6 offset:32768\n\tds_read_b64_tr_b16 v[26:27], v6 offset:34816\n\tv_mfma_f32_16x16x32_bf16 v[132:135], a[232:235], v[36:39], v[132:135]\n\tv_mfma_f32_16x16x32_bf16 v[140:143], a[236:239], v[36:39], v[140:143]\n\t" \
    "s_waitcnt lgkmcnt(8)\n\tv_mfma_f32_16x16x32_bf16 v[144:147], a[168:171], v[40:43], v[144:147]\n\tds_read_b64_tr_b16 v[28:29], v7 offset:32768\n\tds_read_b64_tr_b16 v[30:31], v7 offset:34816\n\ts_xor_b32 s68, s53, 12\n\tv_mfma_f32_16x16x32_bf16 v[152:155], a[172:175], v[40:43], v[152:155]\n\ts_mul_i32 s68, s68, 9984\n\ts_add_u32 s68, s68, 7208960\n\t" \
    "v_cvt_pk_bf16_f32 v120, v120, v121\n\tv_mfma_f32_16x16x32_bf16 v[160:163], a[176:179], v[40:43], v[160:163]\n\tv_cvt_pk_bf16_f32 v121, v122, v123\n\tv_cvt_pk_bf16_f32 v122, v124, v125\n\ts_waitcnt lgkmcnt(8)\n\tv_mfma_f32_16x16x32_bf16 v[148:151], a[168:171], v[44:47], v[148:151]\n\tds_read_b64_tr_b16 v[32:33], v226 offset:28672\n\tds_read_b64_tr_b16 v[34:35], v226 offset:30720\n\t" \
    "v_cvt_pk_bf16_f32 v123, v126, v127\n\tv_mfma_f32_16x16x32_bf16 v[156:159], a[172:175], v[44:47], v[156:159]\n\tbuffer_store_dwordx4 v[120:123], v8, s[8:11], s68 offen offset:0 sc1\n\tv_cvt_pk_bf16_f32 v124, v128, v129\n\tv_cvt_pk_bf16_f32 v125, v130, v131\n\tv_mfma_f32_16x16x32_bf16 v[164:167], a[176:179], v[44:47], v[164:167]\n\tv_cvt_pk_bf16_f32 v126, v132, v133\n\tv_cvt_pk_bf16_f32 v127, v134, v135\n\t" \
    "buffer_store_dwordx4 v[124:127], v8, s[8:11], s68 offen offset:1024 sc1\n\tv_cvt_pk_bf16_f32 v128, v136, v137\n\tv_cvt_pk_bf16_f32 v129, v138, v139\n\tv_cvt_pk_bf16_f32 v130, v140, v141\n\tv_cvt_pk_bf16_f32 v131, v142, v143\n\tbuffer_store_dwordx4 v[128:131], v8, s[8:11], s68 offen offset:2048 sc1\n\tv_add3_u32 v132, s36, v120, v121\n\tv_add3_u32 v133, v122, v123, v124\n\t" \
    "v_add3_u32 v132, v132, v125, v126\n\tv_add3_u32 v133, v133, v127, v128\n\tv_add3_u32 v132, v132, v129, v130\n\tv_add3_u32 v132, v132, v133, v131\n\tbuffer_store_dword v132, v236, s[8:11], s68 offen offset:3072 sc1\n\ts_waitcnt vmcnt(10)\n\ts_barrier\n\ts_waitcnt lgkmcnt(8)\n\t" \
    "v_mfma_f32_16x16x32_bf16 v[144:147], a[180:183], v[16:19], v[144:147]\n\tds_read_b64_tr_b16 v[36:37], v227 offset:28672\n\tds_read_b64_tr_b16 v[38:39], v227 offset:30720\n\tv_mfma_f32_16x16x32_bf16 v[152:155], a[184:187], v[16:19], v[152:155]\n\tv_mfma_f32_16x16x32_bf16 v[160:163], a[188:191], v[16:19], v[160:163]\n\ts_waitcnt lgkmcnt(8)\n\tv_mfma_f32_16x16x32_bf16 v[148:151], a[180:183], v[20:23], v[148:151]\n\tds_read_b64_tr_b16 v[40:41], v6 offset:36864\n\t" \
    "ds_read_b64_tr_b16 v[42:43], v6 offset:38912\n\tv_mfma_f32_16x16x32_bf16 v[156:159], a[184:187], v[20:23], v[156:159]\n\tv_mfma_f32_16x16x32_bf16 v[164:167], a[188:191], v[20:23], v[164:167]\n\ts_waitcnt lgkmcnt(8)\n\tv_mfma_f32_16x16x32_bf16 v[144:147], a[192:195], v[24:27], v[144:147]\n\tds_read_b64_tr_b16 v[44:45], v7 offset:36864\n\tds_read_b64_tr_b16 v[46:47], v7 offset:38912\n\tv_mfma_f32_16x16x32_bf16 v[152:155], a[196:199], v[24:27], v[152:155]\n\t" \
    "v_mfma_f32_16x16x32_bf16 v[160:163], a[200:203], v[24:27], v[160:163]\n\ts_waitcnt lgkmcnt(8)\n\tv_mfma_f32_16x16x32_bf16 v[148:151], a[192:195], v[28:31], v[148:151]\n\tds_read_b64_tr_b16 v[16:17], v6 offset:40960\n\tds_read_b64_tr_b16 v[18:19], v6 offset:43008\n\tv_mfma_f32_16x16x32_bf16 v[156:159], a[196:199], v[28:31], v[156:159]\n\tv_mfma_f32_16x16x32_bf16 v[164:167], a[200:203], v[28:31], v[164:167]\n\ts_waitcnt lgkmcnt(8)\n\t" \
    "v_mfma_f32_16x16x32_bf16 v[144:147], a[204:207], v[32:35], v[144:147]\n\tds_read_b64_tr_b16 v[20:21], v7 offset:40960\n\tds_read_b64_tr_b16 v[22:23], v7 offset:43008\n\tv_mfma_f32_16x16x32_bf16 v[152:155], a[208:211], v[32:35], v[152:155]\n\tv_mfma_f32_16x16x32_bf16 v[160:163], a[212:215], v[32:35], v[160:163]\n\ts_waitcnt lgkmcnt(8)\n\tv_mfma_f32_16x16x32_bf16 v[148:151], a[204:207], v[36:39], v[148:151]\n\tds_read_b64_tr_b16 v[24:25], v220 offset:0\n\t" \
    "ds_read_b64_tr_b16 v[26:27], v220 offset:2048\n\tv_mfma_f32_16x16x32_bf16 v[156:159], a[208:211], v[36:39], v[156:159]\n\tv_mfma_f32_16x16x32_bf16 v[164:167], a[212:215], v[36:39], v[164:167]\n\ts_waitcnt lgkmcnt(8)\n\tv_mfma_f32_16x16x32_bf16 v[144:147], a[216:219], v[40:43], v[144:147]\n\tds_read_b64_tr_b16 v[28:29], v221 offset:0\n\tds_read_b64_tr_b16 v[30:31], v221 offset:2048\n\tv_mfma_f32_16x16x32_bf16 v[152:155], a[220:223], v[40:43], v[152:155]\n\t" \
    "v_mfma_f32_16x16x32_bf16 v[160:163], a[224:227], v[40:43], v[160:163]\n\ts_waitcnt lgkmcnt(8)\n\tv_mfma_f32_16x16x32_bf16 v[148:151], a[216:219], v[44:47], v[148:151]\n\tds_read_b64_tr_b16 v[32:33], v220 offset:8192\n\tds_read_b64_tr_b16 v[34:35], v220 offset:10240\n\tv_mfma_f32_16x16x32_bf16 v[156:159], a[220:223], v[44:47], v[156:159]\n\tv_mfma_f32_16x16x32_bf16 v[164:167], a[224:227], v[44:47], v[164:167]\n\ts_waitcnt lgkmcnt(8)\n\t" \
    "v_mfma_f32_16x16x32_bf16 v[144:147], a[228:231], v[16:19], v[144:147]\n\tds_read_b64_tr_b16 v[36:37], v221 offset:8192\n\tds_read_b64_tr_b16 v[38:39], v221 offset:10240\n\tv_mfma_f32_16x16x32_bf16 v[152:155], a[232:235], v[16:19], v[152:155]\n\tv_mfma_f32_16x16x32_bf16 v[160:163], a[236:239], v[16:19], v[160:163]\n\ts_waitcnt lgkmcnt(8)\n\tv_mfma_f32_16x16x32_bf16 v[148:151], a[228:231], v[20:23], v[148:151]\n\tds_read_b64_tr_b16 v[40:41], v220 offset:20480\n\t" \
    "ds_read_b64_tr_b16 v[42:43], v220 offset:22528\n\tv_mfma_f32_16x16x32_bf16 v[156:159], a[232:235], v[20:23], v[156:159]\n\tv_mfma_f32_16x16x32_bf16 v[164:167], a[236:239], v[20:23], v[164:167]\n\ts_waitcnt lgkmcnt(8)\n\tv_mfma_f32_16x16x32_bf16 v[168:171], a[168:171], v[24:27], v[168:171]\n\tds_read_b64_tr_b16 v[44:45], v221 offset:20480\n\tds_read_b64_tr_b16 v[46:47], v221 offset:22528\n\ts_xor_b32 s68, s53, 8\n\t" \
    "v_mfma_f32_16x16x32_bf16 v[176:179], a[172:175], v[24:27], v[176:179]\n\ts_mul_i32 s68, s68, 9984\n\ts_add_u32 s68, s68, 7212288\n\tv_cvt_pk_bf16_f32 v144, v144, v145\n\tv_mfma_f32_16x16x32_bf16 v[184:187], a[176:179], v[24:27], v[184:187]\n\tv_cvt_pk_bf16_f32 v145, v146, v147\n\tv_cvt_pk_bf16_f32 v146, v148, v149\n\ts_waitcnt lgkmcnt(8)\n\t" \
    "v_mfma_f32_16x16x32_bf16 v[172:175], a[168:171], v[28:31], v[172:175]\n\tds_read_b64_tr_b16 v[16:17], v4 offset:45056\n\tds_read_b64_tr_b16 v[18:19], v4 offset:47104\n\tv_cvt_pk_bf16_f32 v147, v150, v151\n\tv_mfma_f32_16x16x32_bf16 v[180:183], a[172:175], v[28:31], v[180:183]\n\tbuffer_store_dwordx4 v[144:147], v8, s[8:11], s68 offen offset:0 sc1\n\tv_cvt_pk_bf16_f32 v148, v152, v153\n\tv_cvt_pk_bf16_f32 v149, v154, v155\n\t" \
    "v_mfma_f32_16x16x32_bf16 v[188:191], a[176:179], v[28:31], v[188:191]\n\tv_cvt_pk_bf16_f32 v150, v156, v157\n\tv_cvt_pk_bf16_f32 v151, v158, v159\n\ts_waitcnt lgkmcnt(8)\n\tv_mfma_f32_16x16x32_bf16 v[168:171], a[180:183], v[32:35], v[168:171]\n\tds_read_b64_tr_b16 v[20:21], v5 offset:45056\n\tds_read_b64_tr_b16 v[22:23], v5 offset:47104\n\tv_mfma_f32_16x16x32_bf16 v[176:179], a[184:187], v[32:35], v[176:179]\n\t" \
    "buffer_store_dwordx4 v[148:151], v8, s[8:11], s68 offen offset:1024 sc1\n\tv_cvt_pk_bf16_f32 v152, v160, v161\n\tv_cvt_pk_bf16_f32 v153, v162, v163\n\tv_mfma_f32_16x16x32_bf16 v[184:187], a[188:191], v[32:35], v[184:187]\n\tv_cvt_pk_bf16_f32 v154, v164, v165\n\tv_cvt_pk_bf16_f32 v155, v166, v167\n\ts_waitcnt lgkmcnt(8)\n\tv_mfma_f32_16x16x32_bf16 v[172:175], a[180:183], v[36:39], v[172:175]\n\t" \
    "ds_read_b64_tr_b16 v[24:25], v4 offset:49152\n\tds_read_b64_tr_b16 v[26:27], v4 offset:51200\n\tv_mfma_f32_16x16x32_bf16 v[180:183], a[184:187], v[36:39], v[180:183]\n\tbuffer_store_dwordx4 v[152:155], v8, s[8:11], s68 offen offset:2048 sc1\n\tv_add3_u32 v156, s36, v144, v145\n\tv_add3_u32 v157, v146, v147, v148\n\tv_mfma_f32_16x16x32_bf16 v[188:191], a[188:191], v[36:39], v[188:191]\n\tv_add3_u32 v156, v156, v149, v150\n\t" \
    "v_add3_u32 v157, v157, v151, v152\n\ts_waitcnt lgkmcnt(8)\n\tv_mfma_f32_16x16x32_bf16 v[168:171], a[192:195], v[40:43], v[168:171]\n\tds_read_b64_tr_b16 v[28:29], v5 offset:49152\n\tds_read_b64_tr_b16 v[30:31], v5 offset:51200\n\tv_add3_u32 v156, v156, v153, v154\n\tv_mfma_f32_16x16x32_bf16 v[176:179], a[196:199], v[40:43], v[176:179]\n\tv_add3_u32 v156, v156, v157, v155\n\t" \
    "v_mfma_f32_16x16x32_bf16 v[184:187], a[200:203], v[40:43], v[184:187]\n\ts_waitcnt lgkmcnt(8)\n\tv_mfma_f32_16x16x32_bf16 v[172:175], a[192:195], v[44:47], v[172:175]\n\tds_read_b64_tr_b16 v[32:33], v4 offset:53248\n\tds_read_b64_tr_b16 v[34:35], v4 offset:55296\n\tv_mfma_f32_16x16x32_bf16 v[180:183], a[196:199], v[44:47], v[180:183]\n\tbuffer_store_dword v156, v236, s[8:11], s68 offen offset:3072 sc1\n\tv_mfma_f32_16x16x32_bf16 v[188:191], a[200:203], v[44:47], v[188:191]\n\t" \
    "s_waitcnt lgkmcnt(8)\n\tv_mfma_f32_16x16x32_bf16 v[168:171], a[204:207], v[16:19], v[168:171]\n\tds_read_b64_tr_b16 v[36:37], v5 offset:53248\n\tds_read_b64_tr_b16 v[38:39], v5 offset:55296\n\tv_mfma_f32_16x16x32_bf16 v[176:179], a[208:211], v[16:19], v[176:179]\n\tv_mfma_f32_16x16x32_bf16 v[184:187], a[212:215], v[16:19], v[184:187]\n\ts_waitcnt lgkmcnt(8)\n\tv_mfma_f32_16x16x32_bf16 v[172:175], a[204:207], v[20:23], v[172:175]\n\t" \
    "ds_read_b64_tr_b16 v[40:41], v222 offset:28672\n\tds_read_b64_tr_b16 v[42:43], v222 offset:30720\n\tv_mfma_f32_16x16x32_bf16 v[180:183], a[208:211], v[20:23], v[180:183]\n\tv_mfma_f32_16x16x32_bf16 v[188:191], a[212:215], v[20:23], v[188:191]\n\ts_waitcnt lgkmcnt(8)\n\tv_mfma_f32_16x16x32_bf16 v[168:171], a[216:219], v[24:27], v[168:171]\n\tds_read_b64_tr_b16 v[44:45], v223 offset:28672\n\tds_read_b64_tr_b16 v[46:47], v223 offset:30720\n\t" \
    "v_mfma_f32_16x16x32_bf16 v[176:179], a[220:223], v[24:27], v[176:179]\n\tv_mfma_f32_16x16x32_bf16 v[184:187], a[224:227], v[24:27], v[184:187]\n\ts_waitcnt lgkmcnt(8)\n\tv_mfma_f32_16x16x32_bf16 v[172:175], a[216:219], v[28:31], v[172:175]\n\tds_read_b64_tr_b16 v[16:17], v226 offset:4096\n\tds_read_b64_tr_b16 v[18:19], v226 offset:6144\n\tv_mfma_f32_16x16x32_bf16 v[180:183], a[220:223], v[28:31], v[180:183]\n\tv_mfma_f32_16x16x32_bf16 v[188:191], a[224:227], v[28:31], v[188:191]\n\t" \
    "s_waitcnt vmcnt(8)\n\tv_cmp_ne_u64 vcc, s[36:37], v[250:251]\n\ts_cmp_lg_u64 vcc, 0\n\ts_cselect_b32 s70, 1, 0\n\ts_waitcnt lgkmcnt(8)\n\tv_mfma_f32_16x16x32_bf16 v[168:171], a[228:231], v[32:35], v[168:171]\n\tds_read_b64_tr_b16 v[20:21], v227 offset:4096\n\tds_read_b64_tr_b16 v[22:23], v227 offset:6144\n\t" \
    "v_mfma_f32_16x16x32_bf16 v[176:179], a[232:235], v[32:35], v[176:179]\n\tbuffer_load_dwordx4 v[204:207], v8, s[8:11], s56 offen offset:0 sc1\n\tv_mfma_f32_16x16x32_bf16 v[184:187], a[236:239], v[32:35], v[184:187]\n\ts_waitcnt lgkmcnt(8)\n\tv_mfma_f32_16x16x32_bf16 v[172:175], a[228:231], v[36:39], v[172:175]\n\tds_read_b64_tr_b16 v[24:25], v226 offset:12288\n\tds_read_b64_tr_b16 v[26:27], v226 offset:14336\n\tv_mfma_f32_16x16x32_bf16 v[180:183], a[232:235], v[36:39], v[180:183]\n\t" \
    "buffer_load_dwordx4 v[208:211], v8, s[8:11], s56 offen offset:1024 sc1\n\tv_mfma_f32_16x16x32_bf16 v[188:191], a[236:239], v[36:39], v[188:191]\n\ts_waitcnt lgkmcnt(8)\n\tv_mfma_f32_16x16x32_bf16 v[48:51], v[108:111], v[40:43], v[48:51]\n\tds_read_b64_tr_b16 v[28:29], v227 offset:12288\n\tds_read_b64_tr_b16 v[30:31], v227 offset:14336\n\tv_mfma_f32_16x16x32_bf16 v[56:59], v[112:115], v[40:43], v[56:59]\n\tbuffer_load_dwordx4 v[212:215], v8, s[8:11], s56 offen offset:2048 sc1\n\t" \
    "s_xor_b32 s68, s53, 4\n\ts_mul_i32 s68, s68, 9984\n\tv_mfma_f32_16x16x32_bf16 v[64:67], v[116:119], v[40:43], v[64:67]\n\ts_add_u32 s68, s68, 7215616\n\tv_cvt_pk_bf16_f32 v168, v168, v169\n\ts_waitcnt lgkmcnt(8)\n\tv_mfma_f32_16x16x32_bf16 v[52:55], v[108:111], v[44:47], v[52:55]\n\tds_read_b64_tr_b16 v[32:33], v226 offset:20480\n\t" \
    "ds_read_b64_tr_b16 v[34:35], v226 offset:22528\n\tv_cvt_pk_bf16_f32 v169, v170, v171\n\tv_mfma_f32_16x16x32_bf16 v[60:63], v[112:115], v[44:47], v[60:63]\n\tv_cvt_pk_bf16_f32 v170, v172, v173\n\tv_cvt_pk_bf16_f32 v171, v174, v175\n\tv_mfma_f32_16x16x32_bf16 v[68:71], v[116:119], v[44:47], v[68:71]\n\ts_waitcnt lgkmcnt(8)\n\tv_mfma_f32_16x16x32_bf16 v[48:51], a[8:11], v[16:19], v[48:51]\n\t" \
    "ds_read_b64_tr_b16 v[36:37], v227 offset:20480\n\tds_read_b64_tr_b16 v[38:39], v227 offset:22528\n\tv_mfma_f32_16x16x32_bf16 v[56:59], a[12:15], v[16:19], v[56:59]\n\tbuffer_store_dwordx4 v[168:171], v8, s[8:11], s68 offen offset:0 sc1\n\tv_cvt_pk_bf16_f32 v172, v176, v177\n\tv_cvt_pk_bf16_f32 v173, v178, v179\n\tv_mfma_f32_16x16x32_bf16 v[64:67], a[16:19], v[16:19], v[64:67]\n\tv_cvt_pk_bf16_f32 v174, v180, v181\n\t" \
    "v_cvt_pk_bf16_f32 v175, v182, v183\n\ts_waitcnt lgkmcnt(8)\n\tv_mfma_f32_16x16x32_bf16 v[52:55], a[8:11], v[20:23], v[52:55]\n\tds_read_b64_tr_b16 v[40:41], v226 offset:32768\n\tds_read_b64_tr_b16 v[42:43], v226 offset:34816\n\tv_mfma_f32_16x16x32_bf16 v[60:63], a[12:15], v[20:23], v[60:63]\n\tbuffer_store_dwordx4 v[172:175], v8, s[8:11], s68 offen offset:1024 sc1\n\tv_cvt_pk_bf16_f32 v176, v184, v185\n\t" \
    "v_cvt_pk_bf16_f32 v177, v186, v187\n\tv_mfma_f32_16x16x32_bf16 v[68:71], a[16:19], v[20:23], v[68:71]\n\tv_cvt_pk_bf16_f32 v178, v188, v189\n\tv_cvt_pk_bf16_f32 v179, v190, v191\n\ts_waitcnt lgkmcnt(8)\n\tv_mfma_f32_16x16x32_bf16 v[48:51], a[20:23], v[24:27], v[48:51]\n\tds_read_b64_tr_b16 v[44:45], v227 offset:32768\n\tds_read_b64_tr_b16 v[46:47], v227 offset:34816\n\t" \
    "v_mfma_f32_16x16x32_bf16 v[56:59], a[24:27], v[24:27], v[56:59]\n\tbuffer_store_dwordx4 v[176:179], v8, s[8:11], s68 offen offset:2048 sc1\n\tv_add3_u32 v180, s36, v168, v169\n\tv_add3_u32 v181, v170, v171, v172\n\tv_mfma_f32_16x16x32_bf16 v[64:67], a[28:31], v[24:27], v[64:67]\n\tv_add3_u32 v180, v180, v173, v174\n\tv_add3_u32 v181, v181, v175, v176\n\ts_waitcnt lgkmcnt(8)\n\t" \
    "v_mfma_f32_16x16x32_bf16 v[52:55], a[20:23], v[28:31], v[52:55]\n\tds_read_b64_tr_b16 v[16:17], v226 offset:40960\n\tds_read_b64_tr_b16 v[18:19], v226 offset:43008\n\tv_add3_u32 v180, v180, v177, v178\n\tv_mfma_f32_16x16x32_bf16 v[60:63], a[24:27], v[28:31], v[60:63]\n\tv_add3_u32 v180, v180, v181, v179\n\tv_mfma_f32_16x16x32_bf16 v[68:71], a[28:31], v[28:31], v[68:71]\n\ts_waitcnt lgkmcnt(8)\n\t" \
    "v_mfma_f32_16x16x32_bf16 v[48:51], a[0:3], v[32:35], v[48:51]\n\tds_read_b64_tr_b16 v[20:21], v227 offset:40960\n\tds_read_b64_tr_b16 v[22:23], v227 offset:43008\n\tv_mfma_f32_16x16x32_bf16 v[56:59], a[4:7], v[32:35], v[56:59]\n\tbuffer_store_dword v180, v236, s[8:11], s68 offen offset:3072 sc1\n\tv_mfma_f32_16x16x32_bf16 v[64:67], a[32:35], v[32:35], v[64:67]\n\ts_waitcnt lgkmcnt(8)\n\tv_mfma_f32_16x16x32_bf16 v[52:55], a[0:3], v[36:39], v[52:55]\n\t" \
    "ds_read_b64_tr_b16 v[24:25], v226 offset:49152\n\tds_read_b64_tr_b16 v[26:27], v226 offset:51200\n\tv_mfma_f32_16x16x32_bf16 v[60:63], a[4:7], v[36:39], v[60:63]\n\tv_mfma_f32_16x16x32_bf16 v[68:71], a[32:35], v[36:39], v[68:71]\n\ts_waitcnt lgkmcnt(8)\n\tv_mfma_f32_16x16x32_bf16 v[48:51], a[36:39], v[40:43], v[48:51]\n\tds_read_b64_tr_b16 v[28:29], v227 offset:49152\n\tds_read_b64_tr_b16 v[30:31], v227 offset:51200\n\t" \
    "v_mfma_f32_16x16x32_bf16 v[56:59], a[40:43], v[40:43], v[56:59]\n\tv_permlane16_swap_b32 v192, v194\n\tv_permlane16_swap_b32 v193, v195\n\tv_mfma_f32_16x16x32_bf16 v[64:67], a[44:47], v[40:43], v[64:67]\n\tv_permlane16_swap_b32 v196, v198\n\tv_permlane16_swap_b32 v197, v199\n\ts_waitcnt lgkmcnt(8)\n\tv_mfma_f32_16x16x32_bf16 v[52:55], a[36:39], v[44:47], v[52:55]\n\t" \
    "ds_read_b64_tr_b16 v[32:33], v226 offset:57344\n\tds_read_b64_tr_b16 v[34:35], v226 offset:59392\n\tv_mfma_f32_16x16x32_bf16 v[60:63], a[40:43], v[44:47], v[60:63]\n\tv_permlane16_swap_b32 v200, v202\n\tv_permlane16_swap_b32 v201, v203\n\tv_mfma_f32_16x16x32_bf16 v[68:71], a[44:47], v[44:47], v[68:71]\n\tv_lshlrev_b32 v72, 16, v192\n\tv_and_b32 v73, 0xffff0000, v192\n\t" \
    "s_waitcnt lgkmcnt(8)\n\tv_mfma_f32_16x16x32_bf16 v[48:51], a[48:51], v[16:19], v[48:51]\n\tds_read_b64_tr_b16 v[36:37], v227 offset:57344\n\tds_read_b64_tr_b16 v[38:39], v227 offset:59392\n\tv_mfma_f32_16x16x32_bf16 v[56:59], a[52:55], v[16:19], v[56:59]\n\tv_lshlrev_b32 v74, 16, v193\n\tv_and_b32 v75, 0xffff0000, v193\n\tv_mfma_f32_16x16x32_bf16 v[64:67], a[56:59], v[16:19], v[64:67]\n\t" \
    "v_lshlrev_b32 v76, 16, v194\n\tv_and_b32 v77, 0xffff0000, v194\n\ts_waitcnt lgkmcnt(8)\n\tv_mfma_f32_16x16x32_bf16 v[52:55], a[48:51], v[20:23], v[52:55]\n\tds_read_b64_tr_b16 v[40:41], v222 offset:4096\n\tds_read_b64_tr_b16 v[42:43], v222 offset:6144\n\tv_mfma_f32_16x16x32_bf16 v[60:63], a[52:55], v[20:23], v[60:63]\n\tv_lshlrev_b32 v78, 16, v195\n\t" \
    "v_and_b32 v79, 0xffff0000, v195\n\tv_mfma_f32_16x16x32_bf16 v[68:71], a[56:59], v[20:23], v[68:71]\n\tv_lshlrev_b32 v80, 16, v196\n\tv_and_b32 v81, 0xffff0000, v196\n\ts_waitcnt lgkmcnt(8)\n\tv_mfma_f32_16x16x32_bf16 v[48:51], a[60:63], v[24:27], v[48:51]\n\tds_read_b64_tr_b16 v[44:45], v223 offset:4096\n\tds_read_b64_tr_b16 v[46:47], v223 offset:6144\n\t" \
    "v_mfma_f32_16x16x32_bf16 v[56:59], a[64:67], v[24:27], v[56:59]\n\tv_lshlrev_b32 v82, 16, v197\n\tv_and_b32 v83, 0xffff0000, v197\n\tv_mfma_f32_16x16x32_bf16 v[64:67], a[68:71], v[24:27], v[64:67]\n\tv_lshlrev_b32 v84, 16, v198\n\tv_and_b32 v85, 0xffff0000, v198\n\ts_waitcnt lgkmcnt(8)\n\tv_mfma_f32_16x16x32_bf16 v[52:55], a[60:63], v[28:31], v[52:55]\n\t" \
    "ds_read_b64_tr_b16 v[16:17], v222 offset:12288\n\tds_read_b64_tr_b16 v[18:19], v222 offset:14336\n\tv_mfma_f32_16x16x32_bf16 v[60:63], a[64:67], v[28:31], v[60:63]\n\tv_lshlrev_b32 v86, 16, v199\n\tv_and_b32 v87, 0xffff0000, v199\n\tv_mfma_f32_16x16x32_bf16 v[68:71], a[68:71], v[28:31], v[68:71]\n\tv_lshlrev_b32 v88, 16, v200\n\tv_and_b32 v89, 0xffff0000, v200\n\t" \
    "s_waitcnt lgkmcnt(8)\n\tv_mfma_f32_16x16x32_bf16 v[48:51], a[72:75], v[32:35], v[48:51]\n\tds_read_b64_tr_b16 v[20:21], v223 offset:12288\n\tds_read_b64_tr_b16 v[22:23], v223 offset:14336\n\tv_mfma_f32_16x16x32_bf16 v[56:59], a[76:79], v[32:35], v[56:59]\n\tv_lshlrev_b32 v90, 16, v201\n\tv_and_b32 v91, 0xffff0000, v201\n\tv_mfma_f32_16x16x32_bf16 v[64:67], a[80:83], v[32:35], v[64:67]\n\t" \
    "v_lshlrev_b32 v92, 16, v202\n\tv_and_b32 v93, 0xffff0000, v202\n\ts_waitcnt lgkmcnt(8)\n\tv_mfma_f32_16x16x32_bf16 v[52:55], a[72:75], v[36:39], v[52:55]\n\tds_read_b64_tr_b16 v[24:25], v222 offset:16384\n\tds_read_b64_tr_b16 v[26:27], v222 offset:18432\n\tv_mfma_f32_16x16x32_bf16 v[60:63], a[76:79], v[36:39], v[60:63]\n\tv_lshlrev_b32 v94, 16, v203\n\t" \
    "v_and_b32 v95, 0xffff0000, v203\n\tv_mfma_f32_16x16x32_bf16 v[68:71], a[80:83], v[36:39], v[68:71]\n\tv_fma_f32 v96, v72, v216, v217\n\tv_fma_f32 v97, v73, v216, v217\n\ts_waitcnt lgkmcnt(8)\n\tv_mfma_f32_16x16x32_bf16 v[48:51], a[84:87], v[40:43], v[48:51]\n\tds_read_b64_tr_b16 v[28:29], v223 offset:16384\n\tds_read_b64_tr_b16 v[30:31], v223 offset:18432\n\t" \
    "v_mfma_f32_16x16x32_bf16 v[56:59], a[88:91], v[40:43], v[56:59]\n\tv_fma_f32 v98, v74, v216, v217\n\tv_fma_f32 v99, v75, v216, v217\n\tv_mfma_f32_16x16x32_bf16 v[64:67], a[92:95], v[40:43], v[64:67]\n\tv_fma_f32 v100, v76, v218, v219\n\tv_fma_f32 v101, v77, v218, v219\n\ts_waitcnt lgkmcnt(8)\n\tv_mfma_f32_16x16x32_bf16 v[52:55], a[84:87], v[44:47], v[52:55]\n\t" \
    "ds_read_b64_tr_b16 v[32:33], v222 offset:24576\n\tds_read_b64_tr_b16 v[34:35], v222 offset:26624\n\tv_mfma_f32_16x16x32_bf16 v[60:63], a[88:91], v[44:47], v[60:63]\n\tv_fma_f32 v102, v78, v218, v219\n\tv_fma_f32 v103, v79, v218, v219\n\tv_mfma_f32_16x16x32_bf16 v[68:71], a[92:95], v[44:47], v[68:71]\n\tv_fma_f32 v104, v80, v216, v217\n\tv_fma_f32 v105, v81, v216, v217\n\t" \
    "s_waitcnt lgkmcnt(8)\n\tv_mfma_f32_16x16x32_bf16 v[48:51], a[96:99], v[16:19], v[48:51]\n\tds_read_b64_tr_b16 v[36:37], v223 offset:24576\n\tds_read_b64_tr_b16 v[38:39], v223 offset:26624\n\tv_mfma_f32_16x16x32_bf16 v[56:59], a[100:103], v[16:19], v[56:59]\n\tv_fma_f32 v106, v82, v216, v217\n\tv_fma_f32 v107, v83, v216, v217\n\tv_mfma_f32_16x16x32_bf16 v[64:67], a[104:107], v[16:19], v[64:67]\n\t" \
    "v_fma_f32 v108, v84, v218, v219\n\tv_fma_f32 v109, v85, v218, v219\n\ts_waitcnt lgkmcnt(8)\n\tv_mfma_f32_16x16x32_bf16 v[52:55], a[96:99], v[20:23], v[52:55]\n\tds_read_b64_tr_b16 v[40:41], v6 offset:4096\n\tds_read_b64_tr_b16 v[42:43], v6 offset:6144\n\tv_mfma_f32_16x16x32_bf16 v[60:63], a[100:103], v[20:23], v[60:63]\n\tv_fma_f32 v110, v86, v218, v219\n\t" \
    "v_fma_f32 v111, v87, v218, v219\n\tv_mfma_f32_16x16x32_bf16 v[68:71], a[104:107], v[20:23], v[68:71]\n\tv_fma_f32 v112, v88, v216, v217\n\tv_fma_f32 v113, v89, v216, v217\n\ts_waitcnt lgkmcnt(8)\n\tv_mfma_f32_16x16x32_bf16 v[48:51], a[108:111], v[24:27], v[48:51]\n\tds_read_b64_tr_b16 v[44:45], v7 offset:4096\n\tds_read_b64_tr_b16 v[46:47], v7 offset:6144\n\t" \
    "v_mfma_f32_16x16x32_bf16 v[56:59], a[112:115], v[24:27], v[56:59]\n\tv_fma_f32 v114, v90, v216, v217\n\tv_fma_f32 v115, v91, v216, v217\n\tv_mfma_f32_16x16x32_bf16 v[64:67], a[116:119], v[24:27], v[64:67]\n\tv_fma_f32 v116, v92, v218, v219\n\tv_fma_f32 v117, v93, v218, v219\n\ts_waitcnt lgkmcnt(8)\n\tv_mfma_f32_16x16x32_bf16 v[52:55], a[108:111], v[28:31], v[52:55]\n\t" \
    "ds_read_b64_tr_b16 v[16:17], v6 offset:12288\n\tds_read_b64_tr_b16 v[18:19], v6 offset:14336\n\tv_mfma_f32_16x16x32_bf16 v[60:63], a[112:115], v[28:31], v[60:63]\n\tv_fma_f32 v118, v94, v218, v219\n\tv_fma_f32 v119, v95, v218, v219\n\tv_mfma_f32_16x16x32_bf16 v[68:71], a[116:119], v[28:31], v[68:71]\n\ts_waitcnt lgkmcnt(8)\n\tv_mfma_f32_16x16x32_bf16 v[48:51], a[120:123], v[32:35], v[48:51]\n\t" \
    "ds_read_b64_tr_b16 v[20:21], v7 offset:12288\n\tds_read_b64_tr_b16 v[22:23], v7 offset:14336\n\tv_mfma_f32_16x16x32_bf16 v[56:59], a[124:127], v[32:35], v[56:59]\n\tv_mfma_f32_16x16x32_bf16 v[64:67], a[128:131], v[32:35], v[64:67]\n\ts_waitcnt lgkmcnt(8)\n\tv_mfma_f32_16x16x32_bf16 v[52:55], a[120:123], v[36:39], v[52:55]\n\tds_read_b64_tr_b16 v[24:25], v6 offset:20480\n\tds_read_b64_tr_b16 v[26:27], v6 offset:22528\n\t" \
    "v_mfma_f32_16x16x32_bf16 v[60:63], a[124:127], v[36:39], v[60:63]\n\tv_mfma_f32_16x16x32_bf16 v[68:71], a[128:131], v[36:39], v[68:71]\n\ts_waitcnt lgkmcnt(8)\n\tv_mfma_f32_16x16x32_bf16 v[48:51], a[132:135], v[40:43], v[48:51]\n\tds_read_b64_tr_b16 v[28:29], v7 offset:20480\n\tds_read_b64_tr_b16 v[30:31], v7 offset:22528\n\tv_mfma_f32_16x16x32_bf16 v[56:59], a[136:139], v[40:43], v[56:59]\n\tv_mfma_f32_16x16x32_bf16 v[64:67], a[140:143], v[40:43], v[64:67]\n\t" \
    "s_waitcnt lgkmcnt(8)\n\tv_mfma_f32_16x16x32_bf16 v[52:55], a[132:135], v[44:47], v[52:55]\n\tds_read_b64_tr_b16 v[32:33], v222 offset:0\n\tds_read_b64_tr_b16 v[34:35], v222 offset:2048\n\tv_mfma_f32_16x16x32_bf16 v[60:63], a[136:139], v[44:47], v[60:63]\n\tv_mfma_f32_16x16x32_bf16 v[68:71], a[140:143], v[44:47], v[68:71]\n\ts_waitcnt lgkmcnt(8)\n\tv_mfma_f32_16x16x32_bf16 v[48:51], a[144:147], v[16:19], v[48:51]\n\t" \
    "ds_read_b64_tr_b16 v[36:37], v223 offset:0\n\tds_read_b64_tr_b16 v[38:39], v223 offset:2048\n\tv_mfma_f32_16x16x32_bf16 v[56:59], a[148:151], v[16:19], v[56:59]\n\tv_mfma_f32_16x16x32_bf16 v[64:67], a[152:155], v[16:19], v[64:67]\n\ts_waitcnt lgkmcnt(8)\n\tv_mfma_f32_16x16x32_bf16 v[52:55], a[144:147], v[20:23], v[52:55]\n\tds_read_b64_tr_b16 v[40:41], v222 offset:8192\n\tds_read_b64_tr_b16 v[42:43], v222 offset:10240\n\t" \
    "v_mfma_f32_16x16x32_bf16 v[60:63], a[148:151], v[20:23], v[60:63]\n\tv_mfma_f32_16x16x32_bf16 v[68:71], a[152:155], v[20:23], v[68:71]\n\ts_waitcnt lgkmcnt(8)\n\tv_mfma_f32_16x16x32_bf16 v[48:51], a[156:159], v[24:27], v[48:51]\n\tds_read_b64_tr_b16 v[44:45], v223 offset:8192\n\tds_read_b64_tr_b16 v[46:47], v223 offset:10240\n\ts_add_u32 s68, s55, 0\n\tv_mfma_f32_16x16x32_bf16 v[56:59], a[160:163], v[24:27], v[56:59]\n\t" \
    "buffer_load_dwordx4 v[120:123], v8, s[8:11], s68 offen offset:0 sc1\n\tv_mfma_f32_16x16x32_bf16 v[64:67], a[164:167], v[24:27], v[64:67]\n\ts_waitcnt lgkmcnt(8)\n\tv_mfma_f32_16x16x32_bf16 v[52:55], a[156:159], v[28:31], v[52:55]\n\tds_read_b64_tr_b16 v[16:17], v222 offset:20480\n\tds_read_b64_tr_b16 v[18:19], v222 offset:22528\n\tv_mfma_f32_16x16x32_bf16 v[60:63], a[160:163], v[28:31], v[60:63]\n\tbuffer_load_dwordx4 v[124:127], v8, s[8:11], s68 offen offset:1024 sc1\n\t" \
    "v_mfma_f32_16x16x32_bf16 v[68:71], a[164:167], v[28:31], v[68:71]\n\ts_waitcnt lgkmcnt(8)\n\tv_mfma_f32_16x16x32_bf16 v[48:51], a[168:171], v[32:35], v[48:51]\n\tds_read_b64_tr_b16 v[20:21], v223 offset:20480\n\tds_read_b64_tr_b16 v[22:23], v223 offset:22528\n\tv_mfma_f32_16x16x32_bf16 v[56:59], a[172:175], v[32:35], v[56:59]\n\tbuffer_load_dwordx4 v[128:131], v8, s[8:11], s68 offen offset:2048 sc1\n\tv_mfma_f32_16x16x32_bf16 v[64:67], a[176:179], v[32:35], v[64:67]\n\t" \
    "s_waitcnt lgkmcnt(8)\n\tv_mfma_f32_16x16x32_bf16 v[52:55], a[168:171], v[36:39], v[52:55]\n\tds_read_b64_tr_b16 v[24:25], v6 offset:45056\n\tds_read_b64_tr_b16 v[26:27], v6 offset:47104\n\tv_mfma_f32_16x16x32_bf16 v[60:63], a[172:175], v[36:39], v[60:63]\n\tbuffer_load_dword v132, v236, s[8:11], s68 offen offset:3072 sc1\n\tv_mfma_f32_16x16x32_bf16 v[68:71], a[176:179], v[36:39], v[68:71]\n\ts_waitcnt lgkmcnt(8)\n\t" \
    "v_mfma_f32_16x16x32_bf16 v[48:51], a[180:183], v[40:43], v[48:51]\n\tds_read_b64_tr_b16 v[28:29], v7 offset:45056\n\tds_read_b64_tr_b16 v[30:31], v7 offset:47104\n\tv_mfma_f32_16x16x32_bf16 v[56:59], a[184:187], v[40:43], v[56:59]\n\tv_mfma_f32_16x16x32_bf16 v[64:67], a[188:191], v[40:43], v[64:67]\n\ts_waitcnt lgkmcnt(8)\n\tv_mfma_f32_16x16x32_bf16 v[52:55], a[180:183], v[44:47], v[52:55]\n\tds_read_b64_tr_b16 v[32:33], v6 offset:49152\n\t" \
    "ds_read_b64_tr_b16 v[34:35], v6 offset:51200\n\ts_add_u32 s68, s55, 3328\n\tv_mfma_f32_16x16x32_bf16 v[60:63], a[184:187], v[44:47], v[60:63]\n\tbuffer_load_dwordx4 v[144:147], v8, s[8:11], s68 offen offset:0 sc1\n\tv_mfma_f32_16x16x32_bf16 v[68:71], a[188:191], v[44:47], v[68:71]\n\ts_waitcnt lgkmcnt(8)\n\tv_mfma_f32_16x16x32_bf16 v[48:51], a[192:195], v[16:19], v[48:51]\n\tds_read_b64_tr_b16 v[36:37], v7 offset:49152\n\t" \
    "ds_read_b64_tr_b16 v[38:39], v7 offset:51200\n\tv_mfma_f32_16x16x32_bf16 v[56:59], a[196:199], v[16:19], v[56:59]\n\tbuffer_load_dwordx4 v[148:151], v8, s[8:11], s68 offen offset:1024 sc1\n\tv_mfma_f32_16x16x32_bf16 v[64:67], a[200:203], v[16:19], v[64:67]\n\ts_waitcnt lgkmcnt(8)\n\tv_mfma_f32_16x16x32_bf16 v[52:55], a[192:195], v[20:23], v[52:55]\n\tds_read_b64_tr_b16 v[40:41], v6 offset:53248\n\tds_read_b64_tr_b16 v[42:43], v6 offset:55296\n\t" \
    "v_mfma_f32_16x16x32_bf16 v[60:63], a[196:199], v[20:23], v[60:63]\n\tbuffer_load_dwordx4 v[152:155], v8, s[8:11], s68 offen offset:2048 sc1\n\tv_mfma_f32_16x16x32_bf16 v[68:71], a[200:203], v[20:23], v[68:71]\n\ts_waitcnt lgkmcnt(8)\n\tv_mfma_f32_16x16x32_bf16 v[48:51], a[204:207], v[24:27], v[48:51]\n\tds_read_b64_tr_b16 v[44:45], v7 offset:53248\n\tds_read_b64_tr_b16 v[46:47], v7 offset:55296\n\tv_mfma_f32_16x16x32_bf16 v[56:59], a[208:211], v[24:27], v[56:59]\n\t" \
    "buffer_load_dword v156, v236, s[8:11], s68 offen offset:3072 sc1\n\tv_mfma_f32_16x16x32_bf16 v[64:67], a[212:215], v[24:27], v[64:67]\n\ts_waitcnt lgkmcnt(8)\n\tv_mfma_f32_16x16x32_bf16 v[52:55], a[204:207], v[28:31], v[52:55]\n\tv_mfma_f32_16x16x32_bf16 v[60:63], a[208:211], v[28:31], v[60:63]\n\tv_mfma_f32_16x16x32_bf16 v[68:71], a[212:215], v[28:31], v[68:71]\n\ts_waitcnt lgkmcnt(6)\n\tv_mfma_f32_16x16x32_bf16 v[48:51], a[216:219], v[32:35], v[48:51]\n\t" \
    "s_add_u32 s68, s55, 6656\n\tv_mfma_f32_16x16x32_bf16 v[56:59], a[220:223], v[32:35], v[56:59]\n\tbuffer_load_dwordx4 v[168:171], v8, s[8:11], s68 offen offset:0 sc1\n\tv_mfma_f32_16x16x32_bf16 v[64:67], a[224:227], v[32:35], v[64:67]\n\ts_waitcnt lgkmcnt(4)\n\tv_mfma_f32_16x16x32_bf16 v[52:55], a[216:219], v[36:39], v[52:55]\n\tv_mfma_f32_16x16x32_bf16 v[60:63], a[220:223], v[36:39], v[60:63]\n\tbuffer_load_dwordx4 v[172:175], v8, s[8:11], s68 offen offset:1024 sc1\n\t" \
    "v_mfma_f32_16x16x32_bf16 v[68:71], a[224:227], v[36:39], v[68:71]\n\ts_waitcnt lgkmcnt(2)\n\tv_mfma_f32_16x16x32_bf16 v[48:51], a[228:231], v[40:43], v[48:51]\n\tv_mfma_f32_16x16x32_bf16 v[56:59], a[232:235], v[40:43], v[56:59]\n\tbuffer_load_dwordx4 v[176:179], v8, s[8:11], s68 offen offset:2048 sc1\n\tv_mfma_f32_16x16x32_bf16 v[64:67], a[236:239], v[40:43], v[64:67]\n\ts_waitcnt lgkmcnt(0)\n\tv_mfma_f32_16x16x32_bf16 v[52:55], a[228:231], v[44:47], v[52:55]\n\t" \
    "v_mfma_f32_16x16x32_bf16 v[60:63], a[232:235], v[44:47], v[60:63]\n\tbuffer_load_dword v180, v236, s[8:11], s68 offen offset:3072 sc1\n\tv_mfma_f32_16x16x32_bf16 v[68:71], a[236:239], v[44:47], v[68:71]\n\ts_cmp_eq_u32 s70, 0\n\ts_cbranch_scc1 115f\n\tv_cmp_ne_u64 vcc, s[36:37], v[250:251]\n\ts_cbranch_vccz 117f\n\ts_mov_b32 s71, 1048576\n\t" \
    "116:\n\ts_sleep 1\n\tbuffer_load_dwordx2 v[250:251], v241, s[8:11], s51 offen sc1\n\ts_waitcnt vmcnt(0)\n\tv_cmp_ne_u64 vcc, s[36:37], v[250:251]\n\ts_cbranch_vccz 117f\n\ts_sub_u32 s71, s71, 1\n\ts_cbranch_scc0 116b\n\t" \
    "117:\n\tbuffer_load_dwordx4 v[204:207], v8, s[8:11], s56 offen offset:0 sc1\n\tbuffer_load_dwordx4 v[208:211], v8, s[8:11], s56 offen offset:1024 sc1\n\tbuffer_load_dwordx4 v[212:215], v8, s[8:11], s56 offen offset:2048 sc1\n\ts_waitcnt vmcnt(0)\n\t115:\n\ts_waitcnt vmcnt(16)\n\tv_lshlrev_b32 v16, 16, v204\n\t" \
    "v_and_b32 v17, 0xffff0000, v204\n\tv_lshlrev_b32 v18, 16, v205\n\tv_and_b32 v19, 0xffff0000, v205\n\tv_lshlrev_b32 v20, 16, v206\n\tv_and_b32 v21, 0xffff0000, v206\n\tv_lshlrev_b32 v22, 16, v207\n\tv_and_b32 v23, 0xffff0000, v207\n\tv_lshlrev_b32 v24, 16, v208\n\t" \
    "v_and_b32 v25, 0xffff0000, v208\n\tv_lshlrev_b32 v26, 16, v209\n\tv_and_b32 v27, 0xffff0000, v209\n\tv_lshlrev_b32 v28, 16, v210\n\tv_and_b32 v29, 0xffff0000, v210\n\tv_lshlrev_b32 v30, 16, v211\n\tv_and_b32 v31, 0xffff0000, v211\n\tv_lshlrev_b32 v32, 16, v212\n\t" \
    "v_and_b32 v33, 0xffff0000, v212\n\tv_lshlrev_b32 v34, 16, v213\n\tv_and_b32 v35, 0xffff0000, v213\n\tv_lshlrev_b32 v36, 16, v214\n\tv_and_b32 v37, 0xffff0000, v214\n\tv_lshlrev_b32 v38, 16, v215\n\tv_and_b32 v39, 0xffff0000, v215\n\tv_pk_mul_f32 v[96:97], v[96:97], v[16:17]\n\t" \
    "v_pk_mul_f32 v[98:99], v[98:99], v[18:19]\n\tv_pk_mul_f32 v[100:101], v[100:101], v[20:21]\n\tv_pk_mul_f32 v[102:103], v[102:103], v[22:23]\n\tv_pk_mul_f32 v[104:105], v[104:105], v[24:25]\n\tv_pk_mul_f32 v[106:107], v[106:107], v[26:27]\n\tv_pk_mul_f32 v[108:109], v[108:109], v[28:29]\n\tv_pk_mul_f32 v[110:111], v[110:111], v[30:31]\n\tv_pk_mul_f32 v[112:113], v[112:113], v[32:33]\n\t" \
    "v_pk_mul_f32 v[114:115], v[114:115], v[34:35]\n\tv_pk_mul_f32 v[116:117], v[116:117], v[36:37]\n\tv_pk_mul_f32 v[118:119], v[118:119], v[38:39]\n\ts_mov_b32 s71, 1048576\n\ts_waitcnt vmcnt(8)\n\t118:\n\tv_add3_u32 v134, s36, v120, v121\n\tv_add3_u32 v135, v122, v123, v124\n\t" \
    "v_add3_u32 v134, v134, v125, v126\n\tv_add3_u32 v135, v135, v127, v128\n\tv_add3_u32 v134, v134, v129, v130\n\tv_add3_u32 v134, v134, v135, v131\n\tv_cmp_ne_u32 vcc, v134, v132\n\ts_cbranch_vccz 119f\n\ts_sub_u32 s71, s71, 1\n\ts_cbranch_scc1 119f\n\t" \
    "s_sleep 6\n\ts_add_u32 s73, s55, 0\n\tbuffer_load_dwordx4 v[120:123], v8, s[8:11], s73 offen offset:0 sc1\n\tbuffer_load_dwordx4 v[124:127], v8, s[8:11], s73 offen offset:1024 sc1\n\tbuffer_load_dwordx4 v[128:131], v8, s[8:11], s73 offen offset:2048 sc1\n\tbuffer_load_dword v132, v236, s[8:11], s73 offen offset:3072 sc1\n\ts_waitcnt vmcnt(0)\n\ts_branch 118b\n\t" \
    "119:\n\tv_lshlrev_b32 v16, 16, v120\n\tv_and_b32 v17, 0xffff0000, v120\n\tv_lshlrev_b32 v18, 16, v121\n\tv_and_b32 v19, 0xffff0000, v121\n\tv_lshlrev_b32 v20, 16, v122\n\tv_and_b32 v21, 0xffff0000, v122\n\tv_lshlrev_b32 v22, 16, v123\n\t" \
    "v_and_b32 v23, 0xffff0000, v123\n\tv_lshlrev_b32 v24, 16, v124\n\tv_and_b32 v25, 0xffff0000, v124\n\tv_lshlrev_b32 v26, 16, v125\n\tv_and_b32 v27, 0xffff0000, v125\n\tv_lshlrev_b32 v28, 16, v126\n\tv_and_b32 v29, 0xffff0000, v126\n\tv_lshlrev_b32 v30, 16, v127\n\t" \
    "v_and_b32 v31, 0xffff0000, v127\n\tv_lshlrev_b32 v32, 16, v128\n\tv_and_b32 v33, 0xffff0000, v128\n\tv_lshlrev_b32 v34, 16, v129\n\tv_and_b32 v35, 0xffff0000, v129\n\tv_lshlrev_b32 v36, 16, v130\n\tv_and_b32 v37, 0xffff0000, v130\n\tv_lshlrev_b32 v38, 16, v131\n\t" \
    "v_and_b32 v39, 0xffff0000, v131\n\tv_pk_add_f32 v[48:49], v[48:49], v[16:17]\n\tv_pk_add_f32 v[50:51], v[50:51], v[18:19]\n\tv_pk_add_f32 v[52:53], v[52:53], v[20:21]\n\tv_pk_add_f32 v[54:55], v[54:55], v[22:23]\n\tv_pk_add_f32 v[56:57], v[56:57], v[24:25]\n\tv_pk_add_f32 v[58:59], v[58:59], v[26:27]\n\tv_pk_add_f32 v[60:61], v[60:61], v[28:29]\n\t" \
    "v_pk_add_f32 v[62:63], v[62:63], v[30:31]\n\tv_pk_add_f32 v[64:65], v[64:65], v[32:33]\n\tv_pk_add_f32 v[66:67], v[66:67], v[34:35]\n\tv_pk_add_f32 v[68:69], v[68:69], v[36:37]\n\tv_pk_add_f32 v[70:71], v[70:71], v[38:39]\n\ts_mov_b32 s71, 1048576\n\ts_waitcnt vmcnt(4)\n\t120:\n\t" \
    "v_add3_u32 v158, s36, v144, v145\n\tv_add3_u32 v159, v146, v147, v148\n\tv_add3_u32 v158, v158, v149, v150\n\tv_add3_u32 v159, v159, v151, v152\n\tv_add3_u32 v158, v158, v153, v154\n\tv_add3_u32 v158, v158, v159, v155\n\tv_cmp_ne_u32 vcc, v158, v156\n\ts_cbranch_vccz 121f\n\t" \
    "s_sub_u32 s71, s71, 1\n\ts_cbranch_scc1 121f\n\ts_sleep 6\n\ts_add_u32 s73, s55, 3328\n\tbuffer_load_dwordx4 v[144:147], v8, s[8:11], s73 offen offset:0 sc1\n\tbuffer_load_dwordx4 v[148:151], v8, s[8:11], s73 offen offset:1024 sc1\n\tbuffer_load_dwordx4 v[152:155], v8, s[8:11], s73 offen offset:2048 sc1\n\tbuffer_load_dword v156, v236, s[8:11], s73 offen offset:3072 sc1\n\t" \
    "s_waitcnt vmcnt(0)\n\ts_branch 120b\n\t121:\n\tv_lshlrev_b32 v16, 16, v144\n\tv_and_b32 v17, 0xffff0000, v144\n\tv_lshlrev_b32 v18, 16, v145\n\tv_and_b32 v19, 0xffff0000, v145\n\tv_lshlrev_b32 v20, 16, v146\n\t" \
    "v_and_b32 v21, 0xffff0000, v146\n\tv_lshlrev_b32 v22, 16, v147\n\tv_and_b32 v23, 0xffff0000, v147\n\tv_lshlrev_b32 v24, 16, v148\n\tv_and_b32 v25, 0xffff0000, v148\n\tv_lshlrev_b32 v26, 16, v149\n\tv_and_b32 v27, 0xffff0000, v149\n\tv_lshlrev_b32 v28, 16, v150\n\t" \
    "v_and_b32 v29, 0xffff0000, v150\n\tv_lshlrev_b32 v30, 16, v151\n\tv_and_b32 v31, 0xffff0000, v151\n\tv_lshlrev_b32 v32, 16, v152\n\tv_and_b32 v33, 0xffff0000, v152\n\tv_lshlrev_b32 v34, 16, v153\n\tv_and_b32 v35, 0xffff0000, v153\n\tv_lshlrev_b32 v36, 16, v154\n\t" \
    "v_and_b32 v37, 0xffff0000, v154\n\tv_lshlrev_b32 v38, 16, v155\n\tv_and_b32 v39, 0xffff0000, v155\n\tv_pk_add_f32 v[48:49], v[48:49], v[16:17]\n\tv_pk_add_f32 v[50:51], v[50:51], v[18:19]\n\tv_pk_add_f32 v[52:53], v[52:53], v[20:21]\n\tv_pk_add_f32 v[54:55], v[54:55], v[22:23]\n\tv_pk_add_f32 v[56:57], v[56:57], v[24:25]\n\t" \
    "v_pk_add_f32 v[58:59], v[58:59], v[26:27]\n\tv_pk_add_f32 v[60:61], v[60:61], v[28:29]\n\tv_pk_add_f32 v[62:63], v[62:63], v[30:31]\n\tv_pk_add_f32 v[64:65], v[64:65], v[32:33]\n\tv_pk_add_f32 v[66:67], v[66:67], v[34:35]\n\tv_pk_add_f32 v[68:69], v[68:69], v[36:37]\n\tv_pk_add_f32 v[70:71], v[70:71], v[38:39]\n\ts_mov_b32 s71, 1048576\n\t" \
    "s_waitcnt vmcnt(0)\n\t122:\n\tv_add3_u32 v182, s36, v168, v169\n\tv_add3_u32 v183, v170, v171, v172\n\tv_add3_u32 v182, v182, v173, v174\n\tv_add3_u32 v183, v183, v175, v176\n\tv_add3_u32 v182, v182, v177, v178\n\tv_add3_u32 v182, v182, v183, v179\n\t" \
    "v_cmp_ne_u32 vcc, v182, v180\n\ts_cbranch_vccz 123f\n\ts_sub_u32 s71, s71, 1\n\ts_cbranch_scc1 123f\n\ts_sleep 6\n\ts_add_u32 s73, s55, 6656\n\tbuffer_load_dwordx4 v[168:171], v8, s[8:11], s73 offen offset:0 sc1\n\tbuffer_load_dwordx4 v[172:175], v8, s[8:11], s73 offen offset:1024 sc1\n\t" \
    "buffer_load_dwordx4 v[176:179], v8, s[8:11], s73 offen offset:2048 sc1\n\tbuffer_load_dword v180, v236, s[8:11], s73 offen offset:3072 sc1\n\ts_waitcnt vmcnt(0)\n\ts_branch 122b\n\t123:\n\ts_mov_b32 s68, 0xbfb8aa3b\n\ts_mov_b32 s69, 0xbfb8aa3b\n\tv_lshlrev_b32 v16, 16, v168\n\t" \
    "v_and_b32 v17, 0xffff0000, v168\n\tv_lshlrev_b32 v18, 16, v169\n\tv_and_b32 v19, 0xffff0000, v169\n\tv_lshlrev_b32 v20, 16, v170\n\tv_and_b32 v21, 0xffff0000, v170\n\tv_lshlrev_b32 v22, 16, v171\n\tv_and_b32 v23, 0xffff0000, v171\n\tv_pk_add_f32 v[48:49], v[48:49], v[16:17]\n\t" \
    "v_pk_add_f32 v[50:51], v[50:51], v[18:19]\n\tv_pk_add_f32 v[52:53], v[52:53], v[20:21]\n\tv_pk_add_f32 v[54:55], v[54:55], v[22:23]\n\tv_pk_mul_f32 v[16:17], v[48:49], s[68:69]\n\tv_pk_mul_f32 v[18:19], v[50:51], s[68:69]\n\tv_pk_mul_f32 v[20:21], v[52:53], s[68:69]\n\tv_pk_mul_f32 v[22:23], v[54:55], s[68:69]\n\tv_lshlrev_b32 v24, 16, v172\n\t" \
    "v_exp_f32 v16, v16\n\tv_and_b32 v25, 0xffff0000, v172\n\tv_exp_f32 v17, v17\n\tv_lshlrev_b32 v26, 16, v173\n\tv_exp_f32 v18, v18\n\tv_and_b32 v27, 0xffff0000, v173\n\tv_exp_f32 v19, v19\n\tv_lshlrev_b32 v28, 16, v174\n\t" \
    "v_exp_f32 v20, v20\n\tv_and_b32 v29, 0xffff0000, v174\n\tv_exp_f32 v21, v21\n\tv_lshlrev_b32 v30, 16, v175\n\tv_exp_f32 v22, v22\n\tv_and_b32 v31, 0xffff0000, v175\n\tv_exp_f32 v23, v23\n\tv_pk_add_f32 v[56:57], v[56:57], v[24:25]\n\t" \
    "v_pk_add_f32 v[16:17], v[16:17], 1.0 op_sel_hi:[1,0]\n\tv_pk_add_f32 v[18:19], v[18:19], 1.0 op_sel_hi:[1,0]\n\tv_pk_add_f32 v[20:21], v[20:21], 1.0 op_sel_hi:[1,0]\n\tv_pk_add_f32 v[22:23], v[22:23], 1.0 op_sel_hi:[1,0]\n\tv_pk_add_f32 v[58:59], v[58:59], v[26:27]\n\tv_rcp_f32 v16, v16\n\tv_pk_add_f32 v[60:61], v[60:61], v[28:29]\n\tv_rcp_f32 v17, v17\n\t" \
    "v_pk_add_f32 v[62:63], v[62:63], v[30:31]\n\tv_rcp_f32 v18, v18\n\tv_pk_mul_f32 v[24:25], v[56:57], s[68:69]\n\tv_rcp_f32 v19, v19\n\tv_pk_mul_f32 v[26:27], v[58:59], s[68:69]\n\tv_rcp_f32 v20, v20\n\tv_pk_mul_f32 v[28:29], v[60:61], s[68:69]\n\tv_rcp_f32 v21, v21\n\t" \
    "v_pk_mul_f32 v[30:31], v[62:63], s[68:69]\n\tv_rcp_f32 v22, v22\n\tv_lshlrev_b32 v32, 16, v176\n\tv_rcp_f32 v23, v23\n\tv_and_b32 v33, 0xffff0000, v176\n\tv_exp_f32 v24, v24\n\tv_pk_mul_f32 v[48:49], v[48:49], v[96:97]\n\tv_exp_f32 v25, v25\n\t" \
    "v_pk_mul_f32 v[50:51], v[50:51], v[98:99]\n\tv_exp_f32 v26, v26\n\tv_pk_mul_f32 v[52:53], v[52:53], v[100:101]\n\tv_exp_f32 v27, v27\n\tv_pk_mul_f32 v[54:55], v[54:55], v[102:103]\n\tv_exp_f32 v28, v28\n\tv_pk_fma_f32 v[16:17], v[16:17], v[48:49], v[72:73]\n\tv_exp_f32 v29, v29\n\t" \
    "v_pk_fma_f32 v[18:19], v[18:19], v[50:51], v[74:75]\n\tv_exp_f32 v30, v30\n\tv_pk_fma_f32 v[20:21], v[20:21], v[52:53], v[76:77]\n\tv_exp_f32 v31, v31\n\tv_pk_fma_f32 v[22:23], v[22:23], v[54:55], v[78:79]\n\tv_cvt_pk_bf16_f32 v48, v16, v17\n\tv_cvt_pk_bf16_f32 v49, v18, v19\n\tv_cvt_pk_bf16_f32 v50, v20, v21\n\t" \
    "v_cvt_pk_bf16_f32 v51, v22, v23\n\ts_nop 0\n\tv_permlane16_swap_b32 v48, v50\n\tv_permlane16_swap_b32 v49, v51\n\tbuffer_store_dwordx4 v[48:51], v243, s[24:27], 0 offen offset:0\n\tv_pk_add_f32 v[24:25], v[24:25], 1.0 op_sel_hi:[1,0]\n\tv_pk_add_f32 v[26:27], v[26:27], 1.0 op_sel_hi:[1,0]\n\tv_pk_add_f32 v[28:29], v[28:29], 1.0 op_sel_hi:[1,0]\n\t" \
    "v_pk_add_f32 v[30:31], v[30:31], 1.0 op_sel_hi:[1,0]\n\tv_lshlrev_b32 v34, 16, v177\n\tv_rcp_f32 v24, v24\n\tv_and_b32 v35, 0xffff0000, v177\n\tv_rcp_f32 v25, v25\n\tv_lshlrev_b32 v36, 16, v178\n\tv_rcp_f32 v26, v26\n\tv_and_b32 v37, 0xffff0000, v178\n\t" \
    "v_rcp_f32 v27, v27\n\tv_lshlrev_b32 v38, 16, v179\n\tv_rcp_f32 v28, v28\n\tv_and_b32 v39, 0xffff0000, v179\n\tv_rcp_f32 v29, v29\n\tv_pk_add_f32 v[64:65], v[64:65], v[32:33]\n\tv_rcp_f32 v30, v30\n\tv_pk_add_f32 v[66:67], v[66:67], v[34:35]\n\t" \
    "v_rcp_f32 v31, v31\n\tv_pk_add_f32 v[68:69], v[68:69], v[36:37]\n\tv_pk_mul_f32 v[56:57], v[56:57], v[104:105]\n\tv_pk_mul_f32 v[58:59], v[58:59], v[106:107]\n\tv_pk_mul_f32 v[60:61], v[60:61], v[108:109]\n\tv_pk_mul_f32 v[62:63], v[62:63], v[110:111]\n\tv_pk_fma_f32 v[24:25], v[24:25], v[56:57], v[80:81]\n\tv_pk_fma_f32 v[26:27], v[26:27], v[58:59], v[82:83]\n\t" \
    "v_pk_fma_f32 v[28:29], v[28:29], v[60:61], v[84:85]\n\tv_pk_fma_f32 v[30:31], v[30:31], v[62:63], v[86:87]\n\tv_cvt_pk_bf16_f32 v56, v24, v25\n\tv_cvt_pk_bf16_f32 v57, v26, v27\n\tv_cvt_pk_bf16_f32 v58, v28, v29\n\tv_cvt_pk_bf16_f32 v59, v30, v31\n\ts_nop 0\n\tv_permlane16_swap_b32 v56, v58\n\t" \
    "v_permlane16_swap_b32 v57, v59\n\tbuffer_store_dwordx4 v[56:59], v243, s[24:27], 0 offen offset:32\n\tv_pk_add_f32 v[70:71], v[70:71], v[38:39]\n\tv_pk_mul_f32 v[32:33], v[64:65], s[68:69]\n\tv_pk_mul_f32 v[34:35], v[66:67], s[68:69]\n\tv_pk_mul_f32 v[36:37], v[68:69], s[68:69]\n\tv_pk_mul_f32 v[38:39], v[70:71], s[68:69]\n\tv_exp_f32 v32, v32\n\t" \
    "v_exp_f32 v33, v33\n\tv_exp_f32 v34, v34\n\tv_exp_f32 v35, v35\n\tv_exp_f32 v36, v36\n\tv_exp_f32 v37, v37\n\tv_exp_f32 v38, v38\n\tv_exp_f32 v39, v39\n\tv_pk_add_f32 v[32:33], v[32:33], 1.0 op_sel_hi:[1,0]\n\t" \
    "v_pk_add_f32 v[34:35], v[34:35], 1.0 op_sel_hi:[1,0]\n\tv_pk_add_f32 v[36:37], v[36:37], 1.0 op_sel_hi:[1,0]\n\tv_pk_add_f32 v[38:39], v[38:39], 1.0 op_sel_hi:[1,0]\n\tv_rcp_f32 v32, v32\n\tv_rcp_f32 v33, v33\n\tv_rcp_f32 v34, v34\n\tv_rcp_f32 v35, v35\n\tv_rcp_f32 v36, v36\n\t" \
    "v_rcp_f32 v37, v37\n\tv_rcp_f32 v38, v38\n\tv_rcp_f32 v39, v39\n\tv_pk_mul_f32 v[64:65], v[64:65], v[112:113]\n\tv_pk_mul_f32 v[66:67], v[66:67], v[114:115]\n\tv_pk_mul_f32 v[68:69], v[68:69], v[116:117]\n\tv_pk_mul_f32 v[70:71], v[70:71], v[118:119]\n\tv_pk_fma_f32 v[32:33], v[32:33], v[64:65], v[88:89]\n\t" \
    "v_pk_fma_f32 v[34:35], v[34:35], v[66:67], v[90:91]\n\tv_pk_fma_f32 v[36:37], v[36:37], v[68:69], v[92:93]\n\tv_pk_fma_f32 v[38:39], v[38:39], v[70:71], v[94:95]\n\tv_cvt_pk_bf16_f32 v64, v32, v33\n\tv_cvt_pk_bf16_f32 v65, v34, v35\n\tv_cvt_pk_bf16_f32 v66, v36, v37\n\tv_cvt_pk_bf16_f32 v67, v38, v39\n\ts_nop 0\n\t" \
    "v_permlane16_swap_b32 v64, v66\n\tv_permlane16_swap_b32 v65, v67\n\tbuffer_store_dwordx4 v[64:67], v243, s[24:27], 0 offen offset:64\n\ts_branch 103f\n\t102:\n\tbuffer_store_dwordx4 v[72:75], v8, s[8:11], s45 offen offset:0 sc1\n\tbuffer_store_dwordx4 v[80:83], v8, s[8:11], s45 offen offset:1024 sc1\n\tbuffer_store_dwordx4 v[88:91], v8, s[8:11], s45 offen offset:2048 sc1\n\t" \
    "s_add_u32 s49, s49, 4096\n\ts_waitcnt vmcnt(0) lgkmcnt(0)\n\tbuffer_store_dwordx2 v[248:249], off, s[8:11], s49 sc1\n\ts_load_dwordx8 s[12:19], s[0:1], 16\n\ts_lshr_b32 s67, s40, 3\n\ts_sub_u32 s67, s67, 24\n\ts_lshl_b32 s67, s67, 2\n\ts_add_u32 s67, s67, s38\n\t" \
    "s_lshl_b32 s65, s67, 6\n\tv_add_u32 v6, s65, v240\n\tv_mul_u32_u24 v7, 7282, v6\n\tv_lshrrev_b32 v7, 16, v7\n\tv_mul_u32_u24 v5, 9, v7\n\tv_sub_u32 v6, v6, v5\n\tv_mul_u32_u24 v7, 9216, v7\n\tv_lshl_add_u32 v6, v6, 8, v7\n\t" \
    "v_lshlrev_b32 v7, 3, v244\n\tv_lshl_add_u32 v7, v245, 12, v7\n\ts_and_b32 s65, s40, 7\n\ts_lshr_b32 s73, s65, 1\n\ts_mul_i32 s66, s73, 2304\n\ts_mul_i32 s69, s57, 9437184\n\ts_add_u32 s66, s66, s69\n\ts_mul_i32 s73, s73, 4718592\n\t" \
    "s_and_b32 s65, s65, 1\n\ts_lshl_b32 s65, s65, 11\n\ts_add_u32 s65, s65, s73\n\ts_waitcnt lgkmcnt(0)\n\ts_add_u32 s12, s12, s66\n\ts_addc_u32 s13, s13, 0\n\ts_sub_u32 s14, s14, s66\n\ts_add_u32 s16, s16, s65\n\t" \
    "s_addc_u32 s17, s17, 0\n\ts_sub_u32 s18, s18, s65\n\tv_mov_b32 v238, 2146435072\n\tv_mov_b32 v236, 2146435072\n\ts_lshl_b32 s69, s57, 7\n\tv_mov_b32 v237, v240\n\tv_mul_u32_u24 v237, 12, v237\n\ts_add_u32 s73, s67, 220\n\t" \
    "v_add_u32 v237, s73, v237\n\tv_lshrrev_b32 v5, 3, v237\n\tv_lshlrev_b32 v5, 6, v5\n\tv_cmp_le_u32 vcc, 144, v237\n\tv_mov_b32 v4, 1120\n\tv_and_b32 v3, 7, v237\n\tv_cndmask_b32 v4, 0, v4, vcc\n\tv_lshl_add_u32 v5, v3, 2, v5\n\t" \
    "v_sub_u32 v5, v5, v4\n\tv_subrev_u32 v237, 220, v237\n\tv_cmp_gt_u32 vcc, 68, v237\n\tv_lshlrev_b32 v5, 12, v5\n\ts_mov_b32 s43, 0\n\tv_cndmask_b32 v237, v238, v5, vcc\n\ts_nop 0\n\tv_readlane_b32 s41, v236, 0\n\t" \
    "v_readlane_b32 s42, v237, 0\n\ts_nop 4\n\tbuffer_load_dword v15, v7, s[16:19], s42 offen\n\ts_add_u32 s43, s43, 1\n\tv_readlane_b32 s42, v237, s43\n\ts_nop 4\n\tbuffer_load_dword v15, v7, s[16:19], s42 offen\n\ts_add_u32 s43, s43, 1\n\t" \
    "v_readlane_b32 s42, v237, s43\n\ts_nop 4\n\tbuffer_load_dword v15, v7, s[16:19], s42 offen\n\ts_add_u32 s43, s43, 1\n\tv_readlane_b32 s42, v237, s43\n\ts_nop 4\n\tbuffer_load_dword v15, v7, s[16:19], s42 offen\n\ts_add_u32 s43, s43, 1\n\t" \
    "v_readlane_b32 s42, v237, s43\n\ts_nop 4\n\tbuffer_load_dword v15, v7, s[16:19], s42 offen\n\ts_add_u32 s43, s43, 1\n\tv_readlane_b32 s42, v237, s43\n\ts_nop 4\n\tbuffer_load_dword v15, v7, s[16:19], s42 offen\n\ts_add_u32 s43, s43, 1\n\t" \
    "v_readlane_b32 s42, v237, s43\n\ts_nop 4\n\ts_waitcnt vmcnt(0)\n\ts_branch 103f\n\t101:\n\ts_load_dwordx8 s[12:19], s[0:1], 16\n\ts_lshr_b32 s67, s40, 3\n\ts_sub_u32 s67, s67, 27\n\t" \
    "s_lshl_b32 s67, s67, 2\n\ts_add_u32 s67, s67, s38\n\ts_lshl_b32 s65, s67, 6\n\tv_add_u32 v6, s65, v240\n\tv_mul_u32_u24 v7, 7282, v6\n\tv_lshrrev_b32 v7, 16, v7\n\tv_mul_u32_u24 v5, 9, v7\n\tv_sub_u32 v6, v6, v5\n\t" \
    "v_mul_u32_u24 v7, 9216, v7\n\tv_lshl_add_u32 v6, v6, 8, v7\n\tv_lshlrev_b32 v7, 3, v244\n\tv_lshl_add_u32 v7, v245, 12, v7\n\ts_and_b32 s65, s40, 7\n\ts_lshr_b32 s73, s65, 1\n\ts_mul_i32 s66, s73, 2304\n\ts_mul_i32 s69, s57, 9437184\n\t" \
    "s_add_u32 s66, s66, s69\n\ts_mul_i32 s73, s73, 4718592\n\ts_and_b32 s65, s65, 1\n\ts_lshl_b32 s65, s65, 11\n\ts_add_u32 s65, s65, s73\n\ts_waitcnt lgkmcnt(0)\n\ts_add_u32 s12, s12, s66\n\ts_addc_u32 s13, s13, 0\n\t" \
    "s_sub_u32 s14, s14, s66\n\ts_add_u32 s16, s16, s65\n\ts_addc_u32 s17, s17, 0\n\ts_sub_u32 s18, s18, s65\n\tv_mov_b32 v238, 2146435072\n\tv_mov_b32 v236, 2146435072\n\ts_lshl_b32 s69, s57, 7\n\tv_mov_b32 v237, v240\n\t" \
    "s_xor_b32 s73, s69, 6193280\n\tv_writelane_b32 v236, s73, 0\n\tv_writelane_b32 v237, 0, 0\n\ts_xor_b32 s73, s69, 7501952\n\tv_writelane_b32 v236, s73, 1\n\tv_writelane_b32 v237, 0, 1\n\ts_xor_b32 s73, s69, 8810624\n\tv_writelane_b32 v236, s73, 2\n\t" \
    "v_writelane_b32 v237, 0, 2\n\tv_writelane_b32 v237, 6, 3\n\ts_xor_b32 s73, s69, 6193152\n\tv_writelane_b32 v236, s73, 4\n\tv_writelane_b32 v237, 0, 4\n\ts_xor_b32 s73, s69, 7501824\n\tv_writelane_b32 v236, s73, 5\n\tv_writelane_b32 v237, 0, 5\n\t" \
    "s_xor_b32 s73, s69, 8810496\n\tv_writelane_b32 v236, s73, 6\n\tv_writelane_b32 v237, 0, 6\n\tv_mul_u32_u24 v237, 20, v237\n\ts_add_u32 s73, s67, -120\n\tv_add_u32 v237, s73, v237\n\tv_lshrrev_b32 v5, 3, v237\n\tv_lshlrev_b32 v5, 6, v5\n\t" \
    "v_cmp_le_u32 vcc, 144, v237\n\tv_mov_b32 v4, 1120\n\tv_and_b32 v3, 7, v237\n\tv_cndmask_b32 v4, 0, v4, vcc\n\tv_lshl_add_u32 v5, v3, 2, v5\n\tv_sub_u32 v5, v5, v4\n\tv_subrev_u32 v237, 0, v237\n\tv_cmp_gt_u32 vcc, 220, v237\n\t" \
    "v_lshlrev_b32 v5, 12, v5\n\ts_mov_b32 s43, 0\n\tv_cndmask_b32 v237, v238, v5, vcc\n\ts_nop 0\n\tv_readlane_b32 s41, v236, 0\n\tv_readlane_b32 s42, v237, 0\n\ts_nop 4\n\ts_sub_u32 s65, s40, 216\n\t" \
    "s_lshl_b32 s44, s65, 4\n\ts_mul_i32 s66, s65, 12\n\ts_add_u32 s66, s66, 96\n\ts_cmp_lt_u32 s65, 24\n\ts_cselect_b32 s44, s44, s66\n\ts_cselect_b32 s45, 0, 2146435072\n\tv_cmp_eq_u32 vcc, 0, v240\n\tv_subrev_u32 v13, 8, v8\n\t" \
    "v_mov_b32 v12, 2146435072\n\tv_cndmask_b32 v13, v13, v12, vcc\n\ts_mov_b32 s4, s2\n\ts_mov_b32 s5, s3\n\ts_mov_b32 s6, 98f-99b\n\ts_mov_b32 s7, 0x20000\n\tv_lshlrev_b32 v14, 7, v240\n\ts_mov_b32 s46, 0xffff\n\t" \
    "v_and_b32 v204, 15, v240\n\tv_lshlrev_b32 v205, 3, v245\n\tv_sub_u32 v206, v205, v204\n\tv_add_u32 v206, 31, v206\n\tv_and_b32 v207, 3, v206\n\tv_mul_u32_u24 v207, 4160, v207\n\tv_and_b32 v206, 0xfc, v206\n\tv_lshl_add_u32 v0, v206, 1, v207\n\t" \
    "s_mul_i32 s65, s38, 16640\n\ts_add_u32 s65, s65, 73984\n\tv_add_u32 v0, s65, v0\n\ts_add_u32 s66, s65, 1040\n\tv_sub_u32 v2, s66, v8\n\tv_bfe_u32 v206, v240, 1, 2\n\tv_mul_u32_u24 v206, 4160, v206\n\tv_and_b32 v207, 16, v8\n\t" \
    "v_add3_u32 v9, v206, v207, s65\n\ts_mul_i32 s66, s38, 4624\n\tv_lshl_add_u32 v3, v204, 5, v205\n\tv_add_u32 v3, s66, v3\n\tv_add3_u32 v1, v3, v205, 32\n\tv_add_u32 v3, s66, v239\n\ts_lshl_b32 s73, s38, 4\n\tv_add_u32 v206, s73, v245\n\t" \
    "v_bfe_u32 v207, v240, 2, 2\n\tv_mul_u32_u24 v4, 4624, v207\n\tv_lshl_add_u32 v4, v245, 4, v4\n\tv_and_b32 v207, 3, v240\n\tv_lshl_add_u32 v4, v207, 7, v4\n\tv_add_u32 v4, s54, v4\n\tv_mul_u32_u24 v5, 12288, v206\n\tv_add_u32 v5, v5, v244\n\t" \
    "v_lshrrev_b32 v206, 5, v240\n\ts_lshl_b32 s73, s38, 2\n\tv_add_u32 v206, s73, v206\n\tv_mul_u32_u24 v206, 4624, v206\n\tv_and_b32 v207, 0x1f0, v8\n\tv_add_u32 v12, v206, v207\n\tv_mul_u32_u24 v10, 9216, v245\n\tv_add_u32 v10, v10, v244\n\t" \
    "v_mul_u32_u24 v11, 4624, v204\n\tv_lshl_add_u32 v11, v245, 4, v11\n\tv_add_u32 v11, s54, v11\n\ts_mul_i32 s62, s38, 589824\n\ts_lshl_b32 s73, s44, 4\n\ts_add_u32 s62, s62, s73\n\ts_add_u32 s62, s62, 2490368\n\tv_lshrrev_b32 v206, 3, v204\n\t" \
    "v_add_u32 v206, 2, v206\n\tv_cmp_eq_u32 vcc, v206, v245\n\tv_and_b32 v207, 1, v204\n\tv_lshlrev_b32 v207, 4, v207\n\tv_lshlrev_b32 v207, v207, s46\n\tv_cndmask_b32 v207, 0, v207, vcc\n\tv_bfe_u32 v206, v204, 1, 2\n\tv_cmp_eq_u32 vcc, 0, v206\n\t" \
    "v_mov_b32 v196, 0\n\ts_nop 0\n\tv_cndmask_b32 v232, 0, v207, vcc\n\tv_cmp_eq_u32 vcc, 1, v206\n\tv_mov_b32 v197, 0\n\ts_nop 0\n\tv_cndmask_b32 v233, 0, v207, vcc\n\tv_cmp_eq_u32 vcc, 2, v206\n\t" \
    "v_mov_b32 v198, 0\n\ts_nop 0\n\tv_cndmask_b32 v234, 0, v207, vcc\n\tv_cmp_eq_u32 vcc, 3, v206\n\tv_mov_b32 v199, 0\n\ts_nop 0\n\tv_cndmask_b32 v235, 0, v207, vcc\n\ts_add_u32 s65, s44, 0\n\t" \
    "s_lshr_b32 s65, s65, 6\n\ts_cmp_eq_u32 s65, 8\n\ts_cselect_b32 s65, 1, s65\n\ts_and_b32 s65, s65, 3\n\ts_lshl_b32 s47, s65, 11\n\ts_lshr_b32 s65, s54, 1\n\ts_add_u32 s47, s47, s65\n\ts_add_u32 s47, s47, 17408\n\t" \
    "s_add_u32 s65, s44, 11\n\ts_lshr_b32 s65, s65, 6\n\ts_cmp_eq_u32 s65, 8\n\ts_cselect_b32 s65, 1, s65\n\ts_and_b32 s65, s65, 3\n\ts_lshl_b32 s50, s65, 11\n\ts_lshr_b32 s65, s54, 1\n\ts_add_u32 s50, s50, s65\n\t" \
    "s_add_u32 s50, s50, 17408\n\tds_write_b128 v9, v[196:199] offset:4128\n\tbuffer_load_dword v15, v6, s[12:15], s41 offen\n\tbuffer_load_dword v15, v7, s[16:19], s42 offen\n\ts_add_u32 s43, s43, 1\n\tv_readlane_b32 s41, v236, s43\n\tv_readlane_b32 s42, v237, s43\n\ts_nop 4\n\t" \
    "s_waitcnt vmcnt(2)\n\tbuffer_store_dwordx2 v[248:249], off, s[8:11], s48 sc1\n\tv_alignbit_b32 v196, v167, v167, 16\n\tv_alignbit_b32 v197, v166, v166, 16\n\tv_alignbit_b32 v198, v165, v165, 16\n\tv_alignbit_b32 v199, v164, v164, 16\n\tds_write_b128 v2, v[196:199] offset:3072\n\tv_bfi_b32 v200, s46, v167, v166\n\t" \
    "v_bfi_b32 v201, s46, v166, v165\n\tv_bfi_b32 v202, s46, v165, v164\n\tv_bfi_b32 v203, s46, v164, v181\n\tds_write_b128 v2, v[200:203] offset:7232\n\tv_alignbit_b32 v196, v166, v166, 16\n\tv_alignbit_b32 v197, v165, v165, 16\n\tv_alignbit_b32 v198, v164, v164, 16\n\tv_alignbit_b32 v199, v181, v181, 16\n\t" \
    "ds_write_b128 v2, v[196:199] offset:11392\n\tv_bfi_b32 v200, s46, v166, v165\n\tv_bfi_b32 v201, s46, v165, v164\n\tv_bfi_b32 v202, s46, v164, v181\n\tv_bfi_b32 v203, s46, v181, v180\n\tds_write_b128 v2, v[200:203] offset:15552\n\tv_alignbit_b32 v196, v171, v171, 16\n\tv_alignbit_b32 v197, v170, v170, 16\n\t" \
    "v_alignbit_b32 v198, v169, v169, 16\n\tv_alignbit_b32 v199, v168, v168, 16\n\tds_write_b128 v2, v[196:199] offset:2048\n\tv_bfi_b32 v200, s46, v171, v170\n\tv_bfi_b32 v201, s46, v170, v169\n\tv_bfi_b32 v202, s46, v169, v168\n\tv_bfi_b32 v203, s46, v168, v183\n\tds_write_b128 v2, v[200:203] offset:6208\n\t" \
    "v_alignbit_b32 v196, v170, v170, 16\n\tv_alignbit_b32 v197, v169, v169, 16\n\tv_alignbit_b32 v198, v168, v168, 16\n\tv_alignbit_b32 v199, v183, v183, 16\n\tds_write_b128 v2, v[196:199] offset:10368\n\tv_bfi_b32 v200, s46, v170, v169\n\tv_bfi_b32 v201, s46, v169, v168\n\tv_bfi_b32 v202, s46, v168, v183\n\t" \
    "v_bfi_b32 v203, s46, v183, v182\n\tds_write_b128 v2, v[200:203] offset:14528\n\tv_alignbit_b32 v196, v175, v175, 16\n\tv_alignbit_b32 v197, v174, v174, 16\n\tv_alignbit_b32 v198, v173, v173, 16\n\tv_alignbit_b32 v199, v172, v172, 16\n\tds_write_b128 v2, v[196:199] offset:1024\n\tv_bfi_b32 v200, s46, v175, v174\n\t" \
    "v_bfi_b32 v201, s46, v174, v173\n\tv_bfi_b32 v202, s46, v173, v172\n\tv_bfi_b32 v203, s46, v172, v185\n\tds_write_b128 v2, v[200:203] offset:5184\n\tv_alignbit_b32 v196, v174, v174, 16\n\tv_alignbit_b32 v197, v173, v173, 16\n\tv_alignbit_b32 v198, v172, v172, 16\n\tv_alignbit_b32 v199, v185, v185, 16\n\t" \
    "ds_write_b128 v2, v[196:199] offset:9344\n\tv_bfi_b32 v200, s46, v174, v173\n\tv_bfi_b32 v201, s46, v173, v172\n\tv_bfi_b32 v202, s46, v172, v185\n\tv_bfi_b32 v203, s46, v185, v184\n\tds_write_b128 v2, v[200:203] offset:13504\n\tv_alignbit_b32 v196, v179, v179, 16\n\tv_alignbit_b32 v197, v178, v178, 16\n\t" \
    "v_alignbit_b32 v198, v177, v177, 16\n\tv_alignbit_b32 v199, v176, v176, 16\n\tds_write_b128 v2, v[196:199] offset:0\n\tv_bfi_b32 v200, s46, v179, v178\n\tv_bfi_b32 v201, s46, v178, v177\n\tv_bfi_b32 v202, s46, v177, v176\n\tv_bfi_b32 v203, s46, v176, v187\n\tds_write_b128 v2, v[200:203] offset:4160\n\t" \
    "v_alignbit_b32 v196, v178, v178, 16\n\tv_alignbit_b32 v197, v177, v177, 16\n\tv_alignbit_b32 v198, v176, v176, 16\n\tv_alignbit_b32 v199, v187, v187, 16\n\tds_write_b128 v2, v[196:199] offset:8320\n\tv_bfi_b32 v200, s46, v178, v177\n\tv_bfi_b32 v201, s46, v177, v176\n\tv_bfi_b32 v202, s46, v176, v187\n\t" \
    "v_bfi_b32 v203, s46, v187, v186\n\tds_write_b128 v2, v[200:203] offset:12480\n\tv_lshl_or_b32 v188, v188, 16, v188\n\tv_and_b32 v228, v188, v232\n\tv_and_b32 v229, v188, v233\n\tv_and_b32 v230, v188, v234\n\tv_and_b32 v231, v188, v235\n\tds_read_b64 a[0:1], v0 offset:4032\n\t" \
    "ds_read_b64 a[2:3], v0 offset:4040\n\tds_read_b64 a[4:5], v0 offset:3520\n\tds_read_b64 a[6:7], v0 offset:3528\n\tds_read_b64 a[8:9], v0 offset:3008\n\tds_read_b64 a[10:11], v0 offset:3016\n\tds_read_b64 a[12:13], v0 offset:2496\n\tds_read_b64 a[14:15], v0 offset:2504\n\tds_read_b64 a[16:17], v0 offset:1984\n\t" \
    "ds_read_b64 a[18:19], v0 offset:1992\n\tds_read_b64 a[20:21], v0 offset:1472\n\tds_read_b64 a[22:23], v0 offset:1480\n\tds_read_b64 a[24:25], v0 offset:960\n\tds_read_b64 a[26:27], v0 offset:968\n\tds_read_b64 a[28:29], v0 offset:448\n\tds_read_b64 a[30:31], v0 offset:456\n\ts_barrier\n\t" \
    "v_mov_b32 v196, 0\n\tv_mov_b32 v197, 0\n\tv_mov_b32 v198, 0\n\tv_mov_b32 v199, 0\n\tds_write_b128 v12, v[196:199]\n\tds_write_b128 v12, v[196:199] offset:9248\n\ts_sleep 30\n\ts_mov_b32 s71, 1048576\n\t" \
    "124:\n\tbuffer_load_dwordx2 v[250:251], v239, s[8:11], s47 offen sc1\n\tbuffer_load_dwordx2 v[252:253], v239, s[8:11], s50 offen sc1\n\ts_waitcnt vmcnt(0)\n\tv_cmp_ne_u64 vcc, s[36:37], v[250:251]\n\tv_cmp_ne_u64 s[74:75], s[36:37], v[252:253]\n\ts_or_b64 s[74:75], s[74:75], vcc\n\ts_cbranch_scc0 125f\n\t" \
    "s_sleep 16\n\ts_sub_u32 s71, s71, 1\n\ts_cbranch_scc0 124b\n\t125:\n\tbuffer_load_dwordx4 v[68:71], v10, s[8:11], s62 offen sc1\n\ts_add_u32 s62, s62, 36864\n\tbuffer_load_dwordx4 v[72:75], v10, s[8:11], s62 offen sc1\n\ts_add_u32 s62, s62, 36864\n\t" \
    "buffer_load_dwordx4 v[76:79], v10, s[8:11], s62 offen sc1\n\ts_add_u32 s62, s62, 36864\n\tbuffer_load_dwordx4 v[80:83], v10, s[8:11], s62 offen sc1\n\ts_add_u32 s62, s62, 36864\n\tbuffer_load_dwordx4 v[84:87], v10, s[8:11], s62 offen sc1\n\ts_add_u32 s62, s62, 36864\n\tbuffer_load_dwordx4 v[88:91], v10, s[8:11], s62 offen sc1\n\ts_add_u32 s62, s62, 36864\n\t" \
    "buffer_load_dwordx4 v[92:95], v10, s[8:11], s62 offen sc1\n\ts_add_u32 s62, s62, 36864\n\tbuffer_load_dwordx4 v[96:99], v10, s[8:11], s62 offen sc1\n\ts_add_u32 s62, s62, 36864\n\tbuffer_load_dwordx4 v[100:103], v10, s[8:11], s62 offen sc1\n\ts_add_u32 s62, s62, 36864\n\tbuffer_load_dwordx4 v[104:107], v10, s[8:11], s62 offen sc1\n\ts_add_u32 s62, s62, 36864\n\t" \
    "buffer_load_dwordx4 v[108:111], v10, s[8:11], s62 offen sc1\n\ts_add_u32 s62, s62, 36864\n\tbuffer_load_dwordx4 v[112:115], v10, s[8:11], s62 offen sc1\n\ts_add_u32 s62, s62, 36864\n\tbuffer_load_dwordx4 v[116:119], v10, s[8:11], s62 offen sc1\n\ts_add_u32 s62, s62, 36864\n\tbuffer_load_dwordx4 v[120:123], v10, s[8:11], s62 offen sc1\n\ts_add_u32 s62, s62, 36864\n\t" \
    "buffer_load_dwordx4 v[124:127], v10, s[8:11], s62 offen sc1\n\ts_add_u32 s62, s62, 36864\n\tbuffer_load_dwordx4 v[128:131], v10, s[8:11], s62 offen sc1\n\ts_add_u32 s62, s62, 36864\n\ts_waitcnt vmcnt(15)\n\tds_write_b128 v11, v[68:71] offset:512\n\ts_waitcnt vmcnt(14)\n\tds_write_b128 v11, v[72:75] offset:576\n\t" \
    "s_waitcnt vmcnt(13)\n\tds_write_b128 v11, v[76:79] offset:640\n\ts_waitcnt vmcnt(12)\n\tds_write_b128 v11, v[80:83] offset:704\n\ts_waitcnt vmcnt(11)\n\tds_write_b128 v11, v[84:87] offset:768\n\ts_waitcnt vmcnt(10)\n\tds_write_b128 v11, v[88:91] offset:832\n\t" \
    "s_waitcnt vmcnt(9)\n\tds_write_b128 v11, v[92:95] offset:896\n\ts_waitcnt vmcnt(8)\n\tds_write_b128 v11, v[96:99] offset:960\n\ts_waitcnt vmcnt(7)\n\tds_write_b128 v11, v[100:103] offset:1024\n\ts_waitcnt vmcnt(6)\n\tds_write_b128 v11, v[104:107] offset:1088\n\t" \
    "s_waitcnt vmcnt(5)\n\tds_write_b128 v11, v[108:111] offset:1152\n\ts_waitcnt vmcnt(4)\n\tds_write_b128 v11, v[112:115] offset:1216\n\ts_waitcnt vmcnt(3)\n\tds_write_b128 v11, v[116:119] offset:1280\n\ts_waitcnt vmcnt(2)\n\tds_write_b128 v11, v[120:123] offset:1344\n\t" \
    "s_waitcnt vmcnt(1)\n\tds_write_b128 v11, v[124:127] offset:1408\n\ts_waitcnt vmcnt(0)\n\tds_write_b128 v11, v[128:131] offset:1472\n\ts_waitcnt lgkmcnt(0)\n\ts_barrier\n\tds_read_b128 v[68:71], v1 offset:448\n\tds_read_b128 v[72:75], v1 offset:960\n\t" \
    "ds_read_b128 v[76:79], v1 offset:1472\n\tds_read_b128 v[80:83], v1 offset:1984\n\tds_read_b128 v[84:87], v1 offset:2496\n\tds_read_b128 v[88:91], v1 offset:3008\n\tds_read_b128 v[92:95], v1 offset:3520\n\tds_read_b128 v[96:99], v1 offset:4032\n\ts_add_u32 s68, s44, s38\n\ts_add_u32 s68, s68, 4\n\t" \
    "s_lshl_b32 s66, s68, 1\n\ts_lshl_b32 s68, s68, 12\n\tbuffer_load_dwordx4 v[42:45], v8, s[76:79], s68 offen offset:0\n\tbuffer_load_dwordx4 v[46:49], v8, s[76:79], s68 offen offset:1024\n\tbuffer_load_dwordx4 v[50:53], v8, s[76:79], s68 offen offset:2048\n\tbuffer_load_dwordx4 v[54:57], v8, s[76:79], s68 offen offset:3072\n\tbuffer_load_dwordx2 v[58:59], v13, s[76:79], s68 offen\n\tbuffer_load_dwordx2 v[60:61], v8, s[76:79], s68 offen offset:1016\n\t" \
    "buffer_load_dwordx2 v[62:63], v8, s[76:79], s68 offen offset:2040\n\tbuffer_load_dwordx2 v[64:65], v8, s[76:79], s68 offen offset:3064\n\tbuffer_load_ushort v66, off, s[80:83], s66\n\tbuffer_load_dword v15, v6, s[12:15], s41 offen\n\tbuffer_load_dword v15, v7, s[16:19], s42 offen\n\ts_add_u32 s43, s43, 1\n\tv_readlane_b32 s41, v236, s43\n\tv_readlane_b32 s42, v237, s43\n\t" \
    "s_nop 4\n\ts_waitcnt lgkmcnt(0)\n\tv_mfma_f32_16x16x32_bf16 v[132:135], v[228:231], v[68:71], 0\n\tds_read_b64 a[32:33], v0 offset:3968\n\tds_read_b64 a[34:35], v0 offset:3976\n\tv_mfma_f32_16x16x32_bf16 v[136:139], v[228:231], v[72:75], 0\n\tds_read_b64 a[36:37], v0 offset:3456\n\tds_read_b64 a[38:39], v0 offset:3464\n\t" \
    "v_mfma_f32_16x16x32_bf16 v[140:143], v[228:231], v[76:79], 0\n\tds_read_b64 a[40:41], v0 offset:2944\n\tds_read_b64 a[42:43], v0 offset:2952\n\tv_mfma_f32_16x16x32_bf16 v[144:147], v[228:231], v[80:83], 0\n\tds_read_b64 a[44:45], v0 offset:2432\n\tds_read_b64 a[46:47], v0 offset:2440\n\tv_mfma_f32_16x16x32_bf16 v[148:151], v[228:231], v[84:87], 0\n\tds_read_b64 a[48:49], v0 offset:1920\n\t" \
    "ds_read_b64 a[50:51], v0 offset:1928\n\tv_mfma_f32_16x16x32_bf16 v[152:155], v[228:231], v[88:91], 0\n\tds_read_b64 a[52:53], v0 offset:1408\n\tv_mfma_f32_16x16x32_bf16 v[156:159], v[228:231], v[92:95], 0\n\tds_read_b64 a[54:55], v0 offset:1416\n\tv_mfma_f32_16x16x32_bf16 v[160:163], v[228:231], v[96:99], 0\n\tds_read_b64 a[56:57], v0 offset:896\n\tv_mfma_f32_16x16x32_bf16 v[132:135], a[0:3], v[68:71], v[132:135]\n\t" \
    "ds_read_b64 a[58:59], v0 offset:904\n\tv_mfma_f32_16x16x32_bf16 v[136:139], a[0:3], v[72:75], v[136:139]\n\tds_read_b64 a[60:61], v0 offset:384\n\tv_mfma_f32_16x16x32_bf16 v[140:143], a[0:3], v[76:79], v[140:143]\n\tds_read_b64 a[62:63], v0 offset:392\n\tv_mfma_f32_16x16x32_bf16 v[144:147], a[0:3], v[80:83], v[144:147]\n\tds_read_b128 v[100:103], v1 offset:384\n\tv_mfma_f32_16x16x32_bf16 v[148:151], a[0:3], v[84:87], v[148:151]\n\t" \
    "ds_read_b128 v[104:107], v1 offset:896\n\tv_mfma_f32_16x16x32_bf16 v[152:155], a[0:3], v[88:91], v[152:155]\n\tds_read_b128 v[108:111], v1 offset:1408\n\tv_mfma_f32_16x16x32_bf16 v[156:159], a[0:3], v[92:95], v[156:159]\n\tds_read_b128 v[112:115], v1 offset:1920\n\tv_mfma_f32_16x16x32_bf16 v[160:163], a[0:3], v[96:99], v[160:163]\n\tds_read_b128 v[116:119], v1 offset:2432\n\tv_mfma_f32_16x16x32_bf16 v[136:139], a[4:7], v[68:71], v[136:139]\n\t" \
    "ds_read_b128 v[120:123], v1 offset:2944\n\tv_mfma_f32_16x16x32_bf16 v[140:143], a[4:7], v[72:75], v[140:143]\n\tbuffer_load_dword v15, v6, s[12:15], s41 offen\n\tds_read_b128 v[124:127], v1 offset:3456\n\tv_mfma_f32_16x16x32_bf16 v[144:147], a[4:7], v[76:79], v[144:147]\n\tbuffer_load_dword v15, v7, s[16:19], s42 offen\n\tds_read_b128 v[128:131], v1 offset:3968\n\tv_mfma_f32_16x16x32_bf16 v[148:151], a[4:7], v[80:83], v[148:151]\n\t" \
    "s_add_u32 s43, s43, 1\n\tds_read_b64 a[64:65], v0 offset:3904\n\tv_mfma_f32_16x16x32_bf16 v[152:155], a[4:7], v[84:87], v[152:155]\n\tv_readlane_b32 s41, v236, s43\n\tv_readlane_b32 s42, v237, s43\n\ts_nop 4\n\tds_read_b64 a[66:67], v0 offset:3912\n\tv_mfma_f32_16x16x32_bf16 v[156:159], a[4:7], v[88:91], v[156:159]\n\t" \
    "ds_read_b64 a[68:69], v0 offset:3392\n\tv_mfma_f32_16x16x32_bf16 v[160:163], a[4:7], v[92:95], v[160:163]\n\tds_read_b64 a[70:71], v0 offset:3400\n\tv_mfma_f32_16x16x32_bf16 v[140:143], a[8:11], v[68:71], v[140:143]\n\tds_read_b64 a[72:73], v0 offset:2880\n\tv_mfma_f32_16x16x32_bf16 v[144:147], a[8:11], v[72:75], v[144:147]\n\tds_read_b64 a[74:75], v0 offset:2888\n\tv_mfma_f32_16x16x32_bf16 v[148:151], a[8:11], v[76:79], v[148:151]\n\t" \
    "ds_read_b64 a[76:77], v0 offset:2368\n\tv_mfma_f32_16x16x32_bf16 v[152:155], a[8:11], v[80:83], v[152:155]\n\tds_read_b64 a[78:79], v0 offset:2376\n\tv_mfma_f32_16x16x32_bf16 v[156:159], a[8:11], v[84:87], v[156:159]\n\tds_read_b64 a[80:81], v0 offset:1856\n\tv_mfma_f32_16x16x32_bf16 v[160:163], a[8:11], v[88:91], v[160:163]\n\tds_read_b64 a[82:83], v0 offset:1864\n\tv_mfma_f32_16x16x32_bf16 v[144:147], a[12:15], v[68:71], v[144:147]\n\t" \
    "ds_read_b64 a[84:85], v0 offset:1344\n\tv_mfma_f32_16x16x32_bf16 v[148:151], a[12:15], v[72:75], v[148:151]\n\tds_read_b64 a[86:87], v0 offset:1352\n\tv_mfma_f32_16x16x32_bf16 v[152:155], a[12:15], v[76:79], v[152:155]\n\tds_read_b64 a[88:89], v0 offset:832\n\tv_mfma_f32_16x16x32_bf16 v[156:159], a[12:15], v[80:83], v[156:159]\n\tds_read_b64 a[90:91], v0 offset:840\n\tv_mfma_f32_16x16x32_bf16 v[160:163], a[12:15], v[84:87], v[160:163]\n\t" \
    "ds_read_b64 a[92:93], v0 offset:320\n\tv_mfma_f32_16x16x32_bf16 v[148:151], a[16:19], v[68:71], v[148:151]\n\tds_read_b64 a[94:95], v0 offset:328\n\tv_mfma_f32_16x16x32_bf16 v[152:155], a[16:19], v[72:75], v[152:155]\n\tds_read_b64 a[96:97], v0 offset:3840\n\tv_mfma_f32_16x16x32_bf16 v[156:159], a[16:19], v[76:79], v[156:159]\n\tds_read_b64 a[98:99], v0 offset:3848\n\tv_mfma_f32_16x16x32_bf16 v[160:163], a[16:19], v[80:83], v[160:163]\n\t" \
    "ds_read_b64 a[100:101], v0 offset:3328\n\tv_mfma_f32_16x16x32_bf16 v[152:155], a[20:23], v[68:71], v[152:155]\n\tds_read_b64 a[102:103], v0 offset:3336\n\tv_mfma_f32_16x16x32_bf16 v[156:159], a[20:23], v[72:75], v[156:159]\n\tv_mfma_f32_16x16x32_bf16 v[160:163], a[20:23], v[76:79], v[160:163]\n\tv_mfma_f32_16x16x32_bf16 v[156:159], a[24:27], v[68:71], v[156:159]\n\tv_mfma_f32_16x16x32_bf16 v[160:163], a[24:27], v[72:75], v[160:163]\n\tv_mfma_f32_16x16x32_bf16 v[160:163], a[28:31], v[68:71], v[160:163]\n\t" \
    "s_waitcnt lgkmcnt(15)\n\tv_mfma_f32_16x16x32_bf16 v[132:135], a[32:35], v[100:103], v[132:135]\n\tds_read_b64 a[104:105], v0 offset:2816\n\tds_read_b64 a[106:107], v0 offset:2824\n\tv_mfma_f32_16x16x32_bf16 v[136:139], a[32:35], v[104:107], v[136:139]\n\tds_read_b64 a[108:109], v0 offset:2304\n\tds_read_b64 a[110:111], v0 offset:2312\n\tv_mfma_f32_16x16x32_bf16 v[140:143], a[32:35], v[108:111], v[140:143]\n\t" \
    "ds_read_b64 a[112:113], v0 offset:1792\n\tds_read_b64 a[114:115], v0 offset:1800\n\tv_mfma_f32_16x16x32_bf16 v[144:147], a[32:35], v[112:115], v[144:147]\n\tds_read_b64 a[116:117], v0 offset:1280\n\tds_read_b64 a[118:119], v0 offset:1288\n\tv_mfma_f32_16x16x32_bf16 v[148:151], a[32:35], v[116:119], v[148:151]\n\tds_read_b64 a[120:121], v0 offset:768\n\tds_read_b64 a[122:123], v0 offset:776\n\t" \
    "v_mfma_f32_16x16x32_bf16 v[152:155], a[32:35], v[120:123], v[152:155]\n\tds_read_b64 a[124:125], v0 offset:256\n\tv_mfma_f32_16x16x32_bf16 v[156:159], a[32:35], v[124:127], v[156:159]\n\tds_read_b64 a[126:127], v0 offset:264\n\tv_mfma_f32_16x16x32_bf16 v[160:163], a[32:35], v[128:131], v[160:163]\n\tds_read_b64 a[128:129], v0 offset:3776\n\tv_mfma_f32_16x16x32_bf16 v[136:139], a[36:39], v[100:103], v[136:139]\n\tds_read_b64 a[130:131], v0 offset:3784\n\t" \
    "v_mfma_f32_16x16x32_bf16 v[140:143], a[36:39], v[104:107], v[140:143]\n\tds_read_b64 a[132:133], v0 offset:3264\n\tv_mfma_f32_16x16x32_bf16 v[144:147], a[36:39], v[108:111], v[144:147]\n\tds_read_b64 a[134:135], v0 offset:3272\n\tv_mfma_f32_16x16x32_bf16 v[148:151], a[36:39], v[112:115], v[148:151]\n\tds_read_b128 v[68:71], v1 offset:320\n\tv_mfma_f32_16x16x32_bf16 v[152:155], a[36:39], v[116:119], v[152:155]\n\tds_read_b128 v[72:75], v1 offset:832\n\t" \
    "v_mfma_f32_16x16x32_bf16 v[156:159], a[36:39], v[120:123], v[156:159]\n\tds_read_b128 v[76:79], v1 offset:1344\n\tv_mfma_f32_16x16x32_bf16 v[160:163], a[36:39], v[124:127], v[160:163]\n\tds_read_b128 v[80:83], v1 offset:1856\n\tv_mfma_f32_16x16x32_bf16 v[140:143], a[40:43], v[100:103], v[140:143]\n\tds_read_b128 v[84:87], v1 offset:2368\n\tv_mfma_f32_16x16x32_bf16 v[144:147], a[40:43], v[104:107], v[144:147]\n\tds_read_b128 v[88:91], v1 offset:2880\n\t" \
    "v_mfma_f32_16x16x32_bf16 v[148:151], a[40:43], v[108:111], v[148:151]\n\tds_read_b128 v[92:95], v1 offset:3392\n\tv_mfma_f32_16x16x32_bf16 v[152:155], a[40:43], v[112:115], v[152:155]\n\tds_read_b128 v[96:99], v1 offset:3904\n\tv_mfma_f32_16x16x32_bf16 v[156:159], a[40:43], v[116:119], v[156:159]\n\tds_read_b64 a[136:137], v0 offset:2752\n\tv_mfma_f32_16x16x32_bf16 v[160:163], a[40:43], v[120:123], v[160:163]\n\tds_read_b64 a[138:139], v0 offset:2760\n\t" \
    "v_mfma_f32_16x16x32_bf16 v[144:147], a[44:47], v[100:103], v[144:147]\n\tds_read_b64 a[140:141], v0 offset:2240\n\tv_mfma_f32_16x16x32_bf16 v[148:151], a[44:47], v[104:107], v[148:151]\n\tds_read_b64 a[142:143], v0 offset:2248\n\tv_mfma_f32_16x16x32_bf16 v[152:155], a[44:47], v[108:111], v[152:155]\n\tds_read_b64 a[144:145], v0 offset:1728\n\tv_mfma_f32_16x16x32_bf16 v[156:159], a[44:47], v[112:115], v[156:159]\n\tds_read_b64 a[146:147], v0 offset:1736\n\t" \
    "v_mfma_f32_16x16x32_bf16 v[160:163], a[44:47], v[116:119], v[160:163]\n\tds_read_b64 a[148:149], v0 offset:1216\n\tv_mfma_f32_16x16x32_bf16 v[148:151], a[48:51], v[100:103], v[148:151]\n\tds_read_b64 a[150:151], v0 offset:1224\n\tv_mfma_f32_16x16x32_bf16 v[152:155], a[48:51], v[104:107], v[152:155]\n\tds_read_b64 a[152:153], v0 offset:704\n\tv_mfma_f32_16x16x32_bf16 v[156:159], a[48:51], v[108:111], v[156:159]\n\tds_read_b64 a[154:155], v0 offset:712\n\t" \
    "v_mfma_f32_16x16x32_bf16 v[160:163], a[48:51], v[112:115], v[160:163]\n\tds_read_b64 a[156:157], v0 offset:192\n\tv_mfma_f32_16x16x32_bf16 v[152:155], a[52:55], v[100:103], v[152:155]\n\tds_read_b64 a[158:159], v0 offset:200\n\tv_mfma_f32_16x16x32_bf16 v[156:159], a[52:55], v[104:107], v[156:159]\n\tv_mfma_f32_16x16x32_bf16 v[160:163], a[52:55], v[108:111], v[160:163]\n\tbuffer_load_dword v15, v6, s[12:15], s41 offen\n\tv_mfma_f32_16x16x32_bf16 v[156:159], a[56:59], v[100:103], v[156:159]\n\t" \
    "buffer_load_dword v15, v7, s[16:19], s42 offen\n\tv_mfma_f32_16x16x32_bf16 v[160:163], a[56:59], v[104:107], v[160:163]\n\ts_add_u32 s43, s43, 1\n\tv_mfma_f32_16x16x32_bf16 v[160:163], a[60:63], v[100:103], v[160:163]\n\tv_readlane_b32 s41, v236, s43\n\tv_readlane_b32 s42, v237, s43\n\ts_nop 4\n\ts_waitcnt lgkmcnt(12)\n\t" \
    "v_mfma_f32_16x16x32_bf16 v[132:135], a[64:67], v[68:71], v[132:135]\n\tds_read_b64 a[160:161], v0 offset:3712\n\tds_read_b64 a[162:163], v0 offset:3720\n\tv_mfma_f32_16x16x32_bf16 v[136:139], a[64:67], v[72:75], v[136:139]\n\tds_read_b64 a[164:165], v0 offset:3200\n\tds_read_b64 a[166:167], v0 offset:3208\n\tv_mfma_f32_16x16x32_bf16 v[140:143], a[64:67], v[76:79], v[140:143]\n\tds_read_b64 a[168:169], v0 offset:2688\n\t" \
    "ds_read_b64 a[170:171], v0 offset:2696\n\tv_mfma_f32_16x16x32_bf16 v[144:147], a[64:67], v[80:83], v[144:147]\n\tds_read_b64 a[172:173], v0 offset:2176\n\tds_read_b64 a[174:175], v0 offset:2184\n\tv_mfma_f32_16x16x32_bf16 v[148:151], a[64:67], v[84:87], v[148:151]\n\tds_read_b64 a[176:177], v0 offset:1664\n\tds_read_b64 a[178:179], v0 offset:1672\n\tv_mfma_f32_16x16x32_bf16 v[152:155], a[64:67], v[88:91], v[152:155]\n\t" \
    "ds_read_b64 a[180:181], v0 offset:1152\n\tv_mfma_f32_16x16x32_bf16 v[156:159], a[64:67], v[92:95], v[156:159]\n\tds_read_b64 a[182:183], v0 offset:1160\n\tv_mfma_f32_16x16x32_bf16 v[160:163], a[64:67], v[96:99], v[160:163]\n\tds_read_b64 a[184:185], v0 offset:640\n\tv_mfma_f32_16x16x32_bf16 v[136:139], a[68:71], v[68:71], v[136:139]\n\tds_read_b64 a[186:187], v0 offset:648\n\tv_mfma_f32_16x16x32_bf16 v[140:143], a[68:71], v[72:75], v[140:143]\n\t" \
    "ds_read_b64 a[188:189], v0 offset:128\n\tv_mfma_f32_16x16x32_bf16 v[144:147], a[68:71], v[76:79], v[144:147]\n\tds_read_b64 a[190:191], v0 offset:136\n\tv_mfma_f32_16x16x32_bf16 v[148:151], a[68:71], v[80:83], v[148:151]\n\tds_read_b128 v[100:103], v1 offset:256\n\tv_mfma_f32_16x16x32_bf16 v[152:155], a[68:71], v[84:87], v[152:155]\n\tds_read_b128 v[104:107], v1 offset:768\n\tv_mfma_f32_16x16x32_bf16 v[156:159], a[68:71], v[88:91], v[156:159]\n\t" \
    "ds_read_b128 v[108:111], v1 offset:1280\n\tv_mfma_f32_16x16x32_bf16 v[160:163], a[68:71], v[92:95], v[160:163]\n\tds_read_b128 v[112:115], v1 offset:1792\n\tv_mfma_f32_16x16x32_bf16 v[140:143], a[72:75], v[68:71], v[140:143]\n\tds_read_b128 v[116:119], v1 offset:2304\n\tv_mfma_f32_16x16x32_bf16 v[144:147], a[72:75], v[72:75], v[144:147]\n\tds_read_b128 v[120:123], v1 offset:2816\n\tv_mfma_f32_16x16x32_bf16 v[148:151], a[72:75], v[76:79], v[148:151]\n\t" \
    "ds_read_b128 v[124:127], v1 offset:3328\n\tv_mfma_f32_16x16x32_bf16 v[152:155], a[72:75], v[80:83], v[152:155]\n\tds_read_b128 v[128:131], v1 offset:3840\n\tv_mfma_f32_16x16x32_bf16 v[156:159], a[72:75], v[84:87], v[156:159]\n\tds_read_b64 a[192:193], v0 offset:3648\n\tv_mfma_f32_16x16x32_bf16 v[160:163], a[72:75], v[88:91], v[160:163]\n\tds_read_b64 a[194:195], v0 offset:3656\n\tv_mfma_f32_16x16x32_bf16 v[144:147], a[76:79], v[68:71], v[144:147]\n\t" \
    "ds_read_b64 a[196:197], v0 offset:3136\n\tv_mfma_f32_16x16x32_bf16 v[148:151], a[76:79], v[72:75], v[148:151]\n\tds_read_b64 a[198:199], v0 offset:3144\n\tv_mfma_f32_16x16x32_bf16 v[152:155], a[76:79], v[76:79], v[152:155]\n\tds_read_b64 a[200:201], v0 offset:2624\n\tv_mfma_f32_16x16x32_bf16 v[156:159], a[76:79], v[80:83], v[156:159]\n\tds_read_b64 a[202:203], v0 offset:2632\n\tv_mfma_f32_16x16x32_bf16 v[160:163], a[76:79], v[84:87], v[160:163]\n\t" \
    "ds_read_b64 a[204:205], v0 offset:2112\n\tv_mfma_f32_16x16x32_bf16 v[148:151], a[80:83], v[68:71], v[148:151]\n\tds_read_b64 a[206:207], v0 offset:2120\n\tv_mfma_f32_16x16x32_bf16 v[152:155], a[80:83], v[72:75], v[152:155]\n\tds_read_b64 a[208:209], v0 offset:1600\n\tv_mfma_f32_16x16x32_bf16 v[156:159], a[80:83], v[76:79], v[156:159]\n\tds_read_b64 a[210:211], v0 offset:1608\n\tv_mfma_f32_16x16x32_bf16 v[160:163], a[80:83], v[80:83], v[160:163]\n\t" \
    "ds_read_b64 a[212:213], v0 offset:1088\n\tv_mfma_f32_16x16x32_bf16 v[152:155], a[84:87], v[68:71], v[152:155]\n\tds_read_b64 a[214:215], v0 offset:1096\n\tv_mfma_f32_16x16x32_bf16 v[156:159], a[84:87], v[72:75], v[156:159]\n\tv_mfma_f32_16x16x32_bf16 v[160:163], a[84:87], v[76:79], v[160:163]\n\tv_mfma_f32_16x16x32_bf16 v[156:159], a[88:91], v[68:71], v[156:159]\n\tv_mfma_f32_16x16x32_bf16 v[160:163], a[88:91], v[72:75], v[160:163]\n\tv_mfma_f32_16x16x32_bf16 v[160:163], a[92:95], v[68:71], v[160:163]\n\t" \
    "s_waitcnt lgkmcnt(12)\n\tv_mfma_f32_16x16x32_bf16 v[132:135], a[96:99], v[100:103], v[132:135]\n\tds_read_b64 a[216:217], v0 offset:576\n\tv_mfma_f32_16x16x32_bf16 v[136:139], a[96:99], v[104:107], v[136:139]\n\tds_read_b64 a[218:219], v0 offset:584\n\tv_mfma_f32_16x16x32_bf16 v[140:143], a[96:99], v[108:111], v[140:143]\n\tds_read_b64 a[220:221], v0 offset:64\n\tv_mfma_f32_16x16x32_bf16 v[144:147], a[96:99], v[112:115], v[144:147]\n\t" \
    "ds_read_b64 a[222:223], v0 offset:72\n\tv_mfma_f32_16x16x32_bf16 v[148:151], a[96:99], v[116:119], v[148:151]\n\tds_read_b64 a[224:225], v0 offset:3584\n\tv_mfma_f32_16x16x32_bf16 v[152:155], a[96:99], v[120:123], v[152:155]\n\tds_read_b64 a[226:227], v0 offset:3592\n\tv_mfma_f32_16x16x32_bf16 v[156:159], a[96:99], v[124:127], v[156:159]\n\tds_read_b64 a[228:229], v0 offset:3072\n\tv_mfma_f32_16x16x32_bf16 v[160:163], a[96:99], v[128:131], v[160:163]\n\t" \
    "ds_read_b64 a[230:231], v0 offset:3080\n\tv_mfma_f32_16x16x32_bf16 v[136:139], a[100:103], v[100:103], v[136:139]\n\tds_read_b64 a[232:233], v0 offset:2560\n\tv_mfma_f32_16x16x32_bf16 v[140:143], a[100:103], v[104:107], v[140:143]\n\tds_read_b64 a[234:235], v0 offset:2568\n\tv_mfma_f32_16x16x32_bf16 v[144:147], a[100:103], v[108:111], v[144:147]\n\tds_read_b64 a[236:237], v0 offset:2048\n\tv_mfma_f32_16x16x32_bf16 v[148:151], a[100:103], v[112:115], v[148:151]\n\t" \
    "ds_read_b64 a[238:239], v0 offset:2056\n\tv_mfma_f32_16x16x32_bf16 v[152:155], a[100:103], v[116:119], v[152:155]\n\tds_read_b64 a[240:241], v0 offset:1536\n\tv_mfma_f32_16x16x32_bf16 v[156:159], a[100:103], v[120:123], v[156:159]\n\tds_read_b64 a[242:243], v0 offset:1544\n\tv_mfma_f32_16x16x32_bf16 v[160:163], a[100:103], v[124:127], v[160:163]\n\tds_read_b64 a[244:245], v0 offset:1024\n\tv_mfma_f32_16x16x32_bf16 v[140:143], a[104:107], v[100:103], v[140:143]\n\t" \
    "ds_read_b64 a[246:247], v0 offset:1032\n\tv_mfma_f32_16x16x32_bf16 v[144:147], a[104:107], v[104:107], v[144:147]\n\tds_read_b128 v[68:71], v1 offset:192\n\tv_mfma_f32_16x16x32_bf16 v[148:151], a[104:107], v[108:111], v[148:151]\n\tds_read_b128 v[72:75], v1 offset:704\n\tv_mfma_f32_16x16x32_bf16 v[152:155], a[104:107], v[112:115], v[152:155]\n\tds_read_b128 v[76:79], v1 offset:1216\n\tv_mfma_f32_16x16x32_bf16 v[156:159], a[104:107], v[116:119], v[156:159]\n\t" \
    "ds_read_b128 v[80:83], v1 offset:1728\n\tv_mfma_f32_16x16x32_bf16 v[160:163], a[104:107], v[120:123], v[160:163]\n\tbuffer_load_dword v15, v6, s[12:15], s41 offen\n\tds_read_b128 v[84:87], v1 offset:2240\n\tv_mfma_f32_16x16x32_bf16 v[144:147], a[108:111], v[100:103], v[144:147]\n\tbuffer_load_dword v15, v7, s[16:19], s42 offen\n\tds_read_b128 v[88:91], v1 offset:2752\n\tv_mfma_f32_16x16x32_bf16 v[148:151], a[108:111], v[104:107], v[148:151]\n\t" \
    "s_add_u32 s43, s43, 1\n\tds_read_b128 v[92:95], v1 offset:3264\n\tv_mfma_f32_16x16x32_bf16 v[152:155], a[108:111], v[108:111], v[152:155]\n\tv_readlane_b32 s41, v236, s43\n\tv_readlane_b32 s42, v237, s43\n\ts_nop 4\n\tds_read_b128 v[96:99], v1 offset:3776\n\tv_mfma_f32_16x16x32_bf16 v[156:159], a[108:111], v[112:115], v[156:159]\n\t" \
    "ds_read_b64 a[248:249], v0 offset:512\n\tv_mfma_f32_16x16x32_bf16 v[160:163], a[108:111], v[116:119], v[160:163]\n\tds_read_b64 a[250:251], v0 offset:520\n\tv_mfma_f32_16x16x32_bf16 v[148:151], a[112:115], v[100:103], v[148:151]\n\tds_read_b64 a[252:253], v0 offset:0\n\tv_mfma_f32_16x16x32_bf16 v[152:155], a[112:115], v[104:107], v[152:155]\n\tds_read_b64 a[254:255], v0 offset:8\n\tv_mfma_f32_16x16x32_bf16 v[156:159], a[112:115], v[108:111], v[156:159]\n\t" \
    "v_mfma_f32_16x16x32_bf16 v[160:163], a[112:115], v[112:115], v[160:163]\n\tv_mfma_f32_16x16x32_bf16 v[152:155], a[116:119], v[100:103], v[152:155]\n\tv_mfma_f32_16x16x32_bf16 v[156:159], a[116:119], v[104:107], v[156:159]\n\tv_mfma_f32_16x16x32_bf16 v[160:163], a[116:119], v[108:111], v[160:163]\n\tv_mfma_f32_16x16x32_bf16 v[156:159], a[120:123], v[100:103], v[156:159]\n\tv_mfma_f32_16x16x32_bf16 v[160:163], a[120:123], v[104:107], v[160:163]\n\tv_mfma_f32_16x16x32_bf16 v[160:163], a[124:127], v[100:103], v[160:163]\n\ts_waitcnt lgkmcnt(4)\n\t" \
    "v_mfma_f32_16x16x32_bf16 v[132:135], a[128:131], v[68:71], v[132:135]\n\tds_read_b128 v[100:103], v1 offset:128\n\ts_waitcnt vmcnt(8)\n\tv_alignbit_b32 v196, v45, v45, 16\n\tv_mfma_f32_16x16x32_bf16 v[136:139], a[128:131], v[72:75], v[136:139]\n\tds_read_b128 v[104:107], v1 offset:640\n\tv_alignbit_b32 v197, v44, v44, 16\n\tv_mfma_f32_16x16x32_bf16 v[140:143], a[128:131], v[76:79], v[140:143]\n\t" \
    "ds_read_b128 v[108:111], v1 offset:1152\n\tv_alignbit_b32 v198, v43, v43, 16\n\tv_mfma_f32_16x16x32_bf16 v[144:147], a[128:131], v[80:83], v[144:147]\n\tds_read_b128 v[112:115], v1 offset:1664\n\tv_alignbit_b32 v199, v42, v42, 16\n\tv_mfma_f32_16x16x32_bf16 v[148:151], a[128:131], v[84:87], v[148:151]\n\tds_write_b128 v2, v[196:199] offset:3072\n\tv_mfma_f32_16x16x32_bf16 v[152:155], a[128:131], v[88:91], v[152:155]\n\t" \
    "ds_read_b128 v[116:119], v1 offset:2176\n\tv_bfi_b32 v200, s46, v45, v44\n\tv_mfma_f32_16x16x32_bf16 v[156:159], a[128:131], v[92:95], v[156:159]\n\tds_read_b128 v[120:123], v1 offset:2688\n\tv_bfi_b32 v201, s46, v44, v43\n\tv_mfma_f32_16x16x32_bf16 v[160:163], a[128:131], v[96:99], v[160:163]\n\tds_read_b128 v[124:127], v1 offset:3200\n\tv_bfi_b32 v202, s46, v43, v42\n\t" \
    "v_mfma_f32_16x16x32_bf16 v[136:139], a[132:135], v[68:71], v[136:139]\n\tds_read_b128 v[128:131], v1 offset:3712\n\tv_bfi_b32 v203, s46, v42, v59\n\tv_mfma_f32_16x16x32_bf16 v[140:143], a[132:135], v[72:75], v[140:143]\n\tds_write_b128 v2, v[200:203] offset:7232\n\tv_mfma_f32_16x16x32_bf16 v[144:147], a[132:135], v[76:79], v[144:147]\n\tv_alignbit_b32 v196, v44, v44, 16\n\tv_alignbit_b32 v197, v43, v43, 16\n\t" \
    "v_mfma_f32_16x16x32_bf16 v[148:151], a[132:135], v[80:83], v[148:151]\n\tv_alignbit_b32 v198, v42, v42, 16\n\tv_alignbit_b32 v199, v59, v59, 16\n\tv_mfma_f32_16x16x32_bf16 v[152:155], a[132:135], v[84:87], v[152:155]\n\tds_write_b128 v2, v[196:199] offset:11392\n\tv_mfma_f32_16x16x32_bf16 v[156:159], a[132:135], v[88:91], v[156:159]\n\tv_bfi_b32 v200, s46, v44, v43\n\tv_bfi_b32 v201, s46, v43, v42\n\t" \
    "v_mfma_f32_16x16x32_bf16 v[160:163], a[132:135], v[92:95], v[160:163]\n\tv_bfi_b32 v202, s46, v42, v59\n\tv_bfi_b32 v203, s46, v59, v58\n\tv_mfma_f32_16x16x32_bf16 v[140:143], a[136:139], v[68:71], v[140:143]\n\tds_write_b128 v2, v[200:203] offset:15552\n\tv_mfma_f32_16x16x32_bf16 v[144:147], a[136:139], v[72:75], v[144:147]\n\tv_alignbit_b32 v196, v49, v49, 16\n\tv_alignbit_b32 v197, v48, v48, 16\n\t" \
    "v_mfma_f32_16x16x32_bf16 v[148:151], a[136:139], v[76:79], v[148:151]\n\tv_alignbit_b32 v198, v47, v47, 16\n\tv_alignbit_b32 v199, v46, v46, 16\n\tv_mfma_f32_16x16x32_bf16 v[152:155], a[136:139], v[80:83], v[152:155]\n\tds_write_b128 v2, v[196:199] offset:2048\n\tv_mfma_f32_16x16x32_bf16 v[156:159], a[136:139], v[84:87], v[156:159]\n\tv_bfi_b32 v200, s46, v49, v48\n\tv_bfi_b32 v201, s46, v48, v47\n\t" \
    "v_mfma_f32_16x16x32_bf16 v[160:163], a[136:139], v[88:91], v[160:163]\n\tv_bfi_b32 v202, s46, v47, v46\n\tv_bfi_b32 v203, s46, v46, v61\n\tv_mfma_f32_16x16x32_bf16 v[144:147], a[140:143], v[68:71], v[144:147]\n\tds_write_b128 v2, v[200:203] offset:6208\n\tv_mfma_f32_16x16x32_bf16 v[148:151], a[140:143], v[72:75], v[148:151]\n\tv_alignbit_b32 v196, v48, v48, 16\n\tv_alignbit_b32 v197, v47, v47, 16\n\t" \
    "v_mfma_f32_16x16x32_bf16 v[152:155], a[140:143], v[76:79], v[152:155]\n\tv_alignbit_b32 v198, v46, v46, 16\n\tv_alignbit_b32 v199, v61, v61, 16\n\tv_mfma_f32_16x16x32_bf16 v[156:159], a[140:143], v[80:83], v[156:159]\n\tds_write_b128 v2, v[196:199] offset:10368\n\tv_mfma_f32_16x16x32_bf16 v[160:163], a[140:143], v[84:87], v[160:163]\n\tv_bfi_b32 v200, s46, v48, v47\n\tv_bfi_b32 v201, s46, v47, v46\n\t" \
    "v_mfma_f32_16x16x32_bf16 v[148:151], a[144:147], v[68:71], v[148:151]\n\tv_bfi_b32 v202, s46, v46, v61\n\tv_bfi_b32 v203, s46, v61, v60\n\tv_mfma_f32_16x16x32_bf16 v[152:155], a[144:147], v[72:75], v[152:155]\n\tds_write_b128 v2, v[200:203] offset:14528\n\tv_mfma_f32_16x16x32_bf16 v[156:159], a[144:147], v[76:79], v[156:159]\n\tv_alignbit_b32 v196, v53, v53, 16\n\tv_alignbit_b32 v197, v52, v52, 16\n\t" \
    "v_mfma_f32_16x16x32_bf16 v[160:163], a[144:147], v[80:83], v[160:163]\n\tv_alignbit_b32 v198, v51, v51, 16\n\tv_alignbit_b32 v199, v50, v50, 16\n\tv_mfma_f32_16x16x32_bf16 v[152:155], a[148:151], v[68:71], v[152:155]\n\tds_write_b128 v2, v[196:199] offset:1024\n\tv_mfma_f32_16x16x32_bf16 v[156:159], a[148:151], v[72:75], v[156:159]\n\tv_bfi_b32 v200, s46, v53, v52\n\tv_bfi_b32 v201, s46, v52, v51\n\t" \
    "v_mfma_f32_16x16x32_bf16 v[160:163], a[148:151], v[76:79], v[160:163]\n\tv_bfi_b32 v202, s46, v51, v50\n\tv_bfi_b32 v203, s46, v50, v63\n\tv_mfma_f32_16x16x32_bf16 v[156:159], a[152:155], v[68:71], v[156:159]\n\tds_write_b128 v2, v[200:203] offset:5184\n\tv_mfma_f32_16x16x32_bf16 v[160:163], a[152:155], v[72:75], v[160:163]\n\tv_alignbit_b32 v196, v52, v52, 16\n\tv_alignbit_b32 v197, v51, v51, 16\n\t" \
    "v_mfma_f32_16x16x32_bf16 v[160:163], a[156:159], v[68:71], v[160:163]\n\tv_alignbit_b32 v198, v50, v50, 16\n\tv_alignbit_b32 v199, v63, v63, 16\n\ts_waitcnt lgkmcnt(9)\n\tv_mfma_f32_16x16x32_bf16 v[132:135], a[160:163], v[100:103], v[132:135]\n\tds_write_b128 v2, v[196:199] offset:9344\n\tv_mfma_f32_16x16x32_bf16 v[136:139], a[160:163], v[104:107], v[136:139]\n\tds_read_b128 v[68:71], v1 offset:64\n\t" \
    "v_bfi_b32 v200, s46, v52, v51\n\tv_mfma_f32_16x16x32_bf16 v[140:143], a[160:163], v[108:111], v[140:143]\n\tds_read_b128 v[72:75], v1 offset:576\n\tv_bfi_b32 v201, s46, v51, v50\n\tv_mfma_f32_16x16x32_bf16 v[144:147], a[160:163], v[112:115], v[144:147]\n\tds_read_b128 v[76:79], v1 offset:1088\n\tv_bfi_b32 v202, s46, v50, v63\n\tv_mfma_f32_16x16x32_bf16 v[148:151], a[160:163], v[116:119], v[148:151]\n\t" \
    "ds_read_b128 v[80:83], v1 offset:1600\n\tv_bfi_b32 v203, s46, v63, v62\n\tv_mfma_f32_16x16x32_bf16 v[152:155], a[160:163], v[120:123], v[152:155]\n\tds_write_b128 v2, v[200:203] offset:13504\n\tv_mfma_f32_16x16x32_bf16 v[156:159], a[160:163], v[124:127], v[156:159]\n\tds_read_b128 v[84:87], v1 offset:2112\n\tv_alignbit_b32 v196, v57, v57, 16\n\tv_mfma_f32_16x16x32_bf16 v[160:163], a[160:163], v[128:131], v[160:163]\n\t" \
    "buffer_load_dword v15, v6, s[12:15], s41 offen\n\tds_read_b128 v[88:91], v1 offset:2624\n\tv_mfma_f32_16x16x32_bf16 v[136:139], a[164:167], v[100:103], v[136:139]\n\tbuffer_load_dword v15, v7, s[16:19], s42 offen\n\tds_read_b128 v[92:95], v1 offset:3136\n\tv_mfma_f32_16x16x32_bf16 v[140:143], a[164:167], v[104:107], v[140:143]\n\ts_add_u32 s43, s43, 1\n\tds_read_b128 v[96:99], v1 offset:3648\n\t" \
    "v_alignbit_b32 v197, v56, v56, 16\n\tv_mfma_f32_16x16x32_bf16 v[144:147], a[164:167], v[108:111], v[144:147]\n\tv_readlane_b32 s41, v236, s43\n\tv_readlane_b32 s42, v237, s43\n\ts_nop 4\n\tv_alignbit_b32 v198, v55, v55, 16\n\tv_alignbit_b32 v199, v54, v54, 16\n\tv_mfma_f32_16x16x32_bf16 v[148:151], a[164:167], v[112:115], v[148:151]\n\t" \
    "ds_write_b128 v2, v[196:199] offset:0\n\tv_mfma_f32_16x16x32_bf16 v[152:155], a[164:167], v[116:119], v[152:155]\n\tv_bfi_b32 v200, s46, v57, v56\n\tv_bfi_b32 v201, s46, v56, v55\n\tv_mfma_f32_16x16x32_bf16 v[156:159], a[164:167], v[120:123], v[156:159]\n\tv_bfi_b32 v202, s46, v55, v54\n\tv_bfi_b32 v203, s46, v54, v65\n\tv_mfma_f32_16x16x32_bf16 v[160:163], a[164:167], v[124:127], v[160:163]\n\t" \
    "ds_write_b128 v2, v[200:203] offset:4160\n\tv_mfma_f32_16x16x32_bf16 v[140:143], a[168:171], v[100:103], v[140:143]\n\tv_alignbit_b32 v196, v56, v56, 16\n\tv_alignbit_b32 v197, v55, v55, 16\n\tv_mfma_f32_16x16x32_bf16 v[144:147], a[168:171], v[104:107], v[144:147]\n\tv_alignbit_b32 v198, v54, v54, 16\n\tv_alignbit_b32 v199, v65, v65, 16\n\tv_mfma_f32_16x16x32_bf16 v[148:151], a[168:171], v[108:111], v[148:151]\n\t" \
    "ds_write_b128 v2, v[196:199] offset:8320\n\tv_mfma_f32_16x16x32_bf16 v[152:155], a[168:171], v[112:115], v[152:155]\n\tv_bfi_b32 v200, s46, v56, v55\n\tv_bfi_b32 v201, s46, v55, v54\n\tv_mfma_f32_16x16x32_bf16 v[156:159], a[168:171], v[116:119], v[156:159]\n\tv_bfi_b32 v202, s46, v54, v65\n\tv_bfi_b32 v203, s46, v65, v64\n\tv_mfma_f32_16x16x32_bf16 v[160:163], a[168:171], v[120:123], v[160:163]\n\t" \
    "ds_write_b128 v2, v[200:203] offset:12480\n\tv_mfma_f32_16x16x32_bf16 v[144:147], a[172:175], v[100:103], v[144:147]\n\tv_lshl_or_b32 v66, v66, 16, v66\n\tv_and_b32 v228, v66, v232\n\tv_mfma_f32_16x16x32_bf16 v[148:151], a[172:175], v[104:107], v[148:151]\n\tv_and_b32 v229, v66, v233\n\tv_and_b32 v230, v66, v234\n\tv_mfma_f32_16x16x32_bf16 v[152:155], a[172:175], v[108:111], v[152:155]\n\t" \
    "v_and_b32 v231, v66, v235\n\tv_mfma_f32_16x16x32_bf16 v[156:159], a[172:175], v[112:115], v[156:159]\n\tv_mfma_f32_16x16x32_bf16 v[160:163], a[172:175], v[116:119], v[160:163]\n\tv_mfma_f32_16x16x32_bf16 v[148:151], a[176:179], v[100:103], v[148:151]\n\tv_mfma_f32_16x16x32_bf16 v[152:155], a[176:179], v[104:107], v[152:155]\n\tv_mfma_f32_16x16x32_bf16 v[156:159], a[176:179], v[108:111], v[156:159]\n\tv_mfma_f32_16x16x32_bf16 v[160:163], a[176:179], v[112:115], v[160:163]\n\tv_mfma_f32_16x16x32_bf16 v[152:155], a[180:183], v[100:103], v[152:155]\n\t" \
    "v_mfma_f32_16x16x32_bf16 v[156:159], a[180:183], v[104:107], v[156:159]\n\tv_mfma_f32_16x16x32_bf16 v[160:163], a[180:183], v[108:111], v[160:163]\n\tv_mfma_f32_16x16x32_bf16 v[156:159], a[184:187], v[100:103], v[156:159]\n\tv_mfma_f32_16x16x32_bf16 v[160:163], a[184:187], v[104:107], v[160:163]\n\tv_mfma_f32_16x16x32_bf16 v[160:163], a[188:191], v[100:103], v[160:163]\n\ts_waitcnt lgkmcnt(4)\n\tv_mfma_f32_16x16x32_bf16 v[132:135], a[192:195], v[68:71], v[132:135]\n\ts_add_u32 s68, s44, s38\n\t" \
    "ds_read_b128 v[100:103], v1 offset:0\n\tv_mfma_f32_16x16x32_bf16 v[136:139], a[192:195], v[72:75], v[136:139]\n\ts_add_u32 s68, s68, 8\n\tds_read_b128 v[104:107], v1 offset:512\n\tv_mfma_f32_16x16x32_bf16 v[140:143], a[192:195], v[76:79], v[140:143]\n\ts_lshl_b32 s66, s68, 1\n\tds_read_b128 v[108:111], v1 offset:1024\n\tv_mfma_f32_16x16x32_bf16 v[144:147], a[192:195], v[80:83], v[144:147]\n\t" \
    "s_lshl_b32 s68, s68, 12\n\tbuffer_load_dwordx4 v[16:19], v8, s[76:79], s68 offen offset:0\n\tds_read_b128 v[112:115], v1 offset:1536\n\tv_mfma_f32_16x16x32_bf16 v[148:151], a[192:195], v[84:87], v[148:151]\n\tds_read_b128 v[116:119], v1 offset:2048\n\tv_mfma_f32_16x16x32_bf16 v[152:155], a[192:195], v[88:91], v[152:155]\n\tds_read_b128 v[120:123], v1 offset:2560\n\tv_mfma_f32_16x16x32_bf16 v[156:159], a[192:195], v[92:95], v[156:159]\n\t" \
    "ds_read_b128 v[124:127], v1 offset:3072\n\tv_mfma_f32_16x16x32_bf16 v[160:163], a[192:195], v[96:99], v[160:163]\n\tds_read_b128 v[128:131], v1 offset:3584\n\tv_mfma_f32_16x16x32_bf16 v[136:139], a[196:199], v[68:71], v[136:139]\n\tv_mfma_f32_16x16x32_bf16 v[140:143], a[196:199], v[72:75], v[140:143]\n\tbuffer_load_dwordx4 v[20:23], v8, s[76:79], s68 offen offset:1024\n\tv_mfma_f32_16x16x32_bf16 v[144:147], a[196:199], v[76:79], v[144:147]\n\tv_mfma_f32_16x16x32_bf16 v[148:151], a[196:199], v[80:83], v[148:151]\n\t" \
    "v_mfma_f32_16x16x32_bf16 v[152:155], a[196:199], v[84:87], v[152:155]\n\tv_mfma_f32_16x16x32_bf16 v[156:159], a[196:199], v[88:91], v[156:159]\n\tv_mfma_f32_16x16x32_bf16 v[160:163], a[196:199], v[92:95], v[160:163]\n\tv_mfma_f32_16x16x32_bf16 v[140:143], a[200:203], v[68:71], v[140:143]\n\tbuffer_load_dwordx4 v[24:27], v8, s[76:79], s68 offen offset:2048\n\tv_mfma_f32_16x16x32_bf16 v[144:147], a[200:203], v[72:75], v[144:147]\n\tv_mfma_f32_16x16x32_bf16 v[148:151], a[200:203], v[76:79], v[148:151]\n\tv_mfma_f32_16x16x32_bf16 v[152:155], a[200:203], v[80:83], v[152:155]\n\t" \
    "v_mfma_f32_16x16x32_bf16 v[156:159], a[200:203], v[84:87], v[156:159]\n\tv_mfma_f32_16x16x32_bf16 v[160:163], a[200:203], v[88:91], v[160:163]\n\tv_mfma_f32_16x16x32_bf16 v[144:147], a[204:207], v[68:71], v[144:147]\n\tbuffer_load_dwordx4 v[28:31], v8, s[76:79], s68 offen offset:3072\n\tv_mfma_f32_16x16x32_bf16 v[148:151], a[204:207], v[72:75], v[148:151]\n\tv_mfma_f32_16x16x32_bf16 v[152:155], a[204:207], v[76:79], v[152:155]\n\tv_mfma_f32_16x16x32_bf16 v[156:159], a[204:207], v[80:83], v[156:159]\n\tv_mfma_f32_16x16x32_bf16 v[160:163], a[204:207], v[84:87], v[160:163]\n\t" \
    "v_mfma_f32_16x16x32_bf16 v[148:151], a[208:211], v[68:71], v[148:151]\n\tv_mfma_f32_16x16x32_bf16 v[152:155], a[208:211], v[72:75], v[152:155]\n\tbuffer_load_dwordx2 v[32:33], v13, s[76:79], s68 offen\n\tv_mfma_f32_16x16x32_bf16 v[156:159], a[208:211], v[76:79], v[156:159]\n\tv_mfma_f32_16x16x32_bf16 v[160:163], a[208:211], v[80:83], v[160:163]\n\tv_mfma_f32_16x16x32_bf16 v[152:155], a[212:215], v[68:71], v[152:155]\n\tv_mfma_f32_16x16x32_bf16 v[156:159], a[212:215], v[72:75], v[156:159]\n\tv_mfma_f32_16x16x32_bf16 v[160:163], a[212:215], v[76:79], v[160:163]\n\t" \
    "v_mfma_f32_16x16x32_bf16 v[156:159], a[216:219], v[68:71], v[156:159]\n\tbuffer_load_dwordx2 v[34:35], v8, s[76:79], s68 offen offset:1016\n\tv_mfma_f32_16x16x32_bf16 v[160:163], a[216:219], v[72:75], v[160:163]\n\tv_mfma_f32_16x16x32_bf16 v[160:163], a[220:223], v[68:71], v[160:163]\n\ts_waitcnt lgkmcnt(0)\n\tv_mfma_f32_16x16x32_bf16 v[132:135], a[224:227], v[100:103], v[132:135]\n\tds_read_b64 a[0:1], v0 offset:4032\n\tv_mfma_f32_16x16x32_bf16 v[136:139], a[224:227], v[104:107], v[136:139]\n\t" \
    "ds_read_b64 a[2:3], v0 offset:4040\n\tv_mfma_f32_16x16x32_bf16 v[140:143], a[224:227], v[108:111], v[140:143]\n\tds_read_b64 a[4:5], v0 offset:3520\n\tv_mfma_f32_16x16x32_bf16 v[144:147], a[224:227], v[112:115], v[144:147]\n\tbuffer_load_dwordx2 v[36:37], v8, s[76:79], s68 offen offset:2040\n\tds_read_b64 a[6:7], v0 offset:3528\n\tv_mfma_f32_16x16x32_bf16 v[148:151], a[224:227], v[116:119], v[148:151]\n\tds_read_b64 a[8:9], v0 offset:3008\n\t" \
    "v_mfma_f32_16x16x32_bf16 v[152:155], a[224:227], v[120:123], v[152:155]\n\tds_read_b64 a[10:11], v0 offset:3016\n\tv_mfma_f32_16x16x32_bf16 v[156:159], a[224:227], v[124:127], v[156:159]\n\tds_read_b64 a[12:13], v0 offset:2496\n\tv_mfma_f32_16x16x32_bf16 v[160:163], a[224:227], v[128:131], v[160:163]\n\tds_read_b64 a[14:15], v0 offset:2504\n\tv_mfma_f32_16x16x32_bf16 v[136:139], a[228:231], v[100:103], v[136:139]\n\tds_read_b64 a[16:17], v0 offset:1984\n\t" \
    "v_mfma_f32_16x16x32_bf16 v[140:143], a[228:231], v[104:107], v[140:143]\n\tbuffer_load_dwordx2 v[38:39], v8, s[76:79], s68 offen offset:3064\n\tds_read_b64 a[18:19], v0 offset:1992\n\tv_mfma_f32_16x16x32_bf16 v[144:147], a[228:231], v[108:111], v[144:147]\n\tds_read_b64 a[20:21], v0 offset:1472\n\tv_mfma_f32_16x16x32_bf16 v[148:151], a[228:231], v[112:115], v[148:151]\n\tds_read_b64 a[22:23], v0 offset:1480\n\tv_mfma_f32_16x16x32_bf16 v[152:155], a[228:231], v[116:119], v[152:155]\n\t" \
    "ds_read_b64 a[24:25], v0 offset:960\n\tv_mfma_f32_16x16x32_bf16 v[156:159], a[228:231], v[120:123], v[156:159]\n\tds_read_b64 a[26:27], v0 offset:968\n\tv_mfma_f32_16x16x32_bf16 v[160:163], a[228:231], v[124:127], v[160:163]\n\tds_read_b64 a[28:29], v0 offset:448\n\tv_mfma_f32_16x16x32_bf16 v[140:143], a[232:235], v[100:103], v[140:143]\n\tbuffer_load_ushort v40, off, s[80:83], s66\n\tds_read_b64 a[30:31], v0 offset:456\n\t" \
    "v_mfma_f32_16x16x32_bf16 v[144:147], a[232:235], v[104:107], v[144:147]\n\tds_read_b128 v[68:71], v1 offset:18944\n\tv_mfma_f32_16x16x32_bf16 v[148:151], a[232:235], v[108:111], v[148:151]\n\tds_read_b128 v[72:75], v1 offset:19456\n\tv_mfma_f32_16x16x32_bf16 v[152:155], a[232:235], v[112:115], v[152:155]\n\tds_read_b128 v[76:79], v1 offset:19968\n\tv_mfma_f32_16x16x32_bf16 v[156:159], a[232:235], v[116:119], v[156:159]\n\tds_read_b128 v[80:83], v1 offset:20480\n\t" \
    "v_mfma_f32_16x16x32_bf16 v[160:163], a[232:235], v[120:123], v[160:163]\n\tds_read_b128 v[84:87], v1 offset:20992\n\tv_mfma_f32_16x16x32_bf16 v[144:147], a[236:239], v[100:103], v[144:147]\n\tbuffer_load_dword v15, v6, s[12:15], s41 offen\n\tds_read_b128 v[88:91], v1 offset:21504\n\tv_mfma_f32_16x16x32_bf16 v[148:151], a[236:239], v[104:107], v[148:151]\n\tbuffer_load_dword v15, v7, s[16:19], s42 offen\n\tds_read_b128 v[92:95], v1 offset:22016\n\t" \
    "v_mfma_f32_16x16x32_bf16 v[152:155], a[236:239], v[108:111], v[152:155]\n\ts_add_u32 s43, s43, 1\n\tds_read_b128 v[96:99], v1 offset:22528\n\tv_mfma_f32_16x16x32_bf16 v[156:159], a[236:239], v[112:115], v[156:159]\n\tv_readlane_b32 s41, v236, s43\n\tv_readlane_b32 s42, v237, s43\n\ts_nop 4\n\tv_mfma_f32_16x16x32_bf16 v[160:163], a[236:239], v[116:119], v[160:163]\n\t" \
    "v_mfma_f32_16x16x32_bf16 v[148:151], a[240:243], v[100:103], v[148:151]\n\tv_mfma_f32_16x16x32_bf16 v[152:155], a[240:243], v[104:107], v[152:155]\n\tv_mfma_f32_16x16x32_bf16 v[156:159], a[240:243], v[108:111], v[156:159]\n\tv_mfma_f32_16x16x32_bf16 v[160:163], a[240:243], v[112:115], v[160:163]\n\tv_mfma_f32_16x16x32_bf16 v[152:155], a[244:247], v[100:103], v[152:155]\n\tv_mfma_f32_16x16x32_bf16 v[156:159], a[244:247], v[104:107], v[156:159]\n\tv_mfma_f32_16x16x32_bf16 v[160:163], a[244:247], v[108:111], v[160:163]\n\tv_mfma_f32_16x16x32_bf16 v[156:159], a[248:251], v[100:103], v[156:159]\n\t" \
    "v_mfma_f32_16x16x32_bf16 v[160:163], a[248:251], v[104:107], v[160:163]\n\tv_mfma_f32_16x16x32_bf16 v[160:163], a[252:255], v[100:103], v[160:163]\n\ts_waitcnt lgkmcnt(0)\n\tv_mfma_f32_16x16x32_bf16 v[164:167], v[228:231], v[68:71], 0\n\tds_read_b64 a[32:33], v0 offset:3968\n\tds_read_b64 a[34:35], v0 offset:3976\n\tv_mfma_f32_16x16x32_bf16 v[168:171], v[228:231], v[72:75], 0\n\tds_read_b64 a[36:37], v0 offset:3456\n\t" \
    "ds_read_b64 a[38:39], v0 offset:3464\n\tv_mfma_f32_16x16x32_bf16 v[172:175], v[228:231], v[76:79], 0\n\tds_read_b64 a[40:41], v0 offset:2944\n\tds_read_b64 a[42:43], v0 offset:2952\n\tv_mfma_f32_16x16x32_bf16 v[176:179], v[228:231], v[80:83], 0\n\tds_read_b64 a[44:45], v0 offset:2432\n\tds_read_b64 a[46:47], v0 offset:2440\n\tv_mfma_f32_16x16x32_bf16 v[180:183], v[228:231], v[84:87], 0\n\t" \
    "ds_read_b64 a[48:49], v0 offset:1920\n\tds_read_b64 a[50:51], v0 offset:1928\n\tv_mfma_f32_16x16x32_bf16 v[184:187], v[228:231], v[88:91], 0\n\tds_read_b64 a[52:53], v0 offset:1408\n\tv_cvt_pk_bf16_f32 v132, v132, v133\n\tv_mfma_f32_16x16x32_bf16 v[188:191], v[228:231], v[92:95], 0\n\tds_read_b64 a[54:55], v0 offset:1416\n\tv_cvt_pk_bf16_f32 v133, v134, v135\n\t" \
    "v_mfma_f32_16x16x32_bf16 v[192:195], v[228:231], v[96:99], 0\n\tds_read_b64 a[56:57], v0 offset:896\n\tds_write_b64 v3, v[132:133] offset:0\n\tv_mfma_f32_16x16x32_bf16 v[164:167], a[0:3], v[68:71], v[164:167]\n\tds_read_b64 a[58:59], v0 offset:904\n\tv_cvt_pk_bf16_f32 v136, v136, v137\n\tv_mfma_f32_16x16x32_bf16 v[168:171], a[0:3], v[72:75], v[168:171]\n\tds_read_b64 a[60:61], v0 offset:384\n\t" \
    "v_cvt_pk_bf16_f32 v137, v138, v139\n\tv_mfma_f32_16x16x32_bf16 v[172:175], a[0:3], v[76:79], v[172:175]\n\tds_read_b64 a[62:63], v0 offset:392\n\tds_write_b64 v3, v[136:137] offset:512\n\tv_mfma_f32_16x16x32_bf16 v[176:179], a[0:3], v[80:83], v[176:179]\n\tds_read_b128 v[100:103], v1 offset:18880\n\tv_cvt_pk_bf16_f32 v140, v140, v141\n\tv_mfma_f32_16x16x32_bf16 v[180:183], a[0:3], v[84:87], v[180:183]\n\t" \
    "ds_read_b128 v[104:107], v1 offset:19392\n\tv_cvt_pk_bf16_f32 v141, v142, v143\n\tv_mfma_f32_16x16x32_bf16 v[184:187], a[0:3], v[88:91], v[184:187]\n\tds_read_b128 v[108:111], v1 offset:19904\n\tds_write_b64 v3, v[140:141] offset:1024\n\tv_mfma_f32_16x16x32_bf16 v[188:191], a[0:3], v[92:95], v[188:191]\n\tds_read_b128 v[112:115], v1 offset:20416\n\tv_cvt_pk_bf16_f32 v144, v144, v145\n\t" \
    "v_mfma_f32_16x16x32_bf16 v[192:195], a[0:3], v[96:99], v[192:195]\n\tds_read_b128 v[116:119], v1 offset:20928\n\tv_cvt_pk_bf16_f32 v145, v146, v147\n\tv_mfma_f32_16x16x32_bf16 v[168:171], a[4:7], v[68:71], v[168:171]\n\tds_read_b128 v[120:123], v1 offset:21440\n\tds_write_b64 v3, v[144:145] offset:1536\n\tv_mfma_f32_16x16x32_bf16 v[172:175], a[4:7], v[72:75], v[172:175]\n\tds_read_b128 v[124:127], v1 offset:21952\n\t" \
    "v_cvt_pk_bf16_f32 v148, v148, v149\n\tv_mfma_f32_16x16x32_bf16 v[176:179], a[4:7], v[76:79], v[176:179]\n\tds_read_b128 v[128:131], v1 offset:22464\n\tv_cvt_pk_bf16_f32 v149, v150, v151\n\tv_mfma_f32_16x16x32_bf16 v[180:183], a[4:7], v[80:83], v[180:183]\n\tds_read_b64 a[64:65], v0 offset:3904\n\tds_write_b64 v3, v[148:149] offset:2048\n\tv_mfma_f32_16x16x32_bf16 v[184:187], a[4:7], v[84:87], v[184:187]\n\t" \
    "ds_read_b64 a[66:67], v0 offset:3912\n\tv_cvt_pk_bf16_f32 v152, v152, v153\n\tv_mfma_f32_16x16x32_bf16 v[188:191], a[4:7], v[88:91], v[188:191]\n\tds_read_b64 a[68:69], v0 offset:3392\n\tv_cvt_pk_bf16_f32 v153, v154, v155\n\tv_mfma_f32_16x16x32_bf16 v[192:195], a[4:7], v[92:95], v[192:195]\n\tds_read_b64 a[70:71], v0 offset:3400\n\tds_write_b64 v3, v[152:153] offset:2560\n\t" \
    "v_mfma_f32_16x16x32_bf16 v[172:175], a[8:11], v[68:71], v[172:175]\n\tds_read_b64 a[72:73], v0 offset:2880\n\tv_cvt_pk_bf16_f32 v156, v156, v157\n\tv_mfma_f32_16x16x32_bf16 v[176:179], a[8:11], v[72:75], v[176:179]\n\tds_read_b64 a[74:75], v0 offset:2888\n\tv_cvt_pk_bf16_f32 v157, v158, v159\n\tv_mfma_f32_16x16x32_bf16 v[180:183], a[8:11], v[76:79], v[180:183]\n\tds_read_b64 a[76:77], v0 offset:2368\n\t" \
    "ds_write_b64 v3, v[156:157] offset:3072\n\tv_mfma_f32_16x16x32_bf16 v[184:187], a[8:11], v[80:83], v[184:187]\n\tds_read_b64 a[78:79], v0 offset:2376\n\tv_cvt_pk_bf16_f32 v160, v160, v161\n\tv_mfma_f32_16x16x32_bf16 v[188:191], a[8:11], v[84:87], v[188:191]\n\tds_read_b64 a[80:81], v0 offset:1856\n\tv_cvt_pk_bf16_f32 v161, v162, v163\n\tv_mfma_f32_16x16x32_bf16 v[192:195], a[8:11], v[88:91], v[192:195]\n\t" \
    "ds_read_b64 a[82:83], v0 offset:1864\n\tds_write_b64 v3, v[160:161] offset:3584\n\tv_mfma_f32_16x16x32_bf16 v[176:179], a[12:15], v[68:71], v[176:179]\n\tbuffer_load_dword v15, v7, s[16:19], s42 offen\n\tds_read_b64 a[84:85], v0 offset:1344\n\tv_mfma_f32_16x16x32_bf16 v[180:183], a[12:15], v[72:75], v[180:183]\n\ts_add_u32 s43, s43, 1\n\tds_read_b64 a[86:87], v0 offset:1352\n\t" \
    "v_mfma_f32_16x16x32_bf16 v[184:187], a[12:15], v[76:79], v[184:187]\n\tv_readlane_b32 s42, v237, s43\n\ts_nop 4\n\tds_read_b64 a[88:89], v0 offset:832\n\tv_mfma_f32_16x16x32_bf16 v[188:191], a[12:15], v[80:83], v[188:191]\n\tds_read_b64 a[90:91], v0 offset:840\n\tv_mfma_f32_16x16x32_bf16 v[192:195], a[12:15], v[84:87], v[192:195]\n\tds_read_b64 a[92:93], v0 offset:320\n\t" \
    "v_mfma_f32_16x16x32_bf16 v[180:183], a[16:19], v[68:71], v[180:183]\n\tds_read_b64 a[94:95], v0 offset:328\n\tv_mfma_f32_16x16x32_bf16 v[184:187], a[16:19], v[72:75], v[184:187]\n\tds_read_b64 a[96:97], v0 offset:3840\n\tv_mfma_f32_16x16x32_bf16 v[188:191], a[16:19], v[76:79], v[188:191]\n\tds_read_b64 a[98:99], v0 offset:3848\n\tv_mfma_f32_16x16x32_bf16 v[192:195], a[16:19], v[80:83], v[192:195]\n\tds_read_b64 a[100:101], v0 offset:3328\n\t" \
    "v_mfma_f32_16x16x32_bf16 v[184:187], a[20:23], v[68:71], v[184:187]\n\tds_read_b64 a[102:103], v0 offset:3336\n\tv_mfma_f32_16x16x32_bf16 v[188:191], a[20:23], v[72:75], v[188:191]\n\tv_mfma_f32_16x16x32_bf16 v[192:195], a[20:23], v[76:79], v[192:195]\n\tv_mfma_f32_16x16x32_bf16 v[188:191], a[24:27], v[68:71], v[188:191]\n\tv_mfma_f32_16x16x32_bf16 v[192:195], a[24:27], v[72:75], v[192:195]\n\tv_mfma_f32_16x16x32_bf16 v[192:195], a[28:31], v[68:71], v[192:195]\n\ts_waitcnt lgkmcnt(10)\n\t" \
    "s_barrier\n\tv_mfma_f32_16x16x32_bf16 v[164:167], a[32:35], v[100:103], v[164:167]\n\tds_read_b64 a[104:105], v0 offset:2816\n\tds_read_b64 a[106:107], v0 offset:2824\n\tv_mfma_f32_16x16x32_bf16 v[168:171], a[32:35], v[104:107], v[168:171]\n\tds_read_b64 a[108:109], v0 offset:2304\n\tds_read_b64 a[110:111], v0 offset:2312\n\tv_mfma_f32_16x16x32_bf16 v[172:175], a[32:35], v[108:111], v[172:175]\n\t" \
    "ds_read_b64 a[112:113], v0 offset:1792\n\tds_read_b64 a[114:115], v0 offset:1800\n\tv_mfma_f32_16x16x32_bf16 v[176:179], a[32:35], v[112:115], v[176:179]\n\tds_read_b64 a[116:117], v0 offset:1280\n\tds_read_b64 a[118:119], v0 offset:1288\n\tv_mfma_f32_16x16x32_bf16 v[180:183], a[32:35], v[116:119], v[180:183]\n\tds_read_b64 a[120:121], v0 offset:768\n\tds_read_b64 a[122:123], v0 offset:776\n\t" \
    "v_mfma_f32_16x16x32_bf16 v[184:187], a[32:35], v[120:123], v[184:187]\n\tds_read_b64 a[124:125], v0 offset:256\n\ts_add_u32 s51, s44, 0\n\tv_mfma_f32_16x16x32_bf16 v[188:191], a[32:35], v[124:127], v[188:191]\n\tds_read_b64 a[126:127], v0 offset:264\n\ts_mul_i32 s65, s51, 1366\n\tv_mfma_f32_16x16x32_bf16 v[192:195], a[32:35], v[128:131], v[192:195]\n\tds_read_b64 a[128:129], v0 offset:3776\n\t" \
    "s_lshr_b32 s65, s65, 16\n\tv_mfma_f32_16x16x32_bf16 v[168:171], a[36:39], v[100:103], v[168:171]\n\tds_read_b64 a[130:131], v0 offset:3784\n\ts_mul_i32 s56, s65, 48\n\tv_mfma_f32_16x16x32_bf16 v[172:175], a[36:39], v[104:107], v[172:175]\n\tds_read_b64 a[132:133], v0 offset:3264\n\ts_sub_u32 s56, s51, s56\n\tv_mfma_f32_16x16x32_bf16 v[176:179], a[36:39], v[108:111], v[176:179]\n\t" \
    "ds_read_b64 a[134:135], v0 offset:3272\n\ts_lshl_b32 s56, s56, 6\n\tv_mfma_f32_16x16x32_bf16 v[180:183], a[36:39], v[112:115], v[180:183]\n\tds_read_b128 v[68:71], v1 offset:18816\n\ts_lshr_b32 s70, s65, 2\n\tv_mfma_f32_16x16x32_bf16 v[184:187], a[36:39], v[116:119], v[184:187]\n\tds_read_b128 v[72:75], v1 offset:19328\n\ts_mul_i32 s70, s70, 252\n\t" \
    "v_mfma_f32_16x16x32_bf16 v[188:191], a[36:39], v[120:123], v[188:191]\n\tds_read_b128 v[76:79], v1 offset:19840\n\ts_add_u32 s65, s65, s70\n\tv_mfma_f32_16x16x32_bf16 v[192:195], a[36:39], v[124:127], v[192:195]\n\tds_read_b128 v[80:83], v1 offset:20352\n\ts_mul_i32 s65, s65, 3072\n\tv_mfma_f32_16x16x32_bf16 v[172:175], a[40:43], v[100:103], v[172:175]\n\tds_read_b128 v[84:87], v1 offset:20864\n\t" \
    "s_add_u32 s56, s56, s65\n\tv_mfma_f32_16x16x32_bf16 v[176:179], a[40:43], v[104:107], v[176:179]\n\tds_read_b128 v[88:91], v1 offset:21376\n\ts_add_u32 s56, s56, 4849664\n\tv_mfma_f32_16x16x32_bf16 v[180:183], a[40:43], v[108:111], v[180:183]\n\tds_read_b128 v[92:95], v1 offset:21888\n\ts_add_u32 s51, s51, s38\n\tv_mfma_f32_16x16x32_bf16 v[184:187], a[40:43], v[112:115], v[184:187]\n\t" \
    "ds_read_b128 v[96:99], v1 offset:22400\n\ts_lshl_b32 s51, s51, 3\n\tv_mfma_f32_16x16x32_bf16 v[188:191], a[40:43], v[116:119], v[188:191]\n\tds_read_b64 a[136:137], v0 offset:2752\n\ts_add_u32 s51, s51, 50176\n\tds_read_b64_tr_b16 v[204:205], v4 offset:0\n\tv_mfma_f32_16x16x32_bf16 v[192:195], a[40:43], v[120:123], v[192:195]\n\tds_read_b64 a[138:139], v0 offset:2760\n\t" \
    "ds_read_b64_tr_b16 v[206:207], v4 offset:8\n\tv_mfma_f32_16x16x32_bf16 v[176:179], a[44:47], v[100:103], v[176:179]\n\tds_read_b64 a[140:141], v0 offset:2240\n\tds_read_b64_tr_b16 v[208:209], v4 offset:64\n\tv_mfma_f32_16x16x32_bf16 v[180:183], a[44:47], v[104:107], v[180:183]\n\tds_read_b64 a[142:143], v0 offset:2248\n\tds_read_b64_tr_b16 v[210:211], v4 offset:72\n\tv_mfma_f32_16x16x32_bf16 v[184:187], a[44:47], v[108:111], v[184:187]\n\t" \
    "ds_read_b64 a[144:145], v0 offset:1728\n\tds_read_b64_tr_b16 v[212:213], v4 offset:512\n\tv_mfma_f32_16x16x32_bf16 v[188:191], a[44:47], v[112:115], v[188:191]\n\tds_read_b64 a[146:147], v0 offset:1736\n\tds_read_b64_tr_b16 v[214:215], v4 offset:520\n\tv_mfma_f32_16x16x32_bf16 v[192:195], a[44:47], v[116:119], v[192:195]\n\tds_read_b64 a[148:149], v0 offset:1216\n\tds_read_b64_tr_b16 v[216:217], v4 offset:576\n\t" \
    "v_mfma_f32_16x16x32_bf16 v[180:183], a[48:51], v[100:103], v[180:183]\n\tds_read_b64 a[150:151], v0 offset:1224\n\tds_read_b64_tr_b16 v[218:219], v4 offset:584\n\tv_mfma_f32_16x16x32_bf16 v[184:187], a[48:51], v[104:107], v[184:187]\n\tds_read_b64 a[152:153], v0 offset:704\n\tv_mfma_f32_16x16x32_bf16 v[188:191], a[48:51], v[108:111], v[188:191]\n\tds_read_b64 a[154:155], v0 offset:712\n\tv_mfma_f32_16x16x32_bf16 v[192:195], a[48:51], v[112:115], v[192:195]\n\t" \
    "ds_read_b64 a[156:157], v0 offset:192\n\tv_mfma_f32_16x16x32_bf16 v[184:187], a[52:55], v[100:103], v[184:187]\n\tds_read_b64 a[158:159], v0 offset:200\n\tv_mfma_f32_16x16x32_bf16 v[188:191], a[52:55], v[104:107], v[188:191]\n\tv_mfma_f32_16x16x32_bf16 v[192:195], a[52:55], v[108:111], v[192:195]\n\ts_waitcnt lgkmcnt(15)\n\tbuffer_store_dwordx4 v[204:207], v5, s[8:11], s56 offen sc1\n\ts_add_u32 s56, s56, 49152\n\t" \
    "v_mfma_f32_16x16x32_bf16 v[188:191], a[56:59], v[100:103], v[188:191]\n\tv_mfma_f32_16x16x32_bf16 v[192:195], a[56:59], v[104:107], v[192:195]\n\tv_mfma_f32_16x16x32_bf16 v[192:195], a[60:63], v[100:103], v[192:195]\n\tv_mfma_f32_16x16x32_bf16 v[164:167], a[64:67], v[68:71], v[164:167]\n\tds_read_b64 a[160:161], v0 offset:3712\n\tds_read_b64 a[162:163], v0 offset:3720\n\tv_mfma_f32_16x16x32_bf16 v[168:171], a[64:67], v[72:75], v[168:171]\n\tds_read_b64 a[164:165], v0 offset:3200\n\t" \
    "ds_read_b64 a[166:167], v0 offset:3208\n\tv_mfma_f32_16x16x32_bf16 v[172:175], a[64:67], v[76:79], v[172:175]\n\tds_read_b64 a[168:169], v0 offset:2688\n\tds_read_b64 a[170:171], v0 offset:2696\n\tv_mfma_f32_16x16x32_bf16 v[176:179], a[64:67], v[80:83], v[176:179]\n\tds_read_b64 a[172:173], v0 offset:2176\n\tds_read_b64 a[174:175], v0 offset:2184\n\tv_mfma_f32_16x16x32_bf16 v[180:183], a[64:67], v[84:87], v[180:183]\n\t" \
    "ds_read_b64 a[176:177], v0 offset:1664\n\tds_read_b64 a[178:179], v0 offset:1672\n\tv_mfma_f32_16x16x32_bf16 v[184:187], a[64:67], v[88:91], v[184:187]\n\tds_read_b64 a[180:181], v0 offset:1152\n\tv_mfma_f32_16x16x32_bf16 v[188:191], a[64:67], v[92:95], v[188:191]\n\tds_read_b64 a[182:183], v0 offset:1160\n\tv_mfma_f32_16x16x32_bf16 v[192:195], a[64:67], v[96:99], v[192:195]\n\tds_read_b64 a[184:185], v0 offset:640\n\t" \
    "v_mfma_f32_16x16x32_bf16 v[168:171], a[68:71], v[68:71], v[168:171]\n\tds_read_b64 a[186:187], v0 offset:648\n\tv_mfma_f32_16x16x32_bf16 v[172:175], a[68:71], v[72:75], v[172:175]\n\tds_read_b64 a[188:189], v0 offset:128\n\tv_mfma_f32_16x16x32_bf16 v[176:179], a[68:71], v[76:79], v[176:179]\n\tds_read_b64 a[190:191], v0 offset:136\n\tv_mfma_f32_16x16x32_bf16 v[180:183], a[68:71], v[80:83], v[180:183]\n\tds_read_b128 v[100:103], v1 offset:18752\n\t" \
    "v_mfma_f32_16x16x32_bf16 v[184:187], a[68:71], v[84:87], v[184:187]\n\tds_read_b128 v[104:107], v1 offset:19264\n\tv_mfma_f32_16x16x32_bf16 v[188:191], a[68:71], v[88:91], v[188:191]\n\tds_read_b128 v[108:111], v1 offset:19776\n\tv_mfma_f32_16x16x32_bf16 v[192:195], a[68:71], v[92:95], v[192:195]\n\tds_read_b128 v[112:115], v1 offset:20288\n\ts_waitcnt lgkmcnt(15)\n\tbuffer_store_dwordx4 v[208:211], v5, s[8:11], s56 offen sc1\n\t" \
    "v_mfma_f32_16x16x32_bf16 v[172:175], a[72:75], v[68:71], v[172:175]\n\tds_read_b128 v[116:119], v1 offset:20800\n\ts_add_u32 s56, s56, 49152\n\tv_mfma_f32_16x16x32_bf16 v[176:179], a[72:75], v[72:75], v[176:179]\n\tds_read_b128 v[120:123], v1 offset:21312\n\tv_mfma_f32_16x16x32_bf16 v[180:183], a[72:75], v[76:79], v[180:183]\n\tds_read_b128 v[124:127], v1 offset:21824\n\tv_mfma_f32_16x16x32_bf16 v[184:187], a[72:75], v[80:83], v[184:187]\n\t" \
    "ds_read_b128 v[128:131], v1 offset:22336\n\tv_mfma_f32_16x16x32_bf16 v[188:191], a[72:75], v[84:87], v[188:191]\n\tds_read_b64 a[192:193], v0 offset:3648\n\tv_mfma_f32_16x16x32_bf16 v[192:195], a[72:75], v[88:91], v[192:195]\n\tds_read_b64 a[194:195], v0 offset:3656\n\tv_mfma_f32_16x16x32_bf16 v[176:179], a[76:79], v[68:71], v[176:179]\n\tds_read_b64 a[196:197], v0 offset:3136\n\tv_mfma_f32_16x16x32_bf16 v[180:183], a[76:79], v[72:75], v[180:183]\n\t" \
    "ds_read_b64 a[198:199], v0 offset:3144\n\tv_mfma_f32_16x16x32_bf16 v[184:187], a[76:79], v[76:79], v[184:187]\n\tds_read_b64 a[200:201], v0 offset:2624\n\tv_mfma_f32_16x16x32_bf16 v[188:191], a[76:79], v[80:83], v[188:191]\n\tds_read_b64 a[202:203], v0 offset:2632\n\tv_mfma_f32_16x16x32_bf16 v[192:195], a[76:79], v[84:87], v[192:195]\n\tds_read_b64 a[204:205], v0 offset:2112\n\tv_mfma_f32_16x16x32_bf16 v[180:183], a[80:83], v[68:71], v[180:183]\n\t" \
    "ds_read_b64 a[206:207], v0 offset:2120\n\tv_mfma_f32_16x16x32_bf16 v[184:187], a[80:83], v[72:75], v[184:187]\n\tds_read_b64 a[208:209], v0 offset:1600\n\tv_mfma_f32_16x16x32_bf16 v[188:191], a[80:83], v[76:79], v[188:191]\n\tds_read_b64 a[210:211], v0 offset:1608\n\tv_mfma_f32_16x16x32_bf16 v[192:195], a[80:83], v[80:83], v[192:195]\n\tds_read_b64 a[212:213], v0 offset:1088\n\tv_mfma_f32_16x16x32_bf16 v[184:187], a[84:87], v[68:71], v[184:187]\n\t" \
    "ds_read_b64 a[214:215], v0 offset:1096\n\tv_mfma_f32_16x16x32_bf16 v[188:191], a[84:87], v[72:75], v[188:191]\n\tbuffer_store_dwordx4 v[212:215], v5, s[8:11], s56 offen sc1\n\ts_add_u32 s56, s56, 49152\n\tv_mfma_f32_16x16x32_bf16 v[192:195], a[84:87], v[76:79], v[192:195]\n\tv_mfma_f32_16x16x32_bf16 v[188:191], a[88:91], v[68:71], v[188:191]\n\tv_mfma_f32_16x16x32_bf16 v[192:195], a[88:91], v[72:75], v[192:195]\n\tv_mfma_f32_16x16x32_bf16 v[192:195], a[92:95], v[68:71], v[192:195]\n\t" \
    "s_waitcnt lgkmcnt(12)\n\tv_mfma_f32_16x16x32_bf16 v[164:167], a[96:99], v[100:103], v[164:167]\n\tds_read_b64 a[216:217], v0 offset:576\n\tv_mfma_f32_16x16x32_bf16 v[168:171], a[96:99], v[104:107], v[168:171]\n\tds_read_b64 a[218:219], v0 offset:584\n\tv_mfma_f32_16x16x32_bf16 v[172:175], a[96:99], v[108:111], v[172:175]\n\tds_read_b64 a[220:221], v0 offset:64\n\tv_mfma_f32_16x16x32_bf16 v[176:179], a[96:99], v[112:115], v[176:179]\n\t" \
    "ds_read_b64 a[222:223], v0 offset:72\n\tv_mfma_f32_16x16x32_bf16 v[180:183], a[96:99], v[116:119], v[180:183]\n\tds_read_b64 a[224:225], v0 offset:3584\n\tv_mfma_f32_16x16x32_bf16 v[184:187], a[96:99], v[120:123], v[184:187]\n\tds_read_b64 a[226:227], v0 offset:3592\n\tv_mfma_f32_16x16x32_bf16 v[188:191], a[96:99], v[124:127], v[188:191]\n\tds_read_b64 a[228:229], v0 offset:3072\n\tv_mfma_f32_16x16x32_bf16 v[192:195], a[96:99], v[128:131], v[192:195]\n\t" \
    "ds_read_b64 a[230:231], v0 offset:3080\n\tbuffer_store_dwordx4 v[216:219], v5, s[8:11], s56 offen sc1\n\tv_mfma_f32_16x16x32_bf16 v[168:171], a[100:103], v[100:103], v[168:171]\n\tds_read_b64 a[232:233], v0 offset:2560\n\ts_add_u32 s56, s56, 49152\n\tv_mfma_f32_16x16x32_bf16 v[172:175], a[100:103], v[104:107], v[172:175]\n\tds_read_b64 a[234:235], v0 offset:2568\n\tv_mfma_f32_16x16x32_bf16 v[176:179], a[100:103], v[108:111], v[176:179]\n\t" \
    "ds_read_b64 a[236:237], v0 offset:2048\n\tv_mfma_f32_16x16x32_bf16 v[180:183], a[100:103], v[112:115], v[180:183]\n\tds_read_b64 a[238:239], v0 offset:2056\n\tv_mfma_f32_16x16x32_bf16 v[184:187], a[100:103], v[116:119], v[184:187]\n\tbuffer_load_dword v15, v7, s[16:19], s42 offen\n\tds_read_b64 a[240:241], v0 offset:1536\n\tv_mfma_f32_16x16x32_bf16 v[188:191], a[100:103], v[120:123], v[188:191]\n\ts_add_u32 s43, s43, 1\n\t" \
    "ds_read_b64 a[242:243], v0 offset:1544\n\tv_mfma_f32_16x16x32_bf16 v[192:195], a[100:103], v[124:127], v[192:195]\n\tv_readlane_b32 s42, v237, s43\n\ts_nop 4\n\tds_read_b64 a[244:245], v0 offset:1024\n\tv_mfma_f32_16x16x32_bf16 v[172:175], a[104:107], v[100:103], v[172:175]\n\tds_read_b64 a[246:247], v0 offset:1032\n\tv_mfma_f32_16x16x32_bf16 v[176:179], a[104:107], v[104:107], v[176:179]\n\t" \
    "ds_read_b128 v[68:71], v1 offset:18688\n\tv_mfma_f32_16x16x32_bf16 v[180:183], a[104:107], v[108:111], v[180:183]\n\tds_read_b128 v[72:75], v1 offset:19200\n\tv_mfma_f32_16x16x32_bf16 v[184:187], a[104:107], v[112:115], v[184:187]\n\tds_read_b128 v[76:79], v1 offset:19712\n\tv_mfma_f32_16x16x32_bf16 v[188:191], a[104:107], v[116:119], v[188:191]\n\tds_read_b128 v[80:83], v1 offset:20224\n\tv_mfma_f32_16x16x32_bf16 v[192:195], a[104:107], v[120:123], v[192:195]\n\t" \
    "ds_read_b128 v[84:87], v1 offset:20736\n\tv_mfma_f32_16x16x32_bf16 v[176:179], a[108:111], v[100:103], v[176:179]\n\tds_read_b128 v[88:91], v1 offset:21248\n\tv_mfma_f32_16x16x32_bf16 v[180:183], a[108:111], v[104:107], v[180:183]\n\tds_read_b128 v[92:95], v1 offset:21760\n\tv_mfma_f32_16x16x32_bf16 v[184:187], a[108:111], v[108:111], v[184:187]\n\tds_read_b128 v[96:99], v1 offset:22272\n\tv_mfma_f32_16x16x32_bf16 v[188:191], a[108:111], v[112:115], v[188:191]\n\t" \
    "ds_read_b64 a[248:249], v0 offset:512\n\tv_mfma_f32_16x16x32_bf16 v[192:195], a[108:111], v[116:119], v[192:195]\n\tds_read_b64 a[250:251], v0 offset:520\n\tv_mfma_f32_16x16x32_bf16 v[180:183], a[112:115], v[100:103], v[180:183]\n\tds_read_b64 a[252:253], v0 offset:0\n\tv_mfma_f32_16x16x32_bf16 v[184:187], a[112:115], v[104:107], v[184:187]\n\tds_read_b64 a[254:255], v0 offset:8\n\tv_mfma_f32_16x16x32_bf16 v[188:191], a[112:115], v[108:111], v[188:191]\n\t" \
    "v_mfma_f32_16x16x32_bf16 v[192:195], a[112:115], v[112:115], v[192:195]\n\tv_mfma_f32_16x16x32_bf16 v[184:187], a[116:119], v[100:103], v[184:187]\n\tv_mfma_f32_16x16x32_bf16 v[188:191], a[116:119], v[104:107], v[188:191]\n\tv_mfma_f32_16x16x32_bf16 v[192:195], a[116:119], v[108:111], v[192:195]\n\tv_mfma_f32_16x16x32_bf16 v[188:191], a[120:123], v[100:103], v[188:191]\n\tv_mfma_f32_16x16x32_bf16 v[192:195], a[120:123], v[104:107], v[192:195]\n\tv_mfma_f32_16x16x32_bf16 v[192:195], a[124:127], v[100:103], v[192:195]\n\ts_waitcnt lgkmcnt(4)\n\t" \
    "v_mfma_f32_16x16x32_bf16 v[164:167], a[128:131], v[68:71], v[164:167]\n\tds_read_b128 v[100:103], v1 offset:18624\n\ts_waitcnt vmcnt(8)\n\tv_alignbit_b32 v196, v19, v19, 16\n\tv_mfma_f32_16x16x32_bf16 v[168:171], a[128:131], v[72:75], v[168:171]\n\tds_read_b128 v[104:107], v1 offset:19136\n\tv_alignbit_b32 v197, v18, v18, 16\n\tv_mfma_f32_16x16x32_bf16 v[172:175], a[128:131], v[76:79], v[172:175]\n\t" \
    "ds_read_b128 v[108:111], v1 offset:19648\n\tv_alignbit_b32 v198, v17, v17, 16\n\tv_mfma_f32_16x16x32_bf16 v[176:179], a[128:131], v[80:83], v[176:179]\n\tds_read_b128 v[112:115], v1 offset:20160\n\tv_alignbit_b32 v199, v16, v16, 16\n\tv_mfma_f32_16x16x32_bf16 v[180:183], a[128:131], v[84:87], v[180:183]\n\tds_write_b128 v2, v[196:199] offset:3072\n\tv_mfma_f32_16x16x32_bf16 v[184:187], a[128:131], v[88:91], v[184:187]\n\t" \
    "ds_read_b128 v[116:119], v1 offset:20672\n\tv_bfi_b32 v200, s46, v19, v18\n\tv_mfma_f32_16x16x32_bf16 v[188:191], a[128:131], v[92:95], v[188:191]\n\tds_read_b128 v[120:123], v1 offset:21184\n\tv_bfi_b32 v201, s46, v18, v17\n\tv_mfma_f32_16x16x32_bf16 v[192:195], a[128:131], v[96:99], v[192:195]\n\tds_read_b128 v[124:127], v1 offset:21696\n\tv_bfi_b32 v202, s46, v17, v16\n\t" \
    "v_mfma_f32_16x16x32_bf16 v[168:171], a[132:135], v[68:71], v[168:171]\n\tds_read_b128 v[128:131], v1 offset:22208\n\tv_bfi_b32 v203, s46, v16, v33\n\tv_mfma_f32_16x16x32_bf16 v[172:175], a[132:135], v[72:75], v[172:175]\n\tds_write_b128 v2, v[200:203] offset:7232\n\tv_mfma_f32_16x16x32_bf16 v[176:179], a[132:135], v[76:79], v[176:179]\n\tv_alignbit_b32 v196, v18, v18, 16\n\tv_alignbit_b32 v197, v17, v17, 16\n\t" \
    "v_mfma_f32_16x16x32_bf16 v[180:183], a[132:135], v[80:83], v[180:183]\n\tv_alignbit_b32 v198, v16, v16, 16\n\tv_alignbit_b32 v199, v33, v33, 16\n\tv_mfma_f32_16x16x32_bf16 v[184:187], a[132:135], v[84:87], v[184:187]\n\tds_write_b128 v2, v[196:199] offset:11392\n\tv_mfma_f32_16x16x32_bf16 v[188:191], a[132:135], v[88:91], v[188:191]\n\tv_bfi_b32 v200, s46, v18, v17\n\tv_bfi_b32 v201, s46, v17, v16\n\t" \
    "v_mfma_f32_16x16x32_bf16 v[192:195], a[132:135], v[92:95], v[192:195]\n\tv_bfi_b32 v202, s46, v16, v33\n\tv_bfi_b32 v203, s46, v33, v32\n\tv_mfma_f32_16x16x32_bf16 v[172:175], a[136:139], v[68:71], v[172:175]\n\tds_write_b128 v2, v[200:203] offset:15552\n\tv_mfma_f32_16x16x32_bf16 v[176:179], a[136:139], v[72:75], v[176:179]\n\tv_alignbit_b32 v196, v23, v23, 16\n\tv_alignbit_b32 v197, v22, v22, 16\n\t" \
    "v_mfma_f32_16x16x32_bf16 v[180:183], a[136:139], v[76:79], v[180:183]\n\tv_alignbit_b32 v198, v21, v21, 16\n\tv_alignbit_b32 v199, v20, v20, 16\n\tv_mfma_f32_16x16x32_bf16 v[184:187], a[136:139], v[80:83], v[184:187]\n\tds_write_b128 v2, v[196:199] offset:2048\n\tv_mfma_f32_16x16x32_bf16 v[188:191], a[136:139], v[84:87], v[188:191]\n\tv_bfi_b32 v200, s46, v23, v22\n\tv_bfi_b32 v201, s46, v22, v21\n\t" \
    "v_mfma_f32_16x16x32_bf16 v[192:195], a[136:139], v[88:91], v[192:195]\n\tv_bfi_b32 v202, s46, v21, v20\n\tv_bfi_b32 v203, s46, v20, v35\n\tv_mfma_f32_16x16x32_bf16 v[176:179], a[140:143], v[68:71], v[176:179]\n\tds_write_b128 v2, v[200:203] offset:6208\n\tv_mfma_f32_16x16x32_bf16 v[180:183], a[140:143], v[72:75], v[180:183]\n\tv_alignbit_b32 v196, v22, v22, 16\n\tv_alignbit_b32 v197, v21, v21, 16\n\t" \
    "v_mfma_f32_16x16x32_bf16 v[184:187], a[140:143], v[76:79], v[184:187]\n\tv_alignbit_b32 v198, v20, v20, 16\n\tv_alignbit_b32 v199, v35, v35, 16\n\tv_mfma_f32_16x16x32_bf16 v[188:191], a[140:143], v[80:83], v[188:191]\n\tds_write_b128 v2, v[196:199] offset:10368\n\tv_mfma_f32_16x16x32_bf16 v[192:195], a[140:143], v[84:87], v[192:195]\n\tv_bfi_b32 v200, s46, v22, v21\n\tv_bfi_b32 v201, s46, v21, v20\n\t" \
    "v_mfma_f32_16x16x32_bf16 v[180:183], a[144:147], v[68:71], v[180:183]\n\tv_bfi_b32 v202, s46, v20, v35\n\tv_bfi_b32 v203, s46, v35, v34\n\tv_mfma_f32_16x16x32_bf16 v[184:187], a[144:147], v[72:75], v[184:187]\n\tds_write_b128 v2, v[200:203] offset:14528\n\tv_mfma_f32_16x16x32_bf16 v[188:191], a[144:147], v[76:79], v[188:191]\n\tv_alignbit_b32 v196, v27, v27, 16\n\tv_alignbit_b32 v197, v26, v26, 16\n\t" \
    "v_mfma_f32_16x16x32_bf16 v[192:195], a[144:147], v[80:83], v[192:195]\n\tv_alignbit_b32 v198, v25, v25, 16\n\tv_alignbit_b32 v199, v24, v24, 16\n\tv_mfma_f32_16x16x32_bf16 v[184:187], a[148:151], v[68:71], v[184:187]\n\tds_write_b128 v2, v[196:199] offset:1024\n\tv_mfma_f32_16x16x32_bf16 v[188:191], a[148:151], v[72:75], v[188:191]\n\tv_bfi_b32 v200, s46, v27, v26\n\tv_bfi_b32 v201, s46, v26, v25\n\t" \
    "v_mfma_f32_16x16x32_bf16 v[192:195], a[148:151], v[76:79], v[192:195]\n\tv_bfi_b32 v202, s46, v25, v24\n\tv_bfi_b32 v203, s46, v24, v37\n\tv_mfma_f32_16x16x32_bf16 v[188:191], a[152:155], v[68:71], v[188:191]\n\tds_write_b128 v2, v[200:203] offset:5184\n\tv_mfma_f32_16x16x32_bf16 v[192:195], a[152:155], v[72:75], v[192:195]\n\tv_alignbit_b32 v196, v26, v26, 16\n\tv_alignbit_b32 v197, v25, v25, 16\n\t" \
    "v_mfma_f32_16x16x32_bf16 v[192:195], a[156:159], v[68:71], v[192:195]\n\tv_alignbit_b32 v198, v24, v24, 16\n\tv_alignbit_b32 v199, v37, v37, 16\n\ts_waitcnt lgkmcnt(9)\n\tv_mfma_f32_16x16x32_bf16 v[164:167], a[160:163], v[100:103], v[164:167]\n\tds_write_b128 v2, v[196:199] offset:9344\n\tv_mfma_f32_16x16x32_bf16 v[168:171], a[160:163], v[104:107], v[168:171]\n\tds_read_b128 v[68:71], v1 offset:18560\n\t" \
    "v_bfi_b32 v200, s46, v26, v25\n\tv_mfma_f32_16x16x32_bf16 v[172:175], a[160:163], v[108:111], v[172:175]\n\tds_read_b128 v[72:75], v1 offset:19072\n\tv_bfi_b32 v201, s46, v25, v24\n\tv_mfma_f32_16x16x32_bf16 v[176:179], a[160:163], v[112:115], v[176:179]\n\tds_read_b128 v[76:79], v1 offset:19584\n\tv_bfi_b32 v202, s46, v24, v37\n\tv_mfma_f32_16x16x32_bf16 v[180:183], a[160:163], v[116:119], v[180:183]\n\t" \
    "ds_read_b128 v[80:83], v1 offset:20096\n\tv_bfi_b32 v203, s46, v37, v36\n\tv_mfma_f32_16x16x32_bf16 v[184:187], a[160:163], v[120:123], v[184:187]\n\tds_write_b128 v2, v[200:203] offset:13504\n\tv_mfma_f32_16x16x32_bf16 v[188:191], a[160:163], v[124:127], v[188:191]\n\tds_read_b128 v[84:87], v1 offset:20608\n\tv_alignbit_b32 v196, v31, v31, 16\n\tv_mfma_f32_16x16x32_bf16 v[192:195], a[160:163], v[128:131], v[192:195]\n\t" \
    "ds_read_b128 v[88:91], v1 offset:21120\n\tv_alignbit_b32 v197, v30, v30, 16\n\tv_mfma_f32_16x16x32_bf16 v[168:171], a[164:167], v[100:103], v[168:171]\n\tds_read_b128 v[92:95], v1 offset:21632\n\tv_alignbit_b32 v198, v29, v29, 16\n\tv_mfma_f32_16x16x32_bf16 v[172:175], a[164:167], v[104:107], v[172:175]\n\tds_read_b128 v[96:99], v1 offset:22144\n\tv_alignbit_b32 v199, v28, v28, 16\n\t" \
    "v_mfma_f32_16x16x32_bf16 v[176:179], a[164:167], v[108:111], v[176:179]\n\tds_write_b128 v2, v[196:199] offset:0\n\tv_mfma_f32_16x16x32_bf16 v[180:183], a[164:167], v[112:115], v[180:183]\n\tv_bfi_b32 v200, s46, v31, v30\n\tv_bfi_b32 v201, s46, v30, v29\n\tv_mfma_f32_16x16x32_bf16 v[184:187], a[164:167], v[116:119], v[184:187]\n\tv_bfi_b32 v202, s46, v29, v28\n\tv_bfi_b32 v203, s46, v28, v39\n\t" \
    "v_mfma_f32_16x16x32_bf16 v[188:191], a[164:167], v[120:123], v[188:191]\n\tds_write_b128 v2, v[200:203] offset:4160\n\tv_mfma_f32_16x16x32_bf16 v[192:195], a[164:167], v[124:127], v[192:195]\n\tv_alignbit_b32 v196, v30, v30, 16\n\tv_alignbit_b32 v197, v29, v29, 16\n\tv_mfma_f32_16x16x32_bf16 v[172:175], a[168:171], v[100:103], v[172:175]\n\tv_alignbit_b32 v198, v28, v28, 16\n\tv_alignbit_b32 v199, v39, v39, 16\n\t" \
    "v_mfma_f32_16x16x32_bf16 v[176:179], a[168:171], v[104:107], v[176:179]\n\tds_write_b128 v2, v[196:199] offset:8320\n\tv_mfma_f32_16x16x32_bf16 v[180:183], a[168:171], v[108:111], v[180:183]\n\tv_bfi_b32 v200, s46, v30, v29\n\tv_bfi_b32 v201, s46, v29, v28\n\tv_mfma_f32_16x16x32_bf16 v[184:187], a[168:171], v[112:115], v[184:187]\n\tv_bfi_b32 v202, s46, v28, v39\n\tv_bfi_b32 v203, s46, v39, v38\n\t" \
    "v_mfma_f32_16x16x32_bf16 v[188:191], a[168:171], v[116:119], v[188:191]\n\tds_write_b128 v2, v[200:203] offset:12480\n\tv_mfma_f32_16x16x32_bf16 v[192:195], a[168:171], v[120:123], v[192:195]\n\tv_lshl_or_b32 v40, v40, 16, v40\n\tv_and_b32 v228, v40, v232\n\tv_mfma_f32_16x16x32_bf16 v[176:179], a[172:175], v[100:103], v[176:179]\n\tv_and_b32 v229, v40, v233\n\tv_and_b32 v230, v40, v234\n\t" \
    "v_mfma_f32_16x16x32_bf16 v[180:183], a[172:175], v[104:107], v[180:183]\n\tv_and_b32 v231, v40, v235\n\tv_mfma_f32_16x16x32_bf16 v[184:187], a[172:175], v[108:111], v[184:187]\n\tv_mfma_f32_16x16x32_bf16 v[188:191], a[172:175], v[112:115], v[188:191]\n\tv_mfma_f32_16x16x32_bf16 v[192:195], a[172:175], v[116:119], v[192:195]\n\tv_mfma_f32_16x16x32_bf16 v[180:183], a[176:179], v[100:103], v[180:183]\n\tv_mfma_f32_16x16x32_bf16 v[184:187], a[176:179], v[104:107], v[184:187]\n\tv_mfma_f32_16x16x32_bf16 v[188:191], a[176:179], v[108:111], v[188:191]\n\t" \
    "v_mfma_f32_16x16x32_bf16 v[192:195], a[176:179], v[112:115], v[192:195]\n\tv_mfma_f32_16x16x32_bf16 v[184:187], a[180:183], v[100:103], v[184:187]\n\tv_mfma_f32_16x16x32_bf16 v[188:191], a[180:183], v[104:107], v[188:191]\n\tv_mfma_f32_16x16x32_bf16 v[192:195], a[180:183], v[108:111], v[192:195]\n\tv_mfma_f32_16x16x32_bf16 v[188:191], a[184:187], v[100:103], v[188:191]\n\tv_mfma_f32_16x16x32_bf16 v[192:195], a[184:187], v[104:107], v[192:195]\n\tv_mfma_f32_16x16x32_bf16 v[192:195], a[188:191], v[100:103], v[192:195]\n\ts_waitcnt lgkmcnt(4)\n\t" \
    "v_mfma_f32_16x16x32_bf16 v[164:167], a[192:195], v[68:71], v[164:167]\n\ts_add_u32 s68, s44, s38\n\tds_read_b128 v[100:103], v1 offset:18496\n\tv_mfma_f32_16x16x32_bf16 v[168:171], a[192:195], v[72:75], v[168:171]\n\ts_add_u32 s68, s68, 12\n\tds_read_b128 v[104:107], v1 offset:19008\n\tv_mfma_f32_16x16x32_bf16 v[172:175], a[192:195], v[76:79], v[172:175]\n\ts_lshl_b32 s66, s68, 1\n\t" \
    "ds_read_b128 v[108:111], v1 offset:19520\n\tv_mfma_f32_16x16x32_bf16 v[176:179], a[192:195], v[80:83], v[176:179]\n\ts_lshl_b32 s68, s68, 12\n\tds_read_b128 v[112:115], v1 offset:20032\n\tv_mfma_f32_16x16x32_bf16 v[180:183], a[192:195], v[84:87], v[180:183]\n\ts_or_b32 s68, s68, s45\n\tds_read_b128 v[116:119], v1 offset:20544\n\tv_mfma_f32_16x16x32_bf16 v[184:187], a[192:195], v[88:91], v[184:187]\n\t" \
    "s_or_b32 s66, s66, s45\n\tbuffer_load_dwordx4 v[42:45], v8, s[76:79], s68 offen offset:0\n\tds_read_b128 v[120:123], v1 offset:21056\n\tv_mfma_f32_16x16x32_bf16 v[188:191], a[192:195], v[92:95], v[188:191]\n\tds_read_b128 v[124:127], v1 offset:21568\n\tv_mfma_f32_16x16x32_bf16 v[192:195], a[192:195], v[96:99], v[192:195]\n\tds_read_b128 v[128:131], v1 offset:22080\n\tv_mfma_f32_16x16x32_bf16 v[168:171], a[196:199], v[68:71], v[168:171]\n\t" \
    "v_mfma_f32_16x16x32_bf16 v[172:175], a[196:199], v[72:75], v[172:175]\n\tv_mfma_f32_16x16x32_bf16 v[176:179], a[196:199], v[76:79], v[176:179]\n\tv_mfma_f32_16x16x32_bf16 v[180:183], a[196:199], v[80:83], v[180:183]\n\tbuffer_load_dwordx4 v[46:49], v8, s[76:79], s68 offen offset:1024\n\tv_mfma_f32_16x16x32_bf16 v[184:187], a[196:199], v[84:87], v[184:187]\n\tv_mfma_f32_16x16x32_bf16 v[188:191], a[196:199], v[88:91], v[188:191]\n\tv_mfma_f32_16x16x32_bf16 v[192:195], a[196:199], v[92:95], v[192:195]\n\tv_mfma_f32_16x16x32_bf16 v[172:175], a[200:203], v[68:71], v[172:175]\n\t" \
    "v_mfma_f32_16x16x32_bf16 v[176:179], a[200:203], v[72:75], v[176:179]\n\tv_mfma_f32_16x16x32_bf16 v[180:183], a[200:203], v[76:79], v[180:183]\n\tbuffer_load_dwordx4 v[50:53], v8, s[76:79], s68 offen offset:2048\n\tv_mfma_f32_16x16x32_bf16 v[184:187], a[200:203], v[80:83], v[184:187]\n\tv_mfma_f32_16x16x32_bf16 v[188:191], a[200:203], v[84:87], v[188:191]\n\tv_mfma_f32_16x16x32_bf16 v[192:195], a[200:203], v[88:91], v[192:195]\n\tv_mfma_f32_16x16x32_bf16 v[176:179], a[204:207], v[68:71], v[176:179]\n\tv_mfma_f32_16x16x32_bf16 v[180:183], a[204:207], v[72:75], v[180:183]\n\t" \
    "v_mfma_f32_16x16x32_bf16 v[184:187], a[204:207], v[76:79], v[184:187]\n\tbuffer_load_dwordx4 v[54:57], v8, s[76:79], s68 offen offset:3072\n\tv_mfma_f32_16x16x32_bf16 v[188:191], a[204:207], v[80:83], v[188:191]\n\tv_mfma_f32_16x16x32_bf16 v[192:195], a[204:207], v[84:87], v[192:195]\n\tv_mfma_f32_16x16x32_bf16 v[180:183], a[208:211], v[68:71], v[180:183]\n\tv_mfma_f32_16x16x32_bf16 v[184:187], a[208:211], v[72:75], v[184:187]\n\tv_mfma_f32_16x16x32_bf16 v[188:191], a[208:211], v[76:79], v[188:191]\n\tv_mfma_f32_16x16x32_bf16 v[192:195], a[208:211], v[80:83], v[192:195]\n\t" \
    "buffer_load_dwordx2 v[58:59], v13, s[76:79], s68 offen\n\tv_mfma_f32_16x16x32_bf16 v[184:187], a[212:215], v[68:71], v[184:187]\n\tv_mfma_f32_16x16x32_bf16 v[188:191], a[212:215], v[72:75], v[188:191]\n\tv_mfma_f32_16x16x32_bf16 v[192:195], a[212:215], v[76:79], v[192:195]\n\tv_mfma_f32_16x16x32_bf16 v[188:191], a[216:219], v[68:71], v[188:191]\n\tv_mfma_f32_16x16x32_bf16 v[192:195], a[216:219], v[72:75], v[192:195]\n\tv_mfma_f32_16x16x32_bf16 v[192:195], a[220:223], v[68:71], v[192:195]\n\tbuffer_load_dwordx2 v[60:61], v8, s[76:79], s68 offen offset:1016\n\t" \
    "s_waitcnt lgkmcnt(0)\n\tv_mfma_f32_16x16x32_bf16 v[164:167], a[224:227], v[100:103], v[164:167]\n\tds_read_b64 a[0:1], v0 offset:4032\n\tv_mfma_f32_16x16x32_bf16 v[168:171], a[224:227], v[104:107], v[168:171]\n\tds_read_b64 a[2:3], v0 offset:4040\n\tv_mfma_f32_16x16x32_bf16 v[172:175], a[224:227], v[108:111], v[172:175]\n\tds_read_b64 a[4:5], v0 offset:3520\n\tv_mfma_f32_16x16x32_bf16 v[176:179], a[224:227], v[112:115], v[176:179]\n\t" \
    "ds_read_b64 a[6:7], v0 offset:3528\n\tv_mfma_f32_16x16x32_bf16 v[180:183], a[224:227], v[116:119], v[180:183]\n\tds_read_b64 a[8:9], v0 offset:3008\n\tv_mfma_f32_16x16x32_bf16 v[184:187], a[224:227], v[120:123], v[184:187]\n\tbuffer_load_dwordx2 v[62:63], v8, s[76:79], s68 offen offset:2040\n\tds_read_b64 a[10:11], v0 offset:3016\n\tv_mfma_f32_16x16x32_bf16 v[188:191], a[224:227], v[124:127], v[188:191]\n\tds_read_b64 a[12:13], v0 offset:2496\n\t" \
    "v_mfma_f32_16x16x32_bf16 v[192:195], a[224:227], v[128:131], v[192:195]\n\tds_read_b64 a[14:15], v0 offset:2504\n\tv_mfma_f32_16x16x32_bf16 v[168:171], a[228:231], v[100:103], v[168:171]\n\tds_read_b64 a[16:17], v0 offset:1984\n\tv_mfma_f32_16x16x32_bf16 v[172:175], a[228:231], v[104:107], v[172:175]\n\tds_read_b64 a[18:19], v0 offset:1992\n\tv_mfma_f32_16x16x32_bf16 v[176:179], a[228:231], v[108:111], v[176:179]\n\tds_read_b64 a[20:21], v0 offset:1472\n\t" \
    "v_mfma_f32_16x16x32_bf16 v[180:183], a[228:231], v[112:115], v[180:183]\n\tbuffer_load_dwordx2 v[64:65], v8, s[76:79], s68 offen offset:3064\n\tds_read_b64 a[22:23], v0 offset:1480\n\tv_mfma_f32_16x16x32_bf16 v[184:187], a[228:231], v[116:119], v[184:187]\n\tds_read_b64 a[24:25], v0 offset:960\n\tv_mfma_f32_16x16x32_bf16 v[188:191], a[228:231], v[120:123], v[188:191]\n\tds_read_b64 a[26:27], v0 offset:968\n\tv_mfma_f32_16x16x32_bf16 v[192:195], a[228:231], v[124:127], v[192:195]\n\t" \
    "ds_read_b64 a[28:29], v0 offset:448\n\tv_mfma_f32_16x16x32_bf16 v[172:175], a[232:235], v[100:103], v[172:175]\n\tds_read_b64 a[30:31], v0 offset:456\n\tv_mfma_f32_16x16x32_bf16 v[176:179], a[232:235], v[104:107], v[176:179]\n\tds_read_b128 v[68:71], v1 offset:37440\n\tv_mfma_f32_16x16x32_bf16 v[180:183], a[232:235], v[108:111], v[180:183]\n\tbuffer_load_ushort v66, off, s[80:83], s66\n\tds_read_b128 v[72:75], v1 offset:37952\n\t" \
    "v_mfma_f32_16x16x32_bf16 v[184:187], a[232:235], v[112:115], v[184:187]\n\tds_read_b128 v[76:79], v1 offset:38464\n\tv_mfma_f32_16x16x32_bf16 v[188:191], a[232:235], v[116:119], v[188:191]\n\tds_read_b128 v[80:83], v1 offset:38976\n\tv_mfma_f32_16x16x32_bf16 v[192:195], a[232:235], v[120:123], v[192:195]\n\tds_read_b128 v[84:87], v1 offset:39488\n\tv_mfma_f32_16x16x32_bf16 v[176:179], a[236:239], v[100:103], v[176:179]\n\tds_read_b128 v[88:91], v1 offset:40000\n\t" \
    "v_mfma_f32_16x16x32_bf16 v[180:183], a[236:239], v[104:107], v[180:183]\n\tds_read_b128 v[92:95], v1 offset:40512\n\tv_mfma_f32_16x16x32_bf16 v[184:187], a[236:239], v[108:111], v[184:187]\n\ts_lshl_b32 s69, s67, 13\n\tbuffer_load_dword v15, v14, s[4:7], s69 offen\n\tds_read_b128 v[96:99], v1 offset:41024\n\tv_mfma_f32_16x16x32_bf16 v[188:191], a[236:239], v[112:115], v[188:191]\n\ts_waitcnt vmcnt(11)\n\t" \
    "buffer_store_dwordx2 v[248:249], off, s[8:11], s51 sc1\n\tv_mfma_f32_16x16x32_bf16 v[192:195], a[236:239], v[116:119], v[192:195]\n\tv_mfma_f32_16x16x32_bf16 v[180:183], a[240:243], v[100:103], v[180:183]\n\tv_mfma_f32_16x16x32_bf16 v[184:187], a[240:243], v[104:107], v[184:187]\n\tv_mfma_f32_16x16x32_bf16 v[188:191], a[240:243], v[108:111], v[188:191]\n\tv_mfma_f32_16x16x32_bf16 v[192:195], a[240:243], v[112:115], v[192:195]\n\tv_mfma_f32_16x16x32_bf16 v[184:187], a[244:247], v[100:103], v[184:187]\n\tv_mfma_f32_16x16x32_bf16 v[188:191], a[244:247], v[104:107], v[188:191]\n\t" \
    "v_mfma_f32_16x16x32_bf16 v[192:195], a[244:247], v[108:111], v[192:195]\n\tv_mfma_f32_16x16x32_bf16 v[188:191], a[248:251], v[100:103], v[188:191]\n\tv_mfma_f32_16x16x32_bf16 v[192:195], a[248:251], v[104:107], v[192:195]\n\tv_mfma_f32_16x16x32_bf16 v[192:195], a[252:255], v[100:103], v[192:195]\n\ts_waitcnt lgkmcnt(0)\n\tv_mfma_f32_16x16x32_bf16 v[132:135], v[228:231], v[68:71], 0\n\tds_read_b64 a[32:33], v0 offset:3968\n\tds_read_b64 a[34:35], v0 offset:3976\n\t" \
    "v_mfma_f32_16x16x32_bf16 v[136:139], v[228:231], v[72:75], 0\n\tds_read_b64 a[36:37], v0 offset:3456\n\tds_read_b64 a[38:39], v0 offset:3464\n\tv_mfma_f32_16x16x32_bf16 v[140:143], v[228:231], v[76:79], 0\n\tds_read_b64 a[40:41], v0 offset:2944\n\tds_read_b64 a[42:43], v0 offset:2952\n\tv_mfma_f32_16x16x32_bf16 v[144:147], v[228:231], v[80:83], 0\n\tds_read_b64 a[44:45], v0 offset:2432\n\t" \
    "ds_read_b64 a[46:47], v0 offset:2440\n\tv_mfma_f32_16x16x32_bf16 v[148:151], v[228:231], v[84:87], 0\n\tds_read_b64 a[48:49], v0 offset:1920\n\tds_read_b64 a[50:51], v0 offset:1928\n\tv_mfma_f32_16x16x32_bf16 v[152:155], v[228:231], v[88:91], 0\n\tds_read_b64 a[52:53], v0 offset:1408\n\tv_cvt_pk_bf16_f32 v164, v164, v165\n\tv_mfma_f32_16x16x32_bf16 v[156:159], v[228:231], v[92:95], 0\n\t" \
    "ds_read_b64 a[54:55], v0 offset:1416\n\tv_cvt_pk_bf16_f32 v165, v166, v167\n\tv_mfma_f32_16x16x32_bf16 v[160:163], v[228:231], v[96:99], 0\n\tds_read_b64 a[56:57], v0 offset:896\n\tds_write_b64 v3, v[164:165] offset:18496\n\tv_mfma_f32_16x16x32_bf16 v[132:135], a[0:3], v[68:71], v[132:135]\n\tds_read_b64 a[58:59], v0 offset:904\n\tv_cvt_pk_bf16_f32 v168, v168, v169\n\t" \
    "v_mfma_f32_16x16x32_bf16 v[136:139], a[0:3], v[72:75], v[136:139]\n\tds_read_b64 a[60:61], v0 offset:384\n\tv_cvt_pk_bf16_f32 v169, v170, v171\n\tv_mfma_f32_16x16x32_bf16 v[140:143], a[0:3], v[76:79], v[140:143]\n\tds_read_b64 a[62:63], v0 offset:392\n\tds_write_b64 v3, v[168:169] offset:19008\n\tv_mfma_f32_16x16x32_bf16 v[144:147], a[0:3], v[80:83], v[144:147]\n\tds_read_b128 v[100:103], v1 offset:37376\n\t" \
    "v_cvt_pk_bf16_f32 v172, v172, v173\n\tv_mfma_f32_16x16x32_bf16 v[148:151], a[0:3], v[84:87], v[148:151]\n\tds_read_b128 v[104:107], v1 offset:37888\n\tv_cvt_pk_bf16_f32 v173, v174, v175\n\tv_mfma_f32_16x16x32_bf16 v[152:155], a[0:3], v[88:91], v[152:155]\n\tds_read_b128 v[108:111], v1 offset:38400\n\tds_write_b64 v3, v[172:173] offset:19520\n\tv_mfma_f32_16x16x32_bf16 v[156:159], a[0:3], v[92:95], v[156:159]\n\t" \
    "ds_read_b128 v[112:115], v1 offset:38912\n\tv_cvt_pk_bf16_f32 v176, v176, v177\n\tv_mfma_f32_16x16x32_bf16 v[160:163], a[0:3], v[96:99], v[160:163]\n\tds_read_b128 v[116:119], v1 offset:39424\n\tv_cvt_pk_bf16_f32 v177, v178, v179\n\tv_mfma_f32_16x16x32_bf16 v[136:139], a[4:7], v[68:71], v[136:139]\n\tds_read_b128 v[120:123], v1 offset:39936\n\tds_write_b64 v3, v[176:177] offset:20032\n\t" \
    "v_mfma_f32_16x16x32_bf16 v[140:143], a[4:7], v[72:75], v[140:143]\n\tbuffer_load_dword v15, v7, s[16:19], s42 offen\n\tds_read_b128 v[124:127], v1 offset:40448\n\tv_mfma_f32_16x16x32_bf16 v[144:147], a[4:7], v[76:79], v[144:147]\n\ts_add_u32 s43, s43, 1\n\tds_read_b128 v[128:131], v1 offset:40960\n\tv_cvt_pk_bf16_f32 v180, v180, v181\n\tv_mfma_f32_16x16x32_bf16 v[148:151], a[4:7], v[80:83], v[148:151]\n\t" \
    "v_readlane_b32 s42, v237, s43\n\ts_nop 4\n\tds_read_b64 a[64:65], v0 offset:3904\n\tv_cvt_pk_bf16_f32 v181, v182, v183\n\tv_mfma_f32_16x16x32_bf16 v[152:155], a[4:7], v[84:87], v[152:155]\n\tds_read_b64 a[66:67], v0 offset:3912\n\tds_write_b64 v3, v[180:181] offset:20544\n\tv_mfma_f32_16x16x32_bf16 v[156:159], a[4:7], v[88:91], v[156:159]\n\t" \
    "ds_read_b64 a[68:69], v0 offset:3392\n\tv_cvt_pk_bf16_f32 v184, v184, v185\n\tv_mfma_f32_16x16x32_bf16 v[160:163], a[4:7], v[92:95], v[160:163]\n\tds_read_b64 a[70:71], v0 offset:3400\n\tv_cvt_pk_bf16_f32 v185, v186, v187\n\tv_mfma_f32_16x16x32_bf16 v[140:143], a[8:11], v[68:71], v[140:143]\n\tds_read_b64 a[72:73], v0 offset:2880\n\tds_write_b64 v3, v[184:185] offset:21056\n\t" \
    "v_mfma_f32_16x16x32_bf16 v[144:147], a[8:11], v[72:75], v[144:147]\n\tds_read_b64 a[74:75], v0 offset:2888\n\tv_cvt_pk_bf16_f32 v188, v188, v189\n\tv_mfma_f32_16x16x32_bf16 v[148:151], a[8:11], v[76:79], v[148:151]\n\tds_read_b64 a[76:77], v0 offset:2368\n\tv_cvt_pk_bf16_f32 v189, v190, v191\n\tv_mfma_f32_16x16x32_bf16 v[152:155], a[8:11], v[80:83], v[152:155]\n\tds_read_b64 a[78:79], v0 offset:2376\n\t" \
    "ds_write_b64 v3, v[188:189] offset:21568\n\tv_mfma_f32_16x16x32_bf16 v[156:159], a[8:11], v[84:87], v[156:159]\n\tds_read_b64 a[80:81], v0 offset:1856\n\tv_cvt_pk_bf16_f32 v192, v192, v193\n\tv_mfma_f32_16x16x32_bf16 v[160:163], a[8:11], v[88:91], v[160:163]\n\tds_read_b64 a[82:83], v0 offset:1864\n\tv_cvt_pk_bf16_f32 v193, v194, v195\n\tv_mfma_f32_16x16x32_bf16 v[144:147], a[12:15], v[68:71], v[144:147]\n\t" \
    "ds_read_b64 a[84:85], v0 offset:1344\n\tds_write_b64 v3, v[192:193] offset:22080\n\tv_mfma_f32_16x16x32_bf16 v[148:151], a[12:15], v[72:75], v[148:151]\n\tds_read_b64 a[86:87], v0 offset:1352\n\tv_mfma_f32_16x16x32_bf16 v[152:155], a[12:15], v[76:79], v[152:155]\n\tds_read_b64 a[88:89], v0 offset:832\n\tv_mfma_f32_16x16x32_bf16 v[156:159], a[12:15], v[80:83], v[156:159]\n\tds_read_b64 a[90:91], v0 offset:840\n\t" \
    "v_mfma_f32_16x16x32_bf16 v[160:163], a[12:15], v[84:87], v[160:163]\n\tds_read_b64 a[92:93], v0 offset:320\n\tv_mfma_f32_16x16x32_bf16 v[148:151], a[16:19], v[68:71], v[148:151]\n\tds_read_b64 a[94:95], v0 offset:328\n\tv_mfma_f32_16x16x32_bf16 v[152:155], a[16:19], v[72:75], v[152:155]\n\tds_read_b64 a[96:97], v0 offset:3840\n\tv_mfma_f32_16x16x32_bf16 v[156:159], a[16:19], v[76:79], v[156:159]\n\tds_read_b64 a[98:99], v0 offset:3848\n\t" \
    "v_mfma_f32_16x16x32_bf16 v[160:163], a[16:19], v[80:83], v[160:163]\n\tds_read_b64 a[100:101], v0 offset:3328\n\tv_mfma_f32_16x16x32_bf16 v[152:155], a[20:23], v[68:71], v[152:155]\n\tds_read_b64 a[102:103], v0 offset:3336\n\tv_mfma_f32_16x16x32_bf16 v[156:159], a[20:23], v[72:75], v[156:159]\n\tv_mfma_f32_16x16x32_bf16 v[160:163], a[20:23], v[76:79], v[160:163]\n\tv_mfma_f32_16x16x32_bf16 v[156:159], a[24:27], v[68:71], v[156:159]\n\tv_mfma_f32_16x16x32_bf16 v[160:163], a[24:27], v[72:75], v[160:163]\n\t" \
    "v_mfma_f32_16x16x32_bf16 v[160:163], a[28:31], v[68:71], v[160:163]\n\ts_waitcnt lgkmcnt(9)\n\ts_barrier\n\tv_mfma_f32_16x16x32_bf16 v[132:135], a[32:35], v[100:103], v[132:135]\n\tds_read_b64 a[104:105], v0 offset:2816\n\tds_read_b64 a[106:107], v0 offset:2824\n\tv_mfma_f32_16x16x32_bf16 v[136:139], a[32:35], v[104:107], v[136:139]\n\tds_read_b64 a[108:109], v0 offset:2304\n\t" \
    "ds_read_b64 a[110:111], v0 offset:2312\n\tv_mfma_f32_16x16x32_bf16 v[140:143], a[32:35], v[108:111], v[140:143]\n\tds_read_b64 a[112:113], v0 offset:1792\n\tds_read_b64 a[114:115], v0 offset:1800\n\tv_mfma_f32_16x16x32_bf16 v[144:147], a[32:35], v[112:115], v[144:147]\n\tds_read_b64 a[116:117], v0 offset:1280\n\tds_read_b64 a[118:119], v0 offset:1288\n\tv_mfma_f32_16x16x32_bf16 v[148:151], a[32:35], v[116:119], v[148:151]\n\t" \
    "ds_read_b64 a[120:121], v0 offset:768\n\tds_read_b64 a[122:123], v0 offset:776\n\tv_mfma_f32_16x16x32_bf16 v[152:155], a[32:35], v[120:123], v[152:155]\n\tds_read_b64 a[124:125], v0 offset:256\n\ts_add_u32 s51, s44, 4\n\tv_mfma_f32_16x16x32_bf16 v[156:159], a[32:35], v[124:127], v[156:159]\n\tds_read_b64 a[126:127], v0 offset:264\n\ts_mul_i32 s65, s51, 1366\n\t" \
    "v_mfma_f32_16x16x32_bf16 v[160:163], a[32:35], v[128:131], v[160:163]\n\tds_read_b64 a[128:129], v0 offset:3776\n\ts_lshr_b32 s65, s65, 16\n\tv_mfma_f32_16x16x32_bf16 v[136:139], a[36:39], v[100:103], v[136:139]\n\tds_read_b64 a[130:131], v0 offset:3784\n\ts_mul_i32 s56, s65, 48\n\tv_mfma_f32_16x16x32_bf16 v[140:143], a[36:39], v[104:107], v[140:143]\n\tds_read_b64 a[132:133], v0 offset:3264\n\t" \
    "s_sub_u32 s56, s51, s56\n\tv_mfma_f32_16x16x32_bf16 v[144:147], a[36:39], v[108:111], v[144:147]\n\tds_read_b64 a[134:135], v0 offset:3272\n\ts_lshl_b32 s56, s56, 6\n\tv_mfma_f32_16x16x32_bf16 v[148:151], a[36:39], v[112:115], v[148:151]\n\tds_read_b128 v[68:71], v1 offset:37312\n\ts_lshr_b32 s70, s65, 2\n\tv_mfma_f32_16x16x32_bf16 v[152:155], a[36:39], v[116:119], v[152:155]\n\t" \
    "ds_read_b128 v[72:75], v1 offset:37824\n\ts_mul_i32 s70, s70, 252\n\tv_mfma_f32_16x16x32_bf16 v[156:159], a[36:39], v[120:123], v[156:159]\n\tds_read_b128 v[76:79], v1 offset:38336\n\ts_add_u32 s65, s65, s70\n\tv_mfma_f32_16x16x32_bf16 v[160:163], a[36:39], v[124:127], v[160:163]\n\tds_read_b128 v[80:83], v1 offset:38848\n\ts_mul_i32 s65, s65, 3072\n\t" \
    "v_mfma_f32_16x16x32_bf16 v[140:143], a[40:43], v[100:103], v[140:143]\n\tds_read_b128 v[84:87], v1 offset:39360\n\ts_add_u32 s56, s56, s65\n\tv_mfma_f32_16x16x32_bf16 v[144:147], a[40:43], v[104:107], v[144:147]\n\tds_read_b128 v[88:91], v1 offset:39872\n\ts_add_u32 s56, s56, 4849664\n\tv_mfma_f32_16x16x32_bf16 v[148:151], a[40:43], v[108:111], v[148:151]\n\tds_read_b128 v[92:95], v1 offset:40384\n\t" \
    "s_add_u32 s51, s51, s38\n\tv_mfma_f32_16x16x32_bf16 v[152:155], a[40:43], v[112:115], v[152:155]\n\tds_read_b128 v[96:99], v1 offset:40896\n\ts_lshl_b32 s51, s51, 3\n\tv_mfma_f32_16x16x32_bf16 v[156:159], a[40:43], v[116:119], v[156:159]\n\tds_read_b64 a[136:137], v0 offset:2752\n\ts_add_u32 s51, s51, 50176\n\tds_read_b64_tr_b16 v[204:205], v4 offset:18496\n\t" \
    "v_mfma_f32_16x16x32_bf16 v[160:163], a[40:43], v[120:123], v[160:163]\n\tds_read_b64 a[138:139], v0 offset:2760\n\tds_read_b64_tr_b16 v[206:207], v4 offset:18504\n\tv_mfma_f32_16x16x32_bf16 v[144:147], a[44:47], v[100:103], v[144:147]\n\tds_read_b64 a[140:141], v0 offset:2240\n\tds_read_b64_tr_b16 v[208:209], v4 offset:18560\n\tv_mfma_f32_16x16x32_bf16 v[148:151], a[44:47], v[104:107], v[148:151]\n\tds_read_b64 a[142:143], v0 offset:2248\n\t" \
    "ds_read_b64_tr_b16 v[210:211], v4 offset:18568\n\tv_mfma_f32_16x16x32_bf16 v[152:155], a[44:47], v[108:111], v[152:155]\n\tds_read_b64 a[144:145], v0 offset:1728\n\tds_read_b64_tr_b16 v[212:213], v4 offset:19008\n\tv_mfma_f32_16x16x32_bf16 v[156:159], a[44:47], v[112:115], v[156:159]\n\tds_read_b64 a[146:147], v0 offset:1736\n\tds_read_b64_tr_b16 v[214:215], v4 offset:19016\n\tv_mfma_f32_16x16x32_bf16 v[160:163], a[44:47], v[116:119], v[160:163]\n\t" \
    "ds_read_b64 a[148:149], v0 offset:1216\n\tds_read_b64_tr_b16 v[216:217], v4 offset:19072\n\tv_mfma_f32_16x16x32_bf16 v[148:151], a[48:51], v[100:103], v[148:151]\n\tds_read_b64 a[150:151], v0 offset:1224\n\tds_read_b64_tr_b16 v[218:219], v4 offset:19080\n\tv_mfma_f32_16x16x32_bf16 v[152:155], a[48:51], v[104:107], v[152:155]\n\tds_read_b64 a[152:153], v0 offset:704\n\tv_mfma_f32_16x16x32_bf16 v[156:159], a[48:51], v[108:111], v[156:159]\n\t" \
    "ds_read_b64 a[154:155], v0 offset:712\n\tv_mfma_f32_16x16x32_bf16 v[160:163], a[48:51], v[112:115], v[160:163]\n\tds_read_b64 a[156:157], v0 offset:192\n\tv_mfma_f32_16x16x32_bf16 v[152:155], a[52:55], v[100:103], v[152:155]\n\tds_read_b64 a[158:159], v0 offset:200\n\tv_mfma_f32_16x16x32_bf16 v[156:159], a[52:55], v[104:107], v[156:159]\n\tv_mfma_f32_16x16x32_bf16 v[160:163], a[52:55], v[108:111], v[160:163]\n\tbuffer_load_dword v15, v7, s[16:19], s42 offen\n\t" \
    "s_waitcnt lgkmcnt(15)\n\tbuffer_store_dwordx4 v[204:207], v5, s[8:11], s56 offen sc1\n\tv_mfma_f32_16x16x32_bf16 v[156:159], a[56:59], v[100:103], v[156:159]\n\ts_add_u32 s43, s43, 1\n\ts_add_u32 s56, s56, 49152\n\tv_mfma_f32_16x16x32_bf16 v[160:163], a[56:59], v[104:107], v[160:163]\n\tv_readlane_b32 s42, v237, s43\n\ts_nop 4\n\t" \
    "v_mfma_f32_16x16x32_bf16 v[160:163], a[60:63], v[100:103], v[160:163]\n\tv_mfma_f32_16x16x32_bf16 v[132:135], a[64:67], v[68:71], v[132:135]\n\tds_read_b64 a[160:161], v0 offset:3712\n\tds_read_b64 a[162:163], v0 offset:3720\n\tv_mfma_f32_16x16x32_bf16 v[136:139], a[64:67], v[72:75], v[136:139]\n\tds_read_b64 a[164:165], v0 offset:3200\n\tds_read_b64 a[166:167], v0 offset:3208\n\tv_mfma_f32_16x16x32_bf16 v[140:143], a[64:67], v[76:79], v[140:143]\n\t" \
    "ds_read_b64 a[168:169], v0 offset:2688\n\tds_read_b64 a[170:171], v0 offset:2696\n\tv_mfma_f32_16x16x32_bf16 v[144:147], a[64:67], v[80:83], v[144:147]\n\tds_read_b64 a[172:173], v0 offset:2176\n\tds_read_b64 a[174:175], v0 offset:2184\n\tv_mfma_f32_16x16x32_bf16 v[148:151], a[64:67], v[84:87], v[148:151]\n\tds_read_b64 a[176:177], v0 offset:1664\n\tds_read_b64 a[178:179], v0 offset:1672\n\t" \
    "v_mfma_f32_16x16x32_bf16 v[152:155], a[64:67], v[88:91], v[152:155]\n\tds_read_b64 a[180:181], v0 offset:1152\n\tv_mfma_f32_16x16x32_bf16 v[156:159], a[64:67], v[92:95], v[156:159]\n\tds_read_b64 a[182:183], v0 offset:1160\n\tv_mfma_f32_16x16x32_bf16 v[160:163], a[64:67], v[96:99], v[160:163]\n\tds_read_b64 a[184:185], v0 offset:640\n\tv_mfma_f32_16x16x32_bf16 v[136:139], a[68:71], v[68:71], v[136:139]\n\tds_read_b64 a[186:187], v0 offset:648\n\t" \
    "v_mfma_f32_16x16x32_bf16 v[140:143], a[68:71], v[72:75], v[140:143]\n\tds_read_b64 a[188:189], v0 offset:128\n\tv_mfma_f32_16x16x32_bf16 v[144:147], a[68:71], v[76:79], v[144:147]\n\tds_read_b64 a[190:191], v0 offset:136\n\tv_mfma_f32_16x16x32_bf16 v[148:151], a[68:71], v[80:83], v[148:151]\n\tds_read_b128 v[100:103], v1 offset:37248\n\tv_mfma_f32_16x16x32_bf16 v[152:155], a[68:71], v[84:87], v[152:155]\n\tds_read_b128 v[104:107], v1 offset:37760\n\t" \
    "v_mfma_f32_16x16x32_bf16 v[156:159], a[68:71], v[88:91], v[156:159]\n\tds_read_b128 v[108:111], v1 offset:38272\n\tv_mfma_f32_16x16x32_bf16 v[160:163], a[68:71], v[92:95], v[160:163]\n\tds_read_b128 v[112:115], v1 offset:38784\n\tv_mfma_f32_16x16x32_bf16 v[140:143], a[72:75], v[68:71], v[140:143]\n\tds_read_b128 v[116:119], v1 offset:39296\n\ts_waitcnt lgkmcnt(15)\n\tbuffer_store_dwordx4 v[208:211], v5, s[8:11], s56 offen sc1\n\t" \
    "v_mfma_f32_16x16x32_bf16 v[144:147], a[72:75], v[72:75], v[144:147]\n\tds_read_b128 v[120:123], v1 offset:39808\n\ts_add_u32 s56, s56, 49152\n\tv_mfma_f32_16x16x32_bf16 v[148:151], a[72:75], v[76:79], v[148:151]\n\tds_read_b128 v[124:127], v1 offset:40320\n\tv_mfma_f32_16x16x32_bf16 v[152:155], a[72:75], v[80:83], v[152:155]\n\tds_read_b128 v[128:131], v1 offset:40832\n\tv_mfma_f32_16x16x32_bf16 v[156:159], a[72:75], v[84:87], v[156:159]\n\t" \
    "ds_read_b64 a[192:193], v0 offset:3648\n\tv_mfma_f32_16x16x32_bf16 v[160:163], a[72:75], v[88:91], v[160:163]\n\tds_read_b64 a[194:195], v0 offset:3656\n\tv_mfma_f32_16x16x32_bf16 v[144:147], a[76:79], v[68:71], v[144:147]\n\tds_read_b64 a[196:197], v0 offset:3136\n\tv_mfma_f32_16x16x32_bf16 v[148:151], a[76:79], v[72:75], v[148:151]\n\tds_read_b64 a[198:199], v0 offset:3144\n\tv_mfma_f32_16x16x32_bf16 v[152:155], a[76:79], v[76:79], v[152:155]\n\t" \
    "ds_read_b64 a[200:201], v0 offset:2624\n\tv_mfma_f32_16x16x32_bf16 v[156:159], a[76:79], v[80:83], v[156:159]\n\tds_read_b64 a[202:203], v0 offset:2632\n\tv_mfma_f32_16x16x32_bf16 v[160:163], a[76:79], v[84:87], v[160:163]\n\tds_read_b64 a[204:205], v0 offset:2112\n\tv_mfma_f32_16x16x32_bf16 v[148:151], a[80:83], v[68:71], v[148:151]\n\tds_read_b64 a[206:207], v0 offset:2120\n\tv_mfma_f32_16x16x32_bf16 v[152:155], a[80:83], v[72:75], v[152:155]\n\t" \
    "ds_read_b64 a[208:209], v0 offset:1600\n\tv_mfma_f32_16x16x32_bf16 v[156:159], a[80:83], v[76:79], v[156:159]\n\tds_read_b64 a[210:211], v0 offset:1608\n\tv_mfma_f32_16x16x32_bf16 v[160:163], a[80:83], v[80:83], v[160:163]\n\tds_read_b64 a[212:213], v0 offset:1088\n\tv_mfma_f32_16x16x32_bf16 v[152:155], a[84:87], v[68:71], v[152:155]\n\tds_read_b64 a[214:215], v0 offset:1096\n\tv_mfma_f32_16x16x32_bf16 v[156:159], a[84:87], v[72:75], v[156:159]\n\t" \
    "buffer_store_dwordx4 v[212:215], v5, s[8:11], s56 offen sc1\n\tv_mfma_f32_16x16x32_bf16 v[160:163], a[84:87], v[76:79], v[160:163]\n\ts_add_u32 s56, s56, 49152\n\tv_mfma_f32_16x16x32_bf16 v[156:159], a[88:91], v[68:71], v[156:159]\n\tv_mfma_f32_16x16x32_bf16 v[160:163], a[88:91], v[72:75], v[160:163]\n\tv_mfma_f32_16x16x32_bf16 v[160:163], a[92:95], v[68:71], v[160:163]\n\ts_waitcnt lgkmcnt(12)\n\tv_mfma_f32_16x16x32_bf16 v[132:135], a[96:99], v[100:103], v[132:135]\n\t" \
    "ds_read_b64 a[216:217], v0 offset:576\n\tv_mfma_f32_16x16x32_bf16 v[136:139], a[96:99], v[104:107], v[136:139]\n\tds_read_b64 a[218:219], v0 offset:584\n\tv_mfma_f32_16x16x32_bf16 v[140:143], a[96:99], v[108:111], v[140:143]\n\tds_read_b64 a[220:221], v0 offset:64\n\tv_mfma_f32_16x16x32_bf16 v[144:147], a[96:99], v[112:115], v[144:147]\n\tds_read_b64 a[222:223], v0 offset:72\n\tv_mfma_f32_16x16x32_bf16 v[148:151], a[96:99], v[116:119], v[148:151]\n\t" \
    "ds_read_b64 a[224:225], v0 offset:3584\n\tv_mfma_f32_16x16x32_bf16 v[152:155], a[96:99], v[120:123], v[152:155]\n\tds_read_b64 a[226:227], v0 offset:3592\n\tv_mfma_f32_16x16x32_bf16 v[156:159], a[96:99], v[124:127], v[156:159]\n\tds_read_b64 a[228:229], v0 offset:3072\n\tv_mfma_f32_16x16x32_bf16 v[160:163], a[96:99], v[128:131], v[160:163]\n\tds_read_b64 a[230:231], v0 offset:3080\n\tv_mfma_f32_16x16x32_bf16 v[136:139], a[100:103], v[100:103], v[136:139]\n\t" \
    "ds_read_b64 a[232:233], v0 offset:2560\n\tbuffer_store_dwordx4 v[216:219], v5, s[8:11], s56 offen sc1\n\tv_mfma_f32_16x16x32_bf16 v[140:143], a[100:103], v[104:107], v[140:143]\n\tds_read_b64 a[234:235], v0 offset:2568\n\ts_add_u32 s56, s56, 49152\n\tv_mfma_f32_16x16x32_bf16 v[144:147], a[100:103], v[108:111], v[144:147]\n\tds_read_b64 a[236:237], v0 offset:2048\n\tv_mfma_f32_16x16x32_bf16 v[148:151], a[100:103], v[112:115], v[148:151]\n\t" \
    "ds_read_b64 a[238:239], v0 offset:2056\n\tv_mfma_f32_16x16x32_bf16 v[152:155], a[100:103], v[116:119], v[152:155]\n\tds_read_b64 a[240:241], v0 offset:1536\n\tv_mfma_f32_16x16x32_bf16 v[156:159], a[100:103], v[120:123], v[156:159]\n\tds_read_b64 a[242:243], v0 offset:1544\n\tv_mfma_f32_16x16x32_bf16 v[160:163], a[100:103], v[124:127], v[160:163]\n\tds_read_b64 a[244:245], v0 offset:1024\n\tv_mfma_f32_16x16x32_bf16 v[140:143], a[104:107], v[100:103], v[140:143]\n\t" \
    "ds_read_b64 a[246:247], v0 offset:1032\n\tv_mfma_f32_16x16x32_bf16 v[144:147], a[104:107], v[104:107], v[144:147]\n\tds_read_b128 v[68:71], v1 offset:37184\n\tv_mfma_f32_16x16x32_bf16 v[148:151], a[104:107], v[108:111], v[148:151]\n\tds_read_b128 v[72:75], v1 offset:37696\n\tv_mfma_f32_16x16x32_bf16 v[152:155], a[104:107], v[112:115], v[152:155]\n\tds_read_b128 v[76:79], v1 offset:38208\n\tv_mfma_f32_16x16x32_bf16 v[156:159], a[104:107], v[116:119], v[156:159]\n\t" \
    "ds_read_b128 v[80:83], v1 offset:38720\n\tv_mfma_f32_16x16x32_bf16 v[160:163], a[104:107], v[120:123], v[160:163]\n\tbuffer_load_dword v15, v7, s[16:19], s42 offen\n\tds_read_b128 v[84:87], v1 offset:39232\n\tv_mfma_f32_16x16x32_bf16 v[144:147], a[108:111], v[100:103], v[144:147]\n\ts_add_u32 s43, s43, 1\n\tds_read_b128 v[88:91], v1 offset:39744\n\tv_mfma_f32_16x16x32_bf16 v[148:151], a[108:111], v[104:107], v[148:151]\n\t" \
    "v_readlane_b32 s42, v237, s43\n\ts_nop 4\n\tds_read_b128 v[92:95], v1 offset:40256\n\tv_mfma_f32_16x16x32_bf16 v[152:155], a[108:111], v[108:111], v[152:155]\n\tds_read_b128 v[96:99], v1 offset:40768\n\tv_mfma_f32_16x16x32_bf16 v[156:159], a[108:111], v[112:115], v[156:159]\n\tds_read_b64 a[248:249], v0 offset:512\n\tv_mfma_f32_16x16x32_bf16 v[160:163], a[108:111], v[116:119], v[160:163]\n\t" \
    "ds_read_b64 a[250:251], v0 offset:520\n\tv_mfma_f32_16x16x32_bf16 v[148:151], a[112:115], v[100:103], v[148:151]\n\tds_read_b64 a[252:253], v0 offset:0\n\tv_mfma_f32_16x16x32_bf16 v[152:155], a[112:115], v[104:107], v[152:155]\n\tds_read_b64 a[254:255], v0 offset:8\n\tv_mfma_f32_16x16x32_bf16 v[156:159], a[112:115], v[108:111], v[156:159]\n\tv_mfma_f32_16x16x32_bf16 v[160:163], a[112:115], v[112:115], v[160:163]\n\tv_mfma_f32_16x16x32_bf16 v[152:155], a[116:119], v[100:103], v[152:155]\n\t" \
    "v_mfma_f32_16x16x32_bf16 v[156:159], a[116:119], v[104:107], v[156:159]\n\tv_mfma_f32_16x16x32_bf16 v[160:163], a[116:119], v[108:111], v[160:163]\n\tv_mfma_f32_16x16x32_bf16 v[156:159], a[120:123], v[100:103], v[156:159]\n\tv_mfma_f32_16x16x32_bf16 v[160:163], a[120:123], v[104:107], v[160:163]\n\tv_mfma_f32_16x16x32_bf16 v[160:163], a[124:127], v[100:103], v[160:163]\n\ts_waitcnt lgkmcnt(4)\n\tv_mfma_f32_16x16x32_bf16 v[132:135], a[128:131], v[68:71], v[132:135]\n\tds_read_b128 v[100:103], v1 offset:37120\n\t" \
    "s_waitcnt vmcnt(9)\n\tv_alignbit_b32 v196, v45, v45, 16\n\tv_mfma_f32_16x16x32_bf16 v[136:139], a[128:131], v[72:75], v[136:139]\n\tds_read_b128 v[104:107], v1 offset:37632\n\tv_alignbit_b32 v197, v44, v44, 16\n\tv_mfma_f32_16x16x32_bf16 v[140:143], a[128:131], v[76:79], v[140:143]\n\tds_read_b128 v[108:111], v1 offset:38144\n\tv_alignbit_b32 v198, v43, v43, 16\n\t" \
    "v_mfma_f32_16x16x32_bf16 v[144:147], a[128:131], v[80:83], v[144:147]\n\tds_read_b128 v[112:115], v1 offset:38656\n\tv_alignbit_b32 v199, v42, v42, 16\n\tv_mfma_f32_16x16x32_bf16 v[148:151], a[128:131], v[84:87], v[148:151]\n\tds_write_b128 v2, v[196:199] offset:3072\n\tv_mfma_f32_16x16x32_bf16 v[152:155], a[128:131], v[88:91], v[152:155]\n\tds_read_b128 v[116:119], v1 offset:39168\n\tv_bfi_b32 v200, s46, v45, v44\n\t" \
    "v_mfma_f32_16x16x32_bf16 v[156:159], a[128:131], v[92:95], v[156:159]\n\tds_read_b128 v[120:123], v1 offset:39680\n\tv_bfi_b32 v201, s46, v44, v43\n\tv_mfma_f32_16x16x32_bf16 v[160:163], a[128:131], v[96:99], v[160:163]\n\tds_read_b128 v[124:127], v1 offset:40192\n\tv_bfi_b32 v202, s46, v43, v42\n\tv_mfma_f32_16x16x32_bf16 v[136:139], a[132:135], v[68:71], v[136:139]\n\tds_read_b128 v[128:131], v1 offset:40704\n\t" \
    "v_bfi_b32 v203, s46, v42, v59\n\tv_mfma_f32_16x16x32_bf16 v[140:143], a[132:135], v[72:75], v[140:143]\n\tds_write_b128 v2, v[200:203] offset:7232\n\tv_mfma_f32_16x16x32_bf16 v[144:147], a[132:135], v[76:79], v[144:147]\n\tv_alignbit_b32 v196, v44, v44, 16\n\tv_alignbit_b32 v197, v43, v43, 16\n\tv_mfma_f32_16x16x32_bf16 v[148:151], a[132:135], v[80:83], v[148:151]\n\tv_alignbit_b32 v198, v42, v42, 16\n\t" \
    "v_alignbit_b32 v199, v59, v59, 16\n\tv_mfma_f32_16x16x32_bf16 v[152:155], a[132:135], v[84:87], v[152:155]\n\tds_write_b128 v2, v[196:199] offset:11392\n\tv_mfma_f32_16x16x32_bf16 v[156:159], a[132:135], v[88:91], v[156:159]\n\tv_bfi_b32 v200, s46, v44, v43\n\tv_bfi_b32 v201, s46, v43, v42\n\tv_mfma_f32_16x16x32_bf16 v[160:163], a[132:135], v[92:95], v[160:163]\n\tv_bfi_b32 v202, s46, v42, v59\n\t" \
    "v_bfi_b32 v203, s46, v59, v58\n\tv_mfma_f32_16x16x32_bf16 v[140:143], a[136:139], v[68:71], v[140:143]\n\tds_write_b128 v2, v[200:203] offset:15552\n\tv_mfma_f32_16x16x32_bf16 v[144:147], a[136:139], v[72:75], v[144:147]\n\tv_alignbit_b32 v196, v49, v49, 16\n\tv_alignbit_b32 v197, v48, v48, 16\n\tv_mfma_f32_16x16x32_bf16 v[148:151], a[136:139], v[76:79], v[148:151]\n\tv_alignbit_b32 v198, v47, v47, 16\n\t" \
    "v_alignbit_b32 v199, v46, v46, 16\n\tv_mfma_f32_16x16x32_bf16 v[152:155], a[136:139], v[80:83], v[152:155]\n\tds_write_b128 v2, v[196:199] offset:2048\n\tv_mfma_f32_16x16x32_bf16 v[156:159], a[136:139], v[84:87], v[156:159]\n\tv_bfi_b32 v200, s46, v49, v48\n\tv_bfi_b32 v201, s46, v48, v47\n\tv_mfma_f32_16x16x32_bf16 v[160:163], a[136:139], v[88:91], v[160:163]\n\tv_bfi_b32 v202, s46, v47, v46\n\t" \
    "v_bfi_b32 v203, s46, v46, v61\n\tv_mfma_f32_16x16x32_bf16 v[144:147], a[140:143], v[68:71], v[144:147]\n\tds_write_b128 v2, v[200:203] offset:6208\n\tv_mfma_f32_16x16x32_bf16 v[148:151], a[140:143], v[72:75], v[148:151]\n\tv_alignbit_b32 v196, v48, v48, 16\n\tv_alignbit_b32 v197, v47, v47, 16\n\tv_mfma_f32_16x16x32_bf16 v[152:155], a[140:143], v[76:79], v[152:155]\n\tv_alignbit_b32 v198, v46, v46, 16\n\t" \
    "v_alignbit_b32 v199, v61, v61, 16\n\tv_mfma_f32_16x16x32_bf16 v[156:159], a[140:143], v[80:83], v[156:159]\n\tds_write_b128 v2, v[196:199] offset:10368\n\tv_mfma_f32_16x16x32_bf16 v[160:163], a[140:143], v[84:87], v[160:163]\n\tv_bfi_b32 v200, s46, v48, v47\n\tv_bfi_b32 v201, s46, v47, v46\n\tv_mfma_f32_16x16x32_bf16 v[148:151], a[144:147], v[68:71], v[148:151]\n\tv_bfi_b32 v202, s46, v46, v61\n\t" \
    "v_bfi_b32 v203, s46, v61, v60\n\tv_mfma_f32_16x16x32_bf16 v[152:155], a[144:147], v[72:75], v[152:155]\n\tds_write_b128 v2, v[200:203] offset:14528\n\tv_mfma_f32_16x16x32_bf16 v[156:159], a[144:147], v[76:79], v[156:159]\n\tv_alignbit_b32 v196, v53, v53, 16\n\tv_alignbit_b32 v197, v52, v52, 16\n\tv_mfma_f32_16x16x32_bf16 v[160:163], a[144:147], v[80:83], v[160:163]\n\tv_alignbit_b32 v198, v51, v51, 16\n\t" \
    "v_alignbit_b32 v199, v50, v50, 16\n\tv_mfma_f32_16x16x32_bf16 v[152:155], a[148:151], v[68:71], v[152:155]\n\tds_write_b128 v2, v[196:199] offset:1024\n\tv_mfma_f32_16x16x32_bf16 v[156:159], a[148:151], v[72:75], v[156:159]\n\tv_bfi_b32 v200, s46, v53, v52\n\tv_bfi_b32 v201, s46, v52, v51\n\tv_mfma_f32_16x16x32_bf16 v[160:163], a[148:151], v[76:79], v[160:163]\n\tv_bfi_b32 v202, s46, v51, v50\n\t" \
    "v_bfi_b32 v203, s46, v50, v63\n\tv_mfma_f32_16x16x32_bf16 v[156:159], a[152:155], v[68:71], v[156:159]\n\tds_write_b128 v2, v[200:203] offset:5184\n\tv_mfma_f32_16x16x32_bf16 v[160:163], a[152:155], v[72:75], v[160:163]\n\tv_alignbit_b32 v196, v52, v52, 16\n\tv_alignbit_b32 v197, v51, v51, 16\n\tv_mfma_f32_16x16x32_bf16 v[160:163], a[156:159], v[68:71], v[160:163]\n\tv_alignbit_b32 v198, v50, v50, 16\n\t" \
    "v_alignbit_b32 v199, v63, v63, 16\n\ts_waitcnt lgkmcnt(9)\n\tv_mfma_f32_16x16x32_bf16 v[132:135], a[160:163], v[100:103], v[132:135]\n\tds_write_b128 v2, v[196:199] offset:9344\n\tv_mfma_f32_16x16x32_bf16 v[136:139], a[160:163], v[104:107], v[136:139]\n\tds_read_b128 v[68:71], v1 offset:37056\n\tv_bfi_b32 v200, s46, v52, v51\n\tv_mfma_f32_16x16x32_bf16 v[140:143], a[160:163], v[108:111], v[140:143]\n\t" \
    "ds_read_b128 v[72:75], v1 offset:37568\n\tv_bfi_b32 v201, s46, v51, v50\n\tv_mfma_f32_16x16x32_bf16 v[144:147], a[160:163], v[112:115], v[144:147]\n\tds_read_b128 v[76:79], v1 offset:38080\n\tv_bfi_b32 v202, s46, v50, v63\n\tv_mfma_f32_16x16x32_bf16 v[148:151], a[160:163], v[116:119], v[148:151]\n\tds_read_b128 v[80:83], v1 offset:38592\n\tv_bfi_b32 v203, s46, v63, v62\n\t" \
    "v_mfma_f32_16x16x32_bf16 v[152:155], a[160:163], v[120:123], v[152:155]\n\tds_write_b128 v2, v[200:203] offset:13504\n\tv_mfma_f32_16x16x32_bf16 v[156:159], a[160:163], v[124:127], v[156:159]\n\tds_read_b128 v[84:87], v1 offset:39104\n\tv_alignbit_b32 v196, v57, v57, 16\n\tv_mfma_f32_16x16x32_bf16 v[160:163], a[160:163], v[128:131], v[160:163]\n\tbuffer_load_dword v15, v7, s[16:19], s42 offen\n\tds_read_b128 v[88:91], v1 offset:39616\n\t" \
    "v_mfma_f32_16x16x32_bf16 v[136:139], a[164:167], v[100:103], v[136:139]\n\ts_add_u32 s43, s43, 1\n\tds_read_b128 v[92:95], v1 offset:40128\n\tv_alignbit_b32 v197, v56, v56, 16\n\tv_mfma_f32_16x16x32_bf16 v[140:143], a[164:167], v[104:107], v[140:143]\n\tv_readlane_b32 s42, v237, s43\n\ts_nop 4\n\tds_read_b128 v[96:99], v1 offset:40640\n\t" \
    "v_alignbit_b32 v198, v55, v55, 16\n\tv_mfma_f32_16x16x32_bf16 v[144:147], a[164:167], v[108:111], v[144:147]\n\tv_alignbit_b32 v199, v54, v54, 16\n\tv_mfma_f32_16x16x32_bf16 v[148:151], a[164:167], v[112:115], v[148:151]\n\tds_write_b128 v2, v[196:199] offset:0\n\tv_mfma_f32_16x16x32_bf16 v[152:155], a[164:167], v[116:119], v[152:155]\n\tv_bfi_b32 v200, s46, v57, v56\n\tv_bfi_b32 v201, s46, v56, v55\n\t" \
    "v_mfma_f32_16x16x32_bf16 v[156:159], a[164:167], v[120:123], v[156:159]\n\tv_bfi_b32 v202, s46, v55, v54\n\tv_bfi_b32 v203, s46, v54, v65\n\tv_mfma_f32_16x16x32_bf16 v[160:163], a[164:167], v[124:127], v[160:163]\n\tds_write_b128 v2, v[200:203] offset:4160\n\tv_mfma_f32_16x16x32_bf16 v[140:143], a[168:171], v[100:103], v[140:143]\n\tv_alignbit_b32 v196, v56, v56, 16\n\tv_alignbit_b32 v197, v55, v55, 16\n\t" \
    "v_mfma_f32_16x16x32_bf16 v[144:147], a[168:171], v[104:107], v[144:147]\n\tv_alignbit_b32 v198, v54, v54, 16\n\tv_alignbit_b32 v199, v65, v65, 16\n\tv_mfma_f32_16x16x32_bf16 v[148:151], a[168:171], v[108:111], v[148:151]\n\tds_write_b128 v2, v[196:199] offset:8320\n\tv_mfma_f32_16x16x32_bf16 v[152:155], a[168:171], v[112:115], v[152:155]\n\tv_bfi_b32 v200, s46, v56, v55\n\tv_bfi_b32 v201, s46, v55, v54\n\t" \
    "v_mfma_f32_16x16x32_bf16 v[156:159], a[168:171], v[116:119], v[156:159]\n\tv_bfi_b32 v202, s46, v54, v65\n\tv_bfi_b32 v203, s46, v65, v64\n\tv_mfma_f32_16x16x32_bf16 v[160:163], a[168:171], v[120:123], v[160:163]\n\tds_write_b128 v2, v[200:203] offset:12480\n\tv_mfma_f32_16x16x32_bf16 v[144:147], a[172:175], v[100:103], v[144:147]\n\tv_lshl_or_b32 v66, v66, 16, v66\n\tv_and_b32 v228, v66, v232\n\t" \
    "v_mfma_f32_16x16x32_bf16 v[148:151], a[172:175], v[104:107], v[148:151]\n\tv_and_b32 v229, v66, v233\n\tv_and_b32 v230, v66, v234\n\tv_mfma_f32_16x16x32_bf16 v[152:155], a[172:175], v[108:111], v[152:155]\n\tv_and_b32 v231, v66, v235\n\tv_mfma_f32_16x16x32_bf16 v[156:159], a[172:175], v[112:115], v[156:159]\n\tv_mfma_f32_16x16x32_bf16 v[160:163], a[172:175], v[116:119], v[160:163]\n\tv_mfma_f32_16x16x32_bf16 v[148:151], a[176:179], v[100:103], v[148:151]\n\t" \
    "v_mfma_f32_16x16x32_bf16 v[152:155], a[176:179], v[104:107], v[152:155]\n\tv_mfma_f32_16x16x32_bf16 v[156:159], a[176:179], v[108:111], v[156:159]\n\tv_mfma_f32_16x16x32_bf16 v[160:163], a[176:179], v[112:115], v[160:163]\n\tv_mfma_f32_16x16x32_bf16 v[152:155], a[180:183], v[100:103], v[152:155]\n\tv_mfma_f32_16x16x32_bf16 v[156:159], a[180:183], v[104:107], v[156:159]\n\tv_mfma_f32_16x16x32_bf16 v[160:163], a[180:183], v[108:111], v[160:163]\n\tv_mfma_f32_16x16x32_bf16 v[156:159], a[184:187], v[100:103], v[156:159]\n\tv_mfma_f32_16x16x32_bf16 v[160:163], a[184:187], v[104:107], v[160:163]\n\t" \
    "v_mfma_f32_16x16x32_bf16 v[160:163], a[188:191], v[100:103], v[160:163]\n\ts_waitcnt lgkmcnt(4)\n\tv_mfma_f32_16x16x32_bf16 v[132:135], a[192:195], v[68:71], v[132:135]\n\tds_read_b128 v[100:103], v1 offset:36992\n\tv_mfma_f32_16x16x32_bf16 v[136:139], a[192:195], v[72:75], v[136:139]\n\tds_read_b128 v[104:107], v1 offset:37504\n\tv_mfma_f32_16x16x32_bf16 v[140:143], a[192:195], v[76:79], v[140:143]\n\tds_read_b128 v[108:111], v1 offset:38016\n\t" \
    "v_mfma_f32_16x16x32_bf16 v[144:147], a[192:195], v[80:83], v[144:147]\n\tds_read_b128 v[112:115], v1 offset:38528\n\tv_mfma_f32_16x16x32_bf16 v[148:151], a[192:195], v[84:87], v[148:151]\n\tds_read_b128 v[116:119], v1 offset:39040\n\tv_mfma_f32_16x16x32_bf16 v[152:155], a[192:195], v[88:91], v[152:155]\n\tds_read_b128 v[120:123], v1 offset:39552\n\tv_mfma_f32_16x16x32_bf16 v[156:159], a[192:195], v[92:95], v[156:159]\n\tds_read_b128 v[124:127], v1 offset:40064\n\t" \
    "v_mfma_f32_16x16x32_bf16 v[160:163], a[192:195], v[96:99], v[160:163]\n\tds_read_b128 v[128:131], v1 offset:40576\n\tv_mfma_f32_16x16x32_bf16 v[136:139], a[196:199], v[68:71], v[136:139]\n\tv_mfma_f32_16x16x32_bf16 v[140:143], a[196:199], v[72:75], v[140:143]\n\tv_mfma_f32_16x16x32_bf16 v[144:147], a[196:199], v[76:79], v[144:147]\n\tv_mfma_f32_16x16x32_bf16 v[148:151], a[196:199], v[80:83], v[148:151]\n\tv_mfma_f32_16x16x32_bf16 v[152:155], a[196:199], v[84:87], v[152:155]\n\tv_mfma_f32_16x16x32_bf16 v[156:159], a[196:199], v[88:91], v[156:159]\n\t" \
    "v_mfma_f32_16x16x32_bf16 v[160:163], a[196:199], v[92:95], v[160:163]\n\tv_mfma_f32_16x16x32_bf16 v[140:143], a[200:203], v[68:71], v[140:143]\n\tv_mfma_f32_16x16x32_bf16 v[144:147], a[200:203], v[72:75], v[144:147]\n\tv_mfma_f32_16x16x32_bf16 v[148:151], a[200:203], v[76:79], v[148:151]\n\tv_mfma_f32_16x16x32_bf16 v[152:155], a[200:203], v[80:83], v[152:155]\n\tv_mfma_f32_16x16x32_bf16 v[156:159], a[200:203], v[84:87], v[156:159]\n\tv_mfma_f32_16x16x32_bf16 v[160:163], a[200:203], v[88:91], v[160:163]\n\tv_mfma_f32_16x16x32_bf16 v[144:147], a[204:207], v[68:71], v[144:147]\n\t" \
    "v_mfma_f32_16x16x32_bf16 v[148:151], a[204:207], v[72:75], v[148:151]\n\tv_mfma_f32_16x16x32_bf16 v[152:155], a[204:207], v[76:79], v[152:155]\n\tv_mfma_f32_16x16x32_bf16 v[156:159], a[204:207], v[80:83], v[156:159]\n\tv_mfma_f32_16x16x32_bf16 v[160:163], a[204:207], v[84:87], v[160:163]\n\tv_mfma_f32_16x16x32_bf16 v[148:151], a[208:211], v[68:71], v[148:151]\n\tv_mfma_f32_16x16x32_bf16 v[152:155], a[208:211], v[72:75], v[152:155]\n\tv_mfma_f32_16x16x32_bf16 v[156:159], a[208:211], v[76:79], v[156:159]\n\tv_mfma_f32_16x16x32_bf16 v[160:163], a[208:211], v[80:83], v[160:163]\n\t" \
    "v_mfma_f32_16x16x32_bf16 v[152:155], a[212:215], v[68:71], v[152:155]\n\tbuffer_load_dword v15, v7, s[16:19], s42 offen\n\tv_mfma_f32_16x16x32_bf16 v[156:159], a[212:215], v[72:75], v[156:159]\n\ts_add_u32 s43, s43, 1\n\tv_mfma_f32_16x16x32_bf16 v[160:163], a[212:215], v[76:79], v[160:163]\n\tv_readlane_b32 s42, v237, s43\n\ts_nop 4\n\tv_mfma_f32_16x16x32_bf16 v[156:159], a[216:219], v[68:71], v[156:159]\n\t" \
    "v_mfma_f32_16x16x32_bf16 v[160:163], a[216:219], v[72:75], v[160:163]\n\tv_mfma_f32_16x16x32_bf16 v[160:163], a[220:223], v[68:71], v[160:163]\n\ts_waitcnt lgkmcnt(0)\n\tv_mfma_f32_16x16x32_bf16 v[132:135], a[224:227], v[100:103], v[132:135]\n\ts_waitcnt vmcnt(3)\n\tbuffer_store_dwordx2 v[248:249], off, s[8:11], s51 sc1\n\tds_read_b64 a[0:1], v0 offset:4032\n\tv_mfma_f32_16x16x32_bf16 v[136:139], a[224:227], v[104:107], v[136:139]\n\t" \
    "ds_read_b64 a[2:3], v0 offset:4040\n\tv_mfma_f32_16x16x32_bf16 v[140:143], a[224:227], v[108:111], v[140:143]\n\tds_read_b64 a[4:5], v0 offset:3520\n\tv_mfma_f32_16x16x32_bf16 v[144:147], a[224:227], v[112:115], v[144:147]\n\tds_read_b64 a[6:7], v0 offset:3528\n\tv_mfma_f32_16x16x32_bf16 v[148:151], a[224:227], v[116:119], v[148:151]\n\tds_read_b64 a[8:9], v0 offset:3008\n\tv_mfma_f32_16x16x32_bf16 v[152:155], a[224:227], v[120:123], v[152:155]\n\t" \
    "ds_read_b64 a[10:11], v0 offset:3016\n\tv_mfma_f32_16x16x32_bf16 v[156:159], a[224:227], v[124:127], v[156:159]\n\tds_read_b64 a[12:13], v0 offset:2496\n\tv_mfma_f32_16x16x32_bf16 v[160:163], a[224:227], v[128:131], v[160:163]\n\tds_read_b64 a[14:15], v0 offset:2504\n\tv_mfma_f32_16x16x32_bf16 v[136:139], a[228:231], v[100:103], v[136:139]\n\tds_read_b64 a[16:17], v0 offset:1984\n\tv_mfma_f32_16x16x32_bf16 v[140:143], a[228:231], v[104:107], v[140:143]\n\t" \
    "ds_read_b64 a[18:19], v0 offset:1992\n\tv_mfma_f32_16x16x32_bf16 v[144:147], a[228:231], v[108:111], v[144:147]\n\tds_read_b64 a[20:21], v0 offset:1472\n\tv_mfma_f32_16x16x32_bf16 v[148:151], a[228:231], v[112:115], v[148:151]\n\tds_read_b64 a[22:23], v0 offset:1480\n\tv_mfma_f32_16x16x32_bf16 v[152:155], a[228:231], v[116:119], v[152:155]\n\tds_read_b64 a[24:25], v0 offset:960\n\tv_mfma_f32_16x16x32_bf16 v[156:159], a[228:231], v[120:123], v[156:159]\n\t" \
    "ds_read_b64 a[26:27], v0 offset:968\n\tv_mfma_f32_16x16x32_bf16 v[160:163], a[228:231], v[124:127], v[160:163]\n\tds_read_b64 a[28:29], v0 offset:448\n\tv_mfma_f32_16x16x32_bf16 v[140:143], a[232:235], v[100:103], v[140:143]\n\tds_read_b64 a[30:31], v0 offset:456\n\tv_mfma_f32_16x16x32_bf16 v[144:147], a[232:235], v[104:107], v[144:147]\n\tds_read_b128 v[68:71], v1 offset:55936\n\tv_mfma_f32_16x16x32_bf16 v[148:151], a[232:235], v[108:111], v[148:151]\n\t" \
    "ds_read_b128 v[72:75], v1 offset:56448\n\tv_mfma_f32_16x16x32_bf16 v[152:155], a[232:235], v[112:115], v[152:155]\n\tds_read_b128 v[76:79], v1 offset:56960\n\tv_mfma_f32_16x16x32_bf16 v[156:159], a[232:235], v[116:119], v[156:159]\n\tds_read_b128 v[80:83], v1 offset:57472\n\tv_mfma_f32_16x16x32_bf16 v[160:163], a[232:235], v[120:123], v[160:163]\n\tds_read_b128 v[84:87], v1 offset:57984\n\tv_mfma_f32_16x16x32_bf16 v[144:147], a[236:239], v[100:103], v[144:147]\n\t" \
    "ds_read_b128 v[88:91], v1 offset:58496\n\tv_mfma_f32_16x16x32_bf16 v[148:151], a[236:239], v[104:107], v[148:151]\n\tds_read_b128 v[92:95], v1 offset:59008\n\tv_mfma_f32_16x16x32_bf16 v[152:155], a[236:239], v[108:111], v[152:155]\n\tds_read_b128 v[96:99], v1 offset:59520\n\tv_mfma_f32_16x16x32_bf16 v[156:159], a[236:239], v[112:115], v[156:159]\n\tv_mfma_f32_16x16x32_bf16 v[160:163], a[236:239], v[116:119], v[160:163]\n\tv_mfma_f32_16x16x32_bf16 v[148:151], a[240:243], v[100:103], v[148:151]\n\t" \
    "v_mfma_f32_16x16x32_bf16 v[152:155], a[240:243], v[104:107], v[152:155]\n\tv_mfma_f32_16x16x32_bf16 v[156:159], a[240:243], v[108:111], v[156:159]\n\tv_mfma_f32_16x16x32_bf16 v[160:163], a[240:243], v[112:115], v[160:163]\n\tv_mfma_f32_16x16x32_bf16 v[152:155], a[244:247], v[100:103], v[152:155]\n\tv_mfma_f32_16x16x32_bf16 v[156:159], a[244:247], v[104:107], v[156:159]\n\tv_mfma_f32_16x16x32_bf16 v[160:163], a[244:247], v[108:111], v[160:163]\n\tv_mfma_f32_16x16x32_bf16 v[156:159], a[248:251], v[100:103], v[156:159]\n\tv_mfma_f32_16x16x32_bf16 v[160:163], a[248:251], v[104:107], v[160:163]\n\t" \
    "v_mfma_f32_16x16x32_bf16 v[160:163], a[252:255], v[100:103], v[160:163]\n\ts_waitcnt lgkmcnt(0)\n\tv_mfma_f32_16x16x32_bf16 v[164:167], v[228:231], v[68:71], 0\n\tds_read_b64 a[32:33], v0 offset:3968\n\tds_read_b64 a[34:35], v0 offset:3976\n\tv_mfma_f32_16x16x32_bf16 v[168:171], v[228:231], v[72:75], 0\n\tds_read_b64 a[36:37], v0 offset:3456\n\tds_read_b64 a[38:39], v0 offset:3464\n\t" \
    "v_mfma_f32_16x16x32_bf16 v[172:175], v[228:231], v[76:79], 0\n\tds_read_b64 a[40:41], v0 offset:2944\n\tds_read_b64 a[42:43], v0 offset:2952\n\tv_mfma_f32_16x16x32_bf16 v[176:179], v[228:231], v[80:83], 0\n\tds_read_b64 a[44:45], v0 offset:2432\n\tds_read_b64 a[46:47], v0 offset:2440\n\tv_mfma_f32_16x16x32_bf16 v[180:183], v[228:231], v[84:87], 0\n\tds_read_b64 a[48:49], v0 offset:1920\n\t" \
    "ds_read_b64 a[50:51], v0 offset:1928\n\tv_mfma_f32_16x16x32_bf16 v[184:187], v[228:231], v[88:91], 0\n\tds_read_b64 a[52:53], v0 offset:1408\n\tv_cvt_pk_bf16_f32 v132, v132, v133\n\tv_mfma_f32_16x16x32_bf16 v[188:191], v[228:231], v[92:95], 0\n\tds_read_b64 a[54:55], v0 offset:1416\n\tv_cvt_pk_bf16_f32 v133, v134, v135\n\tv_mfma_f32_16x16x32_bf16 v[192:195], v[228:231], v[96:99], 0\n\t" \
    "ds_read_b64 a[56:57], v0 offset:896\n\tds_write_b64 v3, v[132:133] offset:36992\n\tv_mfma_f32_16x16x32_bf16 v[164:167], a[0:3], v[68:71], v[164:167]\n\tds_read_b64 a[58:59], v0 offset:904\n\tv_cvt_pk_bf16_f32 v136, v136, v137\n\tv_mfma_f32_16x16x32_bf16 v[168:171], a[0:3], v[72:75], v[168:171]\n\tds_read_b64 a[60:61], v0 offset:384\n\tv_cvt_pk_bf16_f32 v137, v138, v139\n\t" \
    "v_mfma_f32_16x16x32_bf16 v[172:175], a[0:3], v[76:79], v[172:175]\n\tds_read_b64 a[62:63], v0 offset:392\n\tds_write_b64 v3, v[136:137] offset:37504\n\tv_mfma_f32_16x16x32_bf16 v[176:179], a[0:3], v[80:83], v[176:179]\n\tds_read_b128 v[100:103], v1 offset:55872\n\tv_cvt_pk_bf16_f32 v140, v140, v141\n\tv_mfma_f32_16x16x32_bf16 v[180:183], a[0:3], v[84:87], v[180:183]\n\tds_read_b128 v[104:107], v1 offset:56384\n\t" \
    "v_cvt_pk_bf16_f32 v141, v142, v143\n\tv_mfma_f32_16x16x32_bf16 v[184:187], a[0:3], v[88:91], v[184:187]\n\tds_read_b128 v[108:111], v1 offset:56896\n\tds_write_b64 v3, v[140:141] offset:38016\n\tv_mfma_f32_16x16x32_bf16 v[188:191], a[0:3], v[92:95], v[188:191]\n\tds_read_b128 v[112:115], v1 offset:57408\n\tv_cvt_pk_bf16_f32 v144, v144, v145\n\tv_mfma_f32_16x16x32_bf16 v[192:195], a[0:3], v[96:99], v[192:195]\n\t" \
    "ds_read_b128 v[116:119], v1 offset:57920\n\tv_cvt_pk_bf16_f32 v145, v146, v147\n\tv_mfma_f32_16x16x32_bf16 v[168:171], a[4:7], v[68:71], v[168:171]\n\tds_read_b128 v[120:123], v1 offset:58432\n\tds_write_b64 v3, v[144:145] offset:38528\n\tv_mfma_f32_16x16x32_bf16 v[172:175], a[4:7], v[72:75], v[172:175]\n\tds_read_b128 v[124:127], v1 offset:58944\n\tv_cvt_pk_bf16_f32 v148, v148, v149\n\t" \
    "v_mfma_f32_16x16x32_bf16 v[176:179], a[4:7], v[76:79], v[176:179]\n\tds_read_b128 v[128:131], v1 offset:59456\n\tv_cvt_pk_bf16_f32 v149, v150, v151\n\tv_mfma_f32_16x16x32_bf16 v[180:183], a[4:7], v[80:83], v[180:183]\n\tds_read_b64 a[64:65], v0 offset:3904\n\tds_write_b64 v3, v[148:149] offset:39040\n\tv_mfma_f32_16x16x32_bf16 v[184:187], a[4:7], v[84:87], v[184:187]\n\tds_read_b64 a[66:67], v0 offset:3912\n\t" \
    "v_cvt_pk_bf16_f32 v152, v152, v153\n\tv_mfma_f32_16x16x32_bf16 v[188:191], a[4:7], v[88:91], v[188:191]\n\tds_read_b64 a[68:69], v0 offset:3392\n\tv_cvt_pk_bf16_f32 v153, v154, v155\n\tv_mfma_f32_16x16x32_bf16 v[192:195], a[4:7], v[92:95], v[192:195]\n\tbuffer_load_dword v15, v7, s[16:19], s42 offen\n\tds_read_b64 a[70:71], v0 offset:3400\n\tv_mfma_f32_16x16x32_bf16 v[172:175], a[8:11], v[68:71], v[172:175]\n\t" \
    "s_add_u32 s43, s43, 1\n\tds_read_b64 a[72:73], v0 offset:2880\n\tds_write_b64 v3, v[152:153] offset:39552\n\tv_mfma_f32_16x16x32_bf16 v[176:179], a[8:11], v[72:75], v[176:179]\n\tv_readlane_b32 s42, v237, s43\n\ts_nop 4\n\tds_read_b64 a[74:75], v0 offset:2888\n\tv_cvt_pk_bf16_f32 v156, v156, v157\n\t" \
    "v_mfma_f32_16x16x32_bf16 v[180:183], a[8:11], v[76:79], v[180:183]\n\tds_read_b64 a[76:77], v0 offset:2368\n\tv_cvt_pk_bf16_f32 v157, v158, v159\n\tv_mfma_f32_16x16x32_bf16 v[184:187], a[8:11], v[80:83], v[184:187]\n\tds_read_b64 a[78:79], v0 offset:2376\n\tds_write_b64 v3, v[156:157] offset:40064\n\tv_mfma_f32_16x16x32_bf16 v[188:191], a[8:11], v[84:87], v[188:191]\n\tds_read_b64 a[80:81], v0 offset:1856\n\t" \
    "v_cvt_pk_bf16_f32 v160, v160, v161\n\tv_mfma_f32_16x16x32_bf16 v[192:195], a[8:11], v[88:91], v[192:195]\n\tds_read_b64 a[82:83], v0 offset:1864\n\tv_cvt_pk_bf16_f32 v161, v162, v163\n\tv_mfma_f32_16x16x32_bf16 v[176:179], a[12:15], v[68:71], v[176:179]\n\tds_read_b64 a[84:85], v0 offset:1344\n\tds_write_b64 v3, v[160:161] offset:40576\n\tv_mfma_f32_16x16x32_bf16 v[180:183], a[12:15], v[72:75], v[180:183]\n\t" \
    "ds_read_b64 a[86:87], v0 offset:1352\n\tv_mfma_f32_16x16x32_bf16 v[184:187], a[12:15], v[76:79], v[184:187]\n\tds_read_b64 a[88:89], v0 offset:832\n\tv_mfma_f32_16x16x32_bf16 v[188:191], a[12:15], v[80:83], v[188:191]\n\tds_read_b64 a[90:91], v0 offset:840\n\tv_mfma_f32_16x16x32_bf16 v[192:195], a[12:15], v[84:87], v[192:195]\n\tds_read_b64 a[92:93], v0 offset:320\n\tv_mfma_f32_16x16x32_bf16 v[180:183], a[16:19], v[68:71], v[180:183]\n\t" \
    "ds_read_b64 a[94:95], v0 offset:328\n\tv_mfma_f32_16x16x32_bf16 v[184:187], a[16:19], v[72:75], v[184:187]\n\tds_read_b64 a[96:97], v0 offset:3840\n\tv_mfma_f32_16x16x32_bf16 v[188:191], a[16:19], v[76:79], v[188:191]\n\tds_read_b64 a[98:99], v0 offset:3848\n\tv_mfma_f32_16x16x32_bf16 v[192:195], a[16:19], v[80:83], v[192:195]\n\tds_read_b64 a[100:101], v0 offset:3328\n\tv_mfma_f32_16x16x32_bf16 v[184:187], a[20:23], v[68:71], v[184:187]\n\t" \
    "ds_read_b64 a[102:103], v0 offset:3336\n\tv_mfma_f32_16x16x32_bf16 v[188:191], a[20:23], v[72:75], v[188:191]\n\tv_mfma_f32_16x16x32_bf16 v[192:195], a[20:23], v[76:79], v[192:195]\n\tv_mfma_f32_16x16x32_bf16 v[188:191], a[24:27], v[68:71], v[188:191]\n\tv_mfma_f32_16x16x32_bf16 v[192:195], a[24:27], v[72:75], v[192:195]\n\tv_mfma_f32_16x16x32_bf16 v[192:195], a[28:31], v[68:71], v[192:195]\n\ts_waitcnt lgkmcnt(9)\n\ts_barrier\n\t" \
    "v_mfma_f32_16x16x32_bf16 v[164:167], a[32:35], v[100:103], v[164:167]\n\tds_read_b64 a[104:105], v0 offset:2816\n\tds_read_b64 a[106:107], v0 offset:2824\n\tv_mfma_f32_16x16x32_bf16 v[168:171], a[32:35], v[104:107], v[168:171]\n\tds_read_b64 a[108:109], v0 offset:2304\n\tds_read_b64 a[110:111], v0 offset:2312\n\tv_mfma_f32_16x16x32_bf16 v[172:175], a[32:35], v[108:111], v[172:175]\n\tds_read_b64 a[112:113], v0 offset:1792\n\t" \
    "ds_read_b64 a[114:115], v0 offset:1800\n\tv_mfma_f32_16x16x32_bf16 v[176:179], a[32:35], v[112:115], v[176:179]\n\tds_read_b64 a[116:117], v0 offset:1280\n\tds_read_b64 a[118:119], v0 offset:1288\n\tv_mfma_f32_16x16x32_bf16 v[180:183], a[32:35], v[116:119], v[180:183]\n\tds_read_b64 a[120:121], v0 offset:768\n\tds_read_b64 a[122:123], v0 offset:776\n\tv_mfma_f32_16x16x32_bf16 v[184:187], a[32:35], v[120:123], v[184:187]\n\t" \
    "ds_read_b64 a[124:125], v0 offset:256\n\ts_add_u32 s51, s44, 8\n\tv_mfma_f32_16x16x32_bf16 v[188:191], a[32:35], v[124:127], v[188:191]\n\tds_read_b64 a[126:127], v0 offset:264\n\ts_mul_i32 s65, s51, 1366\n\tv_mfma_f32_16x16x32_bf16 v[192:195], a[32:35], v[128:131], v[192:195]\n\tds_read_b64 a[128:129], v0 offset:3776\n\ts_lshr_b32 s65, s65, 16\n\t" \
    "v_mfma_f32_16x16x32_bf16 v[168:171], a[36:39], v[100:103], v[168:171]\n\tds_read_b64 a[130:131], v0 offset:3784\n\ts_mul_i32 s56, s65, 48\n\tv_mfma_f32_16x16x32_bf16 v[172:175], a[36:39], v[104:107], v[172:175]\n\tds_read_b64 a[132:133], v0 offset:3264\n\ts_sub_u32 s56, s51, s56\n\tv_mfma_f32_16x16x32_bf16 v[176:179], a[36:39], v[108:111], v[176:179]\n\tds_read_b64 a[134:135], v0 offset:3272\n\t" \
    "s_lshl_b32 s56, s56, 6\n\tv_mfma_f32_16x16x32_bf16 v[180:183], a[36:39], v[112:115], v[180:183]\n\tds_read_b128 v[68:71], v1 offset:55808\n\ts_lshr_b32 s70, s65, 2\n\tv_mfma_f32_16x16x32_bf16 v[184:187], a[36:39], v[116:119], v[184:187]\n\tds_read_b128 v[72:75], v1 offset:56320\n\ts_mul_i32 s70, s70, 252\n\tv_mfma_f32_16x16x32_bf16 v[188:191], a[36:39], v[120:123], v[188:191]\n\t" \
    "ds_read_b128 v[76:79], v1 offset:56832\n\ts_add_u32 s65, s65, s70\n\tv_mfma_f32_16x16x32_bf16 v[192:195], a[36:39], v[124:127], v[192:195]\n\tds_read_b128 v[80:83], v1 offset:57344\n\ts_mul_i32 s65, s65, 3072\n\tv_mfma_f32_16x16x32_bf16 v[172:175], a[40:43], v[100:103], v[172:175]\n\tds_read_b128 v[84:87], v1 offset:57856\n\ts_add_u32 s56, s56, s65\n\t" \
    "v_mfma_f32_16x16x32_bf16 v[176:179], a[40:43], v[104:107], v[176:179]\n\tds_read_b128 v[88:91], v1 offset:58368\n\ts_add_u32 s56, s56, 4849664\n\tv_mfma_f32_16x16x32_bf16 v[180:183], a[40:43], v[108:111], v[180:183]\n\tds_read_b128 v[92:95], v1 offset:58880\n\ts_add_u32 s51, s51, s38\n\tv_mfma_f32_16x16x32_bf16 v[184:187], a[40:43], v[112:115], v[184:187]\n\tds_read_b128 v[96:99], v1 offset:59392\n\t" \
    "s_lshl_b32 s51, s51, 3\n\tv_mfma_f32_16x16x32_bf16 v[188:191], a[40:43], v[116:119], v[188:191]\n\tds_read_b64 a[136:137], v0 offset:2752\n\ts_add_u32 s51, s51, 50176\n\tds_read_b64_tr_b16 v[204:205], v4 offset:36992\n\tv_mfma_f32_16x16x32_bf16 v[192:195], a[40:43], v[120:123], v[192:195]\n\tds_read_b64 a[138:139], v0 offset:2760\n\tds_read_b64_tr_b16 v[206:207], v4 offset:37000\n\t" \
    "v_mfma_f32_16x16x32_bf16 v[176:179], a[44:47], v[100:103], v[176:179]\n\tds_read_b64 a[140:141], v0 offset:2240\n\tds_read_b64_tr_b16 v[208:209], v4 offset:37056\n\tv_mfma_f32_16x16x32_bf16 v[180:183], a[44:47], v[104:107], v[180:183]\n\tds_read_b64 a[142:143], v0 offset:2248\n\tds_read_b64_tr_b16 v[210:211], v4 offset:37064\n\tv_mfma_f32_16x16x32_bf16 v[184:187], a[44:47], v[108:111], v[184:187]\n\tds_read_b64 a[144:145], v0 offset:1728\n\t" \
    "ds_read_b64_tr_b16 v[212:213], v4 offset:37504\n\tv_mfma_f32_16x16x32_bf16 v[188:191], a[44:47], v[112:115], v[188:191]\n\tds_read_b64 a[146:147], v0 offset:1736\n\tds_read_b64_tr_b16 v[214:215], v4 offset:37512\n\tv_mfma_f32_16x16x32_bf16 v[192:195], a[44:47], v[116:119], v[192:195]\n\tds_read_b64 a[148:149], v0 offset:1216\n\tds_read_b64_tr_b16 v[216:217], v4 offset:37568\n\tv_mfma_f32_16x16x32_bf16 v[180:183], a[48:51], v[100:103], v[180:183]\n\t" \
    "ds_read_b64 a[150:151], v0 offset:1224\n\tds_read_b64_tr_b16 v[218:219], v4 offset:37576\n\tv_mfma_f32_16x16x32_bf16 v[184:187], a[48:51], v[104:107], v[184:187]\n\tds_read_b64 a[152:153], v0 offset:704\n\tv_mfma_f32_16x16x32_bf16 v[188:191], a[48:51], v[108:111], v[188:191]\n\tds_read_b64 a[154:155], v0 offset:712\n\tv_mfma_f32_16x16x32_bf16 v[192:195], a[48:51], v[112:115], v[192:195]\n\tds_read_b64 a[156:157], v0 offset:192\n\t" \
    "v_mfma_f32_16x16x32_bf16 v[184:187], a[52:55], v[100:103], v[184:187]\n\tds_read_b64 a[158:159], v0 offset:200\n\tv_mfma_f32_16x16x32_bf16 v[188:191], a[52:55], v[104:107], v[188:191]\n\tv_mfma_f32_16x16x32_bf16 v[192:195], a[52:55], v[108:111], v[192:195]\n\ts_waitcnt lgkmcnt(15)\n\tbuffer_store_dwordx4 v[204:207], v5, s[8:11], s56 offen sc1\n\ts_add_u32 s56, s56, 49152\n\tv_mfma_f32_16x16x32_bf16 v[188:191], a[56:59], v[100:103], v[188:191]\n\t" \
    "v_mfma_f32_16x16x32_bf16 v[192:195], a[56:59], v[104:107], v[192:195]\n\tv_mfma_f32_16x16x32_bf16 v[192:195], a[60:63], v[100:103], v[192:195]\n\tv_mfma_f32_16x16x32_bf16 v[164:167], a[64:67], v[68:71], v[164:167]\n\tds_read_b64 a[160:161], v0 offset:3712\n\tds_read_b64 a[162:163], v0 offset:3720\n\tv_mfma_f32_16x16x32_bf16 v[168:171], a[64:67], v[72:75], v[168:171]\n\tds_read_b64 a[164:165], v0 offset:3200\n\tds_read_b64 a[166:167], v0 offset:3208\n\t" \
    "v_mfma_f32_16x16x32_bf16 v[172:175], a[64:67], v[76:79], v[172:175]\n\tds_read_b64 a[168:169], v0 offset:2688\n\tds_read_b64 a[170:171], v0 offset:2696\n\tv_mfma_f32_16x16x32_bf16 v[176:179], a[64:67], v[80:83], v[176:179]\n\tds_read_b64 a[172:173], v0 offset:2176\n\tds_read_b64 a[174:175], v0 offset:2184\n\tv_mfma_f32_16x16x32_bf16 v[180:183], a[64:67], v[84:87], v[180:183]\n\tds_read_b64 a[176:177], v0 offset:1664\n\t" \
    "ds_read_b64 a[178:179], v0 offset:1672\n\tv_mfma_f32_16x16x32_bf16 v[184:187], a[64:67], v[88:91], v[184:187]\n\tds_read_b64 a[180:181], v0 offset:1152\n\tv_mfma_f32_16x16x32_bf16 v[188:191], a[64:67], v[92:95], v[188:191]\n\tds_read_b64 a[182:183], v0 offset:1160\n\tv_mfma_f32_16x16x32_bf16 v[192:195], a[64:67], v[96:99], v[192:195]\n\tds_read_b64 a[184:185], v0 offset:640\n\tv_mfma_f32_16x16x32_bf16 v[168:171], a[68:71], v[68:71], v[168:171]\n\t" \
    "ds_read_b64 a[186:187], v0 offset:648\n\tv_mfma_f32_16x16x32_bf16 v[172:175], a[68:71], v[72:75], v[172:175]\n\tds_read_b64 a[188:189], v0 offset:128\n\tv_mfma_f32_16x16x32_bf16 v[176:179], a[68:71], v[76:79], v[176:179]\n\tds_read_b64 a[190:191], v0 offset:136\n\tv_mfma_f32_16x16x32_bf16 v[180:183], a[68:71], v[80:83], v[180:183]\n\tds_read_b128 v[100:103], v1 offset:55744\n\tv_mfma_f32_16x16x32_bf16 v[184:187], a[68:71], v[84:87], v[184:187]\n\t" \
    "ds_read_b128 v[104:107], v1 offset:56256\n\tv_mfma_f32_16x16x32_bf16 v[188:191], a[68:71], v[88:91], v[188:191]\n\tds_read_b128 v[108:111], v1 offset:56768\n\tv_mfma_f32_16x16x32_bf16 v[192:195], a[68:71], v[92:95], v[192:195]\n\tds_read_b128 v[112:115], v1 offset:57280\n\ts_waitcnt lgkmcnt(15)\n\tbuffer_store_dwordx4 v[208:211], v5, s[8:11], s56 offen sc1\n\tv_mfma_f32_16x16x32_bf16 v[172:175], a[72:75], v[68:71], v[172:175]\n\t" \
    "ds_read_b128 v[116:119], v1 offset:57792\n\ts_add_u32 s56, s56, 49152\n\tv_mfma_f32_16x16x32_bf16 v[176:179], a[72:75], v[72:75], v[176:179]\n\tbuffer_load_dword v15, v7, s[16:19], s42 offen\n\tds_read_b128 v[120:123], v1 offset:58304\n\tv_mfma_f32_16x16x32_bf16 v[180:183], a[72:75], v[76:79], v[180:183]\n\ts_add_u32 s43, s43, 1\n\tds_read_b128 v[124:127], v1 offset:58816\n\t" \
    "v_mfma_f32_16x16x32_bf16 v[184:187], a[72:75], v[80:83], v[184:187]\n\tv_readlane_b32 s42, v237, s43\n\ts_nop 4\n\tds_read_b128 v[128:131], v1 offset:59328\n\tv_mfma_f32_16x16x32_bf16 v[188:191], a[72:75], v[84:87], v[188:191]\n\tds_read_b64 a[192:193], v0 offset:3648\n\tv_mfma_f32_16x16x32_bf16 v[192:195], a[72:75], v[88:91], v[192:195]\n\tds_read_b64 a[194:195], v0 offset:3656\n\t" \
    "v_mfma_f32_16x16x32_bf16 v[176:179], a[76:79], v[68:71], v[176:179]\n\tds_read_b64 a[196:197], v0 offset:3136\n\tv_mfma_f32_16x16x32_bf16 v[180:183], a[76:79], v[72:75], v[180:183]\n\tds_read_b64 a[198:199], v0 offset:3144\n\tv_mfma_f32_16x16x32_bf16 v[184:187], a[76:79], v[76:79], v[184:187]\n\tds_read_b64 a[200:201], v0 offset:2624\n\tv_mfma_f32_16x16x32_bf16 v[188:191], a[76:79], v[80:83], v[188:191]\n\tds_read_b64 a[202:203], v0 offset:2632\n\t" \
    "v_mfma_f32_16x16x32_bf16 v[192:195], a[76:79], v[84:87], v[192:195]\n\tds_read_b64 a[204:205], v0 offset:2112\n\tv_mfma_f32_16x16x32_bf16 v[180:183], a[80:83], v[68:71], v[180:183]\n\tds_read_b64 a[206:207], v0 offset:2120\n\tv_mfma_f32_16x16x32_bf16 v[184:187], a[80:83], v[72:75], v[184:187]\n\tds_read_b64 a[208:209], v0 offset:1600\n\tv_mfma_f32_16x16x32_bf16 v[188:191], a[80:83], v[76:79], v[188:191]\n\tds_read_b64 a[210:211], v0 offset:1608\n\t" \
    "v_mfma_f32_16x16x32_bf16 v[192:195], a[80:83], v[80:83], v[192:195]\n\tds_read_b64 a[212:213], v0 offset:1088\n\tv_mfma_f32_16x16x32_bf16 v[184:187], a[84:87], v[68:71], v[184:187]\n\tds_read_b64 a[214:215], v0 offset:1096\n\tv_mfma_f32_16x16x32_bf16 v[188:191], a[84:87], v[72:75], v[188:191]\n\tbuffer_store_dwordx4 v[212:215], v5, s[8:11], s56 offen sc1\n\tv_mfma_f32_16x16x32_bf16 v[192:195], a[84:87], v[76:79], v[192:195]\n\ts_add_u32 s56, s56, 49152\n\t" \
    "v_mfma_f32_16x16x32_bf16 v[188:191], a[88:91], v[68:71], v[188:191]\n\tv_mfma_f32_16x16x32_bf16 v[192:195], a[88:91], v[72:75], v[192:195]\n\tv_mfma_f32_16x16x32_bf16 v[192:195], a[92:95], v[68:71], v[192:195]\n\ts_waitcnt lgkmcnt(12)\n\tv_mfma_f32_16x16x32_bf16 v[164:167], a[96:99], v[100:103], v[164:167]\n\tds_read_b64 a[216:217], v0 offset:576\n\tv_mfma_f32_16x16x32_bf16 v[168:171], a[96:99], v[104:107], v[168:171]\n\tds_read_b64 a[218:219], v0 offset:584\n\t" \
    "v_mfma_f32_16x16x32_bf16 v[172:175], a[96:99], v[108:111], v[172:175]\n\tds_read_b64 a[220:221], v0 offset:64\n\tv_mfma_f32_16x16x32_bf16 v[176:179], a[96:99], v[112:115], v[176:179]\n\tds_read_b64 a[222:223], v0 offset:72\n\tv_mfma_f32_16x16x32_bf16 v[180:183], a[96:99], v[116:119], v[180:183]\n\tds_read_b64 a[224:225], v0 offset:3584\n\tv_mfma_f32_16x16x32_bf16 v[184:187], a[96:99], v[120:123], v[184:187]\n\tds_read_b64 a[226:227], v0 offset:3592\n\t" \
    "v_mfma_f32_16x16x32_bf16 v[188:191], a[96:99], v[124:127], v[188:191]\n\tds_read_b64 a[228:229], v0 offset:3072\n\tv_mfma_f32_16x16x32_bf16 v[192:195], a[96:99], v[128:131], v[192:195]\n\tds_read_b64 a[230:231], v0 offset:3080\n\tv_mfma_f32_16x16x32_bf16 v[168:171], a[100:103], v[100:103], v[168:171]\n\tds_read_b64 a[232:233], v0 offset:2560\n\tbuffer_store_dwordx4 v[216:219], v5, s[8:11], s56 offen sc1\n\tv_mfma_f32_16x16x32_bf16 v[172:175], a[100:103], v[104:107], v[172:175]\n\t" \
    "ds_read_b64 a[234:235], v0 offset:2568\n\ts_add_u32 s56, s56, 49152\n\tv_mfma_f32_16x16x32_bf16 v[176:179], a[100:103], v[108:111], v[176:179]\n\tds_read_b64 a[236:237], v0 offset:2048\n\tv_mfma_f32_16x16x32_bf16 v[180:183], a[100:103], v[112:115], v[180:183]\n\tds_read_b64 a[238:239], v0 offset:2056\n\tv_mfma_f32_16x16x32_bf16 v[184:187], a[100:103], v[116:119], v[184:187]\n\tds_read_b64 a[240:241], v0 offset:1536\n\t" \
    "v_mfma_f32_16x16x32_bf16 v[188:191], a[100:103], v[120:123], v[188:191]\n\tds_read_b64 a[242:243], v0 offset:1544\n\tv_mfma_f32_16x16x32_bf16 v[192:195], a[100:103], v[124:127], v[192:195]\n\tds_read_b64 a[244:245], v0 offset:1024\n\tv_mfma_f32_16x16x32_bf16 v[172:175], a[104:107], v[100:103], v[172:175]\n\tds_read_b64 a[246:247], v0 offset:1032\n\tv_mfma_f32_16x16x32_bf16 v[176:179], a[104:107], v[104:107], v[176:179]\n\tds_read_b128 v[68:71], v1 offset:55680\n\t" \
    "v_mfma_f32_16x16x32_bf16 v[180:183], a[104:107], v[108:111], v[180:183]\n\tds_read_b128 v[72:75], v1 offset:56192\n\tv_mfma_f32_16x16x32_bf16 v[184:187], a[104:107], v[112:115], v[184:187]\n\tds_read_b128 v[76:79], v1 offset:56704\n\tv_mfma_f32_16x16x32_bf16 v[188:191], a[104:107], v[116:119], v[188:191]\n\tds_read_b128 v[80:83], v1 offset:57216\n\tv_mfma_f32_16x16x32_bf16 v[192:195], a[104:107], v[120:123], v[192:195]\n\tds_read_b128 v[84:87], v1 offset:57728\n\t" \
    "v_mfma_f32_16x16x32_bf16 v[176:179], a[108:111], v[100:103], v[176:179]\n\tds_read_b128 v[88:91], v1 offset:58240\n\tv_mfma_f32_16x16x32_bf16 v[180:183], a[108:111], v[104:107], v[180:183]\n\tds_read_b128 v[92:95], v1 offset:58752\n\tv_mfma_f32_16x16x32_bf16 v[184:187], a[108:111], v[108:111], v[184:187]\n\tds_read_b128 v[96:99], v1 offset:59264\n\tv_mfma_f32_16x16x32_bf16 v[188:191], a[108:111], v[112:115], v[188:191]\n\tds_read_b64 a[248:249], v0 offset:512\n\t" \
    "v_mfma_f32_16x16x32_bf16 v[192:195], a[108:111], v[116:119], v[192:195]\n\tds_read_b64 a[250:251], v0 offset:520\n\tv_mfma_f32_16x16x32_bf16 v[180:183], a[112:115], v[100:103], v[180:183]\n\tds_read_b64 a[252:253], v0 offset:0\n\tv_mfma_f32_16x16x32_bf16 v[184:187], a[112:115], v[104:107], v[184:187]\n\tds_read_b64 a[254:255], v0 offset:8\n\tv_mfma_f32_16x16x32_bf16 v[188:191], a[112:115], v[108:111], v[188:191]\n\tv_mfma_f32_16x16x32_bf16 v[192:195], a[112:115], v[112:115], v[192:195]\n\t" \
    "v_mfma_f32_16x16x32_bf16 v[184:187], a[116:119], v[100:103], v[184:187]\n\tv_mfma_f32_16x16x32_bf16 v[188:191], a[116:119], v[104:107], v[188:191]\n\tv_mfma_f32_16x16x32_bf16 v[192:195], a[116:119], v[108:111], v[192:195]\n\tv_mfma_f32_16x16x32_bf16 v[188:191], a[120:123], v[100:103], v[188:191]\n\tv_mfma_f32_16x16x32_bf16 v[192:195], a[120:123], v[104:107], v[192:195]\n\tv_mfma_f32_16x16x32_bf16 v[192:195], a[124:127], v[100:103], v[192:195]\n\ts_waitcnt lgkmcnt(4)\n\tv_mfma_f32_16x16x32_bf16 v[164:167], a[128:131], v[68:71], v[164:167]\n\t" \
    "ds_read_b128 v[100:103], v1 offset:55616\n\tv_mfma_f32_16x16x32_bf16 v[168:171], a[128:131], v[72:75], v[168:171]\n\tds_read_b128 v[104:107], v1 offset:56128\n\tv_mfma_f32_16x16x32_bf16 v[172:175], a[128:131], v[76:79], v[172:175]\n\tds_read_b128 v[108:111], v1 offset:56640\n\tv_mfma_f32_16x16x32_bf16 v[176:179], a[128:131], v[80:83], v[176:179]\n\tds_read_b128 v[112:115], v1 offset:57152\n\tv_mfma_f32_16x16x32_bf16 v[180:183], a[128:131], v[84:87], v[180:183]\n\t" \
    "ds_read_b128 v[116:119], v1 offset:57664\n\tv_mfma_f32_16x16x32_bf16 v[184:187], a[128:131], v[88:91], v[184:187]\n\tds_read_b128 v[120:123], v1 offset:58176\n\tv_mfma_f32_16x16x32_bf16 v[188:191], a[128:131], v[92:95], v[188:191]\n\tds_read_b128 v[124:127], v1 offset:58688\n\tv_mfma_f32_16x16x32_bf16 v[192:195], a[128:131], v[96:99], v[192:195]\n\tds_read_b128 v[128:131], v1 offset:59200\n\tv_mfma_f32_16x16x32_bf16 v[168:171], a[132:135], v[68:71], v[168:171]\n\t" \
    "v_mfma_f32_16x16x32_bf16 v[172:175], a[132:135], v[72:75], v[172:175]\n\tv_mfma_f32_16x16x32_bf16 v[176:179], a[132:135], v[76:79], v[176:179]\n\tv_mfma_f32_16x16x32_bf16 v[180:183], a[132:135], v[80:83], v[180:183]\n\tv_mfma_f32_16x16x32_bf16 v[184:187], a[132:135], v[84:87], v[184:187]\n\tv_mfma_f32_16x16x32_bf16 v[188:191], a[132:135], v[88:91], v[188:191]\n\tv_mfma_f32_16x16x32_bf16 v[192:195], a[132:135], v[92:95], v[192:195]\n\tv_mfma_f32_16x16x32_bf16 v[172:175], a[136:139], v[68:71], v[172:175]\n\tv_mfma_f32_16x16x32_bf16 v[176:179], a[136:139], v[72:75], v[176:179]\n\t" \
    "v_mfma_f32_16x16x32_bf16 v[180:183], a[136:139], v[76:79], v[180:183]\n\tv_mfma_f32_16x16x32_bf16 v[184:187], a[136:139], v[80:83], v[184:187]\n\tbuffer_load_dword v15, v7, s[16:19], s42 offen\n\tv_mfma_f32_16x16x32_bf16 v[188:191], a[136:139], v[84:87], v[188:191]\n\ts_add_u32 s43, s43, 1\n\tv_mfma_f32_16x16x32_bf16 v[192:195], a[136:139], v[88:91], v[192:195]\n\tv_readlane_b32 s42, v237, s43\n\ts_nop 4\n\t" \
    "v_mfma_f32_16x16x32_bf16 v[176:179], a[140:143], v[68:71], v[176:179]\n\tv_mfma_f32_16x16x32_bf16 v[180:183], a[140:143], v[72:75], v[180:183]\n\tv_mfma_f32_16x16x32_bf16 v[184:187], a[140:143], v[76:79], v[184:187]\n\tv_mfma_f32_16x16x32_bf16 v[188:191], a[140:143], v[80:83], v[188:191]\n\tv_mfma_f32_16x16x32_bf16 v[192:195], a[140:143], v[84:87], v[192:195]\n\tv_mfma_f32_16x16x32_bf16 v[180:183], a[144:147], v[68:71], v[180:183]\n\tv_mfma_f32_16x16x32_bf16 v[184:187], a[144:147], v[72:75], v[184:187]\n\tv_mfma_f32_16x16x32_bf16 v[188:191], a[144:147], v[76:79], v[188:191]\n\t" \
    "v_mfma_f32_16x16x32_bf16 v[192:195], a[144:147], v[80:83], v[192:195]\n\tv_mfma_f32_16x16x32_bf16 v[184:187], a[148:151], v[68:71], v[184:187]\n\tv_mfma_f32_16x16x32_bf16 v[188:191], a[148:151], v[72:75], v[188:191]\n\tv_mfma_f32_16x16x32_bf16 v[192:195], a[148:151], v[76:79], v[192:195]\n\tv_mfma_f32_16x16x32_bf16 v[188:191], a[152:155], v[68:71], v[188:191]\n\tv_mfma_f32_16x16x32_bf16 v[192:195], a[152:155], v[72:75], v[192:195]\n\tv_mfma_f32_16x16x32_bf16 v[192:195], a[156:159], v[68:71], v[192:195]\n\ts_waitcnt lgkmcnt(0)\n\t" \
    "v_mfma_f32_16x16x32_bf16 v[164:167], a[160:163], v[100:103], v[164:167]\n\tds_read_b128 v[68:71], v1 offset:55552\n\tv_mfma_f32_16x16x32_bf16 v[168:171], a[160:163], v[104:107], v[168:171]\n\tds_read_b128 v[72:75], v1 offset:56064\n\tv_mfma_f32_16x16x32_bf16 v[172:175], a[160:163], v[108:111], v[172:175]\n\tds_read_b128 v[76:79], v1 offset:56576\n\tv_mfma_f32_16x16x32_bf16 v[176:179], a[160:163], v[112:115], v[176:179]\n\tds_read_b128 v[80:83], v1 offset:57088\n\t" \
    "v_mfma_f32_16x16x32_bf16 v[180:183], a[160:163], v[116:119], v[180:183]\n\tds_read_b128 v[84:87], v1 offset:57600\n\tv_mfma_f32_16x16x32_bf16 v[184:187], a[160:163], v[120:123], v[184:187]\n\tds_read_b128 v[88:91], v1 offset:58112\n\tv_mfma_f32_16x16x32_bf16 v[188:191], a[160:163], v[124:127], v[188:191]\n\tds_read_b128 v[92:95], v1 offset:58624\n\tv_mfma_f32_16x16x32_bf16 v[192:195], a[160:163], v[128:131], v[192:195]\n\tds_read_b128 v[96:99], v1 offset:59136\n\t" \
    "v_mfma_f32_16x16x32_bf16 v[168:171], a[164:167], v[100:103], v[168:171]\n\tv_mfma_f32_16x16x32_bf16 v[172:175], a[164:167], v[104:107], v[172:175]\n\tv_mfma_f32_16x16x32_bf16 v[176:179], a[164:167], v[108:111], v[176:179]\n\tv_mfma_f32_16x16x32_bf16 v[180:183], a[164:167], v[112:115], v[180:183]\n\tv_mfma_f32_16x16x32_bf16 v[184:187], a[164:167], v[116:119], v[184:187]\n\tv_mfma_f32_16x16x32_bf16 v[188:191], a[164:167], v[120:123], v[188:191]\n\tv_mfma_f32_16x16x32_bf16 v[192:195], a[164:167], v[124:127], v[192:195]\n\tv_mfma_f32_16x16x32_bf16 v[172:175], a[168:171], v[100:103], v[172:175]\n\t" \
    "v_mfma_f32_16x16x32_bf16 v[176:179], a[168:171], v[104:107], v[176:179]\n\tv_mfma_f32_16x16x32_bf16 v[180:183], a[168:171], v[108:111], v[180:183]\n\tv_mfma_f32_16x16x32_bf16 v[184:187], a[168:171], v[112:115], v[184:187]\n\tv_mfma_f32_16x16x32_bf16 v[188:191], a[168:171], v[116:119], v[188:191]\n\tv_mfma_f32_16x16x32_bf16 v[192:195], a[168:171], v[120:123], v[192:195]\n\tv_mfma_f32_16x16x32_bf16 v[176:179], a[172:175], v[100:103], v[176:179]\n\tv_mfma_f32_16x16x32_bf16 v[180:183], a[172:175], v[104:107], v[180:183]\n\tv_mfma_f32_16x16x32_bf16 v[184:187], a[172:175], v[108:111], v[184:187]\n\t" \
    "v_mfma_f32_16x16x32_bf16 v[188:191], a[172:175], v[112:115], v[188:191]\n\tv_mfma_f32_16x16x32_bf16 v[192:195], a[172:175], v[116:119], v[192:195]\n\tv_mfma_f32_16x16x32_bf16 v[180:183], a[176:179], v[100:103], v[180:183]\n\tv_mfma_f32_16x16x32_bf16 v[184:187], a[176:179], v[104:107], v[184:187]\n\tv_mfma_f32_16x16x32_bf16 v[188:191], a[176:179], v[108:111], v[188:191]\n\tv_mfma_f32_16x16x32_bf16 v[192:195], a[176:179], v[112:115], v[192:195]\n\tv_mfma_f32_16x16x32_bf16 v[184:187], a[180:183], v[100:103], v[184:187]\n\tv_mfma_f32_16x16x32_bf16 v[188:191], a[180:183], v[104:107], v[188:191]\n\t" \
    "v_mfma_f32_16x16x32_bf16 v[192:195], a[180:183], v[108:111], v[192:195]\n\tv_mfma_f32_16x16x32_bf16 v[188:191], a[184:187], v[100:103], v[188:191]\n\tv_mfma_f32_16x16x32_bf16 v[192:195], a[184:187], v[104:107], v[192:195]\n\tv_mfma_f32_16x16x32_bf16 v[192:195], a[188:191], v[100:103], v[192:195]\n\ts_waitcnt lgkmcnt(0)\n\tv_mfma_f32_16x16x32_bf16 v[164:167], a[192:195], v[68:71], v[164:167]\n\tds_read_b128 v[100:103], v1 offset:55488\n\tv_mfma_f32_16x16x32_bf16 v[168:171], a[192:195], v[72:75], v[168:171]\n\t" \
    "ds_read_b128 v[104:107], v1 offset:56000\n\tv_mfma_f32_16x16x32_bf16 v[172:175], a[192:195], v[76:79], v[172:175]\n\tds_read_b128 v[108:111], v1 offset:56512\n\tv_mfma_f32_16x16x32_bf16 v[176:179], a[192:195], v[80:83], v[176:179]\n\tds_read_b128 v[112:115], v1 offset:57024\n\tv_mfma_f32_16x16x32_bf16 v[180:183], a[192:195], v[84:87], v[180:183]\n\tds_read_b128 v[116:119], v1 offset:57536\n\tv_mfma_f32_16x16x32_bf16 v[184:187], a[192:195], v[88:91], v[184:187]\n\t" \
    "ds_read_b128 v[120:123], v1 offset:58048\n\tv_mfma_f32_16x16x32_bf16 v[188:191], a[192:195], v[92:95], v[188:191]\n\tds_read_b128 v[124:127], v1 offset:58560\n\tv_mfma_f32_16x16x32_bf16 v[192:195], a[192:195], v[96:99], v[192:195]\n\tds_read_b128 v[128:131], v1 offset:59072\n\tv_mfma_f32_16x16x32_bf16 v[168:171], a[196:199], v[68:71], v[168:171]\n\tv_mfma_f32_16x16x32_bf16 v[172:175], a[196:199], v[72:75], v[172:175]\n\tv_mfma_f32_16x16x32_bf16 v[176:179], a[196:199], v[76:79], v[176:179]\n\t" \
    "v_mfma_f32_16x16x32_bf16 v[180:183], a[196:199], v[80:83], v[180:183]\n\tv_mfma_f32_16x16x32_bf16 v[184:187], a[196:199], v[84:87], v[184:187]\n\tv_mfma_f32_16x16x32_bf16 v[188:191], a[196:199], v[88:91], v[188:191]\n\tv_mfma_f32_16x16x32_bf16 v[192:195], a[196:199], v[92:95], v[192:195]\n\tv_mfma_f32_16x16x32_bf16 v[172:175], a[200:203], v[68:71], v[172:175]\n\tv_mfma_f32_16x16x32_bf16 v[176:179], a[200:203], v[72:75], v[176:179]\n\tv_mfma_f32_16x16x32_bf16 v[180:183], a[200:203], v[76:79], v[180:183]\n\tv_mfma_f32_16x16x32_bf16 v[184:187], a[200:203], v[80:83], v[184:187]\n\t" \
    "v_mfma_f32_16x16x32_bf16 v[188:191], a[200:203], v[84:87], v[188:191]\n\tv_mfma_f32_16x16x32_bf16 v[192:195], a[200:203], v[88:91], v[192:195]\n\tv_mfma_f32_16x16x32_bf16 v[176:179], a[204:207], v[68:71], v[176:179]\n\tv_mfma_f32_16x16x32_bf16 v[180:183], a[204:207], v[72:75], v[180:183]\n\tv_mfma_f32_16x16x32_bf16 v[184:187], a[204:207], v[76:79], v[184:187]\n\tv_mfma_f32_16x16x32_bf16 v[188:191], a[204:207], v[80:83], v[188:191]\n\tv_mfma_f32_16x16x32_bf16 v[192:195], a[204:207], v[84:87], v[192:195]\n\tv_mfma_f32_16x16x32_bf16 v[180:183], a[208:211], v[68:71], v[180:183]\n\t" \
    "v_mfma_f32_16x16x32_bf16 v[184:187], a[208:211], v[72:75], v[184:187]\n\tv_mfma_f32_16x16x32_bf16 v[188:191], a[208:211], v[76:79], v[188:191]\n\tv_mfma_f32_16x16x32_bf16 v[192:195], a[208:211], v[80:83], v[192:195]\n\tv_mfma_f32_16x16x32_bf16 v[184:187], a[212:215], v[68:71], v[184:187]\n\tv_mfma_f32_16x16x32_bf16 v[188:191], a[212:215], v[72:75], v[188:191]\n\tv_mfma_f32_16x16x32_bf16 v[192:195], a[212:215], v[76:79], v[192:195]\n\tv_mfma_f32_16x16x32_bf16 v[188:191], a[216:219], v[68:71], v[188:191]\n\tv_mfma_f32_16x16x32_bf16 v[192:195], a[216:219], v[72:75], v[192:195]\n\t" \
    "v_mfma_f32_16x16x32_bf16 v[192:195], a[220:223], v[68:71], v[192:195]\n\ts_waitcnt lgkmcnt(0)\n\tv_mfma_f32_16x16x32_bf16 v[164:167], a[224:227], v[100:103], v[164:167]\n\ts_waitcnt vmcnt(1)\n\tbuffer_store_dwordx2 v[248:249], off, s[8:11], s51 sc1\n\tv_mfma_f32_16x16x32_bf16 v[168:171], a[224:227], v[104:107], v[168:171]\n\tv_mfma_f32_16x16x32_bf16 v[172:175], a[224:227], v[108:111], v[172:175]\n\tv_mfma_f32_16x16x32_bf16 v[176:179], a[224:227], v[112:115], v[176:179]\n\t" \
    "v_mfma_f32_16x16x32_bf16 v[180:183], a[224:227], v[116:119], v[180:183]\n\tv_mfma_f32_16x16x32_bf16 v[184:187], a[224:227], v[120:123], v[184:187]\n\tv_mfma_f32_16x16x32_bf16 v[188:191], a[224:227], v[124:127], v[188:191]\n\tv_mfma_f32_16x16x32_bf16 v[192:195], a[224:227], v[128:131], v[192:195]\n\tv_mfma_f32_16x16x32_bf16 v[168:171], a[228:231], v[100:103], v[168:171]\n\tv_mfma_f32_16x16x32_bf16 v[172:175], a[228:231], v[104:107], v[172:175]\n\tv_mfma_f32_16x16x32_bf16 v[176:179], a[228:231], v[108:111], v[176:179]\n\tv_mfma_f32_16x16x32_bf16 v[180:183], a[228:231], v[112:115], v[180:183]\n\t" \
    "v_mfma_f32_16x16x32_bf16 v[184:187], a[228:231], v[116:119], v[184:187]\n\tv_mfma_f32_16x16x32_bf16 v[188:191], a[228:231], v[120:123], v[188:191]\n\tv_mfma_f32_16x16x32_bf16 v[192:195], a[228:231], v[124:127], v[192:195]\n\tv_mfma_f32_16x16x32_bf16 v[172:175], a[232:235], v[100:103], v[172:175]\n\tv_mfma_f32_16x16x32_bf16 v[176:179], a[232:235], v[104:107], v[176:179]\n\tv_mfma_f32_16x16x32_bf16 v[180:183], a[232:235], v[108:111], v[180:183]\n\tv_mfma_f32_16x16x32_bf16 v[184:187], a[232:235], v[112:115], v[184:187]\n\tv_mfma_f32_16x16x32_bf16 v[188:191], a[232:235], v[116:119], v[188:191]\n\t" \
    "v_mfma_f32_16x16x32_bf16 v[192:195], a[232:235], v[120:123], v[192:195]\n\tv_mfma_f32_16x16x32_bf16 v[176:179], a[236:239], v[100:103], v[176:179]\n\tv_mfma_f32_16x16x32_bf16 v[180:183], a[236:239], v[104:107], v[180:183]\n\tv_mfma_f32_16x16x32_bf16 v[184:187], a[236:239], v[108:111], v[184:187]\n\tv_mfma_f32_16x16x32_bf16 v[188:191], a[236:239], v[112:115], v[188:191]\n\tv_mfma_f32_16x16x32_bf16 v[192:195], a[236:239], v[116:119], v[192:195]\n\tv_mfma_f32_16x16x32_bf16 v[180:183], a[240:243], v[100:103], v[180:183]\n\tv_mfma_f32_16x16x32_bf16 v[184:187], a[240:243], v[104:107], v[184:187]\n\t" \
    "v_mfma_f32_16x16x32_bf16 v[188:191], a[240:243], v[108:111], v[188:191]\n\tv_mfma_f32_16x16x32_bf16 v[192:195], a[240:243], v[112:115], v[192:195]\n\tv_mfma_f32_16x16x32_bf16 v[184:187], a[244:247], v[100:103], v[184:187]\n\tv_mfma_f32_16x16x32_bf16 v[188:191], a[244:247], v[104:107], v[188:191]\n\tv_mfma_f32_16x16x32_bf16 v[192:195], a[244:247], v[108:111], v[192:195]\n\tv_mfma_f32_16x16x32_bf16 v[188:191], a[248:251], v[100:103], v[188:191]\n\tv_mfma_f32_16x16x32_bf16 v[192:195], a[248:251], v[104:107], v[192:195]\n\tv_mfma_f32_16x16x32_bf16 v[192:195], a[252:255], v[100:103], v[192:195]\n\t" \
    "v_cvt_pk_bf16_f32 v164, v164, v165\n\tv_cvt_pk_bf16_f32 v165, v166, v167\n\tds_write_b64 v3, v[164:165] offset:55488\n\tv_cvt_pk_bf16_f32 v168, v168, v169\n\tv_cvt_pk_bf16_f32 v169, v170, v171\n\tds_write_b64 v3, v[168:169] offset:56000\n\tv_cvt_pk_bf16_f32 v172, v172, v173\n\tv_cvt_pk_bf16_f32 v173, v174, v175\n\t" \
    "ds_write_b64 v3, v[172:173] offset:56512\n\tv_cvt_pk_bf16_f32 v176, v176, v177\n\tv_cvt_pk_bf16_f32 v177, v178, v179\n\tds_write_b64 v3, v[176:177] offset:57024\n\tv_cvt_pk_bf16_f32 v180, v180, v181\n\tv_cvt_pk_bf16_f32 v181, v182, v183\n\tds_write_b64 v3, v[180:181] offset:57536\n\tv_cvt_pk_bf16_f32 v184, v184, v185\n\t" \
    "v_cvt_pk_bf16_f32 v185, v186, v187\n\tds_write_b64 v3, v[184:185] offset:58048\n\tv_cvt_pk_bf16_f32 v188, v188, v189\n\tv_cvt_pk_bf16_f32 v189, v190, v191\n\tds_write_b64 v3, v[188:189] offset:58560\n\tv_cvt_pk_bf16_f32 v192, v192, v193\n\tv_cvt_pk_bf16_f32 v193, v194, v195\n\tds_write_b64 v3, v[192:193] offset:59072\n\t" \
    "s_waitcnt lgkmcnt(0)\n\ts_barrier\n\ts_add_u32 s51, s44, 12\n\ts_mul_i32 s65, s51, 1366\n\ts_lshr_b32 s65, s65, 16\n\ts_mul_i32 s56, s65, 48\n\ts_sub_u32 s56, s51, s56\n\ts_lshl_b32 s56, s56, 6\n\t" \
    "s_lshr_b32 s70, s65, 2\n\ts_mul_i32 s70, s70, 252\n\ts_add_u32 s65, s65, s70\n\ts_mul_i32 s65, s65, 3072\n\ts_add_u32 s56, s56, s65\n\ts_add_u32 s56, s56, 4849664\n\ts_add_u32 s51, s51, s38\n\ts_lshl_b32 s51, s51, 3\n\t" \
    "s_add_u32 s51, s51, 50176\n\ts_or_b32 s56, s56, s45\n\ts_or_b32 s51, s51, s45\n\tds_read_b64_tr_b16 v[204:205], v4 offset:55488\n\tds_read_b64_tr_b16 v[206:207], v4 offset:55496\n\tds_read_b64_tr_b16 v[208:209], v4 offset:55552\n\tds_read_b64_tr_b16 v[210:211], v4 offset:55560\n\tds_read_b64_tr_b16 v[212:213], v4 offset:56000\n\t" \
    "ds_read_b64_tr_b16 v[214:215], v4 offset:56008\n\tds_read_b64_tr_b16 v[216:217], v4 offset:56064\n\tds_read_b64_tr_b16 v[218:219], v4 offset:56072\n\ts_waitcnt lgkmcnt(6)\n\tbuffer_store_dwordx4 v[204:207], v5, s[8:11], s56 offen sc1\n\ts_add_u32 s56, s56, 49152\n\ts_waitcnt lgkmcnt(4)\n\tbuffer_store_dwordx4 v[208:211], v5, s[8:11], s56 offen sc1\n\t" \
    "s_add_u32 s56, s56, 49152\n\ts_waitcnt lgkmcnt(2)\n\tbuffer_store_dwordx4 v[212:215], v5, s[8:11], s56 offen sc1\n\ts_add_u32 s56, s56, 49152\n\ts_waitcnt lgkmcnt(0)\n\tbuffer_store_dwordx4 v[216:219], v5, s[8:11], s56 offen sc1\n\ts_add_u32 s56, s56, 49152\n\ts_waitcnt vmcnt(0)\n\t" \
    "buffer_store_dwordx2 v[248:249], off, s[8:11], s51 sc1\n\ts_waitcnt vmcnt(0)\n\t103:\n\t98:\n\ts_nop 1\n\t" \
    ""
#define MAIN_CLOB "s2", "s3", "s4", "s5", "s6", "s7", "s8", "s9", "s10", "s11", "s12", "s13", "s14", "s15", "s16", "s17", "s18", "s19", "s20", "s21", "s22", "s23", "s24", "s25", "s26", "s27", "s28", "s29", "s30", "s31", "s33", "s34", "s35", "s36", "s37", "s39", "s41", "s42", "s43", "s44", "s45", "s46", "s47", "s48", "s49", "s50", "s51", "s52", "s53", "s54", "s55", "s56", "s57", "s58", "s59", "s60", "s61", "s62", "s63", "s64", "s65", "s66", "s67", "s68", "s69", "s70", "s71", "s72", "s73", "s74", "s75", "s76", "s77", "s78", "s79", "s80", "s81", "s82", "s83", "s84", "s85", "s86", "s87", "v0", "v1", "v2", "v3", "v4", "v5", "v6", "v7", "v8", "v9", "v10", "v11", "v12", "v13", "v14", "v15", "v16", "v17", "v18", "v19", "v20", "v21", "v22", "v23", "v24", "v25", "v26", "v27", "v28", "v29", "v30", "v31", "v32", "v33", "v34", "v35", "v36", "v37", "v38", "v39", "v40", "v41", "v42", "v43", "v44", "v45", "v46", "v47", "v48", "v49", "v50", "v51", "v52", "v53", "v54", "v55", "v56", "v57", "v58", "v59", "v60", "v61", "v62", "v63", "v64", "v65", "v66", "v67", "v68", "v69", "v70", "v71", "v72", "v73", "v74", "v75", "v76", "v77", "v78", "v79", "v80", "v81", "v82", "v83", "v84", "v85", "v86", "v87", "v88", "v89", "v90", "v91", "v92", "v93", "v94", "v95", "v96", "v97", "v98", "v99", "v100", "v101", "v102", "v103", "v104", "v105", "v106", "v107", "v108", "v109", "v110", "v111", "v112", "v113", "v114", "v115", "v116", "v117", "v118", "v119", "v120", "v121", "v122", "v123", "v124", "v125", "v126", "v127", "v128", "v129", "v130", "v131", "v132", "v133", "v134", "v135", "v136", "v137", "v138", "v139", "v140", "v141", "v142", "v143", "v144", "v145", "v146", "v147", "v148", "v149", "v150", "v151", "v152", "v153", "v154", "v155", "v156", "v157", "v158", "v159", "v160", "v161", "v162", "v163", "v164", "v165", "v166", "v167", "v168", "v169", "v170", "v171", "v172", "v173", "v174", "v175", "v176", "v177", "v178", "v179", "v180", "v181", "v182", "v183", "v184", "v185", "v186", "v187", "v188", "v189", "v190", "v191", "v192", "v193", "v194", "v195", "v196", "v197", "v198", "v199", "v200", "v201", "v202", "v203", "v204", "v205", "v206", "v207", "v208", "v209", "v210", "v211", "v212", "v213", "v214", "v215", "v216", "v217", "v218", "v219", "v220", "v221", "v222", "v223", "v224", "v225", "v226", "v227", "v228", "v229", "v230", "v231", "v232", "v233", "v234", "v235", "v236", "v237", "v238", "v239", "v241", "v242", "v243", "v244", "v245", "v246", "v247", "v248", "v249", "v250", "v251", "v252", "v253", "a0", "a1", "a2", "a3", "a4", "a5", "a6", "a7", "a8", "a9", "a10", "a11", "a12", "a13", "a14", "a15", "a16", "a17", "a18", "a19", "a20", "a21", "a22", "a23", "a24", "a25", "a26", "a27", "a28", "a29", "a30", "a31", "a32", "a33", "a34", "a35", "a36", "a37", "a38", "a39", "a40", "a41", "a42", "a43", "a44", "a45", "a46", "a47", "a48", "a49", "a50", "a51", "a52", "a53", "a54", "a55", "a56", "a57", "a58", "a59", "a60", "a61", "a62", "a63", "a64", "a65", "a66", "a67", "a68", "a69", "a70", "a71", "a72", "a73", "a74", "a75", "a76", "a77", "a78", "a79", "a80", "a81", "a82", "a83", "a84", "a85", "a86", "a87", "a88", "a89", "a90", "a91", "a92", "a93", "a94", "a95", "a96", "a97", "a98", "a99", "a100", "a101", "a102", "a103", "a104", "a105", "a106", "a107", "a108", "a109", "a110", "a111", "a112", "a113", "a114", "a115", "a116", "a117", "a118", "a119", "a120", "a121", "a122", "a123", "a124", "a125", "a126", "a127", "a128", "a129", "a130", "a131", "a132", "a133", "a134", "a135", "a136", "a137", "a138", "a139", "a140", "a141", "a142", "a143", "a144", "a145", "a146", "a147", "a148", "a149", "a150", "a151", "a152", "a153", "a154", "a155", "a156", "a157", "a158", "a159", "a160", "a161", "a162", "a163", "a164", "a165", "a166", "a167", "a168", "a169", "a170", "a171", "a172", "a173", "a174", "a175", "a176", "a177", "a178", "a179", "a180", "a181", "a182", "a183", "a184", "a185", "a186", "a187", "a188", "a189", "a190", "a191", "a192", "a193", "a194", "a195", "a196", "a197", "a198", "a199", "a200", "a201", "a202", "a203", "a204", "a205", "a206", "a207", "a208", "a209", "a210", "a211", "a212", "a213", "a214", "a215", "a216", "a217", "a218", "a219", "a220", "a221", "a222", "a223", "a224", "a225", "a226", "a227", "a228", "a229", "a230", "a231", "a232", "a233", "a234", "a235", "a236", "a237", "a238", "a239", "a240", "a241", "a242", "a243", "a244", "a245", "a246", "a247", "a248", "a249", "a250", "a251", "a252", "a253", "a254", "a255"

#define ARG(i) const void* p##i, unsigned n##i, unsigned f##i
__global__ __launch_bounds__(256, 1) void fused_kernel(ARG(0), ARG(1), ARG(2), ARG(3), ARG(4), ARG(5), ARG(6), ARG(7), ARG(8), ARG(9), ARG(10),
                                                       ARG(11)) {
#define MAIN_OUT
    const int b = blockIdx.x;
    unsigned tid = threadIdx.x;
    u64 ka = (u64)__builtin_amdgcn_kernarg_segment_ptr();
    int w;
    asm volatile(MAIN_ASM
                 : "=&{s38}"(w), "+{s[0:1]}"(ka), "+{v240}"(tid) MAIN_OUT
                 : "{s40}"(b)
                 : "memory", "scc", "vcc", MAIN_CLOB);
}

extern "C" void kernel_launch(void* const* d_in, const int* in_sizes, int n_in, void* d_out, int out_size,
                              void* d_ws, size_t ws_size, hipStream_t stream) {
    (void)hipFuncSetAttribute((const void*)fused_kernel, hipFuncAttributeMaxDynamicSharedMemorySize, LDS_BYTES);
    const unsigned F = 0x20000u, NX = S_LEN * D_DIM * 2, NW = S_LEN * F_DIM * 2;
    fused_kernel<<<256, 256, LDS_BYTES, stream>>>(d_ws, WS_END, F, d_in[7], NW, F, d_in[9], NW, F, d_in[0], NX, F, d_out, NX, F, d_in[8], F_DIM * 2, F,
                                                  d_in[10], S_LEN * 2, F, d_in[1], NX, F, d_in[2], NX, F, d_in[3], D_DIM * 8, F, d_in[4], NX, F,
                                                  d_in[5], D_DIM * 2, F);
}
```

```cpp
#include <hip/hip_runtime.h>

typedef unsigned long long u64;

#define S_LEN 2048
#define D_DIM 576
#define F_DIM 4608
#define LDS_BYTES 163840

#define WS_END 29163520u
#define MAIN_ASM \
    "s_load_dwordx16 s[8:23], s[0:1], 0\n\ts_getpc_b64 s[2:3]\n\t99:\n\tv_lshrrev_b32 v20, 5, v240\n\tv_mul_u32_u24 v20, 1152, v20\n\tv_and_b32 v21, 31, v240\n\tv_lshl_add_u32 v22, v21, 2, v20\n\tv_lshl_add_u32 v21, v21, 4, v20\n\t" \
    "s_mul_i32 s68, s40, 9216\n\tv_lshrrev_b32 v245, 6, v240\n\tv_and_b32 v240, 63, v240\n\ts_and_b32 s2, s2, 0xffffff80\n\ts_mov_b32 s4, s2\n\ts_mov_b32 s5, s3\n\ts_mov_b32 s6, 98f-99b\n\ts_mov_b32 s7, 0x20000\n\t" \
    "v_lshl_add_u32 v16, v245, 16, v240\n\tv_lshlrev_b32 v16, 7, v16\n\ts_lshr_b32 s65, s40, 3\n\ts_lshl_b32 s73, s65, 13\n\tbuffer_load_dword v57, v16, s[4:7], s73 offen\n\ts_waitcnt lgkmcnt(0)\n\tbuffer_load_dwordx4 v[48:51], v21, s[20:23], s68 offen\n\tbuffer_load_dwordx4 v[52:55], v21, s[20:23], s68 offen offset:512\n\t" \
    "buffer_load_dword v56, v22, s[20:23], s68 offen offset:1024\n\tbuffer_load_dwordx2 v[58:59], off, s[8:11], 0 sc1\n\ts_load_dwordx8 s[76:83], s[0:1], 112\n\ts_load_dwordx4 s[84:87], s[0:1], 144\n\ts_load_dwordx8 s[24:31], s[0:1], 64\n\ts_load_dwordx4 s[32:35], s[0:1], 96\n\tv_readfirstlane_b32 s38, v245\n\tv_lshlrev_b32 v8, 4, v240\n\t" \
    "v_and_b32 v244, 0xf0, v8\n\tv_lshrrev_b32 v245, 4, v240\n\ts_and_b32 s57, s40, 1\n\ts_and_b32 s58, s38, 1\n\ts_cmp_lt_u32 s65, 27\n\ts_cselect_b32 s39, 1, 0\n\ts_cmp_lt_u32 s65, 24\n\ts_cselect_b32 s39, 2, s39\n\t" \
    "s_min_u32 s65, s65, 26\n\ts_mul_i32 s42, s65, 43\n\ts_lshr_b32 s42, s42, 7\n\ts_mul_i32 s66, s42, 3\n\ts_sub_u32 s41, s65, s66\n\ts_lshl_b32 s69, s42, 8\n\ts_bfe_u32 s66, s40, 0x20001\n\ts_mul_i32 s66, s66, 9\n\t" \
    "s_add_u32 s42, s42, s66\n\ts_mul_i32 s43, s41, 903\n\ts_lshr_b32 s43, s43, 7\n\ts_sub_u32 s70, s43, 21\n\ts_mul_i32 s67, s57, 9437184\n\ts_lshl_b32 s66, s66, 8\n\ts_add_u32 s67, s67, s66\n\ts_lshl_b32 s65, s65, 2\n\t" \
    "s_add_u32 s65, s65, s38\n\ts_lshl_b32 s65, s65, 6\n\tv_add_u32 v252, s65, v240\n\tv_mul_u32_u24 v253, 3641, v252\n\tv_lshrrev_b32 v253, 16, v253\n\tv_mul_u32_u24 v253, 54, v253\n\tv_add_lshl_u32 v252, v252, v253, 7\n\tv_lshrrev_b32 v16, 3, v240\n\t" \
    "s_lshl_b32 s73, s38, 3\n\tv_add_u32 v16, s73, v16\n\tv_mul_u32_u24 v16, 9216, v16\n\tv_and_b32 v17, 0x70, v8\n\tv_and_b32 v18, 16, v240\n\tv_lshlrev_b32 v18, 1, v18\n\tv_xor_b32 v17, v17, v18\n\ts_lshl_b32 s73, s57, 1\n\t" \
    "s_add_u32 s73, s73, s58\n\ts_lshl_b32 s73, s73, 6\n\tv_xor_b32 v17, s73, v17\n\tv_add3_u32 v11, v16, v17, s69\n\tv_xor_b32 v10, 128, v11\n\ts_lshl_b32 s54, s38, 10\n\tv_add_u32 v9, 4096, v8\n\tv_lshlrev_b32 v239, 3, v240\n\t" \
    "s_mul_i32 s73, s40, 9216\n\ts_add_u32 s73, s73, s54\n\tv_add_u32 v23, s73, v8\n\tv_subrev_u32 v19, 3456, v23\n\ts_add_u32 s12, s12, s67\n\ts_addc_u32 s13, s13, 0\n\ts_sub_u32 s14, s14, s67\n\ts_cmp_eq_u32 s39, 0\n\t" \
    "s_cselect_b32 s14, 0, s14\n\tv_and_b32 v88, 31, v240\n\tv_lshrrev_b32 v89, 5, v240\n\ts_lshl_b32 s73, s38, 1\n\tv_add_u32 v89, s73, v89\n\tv_mul_u32_u24 v90, 1168, v89\n\tv_add_u32 v90, 153600, v90\n\tv_lshl_add_u32 v91, v88, 2, v90\n\t" \
    "v_lshl_add_u32 v90, v88, 4, v90\n\tv_cmp_eq_u32 vcc, 0, v88\n\tv_lshlrev_b32 v89, 3, v89\n\tv_mov_b32 v92, 0x7ff00000\n\tv_cndmask_b32 v89, v92, v89, vcc\n\tv_bfe_u32 v88, v240, 2, 2\n\tv_and_b32 v92, 3, v240\n\tv_lshlrev_b32 v92, 3, v92\n\t" \
    "v_lshl_add_u32 v92, v245, 5, v92\n\ts_lshl_b32 s73, s38, 7\n\tv_add_u32 v93, s73, v92\n\tv_mul_u32_u24 v94, 1168, v88\n\tv_add_u32 v94, 153600, v94\n\tv_add_u32 v95, v94, v92\n\tv_add_u32 v94, v94, v93\n\tv_mul_u32_u24 v88, 1152, v88\n\t" \
    "v_add_u32 v88, 131072, v88\n\tv_add_u32 v92, v88, v92\n\tv_add_u32 v93, v88, v93\n\tv_lshl_add_u32 v88, v245, 10, v244\n\ts_waitcnt lgkmcnt(0)\n\ts_lshr_b32 s61, s40, 2\n\ts_mul_i32 s61, s61, 36864\n\ts_and_b32 s65, s40, 3\n\t" \
    "s_lshl_b32 s65, s65, 8\n\ts_add_u32 s61, s61, s65\n\ts_add_u32 s61, s61, 131072\n\ts_mul_i32 s62, s40, 9216\n\ts_add_u32 s62, s62, 2490368\n\ts_lshl_b32 s63, s40, 3\n\ts_lshl_b32 s48, s38, 11\n\ts_add_u32 s48, s48, s63\n\t" \
    "s_add_u32 s48, s48, 17408\n\ts_add_u32 s63, s63, 1024\n\ts_cmp_eq_u32 s38, 0\n\ts_cselect_b32 s63, s63, 2146435072\n\ts_lshl_b32 s64, s40, 6\n\ts_add_u32 s64, s64, 58368\n\ts_add_u32 m0, s54, 131072\n\ts_nop 0\n\t" \
    "buffer_load_dwordx4 v19, s[76:79], 0 offen lds\n\ts_and_b32 s4, s40, 7\n\ts_lshl_b32 s47, s4, 11\n\ts_lshl_b32 s65, s57, 10\n\ts_add_u32 s47, s47, s65\n\tv_add_u32 v19, 4096, v19\n\ts_add_u32 m0, s54, 135168\n\ts_nop 0\n\t" \
    "buffer_load_dwordx4 v19, s[76:79], 0 offen lds\n\ts_add_u32 s47, s47, 1024\n\ts_mul_i32 s44, s57, 1179648\n\ts_mul_i32 s65, s41, 12288\n\ts_add_u32 s44, s44, s65\n\tv_add_u32 v19, 4096, v19\n\ts_add_u32 m0, s54, 139264\n\ts_nop 0\n\t" \
    "buffer_load_dwordx4 v19, s[76:79], 0 offen lds\n\ts_mul_i32 s65, s38, 3072\n\ts_add_u32 s44, s44, s65\n\ts_add_u32 s44, s44, 131072\n\ts_lshr_b32 s5, s4, 1\n\tv_add_u32 v19, 4096, v19\n\ts_add_u32 m0, s54, 144384\n\ts_nop 0\n\t" \
    "buffer_load_dwordx4 v23, s[80:83], 0 offen lds\n\tv_bfe_u32 v16, v240, 2, 2\n\tv_and_b32 v17, 3, v240\n\tv_lshlrev_b32 v17, 3, v17\n\tv_lshl_add_u32 v16, v16, 7, v17\n\tv_add_u32 v23, 4096, v23\n\ts_add_u32 m0, s54, 148480\n\ts_nop 0\n\t" \
    "buffer_load_dwordx4 v23, s[80:83], 0 offen lds\n\tv_bfe_u32 v17, v240, 3, 2\n\tv_lshl_add_u32 v16, v17, 5, v16\n\tv_lshl_add_u32 v0, v245, 10, v16\n\ts_and_b32 s65, s5, 1\n\tv_add_u32 v23, 4096, v23\n\tv_subrev_u32 v19, s54, v19\n\tv_subrev_u32 v23, s54, v23\n\t" \
    "s_cmp_eq_u32 s58, 1\n\ts_cselect_b64 vcc, -1, 0\n\ts_cselect_b64 s[76:77], s[80:81], s[76:77]\n\ts_mov_b32 m0, 143360\n\ts_cselect_b32 m0, 152576, m0\n\tv_cndmask_b32 v19, v19, v23, vcc\n\tbuffer_load_dwordx4 v19, s[76:79], 0 offen lds\n\ts_xor_b32 s65, s65, 1\n\t" \
    "s_lshl_b32 s65, s65, 6\n\tv_xor_b32 v17, s65, v16\n\tv_lshl_add_u32 v4, v245, 9, v17\n\ts_lshr_b32 s71, s54, 1\n\tbuffer_load_dwordx2 v[60:61], v239, s[84:87], s71 offen\n\tv_xor_b32 v1, 32, v0\n\tv_xor_b32 v5, 32, v4\n\tv_xor_b32 v2, 64, v0\n\t" \
    "v_xor_b32 v6, 64, v4\n\tbuffer_load_dwordx2 v[62:63], v239, s[84:87], s71 offen offset:2048\n\tv_xor_b32 v3, 96, v0\n\tv_xor_b32 v7, 96, v4\n\tv_add_u32 v224, 0x10000, v4\n\tv_add_u32 v14, 0x10000, v0\n\ts_mov_b32 s71, 4096\n\tbuffer_load_dwordx2 v[64:65], v239, s[84:87], s71 offen\n\t" \
    "v_add_u32 v220, 0x20000, v4\n\tv_add_u32 v225, 0x10000, v5\n\tv_add_u32 v15, 0x10000, v1\n\tv_add_u32 v221, 0x20000, v5\n\ts_cmp_ge_u32 s43, 21\n\ts_cselect_b32 s69, s70, s43\n\ts_mul_i32 s69, s69, 294912\n\ts_add_u32 s69, s69, 0\n\t" \
    "s_add_u32 m0, s54, 0\n\ts_nop 0\n\tbuffer_load_dwordx4 v10, s[12:15], s69 offen lds\n\tv_add_u32 v226, 0x10000, v6\n\tv_add_u32 v237, 0x10000, v2\n\tv_add_u32 v222, 0x20000, v6\n\tv_add_u32 v227, 0x10000, v7\n\ts_cmp_ge_u32 s43, 21\n\t" \
    "s_cselect_b32 s69, s70, s43\n\ts_mul_i32 s69, s69, 294912\n\ts_add_u32 s69, s69, 0\n\ts_add_u32 m0, s54, 4096\n\ts_nop 0\n\tbuffer_load_dwordx4 v11, s[12:15], s69 offen lds\n\tv_add_u32 v238, 0x10000, v3\n\tv_add_u32 v223, 0x20000, v7\n\t" \
    "s_lshl_b32 s72, s40, 2\n\ts_add_u32 s72, s72, s38\n\ts_waitcnt vmcnt(12)\n\ts_load_dwordx8 s[76:83], s[0:1], 160\n\tv_lshlrev_b32 v24, 16, v48\n\tv_and_b32 v25, 0xffff0000, v48\n\tv_lshlrev_b32 v26, 16, v49\n\tv_and_b32 v27, 0xffff0000, v49\n\t" \
    "v_lshlrev_b32 v28, 16, v50\n\tv_and_b32 v29, 0xffff0000, v50\n\tv_lshlrev_b32 v30, 16, v51\n\tv_and_b32 v31, 0xffff0000, v51\n\tv_lshlrev_b32 v32, 16, v52\n\tv_and_b32 v33, 0xffff0000, v52\n\tv_lshlrev_b32 v34, 16, v53\n\tv_and_b32 v35, 0xffff0000, v53\n\t" \
    "v_lshlrev_b32 v36, 16, v54\n\tv_and_b32 v37, 0xffff0000, v54\n\tv_lshlrev_b32 v38, 16, v55\n\tv_and_b32 v39, 0xffff0000, v55\n\tv_lshlrev_b32 v40, 16, v56\n\tv_and_b32 v41, 0xffff0000, v56\n\tv_pk_add_f32 v[70:71], v[24:25], v[26:27]\n\tv_pk_add_f32 v[72:73], v[28:29], v[30:31]\n\t" \
    "v_pk_add_f32 v[74:75], v[32:33], v[34:35]\n\tv_pk_add_f32 v[76:77], v[36:37], v[38:39]\n\tv_pk_add_f32 v[70:71], v[70:71], v[72:73]\n\tv_pk_add_f32 v[74:75], v[74:75], v[76:77]\n\tv_pk_add_f32 v[70:71], v[70:71], v[40:41]\n\tv_pk_add_f32 v[70:71], v[70:71], v[74:75]\n\tv_add_f32 v66, v70, v71\n\ts_nop 1\n\t" \
    "v_add_f32_dpp v66, v66, v66 row_ror:8 row_mask:0xf bank_mask:0xf\n\ts_nop 1\n\tv_add_f32_dpp v66, v66, v66 row_ror:4 row_mask:0xf bank_mask:0xf\n\ts_nop 1\n\tv_add_f32_dpp v66, v66, v66 row_ror:2 row_mask:0xf bank_mask:0xf\n\ts_nop 1\n\tv_add_f32_dpp v66, v66, v66 row_ror:1 row_mask:0xf bank_mask:0xf\n\tv_mov_b32 v67, v66\n\t" \
    "s_nop 1\n\tv_permlane16_swap_b32 v66, v67\n\tv_add_f32 v66, v66, v67\n\tv_mul_f32 v66, 0x3ae38e39, v66\n\tv_pk_add_f32 v[70:71], v[24:25], v[66:67] op_sel_hi:[1,0] neg_lo:[0,1] neg_hi:[0,1]\n\tv_pk_add_f32 v[72:73], v[26:27], v[66:67] op_sel_hi:[1,0] neg_lo:[0,1] neg_hi:[0,1]\n\tv_pk_add_f32 v[74:75], v[28:29], v[66:67] op_sel_hi:[1,0] neg_lo:[0,1] neg_hi:[0,1]\n\tv_pk_add_f32 v[76:77], v[30:31], v[66:67] op_sel_hi:[1,0] neg_lo:[0,1] neg_hi:[0,1]\n\t" \
    "v_pk_add_f32 v[78:79], v[32:33], v[66:67] op_sel_hi:[1,0] neg_lo:[0,1] neg_hi:[0,1]\n\tv_pk_add_f32 v[80:81], v[34:35], v[66:67] op_sel_hi:[1,0] neg_lo:[0,1] neg_hi:[0,1]\n\tv_pk_add_f32 v[82:83], v[36:37], v[66:67] op_sel_hi:[1,0] neg_lo:[0,1] neg_hi:[0,1]\n\tv_pk_add_f32 v[84:85], v[38:39], v[66:67] op_sel_hi:[1,0] neg_lo:[0,1] neg_hi:[0,1]\n\tv_pk_add_f32 v[86:87], v[40:41], v[66:67] op_sel_hi:[1,0] neg_lo:[0,1] neg_hi:[0,1]\n\tv_pk_mul_f32 v[44:45], v[70:71], v[70:71]\n\tv_pk_mul_f32 v[46:47], v[72:73], v[72:73]\n\tv_pk_fma_f32 v[44:45], v[74:75], v[74:75], v[44:45]\n\t" \
    "v_pk_fma_f32 v[46:47], v[76:77], v[76:77], v[46:47]\n\tv_pk_fma_f32 v[44:45], v[78:79], v[78:79], v[44:45]\n\tv_pk_fma_f32 v[46:47], v[80:81], v[80:81], v[46:47]\n\tv_pk_fma_f32 v[44:45], v[82:83], v[82:83], v[44:45]\n\tv_pk_fma_f32 v[46:47], v[84:85], v[84:85], v[46:47]\n\tv_pk_fma_f32 v[44:45], v[86:87], v[86:87], v[44:45]\n\tv_pk_add_f32 v[44:45], v[44:45], v[46:47]\n\tv_add_f32 v68, v44, v45\n\t" \
    "s_nop 1\n\tv_add_f32_dpp v68, v68, v68 row_ror:8 row_mask:0xf bank_mask:0xf\n\ts_nop 1\n\tv_add_f32_dpp v68, v68, v68 row_ror:4 row_mask:0xf bank_mask:0xf\n\ts_nop 1\n\tv_add_f32_dpp v68, v68, v68 row_ror:2 row_mask:0xf bank_mask:0xf\n\ts_nop 1\n\tv_add_f32_dpp v68, v68, v68 row_ror:1 row_mask:0xf bank_mask:0xf\n\t" \
    "v_mov_b32 v69, v68\n\ts_nop 1\n\tv_permlane16_swap_b32 v68, v69\n\tv_add_f32 v68, v68, v69\n\tv_mul_f32 v68, 0x3ae38e39, v68\n\tv_cvt_pk_bf16_f32 v68, v68, v68\n\tv_lshlrev_b32 v68, 16, v68\n\tv_add_f32 v68, 0x3727c5ac, v68\n\t" \
    "v_cvt_pk_bf16_f32 v68, v68, v68\n\tv_lshlrev_b32 v68, 16, v68\n\tv_sqrt_f32 v68, v68\n\tv_cvt_pk_bf16_f32 v66, v66, v66\n\tv_lshlrev_b32 v66, 16, v66\n\tv_cvt_pk_bf16_f32 v68, v68, v68\n\tv_lshlrev_b32 v68, 16, v68\n\tv_rcp_f32 v68, v68\n\t" \
    "s_nop 0\n\tv_mul_f32 v69, v66, v68\n\tv_xor_b32 v69, 0x80000000, v69\n\tv_pk_fma_f32 v[24:25], v[24:25], v[68:69], v[68:69] op_sel:[0,0,1] op_sel_hi:[1,0,1]\n\tv_pk_fma_f32 v[26:27], v[26:27], v[68:69], v[68:69] op_sel:[0,0,1] op_sel_hi:[1,0,1]\n\tv_pk_fma_f32 v[28:29], v[28:29], v[68:69], v[68:69] op_sel:[0,0,1] op_sel_hi:[1,0,1]\n\tv_pk_fma_f32 v[30:31], v[30:31], v[68:69], v[68:69] op_sel:[0,0,1] op_sel_hi:[1,0,1]\n\tv_pk_fma_f32 v[32:33], v[32:33], v[68:69], v[68:69] op_sel:[0,0,1] op_sel_hi:[1,0,1]\n\t" \
    "v_pk_fma_f32 v[34:35], v[34:35], v[68:69], v[68:69] op_sel:[0,0,1] op_sel_hi:[1,0,1]\n\tv_pk_fma_f32 v[36:37], v[36:37], v[68:69], v[68:69] op_sel:[0,0,1] op_sel_hi:[1,0,1]\n\tv_pk_fma_f32 v[38:39], v[38:39], v[68:69], v[68:69] op_sel:[0,0,1] op_sel_hi:[1,0,1]\n\tv_pk_fma_f32 v[40:41], v[40:41], v[68:69], v[68:69] op_sel:[0,0,1] op_sel_hi:[1,0,1]\n\tv_cvt_pk_bf16_f32 v48, v24, v25\n\tv_cvt_pk_bf16_f32 v49, v26, v27\n\tv_cvt_pk_bf16_f32 v50, v28, v29\n\tv_cvt_pk_bf16_f32 v51, v30, v31\n\t" \
    "v_cvt_pk_bf16_f32 v52, v32, v33\n\tv_cvt_pk_bf16_f32 v53, v34, v35\n\tv_cvt_pk_bf16_f32 v54, v36, v37\n\tv_cvt_pk_bf16_f32 v55, v38, v39\n\tv_cvt_pk_bf16_f32 v56, v40, v41\n\tv_lshlrev_b32 v24, 16, v48\n\tv_and_b32 v25, 0xffff0000, v48\n\tv_lshlrev_b32 v26, 16, v49\n\t" \
    "v_and_b32 v27, 0xffff0000, v49\n\tv_lshlrev_b32 v28, 16, v50\n\tv_and_b32 v29, 0xffff0000, v50\n\tv_lshlrev_b32 v30, 16, v51\n\tv_and_b32 v31, 0xffff0000, v51\n\tv_lshlrev_b32 v32, 16, v52\n\tv_and_b32 v33, 0xffff0000, v52\n\tv_lshlrev_b32 v34, 16, v53\n\t" \
    "v_and_b32 v35, 0xffff0000, v53\n\tv_lshlrev_b32 v36, 16, v54\n\tv_and_b32 v37, 0xffff0000, v54\n\tv_lshlrev_b32 v38, 16, v55\n\tv_and_b32 v39, 0xffff0000, v55\n\tv_lshlrev_b32 v40, 16, v56\n\tv_and_b32 v41, 0xffff0000, v56\n\tv_pk_add_f32 v[70:71], v[24:25], v[26:27]\n\t" \
    "v_pk_add_f32 v[72:73], v[28:29], v[30:31]\n\tv_pk_add_f32 v[74:75], v[32:33], v[34:35]\n\tv_pk_add_f32 v[76:77], v[36:37], v[38:39]\n\tv_pk_mul_f32 v[44:45], v[24:25], v[24:25]\n\tv_pk_mul_f32 v[46:47], v[26:27], v[26:27]\n\tv_pk_add_f32 v[70:71], v[70:71], v[72:73]\n\tv_pk_add_f32 v[74:75], v[74:75], v[76:77]\n\tv_pk_fma_f32 v[44:45], v[28:29], v[28:29], v[44:45]\n\t" \
    "v_pk_fma_f32 v[46:47], v[30:31], v[30:31], v[46:47]\n\tv_pk_add_f32 v[70:71], v[70:71], v[40:41]\n\tv_pk_fma_f32 v[44:45], v[32:33], v[32:33], v[44:45]\n\tv_pk_fma_f32 v[46:47], v[34:35], v[34:35], v[46:47]\n\tv_pk_add_f32 v[70:71], v[70:71], v[74:75]\n\tv_pk_fma_f32 v[44:45], v[36:37], v[36:37], v[44:45]\n\tv_pk_fma_f32 v[46:47], v[38:39], v[38:39], v[46:47]\n\tv_pk_fma_f32 v[44:45], v[40:41], v[40:41], v[44:45]\n\t" \
    "v_pk_add_f32 v[44:45], v[44:45], v[46:47]\n\tv_add_f32 v66, v70, v71\n\tv_add_f32 v42, v44, v45\n\ts_nop 0\n\tv_add_f32_dpp v66, v66, v66 row_ror:8 row_mask:0xf bank_mask:0xf\n\tv_add_f32_dpp v42, v42, v42 row_ror:8 row_mask:0xf bank_mask:0xf\n\ts_nop 0\n\tv_add_f32_dpp v66, v66, v66 row_ror:4 row_mask:0xf bank_mask:0xf\n\t" \
    "v_add_f32_dpp v42, v42, v42 row_ror:4 row_mask:0xf bank_mask:0xf\n\ts_nop 0\n\tv_add_f32_dpp v66, v66, v66 row_ror:2 row_mask:0xf bank_mask:0xf\n\tv_add_f32_dpp v42, v42, v42 row_ror:2 row_mask:0xf bank_mask:0xf\n\ts_nop 0\n\tv_add_f32_dpp v66, v66, v66 row_ror:1 row_mask:0xf bank_mask:0xf\n\tv_add_f32_dpp v42, v42, v42 row_ror:1 row_mask:0xf bank_mask:0xf\n\tv_mov_b32 v67, v66\n\t" \
    "v_mov_b32 v43, v42\n\ts_nop 0\n\tv_permlane16_swap_b32 v66, v67\n\tv_permlane16_swap_b32 v42, v43\n\tv_add_f32 v66, v66, v67\n\tv_add_f32 v42, v42, v43\n\tv_mul_f32 v66, 0x3ae38e39, v66\n\tv_mul_f32 v42, 0x3ae38e39, v42\n\t" \
    "v_fma_f32 v42, -v66, v66, v42\n\tv_max_f32 v42, 0, v42\n\tv_cvt_pk_bf16_f32 v42, v42, v42\n\tv_lshlrev_b32 v42, 16, v42\n\tv_add_f32 v42, 0x3727c5ac, v42\n\tv_cvt_pk_bf16_f32 v42, v42, v42\n\tv_lshlrev_b32 v42, 16, v42\n\tv_sqrt_f32 v42, v42\n\t" \
    "v_cvt_pk_bf16_f32 v66, v66, v66\n\tv_lshlrev_b32 v66, 16, v66\n\tv_cvt_pk_bf16_f32 v42, v42, v42\n\tv_lshlrev_b32 v42, 16, v42\n\tv_rcp_f32 v42, v42\n\ts_nop 0\n\tv_mul_f32 v43, v66, v42\n\tv_xor_b32 v43, 0x80000000, v43\n\t" \
    "v_pk_fma_f32 v[24:25], v[24:25], v[42:43], v[42:43] op_sel:[0,0,1] op_sel_hi:[1,0,1]\n\tv_pk_fma_f32 v[26:27], v[26:27], v[42:43], v[42:43] op_sel:[0,0,1] op_sel_hi:[1,0,1]\n\tv_pk_fma_f32 v[28:29], v[28:29], v[42:43], v[42:43] op_sel:[0,0,1] op_sel_hi:[1,0,1]\n\tv_pk_fma_f32 v[30:31], v[30:31], v[42:43], v[42:43] op_sel:[0,0,1] op_sel_hi:[1,0,1]\n\tv_pk_fma_f32 v[32:33], v[32:33], v[42:43], v[42:43] op_sel:[0,0,1] op_sel_hi:[1,0,1]\n\tv_pk_fma_f32 v[34:35], v[34:35], v[42:43], v[42:43] op_sel:[0,0,1] op_sel_hi:[1,0,1]\n\tv_pk_fma_f32 v[36:37], v[36:37], v[42:43], v[42:43] op_sel:[0,0,1] op_sel_hi:[1,0,1]\n\tv_pk_fma_f32 v[38:39], v[38:39], v[42:43], v[42:43] op_sel:[0,0,1] op_sel_hi:[1,0,1]\n\t" \
    "v_pk_fma_f32 v[40:41], v[40:41], v[42:43], v[42:43] op_sel:[0,0,1] op_sel_hi:[1,0,1]\n\tv_cvt_pk_bf16_f32 v48, v24, v25\n\tv_cvt_pk_bf16_f32 v49, v26, v27\n\tv_cvt_pk_bf16_f32 v50, v28, v29\n\tv_cvt_pk_bf16_f32 v51, v30, v31\n\tv_cvt_pk_bf16_f32 v52, v32, v33\n\tv_cvt_pk_bf16_f32 v53, v34, v35\n\tv_cvt_pk_bf16_f32 v54, v36, v37\n\t" \
    "v_cvt_pk_bf16_f32 v55, v38, v39\n\tv_cvt_pk_bf16_f32 v56, v40, v41\n\tds_write_b128 v90, v[48:51]\n\tds_write_b128 v90, v[52:55] offset:512\n\tds_write_b32 v91, v56 offset:1024\n\ts_waitcnt vmcnt(11) lgkmcnt(0)\n\ts_barrier\n\tds_read_b64_tr_b16 v[24:25], v94 offset:0\n\t" \
    "ds_read_b64_tr_b16 v[26:27], v94 offset:4672\n\tds_read_b64_tr_b16 v[28:29], v94 offset:512\n\tds_read_b64_tr_b16 v[30:31], v94 offset:5184\n\tds_read_b64_tr_b16 v[32:33], v95 offset:1024\n\tds_read_b64_tr_b16 v[34:35], v95 offset:5696\n\tbuffer_store_dwordx2 v[68:69], v89, s[8:11], s64 offen sc1\n\tv_readfirstlane_b32 s36, v58\n\tv_readfirstlane_b32 s37, v59\n\t" \
    "s_lshl_b32 s73, s38, 12\n\ts_add_u32 s68, s61, s73\n\ts_add_u32 s36, s36, 0x7f4a7c15\n\ts_addc_u32 s37, s37, 0x9e3779b9\n\ts_waitcnt lgkmcnt(4)\n\tbuffer_store_dwordx4 v[24:27], v88, s[8:11], s68 offen sc1\n\ts_add_u32 s68, s68, 16384\n\ts_waitcnt lgkmcnt(2)\n\t" \
    "buffer_store_dwordx4 v[28:31], v88, s[8:11], s68 offen sc1\n\ts_add_u32 s68, s61, 32768\n\ts_cmp_eq_u32 s38, 0\n\ts_cselect_b32 s68, s68, 0x7ff00000\n\ts_waitcnt lgkmcnt(0)\n\tbuffer_store_dwordx4 v[32:35], v88, s[8:11], s68 offen sc1\n\tv_mov_b64 v[248:249], s[36:37]\n\tv_and_b32 v89, 7, v240\n\t" \
    "v_lshlrev_b32 v89, 11, v89\n\ts_cmp_ge_u32 s43, 20\n\ts_cselect_b32 s69, s70, s43\n\ts_mul_i32 s69, s69, 294912\n\ts_add_u32 s69, s69, 294912\n\ts_add_u32 m0, s54, 8192\n\ts_nop 0\n\tbuffer_load_dwordx4 v10, s[12:15], s69 offen lds\n\t" \
    "s_xor_b32 s52, s72, 4\n\ts_mul_i32 s72, s72, 6656\n\ts_add_u32 s72, s72, 22347776\n\ts_mul_i32 s52, s52, 6656\n\ts_add_u32 s52, s52, 22347776\n\tv_mov_b32 v246, 0xbdd2d3e8\n\tv_mov_b32 v247, 0xc0135761\n\ts_lshr_b32 s65, s40, 3\n\t" \
    "s_mul_i32 s66, s65, 43\n\ts_lshr_b32 s66, s66, 7\n\ts_mul_i32 s6, s66, 3\n\ts_sub_u32 s6, s65, s6\n\ts_and_b32 s66, s66, 7\n\ts_and_b32 s7, s4, 1\n\ts_lshl_b32 s7, s7, 3\n\ts_add_u32 s7, s7, s66\n\t" \
    "s_mul_i32 s65, s5, 4718592\n\ts_lshl_b32 s67, s7, 8\n\ts_add_u32 s65, s65, s67\n\ts_add_u32 s16, s16, s65\n\ts_addc_u32 s17, s17, 0\n\ts_sub_u32 s18, s18, s65\n\ts_cmp_eq_u32 s39, 2\n\ts_cselect_b32 s18, s18, 0\n\t" \
    "v_lshrrev_b32 v16, 3, v240\n\ts_lshl_b32 s65, s38, 3\n\tv_add_u32 v16, s65, v16\n\tv_and_b32 v17, 0x70, v8\n\tv_lshlrev_b32 v18, 5, v245\n\tv_xor_b32 v17, v17, v18\n\tv_lshl_add_u32 v16, v16, 12, v17\n\ts_lshr_b32 s65, s5, 1\n\t" \
    "s_cmp_ge_u32 s43, 20\n\ts_cselect_b32 s69, s70, s43\n\ts_mul_i32 s69, s69, 294912\n\ts_add_u32 s69, s69, 294912\n\ts_add_u32 m0, s54, 12288\n\ts_nop 0\n\tbuffer_load_dwordx4 v11, s[12:15], s69 offen lds\n\ts_lshl_b32 s65, s65, 7\n\t" \
    "v_add_u32 v13, s65, v16\n\tv_xor_b32 v12, 128, v13\n\ts_lshl_b32 s65, s7, 7\n\ts_lshl_b32 s66, s5, 5\n\ts_add_u32 s65, s65, s66\n\ts_lshl_b32 s59, s65, 3\n\ts_add_u32 s59, s59, 58368\n\ts_lshl_b32 s66, s65, 1\n\t" \
    "s_add_u32 s32, s32, s66\n\ts_addc_u32 s33, s33, 0\n\ts_sub_u32 s34, s34, s66\n\ts_lshl_b32 s66, s6, 2\n\ts_add_u32 s66, s66, s38\n\ts_mul_i32 s67, s66, 96\n\ts_mul_i32 s65, s65, 1152\n\ts_add_u32 s65, s65, s67\n\t" \
    "s_add_u32 s24, s24, s65\n\ts_addc_u32 s25, s25, 0\n\ts_sub_u32 s26, s26, s65\n\ts_add_u32 s20, s20, s65\n\ts_addc_u32 s21, s21, 0\n\ts_sub_u32 s22, s22, s65\n\ts_lshl_b32 s51, s67, 2\n\ts_add_u32 s51, s51, 50176\n\t" \
    "s_lshl_b32 s66, s66, 2\n\ts_add_u32 s66, s66, s5\n\ts_mul_i32 s46, s66, 110592\n\ts_add_u32 s46, s46, 16646144\n\ts_lshl_b32 s50, s66, 13\n\ts_add_u32 s50, s50, 21954560\n\ts_mul_i32 s65, s5, 9\n\ts_sub_u32 s65, s42, s65\n\t" \
    "s_cmp_ge_u32 s43, 19\n\ts_cselect_b32 s69, s70, s43\n\ts_mul_i32 s69, s69, 294912\n\ts_add_u32 s69, s69, 589824\n\ts_add_u32 m0, s54, 16384\n\ts_nop 0\n\tbuffer_load_dwordx4 v10, s[12:15], s69 offen lds\n\ts_lshl_b32 s65, s65, 1\n\t" \
    "s_add_u32 s65, s65, s57\n\ts_mov_b32 s4, s65\n\ts_lshl_b32 s49, s65, 7\n\ts_add_u32 s49, s49, s50\n\ts_mul_i32 s45, s65, 3072\n\ts_add_u32 s45, s45, s46\n\ts_cmp_eq_u32 s39, 2\n\ts_cselect_b32 s46, s46, 2146435072\n\t" \
    "v_and_b32 v16, 31, v240\n\tv_mul_u32_u24 v243, 1152, v16\n\tv_lshrrev_b32 v16, 5, v240\n\tv_lshl_add_u32 v243, v16, 4, v243\n\tv_lshrrev_b32 v242, 1, v244\n\tv_lshlrev_b32 v236, 2, v240\n\tv_min_u32 v241, 17, v240\n\tv_lshlrev_b32 v241, 7, v241\n\t" \
    "s_lshl_b32 s65, s6, 4\n\ts_add_u32 s65, s65, s7\n\ts_lshl_b32 s65, s65, 2\n\ts_add_u32 s65, s65, s5\n\ts_lshl_b32 s65, s65, 2\n\ts_add_u32 s53, s65, s38\n\ts_mul_i32 s55, s53, 9984\n\ts_add_u32 s55, s55, 7208960\n\t" \
    "s_cmp_eq_u32 s39, 2\n\ts_cselect_b32 s55, s55, 2146435072\n\ts_mul_i32 s56, s53, 3072\n\ts_add_u32 s56, s56, 4849664\n\ts_or_b32 s65, s40, s38\n\ts_cselect_b32 s60, 2146435072, 0\n\ts_cmp_ge_u32 s43, 19\n\ts_cselect_b32 s69, s70, s43\n\t" \
    "s_mul_i32 s69, s69, 294912\n\ts_add_u32 s69, s69, 589824\n\ts_add_u32 m0, s54, 20480\n\ts_nop 0\n\tbuffer_load_dwordx4 v11, s[12:15], s69 offen lds\n\ts_cmp_ge_u32 s43, 18\n\ts_cselect_b32 s69, s70, s43\n\ts_mul_i32 s69, s69, 294912\n\t" \
    "s_add_u32 s69, s69, 884736\n\ts_add_u32 m0, s54, 24576\n\ts_nop 0\n\tbuffer_load_dwordx4 v10, s[12:15], s69 offen lds\n\ts_cmp_ge_u32 s43, 18\n\ts_cselect_b32 s69, s70, s43\n\ts_mul_i32 s69, s69, 294912\n\ts_add_u32 s69, s69, 884736\n\t" \
    "s_add_u32 m0, s54, 28672\n\ts_nop 0\n\tbuffer_load_dwordx4 v11, s[12:15], s69 offen lds\n\ts_cmp_ge_u32 s43, 17\n\ts_cselect_b32 s69, s70, s43\n\ts_mul_i32 s69, s69, 294912\n\ts_add_u32 s69, s69, 1179648\n\ts_add_u32 m0, s54, 32768\n\t" \
    "s_nop 0\n\tbuffer_load_dwordx4 v10, s[12:15], s69 offen lds\n\ts_cmp_ge_u32 s43, 17\n\ts_cselect_b32 s69, s70, s43\n\ts_mul_i32 s69, s69, 294912\n\ts_add_u32 s69, s69, 1179648\n\ts_add_u32 m0, s54, 36864\n\ts_nop 0\n\t" \
    "buffer_load_dwordx4 v11, s[12:15], s69 offen lds\n\ts_waitcnt vmcnt(14)\n\ts_barrier\n\tds_read_b64_tr_b16 v[36:37], v93 offset:0\n\tds_read_b64_tr_b16 v[38:39], v93 offset:4608\n\tds_read_b64_tr_b16 v[40:41], v93 offset:9216\n\tds_read_b64_tr_b16 v[42:43], v93 offset:13312\n\tds_read_b64_tr_b16 v[44:45], v93 offset:17920\n\t" \
    "ds_read_b64_tr_b16 v[46:47], v93 offset:512\n\tds_read_b64_tr_b16 v[48:49], v93 offset:5120\n\tds_read_b64_tr_b16 v[50:51], v93 offset:9728\n\tds_read_b64_tr_b16 v[52:53], v93 offset:13824\n\tds_read_b64_tr_b16 v[54:55], v93 offset:18432\n\tds_read_b64_tr_b16 v[120:121], v92 offset:1024\n\tds_read_b64_tr_b16 v[122:123], v92 offset:5632\n\tds_read_b64_tr_b16 v[124:125], v92 offset:10240\n\t" \
    "ds_read_b64_tr_b16 v[126:127], v92 offset:14336\n\tds_read_b64_tr_b16 v[128:129], v92 offset:18944\n\tv_lshlrev_b32 v78, 16, v60\n\tv_and_b32 v79, 0xffff0000, v60\n\tv_lshlrev_b32 v80, 16, v61\n\tv_and_b32 v81, 0xffff0000, v61\n\ts_waitcnt lgkmcnt(10)\n\tv_lshlrev_b32 v66, 16, v36\n\t" \
    "v_and_b32 v67, 0xffff0000, v36\n\tv_lshlrev_b32 v68, 16, v37\n\tv_and_b32 v69, 0xffff0000, v37\n\tv_lshlrev_b32 v70, 16, v38\n\tv_and_b32 v71, 0xffff0000, v38\n\tv_lshlrev_b32 v72, 16, v39\n\tv_and_b32 v73, 0xffff0000, v39\n\tv_lshlrev_b32 v74, 16, v40\n\t" \
    "v_and_b32 v75, 0xffff0000, v40\n\tv_lshlrev_b32 v76, 16, v41\n\tv_mul_f32 v16, v66, v81\n\tv_mul_f32 v17, v67, v81\n\tv_mul_f32 v18, v68, v81\n\tv_mul_f32 v19, v69, v81\n\tv_mul_f32 v20, v70, v81\n\tv_mul_f32 v21, v71, v81\n\t" \
    "v_mul_f32 v22, v72, v81\n\tv_mul_f32 v23, v73, v81\n\tv_fmac_f32 v16, v67, v80\n\tv_fmac_f32 v17, v68, v80\n\tv_fmac_f32 v18, v69, v80\n\tv_fmac_f32 v19, v70, v80\n\tv_fmac_f32 v20, v71, v80\n\tv_fmac_f32 v21, v72, v80\n\t" \
    "v_fmac_f32 v22, v73, v80\n\tv_fmac_f32 v23, v74, v80\n\tv_fmac_f32 v16, v68, v79\n\tv_fmac_f32 v17, v69, v79\n\tv_fmac_f32 v18, v70, v79\n\tv_fmac_f32 v19, v71, v79\n\tv_fmac_f32 v20, v72, v79\n\tv_fmac_f32 v21, v73, v79\n\t" \
    "v_fmac_f32 v22, v74, v79\n\tv_fmac_f32 v23, v75, v79\n\tv_fmac_f32 v16, v69, v78\n\tv_fmac_f32 v17, v70, v78\n\tv_fmac_f32 v18, v71, v78\n\tv_fmac_f32 v19, v72, v78\n\tv_fmac_f32 v20, v73, v78\n\tv_fmac_f32 v21, v74, v78\n\t" \
    "v_fmac_f32 v22, v75, v78\n\tv_fmac_f32 v23, v76, v78\n\tv_cvt_pk_bf16_f32 v66, v16, v17\n\tv_cvt_pk_bf16_f32 v67, v18, v19\n\tv_cvt_pk_bf16_f32 v68, v20, v21\n\tv_cvt_pk_bf16_f32 v69, v22, v23\n\tv_lshlrev_b32 v16, 16, v66\n\tv_and_b32 v17, 0xffff0000, v66\n\t" \
    "v_lshlrev_b32 v18, 16, v67\n\tv_and_b32 v19, 0xffff0000, v67\n\tv_lshlrev_b32 v20, 16, v68\n\tv_and_b32 v21, 0xffff0000, v68\n\tv_lshlrev_b32 v22, 16, v69\n\tv_and_b32 v23, 0xffff0000, v69\n\tv_lshlrev_b32 v66, 16, v42\n\tv_and_b32 v67, 0xffff0000, v42\n\t" \
    "v_lshlrev_b32 v68, 16, v43\n\tv_and_b32 v69, 0xffff0000, v43\n\tv_lshlrev_b32 v70, 16, v44\n\tv_and_b32 v71, 0xffff0000, v44\n\tv_lshlrev_b32 v72, 16, v45\n\tv_and_b32 v73, 0xffff0000, v45\n\tv_pk_mul_f32 v[16:17], v[16:17], v[66:67]\n\tv_pk_mul_f32 v[18:19], v[18:19], v[68:69]\n\t" \
    "v_pk_mul_f32 v[20:21], v[20:21], v[70:71]\n\tv_pk_mul_f32 v[22:23], v[22:23], v[72:73]\n\tv_cvt_pk_bf16_f32 v24, v16, v17\n\tv_cvt_pk_bf16_f32 v25, v18, v19\n\tv_cvt_pk_bf16_f32 v26, v20, v21\n\tv_cvt_pk_bf16_f32 v27, v22, v23\n\ts_add_u32 s68, s62, s54\n\tbuffer_store_dwordx4 v[24:27], v8, s[8:11], s68 offen sc1\n\t" \
    "s_waitcnt vmcnt(9) lgkmcnt(0)\n\ts_barrier\n\tbuffer_store_dwordx2 v[248:249], v89, s[8:11], s63 offen sc1\n\ts_cmp_lg_u32 s39, 0\n\ts_cbranch_scc1 104f\n\ts_sub_u32 s73, s40, 216\n\ts_lshl_b32 s71, s73, 4\n\ts_mul_i32 s68, s73, 12\n\t" \
    "s_add_u32 s68, s68, 96\n\ts_cmp_lt_u32 s73, 24\n\ts_cselect_b32 s71, s71, s68\n\ts_cselect_b32 s73, 0, 2146435072\n\tv_cmp_eq_u32 vcc, 0, v240\n\tv_subrev_u32 v190, 8, v8\n\tv_mov_b32 v191, 2146435072\n\tv_cndmask_b32 v190, v190, v191, vcc\n\t" \
    "s_add_u32 s68, s71, s38\n\ts_add_u32 s68, s68, 0\n\ts_lshl_b32 s73, s68, 1\n\ts_lshl_b32 s68, s68, 12\n\tbuffer_load_dwordx4 v[164:167], v8, s[76:79], s68 offen offset:0\n\tbuffer_load_dwordx4 v[168:171], v8, s[76:79], s68 offen offset:1024\n\tbuffer_load_dwordx4 v[172:175], v8, s[76:79], s68 offen offset:2048\n\tbuffer_load_dwordx4 v[176:179], v8, s[76:79], s68 offen offset:3072\n\t" \
    "buffer_load_dwordx2 v[180:181], v190, s[76:79], s68 offen\n\tbuffer_load_dwordx2 v[182:183], v8, s[76:79], s68 offen offset:1016\n\tbuffer_load_dwordx2 v[184:185], v8, s[76:79], s68 offen offset:2040\n\tbuffer_load_dwordx2 v[186:187], v8, s[76:79], s68 offen offset:3064\n\tbuffer_load_ushort v188, off, s[80:83], s73\n\t104:\n\tv_lshlrev_b32 v78, 16, v62\n\ts_cmp_ge_u32 s43, 16\n\t" \
    "s_cselect_b32 s69, s70, s43\n\ts_mul_i32 s69, s69, 294912\n\ts_add_u32 s69, s69, 1474560\n\ts_add_u32 m0, s54, 40960\n\tv_and_b32 v79, 0xffff0000, v62\n\tv_lshlrev_b32 v80, 16, v63\n\tv_and_b32 v81, 0xffff0000, v63\n\tv_lshlrev_b32 v66, 16, v46\n\t" \
    "v_and_b32 v67, 0xffff0000, v46\n\tv_lshlrev_b32 v68, 16, v47\n\tv_and_b32 v69, 0xffff0000, v47\n\tv_lshlrev_b32 v70, 16, v48\n\tv_and_b32 v71, 0xffff0000, v48\n\tv_lshlrev_b32 v72, 16, v49\n\tv_and_b32 v73, 0xffff0000, v49\n\tv_lshlrev_b32 v74, 16, v50\n\t" \
    "v_and_b32 v75, 0xffff0000, v50\n\tv_lshlrev_b32 v76, 16, v51\n\tv_mul_f32 v16, v66, v81\n\tv_mul_f32 v17, v67, v81\n\tv_mul_f32 v18, v68, v81\n\tv_mul_f32 v19, v69, v81\n\tv_mul_f32 v20, v70, v81\n\tv_mul_f32 v21, v71, v81\n\t" \
    "v_mul_f32 v22, v72, v81\n\tv_mul_f32 v23, v73, v81\n\tv_fmac_f32 v16, v67, v80\n\tv_fmac_f32 v17, v68, v80\n\tv_fmac_f32 v18, v69, v80\n\tv_fmac_f32 v19, v70, v80\n\tv_fmac_f32 v20, v71, v80\n\tv_fmac_f32 v21, v72, v80\n\t" \
    "v_fmac_f32 v22, v73, v80\n\tv_fmac_f32 v23, v74, v80\n\tv_fmac_f32 v16, v68, v79\n\tv_fmac_f32 v17, v69, v79\n\tv_fmac_f32 v18, v70, v79\n\tv_fmac_f32 v19, v71, v79\n\tv_fmac_f32 v20, v72, v79\n\tv_fmac_f32 v21, v73, v79\n\t" \
    "v_fmac_f32 v22, v74, v79\n\tv_fmac_f32 v23, v75, v79\n\tv_fmac_f32 v16, v69, v78\n\tv_fmac_f32 v17, v70, v78\n\tv_fmac_f32 v18, v71, v78\n\tv_fmac_f32 v19, v72, v78\n\tv_fmac_f32 v20, v73, v78\n\tv_fmac_f32 v21, v74, v78\n\t" \
    "v_fmac_f32 v22, v75, v78\n\tv_fmac_f32 v23, v76, v78\n\tv_cvt_pk_bf16_f32 v66, v16, v17\n\tv_cvt_pk_bf16_f32 v67, v18, v19\n\tv_cvt_pk_bf16_f32 v68, v20, v21\n\tv_cvt_pk_bf16_f32 v69, v22, v23\n\tv_lshlrev_b32 v16, 16, v66\n\tv_and_b32 v17, 0xffff0000, v66\n\t" \
    "v_lshlrev_b32 v18, 16, v67\n\tv_and_b32 v19, 0xffff0000, v67\n\tv_lshlrev_b32 v20, 16, v68\n\tv_and_b32 v21, 0xffff0000, v68\n\tv_lshlrev_b32 v22, 16, v69\n\tv_and_b32 v23, 0xffff0000, v69\n\tv_lshlrev_b32 v66, 16, v52\n\tv_and_b32 v67, 0xffff0000, v52\n\t" \
    "v_lshlrev_b32 v68, 16, v53\n\tv_and_b32 v69, 0xffff0000, v53\n\tv_lshlrev_b32 v70, 16, v54\n\tv_and_b32 v71, 0xffff0000, v54\n\tv_lshlrev_b32 v72, 16, v55\n\tv_and_b32 v73, 0xffff0000, v55\n\tv_pk_mul_f32 v[16:17], v[16:17], v[66:67]\n\tv_pk_mul_f32 v[18:19], v[18:19], v[68:69]\n\t" \
    "v_pk_mul_f32 v[20:21], v[20:21], v[70:71]\n\tv_pk_mul_f32 v[22:23], v[22:23], v[72:73]\n\tv_cvt_pk_bf16_f32 v28, v16, v17\n\tv_cvt_pk_bf16_f32 v29, v18, v19\n\tv_cvt_pk_bf16_f32 v30, v20, v21\n\tv_cvt_pk_bf16_f32 v31, v22, v23\n\ts_add_u32 s68, s62, s54\n\tbuffer_store_dwordx4 v[28:31], v9, s[8:11], s68 offen sc1\n\t" \
    "v_lshlrev_b32 v78, 16, v64\n\tv_and_b32 v79, 0xffff0000, v64\n\tv_lshlrev_b32 v80, 16, v65\n\tv_and_b32 v81, 0xffff0000, v65\n\tv_lshlrev_b32 v66, 16, v120\n\tv_and_b32 v67, 0xffff0000, v120\n\tv_lshlrev_b32 v68, 16, v121\n\tv_and_b32 v69, 0xffff0000, v121\n\t" \
    "v_lshlrev_b32 v70, 16, v122\n\tv_and_b32 v71, 0xffff0000, v122\n\tv_lshlrev_b32 v72, 16, v123\n\tv_and_b32 v73, 0xffff0000, v123\n\tv_lshlrev_b32 v74, 16, v124\n\tv_and_b32 v75, 0xffff0000, v124\n\tv_lshlrev_b32 v76, 16, v125\n\tv_mul_f32 v16, v66, v81\n\t" \
    "v_mul_f32 v17, v67, v81\n\tv_mul_f32 v18, v68, v81\n\tv_mul_f32 v19, v69, v81\n\tv_mul_f32 v20, v70, v81\n\tv_mul_f32 v21, v71, v81\n\tv_mul_f32 v22, v72, v81\n\tv_mul_f32 v23, v73, v81\n\tv_fmac_f32 v16, v67, v80\n\t" \
    "v_fmac_f32 v17, v68, v80\n\tv_fmac_f32 v18, v69, v80\n\tv_fmac_f32 v19, v70, v80\n\tv_fmac_f32 v20, v71, v80\n\tv_fmac_f32 v21, v72, v80\n\tv_fmac_f32 v22, v73, v80\n\tv_fmac_f32 v23, v74, v80\n\tv_fmac_f32 v16, v68, v79\n\t" \
    "v_fmac_f32 v17, v69, v79\n\tv_fmac_f32 v18, v70, v79\n\tv_fmac_f32 v19, v71, v79\n\tv_fmac_f32 v20, v72, v79\n\tv_fmac_f32 v21, v73, v79\n\tv_fmac_f32 v22, v74, v79\n\tv_fmac_f32 v23, v75, v79\n\tv_fmac_f32 v16, v69, v78\n\t" \
    "v_fmac_f32 v17, v70, v78\n\tv_fmac_f32 v18, v71, v78\n\tv_fmac_f32 v19, v72, v78\n\tv_fmac_f32 v20, v73, v78\n\tv_fmac_f32 v21, v74, v78\n\tv_fmac_f32 v22, v75, v78\n\tv_fmac_f32 v23, v76, v78\n\tv_cvt_pk_bf16_f32 v66, v16, v17\n\t" \
    "v_cvt_pk_bf16_f32 v67, v18, v19\n\tv_cvt_pk_bf16_f32 v68, v20, v21\n\tv_cvt_pk_bf16_f32 v69, v22, v23\n\tv_lshlrev_b32 v16, 16, v66\n\tv_and_b32 v17, 0xffff0000, v66\n\tv_lshlrev_b32 v18, 16, v67\n\tv_and_b32 v19, 0xffff0000, v67\n\tv_lshlrev_b32 v20, 16, v68\n\t" \
    "v_and_b32 v21, 0xffff0000, v68\n\tv_lshlrev_b32 v22, 16, v69\n\tv_and_b32 v23, 0xffff0000, v69\n\tv_lshlrev_b32 v66, 16, v126\n\tv_and_b32 v67, 0xffff0000, v126\n\tv_lshlrev_b32 v68, 16, v127\n\tv_and_b32 v69, 0xffff0000, v127\n\tv_lshlrev_b32 v70, 16, v128\n\t" \
    "v_and_b32 v71, 0xffff0000, v128\n\tv_lshlrev_b32 v72, 16, v129\n\tv_and_b32 v73, 0xffff0000, v129\n\tv_pk_mul_f32 v[16:17], v[16:17], v[66:67]\n\tv_pk_mul_f32 v[18:19], v[18:19], v[68:69]\n\tv_pk_mul_f32 v[20:21], v[20:21], v[70:71]\n\tv_pk_mul_f32 v[22:23], v[22:23], v[72:73]\n\tv_cvt_pk_bf16_f32 v32, v16, v17\n\t" \
    "v_cvt_pk_bf16_f32 v33, v18, v19\n\tv_cvt_pk_bf16_f32 v34, v20, v21\n\tv_cvt_pk_bf16_f32 v35, v22, v23\n\ts_add_u32 s68, s62, 8192\n\ts_cmp_eq_u32 s38, 1\n\ts_cselect_b32 s68, s68, 0x7ff00000\n\tbuffer_store_dwordx4 v[32:35], v8, s[8:11], s68 offen sc1\n\ts_cmp_eq_u32 s39, 0\n\t" \
    "s_cbranch_scc1 101f\n\tbuffer_load_dwordx4 v10, s[12:15], s69 offen lds\n\ts_cmp_ge_u32 s43, 16\n\ts_cselect_b32 s69, s70, s43\n\ts_mul_i32 s69, s69, 294912\n\ts_add_u32 s69, s69, 1474560\n\ts_add_u32 m0, s54, 45056\n\ts_nop 0\n\t" \
    "buffer_load_dwordx4 v11, s[12:15], s69 offen lds\n\ts_cmp_ge_u32 s43, 15\n\ts_cselect_b32 s69, s70, s43\n\ts_mul_i32 s69, s69, 294912\n\ts_add_u32 s69, s69, 1769472\n\ts_add_u32 m0, s54, 49152\n\ts_nop 0\n\tbuffer_load_dwordx4 v10, s[12:15], s69 offen lds\n\t" \
    "s_waitcnt vmcnt(3)\n\tbuffer_store_dwordx2 v[248:249], off, s[8:11], s48 sc1\n\ts_cmp_ge_u32 s43, 15\n\ts_cselect_b32 s69, s70, s43\n\ts_mul_i32 s69, s69, 294912\n\ts_add_u32 s69, s69, 1769472\n\ts_add_u32 m0, s54, 53248\n\ts_nop 0\n\t" \
    "buffer_load_dwordx4 v11, s[12:15], s69 offen lds\n\ts_cmp_ge_u32 s43, 14\n\ts_cselect_b32 s69, s70, s43\n\ts_mul_i32 s69, s69, 294912\n\ts_add_u32 s69, s69, 2064384\n\ts_add_u32 m0, s54, 57344\n\ts_nop 0\n\tbuffer_load_dwordx4 v10, s[12:15], s69 offen lds\n\t" \
    "s_cmp_ge_u32 s43, 14\n\ts_cselect_b32 s69, s70, s43\n\ts_mul_i32 s69, s69, 294912\n\ts_add_u32 s69, s69, 2064384\n\ts_add_u32 m0, s54, 61440\n\ts_nop 0\n\tbuffer_load_dwordx4 v11, s[12:15], s69 offen lds\n\ts_cmp_ge_u32 s43, 13\n\t" \
    "s_cselect_b32 s69, s70, s43\n\ts_mul_i32 s69, s69, 294912\n\ts_add_u32 s69, s69, 2359296\n\ts_add_u32 m0, s54, 65536\n\ts_nop 0\n\tbuffer_load_dwordx4 v10, s[12:15], s69 offen lds\n\ts_cmp_ge_u32 s43, 13\n\ts_cselect_b32 s69, s70, s43\n\t" \
    "s_mul_i32 s69, s69, 294912\n\ts_add_u32 s69, s69, 2359296\n\ts_add_u32 m0, s54, 69632\n\ts_nop 0\n\tbuffer_load_dwordx4 v11, s[12:15], s69 offen lds\n\ts_cmp_ge_u32 s43, 12\n\ts_cselect_b32 s69, s70, s43\n\ts_mul_i32 s69, s69, 294912\n\t" \
    "s_add_u32 s69, s69, 2654208\n\ts_add_u32 m0, s54, 73728\n\ts_nop 0\n\tbuffer_load_dwordx4 v10, s[12:15], s69 offen lds\n\ts_cmp_ge_u32 s43, 12\n\ts_cselect_b32 s69, s70, s43\n\ts_mul_i32 s69, s69, 294912\n\ts_add_u32 s69, s69, 2654208\n\t" \
    "s_add_u32 m0, s54, 77824\n\ts_nop 0\n\tbuffer_load_dwordx4 v11, s[12:15], s69 offen lds\n\ts_cmp_ge_u32 s43, 11\n\ts_cselect_b32 s69, s70, s43\n\ts_mul_i32 s69, s69, 294912\n\ts_add_u32 s69, s69, 2949120\n\ts_add_u32 m0, s54, 81920\n\t" \
    "s_nop 0\n\tbuffer_load_dwordx4 v10, s[12:15], s69 offen lds\n\ts_lshl_b32 s71, s57, 7\n\ts_lshl_b32 s69, s42, 8\n\ts_add_u32 s71, s71, s69\n\tv_lshlrev_b32 v96, 3, v245\n\tbuffer_load_dwordx2 v[112:113], v96, s[28:31], s71 offen offset:0\n\tbuffer_load_dwordx2 v[114:115], v96, s[28:31], s71 offen offset:32\n\t" \
    "buffer_load_dwordx2 v[116:117], v96, s[28:31], s71 offen offset:64\n\tbuffer_load_dwordx2 v[118:119], v96, s[28:31], s71 offen offset:96\n\tv_lshrrev_b32 v229, 3, v244\n\tbuffer_load_ushort v228, v229, s[32:35], 0 offen offset:0\n\tbuffer_load_ushort v232, v229, s[32:35], 0 offen offset:32\n\ts_cmp_ge_u32 s43, 11\n\ts_cselect_b32 s69, s70, s43\n\ts_mul_i32 s69, s69, 294912\n\t" \
    "s_add_u32 s69, s69, 2949120\n\ts_add_u32 m0, s54, 86016\n\ts_nop 0\n\tbuffer_load_dwordx4 v11, s[12:15], s69 offen lds\n\tbuffer_load_dwordx4 v[250:253], v8, s[8:11], s47 offen sc1\n\ts_cmp_ge_u32 s43, 10\n\ts_cselect_b32 s69, s70, s43\n\ts_mul_i32 s69, s69, 294912\n\t" \
    "s_add_u32 s69, s69, 3244032\n\ts_add_u32 m0, s54, 90112\n\ts_nop 0\n\tbuffer_load_dwordx4 v10, s[12:15], s69 offen lds\n\ts_cmp_ge_u32 s43, 10\n\ts_cselect_b32 s69, s70, s43\n\ts_mul_i32 s69, s69, 294912\n\ts_add_u32 s69, s69, 3244032\n\t" \
    "s_add_u32 m0, s54, 94208\n\ts_nop 0\n\tbuffer_load_dwordx4 v11, s[12:15], s69 offen lds\n\ts_cmp_ge_u32 s43, 9\n\ts_cselect_b32 s69, s70, s43\n\ts_mul_i32 s69, s69, 294912\n\ts_add_u32 s69, s69, 3538944\n\ts_add_u32 m0, s54, 98304\n\t" \
    "s_nop 0\n\tbuffer_load_dwordx4 v10, s[12:15], s69 offen lds\n\ts_cmp_ge_u32 s43, 8\n\ts_cselect_b32 s69, s70, s43\n\ts_mul_i32 s69, s69, 294912\n\ts_add_u32 s69, s69, 3833856\n\ts_add_u32 m0, s54, 102400\n\ts_nop 0\n\t" \
    "buffer_load_dwordx4 v10, s[12:15], s69 offen lds\n\ts_cmp_ge_u32 s43, 7\n\ts_cselect_b32 s69, s70, s43\n\ts_mul_i32 s69, s69, 294912\n\ts_add_u32 s69, s69, 4128768\n\ts_add_u32 m0, s54, 106496\n\ts_nop 0\n\tbuffer_load_dwordx4 v10, s[12:15], s69 offen lds\n\t" \
    "s_cmp_ge_u32 s43, 6\n\ts_cselect_b32 s69, s70, s43\n\ts_mul_i32 s69, s69, 294912\n\ts_add_u32 s69, s69, 4423680\n\ts_add_u32 m0, s54, 110592\n\ts_nop 0\n\tbuffer_load_dwordx4 v10, s[12:15], s69 offen lds\n\ts_cmp_ge_u32 s43, 5\n\t" \
    "s_cselect_b32 s69, s70, s43\n\ts_mul_i32 s69, s69, 294912\n\ts_add_u32 s69, s69, 4718592\n\ts_add_u32 m0, s54, 114688\n\ts_nop 0\n\tbuffer_load_dwordx4 v10, s[12:15], s69 offen lds\n\ts_cmp_ge_u32 s43, 4\n\ts_cselect_b32 s69, s70, s43\n\t" \
    "s_mul_i32 s69, s69, 294912\n\ts_add_u32 s69, s69, 5013504\n\ts_add_u32 m0, s54, 118784\n\ts_nop 0\n\tbuffer_load_dwordx4 v10, s[12:15], s69 offen lds\n\ts_cmp_ge_u32 s43, 3\n\ts_cselect_b32 s69, s70, s43\n\ts_mul_i32 s69, s69, 294912\n\t" \
    "s_add_u32 s69, s69, 5308416\n\ts_add_u32 m0, s54, 122880\n\ts_nop 0\n\tbuffer_load_dwordx4 v10, s[12:15], s69 offen lds\n\ts_cmp_ge_u32 s43, 2\n\ts_cselect_b32 s69, s70, s43\n\ts_mul_i32 s69, s69, 294912\n\ts_add_u32 s69, s69, 5603328\n\t" \
    "s_add_u32 m0, s54, 126976\n\ts_nop 0\n\tbuffer_load_dwordx4 v10, s[12:15], s69 offen lds\n\ts_waitcnt vmcnt(10)\n\tv_cmp_ne_u64 vcc, s[36:37], v[250:251]\n\tv_cmp_ne_u64 s[74:75], s[36:37], v[252:253]\n\ts_or_b64 s[74:75], s[74:75], vcc\n\ts_cbranch_scc0 106f\n\t" \
    "s_mov_b32 s71, 1048576\n\t105:\n\ts_sleep 1\n\tbuffer_load_dwordx4 v[250:253], v8, s[8:11], s47 offen sc1\n\ts_waitcnt vmcnt(0)\n\tv_cmp_ne_u64 vcc, s[36:37], v[250:251]\n\tv_cmp_ne_u64 s[74:75], s[36:37], v[252:253]\n\ts_or_b64 s[74:75], s[74:75], vcc\n\t" \
    "s_cbranch_scc0 106f\n\ts_sub_u32 s71, s71, 1\n\ts_cbranch_scc0 105b\n\t106:\n\ts_cmp_ge_u32 s43, 21\n\ts_cselect_b32 s68, s70, s43\n\ts_mul_i32 s68, s68, 36864\n\ts_add_u32 s68, s68, s44\n\t" \
    "s_add_u32 s68, s68, 0\n\tbuffer_load_dwordx4 v[192:195], v8, s[8:11], s68 offen offset:0 sc1\n\tbuffer_load_dwordx4 v[196:199], v8, s[8:11], s68 offen offset:1024 sc1\n\tbuffer_load_dwordx4 v[200:203], v8, s[8:11], s68 offen offset:2048 sc1\n\ts_cmp_ge_u32 s43, 20\n\ts_cselect_b32 s68, s70, s43\n\ts_mul_i32 s68, s68, 36864\n\ts_add_u32 s68, s68, s44\n\t" \
    "s_add_u32 s68, s68, 36864\n\tbuffer_load_dwordx4 v[168:171], v8, s[8:11], s68 offen offset:0 sc1\n\tbuffer_load_dwordx4 v[172:175], v8, s[8:11], s68 offen offset:1024 sc1\n\tbuffer_load_dwordx4 v[176:179], v8, s[8:11], s68 offen offset:2048 sc1\n\ts_cmp_ge_u32 s43, 19\n\ts_cselect_b32 s68, s70, s43\n\ts_mul_i32 s68, s68, 36864\n\ts_add_u32 s68, s68, s44\n\t" \
    "s_add_u32 s68, s68, 73728\n\tbuffer_load_dwordx4 v[180:183], v8, s[8:11], s68 offen offset:0 sc1\n\tbuffer_load_dwordx4 v[184:187], v8, s[8:11], s68 offen offset:1024 sc1\n\tbuffer_load_dwordx4 v[188:191], v8, s[8:11], s68 offen offset:2048 sc1\n\ts_cmp_ge_u32 s43, 18\n\ts_cselect_b32 s68, s70, s43\n\ts_mul_i32 s68, s68, 36864\n\ts_add_u32 s68, s68, s44\n\t" \
    "s_add_u32 s68, s68, 110592\n\tbuffer_load_dwordx4 v[204:207], v8, s[8:11], s68 offen offset:0 sc1\n\tbuffer_load_dwordx4 v[208:211], v8, s[8:11], s68 offen offset:1024 sc1\n\tbuffer_load_dwordx4 v[212:215], v8, s[8:11], s68 offen offset:2048 sc1\n\tv_lshlrev_b32 v96, 16, v112\n\tv_and_b32 v97, 0xffff0000, v112\n\tv_lshlrev_b32 v98, 16, v113\n\tv_and_b32 v99, 0xffff0000, v113\n\t" \
    "v_lshlrev_b32 v100, 16, v114\n\tv_and_b32 v101, 0xffff0000, v114\n\tv_lshlrev_b32 v102, 16, v115\n\tv_and_b32 v103, 0xffff0000, v115\n\tv_lshlrev_b32 v104, 16, v116\n\tv_and_b32 v105, 0xffff0000, v116\n\tv_lshlrev_b32 v106, 16, v117\n\tv_and_b32 v107, 0xffff0000, v117\n\t" \
    "v_lshlrev_b32 v108, 16, v118\n\tv_and_b32 v109, 0xffff0000, v118\n\tv_lshlrev_b32 v110, 16, v119\n\tv_and_b32 v111, 0xffff0000, v119\n\tv_lshlrev_b32 v228, 16, v228\n\tv_mov_b32 v229, v228\n\tv_mov_b32 v230, v228\n\tv_mov_b32 v231, v228\n\t" \
    "v_lshlrev_b32 v232, 16, v232\n\tv_mov_b32 v233, v232\n\tv_mov_b32 v234, v232\n\tv_mov_b32 v235, v232\n\ts_barrier\n\tds_read_b64_tr_b16 v[16:17], v0 offset:0\n\tds_read_b64_tr_b16 v[18:19], v0 offset:512\n\tds_read_b64_tr_b16 v[20:21], v1 offset:0\n\t" \
    "ds_read_b64_tr_b16 v[22:23], v1 offset:512\n\tds_read_b64_tr_b16 v[24:25], v2 offset:0\n\tds_read_b64_tr_b16 v[26:27], v2 offset:512\n\tds_read_b64_tr_b16 v[28:29], v3 offset:0\n\tds_read_b64_tr_b16 v[30:31], v3 offset:512\n\tds_read_b64_tr_b16 v[32:33], v0 offset:4096\n\tds_read_b64_tr_b16 v[34:35], v0 offset:4608\n\ts_waitcnt vmcnt(9) lgkmcnt(8)\n\t" \
    "v_mfma_f32_16x16x32_bf16 v[120:123], v[16:19], v[192:195], 0\n\tds_read_b64_tr_b16 v[36:37], v1 offset:4096\n\tds_read_b64_tr_b16 v[38:39], v1 offset:4608\n\ts_cmp_ge_u32 s43, 17\n\ts_cselect_b32 s68, s70, s43\n\tv_mfma_f32_16x16x32_bf16 v[128:131], v[16:19], v[196:199], 0\n\ts_mul_i32 s68, s68, 36864\n\ts_add_u32 s68, s68, s44\n\t" \
    "s_add_u32 s68, s68, 147456\n\tv_mfma_f32_16x16x32_bf16 v[136:139], v[16:19], v[200:203], 0\n\ts_waitcnt lgkmcnt(8)\n\tv_mfma_f32_16x16x32_bf16 v[124:127], v[20:23], v[192:195], 0\n\tds_read_b64_tr_b16 v[40:41], v2 offset:4096\n\tds_read_b64_tr_b16 v[42:43], v2 offset:4608\n\tv_mfma_f32_16x16x32_bf16 v[132:135], v[20:23], v[196:199], 0\n\tbuffer_load_dwordx4 v[216:219], v8, s[8:11], s68 offen offset:0 sc1\n\t" \
    "v_mfma_f32_16x16x32_bf16 v[140:143], v[20:23], v[200:203], 0\n\ts_waitcnt lgkmcnt(8)\n\tv_mfma_f32_16x16x32_bf16 v[144:147], v[24:27], v[192:195], 0\n\tds_read_b64_tr_b16 v[44:45], v3 offset:4096\n\tds_read_b64_tr_b16 v[46:47], v3 offset:4608\n\tv_mfma_f32_16x16x32_bf16 v[152:155], v[24:27], v[196:199], 0\n\tbuffer_load_dwordx4 a[0:3], v8, s[8:11], s68 offen offset:1024 sc1\n\tv_mfma_f32_16x16x32_bf16 v[160:163], v[24:27], v[200:203], 0\n\t" \
    "s_waitcnt lgkmcnt(8)\n\tv_mfma_f32_16x16x32_bf16 v[148:151], v[28:31], v[192:195], 0\n\tds_read_b64_tr_b16 v[16:17], v0 offset:8192\n\tds_read_b64_tr_b16 v[18:19], v0 offset:8704\n\tv_mfma_f32_16x16x32_bf16 v[156:159], v[28:31], v[196:199], 0\n\tbuffer_load_dwordx4 a[4:7], v8, s[8:11], s68 offen offset:2048 sc1\n\ts_cmp_ge_u32 s43, 16\n\ts_cselect_b32 s68, s70, s43\n\t" \
    "s_mul_i32 s68, s68, 36864\n\tv_mfma_f32_16x16x32_bf16 v[164:167], v[28:31], v[200:203], 0\n\ts_add_u32 s68, s68, s44\n\ts_add_u32 s68, s68, 184320\n\ts_waitcnt lgkmcnt(8)\n\tv_mfma_f32_16x16x32_bf16 v[48:51], v[32:35], v[192:195], v[96:99]\n\tds_read_b64_tr_b16 v[20:21], v1 offset:8192\n\tds_read_b64_tr_b16 v[22:23], v1 offset:8704\n\t" \
    "v_mfma_f32_16x16x32_bf16 v[56:59], v[32:35], v[196:199], v[96:99]\n\tbuffer_load_dwordx4 a[8:11], v8, s[8:11], s68 offen offset:0 sc1\n\tv_mfma_f32_16x16x32_bf16 v[64:67], v[32:35], v[200:203], v[96:99]\n\ts_waitcnt lgkmcnt(8)\n\tv_mfma_f32_16x16x32_bf16 v[52:55], v[36:39], v[192:195], v[100:103]\n\tds_read_b64_tr_b16 v[24:25], v2 offset:8192\n\tds_read_b64_tr_b16 v[26:27], v2 offset:8704\n\tv_mfma_f32_16x16x32_bf16 v[60:63], v[36:39], v[196:199], v[100:103]\n\t" \
    "buffer_load_dwordx4 a[12:15], v8, s[8:11], s68 offen offset:1024 sc1\n\tv_mfma_f32_16x16x32_bf16 v[68:71], v[36:39], v[200:203], v[100:103]\n\ts_waitcnt lgkmcnt(8)\n\tv_mfma_f32_16x16x32_bf16 v[72:75], v[40:43], v[192:195], v[104:107]\n\tds_read_b64_tr_b16 v[28:29], v3 offset:8192\n\tds_read_b64_tr_b16 v[30:31], v3 offset:8704\n\tv_mfma_f32_16x16x32_bf16 v[80:83], v[40:43], v[196:199], v[104:107]\n\tbuffer_load_dwordx4 a[16:19], v8, s[8:11], s68 offen offset:2048 sc1\n\t" \
    "s_cmp_ge_u32 s43, 15\n\ts_cselect_b32 s68, s70, s43\n\ts_mul_i32 s68, s68, 36864\n\tv_mfma_f32_16x16x32_bf16 v[88:91], v[40:43], v[200:203], v[104:107]\n\ts_add_u32 s68, s68, s44\n\ts_add_u32 s68, s68, 221184\n\ts_waitcnt lgkmcnt(8)\n\tv_mfma_f32_16x16x32_bf16 v[76:79], v[44:47], v[192:195], v[108:111]\n\t" \
    "ds_read_b64_tr_b16 v[32:33], v0 offset:12288\n\tds_read_b64_tr_b16 v[34:35], v0 offset:12800\n\tv_mfma_f32_16x16x32_bf16 v[84:87], v[44:47], v[196:199], v[108:111]\n\tbuffer_load_dwordx4 a[20:23], v8, s[8:11], s68 offen offset:0 sc1\n\tv_mfma_f32_16x16x32_bf16 v[92:95], v[44:47], v[200:203], v[108:111]\n\ts_waitcnt vmcnt(13) lgkmcnt(8)\n\tv_mfma_f32_16x16x32_bf16 v[120:123], v[16:19], v[168:171], v[120:123]\n\tds_read_b64_tr_b16 v[36:37], v1 offset:12288\n\t" \
    "ds_read_b64_tr_b16 v[38:39], v1 offset:12800\n\tv_mfma_f32_16x16x32_bf16 v[128:131], v[16:19], v[172:175], v[128:131]\n\tv_mfma_f32_16x16x32_bf16 v[136:139], v[16:19], v[176:179], v[136:139]\n\ts_waitcnt lgkmcnt(8)\n\tv_mfma_f32_16x16x32_bf16 v[124:127], v[20:23], v[168:171], v[124:127]\n\tds_read_b64_tr_b16 v[40:41], v2 offset:12288\n\tds_read_b64_tr_b16 v[42:43], v2 offset:12800\n\tv_mfma_f32_16x16x32_bf16 v[132:135], v[20:23], v[172:175], v[132:135]\n\t" \
    "buffer_load_dwordx4 a[24:27], v8, s[8:11], s68 offen offset:1024 sc1\n\tv_mfma_f32_16x16x32_bf16 v[140:143], v[20:23], v[176:179], v[140:143]\n\ts_waitcnt lgkmcnt(8)\n\tv_mfma_f32_16x16x32_bf16 v[144:147], v[24:27], v[168:171], v[144:147]\n\tds_read_b64_tr_b16 v[44:45], v3 offset:12288\n\tds_read_b64_tr_b16 v[46:47], v3 offset:12800\n\tv_mfma_f32_16x16x32_bf16 v[152:155], v[24:27], v[172:175], v[152:155]\n\tbuffer_load_dwordx4 a[28:31], v8, s[8:11], s68 offen offset:2048 sc1\n\t" \
    "s_cmp_ge_u32 s43, 14\n\ts_cselect_b32 s68, s70, s43\n\ts_mul_i32 s68, s68, 36864\n\tv_mfma_f32_16x16x32_bf16 v[160:163], v[24:27], v[176:179], v[160:163]\n\ts_add_u32 s68, s68, s44\n\ts_add_u32 s68, s68, 258048\n\ts_waitcnt lgkmcnt(8)\n\tv_mfma_f32_16x16x32_bf16 v[148:151], v[28:31], v[168:171], v[148:151]\n\t" \
    "ds_read_b64_tr_b16 v[16:17], v0 offset:16384\n\tds_read_b64_tr_b16 v[18:19], v0 offset:16896\n\tv_mfma_f32_16x16x32_bf16 v[156:159], v[28:31], v[172:175], v[156:159]\n\tv_mfma_f32_16x16x32_bf16 v[164:167], v[28:31], v[176:179], v[164:167]\n\ts_waitcnt lgkmcnt(8)\n\tv_mfma_f32_16x16x32_bf16 v[48:51], v[32:35], v[168:171], v[48:51]\n\tds_read_b64_tr_b16 v[20:21], v1 offset:16384\n\tds_read_b64_tr_b16 v[22:23], v1 offset:16896\n\t" \
    "v_mfma_f32_16x16x32_bf16 v[56:59], v[32:35], v[172:175], v[56:59]\n\tbuffer_load_dwordx4 v[192:195], v8, s[8:11], s68 offen offset:0 sc1\n\tv_mfma_f32_16x16x32_bf16 v[64:67], v[32:35], v[176:179], v[64:67]\n\ts_waitcnt lgkmcnt(8)\n\tv_mfma_f32_16x16x32_bf16 v[52:55], v[36:39], v[168:171], v[52:55]\n\tds_read_b64_tr_b16 v[24:25], v2 offset:16384\n\tds_read_b64_tr_b16 v[26:27], v2 offset:16896\n\tv_mfma_f32_16x16x32_bf16 v[60:63], v[36:39], v[172:175], v[60:63]\n\t" \
    "buffer_load_dwordx4 v[196:199], v8, s[8:11], s68 offen offset:1024 sc1\n\tv_mfma_f32_16x16x32_bf16 v[68:71], v[36:39], v[176:179], v[68:71]\n\ts_waitcnt lgkmcnt(8)\n\tv_mfma_f32_16x16x32_bf16 v[72:75], v[40:43], v[168:171], v[72:75]\n\tds_read_b64_tr_b16 v[28:29], v3 offset:16384\n\tds_read_b64_tr_b16 v[30:31], v3 offset:16896\n\tv_mfma_f32_16x16x32_bf16 v[80:83], v[40:43], v[172:175], v[80:83]\n\tbuffer_load_dwordx4 v[200:203], v8, s[8:11], s68 offen offset:2048 sc1\n\t" \
    "s_cmp_ge_u32 s43, 13\n\ts_cselect_b32 s68, s70, s43\n\ts_mul_i32 s68, s68, 36864\n\tv_mfma_f32_16x16x32_bf16 v[88:91], v[40:43], v[176:179], v[88:91]\n\ts_add_u32 s68, s68, s44\n\ts_add_u32 s68, s68, 294912\n\ts_waitcnt lgkmcnt(8)\n\tv_mfma_f32_16x16x32_bf16 v[76:79], v[44:47], v[168:171], v[76:79]\n\t" \
    "ds_read_b64_tr_b16 v[32:33], v0 offset:20480\n\tds_read_b64_tr_b16 v[34:35], v0 offset:20992\n\tv_mfma_f32_16x16x32_bf16 v[84:87], v[44:47], v[172:175], v[84:87]\n\tv_mfma_f32_16x16x32_bf16 v[92:95], v[44:47], v[176:179], v[92:95]\n\ts_barrier\n\ts_waitcnt vmcnt(15) lgkmcnt(8)\n\tv_mfma_f32_16x16x32_bf16 v[120:123], v[16:19], v[180:183], v[120:123]\n\tds_read_b64_tr_b16 v[36:37], v1 offset:20480\n\t" \
    "ds_read_b64_tr_b16 v[38:39], v1 offset:20992\n\tv_mfma_f32_16x16x32_bf16 v[128:131], v[16:19], v[184:187], v[128:131]\n\tbuffer_load_dwordx4 a[32:35], v8, s[8:11], s68 offen offset:0 sc1\n\tv_mfma_f32_16x16x32_bf16 v[136:139], v[16:19], v[188:191], v[136:139]\n\ts_waitcnt lgkmcnt(8)\n\tv_mfma_f32_16x16x32_bf16 v[124:127], v[20:23], v[180:183], v[124:127]\n\tds_read_b64_tr_b16 v[40:41], v2 offset:20480\n\tds_read_b64_tr_b16 v[42:43], v2 offset:20992\n\t" \
    "v_mfma_f32_16x16x32_bf16 v[132:135], v[20:23], v[184:187], v[132:135]\n\tbuffer_load_dwordx4 a[36:39], v8, s[8:11], s68 offen offset:1024 sc1\n\tv_mfma_f32_16x16x32_bf16 v[140:143], v[20:23], v[188:191], v[140:143]\n\ts_waitcnt lgkmcnt(8)\n\tv_mfma_f32_16x16x32_bf16 v[144:147], v[24:27], v[180:183], v[144:147]\n\tds_read_b64_tr_b16 v[44:45], v3 offset:20480\n\tds_read_b64_tr_b16 v[46:47], v3 offset:20992\n\tv_mfma_f32_16x16x32_bf16 v[152:155], v[24:27], v[184:187], v[152:155]\n\t" \
    "buffer_load_dwordx4 a[40:43], v8, s[8:11], s68 offen offset:2048 sc1\n\ts_cmp_ge_u32 s43, 12\n\ts_cselect_b32 s68, s70, s43\n\ts_mul_i32 s68, s68, 36864\n\tv_mfma_f32_16x16x32_bf16 v[160:163], v[24:27], v[188:191], v[160:163]\n\ts_add_u32 s68, s68, s44\n\ts_add_u32 s68, s68, 331776\n\ts_waitcnt lgkmcnt(8)\n\t" \
    "v_mfma_f32_16x16x32_bf16 v[148:151], v[28:31], v[180:183], v[148:151]\n\tds_read_b64_tr_b16 v[16:17], v0 offset:24576\n\tds_read_b64_tr_b16 v[18:19], v0 offset:25088\n\tv_mfma_f32_16x16x32_bf16 v[156:159], v[28:31], v[184:187], v[156:159]\n\tbuffer_load_dwordx4 v[168:171], v8, s[8:11], s68 offen offset:0 sc1\n\tv_mfma_f32_16x16x32_bf16 v[164:167], v[28:31], v[188:191], v[164:167]\n\ts_waitcnt lgkmcnt(8)\n\tv_mfma_f32_16x16x32_bf16 v[48:51], v[32:35], v[180:183], v[48:51]\n\t" \
    "ds_read_b64_tr_b16 v[20:21], v1 offset:24576\n\tds_read_b64_tr_b16 v[22:23], v1 offset:25088\n\tv_mfma_f32_16x16x32_bf16 v[56:59], v[32:35], v[184:187], v[56:59]\n\tbuffer_load_dwordx4 v[172:175], v8, s[8:11], s68 offen offset:1024 sc1\n\tv_mfma_f32_16x16x32_bf16 v[64:67], v[32:35], v[188:191], v[64:67]\n\ts_waitcnt lgkmcnt(8)\n\tv_mfma_f32_16x16x32_bf16 v[52:55], v[36:39], v[180:183], v[52:55]\n\tds_read_b64_tr_b16 v[24:25], v2 offset:24576\n\t" \
    "ds_read_b64_tr_b16 v[26:27], v2 offset:25088\n\tv_mfma_f32_16x16x32_bf16 v[60:63], v[36:39], v[184:187], v[60:63]\n\tv_mfma_f32_16x16x32_bf16 v[68:71], v[36:39], v[188:191], v[68:71]\n\ts_waitcnt lgkmcnt(8)\n\tv_mfma_f32_16x16x32_bf16 v[72:75], v[40:43], v[180:183], v[72:75]\n\tds_read_b64_tr_b16 v[28:29], v3 offset:24576\n\tds_read_b64_tr_b16 v[30:31], v3 offset:25088\n\tv_mfma_f32_16x16x32_bf16 v[80:83], v[40:43], v[184:187], v[80:83]\n\t" \
    "buffer_load_dwordx4 v[176:179], v8, s[8:11], s68 offen offset:2048 sc1\n\ts_cmp_ge_u32 s43, 11\n\ts_cselect_b32 s68, s70, s43\n\ts_mul_i32 s68, s68, 36864\n\tv_mfma_f32_16x16x32_bf16 v[88:91], v[40:43], v[188:191], v[88:91]\n\ts_add_u32 s68, s68, s44\n\ts_add_u32 s68, s68, 368640\n\ts_waitcnt lgkmcnt(8)\n\t" \
    "v_mfma_f32_16x16x32_bf16 v[76:79], v[44:47], v[180:183], v[76:79]\n\tds_read_b64_tr_b16 v[32:33], v0 offset:28672\n\tds_read_b64_tr_b16 v[34:35], v0 offset:29184\n\tv_mfma_f32_16x16x32_bf16 v[84:87], v[44:47], v[184:187], v[84:87]\n\tbuffer_load_dwordx4 a[44:47], v8, s[8:11], s68 offen offset:0 sc1\n\tv_mfma_f32_16x16x32_bf16 v[92:95], v[44:47], v[188:191], v[92:95]\n\ts_waitcnt vmcnt(19) lgkmcnt(8)\n\tv_mfma_f32_16x16x32_bf16 v[120:123], v[16:19], v[204:207], v[120:123]\n\t" \
    "ds_read_b64_tr_b16 v[36:37], v1 offset:28672\n\tds_read_b64_tr_b16 v[38:39], v1 offset:29184\n\tv_mfma_f32_16x16x32_bf16 v[128:131], v[16:19], v[208:211], v[128:131]\n\tbuffer_load_dwordx4 a[48:51], v8, s[8:11], s68 offen offset:1024 sc1\n\tv_mfma_f32_16x16x32_bf16 v[136:139], v[16:19], v[212:215], v[136:139]\n\ts_waitcnt lgkmcnt(8)\n\tv_mfma_f32_16x16x32_bf16 v[124:127], v[20:23], v[204:207], v[124:127]\n\tds_read_b64_tr_b16 v[40:41], v2 offset:28672\n\t" \
    "ds_read_b64_tr_b16 v[42:43], v2 offset:29184\n\tv_mfma_f32_16x16x32_bf16 v[132:135], v[20:23], v[208:211], v[132:135]\n\tv_mfma_f32_16x16x32_bf16 v[140:143], v[20:23], v[212:215], v[140:143]\n\ts_waitcnt lgkmcnt(8)\n\tv_mfma_f32_16x16x32_bf16 v[144:147], v[24:27], v[204:207], v[144:147]\n\tds_read_b64_tr_b16 v[44:45], v3 offset:28672\n\tds_read_b64_tr_b16 v[46:47], v3 offset:29184\n\tv_mfma_f32_16x16x32_bf16 v[152:155], v[24:27], v[208:211], v[152:155]\n\t" \
    "buffer_load_dwordx4 a[52:55], v8, s[8:11], s68 offen offset:2048 sc1\n\ts_cmp_ge_u32 s43, 10\n\ts_cselect_b32 s68, s70, s43\n\ts_mul_i32 s68, s68, 36864\n\tv_mfma_f32_16x16x32_bf16 v[160:163], v[24:27], v[212:215], v[160:163]\n\ts_add_u32 s68, s68, s44\n\ts_add_u32 s68, s68, 405504\n\ts_waitcnt lgkmcnt(8)\n\t" \
    "v_mfma_f32_16x16x32_bf16 v[148:151], v[28:31], v[204:207], v[148:151]\n\tds_read_b64_tr_b16 v[16:17], v0 offset:32768\n\tds_read_b64_tr_b16 v[18:19], v0 offset:33280\n\tv_mfma_f32_16x16x32_bf16 v[156:159], v[28:31], v[208:211], v[156:159]\n\tbuffer_load_dwordx4 v[180:183], v8, s[8:11], s68 offen offset:0 sc1\n\tv_mfma_f32_16x16x32_bf16 v[164:167], v[28:31], v[212:215], v[164:167]\n\ts_waitcnt lgkmcnt(8)\n\tv_mfma_f32_16x16x32_bf16 v[48:51], v[32:35], v[204:207], v[48:51]\n\t" \
    "ds_read_b64_tr_b16 v[20:21], v1 offset:32768\n\tds_read_b64_tr_b16 v[22:23], v1 offset:33280\n\tv_mfma_f32_16x16x32_bf16 v[56:59], v[32:35], v[208:211], v[56:59]\n\tbuffer_load_dwordx4 v[184:187], v8, s[8:11], s68 offen offset:1024 sc1\n\tv_mfma_f32_16x16x32_bf16 v[64:67], v[32:35], v[212:215], v[64:67]\n\ts_waitcnt lgkmcnt(8)\n\tv_mfma_f32_16x16x32_bf16 v[52:55], v[36:39], v[204:207], v[52:55]\n\tds_read_b64_tr_b16 v[24:25], v2 offset:32768\n\t" \
    "ds_read_b64_tr_b16 v[26:27], v2 offset:33280\n\tv_mfma_f32_16x16x32_bf16 v[60:63], v[36:39], v[208:211], v[60:63]\n\tv_mfma_f32_16x16x32_bf16 v[68:71], v[36:39], v[212:215], v[68:71]\n\ts_waitcnt lgkmcnt(8)\n\tv_mfma_f32_16x16x32_bf16 v[72:75], v[40:43], v[204:207], v[72:75]\n\tds_read_b64_tr_b16 v[28:29], v3 offset:32768\n\tds_read_b64_tr_b16 v[30:31], v3 offset:33280\n\tv_mfma_f32_16x16x32_bf16 v[80:83], v[40:43], v[208:211], v[80:83]\n\t" \
    "buffer_load_dwordx4 v[188:191], v8, s[8:11], s68 offen offset:2048 sc1\n\ts_cmp_ge_u32 s43, 9\n\ts_cselect_b32 s68, s70, s43\n\ts_mul_i32 s68, s68, 36864\n\tv_mfma_f32_16x16x32_bf16 v[88:91], v[40:43], v[212:215], v[88:91]\n\ts_add_u32 s68, s68, s44\n\ts_add_u32 s68, s68, 442368\n\ts_waitcnt lgkmcnt(8)\n\t" \
    "v_mfma_f32_16x16x32_bf16 v[76:79], v[44:47], v[204:207], v[76:79]\n\tds_read_b64_tr_b16 v[32:33], v0 offset:36864\n\tds_read_b64_tr_b16 v[34:35], v0 offset:37376\n\tv_mfma_f32_16x16x32_bf16 v[84:87], v[44:47], v[208:211], v[84:87]\n\tbuffer_load_dwordx4 a[56:59], v8, s[8:11], s68 offen offset:0 sc1\n\tv_mfma_f32_16x16x32_bf16 v[92:95], v[44:47], v[212:215], v[92:95]\n\ts_barrier\n\ts_waitcnt vmcnt(22) lgkmcnt(8)\n\t" \
    "v_mfma_f32_16x16x32_bf16 v[120:123], v[16:19], v[216:219], v[120:123]\n\tds_read_b64_tr_b16 v[36:37], v1 offset:36864\n\tds_read_b64_tr_b16 v[38:39], v1 offset:37376\n\tv_mfma_f32_16x16x32_bf16 v[128:131], v[16:19], a[0:3], v[128:131]\n\tbuffer_load_dwordx4 a[60:63], v8, s[8:11], s68 offen offset:1024 sc1\n\tv_mfma_f32_16x16x32_bf16 v[136:139], v[16:19], a[4:7], v[136:139]\n\ts_waitcnt lgkmcnt(8)\n\tv_mfma_f32_16x16x32_bf16 v[124:127], v[20:23], v[216:219], v[124:127]\n\t" \
    "ds_read_b64_tr_b16 v[40:41], v2 offset:36864\n\tds_read_b64_tr_b16 v[42:43], v2 offset:37376\n\tv_mfma_f32_16x16x32_bf16 v[132:135], v[20:23], a[0:3], v[132:135]\n\tbuffer_load_dwordx4 a[64:67], v8, s[8:11], s68 offen offset:2048 sc1\n\ts_cmp_ge_u32 s43, 8\n\ts_cselect_b32 s68, s70, s43\n\ts_mul_i32 s68, s68, 36864\n\tv_mfma_f32_16x16x32_bf16 v[140:143], v[20:23], a[4:7], v[140:143]\n\t" \
    "s_add_u32 s68, s68, s44\n\ts_add_u32 s68, s68, 479232\n\ts_waitcnt lgkmcnt(8)\n\tv_mfma_f32_16x16x32_bf16 v[144:147], v[24:27], v[216:219], v[144:147]\n\tds_read_b64_tr_b16 v[44:45], v3 offset:36864\n\tds_read_b64_tr_b16 v[46:47], v3 offset:37376\n\tv_mfma_f32_16x16x32_bf16 v[152:155], v[24:27], a[0:3], v[152:155]\n\tbuffer_load_dwordx4 v[204:207], v8, s[8:11], s68 offen offset:0 sc1\n\t" \
    "v_mfma_f32_16x16x32_bf16 v[160:163], v[24:27], a[4:7], v[160:163]\n\ts_waitcnt lgkmcnt(8)\n\tv_mfma_f32_16x16x32_bf16 v[148:151], v[28:31], v[216:219], v[148:151]\n\tds_read_b64_tr_b16 v[16:17], v0 offset:40960\n\tds_read_b64_tr_b16 v[18:19], v0 offset:41472\n\tv_mfma_f32_16x16x32_bf16 v[156:159], v[28:31], a[0:3], v[156:159]\n\tv_mfma_f32_16x16x32_bf16 v[164:167], v[28:31], a[4:7], v[164:167]\n\ts_waitcnt lgkmcnt(8)\n\t" \
    "v_mfma_f32_16x16x32_bf16 v[48:51], v[32:35], v[216:219], v[48:51]\n\tds_read_b64_tr_b16 v[20:21], v1 offset:40960\n\tds_read_b64_tr_b16 v[22:23], v1 offset:41472\n\tv_mfma_f32_16x16x32_bf16 v[56:59], v[32:35], a[0:3], v[56:59]\n\tbuffer_load_dwordx4 v[208:211], v8, s[8:11], s68 offen offset:1024 sc1\n\tv_mfma_f32_16x16x32_bf16 v[64:67], v[32:35], a[4:7], v[64:67]\n\ts_waitcnt lgkmcnt(8)\n\tv_mfma_f32_16x16x32_bf16 v[52:55], v[36:39], v[216:219], v[52:55]\n\t" \
    "ds_read_b64_tr_b16 v[24:25], v2 offset:40960\n\tds_read_b64_tr_b16 v[26:27], v2 offset:41472\n\tv_mfma_f32_16x16x32_bf16 v[60:63], v[36:39], a[0:3], v[60:63]\n\tbuffer_load_dwordx4 v[212:215], v8, s[8:11], s68 offen offset:2048 sc1\n\ts_cmp_ge_u32 s43, 7\n\ts_cselect_b32 s68, s70, s43\n\ts_mul_i32 s68, s68, 36864\n\tv_mfma_f32_16x16x32_bf16 v[68:71], v[36:39], a[4:7], v[68:71]\n\t" \
    "s_add_u32 s68, s68, s44\n\ts_add_u32 s68, s68, 516096\n\ts_waitcnt lgkmcnt(8)\n\tv_mfma_f32_16x16x32_bf16 v[72:75], v[40:43], v[216:219], v[72:75]\n\tds_read_b64_tr_b16 v[28:29], v3 offset:40960\n\tds_read_b64_tr_b16 v[30:31], v3 offset:41472\n\tv_mfma_f32_16x16x32_bf16 v[80:83], v[40:43], a[0:3], v[80:83]\n\tbuffer_load_dwordx4 a[68:71], v8, s[8:11], s68 offen offset:0 sc1\n\t" \
    "v_mfma_f32_16x16x32_bf16 v[88:91], v[40:43], a[4:7], v[88:91]\n\ts_waitcnt lgkmcnt(8)\n\tv_mfma_f32_16x16x32_bf16 v[76:79], v[44:47], v[216:219], v[76:79]\n\tds_read_b64_tr_b16 v[32:33], v0 offset:45056\n\tds_read_b64_tr_b16 v[34:35], v0 offset:45568\n\tv_mfma_f32_16x16x32_bf16 v[84:87], v[44:47], a[0:3], v[84:87]\n\tv_mfma_f32_16x16x32_bf16 v[92:95], v[44:47], a[4:7], v[92:95]\n\ts_waitcnt vmcnt(25) lgkmcnt(8)\n\t" \
    "v_mfma_f32_16x16x32_bf16 v[120:123], v[16:19], a[8:11], v[120:123]\n\tds_read_b64_tr_b16 v[36:37], v1 offset:45056\n\tds_read_b64_tr_b16 v[38:39], v1 offset:45568\n\tv_mfma_f32_16x16x32_bf16 v[128:131], v[16:19], a[12:15], v[128:131]\n\tbuffer_load_dwordx4 a[72:75], v8, s[8:11], s68 offen offset:1024 sc1\n\tv_mfma_f32_16x16x32_bf16 v[136:139], v[16:19], a[16:19], v[136:139]\n\ts_waitcnt lgkmcnt(8)\n\tv_mfma_f32_16x16x32_bf16 v[124:127], v[20:23], a[8:11], v[124:127]\n\t" \
    "ds_read_b64_tr_b16 v[40:41], v2 offset:45056\n\tds_read_b64_tr_b16 v[42:43], v2 offset:45568\n\tv_mfma_f32_16x16x32_bf16 v[132:135], v[20:23], a[12:15], v[132:135]\n\tbuffer_load_dwordx4 a[76:79], v8, s[8:11], s68 offen offset:2048 sc1\n\ts_cmp_ge_u32 s43, 6\n\ts_cselect_b32 s68, s70, s43\n\ts_mul_i32 s68, s68, 36864\n\tv_mfma_f32_16x16x32_bf16 v[140:143], v[20:23], a[16:19], v[140:143]\n\t" \
    "s_add_u32 s68, s68, s44\n\ts_add_u32 s68, s68, 552960\n\ts_waitcnt lgkmcnt(8)\n\tv_mfma_f32_16x16x32_bf16 v[144:147], v[24:27], a[8:11], v[144:147]\n\tds_read_b64_tr_b16 v[44:45], v3 offset:45056\n\tds_read_b64_tr_b16 v[46:47], v3 offset:45568\n\tv_mfma_f32_16x16x32_bf16 v[152:155], v[24:27], a[12:15], v[152:155]\n\tbuffer_load_dwordx4 v[216:219], v8, s[8:11], s68 offen offset:0 sc1\n\t" \
    "v_mfma_f32_16x16x32_bf16 v[160:163], v[24:27], a[16:19], v[160:163]\n\ts_waitcnt lgkmcnt(8)\n\tv_mfma_f32_16x16x32_bf16 v[148:151], v[28:31], a[8:11], v[148:151]\n\tds_read_b64_tr_b16 v[16:17], v0 offset:49152\n\tds_read_b64_tr_b16 v[18:19], v0 offset:49664\n\tv_mfma_f32_16x16x32_bf16 v[156:159], v[28:31], a[12:15], v[156:159]\n\tv_mfma_f32_16x16x32_bf16 v[164:167], v[28:31], a[16:19], v[164:167]\n\ts_waitcnt lgkmcnt(8)\n\t" \
    "v_mfma_f32_16x16x32_bf16 v[48:51], v[32:35], a[8:11], v[48:51]\n\tds_read_b64_tr_b16 v[20:21], v1 offset:49152\n\tds_read_b64_tr_b16 v[22:23], v1 offset:49664\n\tv_mfma_f32_16x16x32_bf16 v[56:59], v[32:35], a[12:15], v[56:59]\n\tbuffer_load_dwordx4 a[0:3], v8, s[8:11], s68 offen offset:1024 sc1\n\tv_mfma_f32_16x16x32_bf16 v[64:67], v[32:35], a[16:19], v[64:67]\n\ts_waitcnt lgkmcnt(8)\n\tv_mfma_f32_16x16x32_bf16 v[52:55], v[36:39], a[8:11], v[52:55]\n\t" \
    "ds_read_b64_tr_b16 v[24:25], v2 offset:49152\n\tds_read_b64_tr_b16 v[26:27], v2 offset:49664\n\tv_mfma_f32_16x16x32_bf16 v[60:63], v[36:39], a[12:15], v[60:63]\n\tbuffer_load_dwordx4 a[4:7], v8, s[8:11], s68 offen offset:2048 sc1\n\ts_cmp_ge_u32 s43, 5\n\ts_cselect_b32 s68, s70, s43\n\ts_mul_i32 s68, s68, 36864\n\tv_mfma_f32_16x16x32_bf16 v[68:71], v[36:39], a[16:19], v[68:71]\n\t" \
    "s_add_u32 s68, s68, s44\n\ts_add_u32 s68, s68, 589824\n\ts_waitcnt lgkmcnt(8)\n\tv_mfma_f32_16x16x32_bf16 v[72:75], v[40:43], a[8:11], v[72:75]\n\tds_read_b64_tr_b16 v[28:29], v3 offset:49152\n\tds_read_b64_tr_b16 v[30:31], v3 offset:49664\n\tv_mfma_f32_16x16x32_bf16 v[80:83], v[40:43], a[12:15], v[80:83]\n\tbuffer_load_dwordx4 a[80:83], v8, s[8:11], s68 offen offset:0 sc1\n\t" \
    "v_mfma_f32_16x16x32_bf16 v[88:91], v[40:43], a[16:19], v[88:91]\n\ts_waitcnt lgkmcnt(8)\n\tv_mfma_f32_16x16x32_bf16 v[76:79], v[44:47], a[8:11], v[76:79]\n\tds_read_b64_tr_b16 v[32:33], v0 offset:53248\n\tds_read_b64_tr_b16 v[34:35], v0 offset:53760\n\tv_mfma_f32_16x16x32_bf16 v[84:87], v[44:47], a[12:15], v[84:87]\n\tv_mfma_f32_16x16x32_bf16 v[92:95], v[44:47], a[16:19], v[92:95]\n\ts_barrier\n\t" \
    "s_waitcnt vmcnt(28) lgkmcnt(8)\n\tv_mfma_f32_16x16x32_bf16 v[120:123], v[16:19], a[20:23], v[120:123]\n\tds_read_b64_tr_b16 v[36:37], v1 offset:53248\n\tds_read_b64_tr_b16 v[38:39], v1 offset:53760\n\tv_mfma_f32_16x16x32_bf16 v[128:131], v[16:19], a[24:27], v[128:131]\n\tbuffer_load_dwordx4 a[84:87], v8, s[8:11], s68 offen offset:1024 sc1\n\tv_mfma_f32_16x16x32_bf16 v[136:139], v[16:19], a[28:31], v[136:139]\n\ts_waitcnt lgkmcnt(8)\n\t" \
    "v_mfma_f32_16x16x32_bf16 v[124:127], v[20:23], a[20:23], v[124:127]\n\tds_read_b64_tr_b16 v[40:41], v2 offset:53248\n\tds_read_b64_tr_b16 v[42:43], v2 offset:53760\n\tv_mfma_f32_16x16x32_bf16 v[132:135], v[20:23], a[24:27], v[132:135]\n\tbuffer_load_dwordx4 a[88:91], v8, s[8:11], s68 offen offset:2048 sc1\n\ts_cmp_ge_u32 s43, 1\n\ts_cselect_b32 s69, s70, s43\n\ts_mul_i32 s69, s69, 294912\n\t" \
    "v_mfma_f32_16x16x32_bf16 v[140:143], v[20:23], a[28:31], v[140:143]\n\ts_add_u32 s69, s69, 5898240\n\ts_add_u32 m0, s54, 0\n\ts_waitcnt lgkmcnt(8)\n\tv_mfma_f32_16x16x32_bf16 v[144:147], v[24:27], a[20:23], v[144:147]\n\tds_read_b64_tr_b16 v[44:45], v3 offset:53248\n\tds_read_b64_tr_b16 v[46:47], v3 offset:53760\n\tv_mfma_f32_16x16x32_bf16 v[152:155], v[24:27], a[24:27], v[152:155]\n\t" \
    "buffer_load_dwordx4 v10, s[12:15], s69 offen lds\n\ts_mov_b32 s69, 6193152\n\ts_add_u32 m0, s54, 4096\n\tv_mfma_f32_16x16x32_bf16 v[160:163], v[24:27], a[28:31], v[160:163]\n\ts_waitcnt lgkmcnt(8)\n\tv_mfma_f32_16x16x32_bf16 v[148:151], v[28:31], a[20:23], v[148:151]\n\tds_read_b64_tr_b16 v[16:17], v0 offset:57344\n\tds_read_b64_tr_b16 v[18:19], v0 offset:57856\n\t" \
    "v_mfma_f32_16x16x32_bf16 v[156:159], v[28:31], a[24:27], v[156:159]\n\tbuffer_load_dwordx4 v10, s[12:15], s69 offen lds\n\ts_cmp_ge_u32 s43, 4\n\ts_cselect_b32 s68, s70, s43\n\ts_mul_i32 s68, s68, 36864\n\tv_mfma_f32_16x16x32_bf16 v[164:167], v[28:31], a[28:31], v[164:167]\n\ts_add_u32 s68, s68, s44\n\ts_add_u32 s68, s68, 626688\n\t" \
    "s_waitcnt lgkmcnt(8)\n\tv_mfma_f32_16x16x32_bf16 v[48:51], v[32:35], a[20:23], v[48:51]\n\tds_read_b64_tr_b16 v[20:21], v1 offset:57344\n\tds_read_b64_tr_b16 v[22:23], v1 offset:57856\n\tv_mfma_f32_16x16x32_bf16 v[56:59], v[32:35], a[24:27], v[56:59]\n\tbuffer_load_dwordx4 a[8:11], v8, s[8:11], s68 offen offset:0 sc1\n\tv_mfma_f32_16x16x32_bf16 v[64:67], v[32:35], a[28:31], v[64:67]\n\ts_waitcnt lgkmcnt(8)\n\t" \
    "v_mfma_f32_16x16x32_bf16 v[52:55], v[36:39], a[20:23], v[52:55]\n\tds_read_b64_tr_b16 v[24:25], v2 offset:57344\n\tds_read_b64_tr_b16 v[26:27], v2 offset:57856\n\tv_mfma_f32_16x16x32_bf16 v[60:63], v[36:39], a[24:27], v[60:63]\n\tv_mfma_f32_16x16x32_bf16 v[68:71], v[36:39], a[28:31], v[68:71]\n\ts_waitcnt lgkmcnt(8)\n\tv_mfma_f32_16x16x32_bf16 v[72:75], v[40:43], a[20:23], v[72:75]\n\tds_read_b64_tr_b16 v[28:29], v3 offset:57344\n\t" \
    "ds_read_b64_tr_b16 v[30:31], v3 offset:57856\n\tv_mfma_f32_16x16x32_bf16 v[80:83], v[40:43], a[24:27], v[80:83]\n\tbuffer_load_dwordx4 a[12:15], v8, s[8:11], s68 offen offset:1024 sc1\n\tv_mfma_f32_16x16x32_bf16 v[88:91], v[40:43], a[28:31], v[88:91]\n\ts_waitcnt lgkmcnt(8)\n\tv_mfma_f32_16x16x32_bf16 v[76:79], v[44:47], a[20:23], v[76:79]\n\tds_read_b64_tr_b16 v[32:33], v0 offset:61440\n\tds_read_b64_tr_b16 v[34:35], v0 offset:61952\n\t" \
    "v_mfma_f32_16x16x32_bf16 v[84:87], v[44:47], a[24:27], v[84:87]\n\tbuffer_load_dwordx4 a[16:19], v8, s[8:11], s68 offen offset:2048 sc1\n\ts_cmp_ge_u32 s43, 3\n\ts_cselect_b32 s68, s70, s43\n\ts_mul_i32 s68, s68, 36864\n\tv_mfma_f32_16x16x32_bf16 v[92:95], v[44:47], a[28:31], v[92:95]\n\ts_add_u32 s68, s68, s44\n\ts_add_u32 s68, s68, 663552\n\t" \
    "s_waitcnt vmcnt(32) lgkmcnt(8)\n\tv_mfma_f32_16x16x32_bf16 v[120:123], v[16:19], v[192:195], v[120:123]\n\tds_read_b64_tr_b16 v[36:37], v1 offset:61440\n\tds_read_b64_tr_b16 v[38:39], v1 offset:61952\n\tv_mfma_f32_16x16x32_bf16 v[128:131], v[16:19], v[196:199], v[128:131]\n\tbuffer_load_dwordx4 a[92:95], v8, s[8:11], s68 offen offset:0 sc1\n\tv_mfma_f32_16x16x32_bf16 v[136:139], v[16:19], v[200:203], v[136:139]\n\ts_waitcnt lgkmcnt(8)\n\t" \
    "v_mfma_f32_16x16x32_bf16 v[124:127], v[20:23], v[192:195], v[124:127]\n\tds_read_b64_tr_b16 v[40:41], v2 offset:61440\n\tds_read_b64_tr_b16 v[42:43], v2 offset:61952\n\tv_mfma_f32_16x16x32_bf16 v[132:135], v[20:23], v[196:199], v[132:135]\n\tv_mfma_f32_16x16x32_bf16 v[140:143], v[20:23], v[200:203], v[140:143]\n\ts_waitcnt lgkmcnt(8)\n\tv_mfma_f32_16x16x32_bf16 v[144:147], v[24:27], v[192:195], v[144:147]\n\tds_read_b64_tr_b16 v[44:45], v3 offset:61440\n\t" \
    "ds_read_b64_tr_b16 v[46:47], v3 offset:61952\n\tv_mfma_f32_16x16x32_bf16 v[152:155], v[24:27], v[196:199], v[152:155]\n\tbuffer_load_dwordx4 a[96:99], v8, s[8:11], s68 offen offset:1024 sc1\n\tv_mfma_f32_16x16x32_bf16 v[160:163], v[24:27], v[200:203], v[160:163]\n\ts_waitcnt lgkmcnt(8)\n\tv_mfma_f32_16x16x32_bf16 v[148:151], v[28:31], v[192:195], v[148:151]\n\tds_read_b64_tr_b16 v[16:17], v14 offset:0\n\tds_read_b64_tr_b16 v[18:19], v14 offset:512\n\t" \
    "v_mfma_f32_16x16x32_bf16 v[156:159], v[28:31], v[196:199], v[156:159]\n\tbuffer_load_dwordx4 a[100:103], v8, s[8:11], s68 offen offset:2048 sc1\n\ts_cmp_ge_u32 s43, 2\n\ts_cselect_b32 s68, s70, s43\n\ts_mul_i32 s68, s68, 36864\n\tv_mfma_f32_16x16x32_bf16 v[164:167], v[28:31], v[200:203], v[164:167]\n\ts_add_u32 s68, s68, s44\n\ts_add_u32 s68, s68, 700416\n\t" \
    "s_waitcnt lgkmcnt(8)\n\tv_mfma_f32_16x16x32_bf16 v[48:51], v[32:35], v[192:195], v[48:51]\n\tds_read_b64_tr_b16 v[20:21], v15 offset:0\n\tds_read_b64_tr_b16 v[22:23], v15 offset:512\n\tv_mfma_f32_16x16x32_bf16 v[56:59], v[32:35], v[196:199], v[56:59]\n\tbuffer_load_dwordx4 a[20:23], v8, s[8:11], s68 offen offset:0 sc1\n\tv_mfma_f32_16x16x32_bf16 v[64:67], v[32:35], v[200:203], v[64:67]\n\ts_waitcnt lgkmcnt(8)\n\t" \
    "v_mfma_f32_16x16x32_bf16 v[52:55], v[36:39], v[192:195], v[52:55]\n\tds_read_b64_tr_b16 v[24:25], v237 offset:0\n\tds_read_b64_tr_b16 v[26:27], v237 offset:512\n\tv_mfma_f32_16x16x32_bf16 v[60:63], v[36:39], v[196:199], v[60:63]\n\tv_mfma_f32_16x16x32_bf16 v[68:71], v[36:39], v[200:203], v[68:71]\n\ts_waitcnt lgkmcnt(8)\n\tv_mfma_f32_16x16x32_bf16 v[72:75], v[40:43], v[192:195], v[72:75]\n\tds_read_b64_tr_b16 v[28:29], v238 offset:0\n\t" \
    "ds_read_b64_tr_b16 v[30:31], v238 offset:512\n\tv_mfma_f32_16x16x32_bf16 v[80:83], v[40:43], v[196:199], v[80:83]\n\tbuffer_load_dwordx4 a[24:27], v8, s[8:11], s68 offen offset:1024 sc1\n\tv_mfma_f32_16x16x32_bf16 v[88:91], v[40:43], v[200:203], v[88:91]\n\ts_waitcnt lgkmcnt(8)\n\tv_mfma_f32_16x16x32_bf16 v[76:79], v[44:47], v[192:195], v[76:79]\n\tds_read_b64_tr_b16 v[32:33], v14 offset:4096\n\tds_read_b64_tr_b16 v[34:35], v14 offset:4608\n\t" \
    "v_mfma_f32_16x16x32_bf16 v[84:87], v[44:47], v[196:199], v[84:87]\n\tbuffer_load_dwordx4 a[28:31], v8, s[8:11], s68 offen offset:2048 sc1\n\ts_cmp_ge_u32 s43, 1\n\ts_cselect_b32 s68, s70, s43\n\ts_mul_i32 s68, s68, 36864\n\tv_mfma_f32_16x16x32_bf16 v[92:95], v[44:47], v[200:203], v[92:95]\n\ts_add_u32 s68, s68, s44\n\ts_add_u32 s68, s68, 737280\n\t" \
    "s_barrier\n\ts_waitcnt vmcnt(35) lgkmcnt(8)\n\tv_mfma_f32_16x16x32_bf16 v[120:123], v[16:19], a[32:35], v[120:123]\n\tds_read_b64_tr_b16 v[36:37], v15 offset:4096\n\tds_read_b64_tr_b16 v[38:39], v15 offset:4608\n\tv_mfma_f32_16x16x32_bf16 v[128:131], v[16:19], a[36:39], v[128:131]\n\tbuffer_load_dwordx4 a[104:107], v8, s[8:11], s68 offen offset:0 sc1\n\tv_mfma_f32_16x16x32_bf16 v[136:139], v[16:19], a[40:43], v[136:139]\n\t" \
    "s_waitcnt lgkmcnt(8)\n\tv_mfma_f32_16x16x32_bf16 v[124:127], v[20:23], a[32:35], v[124:127]\n\tds_read_b64_tr_b16 v[40:41], v237 offset:4096\n\tds_read_b64_tr_b16 v[42:43], v237 offset:4608\n\tv_mfma_f32_16x16x32_bf16 v[132:135], v[20:23], a[36:39], v[132:135]\n\tbuffer_load_dwordx4 a[108:111], v8, s[8:11], s68 offen offset:1024 sc1\n\tv_mfma_f32_16x16x32_bf16 v[140:143], v[20:23], a[40:43], v[140:143]\n\ts_waitcnt lgkmcnt(8)\n\t" \
    "v_mfma_f32_16x16x32_bf16 v[144:147], v[24:27], a[32:35], v[144:147]\n\tds_read_b64_tr_b16 v[44:45], v238 offset:4096\n\tds_read_b64_tr_b16 v[46:47], v238 offset:4608\n\tv_mfma_f32_16x16x32_bf16 v[152:155], v[24:27], a[36:39], v[152:155]\n\tbuffer_load_dwordx4 a[112:115], v8, s[8:11], s68 offen offset:2048 sc1\n\ts_mov_b32 s69, 6488064\n\ts_add_u32 m0, s54, 8192\n\tv_mfma_f32_16x16x32_bf16 v[160:163], v[24:27], a[40:43], v[160:163]\n\t" \
    "s_waitcnt lgkmcnt(8)\n\tv_mfma_f32_16x16x32_bf16 v[148:151], v[28:31], a[32:35], v[148:151]\n\tds_read_b64_tr_b16 v[16:17], v14 offset:8192\n\tds_read_b64_tr_b16 v[18:19], v14 offset:8704\n\tv_mfma_f32_16x16x32_bf16 v[156:159], v[28:31], a[36:39], v[156:159]\n\tv_mfma_f32_16x16x32_bf16 v[164:167], v[28:31], a[40:43], v[164:167]\n\ts_waitcnt lgkmcnt(8)\n\tv_mfma_f32_16x16x32_bf16 v[48:51], v[32:35], a[32:35], v[48:51]\n\t" \
    "ds_read_b64_tr_b16 v[20:21], v15 offset:8192\n\tds_read_b64_tr_b16 v[22:23], v15 offset:8704\n\tv_mfma_f32_16x16x32_bf16 v[56:59], v[32:35], a[36:39], v[56:59]\n\tbuffer_load_dwordx4 v10, s[12:15], s69 offen lds\n\ts_mov_b32 s69, 6782976\n\ts_add_u32 m0, s54, 12288\n\tv_mfma_f32_16x16x32_bf16 v[64:67], v[32:35], a[40:43], v[64:67]\n\ts_waitcnt lgkmcnt(8)\n\t" \
    "v_mfma_f32_16x16x32_bf16 v[52:55], v[36:39], a[32:35], v[52:55]\n\tds_read_b64_tr_b16 v[24:25], v237 offset:8192\n\tds_read_b64_tr_b16 v[26:27], v237 offset:8704\n\tv_mfma_f32_16x16x32_bf16 v[60:63], v[36:39], a[36:39], v[60:63]\n\tbuffer_load_dwordx4 v10, s[12:15], s69 offen lds\n\ts_mov_b32 s69, 7077888\n\ts_add_u32 m0, s54, 16384\n\tv_mfma_f32_16x16x32_bf16 v[68:71], v[36:39], a[40:43], v[68:71]\n\t" \
    "s_waitcnt lgkmcnt(8)\n\tv_mfma_f32_16x16x32_bf16 v[72:75], v[40:43], a[32:35], v[72:75]\n\tds_read_b64_tr_b16 v[28:29], v238 offset:8192\n\tds_read_b64_tr_b16 v[30:31], v238 offset:8704\n\tv_mfma_f32_16x16x32_bf16 v[80:83], v[40:43], a[36:39], v[80:83]\n\tbuffer_load_dwordx4 v10, s[12:15], s69 offen lds\n\ts_mov_b32 s69, 7372800\n\ts_add_u32 m0, s54, 20480\n\t" \
    "v_mfma_f32_16x16x32_bf16 v[88:91], v[40:43], a[40:43], v[88:91]\n\ts_waitcnt lgkmcnt(8)\n\tv_mfma_f32_16x16x32_bf16 v[76:79], v[44:47], a[32:35], v[76:79]\n\tds_read_b64_tr_b16 v[32:33], v14 offset:12288\n\tds_read_b64_tr_b16 v[34:35], v14 offset:12800\n\tv_mfma_f32_16x16x32_bf16 v[84:87], v[44:47], a[36:39], v[84:87]\n\tv_mfma_f32_16x16x32_bf16 v[92:95], v[44:47], a[40:43], v[92:95]\n\ts_waitcnt vmcnt(38) lgkmcnt(8)\n\t" \
    "v_mfma_f32_16x16x32_bf16 v[120:123], v[16:19], v[168:171], v[120:123]\n\tds_read_b64_tr_b16 v[36:37], v15 offset:12288\n\tds_read_b64_tr_b16 v[38:39], v15 offset:12800\n\tv_mfma_f32_16x16x32_bf16 v[128:131], v[16:19], v[172:175], v[128:131]\n\tbuffer_load_dwordx4 v10, s[12:15], s69 offen lds\n\ts_mov_b32 s69, 7667712\n\ts_add_u32 m0, s54, 24576\n\tv_mfma_f32_16x16x32_bf16 v[136:139], v[16:19], v[176:179], v[136:139]\n\t" \
    "s_waitcnt lgkmcnt(8)\n\tv_mfma_f32_16x16x32_bf16 v[124:127], v[20:23], v[168:171], v[124:127]\n\tds_read_b64_tr_b16 v[40:41], v237 offset:12288\n\tds_read_b64_tr_b16 v[42:43], v237 offset:12800\n\tv_mfma_f32_16x16x32_bf16 v[132:135], v[20:23], v[172:175], v[132:135]\n\tbuffer_load_dwordx4 v10, s[12:15], s69 offen lds\n\ts_mov_b32 s69, 7962624\n\ts_add_u32 m0, s54, 28672\n\t" \
    "v_mfma_f32_16x16x32_bf16 v[140:143], v[20:23], v[176:179], v[140:143]\n\ts_waitcnt lgkmcnt(8)\n\tv_mfma_f32_16x16x32_bf16 v[144:147], v[24:27], v[168:171], v[144:147]\n\tds_read_b64_tr_b16 v[44:45], v238 offset:12288\n\tds_read_b64_tr_b16 v[46:47], v238 offset:12800\n\tv_mfma_f32_16x16x32_bf16 v[152:155], v[24:27], v[172:175], v[152:155]\n\tbuffer_load_dwordx4 v10, s[12:15], s69 offen lds\n\ts_mov_b32 s69, 8257536\n\t" \
    "s_add_u32 m0, s54, 32768\n\tv_mfma_f32_16x16x32_bf16 v[160:163], v[24:27], v[176:179], v[160:163]\n\ts_waitcnt lgkmcnt(8)\n\tv_mfma_f32_16x16x32_bf16 v[148:151], v[28:31], v[168:171], v[148:151]\n\tds_read_b64_tr_b16 v[16:17], v14 offset:16384\n\tds_read_b64_tr_b16 v[18:19], v14 offset:16896\n\tv_mfma_f32_16x16x32_bf16 v[156:159], v[28:31], v[172:175], v[156:159]\n\tv_mfma_f32_16x16x32_bf16 v[164:167], v[28:31], v[176:179], v[164:167]\n\t" \
    "s_waitcnt lgkmcnt(8)\n\tv_mfma_f32_16x16x32_bf16 v[48:51], v[32:35], v[168:171], v[48:51]\n\tds_read_b64_tr_b16 v[20:21], v15 offset:16384\n\tds_read_b64_tr_b16 v[22:23], v15 offset:16896\n\tv_mfma_f32_16x16x32_bf16 v[56:59], v[32:35], v[172:175], v[56:59]\n\tbuffer_load_dwordx4 v10, s[12:15], s69 offen lds\n\ts_mov_b32 s69, 8552448\n\ts_add_u32 m0, s54, 36864\n\t" \
    "v_mfma_f32_16x16x32_bf16 v[64:67], v[32:35], v[176:179], v[64:67]\n\ts_waitcnt lgkmcnt(8)\n\tv_mfma_f32_16x16x32_bf16 v[52:55], v[36:39], v[168:171], v[52:55]\n\tds_read_b64_tr_b16 v[24:25], v237 offset:16384\n\tds_read_b64_tr_b16 v[26:27], v237 offset:16896\n\tv_mfma_f32_16x16x32_bf16 v[60:63], v[36:39], v[172:175], v[60:63]\n\tbuffer_load_dwordx4 v10, s[12:15], s69 offen lds\n\ts_add_u32 s68, s44, 774144\n\t" \
    "v_mfma_f32_16x16x32_bf16 v[68:71], v[36:39], v[176:179], v[68:71]\n\ts_waitcnt lgkmcnt(8)\n\tv_mfma_f32_16x16x32_bf16 v[72:75], v[40:43], v[168:171], v[72:75]\n\tds_read_b64_tr_b16 v[28:29], v238 offset:16384\n\tds_read_b64_tr_b16 v[30:31], v238 offset:16896\n\tv_mfma_f32_16x16x32_bf16 v[80:83], v[40:43], v[172:175], v[80:83]\n\tv_mfma_f32_16x16x32_bf16 v[88:91], v[40:43], v[176:179], v[88:91]\n\ts_waitcnt lgkmcnt(8)\n\t" \
    "v_mfma_f32_16x16x32_bf16 v[76:79], v[44:47], v[168:171], v[76:79]\n\tds_read_b64_tr_b16 v[32:33], v14 offset:20480\n\tds_read_b64_tr_b16 v[34:35], v14 offset:20992\n\tv_mfma_f32_16x16x32_bf16 v[84:87], v[44:47], v[172:175], v[84:87]\n\tbuffer_load_dwordx4 a[32:35], v8, s[8:11], s68 offen offset:0 sc1\n\tv_mfma_f32_16x16x32_bf16 v[92:95], v[44:47], v[176:179], v[92:95]\n\ts_barrier\n\ts_waitcnt vmcnt(41) lgkmcnt(8)\n\t" \
    "v_mfma_f32_16x16x32_bf16 v[120:123], v[16:19], a[44:47], v[120:123]\n\tds_read_b64_tr_b16 v[36:37], v15 offset:20480\n\tds_read_b64_tr_b16 v[38:39], v15 offset:20992\n\tv_mfma_f32_16x16x32_bf16 v[128:131], v[16:19], a[48:51], v[128:131]\n\tbuffer_load_dwordx4 a[36:39], v8, s[8:11], s68 offen offset:1024 sc1\n\tv_mfma_f32_16x16x32_bf16 v[136:139], v[16:19], a[52:55], v[136:139]\n\ts_waitcnt lgkmcnt(8)\n\tv_mfma_f32_16x16x32_bf16 v[124:127], v[20:23], a[44:47], v[124:127]\n\t" \
    "ds_read_b64_tr_b16 v[40:41], v237 offset:20480\n\tds_read_b64_tr_b16 v[42:43], v237 offset:20992\n\tv_mfma_f32_16x16x32_bf16 v[132:135], v[20:23], a[48:51], v[132:135]\n\tbuffer_load_dwordx4 a[40:43], v8, s[8:11], s68 offen offset:2048 sc1\n\ts_add_u32 s68, s44, 811008\n\tv_mfma_f32_16x16x32_bf16 v[140:143], v[20:23], a[52:55], v[140:143]\n\ts_waitcnt lgkmcnt(8)\n\tv_mfma_f32_16x16x32_bf16 v[144:147], v[24:27], a[44:47], v[144:147]\n\t" \
    "ds_read_b64_tr_b16 v[44:45], v238 offset:20480\n\tds_read_b64_tr_b16 v[46:47], v238 offset:20992\n\tv_mfma_f32_16x16x32_bf16 v[152:155], v[24:27], a[48:51], v[152:155]\n\tbuffer_load_dwordx4 v[168:171], v8, s[8:11], s68 offen offset:0 sc1\n\tv_mfma_f32_16x16x32_bf16 v[160:163], v[24:27], a[52:55], v[160:163]\n\ts_waitcnt lgkmcnt(8)\n\tv_mfma_f32_16x16x32_bf16 v[148:151], v[28:31], a[44:47], v[148:151]\n\tds_read_b64_tr_b16 v[16:17], v14 offset:24576\n\t" \
    "ds_read_b64_tr_b16 v[18:19], v14 offset:25088\n\tv_mfma_f32_16x16x32_bf16 v[156:159], v[28:31], a[48:51], v[156:159]\n\tbuffer_load_dwordx4 v[172:175], v8, s[8:11], s68 offen offset:1024 sc1\n\tv_mfma_f32_16x16x32_bf16 v[164:167], v[28:31], a[52:55], v[164:167]\n\ts_waitcnt lgkmcnt(8)\n\tv_mfma_f32_16x16x32_bf16 v[48:51], v[32:35], a[44:47], v[48:51]\n\tds_read_b64_tr_b16 v[20:21], v15 offset:24576\n\tds_read_b64_tr_b16 v[22:23], v15 offset:25088\n\t" \
    "v_mfma_f32_16x16x32_bf16 v[56:59], v[32:35], a[48:51], v[56:59]\n\tv_mfma_f32_16x16x32_bf16 v[64:67], v[32:35], a[52:55], v[64:67]\n\ts_waitcnt lgkmcnt(8)\n\tv_mfma_f32_16x16x32_bf16 v[52:55], v[36:39], a[44:47], v[52:55]\n\tds_read_b64_tr_b16 v[24:25], v237 offset:24576\n\tds_read_b64_tr_b16 v[26:27], v237 offset:25088\n\tv_mfma_f32_16x16x32_bf16 v[60:63], v[36:39], a[48:51], v[60:63]\n\tbuffer_load_dwordx4 v[176:179], v8, s[8:11], s68 offen offset:2048 sc1\n\t" \
    "s_add_u32 s68, s44, 847872\n\tv_mfma_f32_16x16x32_bf16 v[68:71], v[36:39], a[52:55], v[68:71]\n\ts_waitcnt lgkmcnt(8)\n\tv_mfma_f32_16x16x32_bf16 v[72:75], v[40:43], a[44:47], v[72:75]\n\tds_read_b64_tr_b16 v[28:29], v238 offset:24576\n\tds_read_b64_tr_b16 v[30:31], v238 offset:25088\n\tv_mfma_f32_16x16x32_bf16 v[80:83], v[40:43], a[48:51], v[80:83]\n\tbuffer_load_dwordx4 a[116:119], v8, s[8:11], s68 offen offset:0 sc1\n\t" \
    "v_mfma_f32_16x16x32_bf16 v[88:91], v[40:43], a[52:55], v[88:91]\n\ts_waitcnt lgkmcnt(8)\n\tv_mfma_f32_16x16x32_bf16 v[76:79], v[44:47], a[44:47], v[76:79]\n\tds_read_b64_tr_b16 v[32:33], v14 offset:28672\n\tds_read_b64_tr_b16 v[34:35], v14 offset:29184\n\tv_mfma_f32_16x16x32_bf16 v[84:87], v[44:47], a[48:51], v[84:87]\n\tbuffer_load_dwordx4 a[120:123], v8, s[8:11], s68 offen offset:1024 sc1\n\tv_mfma_f32_16x16x32_bf16 v[92:95], v[44:47], a[52:55], v[92:95]\n\t" \
    "s_waitcnt vmcnt(45) lgkmcnt(8)\n\tv_mfma_f32_16x16x32_bf16 v[120:123], v[16:19], v[180:183], v[120:123]\n\tds_read_b64_tr_b16 v[36:37], v15 offset:28672\n\tds_read_b64_tr_b16 v[38:39], v15 offset:29184\n\tv_mfma_f32_16x16x32_bf16 v[128:131], v[16:19], v[184:187], v[128:131]\n\tv_mfma_f32_16x16x32_bf16 v[136:139], v[16:19], v[188:191], v[136:139]\n\ts_waitcnt lgkmcnt(8)\n\tv_mfma_f32_16x16x32_bf16 v[124:127], v[20:23], v[180:183], v[124:127]\n\t" \
    "ds_read_b64_tr_b16 v[40:41], v14 offset:32768\n\tds_read_b64_tr_b16 v[42:43], v14 offset:33280\n\tv_mfma_f32_16x16x32_bf16 v[132:135], v[20:23], v[184:187], v[132:135]\n\tbuffer_load_dwordx4 a[124:127], v8, s[8:11], s68 offen offset:2048 sc1\n\ts_add_u32 s68, s44, 884736\n\tv_mfma_f32_16x16x32_bf16 v[140:143], v[20:23], v[188:191], v[140:143]\n\ts_waitcnt lgkmcnt(8)\n\tv_mfma_f32_16x16x32_bf16 v[144:147], v[24:27], v[180:183], v[144:147]\n\t" \
    "ds_read_b64_tr_b16 v[44:45], v15 offset:32768\n\tds_read_b64_tr_b16 v[46:47], v15 offset:33280\n\tv_mfma_f32_16x16x32_bf16 v[152:155], v[24:27], v[184:187], v[152:155]\n\tbuffer_load_dwordx4 a[44:47], v8, s[8:11], s68 offen offset:0 sc1\n\tv_mfma_f32_16x16x32_bf16 v[160:163], v[24:27], v[188:191], v[160:163]\n\ts_waitcnt lgkmcnt(8)\n\tv_mfma_f32_16x16x32_bf16 v[148:151], v[28:31], v[180:183], v[148:151]\n\tds_read_b64_tr_b16 v[16:17], v237 offset:32768\n\t" \
    "ds_read_b64_tr_b16 v[18:19], v237 offset:33280\n\tv_mfma_f32_16x16x32_bf16 v[156:159], v[28:31], v[184:187], v[156:159]\n\tv_mfma_f32_16x16x32_bf16 v[164:167], v[28:31], v[188:191], v[164:167]\n\ts_waitcnt lgkmcnt(8)\n\tv_mfma_f32_16x16x32_bf16 v[48:51], v[32:35], v[180:183], v[48:51]\n\tds_read_b64_tr_b16 v[20:21], v238 offset:32768\n\tds_read_b64_tr_b16 v[22:23], v238 offset:33280\n\tv_mfma_f32_16x16x32_bf16 v[56:59], v[32:35], v[184:187], v[56:59]\n\t" \
    "buffer_load_dwordx4 a[48:51], v8, s[8:11], s68 offen offset:1024 sc1\n\tv_mfma_f32_16x16x32_bf16 v[64:67], v[32:35], v[188:191], v[64:67]\n\ts_waitcnt lgkmcnt(8)\n\tv_mfma_f32_16x16x32_bf16 v[52:55], v[36:39], v[180:183], v[52:55]\n\tds_read_b64_tr_b16 v[24:25], v14 offset:36864\n\tds_read_b64_tr_b16 v[26:27], v14 offset:37376\n\tv_mfma_f32_16x16x32_bf16 v[60:63], v[36:39], v[184:187], v[60:63]\n\tbuffer_load_dwordx4 a[52:55], v8, s[8:11], s68 offen offset:2048 sc1\n\t" \
    "s_add_u32 s68, s44, 921600\n\tv_mfma_f32_16x16x32_bf16 v[68:71], v[36:39], v[188:191], v[68:71]\n\ts_waitcnt vmcnt(46) lgkmcnt(8)\n\tv_mfma_f32_16x16x32_bf16 v[120:123], v[40:43], a[56:59], v[120:123]\n\tds_read_b64_tr_b16 v[28:29], v15 offset:36864\n\tds_read_b64_tr_b16 v[30:31], v15 offset:37376\n\tv_mfma_f32_16x16x32_bf16 v[128:131], v[40:43], a[60:63], v[128:131]\n\tbuffer_load_dwordx4 a[128:131], v8, s[8:11], s68 offen offset:0 sc1\n\t" \
    "v_mfma_f32_16x16x32_bf16 v[136:139], v[40:43], a[64:67], v[136:139]\n\ts_waitcnt lgkmcnt(8)\n\tv_mfma_f32_16x16x32_bf16 v[124:127], v[44:47], a[56:59], v[124:127]\n\tds_read_b64_tr_b16 v[32:33], v237 offset:36864\n\tds_read_b64_tr_b16 v[34:35], v237 offset:37376\n\tv_mfma_f32_16x16x32_bf16 v[132:135], v[44:47], a[60:63], v[132:135]\n\tv_mfma_f32_16x16x32_bf16 v[140:143], v[44:47], a[64:67], v[140:143]\n\ts_barrier\n\t" \
    "s_waitcnt lgkmcnt(8)\n\tv_mfma_f32_16x16x32_bf16 v[144:147], v[16:19], a[56:59], v[144:147]\n\tds_read_b64_tr_b16 v[36:37], v238 offset:36864\n\tds_read_b64_tr_b16 v[38:39], v238 offset:37376\n\tv_mfma_f32_16x16x32_bf16 v[152:155], v[16:19], a[60:63], v[152:155]\n\tbuffer_load_dwordx4 a[132:135], v8, s[8:11], s68 offen offset:1024 sc1\n\tv_mfma_f32_16x16x32_bf16 v[160:163], v[16:19], a[64:67], v[160:163]\n\ts_waitcnt lgkmcnt(8)\n\t" \
    "v_mfma_f32_16x16x32_bf16 v[148:151], v[20:23], a[56:59], v[148:151]\n\tds_read_b64_tr_b16 v[40:41], v14 offset:40960\n\tds_read_b64_tr_b16 v[42:43], v14 offset:41472\n\tv_mfma_f32_16x16x32_bf16 v[156:159], v[20:23], a[60:63], v[156:159]\n\tbuffer_load_dwordx4 a[136:139], v8, s[8:11], s68 offen offset:2048 sc1\n\ts_add_u32 s68, s44, 958464\n\tv_mfma_f32_16x16x32_bf16 v[164:167], v[20:23], a[64:67], v[164:167]\n\ts_waitcnt vmcnt(46) lgkmcnt(8)\n\t" \
    "v_mfma_f32_16x16x32_bf16 v[120:123], v[24:27], v[204:207], v[120:123]\n\tds_read_b64_tr_b16 v[44:45], v15 offset:40960\n\tds_read_b64_tr_b16 v[46:47], v15 offset:41472\n\tv_mfma_f32_16x16x32_bf16 v[128:131], v[24:27], v[208:211], v[128:131]\n\tbuffer_load_dwordx4 a[140:143], v8, s[8:11], s68 offen offset:0 sc1\n\tv_mfma_f32_16x16x32_bf16 v[136:139], v[24:27], v[212:215], v[136:139]\n\ts_waitcnt lgkmcnt(8)\n\tv_mfma_f32_16x16x32_bf16 v[124:127], v[28:31], v[204:207], v[124:127]\n\t" \
    "ds_read_b64_tr_b16 v[16:17], v237 offset:40960\n\tds_read_b64_tr_b16 v[18:19], v237 offset:41472\n\tv_mfma_f32_16x16x32_bf16 v[132:135], v[28:31], v[208:211], v[132:135]\n\tbuffer_load_dwordx4 a[144:147], v8, s[8:11], s68 offen offset:1024 sc1\n\tv_mfma_f32_16x16x32_bf16 v[140:143], v[28:31], v[212:215], v[140:143]\n\ts_waitcnt lgkmcnt(8)\n\tv_mfma_f32_16x16x32_bf16 v[144:147], v[32:35], v[204:207], v[144:147]\n\tds_read_b64_tr_b16 v[20:21], v238 offset:40960\n\t" \
    "ds_read_b64_tr_b16 v[22:23], v238 offset:41472\n\tv_mfma_f32_16x16x32_bf16 v[152:155], v[32:35], v[208:211], v[152:155]\n\tbuffer_load_dwordx4 a[148:151], v8, s[8:11], s68 offen offset:2048 sc1\n\ts_mov_b32 s69, 8847360\n\ts_add_u32 m0, s54, 40960\n\tv_mfma_f32_16x16x32_bf16 v[160:163], v[32:35], v[212:215], v[160:163]\n\ts_waitcnt lgkmcnt(8)\n\tv_mfma_f32_16x16x32_bf16 v[148:151], v[36:39], v[204:207], v[148:151]\n\t" \
    "ds_read_b64_tr_b16 v[24:25], v14 offset:45056\n\tds_read_b64_tr_b16 v[26:27], v14 offset:45568\n\tv_mfma_f32_16x16x32_bf16 v[156:159], v[36:39], v[208:211], v[156:159]\n\tv_mfma_f32_16x16x32_bf16 v[164:167], v[36:39], v[212:215], v[164:167]\n\ts_waitcnt vmcnt(46) lgkmcnt(8)\n\tv_mfma_f32_16x16x32_bf16 v[120:123], v[40:43], a[68:71], v[120:123]\n\tds_read_b64_tr_b16 v[28:29], v15 offset:45056\n\tds_read_b64_tr_b16 v[30:31], v15 offset:45568\n\t" \
    "v_mfma_f32_16x16x32_bf16 v[128:131], v[40:43], a[72:75], v[128:131]\n\tbuffer_load_dwordx4 v10, s[12:15], s69 offen lds\n\ts_mov_b32 s69, 9142272\n\ts_add_u32 m0, s54, 45056\n\tv_mfma_f32_16x16x32_bf16 v[136:139], v[40:43], a[76:79], v[136:139]\n\ts_waitcnt lgkmcnt(8)\n\tv_mfma_f32_16x16x32_bf16 v[124:127], v[44:47], a[68:71], v[124:127]\n\tds_read_b64_tr_b16 v[32:33], v237 offset:45056\n\t" \
    "ds_read_b64_tr_b16 v[34:35], v237 offset:45568\n\tv_mfma_f32_16x16x32_bf16 v[132:135], v[44:47], a[72:75], v[132:135]\n\tbuffer_load_dwordx4 v10, s[12:15], s69 offen lds\n\ts_cmp_ge_u32 s43, 9\n\ts_cselect_b32 s69, s70, s43\n\ts_mul_i32 s69, s69, 294912\n\tv_mfma_f32_16x16x32_bf16 v[140:143], v[44:47], a[76:79], v[140:143]\n\ts_add_u32 s69, s69, 3538944\n\t" \
    "s_add_u32 m0, s54, 49152\n\ts_waitcnt lgkmcnt(8)\n\tv_mfma_f32_16x16x32_bf16 v[144:147], v[16:19], a[68:71], v[144:147]\n\tds_read_b64_tr_b16 v[36:37], v238 offset:45056\n\tds_read_b64_tr_b16 v[38:39], v238 offset:45568\n\tv_mfma_f32_16x16x32_bf16 v[152:155], v[16:19], a[72:75], v[152:155]\n\tbuffer_load_dwordx4 v11, s[12:15], s69 offen lds\n\ts_cmp_ge_u32 s43, 8\n\t" \
    "s_cselect_b32 s69, s70, s43\n\ts_mul_i32 s69, s69, 294912\n\tv_mfma_f32_16x16x32_bf16 v[160:163], v[16:19], a[76:79], v[160:163]\n\ts_add_u32 s69, s69, 3833856\n\ts_add_u32 m0, s54, 53248\n\ts_waitcnt lgkmcnt(8)\n\tv_mfma_f32_16x16x32_bf16 v[148:151], v[20:23], a[68:71], v[148:151]\n\tds_read_b64_tr_b16 v[40:41], v14 offset:49152\n\t" \
    "ds_read_b64_tr_b16 v[42:43], v14 offset:49664\n\tv_mfma_f32_16x16x32_bf16 v[156:159], v[20:23], a[72:75], v[156:159]\n\tv_mfma_f32_16x16x32_bf16 v[164:167], v[20:23], a[76:79], v[164:167]\n\ts_waitcnt vmcnt(46) lgkmcnt(8)\n\tv_mfma_f32_16x16x32_bf16 v[120:123], v[24:27], v[216:219], v[120:123]\n\tds_read_b64_tr_b16 v[44:45], v15 offset:49152\n\tds_read_b64_tr_b16 v[46:47], v15 offset:49664\n\tv_mfma_f32_16x16x32_bf16 v[128:131], v[24:27], a[0:3], v[128:131]\n\t" \
    "buffer_load_dwordx4 v11, s[12:15], s69 offen lds\n\ts_cmp_ge_u32 s43, 7\n\ts_cselect_b32 s69, s70, s43\n\ts_mul_i32 s69, s69, 294912\n\tv_mfma_f32_16x16x32_bf16 v[136:139], v[24:27], a[4:7], v[136:139]\n\ts_add_u32 s69, s69, 4128768\n\ts_add_u32 m0, s54, 57344\n\ts_waitcnt lgkmcnt(8)\n\t" \
    "v_mfma_f32_16x16x32_bf16 v[124:127], v[28:31], v[216:219], v[124:127]\n\tds_read_b64_tr_b16 v[16:17], v237 offset:49152\n\tds_read_b64_tr_b16 v[18:19], v237 offset:49664\n\tv_mfma_f32_16x16x32_bf16 v[132:135], v[28:31], a[0:3], v[132:135]\n\tbuffer_load_dwordx4 v11, s[12:15], s69 offen lds\n\ts_cmp_ge_u32 s43, 6\n\ts_cselect_b32 s69, s70, s43\n\ts_mul_i32 s69, s69, 294912\n\t" \
    "v_mfma_f32_16x16x32_bf16 v[140:143], v[28:31], a[4:7], v[140:143]\n\ts_add_u32 s69, s69, 4423680\n\ts_add_u32 m0, s54, 61440\n\ts_waitcnt lgkmcnt(8)\n\tv_mfma_f32_16x16x32_bf16 v[144:147], v[32:35], v[216:219], v[144:147]\n\tds_read_b64_tr_b16 v[20:21], v238 offset:49152\n\tds_read_b64_tr_b16 v[22:23], v238 offset:49664\n\tv_mfma_f32_16x16x32_bf16 v[152:155], v[32:35], a[0:3], v[152:155]\n\t" \
    "buffer_load_dwordx4 v11, s[12:15], s69 offen lds\n\ts_cmp_ge_u32 s43, 5\n\ts_cselect_b32 s69, s70, s43\n\ts_mul_i32 s69, s69, 294912\n\tv_mfma_f32_16x16x32_bf16 v[160:163], v[32:35], a[4:7], v[160:163]\n\ts_add_u32 s69, s69, 4718592\n\ts_add_u32 m0, s54, 65536\n\ts_waitcnt lgkmcnt(8)\n\t" \
    "v_mfma_f32_16x16x32_bf16 v[148:151], v[36:39], v[216:219], v[148:151]\n\tds_read_b64_tr_b16 v[24:25], v14 offset:53248\n\tds_read_b64_tr_b16 v[26:27], v14 offset:53760\n\tv_mfma_f32_16x16x32_bf16 v[156:159], v[36:39], a[0:3], v[156:159]\n\tv_mfma_f32_16x16x32_bf16 v[164:167], v[36:39], a[4:7], v[164:167]\n\ts_waitcnt vmcnt(46) lgkmcnt(8)\n\tv_mfma_f32_16x16x32_bf16 v[120:123], v[40:43], a[80:83], v[120:123]\n\tds_read_b64_tr_b16 v[28:29], v15 offset:53248\n\t" \
    "ds_read_b64_tr_b16 v[30:31], v15 offset:53760\n\tv_mfma_f32_16x16x32_bf16 v[128:131], v[40:43], a[84:87], v[128:131]\n\tbuffer_load_dwordx4 v11, s[12:15], s69 offen lds\n\ts_cmp_ge_u32 s43, 4\n\ts_cselect_b32 s69, s70, s43\n\ts_mul_i32 s69, s69, 294912\n\tv_mfma_f32_16x16x32_bf16 v[136:139], v[40:43], a[88:91], v[136:139]\n\ts_add_u32 s69, s69, 5013504\n\t" \
    "s_add_u32 m0, s54, 69632\n\ts_waitcnt lgkmcnt(8)\n\tv_mfma_f32_16x16x32_bf16 v[124:127], v[44:47], a[80:83], v[124:127]\n\tds_read_b64_tr_b16 v[32:33], v237 offset:53248\n\tds_read_b64_tr_b16 v[34:35], v237 offset:53760\n\tv_mfma_f32_16x16x32_bf16 v[132:135], v[44:47], a[84:87], v[132:135]\n\tbuffer_load_dwordx4 v11, s[12:15], s69 offen lds\n\ts_add_u32 s68, s44, 995328\n\t" \
    "v_mfma_f32_16x16x32_bf16 v[140:143], v[44:47], a[88:91], v[140:143]\n\ts_waitcnt vmcnt(46)\n\ts_barrier\n\ts_waitcnt lgkmcnt(8)\n\tv_mfma_f32_16x16x32_bf16 v[144:147], v[16:19], a[80:83], v[144:147]\n\tds_read_b64_tr_b16 v[36:37], v238 offset:53248\n\tds_read_b64_tr_b16 v[38:39], v238 offset:53760\n\tv_mfma_f32_16x16x32_bf16 v[152:155], v[16:19], a[84:87], v[152:155]\n\t" \
    "buffer_load_dwordx4 a[152:155], v8, s[8:11], s68 offen offset:0 sc1\n\tv_mfma_f32_16x16x32_bf16 v[160:163], v[16:19], a[88:91], v[160:163]\n\ts_waitcnt lgkmcnt(8)\n\tv_mfma_f32_16x16x32_bf16 v[148:151], v[20:23], a[80:83], v[148:151]\n\tds_read_b64_tr_b16 v[40:41], v14 offset:57344\n\tds_read_b64_tr_b16 v[42:43], v14 offset:57856\n\tv_mfma_f32_16x16x32_bf16 v[156:159], v[20:23], a[84:87], v[156:159]\n\tbuffer_load_dwordx4 a[156:159], v8, s[8:11], s68 offen offset:1024 sc1\n\t" \
    "v_mfma_f32_16x16x32_bf16 v[164:167], v[20:23], a[88:91], v[164:167]\n\ts_waitcnt vmcnt(45) lgkmcnt(8)\n\tv_mfma_f32_16x16x32_bf16 v[120:123], v[24:27], a[8:11], v[120:123]\n\tds_read_b64_tr_b16 v[44:45], v15 offset:57344\n\tds_read_b64_tr_b16 v[46:47], v15 offset:57856\n\tv_mfma_f32_16x16x32_bf16 v[128:131], v[24:27], a[12:15], v[128:131]\n\tbuffer_load_dwordx4 a[160:163], v8, s[8:11], s68 offen offset:2048 sc1\n\ts_add_u32 s68, s44, 1032192\n\t" \
    "v_mfma_f32_16x16x32_bf16 v[136:139], v[24:27], a[16:19], v[136:139]\n\ts_waitcnt lgkmcnt(8)\n\tv_mfma_f32_16x16x32_bf16 v[124:127], v[28:31], a[8:11], v[124:127]\n\tds_read_b64_tr_b16 v[16:17], v237 offset:57344\n\tds_read_b64_tr_b16 v[18:19], v237 offset:57856\n\tv_mfma_f32_16x16x32_bf16 v[132:135], v[28:31], a[12:15], v[132:135]\n\tbuffer_load_dwordx4 a[164:167], v8, s[8:11], s68 offen offset:0 sc1\n\tv_mfma_f32_16x16x32_bf16 v[140:143], v[28:31], a[16:19], v[140:143]\n\t" \
    "s_waitcnt lgkmcnt(8)\n\tv_mfma_f32_16x16x32_bf16 v[144:147], v[32:35], a[8:11], v[144:147]\n\tds_read_b64_tr_b16 v[20:21], v238 offset:57344\n\tds_read_b64_tr_b16 v[22:23], v238 offset:57856\n\tv_mfma_f32_16x16x32_bf16 v[152:155], v[32:35], a[12:15], v[152:155]\n\tv_mfma_f32_16x16x32_bf16 v[160:163], v[32:35], a[16:19], v[160:163]\n\ts_waitcnt lgkmcnt(8)\n\tv_mfma_f32_16x16x32_bf16 v[148:151], v[36:39], a[8:11], v[148:151]\n\t" \
    "ds_read_b64_tr_b16 v[24:25], v14 offset:61440\n\tds_read_b64_tr_b16 v[26:27], v14 offset:61952\n\tv_mfma_f32_16x16x32_bf16 v[156:159], v[36:39], a[12:15], v[156:159]\n\tbuffer_load_dwordx4 a[168:171], v8, s[8:11], s68 offen offset:1024 sc1\n\tv_mfma_f32_16x16x32_bf16 v[164:167], v[36:39], a[16:19], v[164:167]\n\ts_waitcnt vmcnt(45) lgkmcnt(8)\n\tv_mfma_f32_16x16x32_bf16 v[120:123], v[40:43], a[92:95], v[120:123]\n\tds_read_b64_tr_b16 v[28:29], v15 offset:61440\n\t" \
    "ds_read_b64_tr_b16 v[30:31], v15 offset:61952\n\tv_mfma_f32_16x16x32_bf16 v[128:131], v[40:43], a[96:99], v[128:131]\n\tbuffer_load_dwordx4 a[172:175], v8, s[8:11], s68 offen offset:2048 sc1\n\ts_add_u32 s68, s44, 1069056\n\tv_mfma_f32_16x16x32_bf16 v[136:139], v[40:43], a[100:103], v[136:139]\n\ts_waitcnt lgkmcnt(8)\n\tv_mfma_f32_16x16x32_bf16 v[124:127], v[44:47], a[92:95], v[124:127]\n\tds_read_b64_tr_b16 v[32:33], v237 offset:61440\n\t" \
    "ds_read_b64_tr_b16 v[34:35], v237 offset:61952\n\tv_mfma_f32_16x16x32_bf16 v[132:135], v[44:47], a[96:99], v[132:135]\n\tv_mfma_f32_16x16x32_bf16 v[140:143], v[44:47], a[100:103], v[140:143]\n\ts_waitcnt lgkmcnt(8)\n\tv_mfma_f32_16x16x32_bf16 v[144:147], v[16:19], a[92:95], v[144:147]\n\tds_read_b64_tr_b16 v[36:37], v238 offset:61440\n\tds_read_b64_tr_b16 v[38:39], v238 offset:61952\n\tv_mfma_f32_16x16x32_bf16 v[152:155], v[16:19], a[96:99], v[152:155]\n\t" \
    "buffer_load_dwordx4 a[176:179], v8, s[8:11], s68 offen offset:0 sc1\n\tv_mfma_f32_16x16x32_bf16 v[160:163], v[16:19], a[100:103], v[160:163]\n\ts_waitcnt lgkmcnt(8)\n\tv_mfma_f32_16x16x32_bf16 v[148:151], v[20:23], a[92:95], v[148:151]\n\tds_read_b64_tr_b16 v[40:41], v0 offset:0\n\tds_read_b64_tr_b16 v[42:43], v0 offset:512\n\tv_mfma_f32_16x16x32_bf16 v[156:159], v[20:23], a[96:99], v[156:159]\n\tbuffer_load_dwordx4 a[180:183], v8, s[8:11], s68 offen offset:1024 sc1\n\t" \
    "v_mfma_f32_16x16x32_bf16 v[164:167], v[20:23], a[100:103], v[164:167]\n\ts_waitcnt vmcnt(45) lgkmcnt(8)\n\tv_mfma_f32_16x16x32_bf16 v[120:123], v[24:27], a[20:23], v[120:123]\n\tds_read_b64_tr_b16 v[44:45], v1 offset:0\n\tds_read_b64_tr_b16 v[46:47], v1 offset:512\n\tv_mfma_f32_16x16x32_bf16 v[128:131], v[24:27], a[24:27], v[128:131]\n\tbuffer_load_dwordx4 a[184:187], v8, s[8:11], s68 offen offset:2048 sc1\n\ts_add_u32 s68, s44, 1105920\n\t" \
    "v_mfma_f32_16x16x32_bf16 v[136:139], v[24:27], a[28:31], v[136:139]\n\ts_waitcnt lgkmcnt(8)\n\tv_mfma_f32_16x16x32_bf16 v[124:127], v[28:31], a[20:23], v[124:127]\n\tds_read_b64_tr_b16 v[16:17], v2 offset:0\n\tds_read_b64_tr_b16 v[18:19], v2 offset:512\n\tv_mfma_f32_16x16x32_bf16 v[132:135], v[28:31], a[24:27], v[132:135]\n\tv_mfma_f32_16x16x32_bf16 v[140:143], v[28:31], a[28:31], v[140:143]\n\ts_waitcnt lgkmcnt(8)\n\t" \
    "v_mfma_f32_16x16x32_bf16 v[144:147], v[32:35], a[20:23], v[144:147]\n\tds_read_b64_tr_b16 v[20:21], v3 offset:0\n\tds_read_b64_tr_b16 v[22:23], v3 offset:512\n\tv_mfma_f32_16x16x32_bf16 v[152:155], v[32:35], a[24:27], v[152:155]\n\tbuffer_load_dwordx4 a[188:191], v8, s[8:11], s68 offen offset:0 sc1\n\tv_mfma_f32_16x16x32_bf16 v[160:163], v[32:35], a[28:31], v[160:163]\n\ts_waitcnt lgkmcnt(8)\n\tv_mfma_f32_16x16x32_bf16 v[148:151], v[36:39], a[20:23], v[148:151]\n\t" \
    "ds_read_b64_tr_b16 v[24:25], v0 offset:4096\n\tds_read_b64_tr_b16 v[26:27], v0 offset:4608\n\tv_mfma_f32_16x16x32_bf16 v[156:159], v[36:39], a[24:27], v[156:159]\n\tbuffer_load_dwordx4 a[192:195], v8, s[8:11], s68 offen offset:1024 sc1\n\tv_mfma_f32_16x16x32_bf16 v[164:167], v[36:39], a[28:31], v[164:167]\n\ts_waitcnt vmcnt(45) lgkmcnt(8)\n\tv_mfma_f32_16x16x32_bf16 v[120:123], v[40:43], a[104:107], v[120:123]\n\tds_read_b64_tr_b16 v[28:29], v1 offset:4096\n\t" \
    "ds_read_b64_tr_b16 v[30:31], v1 offset:4608\n\tv_mfma_f32_16x16x32_bf16 v[128:131], v[40:43], a[108:111], v[128:131]\n\tbuffer_load_dwordx4 a[196:199], v8, s[8:11], s68 offen offset:2048 sc1\n\ts_add_u32 s68, s44, 1142784\n\tv_mfma_f32_16x16x32_bf16 v[136:139], v[40:43], a[112:115], v[136:139]\n\ts_waitcnt lgkmcnt(8)\n\tv_mfma_f32_16x16x32_bf16 v[124:127], v[44:47], a[104:107], v[124:127]\n\tds_read_b64_tr_b16 v[32:33], v2 offset:4096\n\t" \
    "ds_read_b64_tr_b16 v[34:35], v2 offset:4608\n\tv_mfma_f32_16x16x32_bf16 v[132:135], v[44:47], a[108:111], v[132:135]\n\tv_mfma_f32_16x16x32_bf16 v[140:143], v[44:47], a[112:115], v[140:143]\n\ts_waitcnt vmcnt(38)\n\ts_barrier\n\ts_waitcnt lgkmcnt(8)\n\tv_mfma_f32_16x16x32_bf16 v[144:147], v[16:19], a[104:107], v[144:147]\n\tds_read_b64_tr_b16 v[36:37], v3 offset:4096\n\t" \
    "ds_read_b64_tr_b16 v[38:39], v3 offset:4608\n\tv_mfma_f32_16x16x32_bf16 v[152:155], v[16:19], a[108:111], v[152:155]\n\tbuffer_load_dwordx4 a[200:203], v8, s[8:11], s68 offen offset:0 sc1\n\tv_mfma_f32_16x16x32_bf16 v[160:163], v[16:19], a[112:115], v[160:163]\n\ts_waitcnt lgkmcnt(8)\n\tv_mfma_f32_16x16x32_bf16 v[148:151], v[20:23], a[104:107], v[148:151]\n\tds_read_b64_tr_b16 v[40:41], v0 offset:8192\n\tds_read_b64_tr_b16 v[42:43], v0 offset:8704\n\t" \
    "v_mfma_f32_16x16x32_bf16 v[156:159], v[20:23], a[108:111], v[156:159]\n\tbuffer_load_dwordx4 a[204:207], v8, s[8:11], s68 offen offset:1024 sc1\n\tv_mfma_f32_16x16x32_bf16 v[164:167], v[20:23], a[112:115], v[164:167]\n\ts_waitcnt vmcnt(37) lgkmcnt(8)\n\tv_mfma_f32_16x16x32_bf16 v[120:123], v[24:27], a[32:35], v[120:123]\n\tds_read_b64_tr_b16 v[44:45], v1 offset:8192\n\tds_read_b64_tr_b16 v[46:47], v1 offset:8704\n\tv_mfma_f32_16x16x32_bf16 v[128:131], v[24:27], a[36:39], v[128:131]\n\t" \
    "buffer_load_dwordx4 a[208:211], v8, s[8:11], s68 offen offset:2048 sc1\n\ts_cmp_ge_u32 s43, 3\n\ts_cselect_b32 s69, s70, s43\n\ts_mul_i32 s69, s69, 294912\n\tv_mfma_f32_16x16x32_bf16 v[136:139], v[24:27], a[40:43], v[136:139]\n\ts_add_u32 s69, s69, 5308416\n\ts_add_u32 m0, s54, 73728\n\ts_waitcnt lgkmcnt(8)\n\t" \
    "v_mfma_f32_16x16x32_bf16 v[124:127], v[28:31], a[32:35], v[124:127]\n\tds_read_b64_tr_b16 v[16:17], v0 offset:12288\n\tds_read_b64_tr_b16 v[18:19], v0 offset:12800\n\tv_mfma_f32_16x16x32_bf16 v[132:135], v[28:31], a[36:39], v[132:135]\n\tbuffer_load_dwordx4 v11, s[12:15], s69 offen lds\n\ts_cmp_ge_u32 s43, 2\n\ts_cselect_b32 s69, s70, s43\n\ts_mul_i32 s69, s69, 294912\n\t" \
    "v_mfma_f32_16x16x32_bf16 v[140:143], v[28:31], a[40:43], v[140:143]\n\ts_add_u32 s69, s69, 5603328\n\ts_add_u32 m0, s54, 77824\n\ts_waitcnt lgkmcnt(8)\n\tv_mfma_f32_16x16x32_bf16 v[144:147], v[32:35], a[32:35], v[144:147]\n\tds_read_b64_tr_b16 v[20:21], v1 offset:12288\n\tds_read_b64_tr_b16 v[22:23], v1 offset:12800\n\tv_mfma_f32_16x16x32_bf16 v[152:155], v[32:35], a[36:39], v[152:155]\n\t" \
    "buffer_load_dwordx4 v11, s[12:15], s69 offen lds\n\ts_cmp_ge_u32 s43, 1\n\ts_cselect_b32 s69, s70, s43\n\ts_mul_i32 s69, s69, 294912\n\tv_mfma_f32_16x16x32_bf16 v[160:163], v[32:35], a[40:43], v[160:163]\n\ts_add_u32 s69, s69, 5898240\n\ts_add_u32 m0, s54, 81920\n\ts_waitcnt lgkmcnt(8)\n\t" \
    "v_mfma_f32_16x16x32_bf16 v[148:151], v[36:39], a[32:35], v[148:151]\n\tds_read_b64_tr_b16 v[24:25], v0 offset:16384\n\tds_read_b64_tr_b16 v[26:27], v0 offset:16896\n\tv_mfma_f32_16x16x32_bf16 v[156:159], v[36:39], a[36:39], v[156:159]\n\tv_mfma_f32_16x16x32_bf16 v[164:167], v[36:39], a[40:43], v[164:167]\n\ts_waitcnt vmcnt(37) lgkmcnt(8)\n\tv_mfma_f32_16x16x32_bf16 v[120:123], v[40:43], v[168:171], v[120:123]\n\tds_read_b64_tr_b16 v[28:29], v1 offset:16384\n\t" \
    "ds_read_b64_tr_b16 v[30:31], v1 offset:16896\n\tv_mfma_f32_16x16x32_bf16 v[128:131], v[40:43], v[172:175], v[128:131]\n\tbuffer_load_dwordx4 v11, s[12:15], s69 offen lds\n\ts_mov_b32 s69, 6193152\n\ts_add_u32 m0, s54, 86016\n\tv_mfma_f32_16x16x32_bf16 v[136:139], v[40:43], v[176:179], v[136:139]\n\ts_waitcnt lgkmcnt(8)\n\tv_mfma_f32_16x16x32_bf16 v[124:127], v[44:47], v[168:171], v[124:127]\n\t" \
    "ds_read_b64_tr_b16 v[32:33], v0 offset:20480\n\tds_read_b64_tr_b16 v[34:35], v0 offset:20992\n\tv_mfma_f32_16x16x32_bf16 v[132:135], v[44:47], v[172:175], v[132:135]\n\tbuffer_load_dwordx4 v11, s[12:15], s69 offen lds\n\ts_mov_b32 s69, 6488064\n\ts_add_u32 m0, s54, 90112\n\tv_mfma_f32_16x16x32_bf16 v[140:143], v[44:47], v[176:179], v[140:143]\n\ts_waitcnt vmcnt(36) lgkmcnt(8)\n\t" \
    "v_mfma_f32_16x16x32_bf16 v[120:123], v[16:19], a[116:119], v[120:123]\n\tds_read_b64_tr_b16 v[36:37], v1 offset:20480\n\tds_read_b64_tr_b16 v[38:39], v1 offset:20992\n\tv_mfma_f32_16x16x32_bf16 v[128:131], v[16:19], a[120:123], v[128:131]\n\tbuffer_load_dwordx4 v11, s[12:15], s69 offen lds\n\ts_mov_b32 s69, 6782976\n\ts_add_u32 m0, s54, 98304\n\tv_mfma_f32_16x16x32_bf16 v[136:139], v[16:19], a[124:127], v[136:139]\n\t" \
    "s_waitcnt lgkmcnt(8)\n\tv_mfma_f32_16x16x32_bf16 v[124:127], v[20:23], a[116:119], v[124:127]\n\tds_read_b64_tr_b16 v[40:41], v0 offset:24576\n\tds_read_b64_tr_b16 v[42:43], v0 offset:25088\n\tv_mfma_f32_16x16x32_bf16 v[132:135], v[20:23], a[120:123], v[132:135]\n\tv_mfma_f32_16x16x32_bf16 v[140:143], v[20:23], a[124:127], v[140:143]\n\ts_waitcnt vmcnt(34) lgkmcnt(8)\n\tv_mfma_f32_16x16x32_bf16 v[120:123], v[24:27], a[44:47], v[120:123]\n\t" \
    "ds_read_b64_tr_b16 v[44:45], v1 offset:24576\n\tds_read_b64_tr_b16 v[46:47], v1 offset:25088\n\tv_mfma_f32_16x16x32_bf16 v[128:131], v[24:27], a[48:51], v[128:131]\n\tbuffer_load_dwordx4 v11, s[12:15], s69 offen lds\n\ts_mov_b32 s69, 7077888\n\ts_add_u32 m0, s54, 102400\n\tv_mfma_f32_16x16x32_bf16 v[136:139], v[24:27], a[52:55], v[136:139]\n\ts_waitcnt lgkmcnt(8)\n\t" \
    "v_mfma_f32_16x16x32_bf16 v[124:127], v[28:31], a[44:47], v[124:127]\n\tds_read_b64_tr_b16 v[16:17], v0 offset:28672\n\tds_read_b64_tr_b16 v[18:19], v0 offset:29184\n\tv_mfma_f32_16x16x32_bf16 v[132:135], v[28:31], a[48:51], v[132:135]\n\tbuffer_load_dwordx4 v11, s[12:15], s69 offen lds\n\ts_mov_b32 s69, 7372800\n\ts_add_u32 m0, s54, 106496\n\tv_mfma_f32_16x16x32_bf16 v[140:143], v[28:31], a[52:55], v[140:143]\n\t" \
    "s_waitcnt vmcnt(33) lgkmcnt(8)\n\tv_mfma_f32_16x16x32_bf16 v[120:123], v[32:35], a[128:131], v[120:123]\n\tds_read_b64_tr_b16 v[20:21], v1 offset:28672\n\tds_read_b64_tr_b16 v[22:23], v1 offset:29184\n\tv_mfma_f32_16x16x32_bf16 v[128:131], v[32:35], a[132:135], v[128:131]\n\tv_mfma_f32_16x16x32_bf16 v[136:139], v[32:35], a[136:139], v[136:139]\n\ts_waitcnt lgkmcnt(8)\n\tv_mfma_f32_16x16x32_bf16 v[124:127], v[36:39], a[128:131], v[124:127]\n\t" \
    "ds_read_b64_tr_b16 v[24:25], v0 offset:32768\n\tds_read_b64_tr_b16 v[26:27], v0 offset:33280\n\tv_mfma_f32_16x16x32_bf16 v[132:135], v[36:39], a[132:135], v[132:135]\n\tbuffer_load_dwordx4 v11, s[12:15], s69 offen lds\n\ts_mov_b32 s69, 7667712\n\ts_add_u32 m0, s54, 110592\n\tv_mfma_f32_16x16x32_bf16 v[140:143], v[36:39], a[136:139], v[140:143]\n\ts_waitcnt vmcnt(31) lgkmcnt(8)\n\t" \
    "v_mfma_f32_16x16x32_bf16 v[120:123], v[40:43], a[140:143], v[120:123]\n\tds_read_b64_tr_b16 v[28:29], v1 offset:32768\n\tds_read_b64_tr_b16 v[30:31], v1 offset:33280\n\tv_mfma_f32_16x16x32_bf16 v[128:131], v[40:43], a[144:147], v[128:131]\n\tbuffer_load_dwordx4 v11, s[12:15], s69 offen lds\n\ts_mov_b32 s69, 7962624\n\ts_add_u32 m0, s54, 114688\n\tv_mfma_f32_16x16x32_bf16 v[136:139], v[40:43], a[148:151], v[136:139]\n\t" \
    "s_waitcnt lgkmcnt(8)\n\tv_mfma_f32_16x16x32_bf16 v[124:127], v[44:47], a[140:143], v[124:127]\n\tds_read_b64_tr_b16 v[32:33], v0 offset:36864\n\tds_read_b64_tr_b16 v[34:35], v0 offset:37376\n\tv_mfma_f32_16x16x32_bf16 v[132:135], v[44:47], a[144:147], v[132:135]\n\tbuffer_load_dwordx4 v11, s[12:15], s69 offen lds\n\ts_mov_b32 s69, 8257536\n\ts_add_u32 m0, s54, 118784\n\t" \
    "v_mfma_f32_16x16x32_bf16 v[140:143], v[44:47], a[148:151], v[140:143]\n\ts_waitcnt vmcnt(25)\n\ts_barrier\n\ts_waitcnt vmcnt(22) lgkmcnt(8)\n\tv_mfma_f32_16x16x32_bf16 v[120:123], v[16:19], a[152:155], v[120:123]\n\tds_read_b64_tr_b16 v[36:37], v1 offset:36864\n\tds_read_b64_tr_b16 v[38:39], v1 offset:37376\n\tv_mfma_f32_16x16x32_bf16 v[128:131], v[16:19], a[156:159], v[128:131]\n\t" \
    "buffer_load_dwordx4 v11, s[12:15], s69 offen lds\n\ts_mov_b32 s69, 8552448\n\ts_add_u32 m0, s54, 122880\n\tv_mfma_f32_16x16x32_bf16 v[136:139], v[16:19], a[160:163], v[136:139]\n\ts_waitcnt lgkmcnt(8)\n\tv_mfma_f32_16x16x32_bf16 v[124:127], v[20:23], a[152:155], v[124:127]\n\tds_read_b64_tr_b16 v[40:41], v0 offset:40960\n\tds_read_b64_tr_b16 v[42:43], v0 offset:41472\n\t" \
    "v_mfma_f32_16x16x32_bf16 v[132:135], v[20:23], a[156:159], v[132:135]\n\tbuffer_load_dwordx4 v11, s[12:15], s69 offen lds\n\ts_mov_b32 s69, 8847360\n\ts_add_u32 m0, s54, 126976\n\tv_mfma_f32_16x16x32_bf16 v[140:143], v[20:23], a[160:163], v[140:143]\n\ts_waitcnt vmcnt(21) lgkmcnt(8)\n\tv_mfma_f32_16x16x32_bf16 v[120:123], v[24:27], a[164:167], v[120:123]\n\tds_read_b64_tr_b16 v[44:45], v1 offset:40960\n\t" \
    "ds_read_b64_tr_b16 v[46:47], v1 offset:41472\n\tv_mfma_f32_16x16x32_bf16 v[128:131], v[24:27], a[168:171], v[128:131]\n\tv_mfma_f32_16x16x32_bf16 v[136:139], v[24:27], a[172:175], v[136:139]\n\ts_waitcnt lgkmcnt(8)\n\tv_mfma_f32_16x16x32_bf16 v[124:127], v[28:31], a[164:167], v[124:127]\n\tds_read_b64_tr_b16 v[16:17], v0 offset:45056\n\tds_read_b64_tr_b16 v[18:19], v0 offset:45568\n\tv_mfma_f32_16x16x32_bf16 v[132:135], v[28:31], a[168:171], v[132:135]\n\t" \
    "buffer_load_dwordx4 v11, s[12:15], s69 offen lds\n\ts_mov_b32 s69, 9142272\n\ts_add_u32 m0, s54, 0\n\tv_mfma_f32_16x16x32_bf16 v[140:143], v[28:31], a[172:175], v[140:143]\n\ts_waitcnt vmcnt(19) lgkmcnt(8)\n\tv_mfma_f32_16x16x32_bf16 v[120:123], v[32:35], a[176:179], v[120:123]\n\tds_read_b64_tr_b16 v[20:21], v1 offset:45056\n\tds_read_b64_tr_b16 v[22:23], v1 offset:45568\n\t" \
    "v_mfma_f32_16x16x32_bf16 v[128:131], v[32:35], a[180:183], v[128:131]\n\tbuffer_load_dwordx4 v11, s[12:15], s69 offen lds\n\tv_mfma_f32_16x16x32_bf16 v[136:139], v[32:35], a[184:187], v[136:139]\n\ts_waitcnt lgkmcnt(8)\n\tv_mfma_f32_16x16x32_bf16 v[124:127], v[36:39], a[176:179], v[124:127]\n\tds_read_b64_tr_b16 v[24:25], v0 offset:49152\n\tds_read_b64_tr_b16 v[26:27], v0 offset:49664\n\tv_mfma_f32_16x16x32_bf16 v[132:135], v[36:39], a[180:183], v[132:135]\n\t" \
    "v_mfma_f32_16x16x32_bf16 v[140:143], v[36:39], a[184:187], v[140:143]\n\ts_waitcnt vmcnt(17) lgkmcnt(8)\n\tv_mfma_f32_16x16x32_bf16 v[120:123], v[40:43], a[188:191], v[120:123]\n\tds_read_b64_tr_b16 v[28:29], v1 offset:49152\n\tds_read_b64_tr_b16 v[30:31], v1 offset:49664\n\tv_mfma_f32_16x16x32_bf16 v[128:131], v[40:43], a[192:195], v[128:131]\n\tv_mfma_f32_16x16x32_bf16 v[136:139], v[40:43], a[196:199], v[136:139]\n\ts_waitcnt lgkmcnt(8)\n\t" \
    "v_mfma_f32_16x16x32_bf16 v[124:127], v[44:47], a[188:191], v[124:127]\n\tds_read_b64_tr_b16 v[32:33], v0 offset:53248\n\tds_read_b64_tr_b16 v[34:35], v0 offset:53760\n\tv_mfma_f32_16x16x32_bf16 v[132:135], v[44:47], a[192:195], v[132:135]\n\tv_mfma_f32_16x16x32_bf16 v[140:143], v[44:47], a[196:199], v[140:143]\n\ts_waitcnt vmcnt(14) lgkmcnt(8)\n\tv_mfma_f32_16x16x32_bf16 v[120:123], v[16:19], a[200:203], v[120:123]\n\tds_read_b64_tr_b16 v[36:37], v1 offset:53248\n\t" \
    "ds_read_b64_tr_b16 v[38:39], v1 offset:53760\n\tv_mfma_f32_16x16x32_bf16 v[128:131], v[16:19], a[204:207], v[128:131]\n\tv_mfma_f32_16x16x32_bf16 v[136:139], v[16:19], a[208:211], v[136:139]\n\ts_waitcnt lgkmcnt(8)\n\tv_mfma_f32_16x16x32_bf16 v[124:127], v[20:23], a[200:203], v[124:127]\n\tds_read_b64_tr_b16 v[40:41], v0 offset:57344\n\tds_read_b64_tr_b16 v[42:43], v0 offset:57856\n\tv_mfma_f32_16x16x32_bf16 v[132:135], v[20:23], a[204:207], v[132:135]\n\t" \
    "v_mfma_f32_16x16x32_bf16 v[140:143], v[20:23], a[208:211], v[140:143]\n\ts_waitcnt lgkmcnt(8)\n\tv_mfma_f32_16x16x32_bf16 v[48:51], v[24:27], a[56:59], v[48:51]\n\tds_read_b64_tr_b16 v[44:45], v1 offset:57344\n\tds_read_b64_tr_b16 v[46:47], v1 offset:57856\n\tv_mfma_f32_16x16x32_bf16 v[56:59], v[24:27], a[60:63], v[56:59]\n\tv_mfma_f32_16x16x32_bf16 v[64:67], v[24:27], a[64:67], v[64:67]\n\ts_add_u32 s68, s52, 0\n\t" \
    "v_cvt_pk_bf16_f32 v120, v120, v121\n\tv_cvt_pk_bf16_f32 v121, v122, v123\n\tv_cvt_pk_bf16_f32 v122, v124, v125\n\tv_cvt_pk_bf16_f32 v123, v126, v127\n\tbuffer_store_dwordx4 v[120:123], v8, s[8:11], s68 offen offset:0 sc1\n\tv_cvt_pk_bf16_f32 v124, v128, v129\n\tv_cvt_pk_bf16_f32 v125, v130, v131\n\tv_cvt_pk_bf16_f32 v126, v132, v133\n\t" \
    "v_cvt_pk_bf16_f32 v127, v134, v135\n\tbuffer_store_dwordx4 v[124:127], v8, s[8:11], s68 offen offset:1024 sc1\n\tv_cvt_pk_bf16_f32 v128, v136, v137\n\tv_cvt_pk_bf16_f32 v129, v138, v139\n\tv_cvt_pk_bf16_f32 v130, v140, v141\n\tv_cvt_pk_bf16_f32 v131, v142, v143\n\tbuffer_store_dwordx4 v[128:131], v8, s[8:11], s68 offen offset:2048 sc1\n\tv_add3_u32 v132, s36, v120, v121\n\t" \
    "v_add3_u32 v133, v122, v123, v124\n\tv_add3_u32 v132, v132, v125, v126\n\tv_add3_u32 v133, v133, v127, v128\n\tv_add3_u32 v132, v132, v129, v130\n\tv_add3_u32 v132, v132, v133, v131\n\tbuffer_store_dword v132, v236, s[8:11], s68 offen offset:3072 sc1\n\ts_waitcnt lgkmcnt(8)\n\tv_mfma_f32_16x16x32_bf16 v[52:55], v[28:31], a[56:59], v[52:55]\n\t" \
    "ds_read_b64_tr_b16 v[16:17], v0 offset:61440\n\tds_read_b64_tr_b16 v[18:19], v0 offset:61952\n\tv_mfma_f32_16x16x32_bf16 v[60:63], v[28:31], a[60:63], v[60:63]\n\tv_mfma_f32_16x16x32_bf16 v[68:71], v[28:31], a[64:67], v[68:71]\n\ts_waitcnt lgkmcnt(8)\n\tv_mfma_f32_16x16x32_bf16 v[48:51], v[32:35], v[204:207], v[48:51]\n\tds_read_b64_tr_b16 v[20:21], v1 offset:61440\n\tds_read_b64_tr_b16 v[22:23], v1 offset:61952\n\t" \
    "v_mfma_f32_16x16x32_bf16 v[56:59], v[32:35], v[208:211], v[56:59]\n\tv_mfma_f32_16x16x32_bf16 v[64:67], v[32:35], v[212:215], v[64:67]\n\ts_waitcnt lgkmcnt(8)\n\tv_mfma_f32_16x16x32_bf16 v[52:55], v[36:39], v[204:207], v[52:55]\n\tds_read_b64_tr_b16 v[24:25], v14 offset:0\n\tds_read_b64_tr_b16 v[26:27], v14 offset:512\n\ts_lshl_b32 s69, s4, 18\n\tv_mfma_f32_16x16x32_bf16 v[60:63], v[36:39], v[208:211], v[60:63]\n\t" \
    "s_add_u32 s69, s69, 0\n\ts_add_u32 m0, s54, 131072\n\tv_mfma_f32_16x16x32_bf16 v[68:71], v[36:39], v[212:215], v[68:71]\n\ts_waitcnt lgkmcnt(8)\n\tv_mfma_f32_16x16x32_bf16 v[48:51], v[40:43], a[68:71], v[48:51]\n\tds_read_b64_tr_b16 v[28:29], v15 offset:0\n\tds_read_b64_tr_b16 v[30:31], v15 offset:512\n\tv_mfma_f32_16x16x32_bf16 v[56:59], v[40:43], a[72:75], v[56:59]\n\t" \
    "buffer_load_dwordx4 v12, s[16:19], s69 offen lds\n\ts_lshl_b32 s69, s4, 18\n\ts_add_u32 s69, s69, 0\n\tv_mfma_f32_16x16x32_bf16 v[64:67], v[40:43], a[76:79], v[64:67]\n\ts_add_u32 m0, s54, 135168\n\ts_waitcnt lgkmcnt(8)\n\tv_mfma_f32_16x16x32_bf16 v[52:55], v[44:47], a[68:71], v[52:55]\n\tds_read_b64_tr_b16 v[32:33], v14 offset:4096\n\t" \
    "ds_read_b64_tr_b16 v[34:35], v14 offset:4608\n\tv_mfma_f32_16x16x32_bf16 v[60:63], v[44:47], a[72:75], v[60:63]\n\tbuffer_load_dwordx4 v13, s[16:19], s69 offen lds\n\ts_lshl_b32 s69, s4, 18\n\ts_add_u32 s69, s69, 131072\n\tv_mfma_f32_16x16x32_bf16 v[68:71], v[44:47], a[76:79], v[68:71]\n\ts_add_u32 m0, s54, 139264\n\ts_waitcnt vmcnt(12)\n\t" \
    "s_barrier\n\ts_waitcnt lgkmcnt(8)\n\tv_mfma_f32_16x16x32_bf16 v[48:51], v[16:19], v[216:219], v[48:51]\n\tds_read_b64_tr_b16 v[36:37], v15 offset:4096\n\tds_read_b64_tr_b16 v[38:39], v15 offset:4608\n\tv_mfma_f32_16x16x32_bf16 v[56:59], v[16:19], a[0:3], v[56:59]\n\tbuffer_load_dwordx4 v12, s[16:19], s69 offen lds\n\ts_lshl_b32 s69, s4, 18\n\t" \
    "s_add_u32 s69, s69, 131072\n\tv_mfma_f32_16x16x32_bf16 v[64:67], v[16:19], a[4:7], v[64:67]\n\ts_add_u32 m0, s54, 143360\n\ts_waitcnt lgkmcnt(8)\n\tv_mfma_f32_16x16x32_bf16 v[52:55], v[20:23], v[216:219], v[52:55]\n\tds_read_b64_tr_b16 v[40:41], v14 offset:8192\n\tds_read_b64_tr_b16 v[42:43], v14 offset:8704\n\tv_mfma_f32_16x16x32_bf16 v[60:63], v[20:23], a[0:3], v[60:63]\n\t" \
    "buffer_load_dwordx4 v13, s[16:19], s69 offen lds\n\tv_mfma_f32_16x16x32_bf16 v[68:71], v[20:23], a[4:7], v[68:71]\n\ts_waitcnt lgkmcnt(8)\n\tv_mfma_f32_16x16x32_bf16 v[48:51], v[24:27], a[80:83], v[48:51]\n\tds_read_b64_tr_b16 v[44:45], v15 offset:8192\n\tds_read_b64_tr_b16 v[46:47], v15 offset:8704\n\tv_mfma_f32_16x16x32_bf16 v[56:59], v[24:27], a[84:87], v[56:59]\n\tv_mfma_f32_16x16x32_bf16 v[64:67], v[24:27], a[88:91], v[64:67]\n\t" \
    "s_waitcnt lgkmcnt(8)\n\tv_mfma_f32_16x16x32_bf16 v[52:55], v[28:31], a[80:83], v[52:55]\n\tds_read_b64_tr_b16 v[16:17], v14 offset:12288\n\tds_read_b64_tr_b16 v[18:19], v14 offset:12800\n\tv_mfma_f32_16x16x32_bf16 v[60:63], v[28:31], a[84:87], v[60:63]\n\tv_mfma_f32_16x16x32_bf16 v[68:71], v[28:31], a[88:91], v[68:71]\n\ts_waitcnt lgkmcnt(8)\n\tv_mfma_f32_16x16x32_bf16 v[48:51], v[32:35], a[8:11], v[48:51]\n\t" \
    "ds_read_b64_tr_b16 v[20:21], v15 offset:12288\n\tds_read_b64_tr_b16 v[22:23], v15 offset:12800\n\tv_mfma_f32_16x16x32_bf16 v[56:59], v[32:35], a[12:15], v[56:59]\n\tv_mfma_f32_16x16x32_bf16 v[64:67], v[32:35], a[16:19], v[64:67]\n\ts_waitcnt lgkmcnt(8)\n\tv_mfma_f32_16x16x32_bf16 v[52:55], v[36:39], a[8:11], v[52:55]\n\tds_read_b64_tr_b16 v[24:25], v14 offset:16384\n\tds_read_b64_tr_b16 v[26:27], v14 offset:16896\n\t" \
    "v_mfma_f32_16x16x32_bf16 v[60:63], v[36:39], a[12:15], v[60:63]\n\tv_mfma_f32_16x16x32_bf16 v[68:71], v[36:39], a[16:19], v[68:71]\n\ts_waitcnt lgkmcnt(8)\n\tv_mfma_f32_16x16x32_bf16 v[48:51], v[40:43], a[92:95], v[48:51]\n\tds_read_b64_tr_b16 v[28:29], v15 offset:16384\n\tds_read_b64_tr_b16 v[30:31], v15 offset:16896\n\tv_mfma_f32_16x16x32_bf16 v[56:59], v[40:43], a[96:99], v[56:59]\n\tv_mfma_f32_16x16x32_bf16 v[64:67], v[40:43], a[100:103], v[64:67]\n\t" \
    "s_waitcnt lgkmcnt(8)\n\tv_mfma_f32_16x16x32_bf16 v[52:55], v[44:47], a[92:95], v[52:55]\n\tds_read_b64_tr_b16 v[32:33], v14 offset:20480\n\tds_read_b64_tr_b16 v[34:35], v14 offset:20992\n\tv_mfma_f32_16x16x32_bf16 v[60:63], v[44:47], a[96:99], v[60:63]\n\tv_mfma_f32_16x16x32_bf16 v[68:71], v[44:47], a[100:103], v[68:71]\n\ts_waitcnt lgkmcnt(8)\n\tv_mfma_f32_16x16x32_bf16 v[48:51], v[16:19], a[20:23], v[48:51]\n\t" \
    "ds_read_b64_tr_b16 v[36:37], v15 offset:20480\n\tds_read_b64_tr_b16 v[38:39], v15 offset:20992\n\tv_mfma_f32_16x16x32_bf16 v[56:59], v[16:19], a[24:27], v[56:59]\n\tv_mfma_f32_16x16x32_bf16 v[64:67], v[16:19], a[28:31], v[64:67]\n\ts_waitcnt lgkmcnt(8)\n\tv_mfma_f32_16x16x32_bf16 v[52:55], v[20:23], a[20:23], v[52:55]\n\tds_read_b64_tr_b16 v[40:41], v14 offset:24576\n\tds_read_b64_tr_b16 v[42:43], v14 offset:25088\n\t" \
    "v_mfma_f32_16x16x32_bf16 v[60:63], v[20:23], a[24:27], v[60:63]\n\tv_mfma_f32_16x16x32_bf16 v[68:71], v[20:23], a[28:31], v[68:71]\n\ts_waitcnt lgkmcnt(8)\n\tv_mfma_f32_16x16x32_bf16 v[48:51], v[24:27], a[104:107], v[48:51]\n\tds_read_b64_tr_b16 v[44:45], v15 offset:24576\n\tds_read_b64_tr_b16 v[46:47], v15 offset:25088\n\tv_mfma_f32_16x16x32_bf16 v[56:59], v[24:27], a[108:111], v[56:59]\n\tv_mfma_f32_16x16x32_bf16 v[64:67], v[24:27], a[112:115], v[64:67]\n\t" \
    "s_waitcnt lgkmcnt(8)\n\tv_mfma_f32_16x16x32_bf16 v[52:55], v[28:31], a[104:107], v[52:55]\n\tds_read_b64_tr_b16 v[16:17], v14 offset:32768\n\tds_read_b64_tr_b16 v[18:19], v14 offset:33280\n\tv_mfma_f32_16x16x32_bf16 v[60:63], v[28:31], a[108:111], v[60:63]\n\tv_mfma_f32_16x16x32_bf16 v[68:71], v[28:31], a[112:115], v[68:71]\n\ts_waitcnt lgkmcnt(8)\n\tv_mfma_f32_16x16x32_bf16 v[48:51], v[32:35], a[32:35], v[48:51]\n\t" \
    "ds_read_b64_tr_b16 v[20:21], v15 offset:32768\n\tds_read_b64_tr_b16 v[22:23], v15 offset:33280\n\ts_add_u32 s68, s72, 0\n\tv_mfma_f32_16x16x32_bf16 v[56:59], v[32:35], a[36:39], v[56:59]\n\tbuffer_load_dwordx4 v[120:123], v8, s[8:11], s68 offen offset:0 sc1\n\tv_mfma_f32_16x16x32_bf16 v[64:67], v[32:35], a[40:43], v[64:67]\n\ts_waitcnt lgkmcnt(8)\n\tv_mfma_f32_16x16x32_bf16 v[52:55], v[36:39], a[32:35], v[52:55]\n\t" \
    "ds_read_b64_tr_b16 v[24:25], v14 offset:36864\n\tds_read_b64_tr_b16 v[26:27], v14 offset:37376\n\tv_mfma_f32_16x16x32_bf16 v[60:63], v[36:39], a[36:39], v[60:63]\n\tbuffer_load_dwordx4 v[124:127], v8, s[8:11], s68 offen offset:1024 sc1\n\tv_mfma_f32_16x16x32_bf16 v[68:71], v[36:39], a[40:43], v[68:71]\n\ts_waitcnt lgkmcnt(8)\n\tv_mfma_f32_16x16x32_bf16 v[48:51], v[40:43], v[168:171], v[48:51]\n\tds_read_b64_tr_b16 v[28:29], v15 offset:36864\n\t" \
    "ds_read_b64_tr_b16 v[30:31], v15 offset:37376\n\tv_mfma_f32_16x16x32_bf16 v[56:59], v[40:43], v[172:175], v[56:59]\n\tbuffer_load_dwordx4 v[128:131], v8, s[8:11], s68 offen offset:2048 sc1\n\tv_mfma_f32_16x16x32_bf16 v[64:67], v[40:43], v[176:179], v[64:67]\n\ts_waitcnt lgkmcnt(8)\n\tv_mfma_f32_16x16x32_bf16 v[52:55], v[44:47], v[168:171], v[52:55]\n\tds_read_b64_tr_b16 v[32:33], v14 offset:40960\n\tds_read_b64_tr_b16 v[34:35], v14 offset:41472\n\t" \
    "v_mfma_f32_16x16x32_bf16 v[60:63], v[44:47], v[172:175], v[60:63]\n\tbuffer_load_dword v132, v236, s[8:11], s68 offen offset:3072 sc1\n\ts_lshl_b32 s65, s41, 2\n\ts_add_u32 s65, s65, s38\n\tv_mfma_f32_16x16x32_bf16 v[68:71], v[44:47], v[176:179], v[68:71]\n\ts_lshr_b32 s65, s65, 1\n\ts_add_u32 s65, s65, 5\n\ts_waitcnt vmcnt(12)\n\t" \
    "s_barrier\n\ts_waitcnt lgkmcnt(8)\n\tv_mfma_f32_16x16x32_bf16 v[48:51], v[16:19], a[116:119], v[48:51]\n\tds_read_b64_tr_b16 v[36:37], v15 offset:40960\n\tds_read_b64_tr_b16 v[38:39], v15 offset:41472\n\ts_cmp_ge_u32 s65, 18\n\ts_cselect_b32 s66, 1120, 0\n\tv_mfma_f32_16x16x32_bf16 v[56:59], v[16:19], a[120:123], v[56:59]\n\t" \
    "s_lshl_b32 s65, s65, 6\n\ts_sub_u32 s65, s65, s66\n\ts_lshl_b32 s66, s58, 4\n\tv_mfma_f32_16x16x32_bf16 v[64:67], v[16:19], a[124:127], v[64:67]\n\ts_add_u32 s65, s65, s66\n\ts_lshl_b32 s65, s65, 12\n\ts_waitcnt lgkmcnt(8)\n\tv_mfma_f32_16x16x32_bf16 v[52:55], v[20:23], a[116:119], v[52:55]\n\t" \
    "ds_read_b64_tr_b16 v[40:41], v14 offset:45056\n\tds_read_b64_tr_b16 v[42:43], v14 offset:45568\n\tv_bfe_u32 v252, v240, 1, 4\n\tv_mfma_f32_16x16x32_bf16 v[60:63], v[20:23], a[120:123], v[60:63]\n\tv_and_b32 v251, 1, v240\n\tv_lshlrev_b32 v252, 12, v252\n\tv_lshl_add_u32 v252, v251, 7, v252\n\tv_mfma_f32_16x16x32_bf16 v[68:71], v[20:23], a[124:127], v[68:71]\n\t" \
    "s_waitcnt lgkmcnt(8)\n\tv_mfma_f32_16x16x32_bf16 v[48:51], v[24:27], a[44:47], v[48:51]\n\tds_read_b64_tr_b16 v[44:45], v15 offset:45056\n\tds_read_b64_tr_b16 v[46:47], v15 offset:45568\n\tv_mfma_f32_16x16x32_bf16 v[56:59], v[24:27], a[48:51], v[56:59]\n\tbuffer_load_dword v253, v252, s[16:19], s65 offen\n\tv_mfma_f32_16x16x32_bf16 v[64:67], v[24:27], a[52:55], v[64:67]\n\ts_waitcnt lgkmcnt(8)\n\t" \
    "v_mfma_f32_16x16x32_bf16 v[52:55], v[28:31], a[44:47], v[52:55]\n\tds_read_b64_tr_b16 v[16:17], v14 offset:49152\n\tds_read_b64_tr_b16 v[18:19], v14 offset:49664\n\tv_mfma_f32_16x16x32_bf16 v[60:63], v[28:31], a[48:51], v[60:63]\n\tv_mfma_f32_16x16x32_bf16 v[68:71], v[28:31], a[52:55], v[68:71]\n\ts_waitcnt lgkmcnt(8)\n\tv_mfma_f32_16x16x32_bf16 v[48:51], v[32:35], a[128:131], v[48:51]\n\tds_read_b64_tr_b16 v[20:21], v15 offset:49152\n\t" \
    "ds_read_b64_tr_b16 v[22:23], v15 offset:49664\n\tv_mfma_f32_16x16x32_bf16 v[56:59], v[32:35], a[132:135], v[56:59]\n\tv_mfma_f32_16x16x32_bf16 v[64:67], v[32:35], a[136:139], v[64:67]\n\ts_waitcnt lgkmcnt(8)\n\tv_mfma_f32_16x16x32_bf16 v[52:55], v[36:39], a[128:131], v[52:55]\n\tds_read_b64_tr_b16 v[24:25], v14 offset:53248\n\tds_read_b64_tr_b16 v[26:27], v14 offset:53760\n\tv_mfma_f32_16x16x32_bf16 v[60:63], v[36:39], a[132:135], v[60:63]\n\t" \
    "v_mfma_f32_16x16x32_bf16 v[68:71], v[36:39], a[136:139], v[68:71]\n\ts_waitcnt lgkmcnt(8)\n\tv_mfma_f32_16x16x32_bf16 v[48:51], v[40:43], a[140:143], v[48:51]\n\tds_read_b64_tr_b16 v[28:29], v15 offset:53248\n\tds_read_b64_tr_b16 v[30:31], v15 offset:53760\n\tv_mfma_f32_16x16x32_bf16 v[56:59], v[40:43], a[144:147], v[56:59]\n\tv_mfma_f32_16x16x32_bf16 v[64:67], v[40:43], a[148:151], v[64:67]\n\ts_waitcnt lgkmcnt(8)\n\t" \
    "v_mfma_f32_16x16x32_bf16 v[52:55], v[44:47], a[140:143], v[52:55]\n\tds_read_b64_tr_b16 v[32:33], v14 offset:57344\n\tds_read_b64_tr_b16 v[34:35], v14 offset:57856\n\tv_mfma_f32_16x16x32_bf16 v[60:63], v[44:47], a[144:147], v[60:63]\n\tv_mfma_f32_16x16x32_bf16 v[68:71], v[44:47], a[148:151], v[68:71]\n\ts_waitcnt lgkmcnt(8)\n\tv_mfma_f32_16x16x32_bf16 v[48:51], v[16:19], a[152:155], v[48:51]\n\tds_read_b64_tr_b16 v[36:37], v15 offset:57344\n\t" \
    "ds_read_b64_tr_b16 v[38:39], v15 offset:57856\n\tv_mfma_f32_16x16x32_bf16 v[56:59], v[16:19], a[156:159], v[56:59]\n\tv_mfma_f32_16x16x32_bf16 v[64:67], v[16:19], a[160:163], v[64:67]\n\ts_waitcnt lgkmcnt(8)\n\tv_mfma_f32_16x16x32_bf16 v[52:55], v[20:23], a[152:155], v[52:55]\n\tds_read_b64_tr_b16 v[40:41], v14 offset:61440\n\tds_read_b64_tr_b16 v[42:43], v14 offset:61952\n\tv_mfma_f32_16x16x32_bf16 v[60:63], v[20:23], a[156:159], v[60:63]\n\t" \
    "v_mfma_f32_16x16x32_bf16 v[68:71], v[20:23], a[160:163], v[68:71]\n\ts_waitcnt lgkmcnt(8)\n\tv_mfma_f32_16x16x32_bf16 v[48:51], v[24:27], a[164:167], v[48:51]\n\tds_read_b64_tr_b16 v[44:45], v15 offset:61440\n\tds_read_b64_tr_b16 v[46:47], v15 offset:61952\n\tv_mfma_f32_16x16x32_bf16 v[56:59], v[24:27], a[168:171], v[56:59]\n\tv_mfma_f32_16x16x32_bf16 v[64:67], v[24:27], a[172:175], v[64:67]\n\ts_waitcnt lgkmcnt(8)\n\t" \
    "v_mfma_f32_16x16x32_bf16 v[52:55], v[28:31], a[164:167], v[52:55]\n\tds_read_b64_tr_b16 v[16:17], v0 offset:0\n\tds_read_b64_tr_b16 v[18:19], v0 offset:512\n\tv_mfma_f32_16x16x32_bf16 v[60:63], v[28:31], a[168:171], v[60:63]\n\tv_mfma_f32_16x16x32_bf16 v[68:71], v[28:31], a[172:175], v[68:71]\n\ts_waitcnt lgkmcnt(8)\n\tv_mfma_f32_16x16x32_bf16 v[48:51], v[32:35], a[176:179], v[48:51]\n\tds_read_b64_tr_b16 v[20:21], v1 offset:0\n\t" \
    "ds_read_b64_tr_b16 v[22:23], v1 offset:512\n\tv_mfma_f32_16x16x32_bf16 v[56:59], v[32:35], a[180:183], v[56:59]\n\tv_mfma_f32_16x16x32_bf16 v[64:67], v[32:35], a[184:187], v[64:67]\n\ts_waitcnt lgkmcnt(8)\n\tv_mfma_f32_16x16x32_bf16 v[52:55], v[36:39], a[176:179], v[52:55]\n\tds_read_b64_tr_b16 v[24:25], v2 offset:8192\n\tds_read_b64_tr_b16 v[26:27], v2 offset:8704\n\tv_mfma_f32_16x16x32_bf16 v[60:63], v[36:39], a[180:183], v[60:63]\n\t" \
    "v_mfma_f32_16x16x32_bf16 v[68:71], v[36:39], a[184:187], v[68:71]\n\ts_waitcnt lgkmcnt(8)\n\tv_mfma_f32_16x16x32_bf16 v[48:51], v[40:43], a[188:191], v[48:51]\n\tds_read_b64_tr_b16 v[28:29], v3 offset:8192\n\tds_read_b64_tr_b16 v[30:31], v3 offset:8704\n\tv_mfma_f32_16x16x32_bf16 v[56:59], v[40:43], a[192:195], v[56:59]\n\tv_mfma_f32_16x16x32_bf16 v[64:67], v[40:43], a[196:199], v[64:67]\n\ts_waitcnt lgkmcnt(8)\n\t" \
    "v_mfma_f32_16x16x32_bf16 v[52:55], v[44:47], a[188:191], v[52:55]\n\tds_read_b64_tr_b16 v[32:33], v2 offset:12288\n\tds_read_b64_tr_b16 v[34:35], v2 offset:12800\n\tv_mfma_f32_16x16x32_bf16 v[60:63], v[44:47], a[192:195], v[60:63]\n\tv_mfma_f32_16x16x32_bf16 v[68:71], v[44:47], a[196:199], v[68:71]\n\ts_barrier\n\ts_waitcnt lgkmcnt(8)\n\tv_mfma_f32_16x16x32_bf16 v[48:51], v[16:19], a[200:203], v[48:51]\n\t" \
    "ds_read_b64_tr_b16 v[36:37], v3 offset:12288\n\tds_read_b64_tr_b16 v[38:39], v3 offset:12800\n\tv_mfma_f32_16x16x32_bf16 v[56:59], v[16:19], a[204:207], v[56:59]\n\tv_mfma_f32_16x16x32_bf16 v[64:67], v[16:19], a[208:211], v[64:67]\n\ts_waitcnt lgkmcnt(8)\n\tv_mfma_f32_16x16x32_bf16 v[52:55], v[20:23], a[200:203], v[52:55]\n\tds_read_b64_tr_b16 v[40:41], v2 offset:16384\n\tds_read_b64_tr_b16 v[42:43], v2 offset:16896\n\t" \
    "v_mfma_f32_16x16x32_bf16 v[60:63], v[20:23], a[204:207], v[60:63]\n\tv_mfma_f32_16x16x32_bf16 v[68:71], v[20:23], a[208:211], v[68:71]\n\ts_mov_b32 s71, 1048576\n\ts_waitcnt vmcnt(1)\n\t107:\n\tv_add3_u32 v134, s36, v120, v121\n\tv_add3_u32 v135, v122, v123, v124\n\tv_add3_u32 v134, v134, v125, v126\n\t" \
    "v_add3_u32 v135, v135, v127, v128\n\tv_add3_u32 v134, v134, v129, v130\n\tv_add3_u32 v134, v134, v135, v131\n\tv_cmp_ne_u32 vcc, v134, v132\n\ts_cbranch_vccz 108f\n\ts_sub_u32 s71, s71, 1\n\ts_cbranch_scc1 108f\n\ts_sleep 6\n\t" \
    "s_add_u32 s73, s72, 0\n\tbuffer_load_dwordx4 v[120:123], v8, s[8:11], s73 offen offset:0 sc1\n\tbuffer_load_dwordx4 v[124:127], v8, s[8:11], s73 offen offset:1024 sc1\n\tbuffer_load_dwordx4 v[128:131], v8, s[8:11], s73 offen offset:2048 sc1\n\tbuffer_load_dword v132, v236, s[8:11], s73 offen offset:3072 sc1\n\ts_waitcnt vmcnt(0)\n\ts_branch 107b\n\t108:\n\t" \
    "v_lshlrev_b32 v96, 16, v120\n\tv_and_b32 v97, 0xffff0000, v120\n\tv_lshlrev_b32 v98, 16, v121\n\tv_and_b32 v99, 0xffff0000, v121\n\tv_lshlrev_b32 v100, 16, v122\n\tv_and_b32 v101, 0xffff0000, v122\n\tv_lshlrev_b32 v102, 16, v123\n\tv_and_b32 v103, 0xffff0000, v123\n\t" \
    "v_pk_add_f32 v[48:49], v[48:49], v[96:97]\n\tv_pk_add_f32 v[50:51], v[50:51], v[98:99]\n\tv_pk_add_f32 v[52:53], v[52:53], v[100:101]\n\tv_pk_add_f32 v[54:55], v[54:55], v[102:103]\n\tv_pk_mul_f32 v[96:97], v[48:49], v[48:49]\n\tv_pk_mul_f32 v[98:99], v[50:51], v[50:51]\n\tv_pk_mul_f32 v[100:101], v[52:53], v[52:53]\n\tv_pk_mul_f32 v[102:103], v[54:55], v[54:55]\n\t" \
    "v_pk_fma_f32 v[96:97], v[96:97], v[246:247], v[246:247] op_sel:[0,0,1] op_sel_hi:[1,0,1]\n\tv_pk_fma_f32 v[98:99], v[98:99], v[246:247], v[246:247] op_sel:[0,0,1] op_sel_hi:[1,0,1]\n\tv_pk_fma_f32 v[100:101], v[100:101], v[246:247], v[246:247] op_sel:[0,0,1] op_sel_hi:[1,0,1]\n\tv_pk_fma_f32 v[102:103], v[102:103], v[246:247], v[246:247] op_sel:[0,0,1] op_sel_hi:[1,0,1]\n\tv_pk_mul_f32 v[96:97], v[96:97], v[48:49]\n\tv_pk_mul_f32 v[98:99], v[98:99], v[50:51]\n\tv_pk_mul_f32 v[100:101], v[100:101], v[52:53]\n\tv_pk_mul_f32 v[102:103], v[102:103], v[54:55]\n\t" \
    "v_lshlrev_b32 v104, 16, v124\n\tv_exp_f32 v96, v96\n\tv_and_b32 v105, 0xffff0000, v124\n\tv_exp_f32 v97, v97\n\tv_lshlrev_b32 v106, 16, v125\n\tv_exp_f32 v98, v98\n\tv_and_b32 v107, 0xffff0000, v125\n\tv_exp_f32 v99, v99\n\t" \
    "v_lshlrev_b32 v108, 16, v126\n\tv_exp_f32 v100, v100\n\tv_and_b32 v109, 0xffff0000, v126\n\tv_exp_f32 v101, v101\n\tv_lshlrev_b32 v110, 16, v127\n\tv_exp_f32 v102, v102\n\tv_and_b32 v111, 0xffff0000, v127\n\tv_exp_f32 v103, v103\n\t" \
    "v_pk_add_f32 v[56:57], v[56:57], v[104:105]\n\tv_pk_add_f32 v[96:97], v[96:97], 1.0 op_sel_hi:[1,0]\n\tv_pk_add_f32 v[98:99], v[98:99], 1.0 op_sel_hi:[1,0]\n\tv_pk_add_f32 v[100:101], v[100:101], 1.0 op_sel_hi:[1,0]\n\tv_pk_add_f32 v[102:103], v[102:103], 1.0 op_sel_hi:[1,0]\n\tv_pk_add_f32 v[58:59], v[58:59], v[106:107]\n\tv_rcp_f32 v96, v96\n\tv_pk_add_f32 v[60:61], v[60:61], v[108:109]\n\t" \
    "v_rcp_f32 v97, v97\n\tv_pk_add_f32 v[62:63], v[62:63], v[110:111]\n\tv_rcp_f32 v98, v98\n\tv_pk_mul_f32 v[104:105], v[56:57], v[56:57]\n\tv_rcp_f32 v99, v99\n\tv_pk_mul_f32 v[106:107], v[58:59], v[58:59]\n\tv_rcp_f32 v100, v100\n\tv_pk_mul_f32 v[108:109], v[60:61], v[60:61]\n\t" \
    "v_rcp_f32 v101, v101\n\tv_pk_mul_f32 v[110:111], v[62:63], v[62:63]\n\tv_rcp_f32 v102, v102\n\tv_pk_fma_f32 v[104:105], v[104:105], v[246:247], v[246:247] op_sel:[0,0,1] op_sel_hi:[1,0,1]\n\tv_rcp_f32 v103, v103\n\tv_pk_fma_f32 v[106:107], v[106:107], v[246:247], v[246:247] op_sel:[0,0,1] op_sel_hi:[1,0,1]\n\tv_pk_mul_f32 v[96:97], v[96:97], v[48:49]\n\tv_pk_mul_f32 v[98:99], v[98:99], v[50:51]\n\t" \
    "v_pk_mul_f32 v[100:101], v[100:101], v[52:53]\n\tv_pk_mul_f32 v[102:103], v[102:103], v[54:55]\n\tv_cvt_pk_bf16_f32 v192, v96, v97\n\tv_cvt_pk_bf16_f32 v193, v98, v99\n\tv_cvt_pk_bf16_f32 v194, v100, v101\n\tv_cvt_pk_bf16_f32 v195, v102, v103\n\tbuffer_store_dwordx4 v[192:195], v8, s[8:11], s45 offen offset:0 sc1\n\tv_pk_fma_f32 v[108:109], v[108:109], v[246:247], v[246:247] op_sel:[0,0,1] op_sel_hi:[1,0,1]\n\t" \
    "v_pk_fma_f32 v[110:111], v[110:111], v[246:247], v[246:247] op_sel:[0,0,1] op_sel_hi:[1,0,1]\n\tv_pk_mul_f32 v[104:105], v[104:105], v[56:57]\n\tv_pk_mul_f32 v[106:107], v[106:107], v[58:59]\n\tv_pk_mul_f32 v[108:109], v[108:109], v[60:61]\n\tv_pk_mul_f32 v[110:111], v[110:111], v[62:63]\n\tv_lshlrev_b32 v112, 16, v128\n\tv_exp_f32 v104, v104\n\tv_and_b32 v113, 0xffff0000, v128\n\t" \
    "v_exp_f32 v105, v105\n\tv_lshlrev_b32 v114, 16, v129\n\tv_exp_f32 v106, v106\n\tv_and_b32 v115, 0xffff0000, v129\n\tv_exp_f32 v107, v107\n\tv_lshlrev_b32 v116, 16, v130\n\tv_exp_f32 v108, v108\n\tv_and_b32 v117, 0xffff0000, v130\n\t" \
    "v_exp_f32 v109, v109\n\tv_lshlrev_b32 v118, 16, v131\n\tv_exp_f32 v110, v110\n\tv_and_b32 v119, 0xffff0000, v131\n\tv_exp_f32 v111, v111\n\tv_pk_add_f32 v[64:65], v[64:65], v[112:113]\n\tv_pk_add_f32 v[104:105], v[104:105], 1.0 op_sel_hi:[1,0]\n\tv_pk_add_f32 v[106:107], v[106:107], 1.0 op_sel_hi:[1,0]\n\t" \
    "v_pk_add_f32 v[108:109], v[108:109], 1.0 op_sel_hi:[1,0]\n\tv_pk_add_f32 v[110:111], v[110:111], 1.0 op_sel_hi:[1,0]\n\tv_pk_add_f32 v[66:67], v[66:67], v[114:115]\n\tv_rcp_f32 v104, v104\n\tv_pk_add_f32 v[68:69], v[68:69], v[116:117]\n\tv_rcp_f32 v105, v105\n\tv_pk_add_f32 v[70:71], v[70:71], v[118:119]\n\tv_rcp_f32 v106, v106\n\t" \
    "v_pk_mul_f32 v[112:113], v[64:65], v[64:65]\n\tv_rcp_f32 v107, v107\n\tv_pk_mul_f32 v[114:115], v[66:67], v[66:67]\n\tv_rcp_f32 v108, v108\n\tv_pk_mul_f32 v[116:117], v[68:69], v[68:69]\n\tv_rcp_f32 v109, v109\n\tv_pk_mul_f32 v[118:119], v[70:71], v[70:71]\n\tv_rcp_f32 v110, v110\n\t" \
    "v_pk_fma_f32 v[112:113], v[112:113], v[246:247], v[246:247] op_sel:[0,0,1] op_sel_hi:[1,0,1]\n\tv_rcp_f32 v111, v111\n\tv_pk_fma_f32 v[114:115], v[114:115], v[246:247], v[246:247] op_sel:[0,0,1] op_sel_hi:[1,0,1]\n\tv_pk_mul_f32 v[104:105], v[104:105], v[56:57]\n\tv_pk_mul_f32 v[106:107], v[106:107], v[58:59]\n\tv_pk_mul_f32 v[108:109], v[108:109], v[60:61]\n\tv_pk_mul_f32 v[110:111], v[110:111], v[62:63]\n\tv_cvt_pk_bf16_f32 v196, v104, v105\n\t" \
    "v_cvt_pk_bf16_f32 v197, v106, v107\n\tv_cvt_pk_bf16_f32 v198, v108, v109\n\tv_cvt_pk_bf16_f32 v199, v110, v111\n\tbuffer_store_dwordx4 v[196:199], v8, s[8:11], s45 offen offset:1024 sc1\n\tv_pk_fma_f32 v[116:117], v[116:117], v[246:247], v[246:247] op_sel:[0,0,1] op_sel_hi:[1,0,1]\n\tv_pk_fma_f32 v[118:119], v[118:119], v[246:247], v[246:247] op_sel:[0,0,1] op_sel_hi:[1,0,1]\n\tv_pk_mul_f32 v[112:113], v[112:113], v[64:65]\n\tv_pk_mul_f32 v[114:115], v[114:115], v[66:67]\n\t" \
    "v_pk_mul_f32 v[116:117], v[116:117], v[68:69]\n\tv_pk_mul_f32 v[118:119], v[118:119], v[70:71]\n\tv_exp_f32 v112, v112\n\tv_exp_f32 v113, v113\n\tv_exp_f32 v114, v114\n\tv_exp_f32 v115, v115\n\tv_exp_f32 v116, v116\n\tv_exp_f32 v117, v117\n\t" \
    "v_exp_f32 v118, v118\n\tv_exp_f32 v119, v119\n\tv_pk_add_f32 v[112:113], v[112:113], 1.0 op_sel_hi:[1,0]\n\tv_pk_add_f32 v[114:115], v[114:115], 1.0 op_sel_hi:[1,0]\n\tv_pk_add_f32 v[116:117], v[116:117], 1.0 op_sel_hi:[1,0]\n\tv_pk_add_f32 v[118:119], v[118:119], 1.0 op_sel_hi:[1,0]\n\tv_rcp_f32 v112, v112\n\tv_rcp_f32 v113, v113\n\t" \
    "v_rcp_f32 v114, v114\n\tv_rcp_f32 v115, v115\n\tv_rcp_f32 v116, v116\n\tv_rcp_f32 v117, v117\n\tv_rcp_f32 v118, v118\n\tv_rcp_f32 v119, v119\n\tv_pk_mul_f32 v[112:113], v[112:113], v[64:65]\n\tv_pk_mul_f32 v[114:115], v[114:115], v[66:67]\n\t" \
    "v_pk_mul_f32 v[116:117], v[116:117], v[68:69]\n\tv_pk_mul_f32 v[118:119], v[118:119], v[70:71]\n\tv_cvt_pk_bf16_f32 v200, v112, v113\n\tv_cvt_pk_bf16_f32 v201, v114, v115\n\tv_cvt_pk_bf16_f32 v202, v116, v117\n\tv_cvt_pk_bf16_f32 v203, v118, v119\n\tbuffer_store_dwordx4 v[200:203], v8, s[8:11], s45 offen offset:2048 sc1\n\ts_waitcnt lgkmcnt(8)\n\t" \
    "v_mfma_f32_16x16x32_bf16 v[144:147], v[24:27], v[168:171], v[144:147]\n\tds_read_b64_tr_b16 v[44:45], v3 offset:16384\n\tds_read_b64_tr_b16 v[46:47], v3 offset:16896\n\tv_mfma_f32_16x16x32_bf16 v[152:155], v[24:27], v[172:175], v[152:155]\n\tbuffer_store_dwordx2 v[248:249], off, s[8:11], s60 sc1\n\tv_mfma_f32_16x16x32_bf16 v[160:163], v[24:27], v[176:179], v[160:163]\n\ts_waitcnt lgkmcnt(8)\n\tv_mfma_f32_16x16x32_bf16 v[148:151], v[28:31], v[168:171], v[148:151]\n\t" \
    "ds_read_b64_tr_b16 v[16:17], v2 offset:20480\n\tds_read_b64_tr_b16 v[18:19], v2 offset:20992\n\tv_mfma_f32_16x16x32_bf16 v[156:159], v[28:31], v[172:175], v[156:159]\n\tv_mfma_f32_16x16x32_bf16 v[164:167], v[28:31], v[176:179], v[164:167]\n\ts_waitcnt lgkmcnt(8)\n\tv_mfma_f32_16x16x32_bf16 v[144:147], v[32:35], a[116:119], v[144:147]\n\tds_read_b64_tr_b16 v[20:21], v3 offset:20480\n\tds_read_b64_tr_b16 v[22:23], v3 offset:20992\n\t" \
    "v_mfma_f32_16x16x32_bf16 v[152:155], v[32:35], a[120:123], v[152:155]\n\tv_mfma_f32_16x16x32_bf16 v[160:163], v[32:35], a[124:127], v[160:163]\n\ts_waitcnt lgkmcnt(8)\n\tv_mfma_f32_16x16x32_bf16 v[148:151], v[36:39], a[116:119], v[148:151]\n\tds_read_b64_tr_b16 v[24:25], v2 offset:24576\n\tds_read_b64_tr_b16 v[26:27], v2 offset:25088\n\tv_mfma_f32_16x16x32_bf16 v[156:159], v[36:39], a[120:123], v[156:159]\n\tv_mfma_f32_16x16x32_bf16 v[164:167], v[36:39], a[124:127], v[164:167]\n\t" \
    "s_waitcnt lgkmcnt(8)\n\tv_mfma_f32_16x16x32_bf16 v[144:147], v[40:43], a[44:47], v[144:147]\n\tds_read_b64_tr_b16 v[28:29], v3 offset:24576\n\tds_read_b64_tr_b16 v[30:31], v3 offset:25088\n\tv_mfma_f32_16x16x32_bf16 v[152:155], v[40:43], a[48:51], v[152:155]\n\tv_mfma_f32_16x16x32_bf16 v[160:163], v[40:43], a[52:55], v[160:163]\n\ts_waitcnt lgkmcnt(8)\n\tv_mfma_f32_16x16x32_bf16 v[148:151], v[44:47], a[44:47], v[148:151]\n\t" \
    "ds_read_b64_tr_b16 v[32:33], v2 offset:28672\n\tds_read_b64_tr_b16 v[34:35], v2 offset:29184\n\tv_mfma_f32_16x16x32_bf16 v[156:159], v[44:47], a[48:51], v[156:159]\n\tv_mfma_f32_16x16x32_bf16 v[164:167], v[44:47], a[52:55], v[164:167]\n\ts_waitcnt lgkmcnt(8)\n\tv_mfma_f32_16x16x32_bf16 v[144:147], v[16:19], a[128:131], v[144:147]\n\tds_read_b64_tr_b16 v[36:37], v3 offset:28672\n\tds_read_b64_tr_b16 v[38:39], v3 offset:29184\n\t" \
    "v_mfma_f32_16x16x32_bf16 v[152:155], v[16:19], a[132:135], v[152:155]\n\tv_mfma_f32_16x16x32_bf16 v[160:163], v[16:19], a[136:139], v[160:163]\n\ts_waitcnt lgkmcnt(8)\n\tv_mfma_f32_16x16x32_bf16 v[148:151], v[20:23], a[128:131], v[148:151]\n\tds_read_b64_tr_b16 v[40:41], v2 offset:32768\n\tds_read_b64_tr_b16 v[42:43], v2 offset:33280\n\tv_mfma_f32_16x16x32_bf16 v[156:159], v[20:23], a[132:135], v[156:159]\n\tv_mfma_f32_16x16x32_bf16 v[164:167], v[20:23], a[136:139], v[164:167]\n\t" \
    "s_waitcnt lgkmcnt(8)\n\tv_mfma_f32_16x16x32_bf16 v[144:147], v[24:27], a[140:143], v[144:147]\n\tds_read_b64_tr_b16 v[44:45], v3 offset:32768\n\tds_read_b64_tr_b16 v[46:47], v3 offset:33280\n\tv_mfma_f32_16x16x32_bf16 v[152:155], v[24:27], a[144:147], v[152:155]\n\tv_mfma_f32_16x16x32_bf16 v[160:163], v[24:27], a[148:151], v[160:163]\n\ts_waitcnt lgkmcnt(8)\n\tv_mfma_f32_16x16x32_bf16 v[148:151], v[28:31], a[140:143], v[148:151]\n\t" \
    "ds_read_b64_tr_b16 v[16:17], v2 offset:36864\n\tds_read_b64_tr_b16 v[18:19], v2 offset:37376\n\tv_mfma_f32_16x16x32_bf16 v[156:159], v[28:31], a[144:147], v[156:159]\n\tv_mfma_f32_16x16x32_bf16 v[164:167], v[28:31], a[148:151], v[164:167]\n\ts_waitcnt lgkmcnt(8)\n\tv_mfma_f32_16x16x32_bf16 v[144:147], v[32:35], a[152:155], v[144:147]\n\tds_read_b64_tr_b16 v[20:21], v3 offset:36864\n\tds_read_b64_tr_b16 v[22:23], v3 offset:37376\n\t" \
    "v_mfma_f32_16x16x32_bf16 v[152:155], v[32:35], a[156:159], v[152:155]\n\tv_mfma_f32_16x16x32_bf16 v[160:163], v[32:35], a[160:163], v[160:163]\n\ts_waitcnt lgkmcnt(8)\n\tv_mfma_f32_16x16x32_bf16 v[148:151], v[36:39], a[152:155], v[148:151]\n\tds_read_b64_tr_b16 v[24:25], v2 offset:40960\n\tds_read_b64_tr_b16 v[26:27], v2 offset:41472\n\tv_mfma_f32_16x16x32_bf16 v[156:159], v[36:39], a[156:159], v[156:159]\n\tv_mfma_f32_16x16x32_bf16 v[164:167], v[36:39], a[160:163], v[164:167]\n\t" \
    "s_waitcnt lgkmcnt(8)\n\tv_mfma_f32_16x16x32_bf16 v[144:147], v[40:43], a[164:167], v[144:147]\n\tds_read_b64_tr_b16 v[28:29], v3 offset:40960\n\tds_read_b64_tr_b16 v[30:31], v3 offset:41472\n\tv_mfma_f32_16x16x32_bf16 v[152:155], v[40:43], a[168:171], v[152:155]\n\tv_mfma_f32_16x16x32_bf16 v[160:163], v[40:43], a[172:175], v[160:163]\n\ts_waitcnt lgkmcnt(8)\n\tv_mfma_f32_16x16x32_bf16 v[148:151], v[44:47], a[164:167], v[148:151]\n\t" \
    "ds_read_b64_tr_b16 v[32:33], v2 offset:45056\n\tds_read_b64_tr_b16 v[34:35], v2 offset:45568\n\tv_mfma_f32_16x16x32_bf16 v[156:159], v[44:47], a[168:171], v[156:159]\n\tv_mfma_f32_16x16x32_bf16 v[164:167], v[44:47], a[172:175], v[164:167]\n\ts_barrier\n\ts_waitcnt lgkmcnt(8)\n\tv_mfma_f32_16x16x32_bf16 v[144:147], v[16:19], a[176:179], v[144:147]\n\tds_read_b64_tr_b16 v[36:37], v3 offset:45056\n\t" \
    "ds_read_b64_tr_b16 v[38:39], v3 offset:45568\n\tv_mfma_f32_16x16x32_bf16 v[152:155], v[16:19], a[180:183], v[152:155]\n\tv_mfma_f32_16x16x32_bf16 v[160:163], v[16:19], a[184:187], v[160:163]\n\ts_waitcnt lgkmcnt(8)\n\tv_mfma_f32_16x16x32_bf16 v[148:151], v[20:23], a[176:179], v[148:151]\n\tds_read_b64_tr_b16 v[40:41], v237 offset:28672\n\tds_read_b64_tr_b16 v[42:43], v237 offset:29184\n\tv_mfma_f32_16x16x32_bf16 v[156:159], v[20:23], a[180:183], v[156:159]\n\t" \
    "v_mfma_f32_16x16x32_bf16 v[164:167], v[20:23], a[184:187], v[164:167]\n\ts_waitcnt lgkmcnt(8)\n\tv_mfma_f32_16x16x32_bf16 v[144:147], v[24:27], a[188:191], v[144:147]\n\tds_read_b64_tr_b16 v[44:45], v238 offset:28672\n\tds_read_b64_tr_b16 v[46:47], v238 offset:29184\n\tv_mfma_f32_16x16x32_bf16 v[152:155], v[24:27], a[192:195], v[152:155]\n\tv_mfma_f32_16x16x32_bf16 v[160:163], v[24:27], a[196:199], v[160:163]\n\ts_waitcnt lgkmcnt(8)\n\t" \
    "v_mfma_f32_16x16x32_bf16 v[148:151], v[28:31], a[188:191], v[148:151]\n\tds_read_b64_tr_b16 v[16:17], v2 offset:49152\n\tds_read_b64_tr_b16 v[18:19], v2 offset:49664\n\tv_mfma_f32_16x16x32_bf16 v[156:159], v[28:31], a[192:195], v[156:159]\n\tv_mfma_f32_16x16x32_bf16 v[164:167], v[28:31], a[196:199], v[164:167]\n\ts_waitcnt lgkmcnt(8)\n\tv_mfma_f32_16x16x32_bf16 v[144:147], v[32:35], a[200:203], v[144:147]\n\tds_read_b64_tr_b16 v[20:21], v3 offset:49152\n\t" \
    "ds_read_b64_tr_b16 v[22:23], v3 offset:49664\n\tv_mfma_f32_16x16x32_bf16 v[152:155], v[32:35], a[204:207], v[152:155]\n\tv_mfma_f32_16x16x32_bf16 v[160:163], v[32:35], a[208:211], v[160:163]\n\ts_waitcnt vmcnt(1)\n\tbuffer_store_dwordx2 v[248:249], off, s[8:11], s49 sc1\n\ts_waitcnt lgkmcnt(8)\n\tv_mfma_f32_16x16x32_bf16 v[148:151], v[36:39], a[200:203], v[148:151]\n\tds_read_b64_tr_b16 v[24:25], v2 offset:53248\n\t" \
    "ds_read_b64_tr_b16 v[26:27], v2 offset:53760\n\tv_mfma_f32_16x16x32_bf16 v[156:159], v[36:39], a[204:207], v[156:159]\n\tv_mfma_f32_16x16x32_bf16 v[164:167], v[36:39], a[208:211], v[164:167]\n\ts_waitcnt lgkmcnt(8)\n\tv_mfma_f32_16x16x32_bf16 v[72:75], v[40:43], v[180:183], v[72:75]\n\tds_read_b64_tr_b16 v[28:29], v3 offset:53248\n\tds_read_b64_tr_b16 v[30:31], v3 offset:53760\n\ts_add_u32 s68, s52, 3328\n\t" \
    "v_mfma_f32_16x16x32_bf16 v[80:83], v[40:43], v[184:187], v[80:83]\n\tv_cvt_pk_bf16_f32 v144, v144, v145\n\tv_cvt_pk_bf16_f32 v145, v146, v147\n\tv_cvt_pk_bf16_f32 v146, v148, v149\n\tv_mfma_f32_16x16x32_bf16 v[88:91], v[40:43], v[188:191], v[88:91]\n\tv_cvt_pk_bf16_f32 v147, v150, v151\n\ts_waitcnt lgkmcnt(8)\n\tv_mfma_f32_16x16x32_bf16 v[76:79], v[44:47], v[180:183], v[76:79]\n\t" \
    "ds_read_b64_tr_b16 v[32:33], v2 offset:57344\n\tds_read_b64_tr_b16 v[34:35], v2 offset:57856\n\tv_mfma_f32_16x16x32_bf16 v[84:87], v[44:47], v[184:187], v[84:87]\n\tbuffer_store_dwordx4 v[144:147], v8, s[8:11], s68 offen offset:0 sc1\n\tv_cvt_pk_bf16_f32 v148, v152, v153\n\tv_cvt_pk_bf16_f32 v149, v154, v155\n\tv_mfma_f32_16x16x32_bf16 v[92:95], v[44:47], v[188:191], v[92:95]\n\tv_cvt_pk_bf16_f32 v150, v156, v157\n\t" \
    "v_cvt_pk_bf16_f32 v151, v158, v159\n\ts_waitcnt lgkmcnt(8)\n\tv_mfma_f32_16x16x32_bf16 v[72:75], v[16:19], a[56:59], v[72:75]\n\tds_read_b64_tr_b16 v[36:37], v3 offset:57344\n\tds_read_b64_tr_b16 v[38:39], v3 offset:57856\n\tv_mfma_f32_16x16x32_bf16 v[80:83], v[16:19], a[60:63], v[80:83]\n\tbuffer_store_dwordx4 v[148:151], v8, s[8:11], s68 offen offset:1024 sc1\n\tv_cvt_pk_bf16_f32 v152, v160, v161\n\t" \
    "v_cvt_pk_bf16_f32 v153, v162, v163\n\tv_mfma_f32_16x16x32_bf16 v[88:91], v[16:19], a[64:67], v[88:91]\n\tv_cvt_pk_bf16_f32 v154, v164, v165\n\tv_cvt_pk_bf16_f32 v155, v166, v167\n\ts_waitcnt lgkmcnt(8)\n\tv_mfma_f32_16x16x32_bf16 v[76:79], v[20:23], a[56:59], v[76:79]\n\tds_read_b64_tr_b16 v[40:41], v2 offset:61440\n\tds_read_b64_tr_b16 v[42:43], v2 offset:61952\n\t" \
    "v_mfma_f32_16x16x32_bf16 v[84:87], v[20:23], a[60:63], v[84:87]\n\tbuffer_store_dwordx4 v[152:155], v8, s[8:11], s68 offen offset:2048 sc1\n\tv_add3_u32 v156, s36, v144, v145\n\tv_add3_u32 v157, v146, v147, v148\n\tv_mfma_f32_16x16x32_bf16 v[92:95], v[20:23], a[64:67], v[92:95]\n\tv_add3_u32 v156, v156, v149, v150\n\tv_add3_u32 v157, v157, v151, v152\n\ts_waitcnt lgkmcnt(8)\n\t" \
    "v_mfma_f32_16x16x32_bf16 v[72:75], v[24:27], v[204:207], v[72:75]\n\tds_read_b64_tr_b16 v[44:45], v3 offset:61440\n\tds_read_b64_tr_b16 v[46:47], v3 offset:61952\n\tv_add3_u32 v156, v156, v153, v154\n\tv_mfma_f32_16x16x32_bf16 v[80:83], v[24:27], v[208:211], v[80:83]\n\tv_add3_u32 v156, v156, v157, v155\n\tv_mfma_f32_16x16x32_bf16 v[88:91], v[24:27], v[212:215], v[88:91]\n\ts_waitcnt lgkmcnt(8)\n\t" \
    "v_mfma_f32_16x16x32_bf16 v[76:79], v[28:31], v[204:207], v[76:79]\n\tds_read_b64_tr_b16 v[16:17], v237 offset:0\n\tds_read_b64_tr_b16 v[18:19], v237 offset:512\n\tv_mfma_f32_16x16x32_bf16 v[84:87], v[28:31], v[208:211], v[84:87]\n\tbuffer_store_dword v156, v236, s[8:11], s68 offen offset:3072 sc1\n\ts_cmp_le_u32 s4, 0\n\ts_cselect_b32 s69, 262144, 0\n\ts_add_u32 s69, s69, 0\n\t" \
    "v_mfma_f32_16x16x32_bf16 v[92:95], v[28:31], v[212:215], v[92:95]\n\ts_add_u32 m0, s54, 147456\n\ts_waitcnt lgkmcnt(8)\n\tv_mfma_f32_16x16x32_bf16 v[72:75], v[32:35], a[68:71], v[72:75]\n\tds_read_b64_tr_b16 v[20:21], v238 offset:0\n\tds_read_b64_tr_b16 v[22:23], v238 offset:512\n\tv_mfma_f32_16x16x32_bf16 v[80:83], v[32:35], a[72:75], v[80:83]\n\tbuffer_load_dwordx4 v12, s[16:19], s69 offen lds\n\t" \
    "s_cmp_le_u32 s4, 0\n\ts_cselect_b32 s69, 262144, 0\n\ts_add_u32 s69, s69, 0\n\tv_mfma_f32_16x16x32_bf16 v[88:91], v[32:35], a[76:79], v[88:91]\n\ts_add_u32 m0, s54, 151552\n\ts_waitcnt lgkmcnt(8)\n\tv_mfma_f32_16x16x32_bf16 v[76:79], v[36:39], a[68:71], v[76:79]\n\tds_read_b64_tr_b16 v[24:25], v237 offset:4096\n\t" \
    "ds_read_b64_tr_b16 v[26:27], v237 offset:4608\n\tv_mfma_f32_16x16x32_bf16 v[84:87], v[36:39], a[72:75], v[84:87]\n\tbuffer_load_dwordx4 v13, s[16:19], s69 offen lds\n\ts_cmp_le_u32 s4, 1\n\ts_cselect_b32 s69, 262144, 0\n\ts_add_u32 s69, s69, 262144\n\tv_mfma_f32_16x16x32_bf16 v[92:95], v[36:39], a[76:79], v[92:95]\n\ts_add_u32 m0, s54, 155648\n\t" \
    "s_waitcnt lgkmcnt(8)\n\tv_mfma_f32_16x16x32_bf16 v[72:75], v[40:43], v[216:219], v[72:75]\n\tds_read_b64_tr_b16 v[28:29], v238 offset:4096\n\tds_read_b64_tr_b16 v[30:31], v238 offset:4608\n\tv_mfma_f32_16x16x32_bf16 v[80:83], v[40:43], a[0:3], v[80:83]\n\tbuffer_load_dwordx4 v12, s[16:19], s69 offen lds\n\ts_cmp_le_u32 s4, 1\n\ts_cselect_b32 s69, 262144, 0\n\t" \
    "s_add_u32 s69, s69, 262144\n\tv_mfma_f32_16x16x32_bf16 v[88:91], v[40:43], a[4:7], v[88:91]\n\ts_add_u32 m0, s54, 159744\n\ts_waitcnt lgkmcnt(8)\n\tv_mfma_f32_16x16x32_bf16 v[76:79], v[44:47], v[216:219], v[76:79]\n\tds_read_b64_tr_b16 v[32:33], v237 offset:8192\n\tds_read_b64_tr_b16 v[34:35], v237 offset:8704\n\tv_mfma_f32_16x16x32_bf16 v[84:87], v[44:47], a[0:3], v[84:87]\n\t" \
    "buffer_load_dwordx4 v13, s[16:19], s69 offen lds\n\ts_cmp_le_u32 s4, 2\n\ts_cselect_b32 s69, 262144, 0\n\ts_add_u32 s69, s69, 524288\n\tv_mfma_f32_16x16x32_bf16 v[92:95], v[44:47], a[4:7], v[92:95]\n\ts_add_u32 m0, s54, 4096\n\ts_barrier\n\ts_waitcnt lgkmcnt(8)\n\t" \
    "v_mfma_f32_16x16x32_bf16 v[72:75], v[16:19], a[80:83], v[72:75]\n\tds_read_b64_tr_b16 v[36:37], v238 offset:8192\n\tds_read_b64_tr_b16 v[38:39], v238 offset:8704\n\tv_mfma_f32_16x16x32_bf16 v[80:83], v[16:19], a[84:87], v[80:83]\n\tbuffer_load_dwordx4 v12, s[16:19], s69 offen lds\n\ts_cmp_le_u32 s4, 2\n\ts_cselect_b32 s69, 262144, 0\n\ts_add_u32 s69, s69, 524288\n\t" \
    "v_mfma_f32_16x16x32_bf16 v[88:91], v[16:19], a[88:91], v[88:91]\n\ts_add_u32 m0, s54, 8192\n\ts_waitcnt lgkmcnt(8)\n\tv_mfma_f32_16x16x32_bf16 v[76:79], v[20:23], a[80:83], v[76:79]\n\tds_read_b64_tr_b16 v[40:41], v237 offset:12288\n\tds_read_b64_tr_b16 v[42:43], v237 offset:12800\n\tv_mfma_f32_16x16x32_bf16 v[84:87], v[20:23], a[84:87], v[84:87]\n\tbuffer_load_dwordx4 v13, s[16:19], s69 offen lds\n\t" \
    "s_cmp_le_u32 s4, 3\n\ts_cselect_b32 s69, 262144, 0\n\ts_add_u32 s69, s69, 786432\n\tv_mfma_f32_16x16x32_bf16 v[92:95], v[20:23], a[88:91], v[92:95]\n\ts_add_u32 m0, s54, 12288\n\ts_waitcnt lgkmcnt(8)\n\tv_mfma_f32_16x16x32_bf16 v[72:75], v[24:27], a[8:11], v[72:75]\n\tds_read_b64_tr_b16 v[44:45], v238 offset:12288\n\t" \
    "ds_read_b64_tr_b16 v[46:47], v238 offset:12800\n\tv_mfma_f32_16x16x32_bf16 v[80:83], v[24:27], a[12:15], v[80:83]\n\tbuffer_load_dwordx4 v12, s[16:19], s69 offen lds\n\ts_cmp_le_u32 s4, 3\n\ts_cselect_b32 s69, 262144, 0\n\ts_add_u32 s69, s69, 786432\n\tv_mfma_f32_16x16x32_bf16 v[88:91], v[24:27], a[16:19], v[88:91]\n\ts_add_u32 m0, s54, 16384\n\t" \
    "s_waitcnt lgkmcnt(8)\n\tv_mfma_f32_16x16x32_bf16 v[76:79], v[28:31], a[8:11], v[76:79]\n\tds_read_b64_tr_b16 v[16:17], v237 offset:16384\n\tds_read_b64_tr_b16 v[18:19], v237 offset:16896\n\tv_mfma_f32_16x16x32_bf16 v[84:87], v[28:31], a[12:15], v[84:87]\n\tbuffer_load_dwordx4 v13, s[16:19], s69 offen lds\n\ts_cmp_le_u32 s4, 4\n\ts_cselect_b32 s69, 262144, 0\n\t" \
    "s_add_u32 s69, s69, 1048576\n\tv_mfma_f32_16x16x32_bf16 v[92:95], v[28:31], a[16:19], v[92:95]\n\ts_add_u32 m0, s54, 20480\n\ts_waitcnt lgkmcnt(8)\n\tv_mfma_f32_16x16x32_bf16 v[72:75], v[32:35], a[92:95], v[72:75]\n\tds_read_b64_tr_b16 v[20:21], v238 offset:16384\n\tds_read_b64_tr_b16 v[22:23], v238 offset:16896\n\tv_mfma_f32_16x16x32_bf16 v[80:83], v[32:35], a[96:99], v[80:83]\n\t" \
    "buffer_load_dwordx4 v12, s[16:19], s69 offen lds\n\ts_cmp_le_u32 s4, 4\n\ts_cselect_b32 s69, 262144, 0\n\ts_add_u32 s69, s69, 1048576\n\tv_mfma_f32_16x16x32_bf16 v[88:91], v[32:35], a[100:103], v[88:91]\n\ts_add_u32 m0, s54, 24576\n\ts_waitcnt lgkmcnt(8)\n\tv_mfma_f32_16x16x32_bf16 v[76:79], v[36:39], a[92:95], v[76:79]\n\t" \
    "ds_read_b64_tr_b16 v[24:25], v237 offset:20480\n\tds_read_b64_tr_b16 v[26:27], v237 offset:20992\n\tv_mfma_f32_16x16x32_bf16 v[84:87], v[36:39], a[96:99], v[84:87]\n\tbuffer_load_dwordx4 v13, s[16:19], s69 offen lds\n\ts_cmp_le_u32 s4, 5\n\ts_cselect_b32 s69, 262144, 0\n\ts_add_u32 s69, s69, 1310720\n\tv_mfma_f32_16x16x32_bf16 v[92:95], v[36:39], a[100:103], v[92:95]\n\t" \
    "s_add_u32 m0, s54, 28672\n\ts_waitcnt lgkmcnt(8)\n\tv_mfma_f32_16x16x32_bf16 v[72:75], v[40:43], a[20:23], v[72:75]\n\tds_read_b64_tr_b16 v[28:29], v238 offset:20480\n\tds_read_b64_tr_b16 v[30:31], v238 offset:20992\n\tv_mfma_f32_16x16x32_bf16 v[80:83], v[40:43], a[24:27], v[80:83]\n\tv_mfma_f32_16x16x32_bf16 v[88:91], v[40:43], a[28:31], v[88:91]\n\ts_waitcnt lgkmcnt(8)\n\t" \
    "v_mfma_f32_16x16x32_bf16 v[76:79], v[44:47], a[20:23], v[76:79]\n\tds_read_b64_tr_b16 v[32:33], v237 offset:24576\n\tds_read_b64_tr_b16 v[34:35], v237 offset:25088\n\tv_mfma_f32_16x16x32_bf16 v[84:87], v[44:47], a[24:27], v[84:87]\n\tbuffer_load_dwordx4 v12, s[16:19], s69 offen lds\n\ts_cmp_le_u32 s4, 5\n\ts_cselect_b32 s69, 262144, 0\n\ts_add_u32 s69, s69, 1310720\n\t" \
    "v_mfma_f32_16x16x32_bf16 v[92:95], v[44:47], a[28:31], v[92:95]\n\ts_add_u32 m0, s54, 32768\n\ts_waitcnt lgkmcnt(8)\n\tv_mfma_f32_16x16x32_bf16 v[72:75], v[16:19], a[104:107], v[72:75]\n\tds_read_b64_tr_b16 v[36:37], v238 offset:24576\n\tds_read_b64_tr_b16 v[38:39], v238 offset:25088\n\tv_mfma_f32_16x16x32_bf16 v[80:83], v[16:19], a[108:111], v[80:83]\n\tbuffer_load_dwordx4 v13, s[16:19], s69 offen lds\n\t" \
    "s_cmp_le_u32 s4, 6\n\ts_cselect_b32 s69, 262144, 0\n\ts_add_u32 s69, s69, 1572864\n\tv_mfma_f32_16x16x32_bf16 v[88:91], v[16:19], a[112:115], v[88:91]\n\ts_add_u32 m0, s54, 94208\n\ts_waitcnt lgkmcnt(8)\n\tv_mfma_f32_16x16x32_bf16 v[76:79], v[20:23], a[104:107], v[76:79]\n\tds_read_b64_tr_b16 v[40:41], v237 offset:32768\n\t" \
    "ds_read_b64_tr_b16 v[42:43], v237 offset:33280\n\tv_mfma_f32_16x16x32_bf16 v[84:87], v[20:23], a[108:111], v[84:87]\n\tv_mfma_f32_16x16x32_bf16 v[92:95], v[20:23], a[112:115], v[92:95]\n\ts_waitcnt lgkmcnt(8)\n\tv_mfma_f32_16x16x32_bf16 v[72:75], v[24:27], a[32:35], v[72:75]\n\tds_read_b64_tr_b16 v[44:45], v238 offset:32768\n\tds_read_b64_tr_b16 v[46:47], v238 offset:33280\n\tv_mfma_f32_16x16x32_bf16 v[80:83], v[24:27], a[36:39], v[80:83]\n\t" \
    "buffer_load_dwordx4 v12, s[16:19], s69 offen lds\n\ts_cmp_le_u32 s4, 6\n\ts_cselect_b32 s69, 262144, 0\n\ts_add_u32 s69, s69, 1572864\n\tv_mfma_f32_16x16x32_bf16 v[88:91], v[24:27], a[40:43], v[88:91]\n\ts_add_u32 m0, s54, 36864\n\ts_waitcnt lgkmcnt(8)\n\tv_mfma_f32_16x16x32_bf16 v[76:79], v[28:31], a[32:35], v[76:79]\n\t" \
    "ds_read_b64_tr_b16 v[16:17], v237 offset:36864\n\tds_read_b64_tr_b16 v[18:19], v237 offset:37376\n\tv_mfma_f32_16x16x32_bf16 v[84:87], v[28:31], a[36:39], v[84:87]\n\tbuffer_load_dwordx4 v13, s[16:19], s69 offen lds\n\ts_cmp_le_u32 s4, 7\n\ts_cselect_b32 s69, 262144, 0\n\ts_add_u32 s69, s69, 1835008\n\tv_mfma_f32_16x16x32_bf16 v[92:95], v[28:31], a[40:43], v[92:95]\n\t" \
    "s_add_u32 m0, s54, 40960\n\ts_waitcnt lgkmcnt(8)\n\tv_mfma_f32_16x16x32_bf16 v[72:75], v[32:35], v[168:171], v[72:75]\n\tds_read_b64_tr_b16 v[20:21], v238 offset:36864\n\tds_read_b64_tr_b16 v[22:23], v238 offset:37376\n\tv_mfma_f32_16x16x32_bf16 v[80:83], v[32:35], v[172:175], v[80:83]\n\tbuffer_load_dwordx4 v12, s[16:19], s69 offen lds\n\ts_add_u32 s68, s72, 3328\n\t" \
    "v_mfma_f32_16x16x32_bf16 v[88:91], v[32:35], v[176:179], v[88:91]\n\ts_waitcnt lgkmcnt(8)\n\tv_mfma_f32_16x16x32_bf16 v[76:79], v[36:39], v[168:171], v[76:79]\n\tds_read_b64_tr_b16 v[24:25], v237 offset:40960\n\tds_read_b64_tr_b16 v[26:27], v237 offset:41472\n\tv_mfma_f32_16x16x32_bf16 v[84:87], v[36:39], v[172:175], v[84:87]\n\tbuffer_load_dwordx4 v[144:147], v8, s[8:11], s68 offen offset:0 sc1\n\tv_mfma_f32_16x16x32_bf16 v[92:95], v[36:39], v[176:179], v[92:95]\n\t" \
    "s_waitcnt lgkmcnt(8)\n\tv_mfma_f32_16x16x32_bf16 v[72:75], v[40:43], a[116:119], v[72:75]\n\tds_read_b64_tr_b16 v[28:29], v238 offset:40960\n\tds_read_b64_tr_b16 v[30:31], v238 offset:41472\n\tv_mfma_f32_16x16x32_bf16 v[80:83], v[40:43], a[120:123], v[80:83]\n\tbuffer_load_dwordx4 v[148:151], v8, s[8:11], s68 offen offset:1024 sc1\n\tv_mfma_f32_16x16x32_bf16 v[88:91], v[40:43], a[124:127], v[88:91]\n\ts_waitcnt lgkmcnt(8)\n\t" \
    "v_mfma_f32_16x16x32_bf16 v[76:79], v[44:47], a[116:119], v[76:79]\n\tds_read_b64_tr_b16 v[32:33], v237 offset:45056\n\tds_read_b64_tr_b16 v[34:35], v237 offset:45568\n\tv_mfma_f32_16x16x32_bf16 v[84:87], v[44:47], a[120:123], v[84:87]\n\tv_mfma_f32_16x16x32_bf16 v[92:95], v[44:47], a[124:127], v[92:95]\n\ts_barrier\n\ts_waitcnt lgkmcnt(8)\n\tv_mfma_f32_16x16x32_bf16 v[72:75], v[16:19], a[44:47], v[72:75]\n\t" \
    "ds_read_b64_tr_b16 v[36:37], v238 offset:45056\n\tds_read_b64_tr_b16 v[38:39], v238 offset:45568\n\tv_mfma_f32_16x16x32_bf16 v[80:83], v[16:19], a[48:51], v[80:83]\n\tbuffer_load_dwordx4 v[152:155], v8, s[8:11], s68 offen offset:2048 sc1\n\tv_mfma_f32_16x16x32_bf16 v[88:91], v[16:19], a[52:55], v[88:91]\n\ts_waitcnt lgkmcnt(8)\n\tv_mfma_f32_16x16x32_bf16 v[76:79], v[20:23], a[44:47], v[76:79]\n\tds_read_b64_tr_b16 v[40:41], v237 offset:49152\n\t" \
    "ds_read_b64_tr_b16 v[42:43], v237 offset:49664\n\tv_mfma_f32_16x16x32_bf16 v[84:87], v[20:23], a[48:51], v[84:87]\n\tbuffer_load_dword v156, v236, s[8:11], s68 offen offset:3072 sc1\n\ts_cmp_le_u32 s4, 7\n\ts_cselect_b32 s69, 262144, 0\n\ts_add_u32 s69, s69, 1835008\n\tv_mfma_f32_16x16x32_bf16 v[92:95], v[20:23], a[52:55], v[92:95]\n\ts_add_u32 m0, s54, 45056\n\t" \
    "s_waitcnt lgkmcnt(8)\n\tv_mfma_f32_16x16x32_bf16 v[72:75], v[24:27], a[128:131], v[72:75]\n\tds_read_b64_tr_b16 v[44:45], v238 offset:49152\n\tds_read_b64_tr_b16 v[46:47], v238 offset:49664\n\tv_mfma_f32_16x16x32_bf16 v[80:83], v[24:27], a[132:135], v[80:83]\n\tbuffer_load_dwordx4 v13, s[16:19], s69 offen lds\n\ts_cmp_le_u32 s4, 8\n\ts_cselect_b32 s69, 262144, 0\n\t" \
    "s_add_u32 s69, s69, 2097152\n\tv_mfma_f32_16x16x32_bf16 v[88:91], v[24:27], a[136:139], v[88:91]\n\ts_add_u32 m0, s54, 49152\n\ts_waitcnt lgkmcnt(8)\n\tv_mfma_f32_16x16x32_bf16 v[76:79], v[28:31], a[128:131], v[76:79]\n\tds_read_b64_tr_b16 v[16:17], v237 offset:53248\n\tds_read_b64_tr_b16 v[18:19], v237 offset:53760\n\tv_mfma_f32_16x16x32_bf16 v[84:87], v[28:31], a[132:135], v[84:87]\n\t" \
    "buffer_load_dwordx4 v12, s[16:19], s69 offen lds\n\ts_cmp_le_u32 s4, 8\n\ts_cselect_b32 s69, 262144, 0\n\ts_add_u32 s69, s69, 2097152\n\tv_mfma_f32_16x16x32_bf16 v[92:95], v[28:31], a[136:139], v[92:95]\n\ts_add_u32 m0, s54, 53248\n\ts_waitcnt lgkmcnt(8)\n\tv_mfma_f32_16x16x32_bf16 v[72:75], v[32:35], a[140:143], v[72:75]\n\t" \
    "ds_read_b64_tr_b16 v[20:21], v238 offset:53248\n\tds_read_b64_tr_b16 v[22:23], v238 offset:53760\n\tv_mfma_f32_16x16x32_bf16 v[80:83], v[32:35], a[144:147], v[80:83]\n\tv_mfma_f32_16x16x32_bf16 v[88:91], v[32:35], a[148:151], v[88:91]\n\ts_waitcnt lgkmcnt(8)\n\tv_mfma_f32_16x16x32_bf16 v[76:79], v[36:39], a[140:143], v[76:79]\n\tds_read_b64_tr_b16 v[24:25], v237 offset:57344\n\tds_read_b64_tr_b16 v[26:27], v237 offset:57856\n\t" \
    "v_mfma_f32_16x16x32_bf16 v[84:87], v[36:39], a[144:147], v[84:87]\n\tbuffer_load_dwordx4 v13, s[16:19], s69 offen lds\n\ts_cmp_le_u32 s4, 9\n\ts_cselect_b32 s69, 262144, 0\n\ts_add_u32 s69, s69, 2359296\n\tv_mfma_f32_16x16x32_bf16 v[92:95], v[36:39], a[148:151], v[92:95]\n\ts_add_u32 m0, s54, 57344\n\ts_waitcnt lgkmcnt(8)\n\t" \
    "v_mfma_f32_16x16x32_bf16 v[72:75], v[40:43], a[152:155], v[72:75]\n\tds_read_b64_tr_b16 v[28:29], v238 offset:57344\n\tds_read_b64_tr_b16 v[30:31], v238 offset:57856\n\tv_mfma_f32_16x16x32_bf16 v[80:83], v[40:43], a[156:159], v[80:83]\n\tbuffer_load_dwordx4 v12, s[16:19], s69 offen lds\n\tv_mfma_f32_16x16x32_bf16 v[88:91], v[40:43], a[160:163], v[88:91]\n\ts_waitcnt lgkmcnt(8)\n\tv_mfma_f32_16x16x32_bf16 v[76:79], v[44:47], a[152:155], v[76:79]\n\t" \
    "ds_read_b64_tr_b16 v[32:33], v237 offset:61440\n\tds_read_b64_tr_b16 v[34:35], v237 offset:61952\n\tv_mfma_f32_16x16x32_bf16 v[84:87], v[44:47], a[156:159], v[84:87]\n\tv_mfma_f32_16x16x32_bf16 v[92:95], v[44:47], a[160:163], v[92:95]\n\ts_waitcnt lgkmcnt(8)\n\tv_mfma_f32_16x16x32_bf16 v[72:75], v[16:19], a[164:167], v[72:75]\n\tds_read_b64_tr_b16 v[36:37], v238 offset:61440\n\tds_read_b64_tr_b16 v[38:39], v238 offset:61952\n\t" \
    "v_mfma_f32_16x16x32_bf16 v[80:83], v[16:19], a[168:171], v[80:83]\n\tv_mfma_f32_16x16x32_bf16 v[88:91], v[16:19], a[172:175], v[88:91]\n\ts_waitcnt lgkmcnt(8)\n\tv_mfma_f32_16x16x32_bf16 v[76:79], v[20:23], a[164:167], v[76:79]\n\tds_read_b64_tr_b16 v[40:41], v2 offset:0\n\tds_read_b64_tr_b16 v[42:43], v2 offset:512\n\tv_mfma_f32_16x16x32_bf16 v[84:87], v[20:23], a[168:171], v[84:87]\n\tv_mfma_f32_16x16x32_bf16 v[92:95], v[20:23], a[172:175], v[92:95]\n\t" \
    "buffer_load_dwordx2 v[250:251], v241, s[8:11], s50 offen sc1\n\ts_waitcnt lgkmcnt(8)\n\tv_mfma_f32_16x16x32_bf16 v[72:75], v[24:27], a[176:179], v[72:75]\n\tds_read_b64_tr_b16 v[44:45], v3 offset:0\n\tds_read_b64_tr_b16 v[46:47], v3 offset:512\n\tv_mfma_f32_16x16x32_bf16 v[80:83], v[24:27], a[180:183], v[80:83]\n\tv_mfma_f32_16x16x32_bf16 v[88:91], v[24:27], a[184:187], v[88:91]\n\ts_waitcnt lgkmcnt(8)\n\t" \
    "v_mfma_f32_16x16x32_bf16 v[76:79], v[28:31], a[176:179], v[76:79]\n\tds_read_b64_tr_b16 v[16:17], v220 offset:0\n\tds_read_b64_tr_b16 v[18:19], v220 offset:2048\n\tv_mfma_f32_16x16x32_bf16 v[84:87], v[28:31], a[180:183], v[84:87]\n\tv_mfma_f32_16x16x32_bf16 v[92:95], v[28:31], a[184:187], v[92:95]\n\ts_waitcnt lgkmcnt(8)\n\tv_mfma_f32_16x16x32_bf16 v[72:75], v[32:35], a[188:191], v[72:75]\n\tds_read_b64_tr_b16 v[20:21], v221 offset:0\n\t" \
    "ds_read_b64_tr_b16 v[22:23], v221 offset:2048\n\tv_mfma_f32_16x16x32_bf16 v[80:83], v[32:35], a[192:195], v[80:83]\n\tv_mfma_f32_16x16x32_bf16 v[88:91], v[32:35], a[196:199], v[88:91]\n\ts_waitcnt lgkmcnt(8)\n\tv_mfma_f32_16x16x32_bf16 v[76:79], v[36:39], a[188:191], v[76:79]\n\tds_read_b64_tr_b16 v[24:25], v222 offset:0\n\tds_read_b64_tr_b16 v[26:27], v222 offset:2048\n\tv_mfma_f32_16x16x32_bf16 v[84:87], v[36:39], a[192:195], v[84:87]\n\t" \
    "v_mfma_f32_16x16x32_bf16 v[92:95], v[36:39], a[196:199], v[92:95]\n\ts_waitcnt lgkmcnt(8)\n\tv_mfma_f32_16x16x32_bf16 v[72:75], v[40:43], a[200:203], v[72:75]\n\tds_read_b64_tr_b16 v[28:29], v223 offset:0\n\tds_read_b64_tr_b16 v[30:31], v223 offset:2048\n\tv_mfma_f32_16x16x32_bf16 v[80:83], v[40:43], a[204:207], v[80:83]\n\tv_mfma_f32_16x16x32_bf16 v[88:91], v[40:43], a[208:211], v[88:91]\n\ts_waitcnt lgkmcnt(8)\n\t" \
    "v_mfma_f32_16x16x32_bf16 v[76:79], v[44:47], a[200:203], v[76:79]\n\tds_read_b64_tr_b16 v[32:33], v220 offset:4096\n\tds_read_b64_tr_b16 v[34:35], v220 offset:6144\n\tv_mfma_f32_16x16x32_bf16 v[84:87], v[44:47], a[204:207], v[84:87]\n\tv_mfma_f32_16x16x32_bf16 v[92:95], v[44:47], a[208:211], v[92:95]\n\ts_mov_b32 s71, 1048576\n\ts_waitcnt vmcnt(5)\n\t109:\n\t" \
    "v_add3_u32 v158, s36, v144, v145\n\tv_add3_u32 v159, v146, v147, v148\n\tv_add3_u32 v158, v158, v149, v150\n\tv_add3_u32 v159, v159, v151, v152\n\tv_add3_u32 v158, v158, v153, v154\n\tv_add3_u32 v158, v158, v159, v155\n\tv_cmp_ne_u32 vcc, v158, v156\n\ts_cbranch_vccz 110f\n\t" \
    "s_sub_u32 s71, s71, 1\n\ts_cbranch_scc1 110f\n\ts_sleep 6\n\ts_add_u32 s73, s72, 3328\n\tbuffer_load_dwordx4 v[144:147], v8, s[8:11], s73 offen offset:0 sc1\n\tbuffer_load_dwordx4 v[148:151], v8, s[8:11], s73 offen offset:1024 sc1\n\tbuffer_load_dwordx4 v[152:155], v8, s[8:11], s73 offen offset:2048 sc1\n\tbuffer_load_dword v156, v236, s[8:11], s73 offen offset:3072 sc1\n\t" \
    "s_waitcnt vmcnt(0)\n\ts_branch 109b\n\t110:\n\ts_add_u32 s45, s45, 55296\n\tv_lshlrev_b32 v96, 16, v144\n\tv_and_b32 v97, 0xffff0000, v144\n\tv_lshlrev_b32 v98, 16, v145\n\tv_and_b32 v99, 0xffff0000, v145\n\t" \
    "v_lshlrev_b32 v100, 16, v146\n\tv_and_b32 v101, 0xffff0000, v146\n\tv_lshlrev_b32 v102, 16, v147\n\tv_and_b32 v103, 0xffff0000, v147\n\tv_pk_add_f32 v[72:73], v[72:73], v[96:97]\n\tv_pk_add_f32 v[74:75], v[74:75], v[98:99]\n\tv_pk_add_f32 v[76:77], v[76:77], v[100:101]\n\tv_pk_add_f32 v[78:79], v[78:79], v[102:103]\n\t" \
    "v_pk_mul_f32 v[96:97], v[72:73], v[72:73]\n\tv_pk_mul_f32 v[98:99], v[74:75], v[74:75]\n\tv_pk_mul_f32 v[100:101], v[76:77], v[76:77]\n\tv_pk_mul_f32 v[102:103], v[78:79], v[78:79]\n\tv_pk_fma_f32 v[96:97], v[96:97], v[246:247], v[246:247] op_sel:[0,0,1] op_sel_hi:[1,0,1]\n\tv_pk_fma_f32 v[98:99], v[98:99], v[246:247], v[246:247] op_sel:[0,0,1] op_sel_hi:[1,0,1]\n\tv_pk_fma_f32 v[100:101], v[100:101], v[246:247], v[246:247] op_sel:[0,0,1] op_sel_hi:[1,0,1]\n\tv_pk_fma_f32 v[102:103], v[102:103], v[246:247], v[246:247] op_sel:[0,0,1] op_sel_hi:[1,0,1]\n\t" \
    "v_pk_mul_f32 v[96:97], v[96:97], v[72:73]\n\tv_pk_mul_f32 v[98:99], v[98:99], v[74:75]\n\tv_pk_mul_f32 v[100:101], v[100:101], v[76:77]\n\tv_pk_mul_f32 v[102:103], v[102:103], v[78:79]\n\tv_lshlrev_b32 v104, 16, v148\n\tv_exp_f32 v96, v96\n\tv_and_b32 v105, 0xffff0000, v148\n\tv_exp_f32 v97, v97\n\t" \
    "v_lshlrev_b32 v106, 16, v149\n\tv_exp_f32 v98, v98\n\tv_and_b32 v107, 0xffff0000, v149\n\tv_exp_f32 v99, v99\n\tv_lshlrev_b32 v108, 16, v150\n\tv_exp_f32 v100, v100\n\tv_and_b32 v109, 0xffff0000, v150\n\tv_exp_f32 v101, v101\n\t" \
    "v_lshlrev_b32 v110, 16, v151\n\tv_exp_f32 v102, v102\n\tv_and_b32 v111, 0xffff0000, v151\n\tv_exp_f32 v103, v103\n\tv_pk_add_f32 v[80:81], v[80:81], v[104:105]\n\tv_pk_add_f32 v[96:97], v[96:97], 1.0 op_sel_hi:[1,0]\n\tv_pk_add_f32 v[98:99], v[98:99], 1.0 op_sel_hi:[1,0]\n\tv_pk_add_f32 v[100:101], v[100:101], 1.0 op_sel_hi:[1,0]\n\t" \
    "v_pk_add_f32 v[102:103], v[102:103], 1.0 op_sel_hi:[1,0]\n\tv_pk_add_f32 v[82:83], v[82:83], v[106:107]\n\tv_rcp_f32 v96, v96\n\tv_pk_add_f32 v[84:85], v[84:85], v[108:109]\n\tv_rcp_f32 v97, v97\n\tv_pk_add_f32 v[86:87], v[86:87], v[110:111]\n\tv_rcp_f32 v98, v98\n\tv_pk_mul_f32 v[104:105], v[80:81], v[80:81]\n\t" \
    "v_rcp_f32 v99, v99\n\tv_pk_mul_f32 v[106:107], v[82:83], v[82:83]\n\tv_rcp_f32 v100, v100\n\tv_pk_mul_f32 v[108:109], v[84:85], v[84:85]\n\tv_rcp_f32 v101, v101\n\tv_pk_mul_f32 v[110:111], v[86:87], v[86:87]\n\tv_rcp_f32 v102, v102\n\tv_pk_fma_f32 v[104:105], v[104:105], v[246:247], v[246:247] op_sel:[0,0,1] op_sel_hi:[1,0,1]\n\t" \
    "v_rcp_f32 v103, v103\n\tv_pk_fma_f32 v[106:107], v[106:107], v[246:247], v[246:247] op_sel:[0,0,1] op_sel_hi:[1,0,1]\n\tv_pk_mul_f32 v[96:97], v[96:97], v[72:73]\n\tv_pk_mul_f32 v[98:99], v[98:99], v[74:75]\n\tv_pk_mul_f32 v[100:101], v[100:101], v[76:77]\n\tv_pk_mul_f32 v[102:103], v[102:103], v[78:79]\n\tv_cvt_pk_bf16_f32 v72, v96, v97\n\tv_cvt_pk_bf16_f32 v73, v98, v99\n\t" \
    "v_cvt_pk_bf16_f32 v74, v100, v101\n\tv_cvt_pk_bf16_f32 v75, v102, v103\n\ts_cmp_lg_u32 s39, 2\n\ts_cbranch_scc1 111f\n\ts_waitcnt vmcnt(0)\n\tv_cmp_ne_u64 vcc, s[36:37], v[250:251]\n\ts_cbranch_vccz 113f\n\ts_mov_b32 s71, 1048576\n\t" \
    "112:\n\ts_sleep 4\n\tbuffer_load_dwordx2 v[250:251], v241, s[8:11], s50 offen sc1\n\ts_waitcnt vmcnt(0)\n\tv_cmp_ne_u64 vcc, s[36:37], v[250:251]\n\ts_cbranch_vccz 113f\n\ts_sub_u32 s71, s71, 1\n\ts_cbranch_scc0 112b\n\t" \
    "113:\n\t111:\n\ts_cmp_le_u32 s4, 0\n\ts_cselect_b32 s68, 3072, 0\n\ts_add_u32 s68, s68, s46\n\ts_add_u32 s68, s68, 0\n\tbuffer_load_dwordx4 v[204:207], v8, s[8:11], s68 offen offset:0 sc1\n\tbuffer_load_dwordx4 v[208:211], v8, s[8:11], s68 offen offset:1024 sc1\n\t" \
    "buffer_load_dwordx4 v[212:215], v8, s[8:11], s68 offen offset:2048 sc1\n\tv_pk_fma_f32 v[108:109], v[108:109], v[246:247], v[246:247] op_sel:[0,0,1] op_sel_hi:[1,0,1]\n\ts_cmp_le_u32 s4, 1\n\ts_cselect_b32 s68, 3072, 0\n\tv_pk_fma_f32 v[110:111], v[110:111], v[246:247], v[246:247] op_sel:[0,0,1] op_sel_hi:[1,0,1]\n\ts_add_u32 s68, s68, s46\n\tv_pk_mul_f32 v[104:105], v[104:105], v[80:81]\n\ts_add_u32 s68, s68, 3072\n\t" \
    "v_pk_mul_f32 v[106:107], v[106:107], v[82:83]\n\tv_pk_mul_f32 v[108:109], v[108:109], v[84:85]\n\tv_pk_mul_f32 v[110:111], v[110:111], v[86:87]\n\tv_lshlrev_b32 v112, 16, v152\n\tv_exp_f32 v104, v104\n\tv_and_b32 v113, 0xffff0000, v152\n\tv_exp_f32 v105, v105\n\tv_lshlrev_b32 v114, 16, v153\n\t" \
    "v_exp_f32 v106, v106\n\tv_and_b32 v115, 0xffff0000, v153\n\tv_exp_f32 v107, v107\n\tv_lshlrev_b32 v116, 16, v154\n\tbuffer_load_dwordx4 v[216:219], v8, s[8:11], s68 offen offset:0 sc1\n\tv_exp_f32 v108, v108\n\tv_and_b32 v117, 0xffff0000, v154\n\tv_exp_f32 v109, v109\n\t" \
    "v_lshlrev_b32 v118, 16, v155\n\tv_exp_f32 v110, v110\n\tv_and_b32 v119, 0xffff0000, v155\n\tv_exp_f32 v111, v111\n\tv_pk_add_f32 v[88:89], v[88:89], v[112:113]\n\tv_pk_add_f32 v[104:105], v[104:105], 1.0 op_sel_hi:[1,0]\n\tv_pk_add_f32 v[106:107], v[106:107], 1.0 op_sel_hi:[1,0]\n\tv_pk_add_f32 v[108:109], v[108:109], 1.0 op_sel_hi:[1,0]\n\t" \
    "buffer_load_dwordx4 a[0:3], v8, s[8:11], s68 offen offset:1024 sc1\n\tv_pk_add_f32 v[110:111], v[110:111], 1.0 op_sel_hi:[1,0]\n\tv_pk_add_f32 v[90:91], v[90:91], v[114:115]\n\tv_rcp_f32 v104, v104\n\tv_pk_add_f32 v[92:93], v[92:93], v[116:117]\n\tv_rcp_f32 v105, v105\n\tv_pk_add_f32 v[94:95], v[94:95], v[118:119]\n\tv_rcp_f32 v106, v106\n\t" \
    "v_pk_mul_f32 v[112:113], v[88:89], v[88:89]\n\tv_rcp_f32 v107, v107\n\tv_pk_mul_f32 v[114:115], v[90:91], v[90:91]\n\tv_rcp_f32 v108, v108\n\tbuffer_load_dwordx4 a[4:7], v8, s[8:11], s68 offen offset:2048 sc1\n\tv_pk_mul_f32 v[116:117], v[92:93], v[92:93]\n\ts_cmp_le_u32 s4, 2\n\ts_cselect_b32 s68, 3072, 0\n\t" \
    "v_rcp_f32 v109, v109\n\ts_add_u32 s68, s68, s46\n\tv_pk_mul_f32 v[118:119], v[94:95], v[94:95]\n\ts_add_u32 s68, s68, 6144\n\tv_rcp_f32 v110, v110\n\tv_pk_fma_f32 v[112:113], v[112:113], v[246:247], v[246:247] op_sel:[0,0,1] op_sel_hi:[1,0,1]\n\tv_rcp_f32 v111, v111\n\tv_pk_fma_f32 v[114:115], v[114:115], v[246:247], v[246:247] op_sel:[0,0,1] op_sel_hi:[1,0,1]\n\t" \
    "v_pk_mul_f32 v[104:105], v[104:105], v[80:81]\n\tbuffer_load_dwordx4 a[8:11], v8, s[8:11], s68 offen offset:0 sc1\n\tv_pk_mul_f32 v[106:107], v[106:107], v[82:83]\n\tv_pk_mul_f32 v[108:109], v[108:109], v[84:85]\n\tv_pk_mul_f32 v[110:111], v[110:111], v[86:87]\n\tv_cvt_pk_bf16_f32 v80, v104, v105\n\tv_cvt_pk_bf16_f32 v81, v106, v107\n\tv_cvt_pk_bf16_f32 v82, v108, v109\n\t" \
    "v_cvt_pk_bf16_f32 v83, v110, v111\n\tv_pk_fma_f32 v[116:117], v[116:117], v[246:247], v[246:247] op_sel:[0,0,1] op_sel_hi:[1,0,1]\n\tv_pk_fma_f32 v[118:119], v[118:119], v[246:247], v[246:247] op_sel:[0,0,1] op_sel_hi:[1,0,1]\n\tv_pk_mul_f32 v[112:113], v[112:113], v[88:89]\n\tv_pk_mul_f32 v[114:115], v[114:115], v[90:91]\n\tv_pk_mul_f32 v[116:117], v[116:117], v[92:93]\n\tv_pk_mul_f32 v[118:119], v[118:119], v[94:95]\n\tv_exp_f32 v112, v112\n\t" \
    "v_exp_f32 v113, v113\n\tbuffer_load_dwordx4 a[12:15], v8, s[8:11], s68 offen offset:1024 sc1\n\tv_exp_f32 v114, v114\n\tv_exp_f32 v115, v115\n\tv_exp_f32 v116, v116\n\tv_exp_f32 v117, v117\n\tv_exp_f32 v118, v118\n\tv_exp_f32 v119, v119\n\t" \
    "v_pk_add_f32 v[112:113], v[112:113], 1.0 op_sel_hi:[1,0]\n\tv_pk_add_f32 v[114:115], v[114:115], 1.0 op_sel_hi:[1,0]\n\tbuffer_load_dwordx4 a[16:19], v8, s[8:11], s68 offen offset:2048 sc1\n\tv_pk_add_f32 v[116:117], v[116:117], 1.0 op_sel_hi:[1,0]\n\ts_cmp_le_u32 s4, 3\n\ts_cselect_b32 s68, 3072, 0\n\tv_pk_add_f32 v[118:119], v[118:119], 1.0 op_sel_hi:[1,0]\n\ts_add_u32 s68, s68, s46\n\t" \
    "v_rcp_f32 v112, v112\n\ts_add_u32 s68, s68, 9216\n\tv_rcp_f32 v113, v113\n\tv_rcp_f32 v114, v114\n\tv_rcp_f32 v115, v115\n\tv_rcp_f32 v116, v116\n\tbuffer_load_dwordx4 a[20:23], v8, s[8:11], s68 offen offset:0 sc1\n\tv_rcp_f32 v117, v117\n\t" \
    "v_rcp_f32 v118, v118\n\tv_rcp_f32 v119, v119\n\tv_pk_mul_f32 v[112:113], v[112:113], v[88:89]\n\tv_pk_mul_f32 v[114:115], v[114:115], v[90:91]\n\tv_pk_mul_f32 v[116:117], v[116:117], v[92:93]\n\tv_pk_mul_f32 v[118:119], v[118:119], v[94:95]\n\tv_cvt_pk_bf16_f32 v88, v112, v113\n\tv_cvt_pk_bf16_f32 v89, v114, v115\n\t" \
    "v_cvt_pk_bf16_f32 v90, v116, v117\n\tv_cvt_pk_bf16_f32 v91, v118, v119\n\ts_cmp_lg_u32 s39, 2\n\ts_cbranch_scc1 102f\n\ts_barrier\n\ts_waitcnt lgkmcnt(8)\n\tv_mfma_f32_16x16x32_bf16 v[120:123], v[192:195], v[16:19], 0\n\tds_read_b64_tr_b16 v[36:37], v221 offset:4096\n\t" \
    "ds_read_b64_tr_b16 v[38:39], v221 offset:6144\n\tv_mfma_f32_16x16x32_bf16 v[128:131], v[196:199], v[16:19], 0\n\tbuffer_load_dwordx4 a[24:27], v8, s[8:11], s68 offen offset:1024 sc1\n\tv_mfma_f32_16x16x32_bf16 v[136:139], v[200:203], v[16:19], 0\n\ts_waitcnt lgkmcnt(8)\n\tv_mfma_f32_16x16x32_bf16 v[124:127], v[192:195], v[20:23], 0\n\tds_read_b64_tr_b16 v[40:41], v222 offset:4096\n\tds_read_b64_tr_b16 v[42:43], v222 offset:6144\n\t" \
    "v_mfma_f32_16x16x32_bf16 v[132:135], v[196:199], v[20:23], 0\n\tbuffer_load_dwordx4 a[28:31], v8, s[8:11], s68 offen offset:2048 sc1\n\ts_cmp_le_u32 s4, 4\n\ts_cselect_b32 s68, 3072, 0\n\ts_add_u32 s68, s68, s46\n\tv_mfma_f32_16x16x32_bf16 v[140:143], v[200:203], v[20:23], 0\n\ts_add_u32 s68, s68, 12288\n\ts_waitcnt lgkmcnt(8)\n\t" \
    "v_mfma_f32_16x16x32_bf16 v[144:147], v[192:195], v[24:27], 0\n\tds_read_b64_tr_b16 v[44:45], v223 offset:4096\n\tds_read_b64_tr_b16 v[46:47], v223 offset:6144\n\tv_mfma_f32_16x16x32_bf16 v[152:155], v[196:199], v[24:27], 0\n\tbuffer_load_dwordx4 a[32:35], v8, s[8:11], s68 offen offset:0 sc1\n\tv_mfma_f32_16x16x32_bf16 v[160:163], v[200:203], v[24:27], 0\n\ts_waitcnt lgkmcnt(8)\n\tv_mfma_f32_16x16x32_bf16 v[148:151], v[192:195], v[28:31], 0\n\t" \
    "ds_read_b64_tr_b16 v[16:17], v220 offset:8192\n\tds_read_b64_tr_b16 v[18:19], v220 offset:10240\n\tv_mfma_f32_16x16x32_bf16 v[156:159], v[196:199], v[28:31], 0\n\tbuffer_load_dwordx4 a[36:39], v8, s[8:11], s68 offen offset:1024 sc1\n\tv_mfma_f32_16x16x32_bf16 v[164:167], v[200:203], v[28:31], 0\n\ts_waitcnt lgkmcnt(8)\n\tv_mfma_f32_16x16x32_bf16 v[168:171], v[192:195], v[32:35], 0\n\tds_read_b64_tr_b16 v[20:21], v221 offset:8192\n\t" \
    "ds_read_b64_tr_b16 v[22:23], v221 offset:10240\n\tv_mfma_f32_16x16x32_bf16 v[176:179], v[196:199], v[32:35], 0\n\tbuffer_load_dwordx4 a[40:43], v8, s[8:11], s68 offen offset:2048 sc1\n\tv_mfma_f32_16x16x32_bf16 v[184:187], v[200:203], v[32:35], 0\n\ts_waitcnt lgkmcnt(8)\n\tv_mfma_f32_16x16x32_bf16 v[172:175], v[192:195], v[36:39], 0\n\tds_read_b64_tr_b16 v[24:25], v222 offset:8192\n\tds_read_b64_tr_b16 v[26:27], v222 offset:10240\n\t" \
    "v_mfma_f32_16x16x32_bf16 v[180:183], v[196:199], v[36:39], 0\n\tv_mfma_f32_16x16x32_bf16 v[188:191], v[200:203], v[36:39], 0\n\ts_waitcnt lgkmcnt(8)\n\tv_mfma_f32_16x16x32_bf16 v[48:51], v[192:195], v[40:43], v[228:231]\n\tds_read_b64_tr_b16 v[28:29], v223 offset:8192\n\tds_read_b64_tr_b16 v[30:31], v223 offset:10240\n\tv_mfma_f32_16x16x32_bf16 v[56:59], v[196:199], v[40:43], v[228:231]\n\tbuffer_store_dwordx4 v[72:75], v8, s[8:11], s45 offen offset:0 sc1\n\t" \
    "v_mfma_f32_16x16x32_bf16 v[64:67], v[200:203], v[40:43], v[228:231]\n\ts_waitcnt lgkmcnt(8)\n\tv_mfma_f32_16x16x32_bf16 v[52:55], v[192:195], v[44:47], v[232:235]\n\tds_read_b64_tr_b16 v[32:33], v220 offset:12288\n\tds_read_b64_tr_b16 v[34:35], v220 offset:14336\n\tv_mfma_f32_16x16x32_bf16 v[60:63], v[196:199], v[44:47], v[232:235]\n\tbuffer_store_dwordx4 v[80:83], v8, s[8:11], s45 offen offset:1024 sc1\n\tv_mfma_f32_16x16x32_bf16 v[68:71], v[200:203], v[44:47], v[232:235]\n\t" \
    "s_waitcnt lgkmcnt(8)\n\tv_mfma_f32_16x16x32_bf16 v[120:123], v[72:75], v[16:19], v[120:123]\n\tds_read_b64_tr_b16 v[36:37], v221 offset:12288\n\tds_read_b64_tr_b16 v[38:39], v221 offset:14336\n\tv_mfma_f32_16x16x32_bf16 v[128:131], v[80:83], v[16:19], v[128:131]\n\tbuffer_store_dwordx4 v[88:91], v8, s[8:11], s45 offen offset:2048 sc1\n\ts_cmp_le_u32 s4, 5\n\ts_cselect_b32 s68, 3072, 0\n\t" \
    "s_add_u32 s68, s68, s46\n\tv_mfma_f32_16x16x32_bf16 v[136:139], v[88:91], v[16:19], v[136:139]\n\ts_add_u32 s68, s68, 15360\n\ts_waitcnt lgkmcnt(8)\n\tv_mfma_f32_16x16x32_bf16 v[124:127], v[72:75], v[20:23], v[124:127]\n\tds_read_b64_tr_b16 v[40:41], v222 offset:12288\n\tds_read_b64_tr_b16 v[42:43], v222 offset:14336\n\tv_mfma_f32_16x16x32_bf16 v[132:135], v[80:83], v[20:23], v[132:135]\n\t" \
    "v_mfma_f32_16x16x32_bf16 v[140:143], v[88:91], v[20:23], v[140:143]\n\ts_waitcnt lgkmcnt(8)\n\tv_mfma_f32_16x16x32_bf16 v[144:147], v[72:75], v[24:27], v[144:147]\n\tds_read_b64_tr_b16 v[44:45], v223 offset:12288\n\tds_read_b64_tr_b16 v[46:47], v223 offset:14336\n\tv_mfma_f32_16x16x32_bf16 v[152:155], v[80:83], v[24:27], v[152:155]\n\tbuffer_load_dwordx4 a[44:47], v8, s[8:11], s68 offen offset:0 sc1\n\tv_mfma_f32_16x16x32_bf16 v[160:163], v[88:91], v[24:27], v[160:163]\n\t" \
    "s_waitcnt lgkmcnt(8)\n\tv_mfma_f32_16x16x32_bf16 v[148:151], v[72:75], v[28:31], v[148:151]\n\tds_read_b64_tr_b16 v[16:17], v220 offset:16384\n\tds_read_b64_tr_b16 v[18:19], v220 offset:18432\n\tv_mfma_f32_16x16x32_bf16 v[156:159], v[80:83], v[28:31], v[156:159]\n\tbuffer_load_dwordx4 a[48:51], v8, s[8:11], s68 offen offset:1024 sc1\n\tv_mfma_f32_16x16x32_bf16 v[164:167], v[88:91], v[28:31], v[164:167]\n\ts_waitcnt lgkmcnt(8)\n\t" \
    "v_mfma_f32_16x16x32_bf16 v[168:171], v[72:75], v[32:35], v[168:171]\n\tds_read_b64_tr_b16 v[20:21], v221 offset:16384\n\tds_read_b64_tr_b16 v[22:23], v221 offset:18432\n\tv_mfma_f32_16x16x32_bf16 v[176:179], v[80:83], v[32:35], v[176:179]\n\tv_mfma_f32_16x16x32_bf16 v[184:187], v[88:91], v[32:35], v[184:187]\n\ts_waitcnt lgkmcnt(8)\n\tv_mfma_f32_16x16x32_bf16 v[172:175], v[72:75], v[36:39], v[172:175]\n\tds_read_b64_tr_b16 v[24:25], v222 offset:16384\n\t" \
    "ds_read_b64_tr_b16 v[26:27], v222 offset:18432\n\tv_mfma_f32_16x16x32_bf16 v[180:183], v[80:83], v[36:39], v[180:183]\n\tbuffer_load_dwordx4 a[52:55], v8, s[8:11], s68 offen offset:2048 sc1\n\ts_cmp_le_u32 s4, 6\n\ts_cselect_b32 s68, 3072, 0\n\ts_add_u32 s68, s68, s46\n\tv_mfma_f32_16x16x32_bf16 v[188:191], v[88:91], v[36:39], v[188:191]\n\ts_add_u32 s68, s68, 18432\n\t" \
    "s_waitcnt lgkmcnt(8)\n\tv_mfma_f32_16x16x32_bf16 v[48:51], v[72:75], v[40:43], v[48:51]\n\tds_read_b64_tr_b16 v[28:29], v223 offset:16384\n\tds_read_b64_tr_b16 v[30:31], v223 offset:18432\n\tv_mfma_f32_16x16x32_bf16 v[56:59], v[80:83], v[40:43], v[56:59]\n\tbuffer_load_dwordx4 a[56:59], v8, s[8:11], s68 offen offset:0 sc1\n\tv_mfma_f32_16x16x32_bf16 v[64:67], v[88:91], v[40:43], v[64:67]\n\ts_waitcnt lgkmcnt(8)\n\t" \
    "v_mfma_f32_16x16x32_bf16 v[52:55], v[72:75], v[44:47], v[52:55]\n\tds_read_b64_tr_b16 v[32:33], v220 offset:20480\n\tds_read_b64_tr_b16 v[34:35], v220 offset:22528\n\tv_mfma_f32_16x16x32_bf16 v[60:63], v[80:83], v[44:47], v[60:63]\n\tbuffer_load_dwordx4 a[60:63], v8, s[8:11], s68 offen offset:1024 sc1\n\tv_mfma_f32_16x16x32_bf16 v[68:71], v[88:91], v[44:47], v[68:71]\n\ts_barrier\n\ts_waitcnt vmcnt(20) lgkmcnt(8)\n\t" \
    "v_mfma_f32_16x16x32_bf16 v[120:123], v[204:207], v[16:19], v[120:123]\n\tds_read_b64_tr_b16 v[36:37], v221 offset:20480\n\tds_read_b64_tr_b16 v[38:39], v221 offset:22528\n\tv_mfma_f32_16x16x32_bf16 v[128:131], v[208:211], v[16:19], v[128:131]\n\tbuffer_load_dwordx4 a[64:67], v8, s[8:11], s68 offen offset:2048 sc1\n\ts_cmp_le_u32 s4, 7\n\ts_cselect_b32 s68, 3072, 0\n\ts_add_u32 s68, s68, s46\n\t" \
    "v_mfma_f32_16x16x32_bf16 v[136:139], v[212:215], v[16:19], v[136:139]\n\ts_add_u32 s68, s68, 21504\n\ts_waitcnt lgkmcnt(8)\n\tv_mfma_f32_16x16x32_bf16 v[124:127], v[204:207], v[20:23], v[124:127]\n\tds_read_b64_tr_b16 v[40:41], v222 offset:20480\n\tds_read_b64_tr_b16 v[42:43], v222 offset:22528\n\tv_mfma_f32_16x16x32_bf16 v[132:135], v[208:211], v[20:23], v[132:135]\n\tbuffer_load_dwordx4 a[68:71], v8, s[8:11], s68 offen offset:0 sc1\n\t" \
    "v_mfma_f32_16x16x32_bf16 v[140:143], v[212:215], v[20:23], v[140:143]\n\ts_waitcnt lgkmcnt(8)\n\tv_mfma_f32_16x16x32_bf16 v[144:147], v[204:207], v[24:27], v[144:147]\n\tds_read_b64_tr_b16 v[44:45], v223 offset:20480\n\tds_read_b64_tr_b16 v[46:47], v223 offset:22528\n\tv_mfma_f32_16x16x32_bf16 v[152:155], v[208:211], v[24:27], v[152:155]\n\tv_mfma_f32_16x16x32_bf16 v[160:163], v[212:215], v[24:27], v[160:163]\n\ts_waitcnt lgkmcnt(8)\n\t" \
    "v_mfma_f32_16x16x32_bf16 v[148:151], v[204:207], v[28:31], v[148:151]\n\tds_read_b64_tr_b16 v[16:17], v220 offset:24576\n\tds_read_b64_tr_b16 v[18:19], v220 offset:26624\n\tv_mfma_f32_16x16x32_bf16 v[156:159], v[208:211], v[28:31], v[156:159]\n\tbuffer_load_dwordx4 a[72:75], v8, s[8:11], s68 offen offset:1024 sc1\n\tv_mfma_f32_16x16x32_bf16 v[164:167], v[212:215], v[28:31], v[164:167]\n\ts_waitcnt lgkmcnt(8)\n\tv_mfma_f32_16x16x32_bf16 v[168:171], v[204:207], v[32:35], v[168:171]\n\t" \
    "ds_read_b64_tr_b16 v[20:21], v221 offset:24576\n\tds_read_b64_tr_b16 v[22:23], v221 offset:26624\n\tv_mfma_f32_16x16x32_bf16 v[176:179], v[208:211], v[32:35], v[176:179]\n\tbuffer_load_dwordx4 a[76:79], v8, s[8:11], s68 offen offset:2048 sc1\n\ts_cmp_le_u32 s4, 9\n\ts_cselect_b32 s69, 262144, 0\n\ts_add_u32 s69, s69, 2359296\n\tv_mfma_f32_16x16x32_bf16 v[184:187], v[212:215], v[32:35], v[184:187]\n\t" \
    "s_add_u32 m0, s54, 131072\n\ts_waitcnt lgkmcnt(8)\n\tv_mfma_f32_16x16x32_bf16 v[172:175], v[204:207], v[36:39], v[172:175]\n\tds_read_b64_tr_b16 v[24:25], v222 offset:24576\n\tds_read_b64_tr_b16 v[26:27], v222 offset:26624\n\tv_mfma_f32_16x16x32_bf16 v[180:183], v[208:211], v[36:39], v[180:183]\n\tbuffer_load_dwordx4 v13, s[16:19], s69 offen lds\n\ts_cmp_le_u32 s4, 10\n\t" \
    "s_cselect_b32 s69, 262144, 0\n\ts_add_u32 s69, s69, 2621440\n\tv_mfma_f32_16x16x32_bf16 v[188:191], v[212:215], v[36:39], v[188:191]\n\ts_add_u32 m0, s54, 135168\n\ts_waitcnt lgkmcnt(8)\n\tv_mfma_f32_16x16x32_bf16 v[48:51], v[204:207], v[40:43], v[48:51]\n\tds_read_b64_tr_b16 v[28:29], v223 offset:24576\n\tds_read_b64_tr_b16 v[30:31], v223 offset:26624\n\t" \
    "v_mfma_f32_16x16x32_bf16 v[56:59], v[208:211], v[40:43], v[56:59]\n\tv_mfma_f32_16x16x32_bf16 v[64:67], v[212:215], v[40:43], v[64:67]\n\ts_waitcnt lgkmcnt(8)\n\tv_mfma_f32_16x16x32_bf16 v[52:55], v[204:207], v[44:47], v[52:55]\n\tds_read_b64_tr_b16 v[32:33], v220 offset:28672\n\tds_read_b64_tr_b16 v[34:35], v220 offset:30720\n\tv_mfma_f32_16x16x32_bf16 v[60:63], v[208:211], v[44:47], v[60:63]\n\tbuffer_load_dwordx4 v12, s[16:19], s69 offen lds\n\t" \
    "s_cmp_le_u32 s4, 10\n\ts_cselect_b32 s69, 262144, 0\n\ts_add_u32 s69, s69, 2621440\n\tv_mfma_f32_16x16x32_bf16 v[68:71], v[212:215], v[44:47], v[68:71]\n\ts_add_u32 m0, s54, 139264\n\ts_waitcnt vmcnt(23) lgkmcnt(8)\n\tv_mfma_f32_16x16x32_bf16 v[120:123], v[216:219], v[16:19], v[120:123]\n\tds_read_b64_tr_b16 v[36:37], v221 offset:28672\n\t" \
    "ds_read_b64_tr_b16 v[38:39], v221 offset:30720\n\tv_mfma_f32_16x16x32_bf16 v[128:131], a[0:3], v[16:19], v[128:131]\n\tbuffer_load_dwordx4 v13, s[16:19], s69 offen lds\n\ts_cmp_le_u32 s4, 8\n\ts_cselect_b32 s68, 3072, 0\n\ts_add_u32 s68, s68, s46\n\tv_mfma_f32_16x16x32_bf16 v[136:139], a[4:7], v[16:19], v[136:139]\n\ts_add_u32 s68, s68, 24576\n\t" \
    "s_waitcnt lgkmcnt(8)\n\tv_mfma_f32_16x16x32_bf16 v[124:127], v[216:219], v[20:23], v[124:127]\n\tds_read_b64_tr_b16 v[40:41], v222 offset:28672\n\tds_read_b64_tr_b16 v[42:43], v222 offset:30720\n\tv_mfma_f32_16x16x32_bf16 v[132:135], a[0:3], v[20:23], v[132:135]\n\tbuffer_load_dwordx4 v[204:207], v8, s[8:11], s68 offen offset:0 sc1\n\tv_mfma_f32_16x16x32_bf16 v[140:143], a[4:7], v[20:23], v[140:143]\n\ts_waitcnt lgkmcnt(8)\n\t" \
    "v_mfma_f32_16x16x32_bf16 v[144:147], v[216:219], v[24:27], v[144:147]\n\tds_read_b64_tr_b16 v[44:45], v223 offset:28672\n\tds_read_b64_tr_b16 v[46:47], v223 offset:30720\n\tv_mfma_f32_16x16x32_bf16 v[152:155], a[0:3], v[24:27], v[152:155]\n\tv_mfma_f32_16x16x32_bf16 v[160:163], a[4:7], v[24:27], v[160:163]\n\ts_waitcnt lgkmcnt(8)\n\tv_mfma_f32_16x16x32_bf16 v[148:151], v[216:219], v[28:31], v[148:151]\n\tds_read_b64_tr_b16 v[16:17], v4 offset:4096\n\t" \
    "ds_read_b64_tr_b16 v[18:19], v4 offset:6144\n\tv_mfma_f32_16x16x32_bf16 v[156:159], a[0:3], v[28:31], v[156:159]\n\tbuffer_load_dwordx4 v[208:211], v8, s[8:11], s68 offen offset:1024 sc1\n\tv_mfma_f32_16x16x32_bf16 v[164:167], a[4:7], v[28:31], v[164:167]\n\ts_waitcnt lgkmcnt(8)\n\tv_mfma_f32_16x16x32_bf16 v[168:171], v[216:219], v[32:35], v[168:171]\n\tds_read_b64_tr_b16 v[20:21], v5 offset:4096\n\tds_read_b64_tr_b16 v[22:23], v5 offset:6144\n\t" \
    "v_mfma_f32_16x16x32_bf16 v[176:179], a[0:3], v[32:35], v[176:179]\n\tbuffer_load_dwordx4 v[212:215], v8, s[8:11], s68 offen offset:2048 sc1\n\ts_cmp_le_u32 s4, 9\n\ts_cselect_b32 s68, 3072, 0\n\ts_add_u32 s68, s68, s46\n\tv_mfma_f32_16x16x32_bf16 v[184:187], a[4:7], v[32:35], v[184:187]\n\ts_add_u32 s68, s68, 27648\n\ts_waitcnt lgkmcnt(8)\n\t" \
    "v_mfma_f32_16x16x32_bf16 v[172:175], v[216:219], v[36:39], v[172:175]\n\tds_read_b64_tr_b16 v[24:25], v6 offset:4096\n\tds_read_b64_tr_b16 v[26:27], v6 offset:6144\n\tv_mfma_f32_16x16x32_bf16 v[180:183], a[0:3], v[36:39], v[180:183]\n\tv_mfma_f32_16x16x32_bf16 v[188:191], a[4:7], v[36:39], v[188:191]\n\ts_waitcnt lgkmcnt(8)\n\tv_mfma_f32_16x16x32_bf16 v[48:51], v[216:219], v[40:43], v[48:51]\n\tds_read_b64_tr_b16 v[28:29], v7 offset:4096\n\t" \
    "ds_read_b64_tr_b16 v[30:31], v7 offset:6144\n\tv_mfma_f32_16x16x32_bf16 v[56:59], a[0:3], v[40:43], v[56:59]\n\tbuffer_load_dwordx4 a[80:83], v8, s[8:11], s68 offen offset:0 sc1\n\tv_mfma_f32_16x16x32_bf16 v[64:67], a[4:7], v[40:43], v[64:67]\n\ts_waitcnt lgkmcnt(8)\n\tv_mfma_f32_16x16x32_bf16 v[52:55], v[216:219], v[44:47], v[52:55]\n\tds_read_b64_tr_b16 v[32:33], v4 offset:8192\n\tds_read_b64_tr_b16 v[34:35], v4 offset:10240\n\t" \
    "v_mfma_f32_16x16x32_bf16 v[60:63], a[0:3], v[44:47], v[60:63]\n\tbuffer_load_dwordx4 a[84:87], v8, s[8:11], s68 offen offset:1024 sc1\n\tv_mfma_f32_16x16x32_bf16 v[68:71], a[4:7], v[44:47], v[68:71]\n\ts_waitcnt vmcnt(17)\n\ts_add_u32 s73, s49, 4096\n\tbuffer_store_dwordx2 v[248:249], off, s[8:11], s73 sc1\n\ts_barrier\n\ts_waitcnt lgkmcnt(8)\n\t" \
    "v_mfma_f32_16x16x32_bf16 v[120:123], a[8:11], v[16:19], v[120:123]\n\tds_read_b64_tr_b16 v[36:37], v5 offset:8192\n\tds_read_b64_tr_b16 v[38:39], v5 offset:10240\n\tv_mfma_f32_16x16x32_bf16 v[128:131], a[12:15], v[16:19], v[128:131]\n\tbuffer_load_dwordx4 a[88:91], v8, s[8:11], s68 offen offset:2048 sc1\n\ts_cmp_le_u32 s4, 11\n\ts_cselect_b32 s69, 262144, 0\n\ts_add_u32 s69, s69, 2883584\n\t" \
    "v_mfma_f32_16x16x32_bf16 v[136:139], a[16:19], v[16:19], v[136:139]\n\ts_add_u32 m0, s54, 143360\n\ts_waitcnt lgkmcnt(8)\n\tv_mfma_f32_16x16x32_bf16 v[124:127], a[8:11], v[20:23], v[124:127]\n\tds_read_b64_tr_b16 v[40:41], v6 offset:8192\n\tds_read_b64_tr_b16 v[42:43], v6 offset:10240\n\tv_mfma_f32_16x16x32_bf16 v[132:135], a[12:15], v[20:23], v[132:135]\n\tbuffer_load_dwordx4 v12, s[16:19], s69 offen lds\n\t" \
    "s_cmp_le_u32 s4, 11\n\ts_cselect_b32 s69, 262144, 0\n\ts_add_u32 s69, s69, 2883584\n\tv_mfma_f32_16x16x32_bf16 v[140:143], a[16:19], v[20:23], v[140:143]\n\ts_add_u32 m0, s54, 147456\n\ts_waitcnt lgkmcnt(8)\n\tv_mfma_f32_16x16x32_bf16 v[144:147], a[8:11], v[24:27], v[144:147]\n\tds_read_b64_tr_b16 v[44:45], v7 offset:8192\n\t" \
    "ds_read_b64_tr_b16 v[46:47], v7 offset:10240\n\tv_mfma_f32_16x16x32_bf16 v[152:155], a[12:15], v[24:27], v[152:155]\n\tbuffer_load_dwordx4 v13, s[16:19], s69 offen lds\n\ts_cmp_le_u32 s4, 12\n\ts_cselect_b32 s69, 262144, 0\n\ts_add_u32 s69, s69, 3145728\n\tv_mfma_f32_16x16x32_bf16 v[160:163], a[16:19], v[24:27], v[160:163]\n\ts_add_u32 m0, s54, 151552\n\t" \
    "s_waitcnt lgkmcnt(8)\n\tv_mfma_f32_16x16x32_bf16 v[148:151], a[8:11], v[28:31], v[148:151]\n\tds_read_b64_tr_b16 v[16:17], v4 offset:12288\n\tds_read_b64_tr_b16 v[18:19], v4 offset:14336\n\tv_mfma_f32_16x16x32_bf16 v[156:159], a[12:15], v[28:31], v[156:159]\n\tbuffer_load_dwordx4 v12, s[16:19], s69 offen lds\n\ts_cmp_le_u32 s4, 12\n\ts_cselect_b32 s69, 262144, 0\n\t" \
    "s_add_u32 s69, s69, 3145728\n\tv_mfma_f32_16x16x32_bf16 v[164:167], a[16:19], v[28:31], v[164:167]\n\ts_add_u32 m0, s54, 155648\n\ts_waitcnt lgkmcnt(8)\n\tv_mfma_f32_16x16x32_bf16 v[168:171], a[8:11], v[32:35], v[168:171]\n\tds_read_b64_tr_b16 v[20:21], v5 offset:12288\n\tds_read_b64_tr_b16 v[22:23], v5 offset:14336\n\tv_mfma_f32_16x16x32_bf16 v[176:179], a[12:15], v[32:35], v[176:179]\n\t" \
    "buffer_load_dwordx4 v13, s[16:19], s69 offen lds\n\ts_cmp_le_u32 s4, 10\n\ts_cselect_b32 s68, 3072, 0\n\ts_add_u32 s68, s68, s46\n\tv_mfma_f32_16x16x32_bf16 v[184:187], a[16:19], v[32:35], v[184:187]\n\ts_add_u32 s68, s68, 30720\n\ts_waitcnt lgkmcnt(8)\n\tv_mfma_f32_16x16x32_bf16 v[172:175], a[8:11], v[36:39], v[172:175]\n\t" \
    "ds_read_b64_tr_b16 v[24:25], v6 offset:12288\n\tds_read_b64_tr_b16 v[26:27], v6 offset:14336\n\tv_mfma_f32_16x16x32_bf16 v[180:183], a[12:15], v[36:39], v[180:183]\n\tbuffer_load_dwordx4 v[192:195], v8, s[8:11], s68 offen offset:0 sc1\n\tv_mfma_f32_16x16x32_bf16 v[188:191], a[16:19], v[36:39], v[188:191]\n\ts_waitcnt lgkmcnt(8)\n\tv_mfma_f32_16x16x32_bf16 v[48:51], a[8:11], v[40:43], v[48:51]\n\tds_read_b64_tr_b16 v[28:29], v7 offset:12288\n\t" \
    "ds_read_b64_tr_b16 v[30:31], v7 offset:14336\n\tv_mfma_f32_16x16x32_bf16 v[56:59], a[12:15], v[40:43], v[56:59]\n\tbuffer_load_dwordx4 v[196:199], v8, s[8:11], s68 offen offset:1024 sc1\n\tv_mfma_f32_16x16x32_bf16 v[64:67], a[16:19], v[40:43], v[64:67]\n\ts_waitcnt lgkmcnt(8)\n\tv_mfma_f32_16x16x32_bf16 v[52:55], a[8:11], v[44:47], v[52:55]\n\tds_read_b64_tr_b16 v[32:33], v4 offset:16384\n\tds_read_b64_tr_b16 v[34:35], v4 offset:18432\n\t" \
    "v_mfma_f32_16x16x32_bf16 v[60:63], a[12:15], v[44:47], v[60:63]\n\tv_mfma_f32_16x16x32_bf16 v[68:71], a[16:19], v[44:47], v[68:71]\n\ts_waitcnt lgkmcnt(8)\n\tv_mfma_f32_16x16x32_bf16 v[120:123], a[20:23], v[16:19], v[120:123]\n\tds_read_b64_tr_b16 v[36:37], v5 offset:16384\n\tds_read_b64_tr_b16 v[38:39], v5 offset:18432\n\tv_mfma_f32_16x16x32_bf16 v[128:131], a[24:27], v[16:19], v[128:131]\n\tbuffer_load_dwordx4 v[200:203], v8, s[8:11], s68 offen offset:2048 sc1\n\t" \
    "s_cmp_le_u32 s4, 11\n\ts_cselect_b32 s68, 3072, 0\n\ts_add_u32 s68, s68, s46\n\tv_mfma_f32_16x16x32_bf16 v[136:139], a[28:31], v[16:19], v[136:139]\n\ts_add_u32 s68, s68, 33792\n\ts_waitcnt lgkmcnt(8)\n\tv_mfma_f32_16x16x32_bf16 v[124:127], a[20:23], v[20:23], v[124:127]\n\tds_read_b64_tr_b16 v[40:41], v6 offset:16384\n\t" \
    "ds_read_b64_tr_b16 v[42:43], v6 offset:18432\n\tv_mfma_f32_16x16x32_bf16 v[132:135], a[24:27], v[20:23], v[132:135]\n\tbuffer_load_dwordx4 v[216:219], v8, s[8:11], s68 offen offset:0 sc1\n\tv_mfma_f32_16x16x32_bf16 v[140:143], a[28:31], v[20:23], v[140:143]\n\ts_waitcnt lgkmcnt(8)\n\tv_mfma_f32_16x16x32_bf16 v[144:147], a[20:23], v[24:27], v[144:147]\n\tds_read_b64_tr_b16 v[44:45], v7 offset:16384\n\tds_read_b64_tr_b16 v[46:47], v7 offset:18432\n\t" \
    "v_mfma_f32_16x16x32_bf16 v[152:155], a[24:27], v[24:27], v[152:155]\n\tbuffer_load_dwordx4 v[72:75], v8, s[8:11], s68 offen offset:1024 sc1\n\tv_mfma_f32_16x16x32_bf16 v[160:163], a[28:31], v[24:27], v[160:163]\n\ts_waitcnt lgkmcnt(8)\n\tv_mfma_f32_16x16x32_bf16 v[148:151], a[20:23], v[28:31], v[148:151]\n\tds_read_b64_tr_b16 v[16:17], v4 offset:20480\n\tds_read_b64_tr_b16 v[18:19], v4 offset:22528\n\tv_mfma_f32_16x16x32_bf16 v[156:159], a[24:27], v[28:31], v[156:159]\n\t" \
    "v_mfma_f32_16x16x32_bf16 v[164:167], a[28:31], v[28:31], v[164:167]\n\ts_waitcnt lgkmcnt(8)\n\tv_mfma_f32_16x16x32_bf16 v[168:171], a[20:23], v[32:35], v[168:171]\n\tds_read_b64_tr_b16 v[20:21], v5 offset:20480\n\tds_read_b64_tr_b16 v[22:23], v5 offset:22528\n\tv_mfma_f32_16x16x32_bf16 v[176:179], a[24:27], v[32:35], v[176:179]\n\tbuffer_load_dwordx4 v[76:79], v8, s[8:11], s68 offen offset:2048 sc1\n\ts_cmp_le_u32 s4, 13\n\t" \
    "s_cselect_b32 s69, 262144, 0\n\ts_add_u32 s69, s69, 3407872\n\tv_mfma_f32_16x16x32_bf16 v[184:187], a[28:31], v[32:35], v[184:187]\n\ts_add_u32 m0, s54, 159744\n\ts_waitcnt lgkmcnt(8)\n\tv_mfma_f32_16x16x32_bf16 v[172:175], a[20:23], v[36:39], v[172:175]\n\tds_read_b64_tr_b16 v[24:25], v6 offset:20480\n\tds_read_b64_tr_b16 v[26:27], v6 offset:22528\n\t" \
    "v_mfma_f32_16x16x32_bf16 v[180:183], a[24:27], v[36:39], v[180:183]\n\tbuffer_load_dwordx4 v12, s[16:19], s69 offen lds\n\ts_cmp_le_u32 s4, 13\n\ts_cselect_b32 s69, 262144, 0\n\ts_add_u32 s69, s69, 3407872\n\tv_mfma_f32_16x16x32_bf16 v[188:191], a[28:31], v[36:39], v[188:191]\n\ts_add_u32 m0, s54, 61440\n\ts_waitcnt lgkmcnt(8)\n\t" \
    "v_mfma_f32_16x16x32_bf16 v[48:51], a[20:23], v[40:43], v[48:51]\n\tds_read_b64_tr_b16 v[28:29], v7 offset:20480\n\tds_read_b64_tr_b16 v[30:31], v7 offset:22528\n\tv_mfma_f32_16x16x32_bf16 v[56:59], a[24:27], v[40:43], v[56:59]\n\tbuffer_load_dwordx4 v13, s[16:19], s69 offen lds\n\ts_cmp_le_u32 s4, 14\n\ts_cselect_b32 s69, 262144, 0\n\ts_add_u32 s69, s69, 3670016\n\t" \
    "v_mfma_f32_16x16x32_bf16 v[64:67], a[28:31], v[40:43], v[64:67]\n\ts_add_u32 m0, s54, 65536\n\ts_waitcnt lgkmcnt(8)\n\tv_mfma_f32_16x16x32_bf16 v[52:55], a[20:23], v[44:47], v[52:55]\n\tds_read_b64_tr_b16 v[32:33], v4 offset:24576\n\tds_read_b64_tr_b16 v[34:35], v4 offset:26624\n\tv_mfma_f32_16x16x32_bf16 v[60:63], a[24:27], v[44:47], v[60:63]\n\tv_mfma_f32_16x16x32_bf16 v[68:71], a[28:31], v[44:47], v[68:71]\n\t" \
    "s_barrier\n\ts_waitcnt lgkmcnt(8)\n\tv_mfma_f32_16x16x32_bf16 v[120:123], a[32:35], v[16:19], v[120:123]\n\tds_read_b64_tr_b16 v[36:37], v5 offset:24576\n\tds_read_b64_tr_b16 v[38:39], v5 offset:26624\n\tv_mfma_f32_16x16x32_bf16 v[128:131], a[36:39], v[16:19], v[128:131]\n\tbuffer_load_dwordx4 v12, s[16:19], s69 offen lds\n\ts_cmp_le_u32 s4, 14\n\t" \
    "s_cselect_b32 s69, 262144, 0\n\ts_add_u32 s69, s69, 3670016\n\tv_mfma_f32_16x16x32_bf16 v[136:139], a[40:43], v[16:19], v[136:139]\n\ts_add_u32 m0, s54, 69632\n\ts_waitcnt lgkmcnt(8)\n\tv_mfma_f32_16x16x32_bf16 v[124:127], a[32:35], v[20:23], v[124:127]\n\tds_read_b64_tr_b16 v[40:41], v6 offset:24576\n\tds_read_b64_tr_b16 v[42:43], v6 offset:26624\n\t" \
    "v_mfma_f32_16x16x32_bf16 v[132:135], a[36:39], v[20:23], v[132:135]\n\tbuffer_load_dwordx4 v13, s[16:19], s69 offen lds\n\ts_cmp_le_u32 s4, 15\n\ts_cselect_b32 s69, 262144, 0\n\ts_add_u32 s69, s69, 3932160\n\tv_mfma_f32_16x16x32_bf16 v[140:143], a[40:43], v[20:23], v[140:143]\n\ts_add_u32 m0, s54, 73728\n\ts_waitcnt lgkmcnt(8)\n\t" \
    "v_mfma_f32_16x16x32_bf16 v[144:147], a[32:35], v[24:27], v[144:147]\n\tds_read_b64_tr_b16 v[44:45], v7 offset:24576\n\tds_read_b64_tr_b16 v[46:47], v7 offset:26624\n\tv_mfma_f32_16x16x32_bf16 v[152:155], a[36:39], v[24:27], v[152:155]\n\tbuffer_load_dwordx4 v12, s[16:19], s69 offen lds\n\ts_cmp_le_u32 s4, 12\n\ts_cselect_b32 s68, 3072, 0\n\ts_add_u32 s68, s68, s46\n\t" \
    "v_mfma_f32_16x16x32_bf16 v[160:163], a[40:43], v[24:27], v[160:163]\n\ts_add_u32 s68, s68, 36864\n\ts_waitcnt lgkmcnt(8)\n\tv_mfma_f32_16x16x32_bf16 v[148:151], a[32:35], v[28:31], v[148:151]\n\tds_read_b64_tr_b16 v[16:17], v4 offset:28672\n\tds_read_b64_tr_b16 v[18:19], v4 offset:30720\n\tv_mfma_f32_16x16x32_bf16 v[156:159], a[36:39], v[28:31], v[156:159]\n\tbuffer_load_dwordx4 v[80:83], v8, s[8:11], s68 offen offset:0 sc1\n\t" \
    "v_mfma_f32_16x16x32_bf16 v[164:167], a[40:43], v[28:31], v[164:167]\n\ts_waitcnt lgkmcnt(8)\n\tv_mfma_f32_16x16x32_bf16 v[168:171], a[32:35], v[32:35], v[168:171]\n\tds_read_b64_tr_b16 v[20:21], v5 offset:28672\n\tds_read_b64_tr_b16 v[22:23], v5 offset:30720\n\tv_mfma_f32_16x16x32_bf16 v[176:179], a[36:39], v[32:35], v[176:179]\n\tbuffer_load_dwordx4 v[84:87], v8, s[8:11], s68 offen offset:1024 sc1\n\tv_mfma_f32_16x16x32_bf16 v[184:187], a[40:43], v[32:35], v[184:187]\n\t" \
    "s_waitcnt lgkmcnt(8)\n\tv_mfma_f32_16x16x32_bf16 v[172:175], a[32:35], v[36:39], v[172:175]\n\tds_read_b64_tr_b16 v[24:25], v6 offset:28672\n\tds_read_b64_tr_b16 v[26:27], v6 offset:30720\n\tv_mfma_f32_16x16x32_bf16 v[180:183], a[36:39], v[36:39], v[180:183]\n\tv_mfma_f32_16x16x32_bf16 v[188:191], a[40:43], v[36:39], v[188:191]\n\ts_waitcnt lgkmcnt(8)\n\tv_mfma_f32_16x16x32_bf16 v[48:51], a[32:35], v[40:43], v[48:51]\n\t" \
    "ds_read_b64_tr_b16 v[28:29], v7 offset:28672\n\tds_read_b64_tr_b16 v[30:31], v7 offset:30720\n\tv_mfma_f32_16x16x32_bf16 v[56:59], a[36:39], v[40:43], v[56:59]\n\tbuffer_load_dwordx4 v[88:91], v8, s[8:11], s68 offen offset:2048 sc1\n\ts_cmp_le_u32 s4, 13\n\ts_cselect_b32 s68, 3072, 0\n\ts_add_u32 s68, s68, s46\n\tv_mfma_f32_16x16x32_bf16 v[64:67], a[40:43], v[40:43], v[64:67]\n\t" \
    "s_add_u32 s68, s68, 39936\n\ts_waitcnt lgkmcnt(8)\n\tv_mfma_f32_16x16x32_bf16 v[52:55], a[32:35], v[44:47], v[52:55]\n\tds_read_b64_tr_b16 v[32:33], v4 offset:32768\n\tds_read_b64_tr_b16 v[34:35], v4 offset:34816\n\tv_mfma_f32_16x16x32_bf16 v[60:63], a[36:39], v[44:47], v[60:63]\n\tbuffer_load_dwordx4 v[92:95], v8, s[8:11], s68 offen offset:0 sc1\n\tv_mfma_f32_16x16x32_bf16 v[68:71], a[40:43], v[44:47], v[68:71]\n\t" \
    "s_waitcnt vmcnt(35) lgkmcnt(8)\n\tv_mfma_f32_16x16x32_bf16 v[120:123], a[44:47], v[16:19], v[120:123]\n\tds_read_b64_tr_b16 v[36:37], v5 offset:32768\n\tds_read_b64_tr_b16 v[38:39], v5 offset:34816\n\tv_mfma_f32_16x16x32_bf16 v[128:131], a[48:51], v[16:19], v[128:131]\n\tbuffer_load_dwordx4 v[96:99], v8, s[8:11], s68 offen offset:1024 sc1\n\tv_mfma_f32_16x16x32_bf16 v[136:139], a[52:55], v[16:19], v[136:139]\n\ts_waitcnt lgkmcnt(8)\n\t" \
    "v_mfma_f32_16x16x32_bf16 v[124:127], a[44:47], v[20:23], v[124:127]\n\tds_read_b64_tr_b16 v[40:41], v6 offset:32768\n\tds_read_b64_tr_b16 v[42:43], v6 offset:34816\n\tv_mfma_f32_16x16x32_bf16 v[132:135], a[48:51], v[20:23], v[132:135]\n\tv_mfma_f32_16x16x32_bf16 v[140:143], a[52:55], v[20:23], v[140:143]\n\ts_waitcnt lgkmcnt(8)\n\tv_mfma_f32_16x16x32_bf16 v[144:147], a[44:47], v[24:27], v[144:147]\n\tds_read_b64_tr_b16 v[44:45], v7 offset:32768\n\t" \
    "ds_read_b64_tr_b16 v[46:47], v7 offset:34816\n\tv_mfma_f32_16x16x32_bf16 v[152:155], a[48:51], v[24:27], v[152:155]\n\tbuffer_load_dwordx4 v[100:103], v8, s[8:11], s68 offen offset:2048 sc1\n\ts_cmp_le_u32 s4, 14\n\ts_cselect_b32 s68, 3072, 0\n\ts_add_u32 s68, s68, s46\n\tv_mfma_f32_16x16x32_bf16 v[160:163], a[52:55], v[24:27], v[160:163]\n\ts_add_u32 s68, s68, 43008\n\t" \
    "s_waitcnt lgkmcnt(8)\n\tv_mfma_f32_16x16x32_bf16 v[148:151], a[44:47], v[28:31], v[148:151]\n\tds_read_b64_tr_b16 v[16:17], v224 offset:28672\n\tds_read_b64_tr_b16 v[18:19], v224 offset:30720\n\tv_mfma_f32_16x16x32_bf16 v[156:159], a[48:51], v[28:31], v[156:159]\n\tbuffer_load_dwordx4 v[104:107], v8, s[8:11], s68 offen offset:0 sc1\n\tv_mfma_f32_16x16x32_bf16 v[164:167], a[52:55], v[28:31], v[164:167]\n\ts_waitcnt lgkmcnt(8)\n\t" \
    "v_mfma_f32_16x16x32_bf16 v[168:171], a[44:47], v[32:35], v[168:171]\n\tds_read_b64_tr_b16 v[20:21], v225 offset:28672\n\tds_read_b64_tr_b16 v[22:23], v225 offset:30720\n\tv_mfma_f32_16x16x32_bf16 v[176:179], a[48:51], v[32:35], v[176:179]\n\tv_mfma_f32_16x16x32_bf16 v[184:187], a[52:55], v[32:35], v[184:187]\n\ts_waitcnt lgkmcnt(8)\n\tv_mfma_f32_16x16x32_bf16 v[172:175], a[44:47], v[36:39], v[172:175]\n\tds_read_b64_tr_b16 v[24:25], v226 offset:28672\n\t" \
    "ds_read_b64_tr_b16 v[26:27], v226 offset:30720\n\tv_mfma_f32_16x16x32_bf16 v[180:183], a[48:51], v[36:39], v[180:183]\n\tbuffer_load_dwordx4 v[108:111], v8, s[8:11], s68 offen offset:1024 sc1\n\tv_mfma_f32_16x16x32_bf16 v[188:191], a[52:55], v[36:39], v[188:191]\n\ts_waitcnt lgkmcnt(8)\n\tv_mfma_f32_16x16x32_bf16 v[48:51], a[44:47], v[40:43], v[48:51]\n\tds_read_b64_tr_b16 v[28:29], v227 offset:28672\n\tds_read_b64_tr_b16 v[30:31], v227 offset:30720\n\t" \
    "v_mfma_f32_16x16x32_bf16 v[56:59], a[48:51], v[40:43], v[56:59]\n\tbuffer_load_dwordx4 v[112:115], v8, s[8:11], s68 offen offset:2048 sc1\n\ts_cmp_le_u32 s4, 15\n\ts_cselect_b32 s69, 262144, 0\n\ts_add_u32 s69, s69, 3932160\n\tv_mfma_f32_16x16x32_bf16 v[64:67], a[52:55], v[40:43], v[64:67]\n\ts_add_u32 m0, s54, 77824\n\ts_waitcnt lgkmcnt(8)\n\t" \
    "v_mfma_f32_16x16x32_bf16 v[52:55], a[44:47], v[44:47], v[52:55]\n\tds_read_b64_tr_b16 v[32:33], v4 offset:36864\n\tds_read_b64_tr_b16 v[34:35], v4 offset:38912\n\tv_mfma_f32_16x16x32_bf16 v[60:63], a[48:51], v[44:47], v[60:63]\n\tbuffer_load_dwordx4 v13, s[16:19], s69 offen lds\n\ts_cmp_le_u32 s4, 16\n\ts_cselect_b32 s69, 262144, 0\n\ts_add_u32 s69, s69, 4194304\n\t" \
    "v_mfma_f32_16x16x32_bf16 v[68:71], a[52:55], v[44:47], v[68:71]\n\ts_add_u32 m0, s54, 81920\n\ts_barrier\n\ts_waitcnt vmcnt(38) lgkmcnt(8)\n\tv_mfma_f32_16x16x32_bf16 v[120:123], a[56:59], v[16:19], v[120:123]\n\tds_read_b64_tr_b16 v[36:37], v5 offset:36864\n\tds_read_b64_tr_b16 v[38:39], v5 offset:38912\n\tv_mfma_f32_16x16x32_bf16 v[128:131], a[60:63], v[16:19], v[128:131]\n\t" \
    "buffer_load_dwordx4 v12, s[16:19], s69 offen lds\n\ts_cmp_le_u32 s4, 16\n\ts_cselect_b32 s69, 262144, 0\n\ts_add_u32 s69, s69, 4194304\n\tv_mfma_f32_16x16x32_bf16 v[136:139], a[64:67], v[16:19], v[136:139]\n\ts_add_u32 m0, s54, 86016\n\ts_waitcnt lgkmcnt(8)\n\tv_mfma_f32_16x16x32_bf16 v[124:127], a[56:59], v[20:23], v[124:127]\n\t" \
    "ds_read_b64_tr_b16 v[40:41], v6 offset:36864\n\tds_read_b64_tr_b16 v[42:43], v6 offset:38912\n\tv_mfma_f32_16x16x32_bf16 v[132:135], a[60:63], v[20:23], v[132:135]\n\tbuffer_load_dwordx4 v13, s[16:19], s69 offen lds\n\ts_cmp_le_u32 s4, 0\n\ts_cselect_b32 s69, 262144, 0\n\ts_add_u32 s69, s69, 131072\n\tv_mfma_f32_16x16x32_bf16 v[140:143], a[64:67], v[20:23], v[140:143]\n\t" \
    "s_add_u32 m0, s54, 90112\n\ts_waitcnt lgkmcnt(8)\n\tv_mfma_f32_16x16x32_bf16 v[144:147], a[56:59], v[24:27], v[144:147]\n\tds_read_b64_tr_b16 v[44:45], v7 offset:36864\n\tds_read_b64_tr_b16 v[46:47], v7 offset:38912\n\tv_mfma_f32_16x16x32_bf16 v[152:155], a[60:63], v[24:27], v[152:155]\n\tv_mfma_f32_16x16x32_bf16 v[160:163], a[64:67], v[24:27], v[160:163]\n\ts_waitcnt lgkmcnt(8)\n\t" \
    "v_mfma_f32_16x16x32_bf16 v[148:151], a[56:59], v[28:31], v[148:151]\n\tds_read_b64_tr_b16 v[16:17], v4 offset:40960\n\tds_read_b64_tr_b16 v[18:19], v4 offset:43008\n\tv_mfma_f32_16x16x32_bf16 v[156:159], a[60:63], v[28:31], v[156:159]\n\tbuffer_load_dwordx4 v12, s[16:19], s69 offen lds\n\ts_cmp_le_u32 s4, 0\n\ts_cselect_b32 s69, 262144, 0\n\ts_add_u32 s69, s69, 131072\n\t" \
    "v_mfma_f32_16x16x32_bf16 v[164:167], a[64:67], v[28:31], v[164:167]\n\ts_add_u32 m0, s54, 98304\n\ts_waitcnt lgkmcnt(8)\n\tv_mfma_f32_16x16x32_bf16 v[168:171], a[56:59], v[32:35], v[168:171]\n\tds_read_b64_tr_b16 v[20:21], v5 offset:40960\n\tds_read_b64_tr_b16 v[22:23], v5 offset:43008\n\tv_mfma_f32_16x16x32_bf16 v[176:179], a[60:63], v[32:35], v[176:179]\n\tbuffer_load_dwordx4 v13, s[16:19], s69 offen lds\n\t" \
    "s_cmp_le_u32 s4, 15\n\ts_cselect_b32 s68, 3072, 0\n\ts_add_u32 s68, s68, s46\n\tv_mfma_f32_16x16x32_bf16 v[184:187], a[64:67], v[32:35], v[184:187]\n\ts_add_u32 s68, s68, 46080\n\ts_waitcnt lgkmcnt(8)\n\tv_mfma_f32_16x16x32_bf16 v[172:175], a[56:59], v[36:39], v[172:175]\n\tds_read_b64_tr_b16 v[24:25], v6 offset:40960\n\t" \
    "ds_read_b64_tr_b16 v[26:27], v6 offset:43008\n\tv_mfma_f32_16x16x32_bf16 v[180:183], a[60:63], v[36:39], v[180:183]\n\tbuffer_load_dwordx4 v[116:119], v8, s[8:11], s68 offen offset:0 sc1\n\tv_mfma_f32_16x16x32_bf16 v[188:191], a[64:67], v[36:39], v[188:191]\n\ts_waitcnt lgkmcnt(8)\n\tv_mfma_f32_16x16x32_bf16 v[48:51], a[56:59], v[40:43], v[48:51]\n\tds_read_b64_tr_b16 v[28:29], v7 offset:40960\n\tds_read_b64_tr_b16 v[30:31], v7 offset:43008\n\t" \
    "v_mfma_f32_16x16x32_bf16 v[56:59], a[60:63], v[40:43], v[56:59]\n\tv_mfma_f32_16x16x32_bf16 v[64:67], a[64:67], v[40:43], v[64:67]\n\ts_waitcnt lgkmcnt(8)\n\tv_mfma_f32_16x16x32_bf16 v[52:55], a[56:59], v[44:47], v[52:55]\n\tds_read_b64_tr_b16 v[32:33], v4 offset:45056\n\tds_read_b64_tr_b16 v[34:35], v4 offset:47104\n\tv_mfma_f32_16x16x32_bf16 v[60:63], a[60:63], v[44:47], v[60:63]\n\tbuffer_load_dwordx4 a[0:3], v8, s[8:11], s68 offen offset:1024 sc1\n\t" \
    "v_mfma_f32_16x16x32_bf16 v[68:71], a[64:67], v[44:47], v[68:71]\n\ts_waitcnt vmcnt(41) lgkmcnt(8)\n\tv_mfma_f32_16x16x32_bf16 v[120:123], a[68:71], v[16:19], v[120:123]\n\tds_read_b64_tr_b16 v[36:37], v5 offset:45056\n\tds_read_b64_tr_b16 v[38:39], v5 offset:47104\n\tv_mfma_f32_16x16x32_bf16 v[128:131], a[72:75], v[16:19], v[128:131]\n\tbuffer_load_dwordx4 a[4:7], v8, s[8:11], s68 offen offset:2048 sc1\n\ts_cmp_le_u32 s4, 16\n\t" \
    "s_cselect_b32 s68, 3072, 0\n\ts_add_u32 s68, s68, s46\n\tv_mfma_f32_16x16x32_bf16 v[136:139], a[76:79], v[16:19], v[136:139]\n\ts_add_u32 s68, s68, 49152\n\ts_waitcnt lgkmcnt(8)\n\tv_mfma_f32_16x16x32_bf16 v[124:127], a[68:71], v[20:23], v[124:127]\n\tds_read_b64_tr_b16 v[40:41], v6 offset:45056\n\tds_read_b64_tr_b16 v[42:43], v6 offset:47104\n\t" \
    "v_mfma_f32_16x16x32_bf16 v[132:135], a[72:75], v[20:23], v[132:135]\n\tbuffer_load_dwordx4 a[8:11], v8, s[8:11], s68 offen offset:0 sc1\n\tv_mfma_f32_16x16x32_bf16 v[140:143], a[76:79], v[20:23], v[140:143]\n\ts_waitcnt lgkmcnt(8)\n\tv_mfma_f32_16x16x32_bf16 v[144:147], a[68:71], v[24:27], v[144:147]\n\tds_read_b64_tr_b16 v[44:45], v7 offset:45056\n\tds_read_b64_tr_b16 v[46:47], v7 offset:47104\n\tv_mfma_f32_16x16x32_bf16 v[152:155], a[72:75], v[24:27], v[152:155]\n\t" \
    "v_mfma_f32_16x16x32_bf16 v[160:163], a[76:79], v[24:27], v[160:163]\n\ts_waitcnt lgkmcnt(8)\n\tv_mfma_f32_16x16x32_bf16 v[148:151], a[68:71], v[28:31], v[148:151]\n\tds_read_b64_tr_b16 v[16:17], v4 offset:49152\n\tds_read_b64_tr_b16 v[18:19], v4 offset:51200\n\tv_mfma_f32_16x16x32_bf16 v[156:159], a[72:75], v[28:31], v[156:159]\n\tbuffer_load_dwordx4 a[12:15], v8, s[8:11], s68 offen offset:1024 sc1\n\tv_mfma_f32_16x16x32_bf16 v[164:167], a[76:79], v[28:31], v[164:167]\n\t" \
    "s_waitcnt lgkmcnt(8)\n\tv_mfma_f32_16x16x32_bf16 v[168:171], a[68:71], v[32:35], v[168:171]\n\tds_read_b64_tr_b16 v[20:21], v5 offset:49152\n\tds_read_b64_tr_b16 v[22:23], v5 offset:51200\n\tv_mfma_f32_16x16x32_bf16 v[176:179], a[72:75], v[32:35], v[176:179]\n\tbuffer_load_dwordx4 a[16:19], v8, s[8:11], s68 offen offset:2048 sc1\n\ts_cmp_le_u32 s4, 1\n\ts_cselect_b32 s69, 262144, 0\n\t" \
    "s_add_u32 s69, s69, 393216\n\tv_mfma_f32_16x16x32_bf16 v[184:187], a[76:79], v[32:35], v[184:187]\n\ts_add_u32 m0, s54, 102400\n\ts_waitcnt lgkmcnt(8)\n\tv_mfma_f32_16x16x32_bf16 v[172:175], a[68:71], v[36:39], v[172:175]\n\tds_read_b64_tr_b16 v[24:25], v6 offset:49152\n\tds_read_b64_tr_b16 v[26:27], v6 offset:51200\n\tv_mfma_f32_16x16x32_bf16 v[180:183], a[72:75], v[36:39], v[180:183]\n\t" \
    "v_mfma_f32_16x16x32_bf16 v[188:191], a[76:79], v[36:39], v[188:191]\n\ts_add_u32 s50, s50, 4096\n\tbuffer_load_dwordx2 v[250:251], v241, s[8:11], s50 offen sc1\n\ts_waitcnt lgkmcnt(8)\n\tv_mfma_f32_16x16x32_bf16 v[48:51], a[68:71], v[40:43], v[48:51]\n\tds_read_b64_tr_b16 v[28:29], v7 offset:49152\n\tds_read_b64_tr_b16 v[30:31], v7 offset:51200\n\tv_mfma_f32_16x16x32_bf16 v[56:59], a[72:75], v[40:43], v[56:59]\n\t" \
    "v_mfma_f32_16x16x32_bf16 v[64:67], a[76:79], v[40:43], v[64:67]\n\ts_waitcnt lgkmcnt(8)\n\tv_mfma_f32_16x16x32_bf16 v[52:55], a[68:71], v[44:47], v[52:55]\n\tds_read_b64_tr_b16 v[32:33], v4 offset:53248\n\tds_read_b64_tr_b16 v[34:35], v4 offset:55296\n\tv_mfma_f32_16x16x32_bf16 v[60:63], a[72:75], v[44:47], v[60:63]\n\tbuffer_load_dwordx4 v12, s[16:19], s69 offen lds\n\ts_cmp_le_u32 s4, 1\n\t" \
    "s_cselect_b32 s69, 262144, 0\n\ts_add_u32 s69, s69, 393216\n\tv_mfma_f32_16x16x32_bf16 v[68:71], a[76:79], v[44:47], v[68:71]\n\ts_add_u32 m0, s54, 106496\n\ts_waitcnt vmcnt(44)\n\ts_barrier\n\ts_waitcnt vmcnt(41) lgkmcnt(8)\n\tv_mfma_f32_16x16x32_bf16 v[120:123], v[204:207], v[16:19], v[120:123]\n\t" \
    "ds_read_b64_tr_b16 v[36:37], v5 offset:53248\n\tds_read_b64_tr_b16 v[38:39], v5 offset:55296\n\tv_mfma_f32_16x16x32_bf16 v[128:131], v[208:211], v[16:19], v[128:131]\n\tbuffer_load_dwordx4 v13, s[16:19], s69 offen lds\n\ts_cmp_le_u32 s4, 2\n\ts_cselect_b32 s69, 262144, 0\n\ts_add_u32 s69, s69, 655360\n\tv_mfma_f32_16x16x32_bf16 v[136:139], v[212:215], v[16:19], v[136:139]\n\t" \
    "s_add_u32 m0, s54, 110592\n\ts_waitcnt lgkmcnt(8)\n\tv_mfma_f32_16x16x32_bf16 v[124:127], v[204:207], v[20:23], v[124:127]\n\tds_read_b64_tr_b16 v[40:41], v6 offset:53248\n\tds_read_b64_tr_b16 v[42:43], v6 offset:55296\n\tv_mfma_f32_16x16x32_bf16 v[132:135], v[208:211], v[20:23], v[132:135]\n\tbuffer_load_dwordx4 v12, s[16:19], s69 offen lds\n\ts_cmp_le_u32 s4, 2\n\t" \
    "s_cselect_b32 s69, 262144, 0\n\ts_add_u32 s69, s69, 655360\n\tv_mfma_f32_16x16x32_bf16 v[140:143], v[212:215], v[20:23], v[140:143]\n\ts_add_u32 m0, s54, 114688\n\ts_waitcnt lgkmcnt(8)\n\tv_mfma_f32_16x16x32_bf16 v[144:147], v[204:207], v[24:27], v[144:147]\n\tds_read_b64_tr_b16 v[44:45], v7 offset:53248\n\tds_read_b64_tr_b16 v[46:47], v7 offset:55296\n\t" \
    "v_mfma_f32_16x16x32_bf16 v[152:155], v[208:211], v[24:27], v[152:155]\n\tbuffer_load_dwordx4 v13, s[16:19], s69 offen lds\n\ts_cmp_le_u32 s4, 3\n\ts_cselect_b32 s69, 262144, 0\n\ts_add_u32 s69, s69, 917504\n\tv_mfma_f32_16x16x32_bf16 v[160:163], v[212:215], v[24:27], v[160:163]\n\ts_add_u32 m0, s54, 118784\n\ts_waitcnt lgkmcnt(8)\n\t" \
    "v_mfma_f32_16x16x32_bf16 v[148:151], v[204:207], v[28:31], v[148:151]\n\tds_read_b64_tr_b16 v[16:17], v4 offset:57344\n\tds_read_b64_tr_b16 v[18:19], v4 offset:59392\n\tv_mfma_f32_16x16x32_bf16 v[156:159], v[208:211], v[28:31], v[156:159]\n\tbuffer_load_dwordx4 v12, s[16:19], s69 offen lds\n\ts_cmp_le_u32 s4, 3\n\ts_cselect_b32 s69, 262144, 0\n\ts_add_u32 s69, s69, 917504\n\t" \
    "v_mfma_f32_16x16x32_bf16 v[164:167], v[212:215], v[28:31], v[164:167]\n\ts_add_u32 m0, s54, 122880\n\ts_waitcnt lgkmcnt(8)\n\tv_mfma_f32_16x16x32_bf16 v[168:171], v[204:207], v[32:35], v[168:171]\n\tds_read_b64_tr_b16 v[20:21], v5 offset:57344\n\tds_read_b64_tr_b16 v[22:23], v5 offset:59392\n\tv_mfma_f32_16x16x32_bf16 v[176:179], v[208:211], v[32:35], v[176:179]\n\tv_mfma_f32_16x16x32_bf16 v[184:187], v[212:215], v[32:35], v[184:187]\n\t" \
    "s_waitcnt lgkmcnt(8)\n\tv_mfma_f32_16x16x32_bf16 v[172:175], v[204:207], v[36:39], v[172:175]\n\tds_read_b64_tr_b16 v[24:25], v6 offset:57344\n\tds_read_b64_tr_b16 v[26:27], v6 offset:59392\n\tv_mfma_f32_16x16x32_bf16 v[180:183], v[208:211], v[36:39], v[180:183]\n\tbuffer_load_dwordx4 v13, s[16:19], s69 offen lds\n\ts_cmp_le_u32 s4, 4\n\ts_cselect_b32 s69, 262144, 0\n\t" \
    "s_add_u32 s69, s69, 1179648\n\tv_mfma_f32_16x16x32_bf16 v[188:191], v[212:215], v[36:39], v[188:191]\n\ts_add_u32 m0, s54, 126976\n\ts_waitcnt lgkmcnt(8)\n\tv_mfma_f32_16x16x32_bf16 v[48:51], v[204:207], v[40:43], v[48:51]\n\tds_read_b64_tr_b16 v[28:29], v7 offset:57344\n\tds_read_b64_tr_b16 v[30:31], v7 offset:59392\n\tv_mfma_f32_16x16x32_bf16 v[56:59], v[208:211], v[40:43], v[56:59]\n\t" \
    "buffer_load_dwordx4 v12, s[16:19], s69 offen lds\n\ts_cmp_le_u32 s4, 4\n\ts_cselect_b32 s69, 262144, 0\n\ts_add_u32 s69, s69, 1179648\n\tv_mfma_f32_16x16x32_bf16 v[64:67], v[212:215], v[40:43], v[64:67]\n\ts_add_u32 m0, s54, 0\n\ts_waitcnt lgkmcnt(8)\n\tv_mfma_f32_16x16x32_bf16 v[52:55], v[204:207], v[44:47], v[52:55]\n\t" \
    "ds_read_b64_tr_b16 v[32:33], v220 offset:0\n\tds_read_b64_tr_b16 v[34:35], v220 offset:2048\n\tv_mfma_f32_16x16x32_bf16 v[60:63], v[208:211], v[44:47], v[60:63]\n\tbuffer_load_dwordx4 v13, s[16:19], s69 offen lds\n\ts_cmp_le_u32 s4, 5\n\ts_cselect_b32 s69, 262144, 0\n\ts_add_u32 s69, s69, 1441792\n\tv_mfma_f32_16x16x32_bf16 v[68:71], v[212:215], v[44:47], v[68:71]\n\t" \
    "s_add_u32 m0, s54, 4096\n\ts_waitcnt vmcnt(44) lgkmcnt(8)\n\tv_mfma_f32_16x16x32_bf16 v[120:123], a[80:83], v[16:19], v[120:123]\n\tds_read_b64_tr_b16 v[36:37], v221 offset:0\n\tds_read_b64_tr_b16 v[38:39], v221 offset:2048\n\tv_mfma_f32_16x16x32_bf16 v[128:131], a[84:87], v[16:19], v[128:131]\n\tv_mfma_f32_16x16x32_bf16 v[136:139], a[88:91], v[16:19], v[136:139]\n\ts_waitcnt lgkmcnt(8)\n\t" \
    "v_mfma_f32_16x16x32_bf16 v[124:127], a[80:83], v[20:23], v[124:127]\n\tds_read_b64_tr_b16 v[40:41], v222 offset:0\n\tds_read_b64_tr_b16 v[42:43], v222 offset:2048\n\tv_mfma_f32_16x16x32_bf16 v[132:135], a[84:87], v[20:23], v[132:135]\n\tbuffer_load_dwordx4 v12, s[16:19], s69 offen lds\n\ts_cmp_le_u32 s4, 5\n\ts_cselect_b32 s69, 262144, 0\n\ts_add_u32 s69, s69, 1441792\n\t" \
    "v_mfma_f32_16x16x32_bf16 v[140:143], a[88:91], v[20:23], v[140:143]\n\ts_add_u32 m0, s54, 8192\n\ts_waitcnt lgkmcnt(8)\n\tv_mfma_f32_16x16x32_bf16 v[144:147], a[80:83], v[24:27], v[144:147]\n\tds_read_b64_tr_b16 v[44:45], v223 offset:0\n\tds_read_b64_tr_b16 v[46:47], v223 offset:2048\n\tv_mfma_f32_16x16x32_bf16 v[152:155], a[84:87], v[24:27], v[152:155]\n\tbuffer_load_dwordx4 v13, s[16:19], s69 offen lds\n\t" \
    "s_cmp_le_u32 s4, 6\n\ts_cselect_b32 s69, 262144, 0\n\ts_add_u32 s69, s69, 1703936\n\tv_mfma_f32_16x16x32_bf16 v[160:163], a[88:91], v[24:27], v[160:163]\n\ts_add_u32 m0, s54, 12288\n\ts_waitcnt lgkmcnt(8)\n\tv_mfma_f32_16x16x32_bf16 v[148:151], a[80:83], v[28:31], v[148:151]\n\tds_read_b64_tr_b16 v[16:17], v220 offset:4096\n\t" \
    "ds_read_b64_tr_b16 v[18:19], v220 offset:6144\n\tv_mfma_f32_16x16x32_bf16 v[156:159], a[84:87], v[28:31], v[156:159]\n\tbuffer_load_dwordx4 v12, s[16:19], s69 offen lds\n\ts_cmp_le_u32 s4, 6\n\ts_cselect_b32 s69, 262144, 0\n\ts_add_u32 s69, s69, 1703936\n\tv_mfma_f32_16x16x32_bf16 v[164:167], a[88:91], v[28:31], v[164:167]\n\ts_add_u32 m0, s54, 16384\n\t" \
    "s_waitcnt lgkmcnt(8)\n\tv_mfma_f32_16x16x32_bf16 v[168:171], a[80:83], v[32:35], v[168:171]\n\tds_read_b64_tr_b16 v[20:21], v221 offset:4096\n\tds_read_b64_tr_b16 v[22:23], v221 offset:6144\n\tv_mfma_f32_16x16x32_bf16 v[176:179], a[84:87], v[32:35], v[176:179]\n\tbuffer_load_dwordx4 v13, s[16:19], s69 offen lds\n\ts_cmp_le_u32 s4, 7\n\ts_cselect_b32 s69, 262144, 0\n\t" \
    "s_add_u32 s69, s69, 1966080\n\tv_mfma_f32_16x16x32_bf16 v[184:187], a[88:91], v[32:35], v[184:187]\n\ts_add_u32 m0, s54, 20480\n\ts_waitcnt lgkmcnt(8)\n\tv_mfma_f32_16x16x32_bf16 v[172:175], a[80:83], v[36:39], v[172:175]\n\tds_read_b64_tr_b16 v[24:25], v222 offset:4096\n\tds_read_b64_tr_b16 v[26:27], v222 offset:6144\n\tv_mfma_f32_16x16x32_bf16 v[180:183], a[84:87], v[36:39], v[180:183]\n\t" \
    "buffer_load_dwordx4 v12, s[16:19], s69 offen lds\n\ts_cmp_le_u32 s4, 7\n\ts_cselect_b32 s69, 262144, 0\n\ts_add_u32 s69, s69, 1966080\n\tv_mfma_f32_16x16x32_bf16 v[188:191], a[88:91], v[36:39], v[188:191]\n\ts_add_u32 m0, s54, 24576\n\ts_waitcnt lgkmcnt(8)\n\tv_mfma_f32_16x16x32_bf16 v[48:51], a[80:83], v[40:43], v[48:51]\n\t" \
    "ds_read_b64_tr_b16 v[28:29], v223 offset:4096\n\tds_read_b64_tr_b16 v[30:31], v223 offset:6144\n\tv_mfma_f32_16x16x32_bf16 v[56:59], a[84:87], v[40:43], v[56:59]\n\tbuffer_load_dwordx4 v13, s[16:19], s69 offen lds\n\ts_cmp_le_u32 s4, 8\n\ts_cselect_b32 s69, 262144, 0\n\ts_add_u32 s69, s69, 2228224\n\tv_mfma_f32_16x16x32_bf16 v[64:67], a[88:91], v[40:43], v[64:67]\n\t" \
    "s_add_u32 m0, s54, 28672\n\ts_waitcnt lgkmcnt(8)\n\tv_mfma_f32_16x16x32_bf16 v[52:55], a[80:83], v[44:47], v[52:55]\n\tds_read_b64_tr_b16 v[32:33], v220 offset:8192\n\tds_read_b64_tr_b16 v[34:35], v220 offset:10240\n\tv_mfma_f32_16x16x32_bf16 v[60:63], a[84:87], v[44:47], v[60:63]\n\tv_mfma_f32_16x16x32_bf16 v[68:71], a[88:91], v[44:47], v[68:71]\n\ts_waitcnt vmcnt(46)\n\t" \
    "s_barrier\n\ts_waitcnt vmcnt(43) lgkmcnt(8)\n\tv_mfma_f32_16x16x32_bf16 v[120:123], v[192:195], v[16:19], v[120:123]\n\tds_read_b64_tr_b16 v[36:37], v221 offset:8192\n\tds_read_b64_tr_b16 v[38:39], v221 offset:10240\n\tv_mfma_f32_16x16x32_bf16 v[128:131], v[196:199], v[16:19], v[128:131]\n\tbuffer_load_dwordx4 v12, s[16:19], s69 offen lds\n\ts_cmp_le_u32 s4, 9\n\t" \
    "s_cselect_b32 s69, 262144, 0\n\ts_add_u32 s69, s69, 2490368\n\tv_mfma_f32_16x16x32_bf16 v[136:139], v[200:203], v[16:19], v[136:139]\n\ts_add_u32 m0, s54, 131072\n\ts_waitcnt lgkmcnt(8)\n\tv_mfma_f32_16x16x32_bf16 v[124:127], v[192:195], v[20:23], v[124:127]\n\tds_read_b64_tr_b16 v[40:41], v222 offset:8192\n\tds_read_b64_tr_b16 v[42:43], v222 offset:10240\n\t" \
    "v_mfma_f32_16x16x32_bf16 v[132:135], v[196:199], v[20:23], v[132:135]\n\tbuffer_load_dwordx4 v12, s[16:19], s69 offen lds\n\ts_cmp_le_u32 s4, 10\n\ts_cselect_b32 s69, 262144, 0\n\ts_add_u32 s69, s69, 2752512\n\tv_mfma_f32_16x16x32_bf16 v[140:143], v[200:203], v[20:23], v[140:143]\n\ts_add_u32 m0, s54, 32768\n\ts_waitcnt lgkmcnt(8)\n\t" \
    "v_mfma_f32_16x16x32_bf16 v[144:147], v[192:195], v[24:27], v[144:147]\n\tds_read_b64_tr_b16 v[44:45], v223 offset:8192\n\tds_read_b64_tr_b16 v[46:47], v223 offset:10240\n\tv_mfma_f32_16x16x32_bf16 v[152:155], v[196:199], v[24:27], v[152:155]\n\tbuffer_load_dwordx4 v12, s[16:19], s69 offen lds\n\ts_cmp_le_u32 s4, 11\n\ts_cselect_b32 s69, 262144, 0\n\ts_add_u32 s69, s69, 3014656\n\t" \
    "v_mfma_f32_16x16x32_bf16 v[160:163], v[200:203], v[24:27], v[160:163]\n\ts_add_u32 m0, s54, 94208\n\ts_waitcnt lgkmcnt(8)\n\tv_mfma_f32_16x16x32_bf16 v[148:151], v[192:195], v[28:31], v[148:151]\n\tds_read_b64_tr_b16 v[16:17], v220 offset:12288\n\tds_read_b64_tr_b16 v[18:19], v220 offset:14336\n\tv_mfma_f32_16x16x32_bf16 v[156:159], v[196:199], v[28:31], v[156:159]\n\tbuffer_load_dwordx4 v12, s[16:19], s69 offen lds\n\t" \
    "s_cmp_le_u32 s4, 12\n\ts_cselect_b32 s69, 262144, 0\n\ts_add_u32 s69, s69, 3276800\n\tv_mfma_f32_16x16x32_bf16 v[164:167], v[200:203], v[28:31], v[164:167]\n\ts_add_u32 m0, s54, 36864\n\ts_waitcnt vmcnt(18)\n\tv_cmp_ne_u64 vcc, s[36:37], v[250:251]\n\ts_cbranch_vccz 115f\n\t" \
    "s_mov_b32 s71, 1048576\n\t114:\n\ts_sleep 1\n\tbuffer_load_dwordx2 v[250:251], v241, s[8:11], s50 offen sc1\n\ts_waitcnt vmcnt(0)\n\tv_cmp_ne_u64 vcc, s[36:37], v[250:251]\n\ts_cbranch_vccz 115f\n\ts_sub_u32 s71, s71, 1\n\t" \
    "s_cbranch_scc0 114b\n\t115:\n\ts_waitcnt lgkmcnt(8)\n\tv_mfma_f32_16x16x32_bf16 v[168:171], v[192:195], v[32:35], v[168:171]\n\tds_read_b64_tr_b16 v[20:21], v221 offset:12288\n\tds_read_b64_tr_b16 v[22:23], v221 offset:14336\n\tv_mfma_f32_16x16x32_bf16 v[176:179], v[196:199], v[32:35], v[176:179]\n\tv_mfma_f32_16x16x32_bf16 v[184:187], v[200:203], v[32:35], v[184:187]\n\t" \
    "s_waitcnt lgkmcnt(8)\n\tv_mfma_f32_16x16x32_bf16 v[172:175], v[192:195], v[36:39], v[172:175]\n\tds_read_b64_tr_b16 v[24:25], v222 offset:12288\n\tds_read_b64_tr_b16 v[26:27], v222 offset:14336\n\tv_mfma_f32_16x16x32_bf16 v[180:183], v[196:199], v[36:39], v[180:183]\n\tbuffer_load_dwordx4 v12, s[16:19], s69 offen lds\n\ts_cmp_le_u32 s4, 0\n\ts_cselect_b32 s68, 3072, 0\n\t" \
    "s_add_u32 s68, s68, s46\n\tv_mfma_f32_16x16x32_bf16 v[188:191], v[200:203], v[36:39], v[188:191]\n\ts_add_u32 s68, s68, 55296\n\ts_waitcnt lgkmcnt(8)\n\tv_mfma_f32_16x16x32_bf16 v[48:51], v[192:195], v[40:43], v[48:51]\n\tds_read_b64_tr_b16 v[28:29], v223 offset:12288\n\tds_read_b64_tr_b16 v[30:31], v223 offset:14336\n\tv_mfma_f32_16x16x32_bf16 v[56:59], v[196:199], v[40:43], v[56:59]\n\t" \
    "buffer_load_dwordx4 a[20:23], v8, s[8:11], s68 offen offset:0 sc1\n\tv_mfma_f32_16x16x32_bf16 v[64:67], v[200:203], v[40:43], v[64:67]\n\ts_waitcnt lgkmcnt(8)\n\tv_mfma_f32_16x16x32_bf16 v[52:55], v[192:195], v[44:47], v[52:55]\n\tds_read_b64_tr_b16 v[32:33], v220 offset:16384\n\tds_read_b64_tr_b16 v[34:35], v220 offset:18432\n\tv_mfma_f32_16x16x32_bf16 v[60:63], v[196:199], v[44:47], v[60:63]\n\tbuffer_load_dwordx4 a[24:27], v8, s[8:11], s68 offen offset:1024 sc1\n\t" \
    "v_mfma_f32_16x16x32_bf16 v[68:71], v[200:203], v[44:47], v[68:71]\n\ts_waitcnt lgkmcnt(8)\n\tv_mfma_f32_16x16x32_bf16 v[120:123], v[216:219], v[16:19], v[120:123]\n\tds_read_b64_tr_b16 v[36:37], v221 offset:16384\n\tds_read_b64_tr_b16 v[38:39], v221 offset:18432\n\tv_mfma_f32_16x16x32_bf16 v[128:131], v[72:75], v[16:19], v[128:131]\n\tbuffer_load_dwordx4 a[28:31], v8, s[8:11], s68 offen offset:2048 sc1\n\ts_cmp_le_u32 s4, 1\n\t" \
    "s_cselect_b32 s68, 3072, 0\n\ts_add_u32 s68, s68, s46\n\tv_mfma_f32_16x16x32_bf16 v[136:139], v[76:79], v[16:19], v[136:139]\n\ts_add_u32 s68, s68, 58368\n\ts_waitcnt lgkmcnt(8)\n\tv_mfma_f32_16x16x32_bf16 v[124:127], v[216:219], v[20:23], v[124:127]\n\tds_read_b64_tr_b16 v[40:41], v222 offset:16384\n\tds_read_b64_tr_b16 v[42:43], v222 offset:18432\n\t" \
    "v_mfma_f32_16x16x32_bf16 v[132:135], v[72:75], v[20:23], v[132:135]\n\tbuffer_load_dwordx4 a[32:35], v8, s[8:11], s68 offen offset:0 sc1\n\tv_mfma_f32_16x16x32_bf16 v[140:143], v[76:79], v[20:23], v[140:143]\n\ts_waitcnt lgkmcnt(8)\n\tv_mfma_f32_16x16x32_bf16 v[144:147], v[216:219], v[24:27], v[144:147]\n\tds_read_b64_tr_b16 v[44:45], v223 offset:16384\n\tds_read_b64_tr_b16 v[46:47], v223 offset:18432\n\tv_mfma_f32_16x16x32_bf16 v[152:155], v[72:75], v[24:27], v[152:155]\n\t" \
    "v_mfma_f32_16x16x32_bf16 v[160:163], v[76:79], v[24:27], v[160:163]\n\ts_waitcnt lgkmcnt(8)\n\tv_mfma_f32_16x16x32_bf16 v[148:151], v[216:219], v[28:31], v[148:151]\n\tds_read_b64_tr_b16 v[16:17], v220 offset:20480\n\tds_read_b64_tr_b16 v[18:19], v220 offset:22528\n\tv_mfma_f32_16x16x32_bf16 v[156:159], v[72:75], v[28:31], v[156:159]\n\tbuffer_load_dwordx4 a[36:39], v8, s[8:11], s68 offen offset:1024 sc1\n\tv_mfma_f32_16x16x32_bf16 v[164:167], v[76:79], v[28:31], v[164:167]\n\t" \
    "s_waitcnt lgkmcnt(8)\n\tv_mfma_f32_16x16x32_bf16 v[168:171], v[216:219], v[32:35], v[168:171]\n\tds_read_b64_tr_b16 v[20:21], v221 offset:20480\n\tds_read_b64_tr_b16 v[22:23], v221 offset:22528\n\tv_mfma_f32_16x16x32_bf16 v[176:179], v[72:75], v[32:35], v[176:179]\n\tbuffer_load_dwordx4 a[40:43], v8, s[8:11], s68 offen offset:2048 sc1\n\ts_cmp_le_u32 s4, 2\n\ts_cselect_b32 s68, 3072, 0\n\t" \
    "s_add_u32 s68, s68, s46\n\tv_mfma_f32_16x16x32_bf16 v[184:187], v[76:79], v[32:35], v[184:187]\n\ts_add_u32 s68, s68, 61440\n\ts_waitcnt lgkmcnt(8)\n\tv_mfma_f32_16x16x32_bf16 v[172:175], v[216:219], v[36:39], v[172:175]\n\tds_read_b64_tr_b16 v[24:25], v222 offset:20480\n\tds_read_b64_tr_b16 v[26:27], v222 offset:22528\n\tv_mfma_f32_16x16x32_bf16 v[180:183], v[72:75], v[36:39], v[180:183]\n\t" \
    "v_mfma_f32_16x16x32_bf16 v[188:191], v[76:79], v[36:39], v[188:191]\n\ts_waitcnt lgkmcnt(8)\n\tv_mfma_f32_16x16x32_bf16 v[48:51], v[216:219], v[40:43], v[48:51]\n\tds_read_b64_tr_b16 v[28:29], v223 offset:20480\n\tds_read_b64_tr_b16 v[30:31], v223 offset:22528\n\tv_mfma_f32_16x16x32_bf16 v[56:59], v[72:75], v[40:43], v[56:59]\n\tbuffer_load_dwordx4 a[44:47], v8, s[8:11], s68 offen offset:0 sc1\n\tv_mfma_f32_16x16x32_bf16 v[64:67], v[76:79], v[40:43], v[64:67]\n\t" \
    "s_waitcnt lgkmcnt(8)\n\tv_mfma_f32_16x16x32_bf16 v[52:55], v[216:219], v[44:47], v[52:55]\n\tds_read_b64_tr_b16 v[32:33], v220 offset:24576\n\tds_read_b64_tr_b16 v[34:35], v220 offset:26624\n\tv_mfma_f32_16x16x32_bf16 v[60:63], v[72:75], v[44:47], v[60:63]\n\tbuffer_load_dwordx4 a[48:51], v8, s[8:11], s68 offen offset:1024 sc1\n\tv_mfma_f32_16x16x32_bf16 v[68:71], v[76:79], v[44:47], v[68:71]\n\ts_barrier\n\t" \
    "s_waitcnt lgkmcnt(8)\n\tv_mfma_f32_16x16x32_bf16 v[120:123], v[80:83], v[16:19], v[120:123]\n\tds_read_b64_tr_b16 v[36:37], v221 offset:24576\n\tds_read_b64_tr_b16 v[38:39], v221 offset:26624\n\tv_mfma_f32_16x16x32_bf16 v[128:131], v[84:87], v[16:19], v[128:131]\n\tbuffer_load_dwordx4 a[52:55], v8, s[8:11], s68 offen offset:2048 sc1\n\ts_cmp_le_u32 s4, 3\n\ts_cselect_b32 s68, 3072, 0\n\t" \
    "s_add_u32 s68, s68, s46\n\tv_mfma_f32_16x16x32_bf16 v[136:139], v[88:91], v[16:19], v[136:139]\n\ts_add_u32 s68, s68, 64512\n\ts_waitcnt lgkmcnt(8)\n\tv_mfma_f32_16x16x32_bf16 v[124:127], v[80:83], v[20:23], v[124:127]\n\tds_read_b64_tr_b16 v[40:41], v222 offset:24576\n\tds_read_b64_tr_b16 v[42:43], v222 offset:26624\n\tv_mfma_f32_16x16x32_bf16 v[132:135], v[84:87], v[20:23], v[132:135]\n\t" \
    "buffer_load_dwordx4 a[56:59], v8, s[8:11], s68 offen offset:0 sc1\n\tv_mfma_f32_16x16x32_bf16 v[140:143], v[88:91], v[20:23], v[140:143]\n\ts_waitcnt lgkmcnt(8)\n\tv_mfma_f32_16x16x32_bf16 v[144:147], v[80:83], v[24:27], v[144:147]\n\tds_read_b64_tr_b16 v[44:45], v223 offset:24576\n\tds_read_b64_tr_b16 v[46:47], v223 offset:26624\n\tv_mfma_f32_16x16x32_bf16 v[152:155], v[84:87], v[24:27], v[152:155]\n\tbuffer_load_dwordx4 a[60:63], v8, s[8:11], s68 offen offset:1024 sc1\n\t" \
    "v_mfma_f32_16x16x32_bf16 v[160:163], v[88:91], v[24:27], v[160:163]\n\ts_waitcnt lgkmcnt(8)\n\tv_mfma_f32_16x16x32_bf16 v[148:151], v[80:83], v[28:31], v[148:151]\n\tds_read_b64_tr_b16 v[16:17], v220 offset:28672\n\tds_read_b64_tr_b16 v[18:19], v220 offset:30720\n\tv_mfma_f32_16x16x32_bf16 v[156:159], v[84:87], v[28:31], v[156:159]\n\tv_mfma_f32_16x16x32_bf16 v[164:167], v[88:91], v[28:31], v[164:167]\n\ts_waitcnt lgkmcnt(8)\n\t" \
    "v_mfma_f32_16x16x32_bf16 v[168:171], v[80:83], v[32:35], v[168:171]\n\tds_read_b64_tr_b16 v[20:21], v221 offset:28672\n\tds_read_b64_tr_b16 v[22:23], v221 offset:30720\n\tv_mfma_f32_16x16x32_bf16 v[176:179], v[84:87], v[32:35], v[176:179]\n\tbuffer_load_dwordx4 a[64:67], v8, s[8:11], s68 offen offset:2048 sc1\n\ts_cmp_le_u32 s4, 4\n\ts_cselect_b32 s68, 3072, 0\n\ts_add_u32 s68, s68, s46\n\t" \
    "v_mfma_f32_16x16x32_bf16 v[184:187], v[88:91], v[32:35], v[184:187]\n\ts_add_u32 s68, s68, 67584\n\ts_waitcnt lgkmcnt(8)\n\tv_mfma_f32_16x16x32_bf16 v[172:175], v[80:83], v[36:39], v[172:175]\n\tds_read_b64_tr_b16 v[24:25], v222 offset:28672\n\tds_read_b64_tr_b16 v[26:27], v222 offset:30720\n\tv_mfma_f32_16x16x32_bf16 v[180:183], v[84:87], v[36:39], v[180:183]\n\tbuffer_load_dwordx4 a[68:71], v8, s[8:11], s68 offen offset:0 sc1\n\t" \
    "v_mfma_f32_16x16x32_bf16 v[188:191], v[88:91], v[36:39], v[188:191]\n\ts_waitcnt lgkmcnt(8)\n\tv_mfma_f32_16x16x32_bf16 v[48:51], v[80:83], v[40:43], v[48:51]\n\tds_read_b64_tr_b16 v[28:29], v223 offset:28672\n\tds_read_b64_tr_b16 v[30:31], v223 offset:30720\n\tv_mfma_f32_16x16x32_bf16 v[56:59], v[84:87], v[40:43], v[56:59]\n\tv_mfma_f32_16x16x32_bf16 v[64:67], v[88:91], v[40:43], v[64:67]\n\ts_waitcnt lgkmcnt(8)\n\t" \
    "v_mfma_f32_16x16x32_bf16 v[52:55], v[80:83], v[44:47], v[52:55]\n\tds_read_b64_tr_b16 v[32:33], v4 offset:61440\n\tds_read_b64_tr_b16 v[34:35], v4 offset:63488\n\tv_mfma_f32_16x16x32_bf16 v[60:63], v[84:87], v[44:47], v[60:63]\n\tbuffer_load_dwordx4 a[72:75], v8, s[8:11], s68 offen offset:1024 sc1\n\tv_mfma_f32_16x16x32_bf16 v[68:71], v[88:91], v[44:47], v[68:71]\n\ts_waitcnt lgkmcnt(8)\n\tv_mfma_f32_16x16x32_bf16 v[120:123], v[92:95], v[16:19], v[120:123]\n\t" \
    "ds_read_b64_tr_b16 v[36:37], v5 offset:61440\n\tds_read_b64_tr_b16 v[38:39], v5 offset:63488\n\tv_mfma_f32_16x16x32_bf16 v[128:131], v[96:99], v[16:19], v[128:131]\n\tbuffer_load_dwordx4 a[76:79], v8, s[8:11], s68 offen offset:2048 sc1\n\ts_cmp_le_u32 s4, 5\n\ts_cselect_b32 s68, 3072, 0\n\ts_add_u32 s68, s68, s46\n\tv_mfma_f32_16x16x32_bf16 v[136:139], v[100:103], v[16:19], v[136:139]\n\t" \
    "s_add_u32 s68, s68, 70656\n\ts_waitcnt lgkmcnt(8)\n\tv_mfma_f32_16x16x32_bf16 v[124:127], v[92:95], v[20:23], v[124:127]\n\tds_read_b64_tr_b16 v[40:41], v224 offset:0\n\tds_read_b64_tr_b16 v[42:43], v224 offset:2048\n\tv_mfma_f32_16x16x32_bf16 v[132:135], v[96:99], v[20:23], v[132:135]\n\tbuffer_load_dwordx4 a[80:83], v8, s[8:11], s68 offen offset:0 sc1\n\tv_mfma_f32_16x16x32_bf16 v[140:143], v[100:103], v[20:23], v[140:143]\n\t" \
    "s_waitcnt lgkmcnt(8)\n\tv_mfma_f32_16x16x32_bf16 v[144:147], v[92:95], v[24:27], v[144:147]\n\tds_read_b64_tr_b16 v[44:45], v225 offset:0\n\tds_read_b64_tr_b16 v[46:47], v225 offset:2048\n\tv_mfma_f32_16x16x32_bf16 v[152:155], v[96:99], v[24:27], v[152:155]\n\tv_mfma_f32_16x16x32_bf16 v[160:163], v[100:103], v[24:27], v[160:163]\n\ts_waitcnt lgkmcnt(8)\n\tv_mfma_f32_16x16x32_bf16 v[148:151], v[92:95], v[28:31], v[148:151]\n\t" \
    "ds_read_b64_tr_b16 v[16:17], v226 offset:0\n\tds_read_b64_tr_b16 v[18:19], v226 offset:2048\n\tv_mfma_f32_16x16x32_bf16 v[156:159], v[96:99], v[28:31], v[156:159]\n\tbuffer_load_dwordx4 a[84:87], v8, s[8:11], s68 offen offset:1024 sc1\n\tv_mfma_f32_16x16x32_bf16 v[164:167], v[100:103], v[28:31], v[164:167]\n\ts_waitcnt lgkmcnt(8)\n\tv_mfma_f32_16x16x32_bf16 v[168:171], v[92:95], v[32:35], v[168:171]\n\tds_read_b64_tr_b16 v[20:21], v227 offset:0\n\t" \
    "ds_read_b64_tr_b16 v[22:23], v227 offset:2048\n\tv_mfma_f32_16x16x32_bf16 v[176:179], v[96:99], v[32:35], v[176:179]\n\tbuffer_load_dwordx4 a[88:91], v8, s[8:11], s68 offen offset:2048 sc1\n\ts_cmp_le_u32 s4, 6\n\ts_cselect_b32 s68, 3072, 0\n\ts_add_u32 s68, s68, s46\n\tv_mfma_f32_16x16x32_bf16 v[184:187], v[100:103], v[32:35], v[184:187]\n\ts_add_u32 s68, s68, 73728\n\t" \
    "s_waitcnt lgkmcnt(8)\n\tv_mfma_f32_16x16x32_bf16 v[172:175], v[92:95], v[36:39], v[172:175]\n\tds_read_b64_tr_b16 v[24:25], v224 offset:4096\n\tds_read_b64_tr_b16 v[26:27], v224 offset:6144\n\tv_mfma_f32_16x16x32_bf16 v[180:183], v[96:99], v[36:39], v[180:183]\n\tbuffer_load_dwordx4 a[92:95], v8, s[8:11], s68 offen offset:0 sc1\n\tv_mfma_f32_16x16x32_bf16 v[188:191], v[100:103], v[36:39], v[188:191]\n\ts_waitcnt lgkmcnt(8)\n\t" \
    "v_mfma_f32_16x16x32_bf16 v[120:123], v[104:107], v[40:43], v[120:123]\n\tds_read_b64_tr_b16 v[28:29], v225 offset:4096\n\tds_read_b64_tr_b16 v[30:31], v225 offset:6144\n\tv_mfma_f32_16x16x32_bf16 v[128:131], v[108:111], v[40:43], v[128:131]\n\tv_mfma_f32_16x16x32_bf16 v[136:139], v[112:115], v[40:43], v[136:139]\n\ts_waitcnt lgkmcnt(8)\n\tv_mfma_f32_16x16x32_bf16 v[124:127], v[104:107], v[44:47], v[124:127]\n\tds_read_b64_tr_b16 v[32:33], v224 offset:8192\n\t" \
    "ds_read_b64_tr_b16 v[34:35], v224 offset:10240\n\tv_mfma_f32_16x16x32_bf16 v[132:135], v[108:111], v[44:47], v[132:135]\n\tbuffer_load_dwordx4 a[96:99], v8, s[8:11], s68 offen offset:1024 sc1\n\tv_mfma_f32_16x16x32_bf16 v[140:143], v[112:115], v[44:47], v[140:143]\n\ts_barrier\n\ts_waitcnt lgkmcnt(8)\n\tv_mfma_f32_16x16x32_bf16 v[144:147], v[104:107], v[16:19], v[144:147]\n\tds_read_b64_tr_b16 v[36:37], v225 offset:8192\n\t" \
    "ds_read_b64_tr_b16 v[38:39], v225 offset:10240\n\tv_mfma_f32_16x16x32_bf16 v[152:155], v[108:111], v[16:19], v[152:155]\n\tbuffer_load_dwordx4 a[100:103], v8, s[8:11], s68 offen offset:2048 sc1\n\ts_cmp_le_u32 s4, 7\n\ts_cselect_b32 s68, 3072, 0\n\ts_add_u32 s68, s68, s46\n\tv_mfma_f32_16x16x32_bf16 v[160:163], v[112:115], v[16:19], v[160:163]\n\ts_add_u32 s68, s68, 76800\n\t" \
    "s_waitcnt lgkmcnt(8)\n\tv_mfma_f32_16x16x32_bf16 v[148:151], v[104:107], v[20:23], v[148:151]\n\tds_read_b64_tr_b16 v[40:41], v226 offset:8192\n\tds_read_b64_tr_b16 v[42:43], v226 offset:10240\n\tv_mfma_f32_16x16x32_bf16 v[156:159], v[108:111], v[20:23], v[156:159]\n\tbuffer_load_dwordx4 a[104:107], v8, s[8:11], s68 offen offset:0 sc1\n\tv_mfma_f32_16x16x32_bf16 v[164:167], v[112:115], v[20:23], v[164:167]\n\ts_waitcnt lgkmcnt(8)\n\t" \
    "v_mfma_f32_16x16x32_bf16 v[168:171], v[104:107], v[24:27], v[168:171]\n\tds_read_b64_tr_b16 v[44:45], v227 offset:8192\n\tds_read_b64_tr_b16 v[46:47], v227 offset:10240\n\tv_mfma_f32_16x16x32_bf16 v[176:179], v[108:111], v[24:27], v[176:179]\n\tbuffer_load_dwordx4 a[108:111], v8, s[8:11], s68 offen offset:1024 sc1\n\tv_mfma_f32_16x16x32_bf16 v[184:187], v[112:115], v[24:27], v[184:187]\n\ts_waitcnt lgkmcnt(8)\n\tv_mfma_f32_16x16x32_bf16 v[172:175], v[104:107], v[28:31], v[172:175]\n\t" \
    "ds_read_b64_tr_b16 v[16:17], v224 offset:12288\n\tds_read_b64_tr_b16 v[18:19], v224 offset:14336\n\tv_mfma_f32_16x16x32_bf16 v[180:183], v[108:111], v[28:31], v[180:183]\n\tv_mfma_f32_16x16x32_bf16 v[188:191], v[112:115], v[28:31], v[188:191]\n\ts_waitcnt lgkmcnt(8)\n\tv_mfma_f32_16x16x32_bf16 v[120:123], v[116:119], v[32:35], v[120:123]\n\tds_read_b64_tr_b16 v[20:21], v225 offset:12288\n\tds_read_b64_tr_b16 v[22:23], v225 offset:14336\n\t" \
    "v_mfma_f32_16x16x32_bf16 v[128:131], a[0:3], v[32:35], v[128:131]\n\tbuffer_load_dwordx4 a[112:115], v8, s[8:11], s68 offen offset:2048 sc1\n\ts_cmp_le_u32 s4, 13\n\ts_cselect_b32 s69, 262144, 0\n\ts_add_u32 s69, s69, 3538944\n\tv_mfma_f32_16x16x32_bf16 v[136:139], a[4:7], v[32:35], v[136:139]\n\ts_add_u32 m0, s54, 135168\n\ts_waitcnt lgkmcnt(8)\n\t" \
    "v_mfma_f32_16x16x32_bf16 v[124:127], v[116:119], v[36:39], v[124:127]\n\tds_read_b64_tr_b16 v[24:25], v224 offset:16384\n\tds_read_b64_tr_b16 v[26:27], v224 offset:18432\n\tv_mfma_f32_16x16x32_bf16 v[132:135], a[0:3], v[36:39], v[132:135]\n\tbuffer_load_dwordx4 v12, s[16:19], s69 offen lds\n\ts_cmp_le_u32 s4, 14\n\ts_cselect_b32 s69, 262144, 0\n\ts_add_u32 s69, s69, 3801088\n\t" \
    "v_mfma_f32_16x16x32_bf16 v[140:143], a[4:7], v[36:39], v[140:143]\n\ts_add_u32 m0, s54, 139264\n\ts_waitcnt lgkmcnt(8)\n\tv_mfma_f32_16x16x32_bf16 v[144:147], v[116:119], v[40:43], v[144:147]\n\tds_read_b64_tr_b16 v[28:29], v225 offset:16384\n\tds_read_b64_tr_b16 v[30:31], v225 offset:18432\n\tv_mfma_f32_16x16x32_bf16 v[152:155], a[0:3], v[40:43], v[152:155]\n\tbuffer_load_dwordx4 v12, s[16:19], s69 offen lds\n\t" \
    "s_cmp_le_u32 s4, 15\n\ts_cselect_b32 s69, 262144, 0\n\ts_add_u32 s69, s69, 4063232\n\tv_mfma_f32_16x16x32_bf16 v[160:163], a[4:7], v[40:43], v[160:163]\n\ts_add_u32 m0, s54, 143360\n\ts_waitcnt lgkmcnt(8)\n\tv_mfma_f32_16x16x32_bf16 v[148:151], v[116:119], v[44:47], v[148:151]\n\tds_read_b64_tr_b16 v[32:33], v226 offset:16384\n\t" \
    "ds_read_b64_tr_b16 v[34:35], v226 offset:18432\n\tv_mfma_f32_16x16x32_bf16 v[156:159], a[0:3], v[44:47], v[156:159]\n\tv_mfma_f32_16x16x32_bf16 v[164:167], a[4:7], v[44:47], v[164:167]\n\ts_waitcnt lgkmcnt(8)\n\tv_mfma_f32_16x16x32_bf16 v[168:171], v[116:119], v[16:19], v[168:171]\n\tds_read_b64_tr_b16 v[36:37], v227 offset:16384\n\tds_read_b64_tr_b16 v[38:39], v227 offset:18432\n\tv_mfma_f32_16x16x32_bf16 v[176:179], a[0:3], v[16:19], v[176:179]\n\t" \
    "buffer_load_dwordx4 v12, s[16:19], s69 offen lds\n\ts_cmp_le_u32 s4, 16\n\ts_cselect_b32 s69, 262144, 0\n\ts_add_u32 s69, s69, 4325376\n\tv_mfma_f32_16x16x32_bf16 v[184:187], a[4:7], v[16:19], v[184:187]\n\ts_add_u32 m0, s54, 147456\n\ts_waitcnt lgkmcnt(8)\n\tv_mfma_f32_16x16x32_bf16 v[172:175], v[116:119], v[20:23], v[172:175]\n\t" \
    "ds_read_b64_tr_b16 v[40:41], v224 offset:20480\n\tds_read_b64_tr_b16 v[42:43], v224 offset:22528\n\tv_mfma_f32_16x16x32_bf16 v[180:183], a[0:3], v[20:23], v[180:183]\n\tbuffer_load_dwordx4 v12, s[16:19], s69 offen lds\n\ts_cmp_le_u32 s4, 8\n\ts_cselect_b32 s68, 3072, 0\n\ts_add_u32 s68, s68, s46\n\tv_mfma_f32_16x16x32_bf16 v[188:191], a[4:7], v[20:23], v[188:191]\n\t" \
    "s_add_u32 s68, s68, 79872\n\ts_waitcnt lgkmcnt(8)\n\tv_mfma_f32_16x16x32_bf16 v[120:123], a[8:11], v[24:27], v[120:123]\n\tds_read_b64_tr_b16 v[44:45], v225 offset:20480\n\tds_read_b64_tr_b16 v[46:47], v225 offset:22528\n\tv_mfma_f32_16x16x32_bf16 v[128:131], a[12:15], v[24:27], v[128:131]\n\tbuffer_load_dwordx4 a[116:119], v8, s[8:11], s68 offen offset:0 sc1\n\tv_mfma_f32_16x16x32_bf16 v[136:139], a[16:19], v[24:27], v[136:139]\n\t" \
    "s_waitcnt lgkmcnt(8)\n\tv_mfma_f32_16x16x32_bf16 v[124:127], a[8:11], v[28:31], v[124:127]\n\tds_read_b64_tr_b16 v[16:17], v224 offset:24576\n\tds_read_b64_tr_b16 v[18:19], v224 offset:26624\n\tv_mfma_f32_16x16x32_bf16 v[132:135], a[12:15], v[28:31], v[132:135]\n\tv_mfma_f32_16x16x32_bf16 v[140:143], a[16:19], v[28:31], v[140:143]\n\ts_waitcnt lgkmcnt(8)\n\tv_mfma_f32_16x16x32_bf16 v[144:147], a[8:11], v[32:35], v[144:147]\n\t" \
    "ds_read_b64_tr_b16 v[20:21], v225 offset:24576\n\tds_read_b64_tr_b16 v[22:23], v225 offset:26624\n\tv_mfma_f32_16x16x32_bf16 v[152:155], a[12:15], v[32:35], v[152:155]\n\tbuffer_load_dwordx4 a[120:123], v8, s[8:11], s68 offen offset:1024 sc1\n\tv_mfma_f32_16x16x32_bf16 v[160:163], a[16:19], v[32:35], v[160:163]\n\ts_waitcnt lgkmcnt(8)\n\tv_mfma_f32_16x16x32_bf16 v[148:151], a[8:11], v[36:39], v[148:151]\n\tds_read_b64_tr_b16 v[24:25], v226 offset:24576\n\t" \
    "ds_read_b64_tr_b16 v[26:27], v226 offset:26624\n\tv_mfma_f32_16x16x32_bf16 v[156:159], a[12:15], v[36:39], v[156:159]\n\tbuffer_load_dwordx4 a[124:127], v8, s[8:11], s68 offen offset:2048 sc1\n\ts_cmp_le_u32 s4, 9\n\ts_cselect_b32 s68, 3072, 0\n\ts_add_u32 s68, s68, s46\n\tv_mfma_f32_16x16x32_bf16 v[164:167], a[16:19], v[36:39], v[164:167]\n\ts_add_u32 s68, s68, 82944\n\t" \
    "s_waitcnt lgkmcnt(8)\n\tv_mfma_f32_16x16x32_bf16 v[168:171], a[8:11], v[40:43], v[168:171]\n\tds_read_b64_tr_b16 v[28:29], v227 offset:24576\n\tds_read_b64_tr_b16 v[30:31], v227 offset:26624\n\tv_mfma_f32_16x16x32_bf16 v[176:179], a[12:15], v[40:43], v[176:179]\n\tbuffer_load_dwordx4 a[128:131], v8, s[8:11], s68 offen offset:0 sc1\n\tv_mfma_f32_16x16x32_bf16 v[184:187], a[16:19], v[40:43], v[184:187]\n\ts_waitcnt lgkmcnt(8)\n\t" \
    "v_mfma_f32_16x16x32_bf16 v[172:175], a[8:11], v[44:47], v[172:175]\n\tds_read_b64_tr_b16 v[32:33], v224 offset:32768\n\tds_read_b64_tr_b16 v[34:35], v224 offset:34816\n\tv_mfma_f32_16x16x32_bf16 v[180:183], a[12:15], v[44:47], v[180:183]\n\tv_mfma_f32_16x16x32_bf16 v[188:191], a[16:19], v[44:47], v[188:191]\n\ts_waitcnt vmcnt(46)\n\ts_barrier\n\ts_waitcnt vmcnt(29) lgkmcnt(8)\n\t" \
    "v_mfma_f32_16x16x32_bf16 v[120:123], a[20:23], v[16:19], v[120:123]\n\tds_read_b64_tr_b16 v[36:37], v225 offset:32768\n\tds_read_b64_tr_b16 v[38:39], v225 offset:34816\n\tv_mfma_f32_16x16x32_bf16 v[128:131], a[24:27], v[16:19], v[128:131]\n\tbuffer_load_dwordx4 a[132:135], v8, s[8:11], s68 offen offset:1024 sc1\n\tv_mfma_f32_16x16x32_bf16 v[136:139], a[28:31], v[16:19], v[136:139]\n\ts_waitcnt lgkmcnt(8)\n\tv_mfma_f32_16x16x32_bf16 v[124:127], a[20:23], v[20:23], v[124:127]\n\t" \
    "ds_read_b64_tr_b16 v[40:41], v224 offset:36864\n\tds_read_b64_tr_b16 v[42:43], v224 offset:38912\n\tv_mfma_f32_16x16x32_bf16 v[132:135], a[24:27], v[20:23], v[132:135]\n\tbuffer_load_dwordx4 a[136:139], v8, s[8:11], s68 offen offset:2048 sc1\n\ts_cmp_le_u32 s4, 10\n\ts_cselect_b32 s68, 3072, 0\n\ts_add_u32 s68, s68, s46\n\tv_mfma_f32_16x16x32_bf16 v[140:143], a[28:31], v[20:23], v[140:143]\n\t" \
    "s_add_u32 s68, s68, 86016\n\ts_waitcnt lgkmcnt(8)\n\tv_mfma_f32_16x16x32_bf16 v[144:147], a[20:23], v[24:27], v[144:147]\n\tds_read_b64_tr_b16 v[44:45], v225 offset:36864\n\tds_read_b64_tr_b16 v[46:47], v225 offset:38912\n\tv_mfma_f32_16x16x32_bf16 v[152:155], a[24:27], v[24:27], v[152:155]\n\tbuffer_load_dwordx4 a[140:143], v8, s[8:11], s68 offen offset:0 sc1\n\tv_mfma_f32_16x16x32_bf16 v[160:163], a[28:31], v[24:27], v[160:163]\n\t" \
    "s_waitcnt lgkmcnt(8)\n\tv_mfma_f32_16x16x32_bf16 v[148:151], a[20:23], v[28:31], v[148:151]\n\tds_read_b64_tr_b16 v[16:17], v226 offset:36864\n\tds_read_b64_tr_b16 v[18:19], v226 offset:38912\n\tv_mfma_f32_16x16x32_bf16 v[156:159], a[24:27], v[28:31], v[156:159]\n\tbuffer_load_dwordx4 a[144:147], v8, s[8:11], s68 offen offset:1024 sc1\n\tv_mfma_f32_16x16x32_bf16 v[164:167], a[28:31], v[28:31], v[164:167]\n\ts_waitcnt lgkmcnt(8)\n\t" \
    "v_mfma_f32_16x16x32_bf16 v[168:171], a[20:23], v[32:35], v[168:171]\n\tds_read_b64_tr_b16 v[20:21], v227 offset:36864\n\tds_read_b64_tr_b16 v[22:23], v227 offset:38912\n\tv_mfma_f32_16x16x32_bf16 v[176:179], a[24:27], v[32:35], v[176:179]\n\tv_mfma_f32_16x16x32_bf16 v[184:187], a[28:31], v[32:35], v[184:187]\n\ts_waitcnt lgkmcnt(8)\n\tv_mfma_f32_16x16x32_bf16 v[172:175], a[20:23], v[36:39], v[172:175]\n\tds_read_b64_tr_b16 v[24:25], v224 offset:40960\n\t" \
    "ds_read_b64_tr_b16 v[26:27], v224 offset:43008\n\tv_mfma_f32_16x16x32_bf16 v[180:183], a[24:27], v[36:39], v[180:183]\n\tbuffer_load_dwordx4 a[148:151], v8, s[8:11], s68 offen offset:2048 sc1\n\ts_cmp_le_u32 s4, 11\n\ts_cselect_b32 s68, 3072, 0\n\ts_add_u32 s68, s68, s46\n\tv_mfma_f32_16x16x32_bf16 v[188:191], a[28:31], v[36:39], v[188:191]\n\ts_add_u32 s68, s68, 89088\n\t" \
    "s_waitcnt vmcnt(31) lgkmcnt(8)\n\tv_mfma_f32_16x16x32_bf16 v[120:123], a[32:35], v[40:43], v[120:123]\n\tds_read_b64_tr_b16 v[28:29], v225 offset:40960\n\tds_read_b64_tr_b16 v[30:31], v225 offset:43008\n\tv_mfma_f32_16x16x32_bf16 v[128:131], a[36:39], v[40:43], v[128:131]\n\tbuffer_load_dwordx4 a[152:155], v8, s[8:11], s68 offen offset:0 sc1\n\tv_mfma_f32_16x16x32_bf16 v[136:139], a[40:43], v[40:43], v[136:139]\n\ts_waitcnt lgkmcnt(8)\n\t" \
    "v_mfma_f32_16x16x32_bf16 v[124:127], a[32:35], v[44:47], v[124:127]\n\tds_read_b64_tr_b16 v[32:33], v224 offset:45056\n\tds_read_b64_tr_b16 v[34:35], v224 offset:47104\n\tv_mfma_f32_16x16x32_bf16 v[132:135], a[36:39], v[44:47], v[132:135]\n\tbuffer_load_dwordx4 a[156:159], v8, s[8:11], s68 offen offset:1024 sc1\n\tv_mfma_f32_16x16x32_bf16 v[140:143], a[40:43], v[44:47], v[140:143]\n\ts_waitcnt lgkmcnt(8)\n\tv_mfma_f32_16x16x32_bf16 v[144:147], a[32:35], v[16:19], v[144:147]\n\t" \
    "ds_read_b64_tr_b16 v[36:37], v225 offset:45056\n\tds_read_b64_tr_b16 v[38:39], v225 offset:47104\n\tv_mfma_f32_16x16x32_bf16 v[152:155], a[36:39], v[16:19], v[152:155]\n\tv_mfma_f32_16x16x32_bf16 v[160:163], a[40:43], v[16:19], v[160:163]\n\ts_waitcnt lgkmcnt(8)\n\tv_mfma_f32_16x16x32_bf16 v[148:151], a[32:35], v[20:23], v[148:151]\n\tds_read_b64_tr_b16 v[40:41], v226 offset:45056\n\tds_read_b64_tr_b16 v[42:43], v226 offset:47104\n\t" \
    "v_mfma_f32_16x16x32_bf16 v[156:159], a[36:39], v[20:23], v[156:159]\n\tbuffer_load_dwordx4 a[160:163], v8, s[8:11], s68 offen offset:2048 sc1\n\ts_cmp_le_u32 s4, 12\n\ts_cselect_b32 s68, 3072, 0\n\ts_add_u32 s68, s68, s46\n\tv_mfma_f32_16x16x32_bf16 v[164:167], a[40:43], v[20:23], v[164:167]\n\ts_add_u32 s68, s68, 92160\n\ts_waitcnt lgkmcnt(8)\n\t" \
    "v_mfma_f32_16x16x32_bf16 v[168:171], a[32:35], v[24:27], v[168:171]\n\tds_read_b64_tr_b16 v[44:45], v227 offset:45056\n\tds_read_b64_tr_b16 v[46:47], v227 offset:47104\n\tv_mfma_f32_16x16x32_bf16 v[176:179], a[36:39], v[24:27], v[176:179]\n\tbuffer_load_dwordx4 a[164:167], v8, s[8:11], s68 offen offset:0 sc1\n\tv_mfma_f32_16x16x32_bf16 v[184:187], a[40:43], v[24:27], v[184:187]\n\ts_waitcnt lgkmcnt(8)\n\tv_mfma_f32_16x16x32_bf16 v[172:175], a[32:35], v[28:31], v[172:175]\n\t" \
    "ds_read_b64_tr_b16 v[16:17], v224 offset:49152\n\tds_read_b64_tr_b16 v[18:19], v224 offset:51200\n\tv_mfma_f32_16x16x32_bf16 v[180:183], a[36:39], v[28:31], v[180:183]\n\tv_mfma_f32_16x16x32_bf16 v[188:191], a[40:43], v[28:31], v[188:191]\n\ts_waitcnt vmcnt(32) lgkmcnt(8)\n\tv_mfma_f32_16x16x32_bf16 v[120:123], a[44:47], v[32:35], v[120:123]\n\tds_read_b64_tr_b16 v[20:21], v225 offset:49152\n\tds_read_b64_tr_b16 v[22:23], v225 offset:51200\n\t" \
    "v_mfma_f32_16x16x32_bf16 v[128:131], a[48:51], v[32:35], v[128:131]\n\tbuffer_load_dwordx4 a[168:171], v8, s[8:11], s68 offen offset:1024 sc1\n\tv_mfma_f32_16x16x32_bf16 v[136:139], a[52:55], v[32:35], v[136:139]\n\ts_waitcnt lgkmcnt(8)\n\tv_mfma_f32_16x16x32_bf16 v[124:127], a[44:47], v[36:39], v[124:127]\n\tds_read_b64_tr_b16 v[24:25], v224 offset:53248\n\tds_read_b64_tr_b16 v[26:27], v224 offset:55296\n\tv_mfma_f32_16x16x32_bf16 v[132:135], a[48:51], v[36:39], v[132:135]\n\t" \
    "buffer_load_dwordx4 a[172:175], v8, s[8:11], s68 offen offset:2048 sc1\n\ts_cmp_le_u32 s4, 13\n\ts_cselect_b32 s68, 3072, 0\n\ts_add_u32 s68, s68, s46\n\tv_mfma_f32_16x16x32_bf16 v[140:143], a[52:55], v[36:39], v[140:143]\n\ts_add_u32 s68, s68, 95232\n\ts_waitcnt lgkmcnt(8)\n\tv_mfma_f32_16x16x32_bf16 v[144:147], a[44:47], v[40:43], v[144:147]\n\t" \
    "ds_read_b64_tr_b16 v[28:29], v225 offset:53248\n\tds_read_b64_tr_b16 v[30:31], v225 offset:55296\n\tv_mfma_f32_16x16x32_bf16 v[152:155], a[48:51], v[40:43], v[152:155]\n\tbuffer_load_dwordx4 a[176:179], v8, s[8:11], s68 offen offset:0 sc1\n\tv_mfma_f32_16x16x32_bf16 v[160:163], a[52:55], v[40:43], v[160:163]\n\ts_waitcnt lgkmcnt(8)\n\tv_mfma_f32_16x16x32_bf16 v[148:151], a[44:47], v[44:47], v[148:151]\n\tds_read_b64_tr_b16 v[32:33], v226 offset:53248\n\t" \
    "ds_read_b64_tr_b16 v[34:35], v226 offset:55296\n\tv_mfma_f32_16x16x32_bf16 v[156:159], a[48:51], v[44:47], v[156:159]\n\tv_mfma_f32_16x16x32_bf16 v[164:167], a[52:55], v[44:47], v[164:167]\n\ts_barrier\n\ts_waitcnt lgkmcnt(8)\n\tv_mfma_f32_16x16x32_bf16 v[168:171], a[44:47], v[16:19], v[168:171]\n\tds_read_b64_tr_b16 v[36:37], v227 offset:53248\n\tds_read_b64_tr_b16 v[38:39], v227 offset:55296\n\t" \
    "v_mfma_f32_16x16x32_bf16 v[176:179], a[48:51], v[16:19], v[176:179]\n\tbuffer_load_dwordx4 a[180:183], v8, s[8:11], s68 offen offset:1024 sc1\n\tv_mfma_f32_16x16x32_bf16 v[184:187], a[52:55], v[16:19], v[184:187]\n\ts_waitcnt lgkmcnt(8)\n\tv_mfma_f32_16x16x32_bf16 v[172:175], a[44:47], v[20:23], v[172:175]\n\tds_read_b64_tr_b16 v[40:41], v224 offset:57344\n\tds_read_b64_tr_b16 v[42:43], v224 offset:59392\n\tv_mfma_f32_16x16x32_bf16 v[180:183], a[48:51], v[20:23], v[180:183]\n\t" \
    "buffer_load_dwordx4 a[184:187], v8, s[8:11], s68 offen offset:2048 sc1\n\ts_cmp_le_u32 s4, 14\n\ts_cselect_b32 s68, 3072, 0\n\ts_add_u32 s68, s68, s46\n\tv_mfma_f32_16x16x32_bf16 v[188:191], a[52:55], v[20:23], v[188:191]\n\ts_add_u32 s68, s68, 98304\n\ts_waitcnt vmcnt(34) lgkmcnt(8)\n\tv_mfma_f32_16x16x32_bf16 v[120:123], a[56:59], v[24:27], v[120:123]\n\t" \
    "ds_read_b64_tr_b16 v[44:45], v225 offset:57344\n\tds_read_b64_tr_b16 v[46:47], v225 offset:59392\n\tv_mfma_f32_16x16x32_bf16 v[128:131], a[60:63], v[24:27], v[128:131]\n\tbuffer_load_dwordx4 a[188:191], v8, s[8:11], s68 offen offset:0 sc1\n\tv_mfma_f32_16x16x32_bf16 v[136:139], a[64:67], v[24:27], v[136:139]\n\ts_waitcnt lgkmcnt(8)\n\tv_mfma_f32_16x16x32_bf16 v[124:127], a[56:59], v[28:31], v[124:127]\n\tds_read_b64_tr_b16 v[16:17], v224 offset:61440\n\t" \
    "ds_read_b64_tr_b16 v[18:19], v224 offset:63488\n\tv_mfma_f32_16x16x32_bf16 v[132:135], a[60:63], v[28:31], v[132:135]\n\tbuffer_load_dwordx4 a[192:195], v8, s[8:11], s68 offen offset:1024 sc1\n\tv_mfma_f32_16x16x32_bf16 v[140:143], a[64:67], v[28:31], v[140:143]\n\ts_waitcnt lgkmcnt(8)\n\tv_mfma_f32_16x16x32_bf16 v[144:147], a[56:59], v[32:35], v[144:147]\n\tds_read_b64_tr_b16 v[20:21], v225 offset:61440\n\tds_read_b64_tr_b16 v[22:23], v225 offset:63488\n\t" \
    "v_mfma_f32_16x16x32_bf16 v[152:155], a[60:63], v[32:35], v[152:155]\n\tbuffer_load_dwordx4 a[196:199], v8, s[8:11], s68 offen offset:2048 sc1\n\ts_cmp_le_u32 s4, 15\n\ts_cselect_b32 s68, 3072, 0\n\ts_add_u32 s68, s68, s46\n\tv_mfma_f32_16x16x32_bf16 v[160:163], a[64:67], v[32:35], v[160:163]\n\ts_add_u32 s68, s68, 101376\n\ts_waitcnt lgkmcnt(8)\n\t" \
    "v_mfma_f32_16x16x32_bf16 v[148:151], a[56:59], v[36:39], v[148:151]\n\tds_read_b64_tr_b16 v[24:25], v226 offset:61440\n\tds_read_b64_tr_b16 v[26:27], v226 offset:63488\n\tv_mfma_f32_16x16x32_bf16 v[156:159], a[60:63], v[36:39], v[156:159]\n\tv_mfma_f32_16x16x32_bf16 v[164:167], a[64:67], v[36:39], v[164:167]\n\ts_waitcnt lgkmcnt(8)\n\tv_mfma_f32_16x16x32_bf16 v[168:171], a[56:59], v[40:43], v[168:171]\n\tds_read_b64_tr_b16 v[28:29], v227 offset:61440\n\t" \
    "ds_read_b64_tr_b16 v[30:31], v227 offset:63488\n\tv_mfma_f32_16x16x32_bf16 v[176:179], a[60:63], v[40:43], v[176:179]\n\tbuffer_load_dwordx4 a[200:203], v8, s[8:11], s68 offen offset:0 sc1\n\tv_mfma_f32_16x16x32_bf16 v[184:187], a[64:67], v[40:43], v[184:187]\n\ts_waitcnt lgkmcnt(8)\n\tv_mfma_f32_16x16x32_bf16 v[172:175], a[56:59], v[44:47], v[172:175]\n\tds_read_b64_tr_b16 v[32:33], v4 offset:0\n\tds_read_b64_tr_b16 v[34:35], v4 offset:2048\n\t" \
    "v_mfma_f32_16x16x32_bf16 v[180:183], a[60:63], v[44:47], v[180:183]\n\tbuffer_load_dwordx4 a[204:207], v8, s[8:11], s68 offen offset:1024 sc1\n\tv_mfma_f32_16x16x32_bf16 v[188:191], a[64:67], v[44:47], v[188:191]\n\ts_waitcnt vmcnt(36) lgkmcnt(8)\n\tv_mfma_f32_16x16x32_bf16 v[120:123], a[68:71], v[16:19], v[120:123]\n\tds_read_b64_tr_b16 v[36:37], v5 offset:0\n\tds_read_b64_tr_b16 v[38:39], v5 offset:2048\n\tv_mfma_f32_16x16x32_bf16 v[128:131], a[72:75], v[16:19], v[128:131]\n\t" \
    "v_mfma_f32_16x16x32_bf16 v[136:139], a[76:79], v[16:19], v[136:139]\n\ts_waitcnt lgkmcnt(8)\n\tv_mfma_f32_16x16x32_bf16 v[124:127], a[68:71], v[20:23], v[124:127]\n\tds_read_b64_tr_b16 v[40:41], v4 offset:4096\n\tds_read_b64_tr_b16 v[42:43], v4 offset:6144\n\tv_mfma_f32_16x16x32_bf16 v[132:135], a[72:75], v[20:23], v[132:135]\n\tbuffer_load_dwordx4 a[208:211], v8, s[8:11], s68 offen offset:2048 sc1\n\ts_cmp_le_u32 s4, 8\n\t" \
    "s_cselect_b32 s69, 262144, 0\n\ts_add_u32 s69, s69, 2228224\n\tv_mfma_f32_16x16x32_bf16 v[140:143], a[76:79], v[20:23], v[140:143]\n\ts_add_u32 m0, s54, 151552\n\ts_waitcnt lgkmcnt(8)\n\tv_mfma_f32_16x16x32_bf16 v[144:147], a[68:71], v[24:27], v[144:147]\n\tds_read_b64_tr_b16 v[44:45], v5 offset:4096\n\tds_read_b64_tr_b16 v[46:47], v5 offset:6144\n\t" \
    "v_mfma_f32_16x16x32_bf16 v[152:155], a[72:75], v[24:27], v[152:155]\n\tbuffer_load_dwordx4 v13, s[16:19], s69 offen lds\n\ts_cmp_le_u32 s4, 16\n\ts_cselect_b32 s68, 3072, 0\n\ts_add_u32 s68, s68, s46\n\tv_mfma_f32_16x16x32_bf16 v[160:163], a[76:79], v[24:27], v[160:163]\n\ts_add_u32 s68, s68, 104448\n\ts_waitcnt lgkmcnt(8)\n\t" \
    "v_mfma_f32_16x16x32_bf16 v[148:151], a[68:71], v[28:31], v[148:151]\n\tds_read_b64_tr_b16 v[16:17], v6 offset:4096\n\tds_read_b64_tr_b16 v[18:19], v6 offset:6144\n\tv_mfma_f32_16x16x32_bf16 v[156:159], a[72:75], v[28:31], v[156:159]\n\tbuffer_load_dwordx4 a[212:215], v8, s[8:11], s68 offen offset:0 sc1\n\tv_mfma_f32_16x16x32_bf16 v[164:167], a[76:79], v[28:31], v[164:167]\n\ts_waitcnt lgkmcnt(8)\n\tv_mfma_f32_16x16x32_bf16 v[168:171], a[68:71], v[32:35], v[168:171]\n\t" \
    "ds_read_b64_tr_b16 v[20:21], v7 offset:4096\n\tds_read_b64_tr_b16 v[22:23], v7 offset:6144\n\tv_mfma_f32_16x16x32_bf16 v[176:179], a[72:75], v[32:35], v[176:179]\n\tv_mfma_f32_16x16x32_bf16 v[184:187], a[76:79], v[32:35], v[184:187]\n\ts_waitcnt lgkmcnt(8)\n\tv_mfma_f32_16x16x32_bf16 v[172:175], a[68:71], v[36:39], v[172:175]\n\tds_read_b64_tr_b16 v[24:25], v4 offset:8192\n\tds_read_b64_tr_b16 v[26:27], v4 offset:10240\n\t" \
    "v_mfma_f32_16x16x32_bf16 v[180:183], a[72:75], v[36:39], v[180:183]\n\tbuffer_load_dwordx4 a[216:219], v8, s[8:11], s68 offen offset:1024 sc1\n\tv_mfma_f32_16x16x32_bf16 v[188:191], a[76:79], v[36:39], v[188:191]\n\ts_waitcnt vmcnt(37) lgkmcnt(8)\n\tv_mfma_f32_16x16x32_bf16 v[120:123], a[80:83], v[40:43], v[120:123]\n\tds_read_b64_tr_b16 v[28:29], v5 offset:8192\n\tds_read_b64_tr_b16 v[30:31], v5 offset:10240\n\tv_mfma_f32_16x16x32_bf16 v[128:131], a[84:87], v[40:43], v[128:131]\n\t" \
    "buffer_load_dwordx4 a[220:223], v8, s[8:11], s68 offen offset:2048 sc1\n\ts_cmp_le_u32 s4, 9\n\ts_cselect_b32 s69, 262144, 0\n\ts_add_u32 s69, s69, 2490368\n\tv_mfma_f32_16x16x32_bf16 v[136:139], a[88:91], v[40:43], v[136:139]\n\ts_add_u32 m0, s54, 155648\n\ts_waitcnt lgkmcnt(8)\n\tv_mfma_f32_16x16x32_bf16 v[124:127], a[80:83], v[44:47], v[124:127]\n\t" \
    "ds_read_b64_tr_b16 v[32:33], v4 offset:12288\n\tds_read_b64_tr_b16 v[34:35], v4 offset:14336\n\tv_mfma_f32_16x16x32_bf16 v[132:135], a[84:87], v[44:47], v[132:135]\n\tbuffer_load_dwordx4 v13, s[16:19], s69 offen lds\n\ts_cmp_le_u32 s4, 10\n\ts_cselect_b32 s69, 262144, 0\n\ts_add_u32 s69, s69, 2752512\n\tv_mfma_f32_16x16x32_bf16 v[140:143], a[88:91], v[44:47], v[140:143]\n\t" \
    "s_add_u32 m0, s54, 159744\n\ts_barrier\n\ts_waitcnt lgkmcnt(8)\n\tv_mfma_f32_16x16x32_bf16 v[144:147], a[80:83], v[16:19], v[144:147]\n\tds_read_b64_tr_b16 v[36:37], v5 offset:12288\n\tds_read_b64_tr_b16 v[38:39], v5 offset:14336\n\tv_mfma_f32_16x16x32_bf16 v[152:155], a[84:87], v[16:19], v[152:155]\n\tbuffer_load_dwordx4 v13, s[16:19], s69 offen lds\n\t" \
    "s_cmp_le_u32 s4, 11\n\ts_cselect_b32 s69, 262144, 0\n\ts_add_u32 s69, s69, 3014656\n\tv_mfma_f32_16x16x32_bf16 v[160:163], a[88:91], v[16:19], v[160:163]\n\ts_add_u32 m0, s54, 40960\n\ts_waitcnt lgkmcnt(8)\n\tv_mfma_f32_16x16x32_bf16 v[148:151], a[80:83], v[20:23], v[148:151]\n\tds_read_b64_tr_b16 v[40:41], v6 offset:12288\n\t" \
    "ds_read_b64_tr_b16 v[42:43], v6 offset:14336\n\tv_mfma_f32_16x16x32_bf16 v[156:159], a[84:87], v[20:23], v[156:159]\n\tbuffer_load_dwordx4 v13, s[16:19], s69 offen lds\n\ts_cmp_le_u32 s4, 12\n\ts_cselect_b32 s69, 262144, 0\n\ts_add_u32 s69, s69, 3276800\n\tv_mfma_f32_16x16x32_bf16 v[164:167], a[88:91], v[20:23], v[164:167]\n\ts_add_u32 m0, s54, 45056\n\t" \
    "s_waitcnt lgkmcnt(8)\n\tv_mfma_f32_16x16x32_bf16 v[168:171], a[80:83], v[24:27], v[168:171]\n\tds_read_b64_tr_b16 v[44:45], v7 offset:12288\n\tds_read_b64_tr_b16 v[46:47], v7 offset:14336\n\tv_mfma_f32_16x16x32_bf16 v[176:179], a[84:87], v[24:27], v[176:179]\n\tv_mfma_f32_16x16x32_bf16 v[184:187], a[88:91], v[24:27], v[184:187]\n\ts_waitcnt lgkmcnt(8)\n\tv_mfma_f32_16x16x32_bf16 v[172:175], a[80:83], v[28:31], v[172:175]\n\t" \
    "ds_read_b64_tr_b16 v[16:17], v4 offset:16384\n\tds_read_b64_tr_b16 v[18:19], v4 offset:18432\n\tv_mfma_f32_16x16x32_bf16 v[180:183], a[84:87], v[28:31], v[180:183]\n\tbuffer_load_dwordx4 v13, s[16:19], s69 offen lds\n\ts_cmp_le_u32 s4, 13\n\ts_cselect_b32 s69, 262144, 0\n\ts_add_u32 s69, s69, 3538944\n\tv_mfma_f32_16x16x32_bf16 v[188:191], a[88:91], v[28:31], v[188:191]\n\t" \
    "s_add_u32 m0, s54, 49152\n\ts_waitcnt vmcnt(39) lgkmcnt(8)\n\tv_mfma_f32_16x16x32_bf16 v[120:123], a[92:95], v[32:35], v[120:123]\n\tds_read_b64_tr_b16 v[20:21], v5 offset:16384\n\tds_read_b64_tr_b16 v[22:23], v5 offset:18432\n\tv_mfma_f32_16x16x32_bf16 v[128:131], a[96:99], v[32:35], v[128:131]\n\tbuffer_load_dwordx4 v13, s[16:19], s69 offen lds\n\ts_cmp_le_u32 s4, 14\n\t" \
    "s_cselect_b32 s69, 262144, 0\n\ts_add_u32 s69, s69, 3801088\n\tv_mfma_f32_16x16x32_bf16 v[136:139], a[100:103], v[32:35], v[136:139]\n\ts_add_u32 m0, s54, 53248\n\ts_waitcnt lgkmcnt(8)\n\tv_mfma_f32_16x16x32_bf16 v[124:127], a[92:95], v[36:39], v[124:127]\n\tds_read_b64_tr_b16 v[24:25], v4 offset:20480\n\tds_read_b64_tr_b16 v[26:27], v4 offset:22528\n\t" \
    "v_mfma_f32_16x16x32_bf16 v[132:135], a[96:99], v[36:39], v[132:135]\n\tbuffer_load_dwordx4 v13, s[16:19], s69 offen lds\n\ts_cmp_le_u32 s4, 15\n\ts_cselect_b32 s69, 262144, 0\n\ts_add_u32 s69, s69, 4063232\n\tv_mfma_f32_16x16x32_bf16 v[140:143], a[100:103], v[36:39], v[140:143]\n\ts_add_u32 m0, s54, 57344\n\ts_waitcnt lgkmcnt(8)\n\t" \
    "v_mfma_f32_16x16x32_bf16 v[144:147], a[92:95], v[40:43], v[144:147]\n\tds_read_b64_tr_b16 v[28:29], v5 offset:20480\n\tds_read_b64_tr_b16 v[30:31], v5 offset:22528\n\tv_mfma_f32_16x16x32_bf16 v[152:155], a[96:99], v[40:43], v[152:155]\n\tv_mfma_f32_16x16x32_bf16 v[160:163], a[100:103], v[40:43], v[160:163]\n\ts_waitcnt lgkmcnt(8)\n\tv_mfma_f32_16x16x32_bf16 v[148:151], a[92:95], v[44:47], v[148:151]\n\tds_read_b64_tr_b16 v[32:33], v6 offset:20480\n\t" \
    "ds_read_b64_tr_b16 v[34:35], v6 offset:22528\n\tv_mfma_f32_16x16x32_bf16 v[156:159], a[96:99], v[44:47], v[156:159]\n\tbuffer_load_dwordx4 v13, s[16:19], s69 offen lds\n\ts_cmp_le_u32 s4, 16\n\ts_cselect_b32 s69, 262144, 0\n\ts_add_u32 s69, s69, 4325376\n\tv_mfma_f32_16x16x32_bf16 v[164:167], a[100:103], v[44:47], v[164:167]\n\ts_add_u32 m0, s54, 65536\n\t" \
    "s_waitcnt lgkmcnt(8)\n\tv_mfma_f32_16x16x32_bf16 v[168:171], a[92:95], v[16:19], v[168:171]\n\tds_read_b64_tr_b16 v[36:37], v7 offset:20480\n\tds_read_b64_tr_b16 v[38:39], v7 offset:22528\n\tv_mfma_f32_16x16x32_bf16 v[176:179], a[96:99], v[16:19], v[176:179]\n\tbuffer_load_dwordx4 v13, s[16:19], s69 offen lds\n\tv_mfma_f32_16x16x32_bf16 v[184:187], a[100:103], v[16:19], v[184:187]\n\ts_waitcnt lgkmcnt(8)\n\t" \
    "v_mfma_f32_16x16x32_bf16 v[172:175], a[92:95], v[20:23], v[172:175]\n\tds_read_b64_tr_b16 v[40:41], v4 offset:24576\n\tds_read_b64_tr_b16 v[42:43], v4 offset:26624\n\tv_mfma_f32_16x16x32_bf16 v[180:183], a[96:99], v[20:23], v[180:183]\n\tv_mfma_f32_16x16x32_bf16 v[188:191], a[100:103], v[20:23], v[188:191]\n\ts_waitcnt vmcnt(40) lgkmcnt(8)\n\tv_mfma_f32_16x16x32_bf16 v[120:123], a[104:107], v[24:27], v[120:123]\n\tds_read_b64_tr_b16 v[44:45], v5 offset:24576\n\t" \
    "ds_read_b64_tr_b16 v[46:47], v5 offset:26624\n\tv_mfma_f32_16x16x32_bf16 v[128:131], a[108:111], v[24:27], v[128:131]\n\tbuffer_load_dwordx4 v[192:195], v243, s[20:23], 0 offen offset:0\n\tv_mfma_f32_16x16x32_bf16 v[136:139], a[112:115], v[24:27], v[136:139]\n\ts_waitcnt lgkmcnt(8)\n\tv_mfma_f32_16x16x32_bf16 v[124:127], a[104:107], v[28:31], v[124:127]\n\tds_read_b64_tr_b16 v[16:17], v4 offset:28672\n\tds_read_b64_tr_b16 v[18:19], v4 offset:30720\n\t" \
    "v_mfma_f32_16x16x32_bf16 v[132:135], a[108:111], v[28:31], v[132:135]\n\tbuffer_load_dwordx4 v[196:199], v243, s[20:23], 0 offen offset:32\n\tv_mfma_f32_16x16x32_bf16 v[140:143], a[112:115], v[28:31], v[140:143]\n\ts_waitcnt lgkmcnt(8)\n\tv_mfma_f32_16x16x32_bf16 v[144:147], a[104:107], v[32:35], v[144:147]\n\tds_read_b64_tr_b16 v[20:21], v5 offset:28672\n\tds_read_b64_tr_b16 v[22:23], v5 offset:30720\n\tv_mfma_f32_16x16x32_bf16 v[152:155], a[108:111], v[32:35], v[152:155]\n\t" \
    "buffer_load_dwordx4 v[200:203], v243, s[20:23], 0 offen offset:64\n\tv_mfma_f32_16x16x32_bf16 v[160:163], a[112:115], v[32:35], v[160:163]\n\ts_waitcnt lgkmcnt(8)\n\tv_mfma_f32_16x16x32_bf16 v[148:151], a[104:107], v[36:39], v[148:151]\n\tds_read_b64_tr_b16 v[24:25], v220 offset:0\n\tds_read_b64_tr_b16 v[26:27], v220 offset:2048\n\tv_mfma_f32_16x16x32_bf16 v[156:159], a[108:111], v[36:39], v[156:159]\n\tv_mfma_f32_16x16x32_bf16 v[164:167], a[112:115], v[36:39], v[164:167]\n\t" \
    "s_waitcnt lgkmcnt(8)\n\tv_mfma_f32_16x16x32_bf16 v[168:171], a[104:107], v[40:43], v[168:171]\n\tds_read_b64_tr_b16 v[28:29], v221 offset:0\n\tds_read_b64_tr_b16 v[30:31], v221 offset:2048\n\tv_mfma_f32_16x16x32_bf16 v[176:179], a[108:111], v[40:43], v[176:179]\n\tbuffer_load_dwordx2 v[216:217], v242, s[8:11], s59 offen offset:0 sc1\n\tv_mfma_f32_16x16x32_bf16 v[184:187], a[112:115], v[40:43], v[184:187]\n\ts_waitcnt lgkmcnt(8)\n\t" \
    "v_mfma_f32_16x16x32_bf16 v[172:175], a[104:107], v[44:47], v[172:175]\n\tds_read_b64_tr_b16 v[32:33], v4 offset:32768\n\tds_read_b64_tr_b16 v[34:35], v4 offset:34816\n\tv_mfma_f32_16x16x32_bf16 v[180:183], a[108:111], v[44:47], v[180:183]\n\tbuffer_load_dwordx2 v[218:219], v242, s[8:11], s59 offen offset:128 sc1\n\tv_mfma_f32_16x16x32_bf16 v[188:191], a[112:115], v[44:47], v[188:191]\n\ts_waitcnt vmcnt(12)\n\ts_barrier\n\t" \
    "s_waitcnt lgkmcnt(8)\n\tv_mfma_f32_16x16x32_bf16 v[120:123], a[116:119], v[16:19], v[120:123]\n\tds_read_b64_tr_b16 v[36:37], v5 offset:32768\n\tds_read_b64_tr_b16 v[38:39], v5 offset:34816\n\tv_mfma_f32_16x16x32_bf16 v[128:131], a[120:123], v[16:19], v[128:131]\n\tv_mfma_f32_16x16x32_bf16 v[136:139], a[124:127], v[16:19], v[136:139]\n\ts_waitcnt lgkmcnt(8)\n\tv_mfma_f32_16x16x32_bf16 v[124:127], a[116:119], v[20:23], v[124:127]\n\t" \
    "ds_read_b64_tr_b16 v[40:41], v224 offset:28672\n\tds_read_b64_tr_b16 v[42:43], v224 offset:30720\n\tv_mfma_f32_16x16x32_bf16 v[132:135], a[120:123], v[20:23], v[132:135]\n\tv_mfma_f32_16x16x32_bf16 v[140:143], a[124:127], v[20:23], v[140:143]\n\ts_waitcnt lgkmcnt(8)\n\tv_mfma_f32_16x16x32_bf16 v[120:123], a[128:131], v[24:27], v[120:123]\n\tds_read_b64_tr_b16 v[44:45], v225 offset:28672\n\tds_read_b64_tr_b16 v[46:47], v225 offset:30720\n\t" \
    "v_mfma_f32_16x16x32_bf16 v[128:131], a[132:135], v[24:27], v[128:131]\n\tv_mfma_f32_16x16x32_bf16 v[136:139], a[136:139], v[24:27], v[136:139]\n\ts_waitcnt lgkmcnt(8)\n\tv_mfma_f32_16x16x32_bf16 v[124:127], a[128:131], v[28:31], v[124:127]\n\tds_read_b64_tr_b16 v[16:17], v4 offset:36864\n\tds_read_b64_tr_b16 v[18:19], v4 offset:38912\n\tv_mfma_f32_16x16x32_bf16 v[132:135], a[132:135], v[28:31], v[132:135]\n\tv_mfma_f32_16x16x32_bf16 v[140:143], a[136:139], v[28:31], v[140:143]\n\t" \
    "s_waitcnt lgkmcnt(8)\n\tv_mfma_f32_16x16x32_bf16 v[120:123], a[140:143], v[32:35], v[120:123]\n\tds_read_b64_tr_b16 v[20:21], v5 offset:36864\n\tds_read_b64_tr_b16 v[22:23], v5 offset:38912\n\tv_mfma_f32_16x16x32_bf16 v[128:131], a[144:147], v[32:35], v[128:131]\n\tv_mfma_f32_16x16x32_bf16 v[136:139], a[148:151], v[32:35], v[136:139]\n\ts_waitcnt lgkmcnt(8)\n\tv_mfma_f32_16x16x32_bf16 v[124:127], a[140:143], v[36:39], v[124:127]\n\t" \
    "ds_read_b64_tr_b16 v[24:25], v220 offset:4096\n\tds_read_b64_tr_b16 v[26:27], v220 offset:6144\n\tv_mfma_f32_16x16x32_bf16 v[132:135], a[144:147], v[36:39], v[132:135]\n\tv_mfma_f32_16x16x32_bf16 v[140:143], a[148:151], v[36:39], v[140:143]\n\ts_waitcnt lgkmcnt(8)\n\tv_mfma_f32_16x16x32_bf16 v[120:123], a[152:155], v[40:43], v[120:123]\n\tds_read_b64_tr_b16 v[28:29], v221 offset:4096\n\tds_read_b64_tr_b16 v[30:31], v221 offset:6144\n\t" \
    "v_mfma_f32_16x16x32_bf16 v[128:131], a[156:159], v[40:43], v[128:131]\n\tv_mfma_f32_16x16x32_bf16 v[136:139], a[160:163], v[40:43], v[136:139]\n\ts_waitcnt lgkmcnt(8)\n\tv_mfma_f32_16x16x32_bf16 v[124:127], a[152:155], v[44:47], v[124:127]\n\tds_read_b64_tr_b16 v[32:33], v220 offset:8192\n\tds_read_b64_tr_b16 v[34:35], v220 offset:10240\n\tv_mfma_f32_16x16x32_bf16 v[132:135], a[156:159], v[44:47], v[132:135]\n\tv_mfma_f32_16x16x32_bf16 v[140:143], a[160:163], v[44:47], v[140:143]\n\t" \
    "s_waitcnt lgkmcnt(8)\n\tv_mfma_f32_16x16x32_bf16 v[120:123], a[164:167], v[16:19], v[120:123]\n\tds_read_b64_tr_b16 v[36:37], v221 offset:8192\n\tds_read_b64_tr_b16 v[38:39], v221 offset:10240\n\tv_mfma_f32_16x16x32_bf16 v[128:131], a[168:171], v[16:19], v[128:131]\n\tv_mfma_f32_16x16x32_bf16 v[136:139], a[172:175], v[16:19], v[136:139]\n\ts_waitcnt lgkmcnt(8)\n\tv_mfma_f32_16x16x32_bf16 v[124:127], a[164:167], v[20:23], v[124:127]\n\t" \
    "ds_read_b64_tr_b16 v[40:41], v220 offset:12288\n\tds_read_b64_tr_b16 v[42:43], v220 offset:14336\n\tv_mfma_f32_16x16x32_bf16 v[132:135], a[168:171], v[20:23], v[132:135]\n\tv_mfma_f32_16x16x32_bf16 v[140:143], a[172:175], v[20:23], v[140:143]\n\ts_waitcnt lgkmcnt(8)\n\tv_mfma_f32_16x16x32_bf16 v[120:123], a[176:179], v[24:27], v[120:123]\n\tds_read_b64_tr_b16 v[44:45], v221 offset:12288\n\tds_read_b64_tr_b16 v[46:47], v221 offset:14336\n\t" \
    "v_mfma_f32_16x16x32_bf16 v[128:131], a[180:183], v[24:27], v[128:131]\n\tv_mfma_f32_16x16x32_bf16 v[136:139], a[184:187], v[24:27], v[136:139]\n\ts_waitcnt lgkmcnt(8)\n\tv_mfma_f32_16x16x32_bf16 v[124:127], a[176:179], v[28:31], v[124:127]\n\tds_read_b64_tr_b16 v[16:17], v220 offset:16384\n\tds_read_b64_tr_b16 v[18:19], v220 offset:18432\n\tv_mfma_f32_16x16x32_bf16 v[132:135], a[180:183], v[28:31], v[132:135]\n\tv_mfma_f32_16x16x32_bf16 v[140:143], a[184:187], v[28:31], v[140:143]\n\t" \
    "s_waitcnt lgkmcnt(8)\n\tv_mfma_f32_16x16x32_bf16 v[120:123], a[188:191], v[32:35], v[120:123]\n\tds_read_b64_tr_b16 v[20:21], v221 offset:16384\n\tds_read_b64_tr_b16 v[22:23], v221 offset:18432\n\tv_mfma_f32_16x16x32_bf16 v[128:131], a[192:195], v[32:35], v[128:131]\n\tv_mfma_f32_16x16x32_bf16 v[136:139], a[196:199], v[32:35], v[136:139]\n\ts_waitcnt lgkmcnt(8)\n\tv_mfma_f32_16x16x32_bf16 v[124:127], a[188:191], v[36:39], v[124:127]\n\t" \
    "ds_read_b64_tr_b16 v[24:25], v6 offset:28672\n\tds_read_b64_tr_b16 v[26:27], v6 offset:30720\n\tv_mfma_f32_16x16x32_bf16 v[132:135], a[192:195], v[36:39], v[132:135]\n\tv_mfma_f32_16x16x32_bf16 v[140:143], a[196:199], v[36:39], v[140:143]\n\ts_waitcnt lgkmcnt(8)\n\tv_mfma_f32_16x16x32_bf16 v[120:123], a[200:203], v[40:43], v[120:123]\n\tds_read_b64_tr_b16 v[28:29], v7 offset:28672\n\tds_read_b64_tr_b16 v[30:31], v7 offset:30720\n\t" \
    "v_mfma_f32_16x16x32_bf16 v[128:131], a[204:207], v[40:43], v[128:131]\n\tv_mfma_f32_16x16x32_bf16 v[136:139], a[208:211], v[40:43], v[136:139]\n\ts_waitcnt lgkmcnt(8)\n\tv_mfma_f32_16x16x32_bf16 v[124:127], a[200:203], v[44:47], v[124:127]\n\tds_read_b64_tr_b16 v[32:33], v222 offset:0\n\tds_read_b64_tr_b16 v[34:35], v222 offset:2048\n\tv_mfma_f32_16x16x32_bf16 v[132:135], a[204:207], v[44:47], v[132:135]\n\tv_mfma_f32_16x16x32_bf16 v[140:143], a[208:211], v[44:47], v[140:143]\n\t" \
    "s_waitcnt lgkmcnt(8)\n\tv_mfma_f32_16x16x32_bf16 v[120:123], a[212:215], v[16:19], v[120:123]\n\tds_read_b64_tr_b16 v[36:37], v223 offset:0\n\tds_read_b64_tr_b16 v[38:39], v223 offset:2048\n\tv_mfma_f32_16x16x32_bf16 v[128:131], a[216:219], v[16:19], v[128:131]\n\tv_mfma_f32_16x16x32_bf16 v[136:139], a[220:223], v[16:19], v[136:139]\n\tv_min_u32 v241, 376, v239\n\tbuffer_load_dwordx2 v[250:251], v241, s[8:11], s51 offen sc1\n\t" \
    "s_waitcnt lgkmcnt(8)\n\tv_mfma_f32_16x16x32_bf16 v[124:127], a[212:215], v[20:23], v[124:127]\n\tds_read_b64_tr_b16 v[40:41], v6 offset:32768\n\tds_read_b64_tr_b16 v[42:43], v6 offset:34816\n\tv_mfma_f32_16x16x32_bf16 v[132:135], a[216:219], v[20:23], v[132:135]\n\tv_mfma_f32_16x16x32_bf16 v[140:143], a[220:223], v[20:23], v[140:143]\n\ts_waitcnt lgkmcnt(8)\n\tv_mfma_f32_16x16x32_bf16 v[144:147], a[116:119], v[24:27], v[144:147]\n\t" \
    "ds_read_b64_tr_b16 v[44:45], v7 offset:32768\n\tds_read_b64_tr_b16 v[46:47], v7 offset:34816\n\ts_xor_b32 s68, s53, 12\n\tv_mfma_f32_16x16x32_bf16 v[152:155], a[120:123], v[24:27], v[152:155]\n\ts_mul_i32 s68, s68, 9984\n\ts_add_u32 s68, s68, 7208960\n\tv_cvt_pk_bf16_f32 v120, v120, v121\n\tv_mfma_f32_16x16x32_bf16 v[160:163], a[124:127], v[24:27], v[160:163]\n\t" \
    "v_cvt_pk_bf16_f32 v121, v122, v123\n\tv_cvt_pk_bf16_f32 v122, v124, v125\n\ts_waitcnt lgkmcnt(8)\n\tv_mfma_f32_16x16x32_bf16 v[148:151], a[116:119], v[28:31], v[148:151]\n\tds_read_b64_tr_b16 v[16:17], v226 offset:28672\n\tds_read_b64_tr_b16 v[18:19], v226 offset:30720\n\tv_cvt_pk_bf16_f32 v123, v126, v127\n\tv_mfma_f32_16x16x32_bf16 v[156:159], a[120:123], v[28:31], v[156:159]\n\t" \
    "buffer_store_dwordx4 v[120:123], v8, s[8:11], s68 offen offset:0 sc1\n\tv_cvt_pk_bf16_f32 v124, v128, v129\n\tv_cvt_pk_bf16_f32 v125, v130, v131\n\tv_mfma_f32_16x16x32_bf16 v[164:167], a[124:127], v[28:31], v[164:167]\n\tv_cvt_pk_bf16_f32 v126, v132, v133\n\tv_cvt_pk_bf16_f32 v127, v134, v135\n\ts_waitcnt lgkmcnt(8)\n\tv_mfma_f32_16x16x32_bf16 v[144:147], a[128:131], v[32:35], v[144:147]\n\t" \
    "ds_read_b64_tr_b16 v[20:21], v227 offset:28672\n\tds_read_b64_tr_b16 v[22:23], v227 offset:30720\n\tv_mfma_f32_16x16x32_bf16 v[152:155], a[132:135], v[32:35], v[152:155]\n\tbuffer_store_dwordx4 v[124:127], v8, s[8:11], s68 offen offset:1024 sc1\n\tv_cvt_pk_bf16_f32 v128, v136, v137\n\tv_cvt_pk_bf16_f32 v129, v138, v139\n\tv_mfma_f32_16x16x32_bf16 v[160:163], a[136:139], v[32:35], v[160:163]\n\tv_cvt_pk_bf16_f32 v130, v140, v141\n\t" \
    "v_cvt_pk_bf16_f32 v131, v142, v143\n\ts_waitcnt lgkmcnt(8)\n\tv_mfma_f32_16x16x32_bf16 v[148:151], a[128:131], v[36:39], v[148:151]\n\tds_read_b64_tr_b16 v[24:25], v6 offset:36864\n\tds_read_b64_tr_b16 v[26:27], v6 offset:38912\n\tv_mfma_f32_16x16x32_bf16 v[156:159], a[132:135], v[36:39], v[156:159]\n\tbuffer_store_dwordx4 v[128:131], v8, s[8:11], s68 offen offset:2048 sc1\n\tv_add3_u32 v132, s36, v120, v121\n\t" \
    "v_add3_u32 v133, v122, v123, v124\n\tv_mfma_f32_16x16x32_bf16 v[164:167], a[136:139], v[36:39], v[164:167]\n\tv_add3_u32 v132, v132, v125, v126\n\tv_add3_u32 v133, v133, v127, v128\n\ts_waitcnt lgkmcnt(8)\n\tv_mfma_f32_16x16x32_bf16 v[144:147], a[140:143], v[40:43], v[144:147]\n\tds_read_b64_tr_b16 v[28:29], v7 offset:36864\n\tds_read_b64_tr_b16 v[30:31], v7 offset:38912\n\t" \
    "v_add3_u32 v132, v132, v129, v130\n\tv_mfma_f32_16x16x32_bf16 v[152:155], a[144:147], v[40:43], v[152:155]\n\tv_add3_u32 v132, v132, v133, v131\n\tv_mfma_f32_16x16x32_bf16 v[160:163], a[148:151], v[40:43], v[160:163]\n\ts_waitcnt lgkmcnt(8)\n\tv_mfma_f32_16x16x32_bf16 v[148:151], a[140:143], v[44:47], v[148:151]\n\tds_read_b64_tr_b16 v[32:33], v222 offset:4096\n\tds_read_b64_tr_b16 v[34:35], v222 offset:6144\n\t" \
    "v_mfma_f32_16x16x32_bf16 v[156:159], a[144:147], v[44:47], v[156:159]\n\tbuffer_store_dword v132, v236, s[8:11], s68 offen offset:3072 sc1\n\tv_mfma_f32_16x16x32_bf16 v[164:167], a[148:151], v[44:47], v[164:167]\n\ts_waitcnt lgkmcnt(8)\n\tv_mfma_f32_16x16x32_bf16 v[144:147], a[152:155], v[16:19], v[144:147]\n\tds_read_b64_tr_b16 v[36:37], v223 offset:4096\n\tds_read_b64_tr_b16 v[38:39], v223 offset:6144\n\tv_mfma_f32_16x16x32_bf16 v[152:155], a[156:159], v[16:19], v[152:155]\n\t" \
    "v_mfma_f32_16x16x32_bf16 v[160:163], a[160:163], v[16:19], v[160:163]\n\ts_waitcnt lgkmcnt(8)\n\tv_mfma_f32_16x16x32_bf16 v[148:151], a[152:155], v[20:23], v[148:151]\n\tds_read_b64_tr_b16 v[40:41], v222 offset:8192\n\tds_read_b64_tr_b16 v[42:43], v222 offset:10240\n\tv_mfma_f32_16x16x32_bf16 v[156:159], a[156:159], v[20:23], v[156:159]\n\tv_mfma_f32_16x16x32_bf16 v[164:167], a[160:163], v[20:23], v[164:167]\n\ts_waitcnt lgkmcnt(8)\n\t" \
    "v_mfma_f32_16x16x32_bf16 v[144:147], a[164:167], v[24:27], v[144:147]\n\tds_read_b64_tr_b16 v[44:45], v223 offset:8192\n\tds_read_b64_tr_b16 v[46:47], v223 offset:10240\n\tv_mfma_f32_16x16x32_bf16 v[152:155], a[168:171], v[24:27], v[152:155]\n\tv_mfma_f32_16x16x32_bf16 v[160:163], a[172:175], v[24:27], v[160:163]\n\ts_waitcnt lgkmcnt(8)\n\tv_mfma_f32_16x16x32_bf16 v[148:151], a[164:167], v[28:31], v[148:151]\n\tds_read_b64_tr_b16 v[16:17], v222 offset:12288\n\t" \
    "ds_read_b64_tr_b16 v[18:19], v222 offset:14336\n\tv_mfma_f32_16x16x32_bf16 v[156:159], a[168:171], v[28:31], v[156:159]\n\tv_mfma_f32_16x16x32_bf16 v[164:167], a[172:175], v[28:31], v[164:167]\n\ts_waitcnt lgkmcnt(8)\n\tv_mfma_f32_16x16x32_bf16 v[144:147], a[176:179], v[32:35], v[144:147]\n\tds_read_b64_tr_b16 v[20:21], v223 offset:12288\n\tds_read_b64_tr_b16 v[22:23], v223 offset:14336\n\tv_mfma_f32_16x16x32_bf16 v[152:155], a[180:183], v[32:35], v[152:155]\n\t" \
    "v_mfma_f32_16x16x32_bf16 v[160:163], a[184:187], v[32:35], v[160:163]\n\ts_waitcnt lgkmcnt(8)\n\tv_mfma_f32_16x16x32_bf16 v[148:151], a[176:179], v[36:39], v[148:151]\n\tds_read_b64_tr_b16 v[24:25], v222 offset:16384\n\tds_read_b64_tr_b16 v[26:27], v222 offset:18432\n\tv_mfma_f32_16x16x32_bf16 v[156:159], a[180:183], v[36:39], v[156:159]\n\tv_mfma_f32_16x16x32_bf16 v[164:167], a[184:187], v[36:39], v[164:167]\n\ts_waitcnt lgkmcnt(8)\n\t" \
    "v_mfma_f32_16x16x32_bf16 v[144:147], a[188:191], v[40:43], v[144:147]\n\tds_read_b64_tr_b16 v[28:29], v223 offset:16384\n\tds_read_b64_tr_b16 v[30:31], v223 offset:18432\n\tv_mfma_f32_16x16x32_bf16 v[152:155], a[192:195], v[40:43], v[152:155]\n\tv_mfma_f32_16x16x32_bf16 v[160:163], a[196:199], v[40:43], v[160:163]\n\ts_waitcnt lgkmcnt(8)\n\tv_mfma_f32_16x16x32_bf16 v[148:151], a[188:191], v[44:47], v[148:151]\n\tds_read_b64_tr_b16 v[32:33], v220 offset:20480\n\t" \
    "ds_read_b64_tr_b16 v[34:35], v220 offset:22528\n\tv_mfma_f32_16x16x32_bf16 v[156:159], a[192:195], v[44:47], v[156:159]\n\tv_mfma_f32_16x16x32_bf16 v[164:167], a[196:199], v[44:47], v[164:167]\n\ts_waitcnt vmcnt(10)\n\ts_barrier\n\ts_waitcnt lgkmcnt(8)\n\tv_mfma_f32_16x16x32_bf16 v[144:147], a[200:203], v[16:19], v[144:147]\n\tds_read_b64_tr_b16 v[36:37], v221 offset:20480\n\t" \
    "ds_read_b64_tr_b16 v[38:39], v221 offset:22528\n\tv_mfma_f32_16x16x32_bf16 v[152:155], a[204:207], v[16:19], v[152:155]\n\tv_mfma_f32_16x16x32_bf16 v[160:163], a[208:211], v[16:19], v[160:163]\n\ts_waitcnt lgkmcnt(8)\n\tv_mfma_f32_16x16x32_bf16 v[148:151], a[200:203], v[20:23], v[148:151]\n\tds_read_b64_tr_b16 v[40:41], v220 offset:24576\n\tds_read_b64_tr_b16 v[42:43], v220 offset:26624\n\tv_mfma_f32_16x16x32_bf16 v[156:159], a[204:207], v[20:23], v[156:159]\n\t" \
    "v_mfma_f32_16x16x32_bf16 v[164:167], a[208:211], v[20:23], v[164:167]\n\ts_waitcnt lgkmcnt(8)\n\tv_mfma_f32_16x16x32_bf16 v[144:147], a[212:215], v[24:27], v[144:147]\n\tds_read_b64_tr_b16 v[44:45], v221 offset:24576\n\tds_read_b64_tr_b16 v[46:47], v221 offset:26624\n\tv_mfma_f32_16x16x32_bf16 v[152:155], a[216:219], v[24:27], v[152:155]\n\tv_mfma_f32_16x16x32_bf16 v[160:163], a[220:223], v[24:27], v[160:163]\n\ts_waitcnt lgkmcnt(8)\n\t" \
    "v_mfma_f32_16x16x32_bf16 v[148:151], a[212:215], v[28:31], v[148:151]\n\tds_read_b64_tr_b16 v[16:17], v220 offset:28672\n\tds_read_b64_tr_b16 v[18:19], v220 offset:30720\n\tv_mfma_f32_16x16x32_bf16 v[156:159], a[216:219], v[28:31], v[156:159]\n\tv_mfma_f32_16x16x32_bf16 v[164:167], a[220:223], v[28:31], v[164:167]\n\ts_waitcnt lgkmcnt(8)\n\tv_mfma_f32_16x16x32_bf16 v[168:171], a[116:119], v[32:35], v[168:171]\n\tds_read_b64_tr_b16 v[20:21], v221 offset:28672\n\t" \
    "ds_read_b64_tr_b16 v[22:23], v221 offset:30720\n\ts_xor_b32 s68, s53, 8\n\tv_mfma_f32_16x16x32_bf16 v[176:179], a[120:123], v[32:35], v[176:179]\n\ts_mul_i32 s68, s68, 9984\n\ts_add_u32 s68, s68, 7212288\n\tv_cvt_pk_bf16_f32 v144, v144, v145\n\tv_mfma_f32_16x16x32_bf16 v[184:187], a[124:127], v[32:35], v[184:187]\n\tv_cvt_pk_bf16_f32 v145, v146, v147\n\t" \
    "v_cvt_pk_bf16_f32 v146, v148, v149\n\ts_waitcnt lgkmcnt(8)\n\tv_mfma_f32_16x16x32_bf16 v[172:175], a[116:119], v[36:39], v[172:175]\n\tds_read_b64_tr_b16 v[24:25], v4 offset:40960\n\tds_read_b64_tr_b16 v[26:27], v4 offset:43008\n\tv_cvt_pk_bf16_f32 v147, v150, v151\n\tv_mfma_f32_16x16x32_bf16 v[180:183], a[120:123], v[36:39], v[180:183]\n\tbuffer_store_dwordx4 v[144:147], v8, s[8:11], s68 offen offset:0 sc1\n\t" \
    "v_cvt_pk_bf16_f32 v148, v152, v153\n\tv_cvt_pk_bf16_f32 v149, v154, v155\n\tv_mfma_f32_16x16x32_bf16 v[188:191], a[124:127], v[36:39], v[188:191]\n\tv_cvt_pk_bf16_f32 v150, v156, v157\n\tv_cvt_pk_bf16_f32 v151, v158, v159\n\ts_waitcnt lgkmcnt(8)\n\tv_mfma_f32_16x16x32_bf16 v[168:171], a[128:131], v[40:43], v[168:171]\n\tds_read_b64_tr_b16 v[28:29], v5 offset:40960\n\t" \
    "ds_read_b64_tr_b16 v[30:31], v5 offset:43008\n\tv_mfma_f32_16x16x32_bf16 v[176:179], a[132:135], v[40:43], v[176:179]\n\tbuffer_store_dwordx4 v[148:151], v8, s[8:11], s68 offen offset:1024 sc1\n\tv_cvt_pk_bf16_f32 v152, v160, v161\n\tv_cvt_pk_bf16_f32 v153, v162, v163\n\tv_mfma_f32_16x16x32_bf16 v[184:187], a[136:139], v[40:43], v[184:187]\n\tv_cvt_pk_bf16_f32 v154, v164, v165\n\tv_cvt_pk_bf16_f32 v155, v166, v167\n\t" \
    "s_waitcnt lgkmcnt(8)\n\tv_mfma_f32_16x16x32_bf16 v[172:175], a[128:131], v[44:47], v[172:175]\n\tds_read_b64_tr_b16 v[32:33], v4 offset:45056\n\tds_read_b64_tr_b16 v[34:35], v4 offset:47104\n\tv_mfma_f32_16x16x32_bf16 v[180:183], a[132:135], v[44:47], v[180:183]\n\tbuffer_store_dwordx4 v[152:155], v8, s[8:11], s68 offen offset:2048 sc1\n\tv_add3_u32 v156, s36, v144, v145\n\tv_add3_u32 v157, v146, v147, v148\n\t" \
    "v_mfma_f32_16x16x32_bf16 v[188:191], a[136:139], v[44:47], v[188:191]\n\tv_add3_u32 v156, v156, v149, v150\n\tv_add3_u32 v157, v157, v151, v152\n\ts_waitcnt vmcnt(7)\n\tv_cmp_ne_u64 vcc, s[36:37], v[250:251]\n\ts_cmp_lg_u64 vcc, 0\n\ts_cselect_b32 s70, 1, 0\n\ts_waitcnt lgkmcnt(8)\n\t" \
    "v_mfma_f32_16x16x32_bf16 v[168:171], a[140:143], v[16:19], v[168:171]\n\tds_read_b64_tr_b16 v[36:37], v5 offset:45056\n\tds_read_b64_tr_b16 v[38:39], v5 offset:47104\n\tv_add3_u32 v156, v156, v153, v154\n\tv_mfma_f32_16x16x32_bf16 v[176:179], a[144:147], v[16:19], v[176:179]\n\tv_add3_u32 v156, v156, v157, v155\n\tv_mfma_f32_16x16x32_bf16 v[184:187], a[148:151], v[16:19], v[184:187]\n\ts_waitcnt lgkmcnt(8)\n\t" \
    "v_mfma_f32_16x16x32_bf16 v[172:175], a[140:143], v[20:23], v[172:175]\n\tds_read_b64_tr_b16 v[40:41], v4 offset:49152\n\tds_read_b64_tr_b16 v[42:43], v4 offset:51200\n\tv_mfma_f32_16x16x32_bf16 v[180:183], a[144:147], v[20:23], v[180:183]\n\tbuffer_store_dword v156, v236, s[8:11], s68 offen offset:3072 sc1\n\tv_mfma_f32_16x16x32_bf16 v[188:191], a[148:151], v[20:23], v[188:191]\n\ts_waitcnt lgkmcnt(8)\n\tv_mfma_f32_16x16x32_bf16 v[168:171], a[152:155], v[24:27], v[168:171]\n\t" \
    "ds_read_b64_tr_b16 v[44:45], v5 offset:49152\n\tds_read_b64_tr_b16 v[46:47], v5 offset:51200\n\tv_mfma_f32_16x16x32_bf16 v[176:179], a[156:159], v[24:27], v[176:179]\n\tbuffer_load_dwordx4 v[204:207], v8, s[8:11], s56 offen offset:0 sc1\n\tv_mfma_f32_16x16x32_bf16 v[184:187], a[160:163], v[24:27], v[184:187]\n\ts_waitcnt lgkmcnt(8)\n\tv_mfma_f32_16x16x32_bf16 v[172:175], a[152:155], v[28:31], v[172:175]\n\tds_read_b64_tr_b16 v[16:17], v4 offset:53248\n\t" \
    "ds_read_b64_tr_b16 v[18:19], v4 offset:55296\n\tv_mfma_f32_16x16x32_bf16 v[180:183], a[156:159], v[28:31], v[180:183]\n\tbuffer_load_dwordx4 v[208:211], v8, s[8:11], s56 offen offset:1024 sc1\n\tv_mfma_f32_16x16x32_bf16 v[188:191], a[160:163], v[28:31], v[188:191]\n\ts_waitcnt lgkmcnt(8)\n\tv_mfma_f32_16x16x32_bf16 v[168:171], a[164:167], v[32:35], v[168:171]\n\tds_read_b64_tr_b16 v[20:21], v5 offset:53248\n\tds_read_b64_tr_b16 v[22:23], v5 offset:55296\n\t" \
    "v_mfma_f32_16x16x32_bf16 v[176:179], a[168:171], v[32:35], v[176:179]\n\tbuffer_load_dwordx4 v[212:215], v8, s[8:11], s56 offen offset:2048 sc1\n\tv_mfma_f32_16x16x32_bf16 v[184:187], a[172:175], v[32:35], v[184:187]\n\ts_waitcnt lgkmcnt(8)\n\tv_mfma_f32_16x16x32_bf16 v[172:175], a[164:167], v[36:39], v[172:175]\n\tds_read_b64_tr_b16 v[24:25], v4 offset:57344\n\tds_read_b64_tr_b16 v[26:27], v4 offset:59392\n\tv_mfma_f32_16x16x32_bf16 v[180:183], a[168:171], v[36:39], v[180:183]\n\t" \
    "v_mfma_f32_16x16x32_bf16 v[188:191], a[172:175], v[36:39], v[188:191]\n\ts_waitcnt lgkmcnt(8)\n\tv_mfma_f32_16x16x32_bf16 v[168:171], a[176:179], v[40:43], v[168:171]\n\tds_read_b64_tr_b16 v[28:29], v5 offset:57344\n\tds_read_b64_tr_b16 v[30:31], v5 offset:59392\n\tv_mfma_f32_16x16x32_bf16 v[176:179], a[180:183], v[40:43], v[176:179]\n\tv_mfma_f32_16x16x32_bf16 v[184:187], a[184:187], v[40:43], v[184:187]\n\ts_waitcnt lgkmcnt(8)\n\t" \
    "v_mfma_f32_16x16x32_bf16 v[172:175], a[176:179], v[44:47], v[172:175]\n\tds_read_b64_tr_b16 v[32:33], v224 offset:0\n\tds_read_b64_tr_b16 v[34:35], v224 offset:2048\n\tv_mfma_f32_16x16x32_bf16 v[180:183], a[180:183], v[44:47], v[180:183]\n\tv_mfma_f32_16x16x32_bf16 v[188:191], a[184:187], v[44:47], v[188:191]\n\ts_waitcnt lgkmcnt(8)\n\tv_mfma_f32_16x16x32_bf16 v[168:171], a[188:191], v[16:19], v[168:171]\n\tds_read_b64_tr_b16 v[36:37], v225 offset:0\n\t" \
    "ds_read_b64_tr_b16 v[38:39], v225 offset:2048\n\tv_mfma_f32_16x16x32_bf16 v[176:179], a[192:195], v[16:19], v[176:179]\n\tv_mfma_f32_16x16x32_bf16 v[184:187], a[196:199], v[16:19], v[184:187]\n\ts_waitcnt lgkmcnt(8)\n\tv_mfma_f32_16x16x32_bf16 v[172:175], a[188:191], v[20:23], v[172:175]\n\tds_read_b64_tr_b16 v[40:41], v6 offset:61440\n\tds_read_b64_tr_b16 v[42:43], v6 offset:63488\n\tv_mfma_f32_16x16x32_bf16 v[180:183], a[192:195], v[20:23], v[180:183]\n\t" \
    "v_mfma_f32_16x16x32_bf16 v[188:191], a[196:199], v[20:23], v[188:191]\n\ts_waitcnt lgkmcnt(8)\n\tv_mfma_f32_16x16x32_bf16 v[168:171], a[200:203], v[24:27], v[168:171]\n\tds_read_b64_tr_b16 v[44:45], v7 offset:61440\n\tds_read_b64_tr_b16 v[46:47], v7 offset:63488\n\tv_mfma_f32_16x16x32_bf16 v[176:179], a[204:207], v[24:27], v[176:179]\n\tv_mfma_f32_16x16x32_bf16 v[184:187], a[208:211], v[24:27], v[184:187]\n\ts_waitcnt lgkmcnt(8)\n\t" \
    "v_mfma_f32_16x16x32_bf16 v[172:175], a[200:203], v[28:31], v[172:175]\n\tds_read_b64_tr_b16 v[16:17], v226 offset:4096\n\tds_read_b64_tr_b16 v[18:19], v226 offset:6144\n\tv_mfma_f32_16x16x32_bf16 v[180:183], a[204:207], v[28:31], v[180:183]\n\tv_mfma_f32_16x16x32_bf16 v[188:191], a[208:211], v[28:31], v[188:191]\n\ts_waitcnt lgkmcnt(8)\n\tv_mfma_f32_16x16x32_bf16 v[168:171], a[212:215], v[32:35], v[168:171]\n\tds_read_b64_tr_b16 v[20:21], v227 offset:4096\n\t" \
    "ds_read_b64_tr_b16 v[22:23], v227 offset:6144\n\tv_mfma_f32_16x16x32_bf16 v[176:179], a[216:219], v[32:35], v[176:179]\n\tv_mfma_f32_16x16x32_bf16 v[184:187], a[220:223], v[32:35], v[184:187]\n\ts_waitcnt lgkmcnt(8)\n\tv_mfma_f32_16x16x32_bf16 v[172:175], a[212:215], v[36:39], v[172:175]\n\tds_read_b64_tr_b16 v[24:25], v226 offset:12288\n\tds_read_b64_tr_b16 v[26:27], v226 offset:14336\n\tv_mfma_f32_16x16x32_bf16 v[180:183], a[216:219], v[36:39], v[180:183]\n\t" \
    "v_mfma_f32_16x16x32_bf16 v[188:191], a[220:223], v[36:39], v[188:191]\n\ts_waitcnt lgkmcnt(8)\n\tv_mfma_f32_16x16x32_bf16 v[48:51], v[92:95], v[40:43], v[48:51]\n\tds_read_b64_tr_b16 v[28:29], v227 offset:12288\n\tds_read_b64_tr_b16 v[30:31], v227 offset:14336\n\ts_xor_b32 s68, s53, 4\n\tv_mfma_f32_16x16x32_bf16 v[56:59], v[96:99], v[40:43], v[56:59]\n\ts_mul_i32 s68, s68, 9984\n\t" \
    "s_add_u32 s68, s68, 7215616\n\tv_cvt_pk_bf16_f32 v168, v168, v169\n\tv_mfma_f32_16x16x32_bf16 v[64:67], v[100:103], v[40:43], v[64:67]\n\tv_cvt_pk_bf16_f32 v169, v170, v171\n\tv_cvt_pk_bf16_f32 v170, v172, v173\n\ts_waitcnt lgkmcnt(8)\n\tv_mfma_f32_16x16x32_bf16 v[52:55], v[92:95], v[44:47], v[52:55]\n\tds_read_b64_tr_b16 v[32:33], v226 offset:20480\n\t" \
    "ds_read_b64_tr_b16 v[34:35], v226 offset:22528\n\tv_cvt_pk_bf16_f32 v171, v174, v175\n\tv_mfma_f32_16x16x32_bf16 v[60:63], v[96:99], v[44:47], v[60:63]\n\tbuffer_store_dwordx4 v[168:171], v8, s[8:11], s68 offen offset:0 sc1\n\tv_cvt_pk_bf16_f32 v172, v176, v177\n\tv_cvt_pk_bf16_f32 v173, v178, v179\n\tv_mfma_f32_16x16x32_bf16 v[68:71], v[100:103], v[44:47], v[68:71]\n\tv_cvt_pk_bf16_f32 v174, v180, v181\n\t" \
    "v_cvt_pk_bf16_f32 v175, v182, v183\n\ts_waitcnt lgkmcnt(8)\n\tv_mfma_f32_16x16x32_bf16 v[48:51], v[104:107], v[16:19], v[48:51]\n\tds_read_b64_tr_b16 v[36:37], v227 offset:20480\n\tds_read_b64_tr_b16 v[38:39], v227 offset:22528\n\tv_mfma_f32_16x16x32_bf16 v[56:59], v[108:111], v[16:19], v[56:59]\n\tbuffer_store_dwordx4 v[172:175], v8, s[8:11], s68 offen offset:1024 sc1\n\tv_cvt_pk_bf16_f32 v176, v184, v185\n\t" \
    "v_cvt_pk_bf16_f32 v177, v186, v187\n\tv_mfma_f32_16x16x32_bf16 v[64:67], v[112:115], v[16:19], v[64:67]\n\tv_cvt_pk_bf16_f32 v178, v188, v189\n\tv_cvt_pk_bf16_f32 v179, v190, v191\n\ts_waitcnt lgkmcnt(8)\n\tv_mfma_f32_16x16x32_bf16 v[52:55], v[104:107], v[20:23], v[52:55]\n\tds_read_b64_tr_b16 v[40:41], v226 offset:32768\n\tds_read_b64_tr_b16 v[42:43], v226 offset:34816\n\t" \
    "v_mfma_f32_16x16x32_bf16 v[60:63], v[108:111], v[20:23], v[60:63]\n\tbuffer_store_dwordx4 v[176:179], v8, s[8:11], s68 offen offset:2048 sc1\n\tv_add3_u32 v180, s36, v168, v169\n\tv_add3_u32 v181, v170, v171, v172\n\tv_mfma_f32_16x16x32_bf16 v[68:71], v[112:115], v[20:23], v[68:71]\n\tv_add3_u32 v180, v180, v173, v174\n\tv_add3_u32 v181, v181, v175, v176\n\ts_waitcnt lgkmcnt(8)\n\t" \
    "v_mfma_f32_16x16x32_bf16 v[48:51], v[116:119], v[24:27], v[48:51]\n\tds_read_b64_tr_b16 v[44:45], v227 offset:32768\n\tds_read_b64_tr_b16 v[46:47], v227 offset:34816\n\tv_add3_u32 v180, v180, v177, v178\n\tv_mfma_f32_16x16x32_bf16 v[56:59], a[0:3], v[24:27], v[56:59]\n\tv_add3_u32 v180, v180, v181, v179\n\tv_mfma_f32_16x16x32_bf16 v[64:67], a[4:7], v[24:27], v[64:67]\n\ts_waitcnt lgkmcnt(8)\n\t" \
    "v_mfma_f32_16x16x32_bf16 v[52:55], v[116:119], v[28:31], v[52:55]\n\tds_read_b64_tr_b16 v[16:17], v226 offset:40960\n\tds_read_b64_tr_b16 v[18:19], v226 offset:43008\n\tv_mfma_f32_16x16x32_bf16 v[60:63], a[0:3], v[28:31], v[60:63]\n\tbuffer_store_dword v180, v236, s[8:11], s68 offen offset:3072 sc1\n\tv_mfma_f32_16x16x32_bf16 v[68:71], a[4:7], v[28:31], v[68:71]\n\ts_waitcnt lgkmcnt(8)\n\tv_mfma_f32_16x16x32_bf16 v[48:51], a[8:11], v[32:35], v[48:51]\n\t" \
    "ds_read_b64_tr_b16 v[20:21], v227 offset:40960\n\tds_read_b64_tr_b16 v[22:23], v227 offset:43008\n\tv_mfma_f32_16x16x32_bf16 v[56:59], a[12:15], v[32:35], v[56:59]\n\tv_mfma_f32_16x16x32_bf16 v[64:67], a[16:19], v[32:35], v[64:67]\n\ts_waitcnt lgkmcnt(8)\n\tv_mfma_f32_16x16x32_bf16 v[52:55], a[8:11], v[36:39], v[52:55]\n\tds_read_b64_tr_b16 v[24:25], v226 offset:49152\n\tds_read_b64_tr_b16 v[26:27], v226 offset:51200\n\t" \
    "v_mfma_f32_16x16x32_bf16 v[60:63], a[12:15], v[36:39], v[60:63]\n\tv_mfma_f32_16x16x32_bf16 v[68:71], a[16:19], v[36:39], v[68:71]\n\ts_waitcnt lgkmcnt(8)\n\tv_mfma_f32_16x16x32_bf16 v[48:51], a[20:23], v[40:43], v[48:51]\n\tds_read_b64_tr_b16 v[28:29], v227 offset:49152\n\tds_read_b64_tr_b16 v[30:31], v227 offset:51200\n\tv_mfma_f32_16x16x32_bf16 v[56:59], a[24:27], v[40:43], v[56:59]\n\tv_permlane16_swap_b32 v192, v194\n\t" \
    "v_permlane16_swap_b32 v193, v195\n\tv_mfma_f32_16x16x32_bf16 v[64:67], a[28:31], v[40:43], v[64:67]\n\tv_permlane16_swap_b32 v196, v198\n\tv_permlane16_swap_b32 v197, v199\n\ts_waitcnt lgkmcnt(8)\n\tv_mfma_f32_16x16x32_bf16 v[52:55], a[20:23], v[44:47], v[52:55]\n\tds_read_b64_tr_b16 v[32:33], v226 offset:57344\n\tds_read_b64_tr_b16 v[34:35], v226 offset:59392\n\t" \
    "v_mfma_f32_16x16x32_bf16 v[60:63], a[24:27], v[44:47], v[60:63]\n\tv_permlane16_swap_b32 v200, v202\n\tv_permlane16_swap_b32 v201, v203\n\tv_mfma_f32_16x16x32_bf16 v[68:71], a[28:31], v[44:47], v[68:71]\n\tv_lshlrev_b32 v72, 16, v192\n\tv_and_b32 v73, 0xffff0000, v192\n\ts_waitcnt lgkmcnt(8)\n\tv_mfma_f32_16x16x32_bf16 v[48:51], a[32:35], v[16:19], v[48:51]\n\t" \
    "ds_read_b64_tr_b16 v[36:37], v227 offset:57344\n\tds_read_b64_tr_b16 v[38:39], v227 offset:59392\n\tv_mfma_f32_16x16x32_bf16 v[56:59], a[36:39], v[16:19], v[56:59]\n\tv_lshlrev_b32 v74, 16, v193\n\tv_and_b32 v75, 0xffff0000, v193\n\tv_mfma_f32_16x16x32_bf16 v[64:67], a[40:43], v[16:19], v[64:67]\n\tv_lshlrev_b32 v76, 16, v194\n\tv_and_b32 v77, 0xffff0000, v194\n\t" \
    "s_waitcnt lgkmcnt(8)\n\tv_mfma_f32_16x16x32_bf16 v[52:55], a[32:35], v[20:23], v[52:55]\n\tds_read_b64_tr_b16 v[40:41], v6 offset:0\n\tds_read_b64_tr_b16 v[42:43], v6 offset:2048\n\tv_mfma_f32_16x16x32_bf16 v[60:63], a[36:39], v[20:23], v[60:63]\n\tv_lshlrev_b32 v78, 16, v195\n\tv_and_b32 v79, 0xffff0000, v195\n\tv_mfma_f32_16x16x32_bf16 v[68:71], a[40:43], v[20:23], v[68:71]\n\t" \
    "v_lshlrev_b32 v80, 16, v196\n\tv_and_b32 v81, 0xffff0000, v196\n\ts_waitcnt lgkmcnt(8)\n\tv_mfma_f32_16x16x32_bf16 v[48:51], a[44:47], v[24:27], v[48:51]\n\tds_read_b64_tr_b16 v[44:45], v7 offset:0\n\tds_read_b64_tr_b16 v[46:47], v7 offset:2048\n\tv_mfma_f32_16x16x32_bf16 v[56:59], a[48:51], v[24:27], v[56:59]\n\tv_lshlrev_b32 v82, 16, v197\n\t" \
    "v_and_b32 v83, 0xffff0000, v197\n\tv_mfma_f32_16x16x32_bf16 v[64:67], a[52:55], v[24:27], v[64:67]\n\tv_lshlrev_b32 v84, 16, v198\n\tv_and_b32 v85, 0xffff0000, v198\n\ts_waitcnt lgkmcnt(8)\n\tv_mfma_f32_16x16x32_bf16 v[52:55], a[44:47], v[28:31], v[52:55]\n\tds_read_b64_tr_b16 v[16:17], v6 offset:8192\n\tds_read_b64_tr_b16 v[18:19], v6 offset:10240\n\t" \
    "v_mfma_f32_16x16x32_bf16 v[60:63], a[48:51], v[28:31], v[60:63]\n\tv_lshlrev_b32 v86, 16, v199\n\tv_and_b32 v87, 0xffff0000, v199\n\tv_mfma_f32_16x16x32_bf16 v[68:71], a[52:55], v[28:31], v[68:71]\n\tv_lshlrev_b32 v88, 16, v200\n\tv_and_b32 v89, 0xffff0000, v200\n\ts_waitcnt lgkmcnt(8)\n\tv_mfma_f32_16x16x32_bf16 v[48:51], a[56:59], v[32:35], v[48:51]\n\t" \
    "ds_read_b64_tr_b16 v[20:21], v7 offset:8192\n\tds_read_b64_tr_b16 v[22:23], v7 offset:10240\n\tv_mfma_f32_16x16x32_bf16 v[56:59], a[60:63], v[32:35], v[56:59]\n\tv_lshlrev_b32 v90, 16, v201\n\tv_and_b32 v91, 0xffff0000, v201\n\tv_mfma_f32_16x16x32_bf16 v[64:67], a[64:67], v[32:35], v[64:67]\n\tv_lshlrev_b32 v92, 16, v202\n\tv_and_b32 v93, 0xffff0000, v202\n\t" \
    "s_waitcnt lgkmcnt(8)\n\tv_mfma_f32_16x16x32_bf16 v[52:55], a[56:59], v[36:39], v[52:55]\n\tds_read_b64_tr_b16 v[24:25], v6 offset:16384\n\tds_read_b64_tr_b16 v[26:27], v6 offset:18432\n\tv_mfma_f32_16x16x32_bf16 v[60:63], a[60:63], v[36:39], v[60:63]\n\tv_lshlrev_b32 v94, 16, v203\n\tv_and_b32 v95, 0xffff0000, v203\n\tv_mfma_f32_16x16x32_bf16 v[68:71], a[64:67], v[36:39], v[68:71]\n\t" \
    "v_fma_f32 v96, v72, v216, v217\n\tv_fma_f32 v97, v73, v216, v217\n\ts_waitcnt lgkmcnt(8)\n\tv_mfma_f32_16x16x32_bf16 v[48:51], a[68:71], v[40:43], v[48:51]\n\tds_read_b64_tr_b16 v[28:29], v7 offset:16384\n\tds_read_b64_tr_b16 v[30:31], v7 offset:18432\n\tv_mfma_f32_16x16x32_bf16 v[56:59], a[72:75], v[40:43], v[56:59]\n\tv_fma_f32 v98, v74, v216, v217\n\t" \
    "v_fma_f32 v99, v75, v216, v217\n\tv_mfma_f32_16x16x32_bf16 v[64:67], a[76:79], v[40:43], v[64:67]\n\tv_fma_f32 v100, v76, v218, v219\n\tv_fma_f32 v101, v77, v218, v219\n\ts_waitcnt lgkmcnt(8)\n\tv_mfma_f32_16x16x32_bf16 v[52:55], a[68:71], v[44:47], v[52:55]\n\tds_read_b64_tr_b16 v[32:33], v6 offset:24576\n\tds_read_b64_tr_b16 v[34:35], v6 offset:26624\n\t" \
    "v_mfma_f32_16x16x32_bf16 v[60:63], a[72:75], v[44:47], v[60:63]\n\tv_fma_f32 v102, v78, v218, v219\n\tv_fma_f32 v103, v79, v218, v219\n\tv_mfma_f32_16x16x32_bf16 v[68:71], a[76:79], v[44:47], v[68:71]\n\tv_fma_f32 v104, v80, v216, v217\n\tv_fma_f32 v105, v81, v216, v217\n\ts_waitcnt lgkmcnt(8)\n\tv_mfma_f32_16x16x32_bf16 v[48:51], a[80:83], v[16:19], v[48:51]\n\t" \
    "ds_read_b64_tr_b16 v[36:37], v7 offset:24576\n\tds_read_b64_tr_b16 v[38:39], v7 offset:26624\n\tv_mfma_f32_16x16x32_bf16 v[56:59], a[84:87], v[16:19], v[56:59]\n\tv_fma_f32 v106, v82, v216, v217\n\tv_fma_f32 v107, v83, v216, v217\n\tv_mfma_f32_16x16x32_bf16 v[64:67], a[88:91], v[16:19], v[64:67]\n\tv_fma_f32 v108, v84, v218, v219\n\tv_fma_f32 v109, v85, v218, v219\n\t" \
    "s_waitcnt lgkmcnt(8)\n\tv_mfma_f32_16x16x32_bf16 v[52:55], a[80:83], v[20:23], v[52:55]\n\tds_read_b64_tr_b16 v[40:41], v222 offset:20480\n\tds_read_b64_tr_b16 v[42:43], v222 offset:22528\n\tv_mfma_f32_16x16x32_bf16 v[60:63], a[84:87], v[20:23], v[60:63]\n\tv_fma_f32 v110, v86, v218, v219\n\tv_fma_f32 v111, v87, v218, v219\n\tv_mfma_f32_16x16x32_bf16 v[68:71], a[88:91], v[20:23], v[68:71]\n\t" \
    "v_fma_f32 v112, v88, v216, v217\n\tv_fma_f32 v113, v89, v216, v217\n\ts_waitcnt lgkmcnt(8)\n\tv_mfma_f32_16x16x32_bf16 v[48:51], a[92:95], v[24:27], v[48:51]\n\tds_read_b64_tr_b16 v[44:45], v223 offset:20480\n\tds_read_b64_tr_b16 v[46:47], v223 offset:22528\n\tv_mfma_f32_16x16x32_bf16 v[56:59], a[96:99], v[24:27], v[56:59]\n\tv_fma_f32 v114, v90, v216, v217\n\t" \
    "v_fma_f32 v115, v91, v216, v217\n\tv_mfma_f32_16x16x32_bf16 v[64:67], a[100:103], v[24:27], v[64:67]\n\tv_fma_f32 v116, v92, v218, v219\n\tv_fma_f32 v117, v93, v218, v219\n\ts_waitcnt lgkmcnt(8)\n\tv_mfma_f32_16x16x32_bf16 v[52:55], a[92:95], v[28:31], v[52:55]\n\tds_read_b64_tr_b16 v[16:17], v222 offset:24576\n\tds_read_b64_tr_b16 v[18:19], v222 offset:26624\n\t" \
    "v_mfma_f32_16x16x32_bf16 v[60:63], a[96:99], v[28:31], v[60:63]\n\tv_fma_f32 v118, v94, v218, v219\n\tv_fma_f32 v119, v95, v218, v219\n\tv_mfma_f32_16x16x32_bf16 v[68:71], a[100:103], v[28:31], v[68:71]\n\ts_waitcnt lgkmcnt(8)\n\tv_mfma_f32_16x16x32_bf16 v[48:51], a[104:107], v[32:35], v[48:51]\n\tds_read_b64_tr_b16 v[20:21], v223 offset:24576\n\tds_read_b64_tr_b16 v[22:23], v223 offset:26624\n\t" \
    "v_mfma_f32_16x16x32_bf16 v[56:59], a[108:111], v[32:35], v[56:59]\n\tv_mfma_f32_16x16x32_bf16 v[64:67], a[112:115], v[32:35], v[64:67]\n\ts_waitcnt lgkmcnt(8)\n\tv_mfma_f32_16x16x32_bf16 v[52:55], a[104:107], v[36:39], v[52:55]\n\tds_read_b64_tr_b16 v[24:25], v222 offset:28672\n\tds_read_b64_tr_b16 v[26:27], v222 offset:30720\n\tv_mfma_f32_16x16x32_bf16 v[60:63], a[108:111], v[36:39], v[60:63]\n\tv_mfma_f32_16x16x32_bf16 v[68:71], a[112:115], v[36:39], v[68:71]\n\t" \
    "s_waitcnt lgkmcnt(8)\n\tv_mfma_f32_16x16x32_bf16 v[48:51], a[116:119], v[40:43], v[48:51]\n\tds_read_b64_tr_b16 v[28:29], v223 offset:28672\n\tds_read_b64_tr_b16 v[30:31], v223 offset:30720\n\tv_mfma_f32_16x16x32_bf16 v[56:59], a[120:123], v[40:43], v[56:59]\n\tv_mfma_f32_16x16x32_bf16 v[64:67], a[124:127], v[40:43], v[64:67]\n\ts_waitcnt lgkmcnt(8)\n\tv_mfma_f32_16x16x32_bf16 v[52:55], a[116:119], v[44:47], v[52:55]\n\t" \
    "ds_read_b64_tr_b16 v[32:33], v6 offset:40960\n\tds_read_b64_tr_b16 v[34:35], v6 offset:43008\n\tv_mfma_f32_16x16x32_bf16 v[60:63], a[120:123], v[44:47], v[60:63]\n\tv_mfma_f32_16x16x32_bf16 v[68:71], a[124:127], v[44:47], v[68:71]\n\ts_waitcnt lgkmcnt(8)\n\tv_mfma_f32_16x16x32_bf16 v[48:51], a[128:131], v[16:19], v[48:51]\n\tds_read_b64_tr_b16 v[36:37], v7 offset:40960\n\tds_read_b64_tr_b16 v[38:39], v7 offset:43008\n\t" \
    "v_mfma_f32_16x16x32_bf16 v[56:59], a[132:135], v[16:19], v[56:59]\n\tv_mfma_f32_16x16x32_bf16 v[64:67], a[136:139], v[16:19], v[64:67]\n\ts_waitcnt lgkmcnt(8)\n\tv_mfma_f32_16x16x32_bf16 v[52:55], a[128:131], v[20:23], v[52:55]\n\tds_read_b64_tr_b16 v[40:41], v6 offset:45056\n\tds_read_b64_tr_b16 v[42:43], v6 offset:47104\n\tv_mfma_f32_16x16x32_bf16 v[60:63], a[132:135], v[20:23], v[60:63]\n\tv_mfma_f32_16x16x32_bf16 v[68:71], a[136:139], v[20:23], v[68:71]\n\t" \
    "s_waitcnt lgkmcnt(8)\n\tv_mfma_f32_16x16x32_bf16 v[48:51], a[140:143], v[24:27], v[48:51]\n\tds_read_b64_tr_b16 v[44:45], v7 offset:45056\n\tds_read_b64_tr_b16 v[46:47], v7 offset:47104\n\ts_add_u32 s68, s55, 0\n\tv_mfma_f32_16x16x32_bf16 v[56:59], a[144:147], v[24:27], v[56:59]\n\tbuffer_load_dwordx4 v[120:123], v8, s[8:11], s68 offen offset:0 sc1\n\tv_mfma_f32_16x16x32_bf16 v[64:67], a[148:151], v[24:27], v[64:67]\n\t" \
    "s_waitcnt lgkmcnt(8)\n\tv_mfma_f32_16x16x32_bf16 v[52:55], a[140:143], v[28:31], v[52:55]\n\tds_read_b64_tr_b16 v[16:17], v6 offset:49152\n\tds_read_b64_tr_b16 v[18:19], v6 offset:51200\n\tv_mfma_f32_16x16x32_bf16 v[60:63], a[144:147], v[28:31], v[60:63]\n\tbuffer_load_dwordx4 v[124:127], v8, s[8:11], s68 offen offset:1024 sc1\n\tv_mfma_f32_16x16x32_bf16 v[68:71], a[148:151], v[28:31], v[68:71]\n\ts_waitcnt lgkmcnt(8)\n\t" \
    "v_mfma_f32_16x16x32_bf16 v[48:51], a[152:155], v[32:35], v[48:51]\n\tds_read_b64_tr_b16 v[20:21], v7 offset:49152\n\tds_read_b64_tr_b16 v[22:23], v7 offset:51200\n\tv_mfma_f32_16x16x32_bf16 v[56:59], a[156:159], v[32:35], v[56:59]\n\tbuffer_load_dwordx4 v[128:131], v8, s[8:11], s68 offen offset:2048 sc1\n\tv_mfma_f32_16x16x32_bf16 v[64:67], a[160:163], v[32:35], v[64:67]\n\ts_waitcnt lgkmcnt(8)\n\tv_mfma_f32_16x16x32_bf16 v[52:55], a[152:155], v[36:39], v[52:55]\n\t" \
    "ds_read_b64_tr_b16 v[24:25], v6 offset:53248\n\tds_read_b64_tr_b16 v[26:27], v6 offset:55296\n\tv_mfma_f32_16x16x32_bf16 v[60:63], a[156:159], v[36:39], v[60:63]\n\tbuffer_load_dword v132, v236, s[8:11], s68 offen offset:3072 sc1\n\tv_mfma_f32_16x16x32_bf16 v[68:71], a[160:163], v[36:39], v[68:71]\n\ts_waitcnt lgkmcnt(8)\n\tv_mfma_f32_16x16x32_bf16 v[48:51], a[164:167], v[40:43], v[48:51]\n\tds_read_b64_tr_b16 v[28:29], v7 offset:53248\n\t" \
    "ds_read_b64_tr_b16 v[30:31], v7 offset:55296\n\tv_mfma_f32_16x16x32_bf16 v[56:59], a[168:171], v[40:43], v[56:59]\n\tv_mfma_f32_16x16x32_bf16 v[64:67], a[172:175], v[40:43], v[64:67]\n\ts_waitcnt lgkmcnt(8)\n\tv_mfma_f32_16x16x32_bf16 v[52:55], a[164:167], v[44:47], v[52:55]\n\tds_read_b64_tr_b16 v[32:33], v6 offset:57344\n\tds_read_b64_tr_b16 v[34:35], v6 offset:59392\n\ts_add_u32 s68, s55, 3328\n\t" \
    "v_mfma_f32_16x16x32_bf16 v[60:63], a[168:171], v[44:47], v[60:63]\n\tbuffer_load_dwordx4 v[144:147], v8, s[8:11], s68 offen offset:0 sc1\n\tv_mfma_f32_16x16x32_bf16 v[68:71], a[172:175], v[44:47], v[68:71]\n\ts_waitcnt lgkmcnt(8)\n\tv_mfma_f32_16x16x32_bf16 v[48:51], a[176:179], v[16:19], v[48:51]\n\tds_read_b64_tr_b16 v[36:37], v7 offset:57344\n\tds_read_b64_tr_b16 v[38:39], v7 offset:59392\n\tv_mfma_f32_16x16x32_bf16 v[56:59], a[180:183], v[16:19], v[56:59]\n\t" \
    "buffer_load_dwordx4 v[148:151], v8, s[8:11], s68 offen offset:1024 sc1\n\tv_mfma_f32_16x16x32_bf16 v[64:67], a[184:187], v[16:19], v[64:67]\n\ts_waitcnt lgkmcnt(8)\n\tv_mfma_f32_16x16x32_bf16 v[52:55], a[176:179], v[20:23], v[52:55]\n\tds_read_b64_tr_b16 v[40:41], v226 offset:0\n\tds_read_b64_tr_b16 v[42:43], v226 offset:2048\n\tv_mfma_f32_16x16x32_bf16 v[60:63], a[180:183], v[20:23], v[60:63]\n\tbuffer_load_dwordx4 v[152:155], v8, s[8:11], s68 offen offset:2048 sc1\n\t" \
    "v_mfma_f32_16x16x32_bf16 v[68:71], a[184:187], v[20:23], v[68:71]\n\ts_waitcnt lgkmcnt(8)\n\tv_mfma_f32_16x16x32_bf16 v[48:51], a[188:191], v[24:27], v[48:51]\n\tds_read_b64_tr_b16 v[44:45], v227 offset:0\n\tds_read_b64_tr_b16 v[46:47], v227 offset:2048\n\tv_mfma_f32_16x16x32_bf16 v[56:59], a[192:195], v[24:27], v[56:59]\n\tbuffer_load_dword v156, v236, s[8:11], s68 offen offset:3072 sc1\n\tv_mfma_f32_16x16x32_bf16 v[64:67], a[196:199], v[24:27], v[64:67]\n\t" \
    "s_waitcnt lgkmcnt(8)\n\tv_mfma_f32_16x16x32_bf16 v[52:55], a[188:191], v[28:31], v[52:55]\n\tv_mfma_f32_16x16x32_bf16 v[60:63], a[192:195], v[28:31], v[60:63]\n\tv_mfma_f32_16x16x32_bf16 v[68:71], a[196:199], v[28:31], v[68:71]\n\ts_waitcnt lgkmcnt(6)\n\tv_mfma_f32_16x16x32_bf16 v[48:51], a[200:203], v[32:35], v[48:51]\n\ts_add_u32 s68, s55, 6656\n\tv_mfma_f32_16x16x32_bf16 v[56:59], a[204:207], v[32:35], v[56:59]\n\t" \
    "buffer_load_dwordx4 v[168:171], v8, s[8:11], s68 offen offset:0 sc1\n\tv_mfma_f32_16x16x32_bf16 v[64:67], a[208:211], v[32:35], v[64:67]\n\ts_waitcnt lgkmcnt(4)\n\tv_mfma_f32_16x16x32_bf16 v[52:55], a[200:203], v[36:39], v[52:55]\n\tv_mfma_f32_16x16x32_bf16 v[60:63], a[204:207], v[36:39], v[60:63]\n\tbuffer_load_dwordx4 v[172:175], v8, s[8:11], s68 offen offset:1024 sc1\n\tv_mfma_f32_16x16x32_bf16 v[68:71], a[208:211], v[36:39], v[68:71]\n\ts_waitcnt lgkmcnt(2)\n\t" \
    "v_mfma_f32_16x16x32_bf16 v[48:51], a[212:215], v[40:43], v[48:51]\n\tv_mfma_f32_16x16x32_bf16 v[56:59], a[216:219], v[40:43], v[56:59]\n\tbuffer_load_dwordx4 v[176:179], v8, s[8:11], s68 offen offset:2048 sc1\n\tv_mfma_f32_16x16x32_bf16 v[64:67], a[220:223], v[40:43], v[64:67]\n\ts_waitcnt lgkmcnt(0)\n\tv_mfma_f32_16x16x32_bf16 v[52:55], a[212:215], v[44:47], v[52:55]\n\tv_mfma_f32_16x16x32_bf16 v[60:63], a[216:219], v[44:47], v[60:63]\n\tbuffer_load_dword v180, v236, s[8:11], s68 offen offset:3072 sc1\n\t" \
    "v_mfma_f32_16x16x32_bf16 v[68:71], a[220:223], v[44:47], v[68:71]\n\ts_cmp_eq_u32 s70, 0\n\ts_cbranch_scc1 116f\n\tv_cmp_ne_u64 vcc, s[36:37], v[250:251]\n\ts_cbranch_vccz 118f\n\ts_mov_b32 s71, 1048576\n\t117:\n\ts_sleep 1\n\t" \
    "buffer_load_dwordx2 v[250:251], v241, s[8:11], s51 offen sc1\n\ts_waitcnt vmcnt(0)\n\tv_cmp_ne_u64 vcc, s[36:37], v[250:251]\n\ts_cbranch_vccz 118f\n\ts_sub_u32 s71, s71, 1\n\ts_cbranch_scc0 117b\n\t118:\n\tbuffer_load_dwordx4 v[204:207], v8, s[8:11], s56 offen offset:0 sc1\n\t" \
    "buffer_load_dwordx4 v[208:211], v8, s[8:11], s56 offen offset:1024 sc1\n\tbuffer_load_dwordx4 v[212:215], v8, s[8:11], s56 offen offset:2048 sc1\n\ts_waitcnt vmcnt(0)\n\t116:\n\ts_waitcnt vmcnt(16)\n\tv_lshlrev_b32 v16, 16, v204\n\tv_and_b32 v17, 0xffff0000, v204\n\tv_lshlrev_b32 v18, 16, v205\n\t" \
    "v_and_b32 v19, 0xffff0000, v205\n\tv_lshlrev_b32 v20, 16, v206\n\tv_and_b32 v21, 0xffff0000, v206\n\tv_lshlrev_b32 v22, 16, v207\n\tv_and_b32 v23, 0xffff0000, v207\n\tv_lshlrev_b32 v24, 16, v208\n\tv_and_b32 v25, 0xffff0000, v208\n\tv_lshlrev_b32 v26, 16, v209\n\t" \
    "v_and_b32 v27, 0xffff0000, v209\n\tv_lshlrev_b32 v28, 16, v210\n\tv_and_b32 v29, 0xffff0000, v210\n\tv_lshlrev_b32 v30, 16, v211\n\tv_and_b32 v31, 0xffff0000, v211\n\tv_lshlrev_b32 v32, 16, v212\n\tv_and_b32 v33, 0xffff0000, v212\n\tv_lshlrev_b32 v34, 16, v213\n\t" \
    "v_and_b32 v35, 0xffff0000, v213\n\tv_lshlrev_b32 v36, 16, v214\n\tv_and_b32 v37, 0xffff0000, v214\n\tv_lshlrev_b32 v38, 16, v215\n\tv_and_b32 v39, 0xffff0000, v215\n\tv_pk_mul_f32 v[96:97], v[96:97], v[16:17]\n\tv_pk_mul_f32 v[98:99], v[98:99], v[18:19]\n\tv_pk_mul_f32 v[100:101], v[100:101], v[20:21]\n\t" \
    "v_pk_mul_f32 v[102:103], v[102:103], v[22:23]\n\tv_pk_mul_f32 v[104:105], v[104:105], v[24:25]\n\tv_pk_mul_f32 v[106:107], v[106:107], v[26:27]\n\tv_pk_mul_f32 v[108:109], v[108:109], v[28:29]\n\tv_pk_mul_f32 v[110:111], v[110:111], v[30:31]\n\tv_pk_mul_f32 v[112:113], v[112:113], v[32:33]\n\tv_pk_mul_f32 v[114:115], v[114:115], v[34:35]\n\tv_pk_mul_f32 v[116:117], v[116:117], v[36:37]\n\t" \
    "v_pk_mul_f32 v[118:119], v[118:119], v[38:39]\n\ts_mov_b32 s71, 1048576\n\ts_waitcnt vmcnt(8)\n\t119:\n\tv_add3_u32 v134, s36, v120, v121\n\tv_add3_u32 v135, v122, v123, v124\n\tv_add3_u32 v134, v134, v125, v126\n\tv_add3_u32 v135, v135, v127, v128\n\t" \
    "v_add3_u32 v134, v134, v129, v130\n\tv_add3_u32 v134, v134, v135, v131\n\tv_cmp_ne_u32 vcc, v134, v132\n\ts_cbranch_vccz 120f\n\ts_sub_u32 s71, s71, 1\n\ts_cbranch_scc1 120f\n\ts_sleep 6\n\ts_add_u32 s73, s55, 0\n\t" \
    "buffer_load_dwordx4 v[120:123], v8, s[8:11], s73 offen offset:0 sc1\n\tbuffer_load_dwordx4 v[124:127], v8, s[8:11], s73 offen offset:1024 sc1\n\tbuffer_load_dwordx4 v[128:131], v8, s[8:11], s73 offen offset:2048 sc1\n\tbuffer_load_dword v132, v236, s[8:11], s73 offen offset:3072 sc1\n\ts_waitcnt vmcnt(0)\n\ts_branch 119b\n\t120:\n\tv_lshlrev_b32 v16, 16, v120\n\t" \
    "v_and_b32 v17, 0xffff0000, v120\n\tv_lshlrev_b32 v18, 16, v121\n\tv_and_b32 v19, 0xffff0000, v121\n\tv_lshlrev_b32 v20, 16, v122\n\tv_and_b32 v21, 0xffff0000, v122\n\tv_lshlrev_b32 v22, 16, v123\n\tv_and_b32 v23, 0xffff0000, v123\n\tv_lshlrev_b32 v24, 16, v124\n\t" \
    "v_and_b32 v25, 0xffff0000, v124\n\tv_lshlrev_b32 v26, 16, v125\n\tv_and_b32 v27, 0xffff0000, v125\n\tv_lshlrev_b32 v28, 16, v126\n\tv_and_b32 v29, 0xffff0000, v126\n\tv_lshlrev_b32 v30, 16, v127\n\tv_and_b32 v31, 0xffff0000, v127\n\tv_lshlrev_b32 v32, 16, v128\n\t" \
    "v_and_b32 v33, 0xffff0000, v128\n\tv_lshlrev_b32 v34, 16, v129\n\tv_and_b32 v35, 0xffff0000, v129\n\tv_lshlrev_b32 v36, 16, v130\n\tv_and_b32 v37, 0xffff0000, v130\n\tv_lshlrev_b32 v38, 16, v131\n\tv_and_b32 v39, 0xffff0000, v131\n\tv_pk_add_f32 v[48:49], v[48:49], v[16:17]\n\t" \
    "v_pk_add_f32 v[50:51], v[50:51], v[18:19]\n\tv_pk_add_f32 v[52:53], v[52:53], v[20:21]\n\tv_pk_add_f32 v[54:55], v[54:55], v[22:23]\n\tv_pk_add_f32 v[56:57], v[56:57], v[24:25]\n\tv_pk_add_f32 v[58:59], v[58:59], v[26:27]\n\tv_pk_add_f32 v[60:61], v[60:61], v[28:29]\n\tv_pk_add_f32 v[62:63], v[62:63], v[30:31]\n\tv_pk_add_f32 v[64:65], v[64:65], v[32:33]\n\t" \
    "v_pk_add_f32 v[66:67], v[66:67], v[34:35]\n\tv_pk_add_f32 v[68:69], v[68:69], v[36:37]\n\tv_pk_add_f32 v[70:71], v[70:71], v[38:39]\n\ts_mov_b32 s71, 1048576\n\ts_waitcnt vmcnt(4)\n\t121:\n\tv_add3_u32 v158, s36, v144, v145\n\tv_add3_u32 v159, v146, v147, v148\n\t" \
    "v_add3_u32 v158, v158, v149, v150\n\tv_add3_u32 v159, v159, v151, v152\n\tv_add3_u32 v158, v158, v153, v154\n\tv_add3_u32 v158, v158, v159, v155\n\tv_cmp_ne_u32 vcc, v158, v156\n\ts_cbranch_vccz 122f\n\ts_sub_u32 s71, s71, 1\n\ts_cbranch_scc1 122f\n\t" \
    "s_sleep 6\n\ts_add_u32 s73, s55, 3328\n\tbuffer_load_dwordx4 v[144:147], v8, s[8:11], s73 offen offset:0 sc1\n\tbuffer_load_dwordx4 v[148:151], v8, s[8:11], s73 offen offset:1024 sc1\n\tbuffer_load_dwordx4 v[152:155], v8, s[8:11], s73 offen offset:2048 sc1\n\tbuffer_load_dword v156, v236, s[8:11], s73 offen offset:3072 sc1\n\ts_waitcnt vmcnt(0)\n\ts_branch 121b\n\t" \
    "122:\n\tv_lshlrev_b32 v16, 16, v144\n\tv_and_b32 v17, 0xffff0000, v144\n\tv_lshlrev_b32 v18, 16, v145\n\tv_and_b32 v19, 0xffff0000, v145\n\tv_lshlrev_b32 v20, 16, v146\n\tv_and_b32 v21, 0xffff0000, v146\n\tv_lshlrev_b32 v22, 16, v147\n\t" \
    "v_and_b32 v23, 0xffff0000, v147\n\tv_lshlrev_b32 v24, 16, v148\n\tv_and_b32 v25, 0xffff0000, v148\n\tv_lshlrev_b32 v26, 16, v149\n\tv_and_b32 v27, 0xffff0000, v149\n\tv_lshlrev_b32 v28, 16, v150\n\tv_and_b32 v29, 0xffff0000, v150\n\tv_lshlrev_b32 v30, 16, v151\n\t" \
    "v_and_b32 v31, 0xffff0000, v151\n\tv_lshlrev_b32 v32, 16, v152\n\tv_and_b32 v33, 0xffff0000, v152\n\tv_lshlrev_b32 v34, 16, v153\n\tv_and_b32 v35, 0xffff0000, v153\n\tv_lshlrev_b32 v36, 16, v154\n\tv_and_b32 v37, 0xffff0000, v154\n\tv_lshlrev_b32 v38, 16, v155\n\t" \
    "v_and_b32 v39, 0xffff0000, v155\n\tv_pk_add_f32 v[48:49], v[48:49], v[16:17]\n\tv_pk_add_f32 v[50:51], v[50:51], v[18:19]\n\tv_pk_add_f32 v[52:53], v[52:53], v[20:21]\n\tv_pk_add_f32 v[54:55], v[54:55], v[22:23]\n\tv_pk_add_f32 v[56:57], v[56:57], v[24:25]\n\tv_pk_add_f32 v[58:59], v[58:59], v[26:27]\n\tv_pk_add_f32 v[60:61], v[60:61], v[28:29]\n\t" \
    "v_pk_add_f32 v[62:63], v[62:63], v[30:31]\n\tv_pk_add_f32 v[64:65], v[64:65], v[32:33]\n\tv_pk_add_f32 v[66:67], v[66:67], v[34:35]\n\tv_pk_add_f32 v[68:69], v[68:69], v[36:37]\n\tv_pk_add_f32 v[70:71], v[70:71], v[38:39]\n\ts_mov_b32 s71, 1048576\n\ts_waitcnt vmcnt(0)\n\t123:\n\t" \
    "v_add3_u32 v182, s36, v168, v169\n\tv_add3_u32 v183, v170, v171, v172\n\tv_add3_u32 v182, v182, v173, v174\n\tv_add3_u32 v183, v183, v175, v176\n\tv_add3_u32 v182, v182, v177, v178\n\tv_add3_u32 v182, v182, v183, v179\n\tv_cmp_ne_u32 vcc, v182, v180\n\ts_cbranch_vccz 124f\n\t" \
    "s_sub_u32 s71, s71, 1\n\ts_cbranch_scc1 124f\n\ts_sleep 6\n\ts_add_u32 s73, s55, 6656\n\tbuffer_load_dwordx4 v[168:171], v8, s[8:11], s73 offen offset:0 sc1\n\tbuffer_load_dwordx4 v[172:175], v8, s[8:11], s73 offen offset:1024 sc1\n\tbuffer_load_dwordx4 v[176:179], v8, s[8:11], s73 offen offset:2048 sc1\n\tbuffer_load_dword v180, v236, s[8:11], s73 offen offset:3072 sc1\n\t" \
    "s_waitcnt vmcnt(0)\n\ts_branch 123b\n\t124:\n\ts_mov_b32 s68, 0xbfb8aa3b\n\ts_mov_b32 s69, 0xbfb8aa3b\n\tv_lshlrev_b32 v16, 16, v168\n\tv_and_b32 v17, 0xffff0000, v168\n\tv_lshlrev_b32 v18, 16, v169\n\t" \
    "v_and_b32 v19, 0xffff0000, v169\n\tv_lshlrev_b32 v20, 16, v170\n\tv_and_b32 v21, 0xffff0000, v170\n\tv_lshlrev_b32 v22, 16, v171\n\tv_and_b32 v23, 0xffff0000, v171\n\tv_pk_add_f32 v[48:49], v[48:49], v[16:17]\n\tv_pk_add_f32 v[50:51], v[50:51], v[18:19]\n\tv_pk_add_f32 v[52:53], v[52:53], v[20:21]\n\t" \
    "v_pk_add_f32 v[54:55], v[54:55], v[22:23]\n\tv_pk_mul_f32 v[16:17], v[48:49], s[68:69]\n\tv_pk_mul_f32 v[18:19], v[50:51], s[68:69]\n\tv_pk_mul_f32 v[20:21], v[52:53], s[68:69]\n\tv_pk_mul_f32 v[22:23], v[54:55], s[68:69]\n\tv_lshlrev_b32 v24, 16, v172\n\tv_exp_f32 v16, v16\n\tv_and_b32 v25, 0xffff0000, v172\n\t" \
    "v_exp_f32 v17, v17\n\tv_lshlrev_b32 v26, 16, v173\n\tv_exp_f32 v18, v18\n\tv_and_b32 v27, 0xffff0000, v173\n\tv_exp_f32 v19, v19\n\tv_lshlrev_b32 v28, 16, v174\n\tv_exp_f32 v20, v20\n\tv_and_b32 v29, 0xffff0000, v174\n\t" \
    "v_exp_f32 v21, v21\n\tv_lshlrev_b32 v30, 16, v175\n\tv_exp_f32 v22, v22\n\tv_and_b32 v31, 0xffff0000, v175\n\tv_exp_f32 v23, v23\n\tv_pk_add_f32 v[56:57], v[56:57], v[24:25]\n\tv_pk_add_f32 v[16:17], v[16:17], 1.0 op_sel_hi:[1,0]\n\tv_pk_add_f32 v[18:19], v[18:19], 1.0 op_sel_hi:[1,0]\n\t" \
    "v_pk_add_f32 v[20:21], v[20:21], 1.0 op_sel_hi:[1,0]\n\tv_pk_add_f32 v[22:23], v[22:23], 1.0 op_sel_hi:[1,0]\n\tv_pk_add_f32 v[58:59], v[58:59], v[26:27]\n\tv_rcp_f32 v16, v16\n\tv_pk_add_f32 v[60:61], v[60:61], v[28:29]\n\tv_rcp_f32 v17, v17\n\tv_pk_add_f32 v[62:63], v[62:63], v[30:31]\n\tv_rcp_f32 v18, v18\n\t" \
    "v_pk_mul_f32 v[24:25], v[56:57], s[68:69]\n\tv_rcp_f32 v19, v19\n\tv_pk_mul_f32 v[26:27], v[58:59], s[68:69]\n\tv_rcp_f32 v20, v20\n\tv_pk_mul_f32 v[28:29], v[60:61], s[68:69]\n\tv_rcp_f32 v21, v21\n\tv_pk_mul_f32 v[30:31], v[62:63], s[68:69]\n\tv_rcp_f32 v22, v22\n\t" \
    "v_lshlrev_b32 v32, 16, v176\n\tv_rcp_f32 v23, v23\n\tv_and_b32 v33, 0xffff0000, v176\n\tv_exp_f32 v24, v24\n\tv_pk_mul_f32 v[48:49], v[48:49], v[96:97]\n\tv_exp_f32 v25, v25\n\tv_pk_mul_f32 v[50:51], v[50:51], v[98:99]\n\tv_exp_f32 v26, v26\n\t" \
    "v_pk_mul_f32 v[52:53], v[52:53], v[100:101]\n\tv_exp_f32 v27, v27\n\tv_pk_mul_f32 v[54:55], v[54:55], v[102:103]\n\tv_exp_f32 v28, v28\n\tv_pk_fma_f32 v[16:17], v[16:17], v[48:49], v[72:73]\n\tv_exp_f32 v29, v29\n\tv_pk_fma_f32 v[18:19], v[18:19], v[50:51], v[74:75]\n\tv_exp_f32 v30, v30\n\t" \
    "v_pk_fma_f32 v[20:21], v[20:21], v[52:53], v[76:77]\n\tv_exp_f32 v31, v31\n\tv_pk_fma_f32 v[22:23], v[22:23], v[54:55], v[78:79]\n\tv_cvt_pk_bf16_f32 v48, v16, v17\n\tv_cvt_pk_bf16_f32 v49, v18, v19\n\tv_cvt_pk_bf16_f32 v50, v20, v21\n\tv_cvt_pk_bf16_f32 v51, v22, v23\n\ts_nop 0\n\t" \
    "v_permlane16_swap_b32 v48, v50\n\tv_permlane16_swap_b32 v49, v51\n\tbuffer_store_dwordx4 v[48:51], v243, s[24:27], 0 offen offset:0\n\tv_pk_add_f32 v[24:25], v[24:25], 1.0 op_sel_hi:[1,0]\n\tv_pk_add_f32 v[26:27], v[26:27], 1.0 op_sel_hi:[1,0]\n\tv_pk_add_f32 v[28:29], v[28:29], 1.0 op_sel_hi:[1,0]\n\tv_pk_add_f32 v[30:31], v[30:31], 1.0 op_sel_hi:[1,0]\n\tv_lshlrev_b32 v34, 16, v177\n\t" \
    "v_rcp_f32 v24, v24\n\tv_and_b32 v35, 0xffff0000, v177\n\tv_rcp_f32 v25, v25\n\tv_lshlrev_b32 v36, 16, v178\n\tv_rcp_f32 v26, v26\n\tv_and_b32 v37, 0xffff0000, v178\n\tv_rcp_f32 v27, v27\n\tv_lshlrev_b32 v38, 16, v179\n\t" \
    "v_rcp_f32 v28, v28\n\tv_and_b32 v39, 0xffff0000, v179\n\tv_rcp_f32 v29, v29\n\tv_pk_add_f32 v[64:65], v[64:65], v[32:33]\n\tv_rcp_f32 v30, v30\n\tv_pk_add_f32 v[66:67], v[66:67], v[34:35]\n\tv_rcp_f32 v31, v31\n\tv_pk_add_f32 v[68:69], v[68:69], v[36:37]\n\t" \
    "v_pk_mul_f32 v[56:57], v[56:57], v[104:105]\n\tv_pk_mul_f32 v[58:59], v[58:59], v[106:107]\n\tv_pk_mul_f32 v[60:61], v[60:61], v[108:109]\n\tv_pk_mul_f32 v[62:63], v[62:63], v[110:111]\n\tv_pk_fma_f32 v[24:25], v[24:25], v[56:57], v[80:81]\n\tv_pk_fma_f32 v[26:27], v[26:27], v[58:59], v[82:83]\n\tv_pk_fma_f32 v[28:29], v[28:29], v[60:61], v[84:85]\n\tv_pk_fma_f32 v[30:31], v[30:31], v[62:63], v[86:87]\n\t" \
    "v_cvt_pk_bf16_f32 v56, v24, v25\n\tv_cvt_pk_bf16_f32 v57, v26, v27\n\tv_cvt_pk_bf16_f32 v58, v28, v29\n\tv_cvt_pk_bf16_f32 v59, v30, v31\n\ts_nop 0\n\tv_permlane16_swap_b32 v56, v58\n\tv_permlane16_swap_b32 v57, v59\n\tbuffer_store_dwordx4 v[56:59], v243, s[24:27], 0 offen offset:32\n\t" \
    "v_pk_add_f32 v[70:71], v[70:71], v[38:39]\n\tv_pk_mul_f32 v[32:33], v[64:65], s[68:69]\n\tv_pk_mul_f32 v[34:35], v[66:67], s[68:69]\n\tv_pk_mul_f32 v[36:37], v[68:69], s[68:69]\n\tv_pk_mul_f32 v[38:39], v[70:71], s[68:69]\n\tv_exp_f32 v32, v32\n\tv_exp_f32 v33, v33\n\tv_exp_f32 v34, v34\n\t" \
    "v_exp_f32 v35, v35\n\tv_exp_f32 v36, v36\n\tv_exp_f32 v37, v37\n\tv_exp_f32 v38, v38\n\tv_exp_f32 v39, v39\n\tv_pk_add_f32 v[32:33], v[32:33], 1.0 op_sel_hi:[1,0]\n\tv_pk_add_f32 v[34:35], v[34:35], 1.0 op_sel_hi:[1,0]\n\tv_pk_add_f32 v[36:37], v[36:37], 1.0 op_sel_hi:[1,0]\n\t" \
    "v_pk_add_f32 v[38:39], v[38:39], 1.0 op_sel_hi:[1,0]\n\tv_rcp_f32 v32, v32\n\tv_rcp_f32 v33, v33\n\tv_rcp_f32 v34, v34\n\tv_rcp_f32 v35, v35\n\tv_rcp_f32 v36, v36\n\tv_rcp_f32 v37, v37\n\tv_rcp_f32 v38, v38\n\t" \
    "v_rcp_f32 v39, v39\n\tv_pk_mul_f32 v[64:65], v[64:65], v[112:113]\n\tv_pk_mul_f32 v[66:67], v[66:67], v[114:115]\n\tv_pk_mul_f32 v[68:69], v[68:69], v[116:117]\n\tv_pk_mul_f32 v[70:71], v[70:71], v[118:119]\n\tv_pk_fma_f32 v[32:33], v[32:33], v[64:65], v[88:89]\n\tv_pk_fma_f32 v[34:35], v[34:35], v[66:67], v[90:91]\n\tv_pk_fma_f32 v[36:37], v[36:37], v[68:69], v[92:93]\n\t" \
    "v_pk_fma_f32 v[38:39], v[38:39], v[70:71], v[94:95]\n\tv_cvt_pk_bf16_f32 v64, v32, v33\n\tv_cvt_pk_bf16_f32 v65, v34, v35\n\tv_cvt_pk_bf16_f32 v66, v36, v37\n\tv_cvt_pk_bf16_f32 v67, v38, v39\n\ts_nop 0\n\tv_permlane16_swap_b32 v64, v66\n\tv_permlane16_swap_b32 v65, v67\n\t" \
    "buffer_store_dwordx4 v[64:67], v243, s[24:27], 0 offen offset:64\n\ts_branch 103f\n\t102:\n\tbuffer_store_dwordx4 v[72:75], v8, s[8:11], s45 offen offset:0 sc1\n\tbuffer_store_dwordx4 v[80:83], v8, s[8:11], s45 offen offset:1024 sc1\n\tbuffer_store_dwordx4 v[88:91], v8, s[8:11], s45 offen offset:2048 sc1\n\ts_add_u32 s49, s49, 4096\n\ts_waitcnt vmcnt(0) lgkmcnt(0)\n\t" \
    "buffer_store_dwordx2 v[248:249], off, s[8:11], s49 sc1\n\ts_load_dwordx8 s[12:19], s[0:1], 16\n\ts_lshr_b32 s67, s40, 3\n\ts_sub_u32 s67, s67, 24\n\ts_lshl_b32 s67, s67, 2\n\ts_add_u32 s67, s67, s38\n\ts_lshl_b32 s65, s67, 6\n\tv_add_u32 v6, s65, v240\n\t" \
    "v_mul_u32_u24 v7, 7282, v6\n\tv_lshrrev_b32 v7, 16, v7\n\tv_mul_u32_u24 v5, 9, v7\n\tv_sub_u32 v6, v6, v5\n\tv_mul_u32_u24 v7, 9216, v7\n\tv_lshl_add_u32 v6, v6, 8, v7\n\tv_lshlrev_b32 v7, 3, v244\n\tv_lshl_add_u32 v7, v245, 12, v7\n\t" \
    "s_and_b32 s65, s40, 7\n\ts_lshr_b32 s73, s65, 1\n\ts_mul_i32 s66, s73, 2304\n\ts_mul_i32 s69, s57, 9437184\n\ts_add_u32 s66, s66, s69\n\ts_mul_i32 s73, s73, 4718592\n\ts_and_b32 s65, s65, 1\n\ts_lshl_b32 s65, s65, 11\n\t" \
    "s_add_u32 s65, s65, s73\n\ts_waitcnt lgkmcnt(0)\n\ts_add_u32 s12, s12, s66\n\ts_addc_u32 s13, s13, 0\n\ts_sub_u32 s14, s14, s66\n\ts_add_u32 s16, s16, s65\n\ts_addc_u32 s17, s17, 0\n\ts_sub_u32 s18, s18, s65\n\t" \
    "v_mov_b32 v238, 2146435072\n\tv_mov_b32 v236, 2146435072\n\ts_lshl_b32 s69, s57, 7\n\tv_mov_b32 v237, v240\n\tv_mul_u32_u24 v237, 12, v237\n\ts_add_u32 s73, s67, 220\n\tv_add_u32 v237, s73, v237\n\tv_lshrrev_b32 v5, 3, v237\n\t" \
    "v_lshlrev_b32 v5, 6, v5\n\tv_cmp_le_u32 vcc, 144, v237\n\tv_mov_b32 v4, 1120\n\tv_and_b32 v3, 7, v237\n\tv_cndmask_b32 v4, 0, v4, vcc\n\tv_lshl_add_u32 v5, v3, 2, v5\n\tv_sub_u32 v5, v5, v4\n\tv_subrev_u32 v237, 220, v237\n\t" \
    "v_cmp_gt_u32 vcc, 68, v237\n\tv_lshlrev_b32 v5, 12, v5\n\ts_mov_b32 s43, 0\n\tv_cndmask_b32 v237, v238, v5, vcc\n\ts_nop 0\n\tv_readlane_b32 s41, v236, 0\n\tv_readlane_b32 s42, v237, 0\n\ts_nop 4\n\t" \
    "buffer_load_dword v15, v7, s[16:19], s42 offen\n\ts_add_u32 s43, s43, 1\n\tv_readlane_b32 s42, v237, s43\n\ts_nop 4\n\tbuffer_load_dword v15, v7, s[16:19], s42 offen\n\ts_add_u32 s43, s43, 1\n\tv_readlane_b32 s42, v237, s43\n\ts_nop 4\n\t" \
    "buffer_load_dword v15, v7, s[16:19], s42 offen\n\ts_add_u32 s43, s43, 1\n\tv_readlane_b32 s42, v237, s43\n\ts_nop 4\n\tbuffer_load_dword v15, v7, s[16:19], s42 offen\n\ts_add_u32 s43, s43, 1\n\tv_readlane_b32 s42, v237, s43\n\ts_nop 4\n\t" \
    "buffer_load_dword v15, v7, s[16:19], s42 offen\n\ts_add_u32 s43, s43, 1\n\tv_readlane_b32 s42, v237, s43\n\ts_nop 4\n\tbuffer_load_dword v15, v7, s[16:19], s42 offen\n\ts_add_u32 s43, s43, 1\n\tv_readlane_b32 s42, v237, s43\n\ts_nop 4\n\t" \
    "s_waitcnt vmcnt(0)\n\ts_branch 103f\n\t101:\n\ts_load_dwordx8 s[12:19], s[0:1], 16\n\ts_lshr_b32 s67, s40, 3\n\ts_sub_u32 s67, s67, 27\n\ts_lshl_b32 s67, s67, 2\n\ts_add_u32 s67, s67, s38\n\t" \
    "s_lshl_b32 s65, s67, 6\n\tv_add_u32 v6, s65, v240\n\tv_mul_u32_u24 v7, 7282, v6\n\tv_lshrrev_b32 v7, 16, v7\n\tv_mul_u32_u24 v5, 9, v7\n\tv_sub_u32 v6, v6, v5\n\tv_mul_u32_u24 v7, 9216, v7\n\tv_lshl_add_u32 v6, v6, 8, v7\n\t" \
    "v_lshlrev_b32 v7, 3, v244\n\tv_lshl_add_u32 v7, v245, 12, v7\n\ts_and_b32 s65, s40, 7\n\ts_lshr_b32 s73, s65, 1\n\ts_mul_i32 s66, s73, 2304\n\ts_mul_i32 s69, s57, 9437184\n\ts_add_u32 s66, s66, s69\n\ts_mul_i32 s73, s73, 4718592\n\t" \
    "s_and_b32 s65, s65, 1\n\ts_lshl_b32 s65, s65, 11\n\ts_add_u32 s65, s65, s73\n\ts_waitcnt lgkmcnt(0)\n\ts_add_u32 s12, s12, s66\n\ts_addc_u32 s13, s13, 0\n\ts_sub_u32 s14, s14, s66\n\ts_add_u32 s16, s16, s65\n\t" \
    "s_addc_u32 s17, s17, 0\n\ts_sub_u32 s18, s18, s65\n\tv_mov_b32 v238, 2146435072\n\tv_mov_b32 v236, 2146435072\n\ts_lshl_b32 s69, s57, 7\n\tv_mov_b32 v237, v240\n\ts_xor_b32 s73, s69, 6193280\n\tv_writelane_b32 v236, s73, 0\n\t" \
    "v_writelane_b32 v237, 0, 0\n\ts_xor_b32 s73, s69, 7501952\n\tv_writelane_b32 v236, s73, 1\n\tv_writelane_b32 v237, 0, 1\n\ts_xor_b32 s73, s69, 8810624\n\tv_writelane_b32 v236, s73, 2\n\tv_writelane_b32 v237, 0, 2\n\tv_writelane_b32 v237, 6, 3\n\t" \
    "s_xor_b32 s73, s69, 6193152\n\tv_writelane_b32 v236, s73, 4\n\tv_writelane_b32 v237, 0, 4\n\ts_xor_b32 s73, s69, 7501824\n\tv_writelane_b32 v236, s73, 5\n\tv_writelane_b32 v237, 0, 5\n\ts_xor_b32 s73, s69, 8810496\n\tv_writelane_b32 v236, s73, 6\n\t" \
    "v_writelane_b32 v237, 0, 6\n\tv_mul_u32_u24 v237, 20, v237\n\ts_add_u32 s73, s67, -120\n\tv_add_u32 v237, s73, v237\n\tv_cmp_le_i32 vcc, 40, v237\n\tv_add_u32 v5, 48, v237\n\ts_nop 0\n\tv_cndmask_b32 v237, v237, v5, vcc\n\t" \
    "v_lshrrev_b32 v5, 3, v237\n\tv_lshlrev_b32 v5, 6, v5\n\tv_cmp_le_u32 vcc, 144, v237\n\tv_mov_b32 v4, 1120\n\tv_and_b32 v3, 7, v237\n\tv_cndmask_b32 v4, 0, v4, vcc\n\tv_lshl_add_u32 v5, v3, 2, v5\n\tv_sub_u32 v5, v5, v4\n\t" \
    "v_subrev_u32 v237, 0, v237\n\tv_cmp_gt_u32 vcc, 220, v237\n\tv_lshlrev_b32 v5, 12, v5\n\ts_mov_b32 s43, 0\n\tv_cndmask_b32 v237, v238, v5, vcc\n\ts_nop 0\n\tv_readlane_b32 s41, v236, 0\n\tv_readlane_b32 s42, v237, 0\n\t" \
    "s_nop 4\n\ts_sub_u32 s65, s40, 216\n\ts_lshl_b32 s44, s65, 4\n\ts_mul_i32 s66, s65, 12\n\ts_add_u32 s66, s66, 96\n\ts_cmp_lt_u32 s65, 24\n\ts_cselect_b32 s44, s44, s66\n\ts_cselect_b32 s45, 0, 2146435072\n\t" \
    "v_cmp_eq_u32 vcc, 0, v240\n\tv_subrev_u32 v13, 8, v8\n\tv_mov_b32 v12, 2146435072\n\tv_cndmask_b32 v13, v13, v12, vcc\n\ts_mov_b32 s4, s2\n\ts_mov_b32 s5, s3\n\ts_mov_b32 s6, 98f-99b\n\ts_mov_b32 s7, 0x20000\n\t" \
    "v_lshlrev_b32 v14, 7, v240\n\ts_mov_b32 s46, 0xffff\n\tv_and_b32 v204, 15, v240\n\tv_lshlrev_b32 v205, 3, v245\n\tv_sub_u32 v206, v205, v204\n\tv_add_u32 v206, 31, v206\n\tv_and_b32 v207, 3, v206\n\tv_mul_u32_u24 v207, 4160, v207\n\t" \
    "v_and_b32 v206, 0xfc, v206\n\tv_lshl_add_u32 v0, v206, 1, v207\n\ts_mul_i32 s65, s38, 16640\n\ts_add_u32 s65, s65, 73984\n\tv_add_u32 v0, s65, v0\n\ts_add_u32 s66, s65, 1040\n\tv_sub_u32 v2, s66, v8\n\tv_bfe_u32 v206, v240, 1, 2\n\t" \
    "v_mul_u32_u24 v206, 4160, v206\n\tv_and_b32 v207, 16, v8\n\tv_add3_u32 v9, v206, v207, s65\n\ts_mul_i32 s66, s38, 4624\n\tv_lshl_add_u32 v3, v204, 5, v205\n\tv_add_u32 v3, s66, v3\n\tv_add3_u32 v1, v3, v205, 32\n\tv_add_u32 v3, s66, v239\n\t" \
    "s_lshl_b32 s73, s38, 4\n\tv_add_u32 v206, s73, v245\n\tv_bfe_u32 v207, v240, 2, 2\n\tv_mul_u32_u24 v4, 4624, v207\n\tv_lshl_add_u32 v4, v245, 4, v4\n\tv_and_b32 v207, 3, v240\n\tv_lshl_add_u32 v4, v207, 7, v4\n\tv_add_u32 v4, s54, v4\n\t" \
    "v_mul_u32_u24 v5, 12288, v206\n\tv_add_u32 v5, v5, v244\n\tv_lshrrev_b32 v206, 5, v240\n\ts_lshl_b32 s73, s38, 2\n\tv_add_u32 v206, s73, v206\n\tv_mul_u32_u24 v206, 4624, v206\n\tv_and_b32 v207, 0x1f0, v8\n\tv_add_u32 v12, v206, v207\n\t" \
    "v_mul_u32_u24 v10, 9216, v245\n\tv_add_u32 v10, v10, v244\n\tv_mul_u32_u24 v11, 4624, v204\n\tv_lshl_add_u32 v11, v245, 4, v11\n\tv_add_u32 v11, s54, v11\n\ts_mul_i32 s62, s38, 589824\n\ts_lshl_b32 s73, s44, 4\n\ts_add_u32 s62, s62, s73\n\t" \
    "s_add_u32 s62, s62, 2490368\n\tv_lshrrev_b32 v206, 3, v204\n\tv_add_u32 v206, 2, v206\n\tv_cmp_eq_u32 vcc, v206, v245\n\tv_and_b32 v207, 1, v204\n\tv_lshlrev_b32 v207, 4, v207\n\tv_lshlrev_b32 v207, v207, s46\n\tv_cndmask_b32 v207, 0, v207, vcc\n\t" \
    "v_bfe_u32 v206, v204, 1, 2\n\tv_cmp_eq_u32 vcc, 0, v206\n\tv_mov_b32 v196, 0\n\ts_nop 0\n\tv_cndmask_b32 v232, 0, v207, vcc\n\tv_cmp_eq_u32 vcc, 1, v206\n\tv_mov_b32 v197, 0\n\ts_nop 0\n\t" \
    "v_cndmask_b32 v233, 0, v207, vcc\n\tv_cmp_eq_u32 vcc, 2, v206\n\tv_mov_b32 v198, 0\n\ts_nop 0\n\tv_cndmask_b32 v234, 0, v207, vcc\n\tv_cmp_eq_u32 vcc, 3, v206\n\tv_mov_b32 v199, 0\n\ts_nop 0\n\t" \
    "v_cndmask_b32 v235, 0, v207, vcc\n\ts_add_u32 s65, s44, 0\n\ts_lshr_b32 s65, s65, 6\n\ts_cmp_eq_u32 s65, 8\n\ts_cselect_b32 s65, 1, s65\n\ts_and_b32 s65, s65, 3\n\ts_lshl_b32 s47, s65, 11\n\ts_lshr_b32 s65, s54, 1\n\t" \
    "s_add_u32 s47, s47, s65\n\ts_add_u32 s47, s47, 17408\n\ts_add_u32 s65, s44, 11\n\ts_lshr_b32 s65, s65, 6\n\ts_cmp_eq_u32 s65, 8\n\ts_cselect_b32 s65, 1, s65\n\ts_and_b32 s65, s65, 3\n\ts_lshl_b32 s50, s65, 11\n\t" \
    "s_lshr_b32 s65, s54, 1\n\ts_add_u32 s50, s50, s65\n\ts_add_u32 s50, s50, 17408\n\tds_write_b128 v9, v[196:199] offset:4128\n\tbuffer_load_dword v15, v6, s[12:15], s41 offen\n\tbuffer_load_dword v15, v7, s[16:19], s42 offen\n\ts_add_u32 s43, s43, 1\n\tv_readlane_b32 s41, v236, s43\n\t" \
    "v_readlane_b32 s42, v237, s43\n\ts_nop 4\n\ts_waitcnt vmcnt(2)\n\tbuffer_store_dwordx2 v[248:249], off, s[8:11], s48 sc1\n\tv_alignbit_b32 v196, v167, v167, 16\n\tv_alignbit_b32 v197, v166, v166, 16\n\tv_alignbit_b32 v198, v165, v165, 16\n\tv_alignbit_b32 v199, v164, v164, 16\n\t" \
    "ds_write_b128 v2, v[196:199] offset:3072\n\tv_bfi_b32 v200, s46, v167, v166\n\tv_bfi_b32 v201, s46, v166, v165\n\tv_bfi_b32 v202, s46, v165, v164\n\tv_bfi_b32 v203, s46, v164, v181\n\tds_write_b128 v2, v[200:203] offset:7232\n\tv_alignbit_b32 v196, v166, v166, 16\n\tv_alignbit_b32 v197, v165, v165, 16\n\t" \
    "v_alignbit_b32 v198, v164, v164, 16\n\tv_alignbit_b32 v199, v181, v181, 16\n\tds_write_b128 v2, v[196:199] offset:11392\n\tv_bfi_b32 v200, s46, v166, v165\n\tv_bfi_b32 v201, s46, v165, v164\n\tv_bfi_b32 v202, s46, v164, v181\n\tv_bfi_b32 v203, s46, v181, v180\n\tds_write_b128 v2, v[200:203] offset:15552\n\t" \
    "v_alignbit_b32 v196, v171, v171, 16\n\tv_alignbit_b32 v197, v170, v170, 16\n\tv_alignbit_b32 v198, v169, v169, 16\n\tv_alignbit_b32 v199, v168, v168, 16\n\tds_write_b128 v2, v[196:199] offset:2048\n\tv_bfi_b32 v200, s46, v171, v170\n\tv_bfi_b32 v201, s46, v170, v169\n\tv_bfi_b32 v202, s46, v169, v168\n\t" \
    "v_bfi_b32 v203, s46, v168, v183\n\tds_write_b128 v2, v[200:203] offset:6208\n\tv_alignbit_b32 v196, v170, v170, 16\n\tv_alignbit_b32 v197, v169, v169, 16\n\tv_alignbit_b32 v198, v168, v168, 16\n\tv_alignbit_b32 v199, v183, v183, 16\n\tds_write_b128 v2, v[196:199] offset:10368\n\tv_bfi_b32 v200, s46, v170, v169\n\t" \
    "v_bfi_b32 v201, s46, v169, v168\n\tv_bfi_b32 v202, s46, v168, v183\n\tv_bfi_b32 v203, s46, v183, v182\n\tds_write_b128 v2, v[200:203] offset:14528\n\tv_alignbit_b32 v196, v175, v175, 16\n\tv_alignbit_b32 v197, v174, v174, 16\n\tv_alignbit_b32 v198, v173, v173, 16\n\tv_alignbit_b32 v199, v172, v172, 16\n\t" \
    "ds_write_b128 v2, v[196:199] offset:1024\n\tv_bfi_b32 v200, s46, v175, v174\n\tv_bfi_b32 v201, s46, v174, v173\n\tv_bfi_b32 v202, s46, v173, v172\n\tv_bfi_b32 v203, s46, v172, v185\n\tds_write_b128 v2, v[200:203] offset:5184\n\tv_alignbit_b32 v196, v174, v174, 16\n\tv_alignbit_b32 v197, v173, v173, 16\n\t" \
    "v_alignbit_b32 v198, v172, v172, 16\n\tv_alignbit_b32 v199, v185, v185, 16\n\tds_write_b128 v2, v[196:199] offset:9344\n\tv_bfi_b32 v200, s46, v174, v173\n\tv_bfi_b32 v201, s46, v173, v172\n\tv_bfi_b32 v202, s46, v172, v185\n\tv_bfi_b32 v203, s46, v185, v184\n\tds_write_b128 v2, v[200:203] offset:13504\n\t" \
    "v_alignbit_b32 v196, v179, v179, 16\n\tv_alignbit_b32 v197, v178, v178, 16\n\tv_alignbit_b32 v198, v177, v177, 16\n\tv_alignbit_b32 v199, v176, v176, 16\n\tds_write_b128 v2, v[196:199] offset:0\n\tv_bfi_b32 v200, s46, v179, v178\n\tv_bfi_b32 v201, s46, v178, v177\n\tv_bfi_b32 v202, s46, v177, v176\n\t" \
    "v_bfi_b32 v203, s46, v176, v187\n\tds_write_b128 v2, v[200:203] offset:4160\n\tv_alignbit_b32 v196, v178, v178, 16\n\tv_alignbit_b32 v197, v177, v177, 16\n\tv_alignbit_b32 v198, v176, v176, 16\n\tv_alignbit_b32 v199, v187, v187, 16\n\tds_write_b128 v2, v[196:199] offset:8320\n\tv_bfi_b32 v200, s46, v178, v177\n\t" \
    "v_bfi_b32 v201, s46, v177, v176\n\tv_bfi_b32 v202, s46, v176, v187\n\tv_bfi_b32 v203, s46, v187, v186\n\tds_write_b128 v2, v[200:203] offset:12480\n\tv_lshl_or_b32 v188, v188, 16, v188\n\tv_and_b32 v228, v188, v232\n\tv_and_b32 v229, v188, v233\n\tv_and_b32 v230, v188, v234\n\t" \
    "v_and_b32 v231, v188, v235\n\tds_read_b64 a[0:1], v0 offset:4032\n\tds_read_b64 a[2:3], v0 offset:4040\n\tds_read_b64 a[4:5], v0 offset:3520\n\tds_read_b64 a[6:7], v0 offset:3528\n\tds_read_b64 a[8:9], v0 offset:3008\n\tds_read_b64 a[10:11], v0 offset:3016\n\tds_read_b64 a[12:13], v0 offset:2496\n\t" \
    "ds_read_b64 a[14:15], v0 offset:2504\n\tds_read_b64 a[16:17], v0 offset:1984\n\tds_read_b64 a[18:19], v0 offset:1992\n\tds_read_b64 a[20:21], v0 offset:1472\n\tds_read_b64 a[22:23], v0 offset:1480\n\tds_read_b64 a[24:25], v0 offset:960\n\tds_read_b64 a[26:27], v0 offset:968\n\tds_read_b64 a[28:29], v0 offset:448\n\t" \
    "ds_read_b64 a[30:31], v0 offset:456\n\ts_barrier\n\tv_mov_b32 v196, 0\n\tv_mov_b32 v197, 0\n\tv_mov_b32 v198, 0\n\tv_mov_b32 v199, 0\n\tds_write_b128 v12, v[196:199]\n\tds_write_b128 v12, v[196:199] offset:9248\n\t" \
    "s_sleep 30\n\ts_mov_b32 s71, 1048576\n\t125:\n\tbuffer_load_dwordx2 v[250:251], v239, s[8:11], s47 offen sc1\n\tbuffer_load_dwordx2 v[252:253], v239, s[8:11], s50 offen sc1\n\ts_waitcnt vmcnt(0)\n\tv_cmp_ne_u64 vcc, s[36:37], v[250:251]\n\tv_cmp_ne_u64 s[74:75], s[36:37], v[252:253]\n\t" \
    "s_or_b64 s[74:75], s[74:75], vcc\n\ts_cbranch_scc0 126f\n\ts_sleep 16\n\ts_sub_u32 s71, s71, 1\n\ts_cbranch_scc0 125b\n\t126:\n\tbuffer_load_dwordx4 v[68:71], v10, s[8:11], s62 offen sc1\n\ts_add_u32 s62, s62, 36864\n\t" \
    "buffer_load_dwordx4 v[72:75], v10, s[8:11], s62 offen sc1\n\ts_add_u32 s62, s62, 36864\n\tbuffer_load_dwordx4 v[76:79], v10, s[8:11], s62 offen sc1\n\ts_add_u32 s62, s62, 36864\n\tbuffer_load_dwordx4 v[80:83], v10, s[8:11], s62 offen sc1\n\ts_add_u32 s62, s62, 36864\n\tbuffer_load_dwordx4 v[84:87], v10, s[8:11], s62 offen sc1\n\ts_add_u32 s62, s62, 36864\n\t" \
    "buffer_load_dwordx4 v[88:91], v10, s[8:11], s62 offen sc1\n\ts_add_u32 s62, s62, 36864\n\tbuffer_load_dwordx4 v[92:95], v10, s[8:11], s62 offen sc1\n\ts_add_u32 s62, s62, 36864\n\tbuffer_load_dwordx4 v[96:99], v10, s[8:11], s62 offen sc1\n\ts_add_u32 s62, s62, 36864\n\tbuffer_load_dwordx4 v[100:103], v10, s[8:11], s62 offen sc1\n\ts_add_u32 s62, s62, 36864\n\t" \
    "buffer_load_dwordx4 v[104:107], v10, s[8:11], s62 offen sc1\n\ts_add_u32 s62, s62, 36864\n\tbuffer_load_dwordx4 v[108:111], v10, s[8:11], s62 offen sc1\n\ts_add_u32 s62, s62, 36864\n\tbuffer_load_dwordx4 v[112:115], v10, s[8:11], s62 offen sc1\n\ts_add_u32 s62, s62, 36864\n\tbuffer_load_dwordx4 v[116:119], v10, s[8:11], s62 offen sc1\n\ts_add_u32 s62, s62, 36864\n\t" \
    "buffer_load_dwordx4 v[120:123], v10, s[8:11], s62 offen sc1\n\ts_add_u32 s62, s62, 36864\n\tbuffer_load_dwordx4 v[124:127], v10, s[8:11], s62 offen sc1\n\ts_add_u32 s62, s62, 36864\n\tbuffer_load_dwordx4 v[128:131], v10, s[8:11], s62 offen sc1\n\ts_add_u32 s62, s62, 36864\n\ts_waitcnt vmcnt(15)\n\tds_write_b128 v11, v[68:71] offset:512\n\t" \
    "s_waitcnt vmcnt(14)\n\tds_write_b128 v11, v[72:75] offset:576\n\ts_waitcnt vmcnt(13)\n\tds_write_b128 v11, v[76:79] offset:640\n\ts_waitcnt vmcnt(12)\n\tds_write_b128 v11, v[80:83] offset:704\n\ts_waitcnt vmcnt(11)\n\tds_write_b128 v11, v[84:87] offset:768\n\t" \
    "s_waitcnt vmcnt(10)\n\tds_write_b128 v11, v[88:91] offset:832\n\ts_waitcnt vmcnt(9)\n\tds_write_b128 v11, v[92:95] offset:896\n\ts_waitcnt vmcnt(8)\n\tds_write_b128 v11, v[96:99] offset:960\n\ts_waitcnt vmcnt(7)\n\tds_write_b128 v11, v[100:103] offset:1024\n\t" \
    "s_waitcnt vmcnt(6)\n\tds_write_b128 v11, v[104:107] offset:1088\n\ts_waitcnt vmcnt(5)\n\tds_write_b128 v11, v[108:111] offset:1152\n\ts_waitcnt vmcnt(4)\n\tds_write_b128 v11, v[112:115] offset:1216\n\ts_waitcnt vmcnt(3)\n\tds_write_b128 v11, v[116:119] offset:1280\n\t" \
    "s_waitcnt vmcnt(2)\n\tds_write_b128 v11, v[120:123] offset:1344\n\ts_waitcnt vmcnt(1)\n\tds_write_b128 v11, v[124:127] offset:1408\n\ts_waitcnt vmcnt(0)\n\tds_write_b128 v11, v[128:131] offset:1472\n\ts_waitcnt lgkmcnt(0)\n\ts_barrier\n\t" \
    "ds_read_b128 v[68:71], v1 offset:448\n\tds_read_b128 v[72:75], v1 offset:960\n\tds_read_b128 v[76:79], v1 offset:1472\n\tds_read_b128 v[80:83], v1 offset:1984\n\tds_read_b128 v[84:87], v1 offset:2496\n\tds_read_b128 v[88:91], v1 offset:3008\n\tds_read_b128 v[92:95], v1 offset:3520\n\tds_read_b128 v[96:99], v1 offset:4032\n\t" \
    "s_add_u32 s68, s44, s38\n\ts_add_u32 s68, s68, 4\n\ts_lshl_b32 s66, s68, 1\n\ts_lshl_b32 s68, s68, 12\n\tbuffer_load_dwordx4 v[42:45], v8, s[76:79], s68 offen offset:0\n\tbuffer_load_dwordx4 v[46:49], v8, s[76:79], s68 offen offset:1024\n\tbuffer_load_dwordx4 v[50:53], v8, s[76:79], s68 offen offset:2048\n\tbuffer_load_dwordx4 v[54:57], v8, s[76:79], s68 offen offset:3072\n\t" \
    "buffer_load_dwordx2 v[58:59], v13, s[76:79], s68 offen\n\tbuffer_load_dwordx2 v[60:61], v8, s[76:79], s68 offen offset:1016\n\tbuffer_load_dwordx2 v[62:63], v8, s[76:79], s68 offen offset:2040\n\tbuffer_load_dwordx2 v[64:65], v8, s[76:79], s68 offen offset:3064\n\tbuffer_load_ushort v66, off, s[80:83], s66\n\tbuffer_load_dword v15, v6, s[12:15], s41 offen\n\tbuffer_load_dword v15, v7, s[16:19], s42 offen\n\ts_add_u32 s43, s43, 1\n\t" \
    "v_readlane_b32 s41, v236, s43\n\tv_readlane_b32 s42, v237, s43\n\ts_nop 4\n\ts_waitcnt lgkmcnt(0)\n\tv_mfma_f32_16x16x32_bf16 v[132:135], v[228:231], v[68:71], 0\n\tds_read_b64 a[32:33], v0 offset:3968\n\tds_read_b64 a[34:35], v0 offset:3976\n\tv_mfma_f32_16x16x32_bf16 v[136:139], v[228:231], v[72:75], 0\n\t" \
    "ds_read_b64 a[36:37], v0 offset:3456\n\tds_read_b64 a[38:39], v0 offset:3464\n\tv_mfma_f32_16x16x32_bf16 v[140:143], v[228:231], v[76:79], 0\n\tds_read_b64 a[40:41], v0 offset:2944\n\tds_read_b64 a[42:43], v0 offset:2952\n\tv_mfma_f32_16x16x32_bf16 v[144:147], v[228:231], v[80:83], 0\n\tds_read_b64 a[44:45], v0 offset:2432\n\tds_read_b64 a[46:47], v0 offset:2440\n\t" \
    "v_mfma_f32_16x16x32_bf16 v[148:151], v[228:231], v[84:87], 0\n\tds_read_b64 a[48:49], v0 offset:1920\n\tds_read_b64 a[50:51], v0 offset:1928\n\tv_mfma_f32_16x16x32_bf16 v[152:155], v[228:231], v[88:91], 0\n\tds_read_b64 a[52:53], v0 offset:1408\n\tv_mfma_f32_16x16x32_bf16 v[156:159], v[228:231], v[92:95], 0\n\tds_read_b64 a[54:55], v0 offset:1416\n\tv_mfma_f32_16x16x32_bf16 v[160:163], v[228:231], v[96:99], 0\n\t" \
    "ds_read_b64 a[56:57], v0 offset:896\n\tv_mfma_f32_16x16x32_bf16 v[132:135], a[0:3], v[68:71], v[132:135]\n\tds_read_b64 a[58:59], v0 offset:904\n\tv_mfma_f32_16x16x32_bf16 v[136:139], a[0:3], v[72:75], v[136:139]\n\tds_read_b64 a[60:61], v0 offset:384\n\tv_mfma_f32_16x16x32_bf16 v[140:143], a[0:3], v[76:79], v[140:143]\n\tds_read_b64 a[62:63], v0 offset:392\n\tv_mfma_f32_16x16x32_bf16 v[144:147], a[0:3], v[80:83], v[144:147]\n\t" \
    "ds_read_b128 v[100:103], v1 offset:384\n\tv_mfma_f32_16x16x32_bf16 v[148:151], a[0:3], v[84:87], v[148:151]\n\tds_read_b128 v[104:107], v1 offset:896\n\tv_mfma_f32_16x16x32_bf16 v[152:155], a[0:3], v[88:91], v[152:155]\n\tds_read_b128 v[108:111], v1 offset:1408\n\tv_mfma_f32_16x16x32_bf16 v[156:159], a[0:3], v[92:95], v[156:159]\n\tds_read_b128 v[112:115], v1 offset:1920\n\tv_mfma_f32_16x16x32_bf16 v[160:163], a[0:3], v[96:99], v[160:163]\n\t" \
    "ds_read_b128 v[116:119], v1 offset:2432\n\tv_mfma_f32_16x16x32_bf16 v[136:139], a[4:7], v[68:71], v[136:139]\n\tds_read_b128 v[120:123], v1 offset:2944\n\tv_mfma_f32_16x16x32_bf16 v[140:143], a[4:7], v[72:75], v[140:143]\n\tbuffer_load_dword v15, v6, s[12:15], s41 offen\n\tds_read_b128 v[124:127], v1 offset:3456\n\tv_mfma_f32_16x16x32_bf16 v[144:147], a[4:7], v[76:79], v[144:147]\n\tbuffer_load_dword v15, v7, s[16:19], s42 offen\n\t" \
    "ds_read_b128 v[128:131], v1 offset:3968\n\tv_mfma_f32_16x16x32_bf16 v[148:151], a[4:7], v[80:83], v[148:151]\n\ts_add_u32 s43, s43, 1\n\tds_read_b64 a[64:65], v0 offset:3904\n\tv_mfma_f32_16x16x32_bf16 v[152:155], a[4:7], v[84:87], v[152:155]\n\tv_readlane_b32 s41, v236, s43\n\tv_readlane_b32 s42, v237, s43\n\ts_nop 4\n\t" \
    "ds_read_b64 a[66:67], v0 offset:3912\n\tv_mfma_f32_16x16x32_bf16 v[156:159], a[4:7], v[88:91], v[156:159]\n\tds_read_b64 a[68:69], v0 offset:3392\n\tv_mfma_f32_16x16x32_bf16 v[160:163], a[4:7], v[92:95], v[160:163]\n\tds_read_b64 a[70:71], v0 offset:3400\n\tv_mfma_f32_16x16x32_bf16 v[140:143], a[8:11], v[68:71], v[140:143]\n\tds_read_b64 a[72:73], v0 offset:2880\n\tv_mfma_f32_16x16x32_bf16 v[144:147], a[8:11], v[72:75], v[144:147]\n\t" \
    "ds_read_b64 a[74:75], v0 offset:2888\n\tv_mfma_f32_16x16x32_bf16 v[148:151], a[8:11], v[76:79], v[148:151]\n\tds_read_b64 a[76:77], v0 offset:2368\n\tv_mfma_f32_16x16x32_bf16 v[152:155], a[8:11], v[80:83], v[152:155]\n\tds_read_b64 a[78:79], v0 offset:2376\n\tv_mfma_f32_16x16x32_bf16 v[156:159], a[8:11], v[84:87], v[156:159]\n\tds_read_b64 a[80:81], v0 offset:1856\n\tv_mfma_f32_16x16x32_bf16 v[160:163], a[8:11], v[88:91], v[160:163]\n\t" \
    "ds_read_b64 a[82:83], v0 offset:1864\n\tv_mfma_f32_16x16x32_bf16 v[144:147], a[12:15], v[68:71], v[144:147]\n\tds_read_b64 a[84:85], v0 offset:1344\n\tv_mfma_f32_16x16x32_bf16 v[148:151], a[12:15], v[72:75], v[148:151]\n\tds_read_b64 a[86:87], v0 offset:1352\n\tv_mfma_f32_16x16x32_bf16 v[152:155], a[12:15], v[76:79], v[152:155]\n\tds_read_b64 a[88:89], v0 offset:832\n\tv_mfma_f32_16x16x32_bf16 v[156:159], a[12:15], v[80:83], v[156:159]\n\t" \
    "ds_read_b64 a[90:91], v0 offset:840\n\tv_mfma_f32_16x16x32_bf16 v[160:163], a[12:15], v[84:87], v[160:163]\n\tds_read_b64 a[92:93], v0 offset:320\n\tv_mfma_f32_16x16x32_bf16 v[148:151], a[16:19], v[68:71], v[148:151]\n\tds_read_b64 a[94:95], v0 offset:328\n\tv_mfma_f32_16x16x32_bf16 v[152:155], a[16:19], v[72:75], v[152:155]\n\tds_read_b64 a[96:97], v0 offset:3840\n\tv_mfma_f32_16x16x32_bf16 v[156:159], a[16:19], v[76:79], v[156:159]\n\t" \
    "ds_read_b64 a[98:99], v0 offset:3848\n\tv_mfma_f32_16x16x32_bf16 v[160:163], a[16:19], v[80:83], v[160:163]\n\tds_read_b64 a[100:101], v0 offset:3328\n\tv_mfma_f32_16x16x32_bf16 v[152:155], a[20:23], v[68:71], v[152:155]\n\tds_read_b64 a[102:103], v0 offset:3336\n\tv_mfma_f32_16x16x32_bf16 v[156:159], a[20:23], v[72:75], v[156:159]\n\tv_mfma_f32_16x16x32_bf16 v[160:163], a[20:23], v[76:79], v[160:163]\n\tv_mfma_f32_16x16x32_bf16 v[156:159], a[24:27], v[68:71], v[156:159]\n\t" \
    "v_mfma_f32_16x16x32_bf16 v[160:163], a[24:27], v[72:75], v[160:163]\n\tv_mfma_f32_16x16x32_bf16 v[160:163], a[28:31], v[68:71], v[160:163]\n\ts_waitcnt lgkmcnt(15)\n\tv_mfma_f32_16x16x32_bf16 v[132:135], a[32:35], v[100:103], v[132:135]\n\tds_read_b64 a[104:105], v0 offset:2816\n\tds_read_b64 a[106:107], v0 offset:2824\n\tv_mfma_f32_16x16x32_bf16 v[136:139], a[32:35], v[104:107], v[136:139]\n\tds_read_b64 a[108:109], v0 offset:2304\n\t" \
    "ds_read_b64 a[110:111], v0 offset:2312\n\tv_mfma_f32_16x16x32_bf16 v[140:143], a[32:35], v[108:111], v[140:143]\n\tds_read_b64 a[112:113], v0 offset:1792\n\tds_read_b64 a[114:115], v0 offset:1800\n\tv_mfma_f32_16x16x32_bf16 v[144:147], a[32:35], v[112:115], v[144:147]\n\tds_read_b64 a[116:117], v0 offset:1280\n\tds_read_b64 a[118:119], v0 offset:1288\n\tv_mfma_f32_16x16x32_bf16 v[148:151], a[32:35], v[116:119], v[148:151]\n\t" \
    "ds_read_b64 a[120:121], v0 offset:768\n\tds_read_b64 a[122:123], v0 offset:776\n\tv_mfma_f32_16x16x32_bf16 v[152:155], a[32:35], v[120:123], v[152:155]\n\tds_read_b64 a[124:125], v0 offset:256\n\tv_mfma_f32_16x16x32_bf16 v[156:159], a[32:35], v[124:127], v[156:159]\n\tds_read_b64 a[126:127], v0 offset:264\n\tv_mfma_f32_16x16x32_bf16 v[160:163], a[32:35], v[128:131], v[160:163]\n\tds_read_b64 a[128:129], v0 offset:3776\n\t" \
    "v_mfma_f32_16x16x32_bf16 v[136:139], a[36:39], v[100:103], v[136:139]\n\tds_read_b64 a[130:131], v0 offset:3784\n\tv_mfma_f32_16x16x32_bf16 v[140:143], a[36:39], v[104:107], v[140:143]\n\tds_read_b64 a[132:133], v0 offset:3264\n\tv_mfma_f32_16x16x32_bf16 v[144:147], a[36:39], v[108:111], v[144:147]\n\tds_read_b64 a[134:135], v0 offset:3272\n\tv_mfma_f32_16x16x32_bf16 v[148:151], a[36:39], v[112:115], v[148:151]\n\tds_read_b128 v[68:71], v1 offset:320\n\t" \
    "v_mfma_f32_16x16x32_bf16 v[152:155], a[36:39], v[116:119], v[152:155]\n\tds_read_b128 v[72:75], v1 offset:832\n\tv_mfma_f32_16x16x32_bf16 v[156:159], a[36:39], v[120:123], v[156:159]\n\tds_read_b128 v[76:79], v1 offset:1344\n\tv_mfma_f32_16x16x32_bf16 v[160:163], a[36:39], v[124:127], v[160:163]\n\tds_read_b128 v[80:83], v1 offset:1856\n\tv_mfma_f32_16x16x32_bf16 v[140:143], a[40:43], v[100:103], v[140:143]\n\tds_read_b128 v[84:87], v1 offset:2368\n\t" \
    "v_mfma_f32_16x16x32_bf16 v[144:147], a[40:43], v[104:107], v[144:147]\n\tds_read_b128 v[88:91], v1 offset:2880\n\tv_mfma_f32_16x16x32_bf16 v[148:151], a[40:43], v[108:111], v[148:151]\n\tds_read_b128 v[92:95], v1 offset:3392\n\tv_mfma_f32_16x16x32_bf16 v[152:155], a[40:43], v[112:115], v[152:155]\n\tds_read_b128 v[96:99], v1 offset:3904\n\tv_mfma_f32_16x16x32_bf16 v[156:159], a[40:43], v[116:119], v[156:159]\n\tds_read_b64 a[136:137], v0 offset:2752\n\t" \
    "v_mfma_f32_16x16x32_bf16 v[160:163], a[40:43], v[120:123], v[160:163]\n\tds_read_b64 a[138:139], v0 offset:2760\n\tv_mfma_f32_16x16x32_bf16 v[144:147], a[44:47], v[100:103], v[144:147]\n\tds_read_b64 a[140:141], v0 offset:2240\n\tv_mfma_f32_16x16x32_bf16 v[148:151], a[44:47], v[104:107], v[148:151]\n\tds_read_b64 a[142:143], v0 offset:2248\n\tv_mfma_f32_16x16x32_bf16 v[152:155], a[44:47], v[108:111], v[152:155]\n\tds_read_b64 a[144:145], v0 offset:1728\n\t" \
    "v_mfma_f32_16x16x32_bf16 v[156:159], a[44:47], v[112:115], v[156:159]\n\tds_read_b64 a[146:147], v0 offset:1736\n\tv_mfma_f32_16x16x32_bf16 v[160:163], a[44:47], v[116:119], v[160:163]\n\tds_read_b64 a[148:149], v0 offset:1216\n\tv_mfma_f32_16x16x32_bf16 v[148:151], a[48:51], v[100:103], v[148:151]\n\tds_read_b64 a[150:151], v0 offset:1224\n\tv_mfma_f32_16x16x32_bf16 v[152:155], a[48:51], v[104:107], v[152:155]\n\tds_read_b64 a[152:153], v0 offset:704\n\t" \
    "v_mfma_f32_16x16x32_bf16 v[156:159], a[48:51], v[108:111], v[156:159]\n\tds_read_b64 a[154:155], v0 offset:712\n\tv_mfma_f32_16x16x32_bf16 v[160:163], a[48:51], v[112:115], v[160:163]\n\tds_read_b64 a[156:157], v0 offset:192\n\tv_mfma_f32_16x16x32_bf16 v[152:155], a[52:55], v[100:103], v[152:155]\n\tds_read_b64 a[158:159], v0 offset:200\n\tv_mfma_f32_16x16x32_bf16 v[156:159], a[52:55], v[104:107], v[156:159]\n\tv_mfma_f32_16x16x32_bf16 v[160:163], a[52:55], v[108:111], v[160:163]\n\t" \
    "buffer_load_dword v15, v6, s[12:15], s41 offen\n\tv_mfma_f32_16x16x32_bf16 v[156:159], a[56:59], v[100:103], v[156:159]\n\tbuffer_load_dword v15, v7, s[16:19], s42 offen\n\tv_mfma_f32_16x16x32_bf16 v[160:163], a[56:59], v[104:107], v[160:163]\n\ts_add_u32 s43, s43, 1\n\tv_mfma_f32_16x16x32_bf16 v[160:163], a[60:63], v[100:103], v[160:163]\n\tv_readlane_b32 s41, v236, s43\n\tv_readlane_b32 s42, v237, s43\n\t" \
    "s_nop 4\n\ts_waitcnt lgkmcnt(12)\n\tv_mfma_f32_16x16x32_bf16 v[132:135], a[64:67], v[68:71], v[132:135]\n\tds_read_b64 a[160:161], v0 offset:3712\n\tds_read_b64 a[162:163], v0 offset:3720\n\tv_mfma_f32_16x16x32_bf16 v[136:139], a[64:67], v[72:75], v[136:139]\n\tds_read_b64 a[164:165], v0 offset:3200\n\tds_read_b64 a[166:167], v0 offset:3208\n\t" \
    "v_mfma_f32_16x16x32_bf16 v[140:143], a[64:67], v[76:79], v[140:143]\n\tds_read_b64 a[168:169], v0 offset:2688\n\tds_read_b64 a[170:171], v0 offset:2696\n\tv_mfma_f32_16x16x32_bf16 v[144:147], a[64:67], v[80:83], v[144:147]\n\tds_read_b64 a[172:173], v0 offset:2176\n\tds_read_b64 a[174:175], v0 offset:2184\n\tv_mfma_f32_16x16x32_bf16 v[148:151], a[64:67], v[84:87], v[148:151]\n\tds_read_b64 a[176:177], v0 offset:1664\n\t" \
    "ds_read_b64 a[178:179], v0 offset:1672\n\tv_mfma_f32_16x16x32_bf16 v[152:155], a[64:67], v[88:91], v[152:155]\n\tds_read_b64 a[180:181], v0 offset:1152\n\tv_mfma_f32_16x16x32_bf16 v[156:159], a[64:67], v[92:95], v[156:159]\n\tds_read_b64 a[182:183], v0 offset:1160\n\tv_mfma_f32_16x16x32_bf16 v[160:163], a[64:67], v[96:99], v[160:163]\n\tds_read_b64 a[184:185], v0 offset:640\n\tv_mfma_f32_16x16x32_bf16 v[136:139], a[68:71], v[68:71], v[136:139]\n\t" \
    "ds_read_b64 a[186:187], v0 offset:648\n\tv_mfma_f32_16x16x32_bf16 v[140:143], a[68:71], v[72:75], v[140:143]\n\tds_read_b64 a[188:189], v0 offset:128\n\tv_mfma_f32_16x16x32_bf16 v[144:147], a[68:71], v[76:79], v[144:147]\n\tds_read_b64 a[190:191], v0 offset:136\n\tv_mfma_f32_16x16x32_bf16 v[148:151], a[68:71], v[80:83], v[148:151]\n\tds_read_b128 v[100:103], v1 offset:256\n\tv_mfma_f32_16x16x32_bf16 v[152:155], a[68:71], v[84:87], v[152:155]\n\t" \
    "ds_read_b128 v[104:107], v1 offset:768\n\tv_mfma_f32_16x16x32_bf16 v[156:159], a[68:71], v[88:91], v[156:159]\n\tds_read_b128 v[108:111], v1 offset:1280\n\tv_mfma_f32_16x16x32_bf16 v[160:163], a[68:71], v[92:95], v[160:163]\n\tds_read_b128 v[112:115], v1 offset:1792\n\tv_mfma_f32_16x16x32_bf16 v[140:143], a[72:75], v[68:71], v[140:143]\n\tds_read_b128 v[116:119], v1 offset:2304\n\tv_mfma_f32_16x16x32_bf16 v[144:147], a[72:75], v[72:75], v[144:147]\n\t" \
    "ds_read_b128 v[120:123], v1 offset:2816\n\tv_mfma_f32_16x16x32_bf16 v[148:151], a[72:75], v[76:79], v[148:151]\n\tds_read_b128 v[124:127], v1 offset:3328\n\tv_mfma_f32_16x16x32_bf16 v[152:155], a[72:75], v[80:83], v[152:155]\n\tds_read_b128 v[128:131], v1 offset:3840\n\tv_mfma_f32_16x16x32_bf16 v[156:159], a[72:75], v[84:87], v[156:159]\n\tds_read_b64 a[192:193], v0 offset:3648\n\tv_mfma_f32_16x16x32_bf16 v[160:163], a[72:75], v[88:91], v[160:163]\n\t" \
    "ds_read_b64 a[194:195], v0 offset:3656\n\tv_mfma_f32_16x16x32_bf16 v[144:147], a[76:79], v[68:71], v[144:147]\n\tds_read_b64 a[196:197], v0 offset:3136\n\tv_mfma_f32_16x16x32_bf16 v[148:151], a[76:79], v[72:75], v[148:151]\n\tds_read_b64 a[198:199], v0 offset:3144\n\tv_mfma_f32_16x16x32_bf16 v[152:155], a[76:79], v[76:79], v[152:155]\n\tds_read_b64 a[200:201], v0 offset:2624\n\tv_mfma_f32_16x16x32_bf16 v[156:159], a[76:79], v[80:83], v[156:159]\n\t" \
    "ds_read_b64 a[202:203], v0 offset:2632\n\tv_mfma_f32_16x16x32_bf16 v[160:163], a[76:79], v[84:87], v[160:163]\n\tds_read_b64 a[204:205], v0 offset:2112\n\tv_mfma_f32_16x16x32_bf16 v[148:151], a[80:83], v[68:71], v[148:151]\n\tds_read_b64 a[206:207], v0 offset:2120\n\tv_mfma_f32_16x16x32_bf16 v[152:155], a[80:83], v[72:75], v[152:155]\n\tds_read_b64 a[208:209], v0 offset:1600\n\tv_mfma_f32_16x16x32_bf16 v[156:159], a[80:83], v[76:79], v[156:159]\n\t" \
    "ds_read_b64 a[210:211], v0 offset:1608\n\tv_mfma_f32_16x16x32_bf16 v[160:163], a[80:83], v[80:83], v[160:163]\n\tds_read_b64 a[212:213], v0 offset:1088\n\tv_mfma_f32_16x16x32_bf16 v[152:155], a[84:87], v[68:71], v[152:155]\n\tds_read_b64 a[214:215], v0 offset:1096\n\tv_mfma_f32_16x16x32_bf16 v[156:159], a[84:87], v[72:75], v[156:159]\n\tv_mfma_f32_16x16x32_bf16 v[160:163], a[84:87], v[76:79], v[160:163]\n\tv_mfma_f32_16x16x32_bf16 v[156:159], a[88:91], v[68:71], v[156:159]\n\t" \
    "v_mfma_f32_16x16x32_bf16 v[160:163], a[88:91], v[72:75], v[160:163]\n\tv_mfma_f32_16x16x32_bf16 v[160:163], a[92:95], v[68:71], v[160:163]\n\ts_waitcnt lgkmcnt(12)\n\tv_mfma_f32_16x16x32_bf16 v[132:135], a[96:99], v[100:103], v[132:135]\n\tds_read_b64 a[216:217], v0 offset:576\n\tv_mfma_f32_16x16x32_bf16 v[136:139], a[96:99], v[104:107], v[136:139]\n\tds_read_b64 a[218:219], v0 offset:584\n\tv_mfma_f32_16x16x32_bf16 v[140:143], a[96:99], v[108:111], v[140:143]\n\t" \
    "ds_read_b64 a[220:221], v0 offset:64\n\tv_mfma_f32_16x16x32_bf16 v[144:147], a[96:99], v[112:115], v[144:147]\n\tds_read_b64 a[222:223], v0 offset:72\n\tv_mfma_f32_16x16x32_bf16 v[148:151], a[96:99], v[116:119], v[148:151]\n\tds_read_b64 a[224:225], v0 offset:3584\n\tv_mfma_f32_16x16x32_bf16 v[152:155], a[96:99], v[120:123], v[152:155]\n\tds_read_b64 a[226:227], v0 offset:3592\n\tv_mfma_f32_16x16x32_bf16 v[156:159], a[96:99], v[124:127], v[156:159]\n\t" \
    "ds_read_b64 a[228:229], v0 offset:3072\n\tv_mfma_f32_16x16x32_bf16 v[160:163], a[96:99], v[128:131], v[160:163]\n\tds_read_b64 a[230:231], v0 offset:3080\n\tv_mfma_f32_16x16x32_bf16 v[136:139], a[100:103], v[100:103], v[136:139]\n\tds_read_b64 a[232:233], v0 offset:2560\n\tv_mfma_f32_16x16x32_bf16 v[140:143], a[100:103], v[104:107], v[140:143]\n\tds_read_b64 a[234:235], v0 offset:2568\n\tv_mfma_f32_16x16x32_bf16 v[144:147], a[100:103], v[108:111], v[144:147]\n\t" \
    "ds_read_b64 a[236:237], v0 offset:2048\n\tv_mfma_f32_16x16x32_bf16 v[148:151], a[100:103], v[112:115], v[148:151]\n\tds_read_b64 a[238:239], v0 offset:2056\n\tv_mfma_f32_16x16x32_bf16 v[152:155], a[100:103], v[116:119], v[152:155]\n\tds_read_b64 a[240:241], v0 offset:1536\n\tv_mfma_f32_16x16x32_bf16 v[156:159], a[100:103], v[120:123], v[156:159]\n\tds_read_b64 a[242:243], v0 offset:1544\n\tv_mfma_f32_16x16x32_bf16 v[160:163], a[100:103], v[124:127], v[160:163]\n\t" \
    "ds_read_b64 a[244:245], v0 offset:1024\n\tv_mfma_f32_16x16x32_bf16 v[140:143], a[104:107], v[100:103], v[140:143]\n\tds_read_b64 a[246:247], v0 offset:1032\n\tv_mfma_f32_16x16x32_bf16 v[144:147], a[104:107], v[104:107], v[144:147]\n\tds_read_b128 v[68:71], v1 offset:192\n\tv_mfma_f32_16x16x32_bf16 v[148:151], a[104:107], v[108:111], v[148:151]\n\tds_read_b128 v[72:75], v1 offset:704\n\tv_mfma_f32_16x16x32_bf16 v[152:155], a[104:107], v[112:115], v[152:155]\n\t" \
    "ds_read_b128 v[76:79], v1 offset:1216\n\tv_mfma_f32_16x16x32_bf16 v[156:159], a[104:107], v[116:119], v[156:159]\n\tds_read_b128 v[80:83], v1 offset:1728\n\tv_mfma_f32_16x16x32_bf16 v[160:163], a[104:107], v[120:123], v[160:163]\n\tbuffer_load_dword v15, v6, s[12:15], s41 offen\n\tds_read_b128 v[84:87], v1 offset:2240\n\tv_mfma_f32_16x16x32_bf16 v[144:147], a[108:111], v[100:103], v[144:147]\n\tbuffer_load_dword v15, v7, s[16:19], s42 offen\n\t" \
    "ds_read_b128 v[88:91], v1 offset:2752\n\tv_mfma_f32_16x16x32_bf16 v[148:151], a[108:111], v[104:107], v[148:151]\n\ts_add_u32 s43, s43, 1\n\tds_read_b128 v[92:95], v1 offset:3264\n\tv_mfma_f32_16x16x32_bf16 v[152:155], a[108:111], v[108:111], v[152:155]\n\tv_readlane_b32 s41, v236, s43\n\tv_readlane_b32 s42, v237, s43\n\ts_nop 4\n\t" \
    "ds_read_b128 v[96:99], v1 offset:3776\n\tv_mfma_f32_16x16x32_bf16 v[156:159], a[108:111], v[112:115], v[156:159]\n\tds_read_b64 a[248:249], v0 offset:512\n\tv_mfma_f32_16x16x32_bf16 v[160:163], a[108:111], v[116:119], v[160:163]\n\tds_read_b64 a[250:251], v0 offset:520\n\tv_mfma_f32_16x16x32_bf16 v[148:151], a[112:115], v[100:103], v[148:151]\n\tds_read_b64 a[252:253], v0 offset:0\n\tv_mfma_f32_16x16x32_bf16 v[152:155], a[112:115], v[104:107], v[152:155]\n\t" \
    "ds_read_b64 a[254:255], v0 offset:8\n\tv_mfma_f32_16x16x32_bf16 v[156:159], a[112:115], v[108:111], v[156:159]\n\tv_mfma_f32_16x16x32_bf16 v[160:163], a[112:115], v[112:115], v[160:163]\n\tv_mfma_f32_16x16x32_bf16 v[152:155], a[116:119], v[100:103], v[152:155]\n\tv_mfma_f32_16x16x32_bf16 v[156:159], a[116:119], v[104:107], v[156:159]\n\tv_mfma_f32_16x16x32_bf16 v[160:163], a[116:119], v[108:111], v[160:163]\n\tv_mfma_f32_16x16x32_bf16 v[156:159], a[120:123], v[100:103], v[156:159]\n\tv_mfma_f32_16x16x32_bf16 v[160:163], a[120:123], v[104:107], v[160:163]\n\t" \
    "v_mfma_f32_16x16x32_bf16 v[160:163], a[124:127], v[100:103], v[160:163]\n\ts_waitcnt lgkmcnt(4)\n\tv_mfma_f32_16x16x32_bf16 v[132:135], a[128:131], v[68:71], v[132:135]\n\tds_read_b128 v[100:103], v1 offset:128\n\ts_waitcnt vmcnt(8)\n\tv_alignbit_b32 v196, v45, v45, 16\n\tv_mfma_f32_16x16x32_bf16 v[136:139], a[128:131], v[72:75], v[136:139]\n\tds_read_b128 v[104:107], v1 offset:640\n\t" \
    "v_alignbit_b32 v197, v44, v44, 16\n\tv_mfma_f32_16x16x32_bf16 v[140:143], a[128:131], v[76:79], v[140:143]\n\tds_read_b128 v[108:111], v1 offset:1152\n\tv_alignbit_b32 v198, v43, v43, 16\n\tv_mfma_f32_16x16x32_bf16 v[144:147], a[128:131], v[80:83], v[144:147]\n\tds_read_b128 v[112:115], v1 offset:1664\n\tv_alignbit_b32 v199, v42, v42, 16\n\tv_mfma_f32_16x16x32_bf16 v[148:151], a[128:131], v[84:87], v[148:151]\n\t" \
    "ds_write_b128 v2, v[196:199] offset:3072\n\tv_mfma_f32_16x16x32_bf16 v[152:155], a[128:131], v[88:91], v[152:155]\n\tds_read_b128 v[116:119], v1 offset:2176\n\tv_bfi_b32 v200, s46, v45, v44\n\tv_mfma_f32_16x16x32_bf16 v[156:159], a[128:131], v[92:95], v[156:159]\n\tds_read_b128 v[120:123], v1 offset:2688\n\tv_bfi_b32 v201, s46, v44, v43\n\tv_mfma_f32_16x16x32_bf16 v[160:163], a[128:131], v[96:99], v[160:163]\n\t" \
    "ds_read_b128 v[124:127], v1 offset:3200\n\tv_bfi_b32 v202, s46, v43, v42\n\tv_mfma_f32_16x16x32_bf16 v[136:139], a[132:135], v[68:71], v[136:139]\n\tds_read_b128 v[128:131], v1 offset:3712\n\tv_bfi_b32 v203, s46, v42, v59\n\tv_mfma_f32_16x16x32_bf16 v[140:143], a[132:135], v[72:75], v[140:143]\n\tds_write_b128 v2, v[200:203] offset:7232\n\tv_mfma_f32_16x16x32_bf16 v[144:147], a[132:135], v[76:79], v[144:147]\n\t" \
    "v_alignbit_b32 v196, v44, v44, 16\n\tv_alignbit_b32 v197, v43, v43, 16\n\tv_mfma_f32_16x16x32_bf16 v[148:151], a[132:135], v[80:83], v[148:151]\n\tv_alignbit_b32 v198, v42, v42, 16\n\tv_alignbit_b32 v199, v59, v59, 16\n\tv_mfma_f32_16x16x32_bf16 v[152:155], a[132:135], v[84:87], v[152:155]\n\tds_write_b128 v2, v[196:199] offset:11392\n\tv_mfma_f32_16x16x32_bf16 v[156:159], a[132:135], v[88:91], v[156:159]\n\t" \
    "v_bfi_b32 v200, s46, v44, v43\n\tv_bfi_b32 v201, s46, v43, v42\n\tv_mfma_f32_16x16x32_bf16 v[160:163], a[132:135], v[92:95], v[160:163]\n\tv_bfi_b32 v202, s46, v42, v59\n\tv_bfi_b32 v203, s46, v59, v58\n\tv_mfma_f32_16x16x32_bf16 v[140:143], a[136:139], v[68:71], v[140:143]\n\tds_write_b128 v2, v[200:203] offset:15552\n\tv_mfma_f32_16x16x32_bf16 v[144:147], a[136:139], v[72:75], v[144:147]\n\t" \
    "v_alignbit_b32 v196, v49, v49, 16\n\tv_alignbit_b32 v197, v48, v48, 16\n\tv_mfma_f32_16x16x32_bf16 v[148:151], a[136:139], v[76:79], v[148:151]\n\tv_alignbit_b32 v198, v47, v47, 16\n\tv_alignbit_b32 v199, v46, v46, 16\n\tv_mfma_f32_16x16x32_bf16 v[152:155], a[136:139], v[80:83], v[152:155]\n\tds_write_b128 v2, v[196:199] offset:2048\n\tv_mfma_f32_16x16x32_bf16 v[156:159], a[136:139], v[84:87], v[156:159]\n\t" \
    "v_bfi_b32 v200, s46, v49, v48\n\tv_bfi_b32 v201, s46, v48, v47\n\tv_mfma_f32_16x16x32_bf16 v[160:163], a[136:139], v[88:91], v[160:163]\n\tv_bfi_b32 v202, s46, v47, v46\n\tv_bfi_b32 v203, s46, v46, v61\n\tv_mfma_f32_16x16x32_bf16 v[144:147], a[140:143], v[68:71], v[144:147]\n\tds_write_b128 v2, v[200:203] offset:6208\n\tv_mfma_f32_16x16x32_bf16 v[148:151], a[140:143], v[72:75], v[148:151]\n\t" \
    "v_alignbit_b32 v196, v48, v48, 16\n\tv_alignbit_b32 v197, v47, v47, 16\n\tv_mfma_f32_16x16x32_bf16 v[152:155], a[140:143], v[76:79], v[152:155]\n\tv_alignbit_b32 v198, v46, v46, 16\n\tv_alignbit_b32 v199, v61, v61, 16\n\tv_mfma_f32_16x16x32_bf16 v[156:159], a[140:143], v[80:83], v[156:159]\n\tds_write_b128 v2, v[196:199] offset:10368\n\tv_mfma_f32_16x16x32_bf16 v[160:163], a[140:143], v[84:87], v[160:163]\n\t" \
    "v_bfi_b32 v200, s46, v48, v47\n\tv_bfi_b32 v201, s46, v47, v46\n\tv_mfma_f32_16x16x32_bf16 v[148:151], a[144:147], v[68:71], v[148:151]\n\tv_bfi_b32 v202, s46, v46, v61\n\tv_bfi_b32 v203, s46, v61, v60\n\tv_mfma_f32_16x16x32_bf16 v[152:155], a[144:147], v[72:75], v[152:155]\n\tds_write_b128 v2, v[200:203] offset:14528\n\tv_mfma_f32_16x16x32_bf16 v[156:159], a[144:147], v[76:79], v[156:159]\n\t" \
    "v_alignbit_b32 v196, v53, v53, 16\n\tv_alignbit_b32 v197, v52, v52, 16\n\tv_mfma_f32_16x16x32_bf16 v[160:163], a[144:147], v[80:83], v[160:163]\n\tv_alignbit_b32 v198, v51, v51, 16\n\tv_alignbit_b32 v199, v50, v50, 16\n\tv_mfma_f32_16x16x32_bf16 v[152:155], a[148:151], v[68:71], v[152:155]\n\tds_write_b128 v2, v[196:199] offset:1024\n\tv_mfma_f32_16x16x32_bf16 v[156:159], a[148:151], v[72:75], v[156:159]\n\t" \
    "v_bfi_b32 v200, s46, v53, v52\n\tv_bfi_b32 v201, s46, v52, v51\n\tv_mfma_f32_16x16x32_bf16 v[160:163], a[148:151], v[76:79], v[160:163]\n\tv_bfi_b32 v202, s46, v51, v50\n\tv_bfi_b32 v203, s46, v50, v63\n\tv_mfma_f32_16x16x32_bf16 v[156:159], a[152:155], v[68:71], v[156:159]\n\tds_write_b128 v2, v[200:203] offset:5184\n\tv_mfma_f32_16x16x32_bf16 v[160:163], a[152:155], v[72:75], v[160:163]\n\t" \
    "v_alignbit_b32 v196, v52, v52, 16\n\tv_alignbit_b32 v197, v51, v51, 16\n\tv_mfma_f32_16x16x32_bf16 v[160:163], a[156:159], v[68:71], v[160:163]\n\tv_alignbit_b32 v198, v50, v50, 16\n\tv_alignbit_b32 v199, v63, v63, 16\n\ts_waitcnt lgkmcnt(9)\n\tv_mfma_f32_16x16x32_bf16 v[132:135], a[160:163], v[100:103], v[132:135]\n\tds_write_b128 v2, v[196:199] offset:9344\n\t" \
    "v_mfma_f32_16x16x32_bf16 v[136:139], a[160:163], v[104:107], v[136:139]\n\tds_read_b128 v[68:71], v1 offset:64\n\tv_bfi_b32 v200, s46, v52, v51\n\tv_mfma_f32_16x16x32_bf16 v[140:143], a[160:163], v[108:111], v[140:143]\n\tds_read_b128 v[72:75], v1 offset:576\n\tv_bfi_b32 v201, s46, v51, v50\n\tv_mfma_f32_16x16x32_bf16 v[144:147], a[160:163], v[112:115], v[144:147]\n\tds_read_b128 v[76:79], v1 offset:1088\n\t" \
    "v_bfi_b32 v202, s46, v50, v63\n\tv_mfma_f32_16x16x32_bf16 v[148:151], a[160:163], v[116:119], v[148:151]\n\tds_read_b128 v[80:83], v1 offset:1600\n\tv_bfi_b32 v203, s46, v63, v62\n\tv_mfma_f32_16x16x32_bf16 v[152:155], a[160:163], v[120:123], v[152:155]\n\tds_write_b128 v2, v[200:203] offset:13504\n\tv_mfma_f32_16x16x32_bf16 v[156:159], a[160:163], v[124:127], v[156:159]\n\tds_read_b128 v[84:87], v1 offset:2112\n\t" \
    "v_alignbit_b32 v196, v57, v57, 16\n\tv_mfma_f32_16x16x32_bf16 v[160:163], a[160:163], v[128:131], v[160:163]\n\tbuffer_load_dword v15, v6, s[12:15], s41 offen\n\tds_read_b128 v[88:91], v1 offset:2624\n\tv_mfma_f32_16x16x32_bf16 v[136:139], a[164:167], v[100:103], v[136:139]\n\tbuffer_load_dword v15, v7, s[16:19], s42 offen\n\tds_read_b128 v[92:95], v1 offset:3136\n\tv_mfma_f32_16x16x32_bf16 v[140:143], a[164:167], v[104:107], v[140:143]\n\t" \
    "s_add_u32 s43, s43, 1\n\tds_read_b128 v[96:99], v1 offset:3648\n\tv_alignbit_b32 v197, v56, v56, 16\n\tv_mfma_f32_16x16x32_bf16 v[144:147], a[164:167], v[108:111], v[144:147]\n\tv_readlane_b32 s41, v236, s43\n\tv_readlane_b32 s42, v237, s43\n\ts_nop 4\n\tv_alignbit_b32 v198, v55, v55, 16\n\t" \
    "v_alignbit_b32 v199, v54, v54, 16\n\tv_mfma_f32_16x16x32_bf16 v[148:151], a[164:167], v[112:115], v[148:151]\n\tds_write_b128 v2, v[196:199] offset:0\n\tv_mfma_f32_16x16x32_bf16 v[152:155], a[164:167], v[116:119], v[152:155]\n\tv_bfi_b32 v200, s46, v57, v56\n\tv_bfi_b32 v201, s46, v56, v55\n\tv_mfma_f32_16x16x32_bf16 v[156:159], a[164:167], v[120:123], v[156:159]\n\tv_bfi_b32 v202, s46, v55, v54\n\t" \
    "v_bfi_b32 v203, s46, v54, v65\n\tv_mfma_f32_16x16x32_bf16 v[160:163], a[164:167], v[124:127], v[160:163]\n\tds_write_b128 v2, v[200:203] offset:4160\n\tv_mfma_f32_16x16x32_bf16 v[140:143], a[168:171], v[100:103], v[140:143]\n\tv_alignbit_b32 v196, v56, v56, 16\n\tv_alignbit_b32 v197, v55, v55, 16\n\tv_mfma_f32_16x16x32_bf16 v[144:147], a[168:171], v[104:107], v[144:147]\n\tv_alignbit_b32 v198, v54, v54, 16\n\t" \
    "v_alignbit_b32 v199, v65, v65, 16\n\tv_mfma_f32_16x16x32_bf16 v[148:151], a[168:171], v[108:111], v[148:151]\n\tds_write_b128 v2, v[196:199] offset:8320\n\tv_mfma_f32_16x16x32_bf16 v[152:155], a[168:171], v[112:115], v[152:155]\n\tv_bfi_b32 v200, s46, v56, v55\n\tv_bfi_b32 v201, s46, v55, v54\n\tv_mfma_f32_16x16x32_bf16 v[156:159], a[168:171], v[116:119], v[156:159]\n\tv_bfi_b32 v202, s46, v54, v65\n\t" \
    "v_bfi_b32 v203, s46, v65, v64\n\tv_mfma_f32_16x16x32_bf16 v[160:163], a[168:171], v[120:123], v[160:163]\n\tds_write_b128 v2, v[200:203] offset:12480\n\tv_mfma_f32_16x16x32_bf16 v[144:147], a[172:175], v[100:103], v[144:147]\n\tv_lshl_or_b32 v66, v66, 16, v66\n\tv_and_b32 v228, v66, v232\n\tv_mfma_f32_16x16x32_bf16 v[148:151], a[172:175], v[104:107], v[148:151]\n\tv_and_b32 v229, v66, v233\n\t" \
    "v_and_b32 v230, v66, v234\n\tv_mfma_f32_16x16x32_bf16 v[152:155], a[172:175], v[108:111], v[152:155]\n\tv_and_b32 v231, v66, v235\n\tv_mfma_f32_16x16x32_bf16 v[156:159], a[172:175], v[112:115], v[156:159]\n\tv_mfma_f32_16x16x32_bf16 v[160:163], a[172:175], v[116:119], v[160:163]\n\tv_mfma_f32_16x16x32_bf16 v[148:151], a[176:179], v[100:103], v[148:151]\n\tv_mfma_f32_16x16x32_bf16 v[152:155], a[176:179], v[104:107], v[152:155]\n\tv_mfma_f32_16x16x32_bf16 v[156:159], a[176:179], v[108:111], v[156:159]\n\t" \
    "v_mfma_f32_16x16x32_bf16 v[160:163], a[176:179], v[112:115], v[160:163]\n\tv_mfma_f32_16x16x32_bf16 v[152:155], a[180:183], v[100:103], v[152:155]\n\tv_mfma_f32_16x16x32_bf16 v[156:159], a[180:183], v[104:107], v[156:159]\n\tv_mfma_f32_16x16x32_bf16 v[160:163], a[180:183], v[108:111], v[160:163]\n\tv_mfma_f32_16x16x32_bf16 v[156:159], a[184:187], v[100:103], v[156:159]\n\tv_mfma_f32_16x16x32_bf16 v[160:163], a[184:187], v[104:107], v[160:163]\n\tv_mfma_f32_16x16x32_bf16 v[160:163], a[188:191], v[100:103], v[160:163]\n\ts_waitcnt lgkmcnt(4)\n\t" \
    "v_mfma_f32_16x16x32_bf16 v[132:135], a[192:195], v[68:71], v[132:135]\n\ts_add_u32 s68, s44, s38\n\tds_read_b128 v[100:103], v1 offset:0\n\tv_mfma_f32_16x16x32_bf16 v[136:139], a[192:195], v[72:75], v[136:139]\n\ts_add_u32 s68, s68, 8\n\tds_read_b128 v[104:107], v1 offset:512\n\tv_mfma_f32_16x16x32_bf16 v[140:143], a[192:195], v[76:79], v[140:143]\n\ts_lshl_b32 s66, s68, 1\n\t" \
    "ds_read_b128 v[108:111], v1 offset:1024\n\tv_mfma_f32_16x16x32_bf16 v[144:147], a[192:195], v[80:83], v[144:147]\n\ts_lshl_b32 s68, s68, 12\n\tbuffer_load_dwordx4 v[16:19], v8, s[76:79], s68 offen offset:0\n\tds_read_b128 v[112:115], v1 offset:1536\n\tv_mfma_f32_16x16x32_bf16 v[148:151], a[192:195], v[84:87], v[148:151]\n\tds_read_b128 v[116:119], v1 offset:2048\n\tv_mfma_f32_16x16x32_bf16 v[152:155], a[192:195], v[88:91], v[152:155]\n\t" \
    "ds_read_b128 v[120:123], v1 offset:2560\n\tv_mfma_f32_16x16x32_bf16 v[156:159], a[192:195], v[92:95], v[156:159]\n\tds_read_b128 v[124:127], v1 offset:3072\n\tv_mfma_f32_16x16x32_bf16 v[160:163], a[192:195], v[96:99], v[160:163]\n\tds_read_b128 v[128:131], v1 offset:3584\n\tv_mfma_f32_16x16x32_bf16 v[136:139], a[196:199], v[68:71], v[136:139]\n\tv_mfma_f32_16x16x32_bf16 v[140:143], a[196:199], v[72:75], v[140:143]\n\tbuffer_load_dwordx4 v[20:23], v8, s[76:79], s68 offen offset:1024\n\t" \
    "v_mfma_f32_16x16x32_bf16 v[144:147], a[196:199], v[76:79], v[144:147]\n\tv_mfma_f32_16x16x32_bf16 v[148:151], a[196:199], v[80:83], v[148:151]\n\tv_mfma_f32_16x16x32_bf16 v[152:155], a[196:199], v[84:87], v[152:155]\n\tv_mfma_f32_16x16x32_bf16 v[156:159], a[196:199], v[88:91], v[156:159]\n\tv_mfma_f32_16x16x32_bf16 v[160:163], a[196:199], v[92:95], v[160:163]\n\tv_mfma_f32_16x16x32_bf16 v[140:143], a[200:203], v[68:71], v[140:143]\n\tbuffer_load_dwordx4 v[24:27], v8, s[76:79], s68 offen offset:2048\n\tv_mfma_f32_16x16x32_bf16 v[144:147], a[200:203], v[72:75], v[144:147]\n\t" \
    "v_mfma_f32_16x16x32_bf16 v[148:151], a[200:203], v[76:79], v[148:151]\n\tv_mfma_f32_16x16x32_bf16 v[152:155], a[200:203], v[80:83], v[152:155]\n\tv_mfma_f32_16x16x32_bf16 v[156:159], a[200:203], v[84:87], v[156:159]\n\tv_mfma_f32_16x16x32_bf16 v[160:163], a[200:203], v[88:91], v[160:163]\n\tv_mfma_f32_16x16x32_bf16 v[144:147], a[204:207], v[68:71], v[144:147]\n\tbuffer_load_dwordx4 v[28:31], v8, s[76:79], s68 offen offset:3072\n\tv_mfma_f32_16x16x32_bf16 v[148:151], a[204:207], v[72:75], v[148:151]\n\tv_mfma_f32_16x16x32_bf16 v[152:155], a[204:207], v[76:79], v[152:155]\n\t" \
    "v_mfma_f32_16x16x32_bf16 v[156:159], a[204:207], v[80:83], v[156:159]\n\tv_mfma_f32_16x16x32_bf16 v[160:163], a[204:207], v[84:87], v[160:163]\n\tv_mfma_f32_16x16x32_bf16 v[148:151], a[208:211], v[68:71], v[148:151]\n\tv_mfma_f32_16x16x32_bf16 v[152:155], a[208:211], v[72:75], v[152:155]\n\tbuffer_load_dwordx2 v[32:33], v13, s[76:79], s68 offen\n\tv_mfma_f32_16x16x32_bf16 v[156:159], a[208:211], v[76:79], v[156:159]\n\tv_mfma_f32_16x16x32_bf16 v[160:163], a[208:211], v[80:83], v[160:163]\n\tv_mfma_f32_16x16x32_bf16 v[152:155], a[212:215], v[68:71], v[152:155]\n\t" \
    "v_mfma_f32_16x16x32_bf16 v[156:159], a[212:215], v[72:75], v[156:159]\n\tv_mfma_f32_16x16x32_bf16 v[160:163], a[212:215], v[76:79], v[160:163]\n\tv_mfma_f32_16x16x32_bf16 v[156:159], a[216:219], v[68:71], v[156:159]\n\tbuffer_load_dwordx2 v[34:35], v8, s[76:79], s68 offen offset:1016\n\tv_mfma_f32_16x16x32_bf16 v[160:163], a[216:219], v[72:75], v[160:163]\n\tv_mfma_f32_16x16x32_bf16 v[160:163], a[220:223], v[68:71], v[160:163]\n\ts_waitcnt lgkmcnt(0)\n\tv_mfma_f32_16x16x32_bf16 v[132:135], a[224:227], v[100:103], v[132:135]\n\t" \
    "ds_read_b64 a[0:1], v0 offset:4032\n\tv_mfma_f32_16x16x32_bf16 v[136:139], a[224:227], v[104:107], v[136:139]\n\tds_read_b64 a[2:3], v0 offset:4040\n\tv_mfma_f32_16x16x32_bf16 v[140:143], a[224:227], v[108:111], v[140:143]\n\tds_read_b64 a[4:5], v0 offset:3520\n\tv_mfma_f32_16x16x32_bf16 v[144:147], a[224:227], v[112:115], v[144:147]\n\tbuffer_load_dwordx2 v[36:37], v8, s[76:79], s68 offen offset:2040\n\tds_read_b64 a[6:7], v0 offset:3528\n\t" \
    "v_mfma_f32_16x16x32_bf16 v[148:151], a[224:227], v[116:119], v[148:151]\n\tds_read_b64 a[8:9], v0 offset:3008\n\tv_mfma_f32_16x16x32_bf16 v[152:155], a[224:227], v[120:123], v[152:155]\n\tds_read_b64 a[10:11], v0 offset:3016\n\tv_mfma_f32_16x16x32_bf16 v[156:159], a[224:227], v[124:127], v[156:159]\n\tds_read_b64 a[12:13], v0 offset:2496\n\tv_mfma_f32_16x16x32_bf16 v[160:163], a[224:227], v[128:131], v[160:163]\n\tds_read_b64 a[14:15], v0 offset:2504\n\t" \
    "v_mfma_f32_16x16x32_bf16 v[136:139], a[228:231], v[100:103], v[136:139]\n\tds_read_b64 a[16:17], v0 offset:1984\n\tv_mfma_f32_16x16x32_bf16 v[140:143], a[228:231], v[104:107], v[140:143]\n\tbuffer_load_dwordx2 v[38:39], v8, s[76:79], s68 offen offset:3064\n\tds_read_b64 a[18:19], v0 offset:1992\n\tv_mfma_f32_16x16x32_bf16 v[144:147], a[228:231], v[108:111], v[144:147]\n\tds_read_b64 a[20:21], v0 offset:1472\n\tv_mfma_f32_16x16x32_bf16 v[148:151], a[228:231], v[112:115], v[148:151]\n\t" \
    "ds_read_b64 a[22:23], v0 offset:1480\n\tv_mfma_f32_16x16x32_bf16 v[152:155], a[228:231], v[116:119], v[152:155]\n\tds_read_b64 a[24:25], v0 offset:960\n\tv_mfma_f32_16x16x32_bf16 v[156:159], a[228:231], v[120:123], v[156:159]\n\tds_read_b64 a[26:27], v0 offset:968\n\tv_mfma_f32_16x16x32_bf16 v[160:163], a[228:231], v[124:127], v[160:163]\n\tds_read_b64 a[28:29], v0 offset:448\n\tv_mfma_f32_16x16x32_bf16 v[140:143], a[232:235], v[100:103], v[140:143]\n\t" \
    "buffer_load_ushort v40, off, s[80:83], s66\n\tds_read_b64 a[30:31], v0 offset:456\n\tv_mfma_f32_16x16x32_bf16 v[144:147], a[232:235], v[104:107], v[144:147]\n\tds_read_b128 v[68:71], v1 offset:18944\n\tv_mfma_f32_16x16x32_bf16 v[148:151], a[232:235], v[108:111], v[148:151]\n\tds_read_b128 v[72:75], v1 offset:19456\n\tv_mfma_f32_16x16x32_bf16 v[152:155], a[232:235], v[112:115], v[152:155]\n\tds_read_b128 v[76:79], v1 offset:19968\n\t" \
    "v_mfma_f32_16x16x32_bf16 v[156:159], a[232:235], v[116:119], v[156:159]\n\tds_read_b128 v[80:83], v1 offset:20480\n\tv_mfma_f32_16x16x32_bf16 v[160:163], a[232:235], v[120:123], v[160:163]\n\tds_read_b128 v[84:87], v1 offset:20992\n\tv_mfma_f32_16x16x32_bf16 v[144:147], a[236:239], v[100:103], v[144:147]\n\tbuffer_load_dword v15, v6, s[12:15], s41 offen\n\tds_read_b128 v[88:91], v1 offset:21504\n\tv_mfma_f32_16x16x32_bf16 v[148:151], a[236:239], v[104:107], v[148:151]\n\t" \
    "buffer_load_dword v15, v7, s[16:19], s42 offen\n\tds_read_b128 v[92:95], v1 offset:22016\n\tv_mfma_f32_16x16x32_bf16 v[152:155], a[236:239], v[108:111], v[152:155]\n\ts_add_u32 s43, s43, 1\n\tds_read_b128 v[96:99], v1 offset:22528\n\tv_mfma_f32_16x16x32_bf16 v[156:159], a[236:239], v[112:115], v[156:159]\n\tv_readlane_b32 s41, v236, s43\n\tv_readlane_b32 s42, v237, s43\n\t" \
    "s_nop 4\n\tv_mfma_f32_16x16x32_bf16 v[160:163], a[236:239], v[116:119], v[160:163]\n\tv_mfma_f32_16x16x32_bf16 v[148:151], a[240:243], v[100:103], v[148:151]\n\tv_mfma_f32_16x16x32_bf16 v[152:155], a[240:243], v[104:107], v[152:155]\n\tv_mfma_f32_16x16x32_bf16 v[156:159], a[240:243], v[108:111], v[156:159]\n\tv_mfma_f32_16x16x32_bf16 v[160:163], a[240:243], v[112:115], v[160:163]\n\tv_mfma_f32_16x16x32_bf16 v[152:155], a[244:247], v[100:103], v[152:155]\n\tv_mfma_f32_16x16x32_bf16 v[156:159], a[244:247], v[104:107], v[156:159]\n\t" \
    "v_mfma_f32_16x16x32_bf16 v[160:163], a[244:247], v[108:111], v[160:163]\n\tv_mfma_f32_16x16x32_bf16 v[156:159], a[248:251], v[100:103], v[156:159]\n\tv_mfma_f32_16x16x32_bf16 v[160:163], a[248:251], v[104:107], v[160:163]\n\tv_mfma_f32_16x16x32_bf16 v[160:163], a[252:255], v[100:103], v[160:163]\n\ts_waitcnt lgkmcnt(0)\n\tv_mfma_f32_16x16x32_bf16 v[164:167], v[228:231], v[68:71], 0\n\tds_read_b64 a[32:33], v0 offset:3968\n\tds_read_b64 a[34:35], v0 offset:3976\n\t" \
    "v_mfma_f32_16x16x32_bf16 v[168:171], v[228:231], v[72:75], 0\n\tds_read_b64 a[36:37], v0 offset:3456\n\tds_read_b64 a[38:39], v0 offset:3464\n\tv_mfma_f32_16x16x32_bf16 v[172:175], v[228:231], v[76:79], 0\n\tds_read_b64 a[40:41], v0 offset:2944\n\tds_read_b64 a[42:43], v0 offset:2952\n\tv_mfma_f32_16x16x32_bf16 v[176:179], v[228:231], v[80:83], 0\n\tds_read_b64 a[44:45], v0 offset:2432\n\t" \
    "ds_read_b64 a[46:47], v0 offset:2440\n\tv_mfma_f32_16x16x32_bf16 v[180:183], v[228:231], v[84:87], 0\n\tds_read_b64 a[48:49], v0 offset:1920\n\tds_read_b64 a[50:51], v0 offset:1928\n\tv_mfma_f32_16x16x32_bf16 v[184:187], v[228:231], v[88:91], 0\n\tds_read_b64 a[52:53], v0 offset:1408\n\tv_cvt_pk_bf16_f32 v132, v132, v133\n\tv_mfma_f32_16x16x32_bf16 v[188:191], v[228:231], v[92:95], 0\n\t" \
    "ds_read_b64 a[54:55], v0 offset:1416\n\tv_cvt_pk_bf16_f32 v133, v134, v135\n\tv_mfma_f32_16x16x32_bf16 v[192:195], v[228:231], v[96:99], 0\n\tds_read_b64 a[56:57], v0 offset:896\n\tds_write_b64 v3, v[132:133] offset:0\n\tv_mfma_f32_16x16x32_bf16 v[164:167], a[0:3], v[68:71], v[164:167]\n\tds_read_b64 a[58:59], v0 offset:904\n\tv_cvt_pk_bf16_f32 v136, v136, v137\n\t" \
    "v_mfma_f32_16x16x32_bf16 v[168:171], a[0:3], v[72:75], v[168:171]\n\tds_read_b64 a[60:61], v0 offset:384\n\tv_cvt_pk_bf16_f32 v137, v138, v139\n\tv_mfma_f32_16x16x32_bf16 v[172:175], a[0:3], v[76:79], v[172:175]\n\tds_read_b64 a[62:63], v0 offset:392\n\tds_write_b64 v3, v[136:137] offset:512\n\tv_mfma_f32_16x16x32_bf16 v[176:179], a[0:3], v[80:83], v[176:179]\n\tds_read_b128 v[100:103], v1 offset:18880\n\t" \
    "v_cvt_pk_bf16_f32 v140, v140, v141\n\tv_mfma_f32_16x16x32_bf16 v[180:183], a[0:3], v[84:87], v[180:183]\n\tds_read_b128 v[104:107], v1 offset:19392\n\tv_cvt_pk_bf16_f32 v141, v142, v143\n\tv_mfma_f32_16x16x32_bf16 v[184:187], a[0:3], v[88:91], v[184:187]\n\tds_read_b128 v[108:111], v1 offset:19904\n\tds_write_b64 v3, v[140:141] offset:1024\n\tv_mfma_f32_16x16x32_bf16 v[188:191], a[0:3], v[92:95], v[188:191]\n\t" \
    "ds_read_b128 v[112:115], v1 offset:20416\n\tv_cvt_pk_bf16_f32 v144, v144, v145\n\tv_mfma_f32_16x16x32_bf16 v[192:195], a[0:3], v[96:99], v[192:195]\n\tds_read_b128 v[116:119], v1 offset:20928\n\tv_cvt_pk_bf16_f32 v145, v146, v147\n\tv_mfma_f32_16x16x32_bf16 v[168:171], a[4:7], v[68:71], v[168:171]\n\tds_read_b128 v[120:123], v1 offset:21440\n\tds_write_b64 v3, v[144:145] offset:1536\n\t" \
    "v_mfma_f32_16x16x32_bf16 v[172:175], a[4:7], v[72:75], v[172:175]\n\tds_read_b128 v[124:127], v1 offset:21952\n\tv_cvt_pk_bf16_f32 v148, v148, v149\n\tv_mfma_f32_16x16x32_bf16 v[176:179], a[4:7], v[76:79], v[176:179]\n\tds_read_b128 v[128:131], v1 offset:22464\n\tv_cvt_pk_bf16_f32 v149, v150, v151\n\tv_mfma_f32_16x16x32_bf16 v[180:183], a[4:7], v[80:83], v[180:183]\n\tds_read_b64 a[64:65], v0 offset:3904\n\t" \
    "ds_write_b64 v3, v[148:149] offset:2048\n\tv_mfma_f32_16x16x32_bf16 v[184:187], a[4:7], v[84:87], v[184:187]\n\tds_read_b64 a[66:67], v0 offset:3912\n\tv_cvt_pk_bf16_f32 v152, v152, v153\n\tv_mfma_f32_16x16x32_bf16 v[188:191], a[4:7], v[88:91], v[188:191]\n\tds_read_b64 a[68:69], v0 offset:3392\n\tv_cvt_pk_bf16_f32 v153, v154, v155\n\tv_mfma_f32_16x16x32_bf16 v[192:195], a[4:7], v[92:95], v[192:195]\n\t" \
    "ds_read_b64 a[70:71], v0 offset:3400\n\tds_write_b64 v3, v[152:153] offset:2560\n\tv_mfma_f32_16x16x32_bf16 v[172:175], a[8:11], v[68:71], v[172:175]\n\tds_read_b64 a[72:73], v0 offset:2880\n\tv_cvt_pk_bf16_f32 v156, v156, v157\n\tv_mfma_f32_16x16x32_bf16 v[176:179], a[8:11], v[72:75], v[176:179]\n\tds_read_b64 a[74:75], v0 offset:2888\n\tv_cvt_pk_bf16_f32 v157, v158, v159\n\t" \
    "v_mfma_f32_16x16x32_bf16 v[180:183], a[8:11], v[76:79], v[180:183]\n\tds_read_b64 a[76:77], v0 offset:2368\n\tds_write_b64 v3, v[156:157] offset:3072\n\tv_mfma_f32_16x16x32_bf16 v[184:187], a[8:11], v[80:83], v[184:187]\n\tds_read_b64 a[78:79], v0 offset:2376\n\tv_cvt_pk_bf16_f32 v160, v160, v161\n\tv_mfma_f32_16x16x32_bf16 v[188:191], a[8:11], v[84:87], v[188:191]\n\tds_read_b64 a[80:81], v0 offset:1856\n\t" \
    "v_cvt_pk_bf16_f32 v161, v162, v163\n\tv_mfma_f32_16x16x32_bf16 v[192:195], a[8:11], v[88:91], v[192:195]\n\tds_read_b64 a[82:83], v0 offset:1864\n\tds_write_b64 v3, v[160:161] offset:3584\n\tv_mfma_f32_16x16x32_bf16 v[176:179], a[12:15], v[68:71], v[176:179]\n\tbuffer_load_dword v15, v7, s[16:19], s42 offen\n\tds_read_b64 a[84:85], v0 offset:1344\n\tv_mfma_f32_16x16x32_bf16 v[180:183], a[12:15], v[72:75], v[180:183]\n\t" \
    "s_add_u32 s43, s43, 1\n\tds_read_b64 a[86:87], v0 offset:1352\n\tv_mfma_f32_16x16x32_bf16 v[184:187], a[12:15], v[76:79], v[184:187]\n\tv_readlane_b32 s42, v237, s43\n\ts_nop 4\n\tds_read_b64 a[88:89], v0 offset:832\n\tv_mfma_f32_16x16x32_bf16 v[188:191], a[12:15], v[80:83], v[188:191]\n\tds_read_b64 a[90:91], v0 offset:840\n\t" \
    "v_mfma_f32_16x16x32_bf16 v[192:195], a[12:15], v[84:87], v[192:195]\n\tds_read_b64 a[92:93], v0 offset:320\n\tv_mfma_f32_16x16x32_bf16 v[180:183], a[16:19], v[68:71], v[180:183]\n\tds_read_b64 a[94:95], v0 offset:328\n\tv_mfma_f32_16x16x32_bf16 v[184:187], a[16:19], v[72:75], v[184:187]\n\tds_read_b64 a[96:97], v0 offset:3840\n\tv_mfma_f32_16x16x32_bf16 v[188:191], a[16:19], v[76:79], v[188:191]\n\tds_read_b64 a[98:99], v0 offset:3848\n\t" \
    "v_mfma_f32_16x16x32_bf16 v[192:195], a[16:19], v[80:83], v[192:195]\n\tds_read_b64 a[100:101], v0 offset:3328\n\tv_mfma_f32_16x16x32_bf16 v[184:187], a[20:23], v[68:71], v[184:187]\n\tds_read_b64 a[102:103], v0 offset:3336\n\tv_mfma_f32_16x16x32_bf16 v[188:191], a[20:23], v[72:75], v[188:191]\n\tv_mfma_f32_16x16x32_bf16 v[192:195], a[20:23], v[76:79], v[192:195]\n\tv_mfma_f32_16x16x32_bf16 v[188:191], a[24:27], v[68:71], v[188:191]\n\tv_mfma_f32_16x16x32_bf16 v[192:195], a[24:27], v[72:75], v[192:195]\n\t" \
    "v_mfma_f32_16x16x32_bf16 v[192:195], a[28:31], v[68:71], v[192:195]\n\ts_waitcnt lgkmcnt(10)\n\ts_barrier\n\tv_mfma_f32_16x16x32_bf16 v[164:167], a[32:35], v[100:103], v[164:167]\n\tds_read_b64 a[104:105], v0 offset:2816\n\tds_read_b64 a[106:107], v0 offset:2824\n\tv_mfma_f32_16x16x32_bf16 v[168:171], a[32:35], v[104:107], v[168:171]\n\tds_read_b64 a[108:109], v0 offset:2304\n\t" \
    "ds_read_b64 a[110:111], v0 offset:2312\n\tv_mfma_f32_16x16x32_bf16 v[172:175], a[32:35], v[108:111], v[172:175]\n\tds_read_b64 a[112:113], v0 offset:1792\n\tds_read_b64 a[114:115], v0 offset:1800\n\tv_mfma_f32_16x16x32_bf16 v[176:179], a[32:35], v[112:115], v[176:179]\n\tds_read_b64 a[116:117], v0 offset:1280\n\tds_read_b64 a[118:119], v0 offset:1288\n\tv_mfma_f32_16x16x32_bf16 v[180:183], a[32:35], v[116:119], v[180:183]\n\t" \
    "ds_read_b64 a[120:121], v0 offset:768\n\tds_read_b64 a[122:123], v0 offset:776\n\tv_mfma_f32_16x16x32_bf16 v[184:187], a[32:35], v[120:123], v[184:187]\n\tds_read_b64 a[124:125], v0 offset:256\n\ts_add_u32 s51, s44, 0\n\tv_mfma_f32_16x16x32_bf16 v[188:191], a[32:35], v[124:127], v[188:191]\n\tds_read_b64 a[126:127], v0 offset:264\n\ts_mul_i32 s65, s51, 1366\n\t" \
    "v_mfma_f32_16x16x32_bf16 v[192:195], a[32:35], v[128:131], v[192:195]\n\tds_read_b64 a[128:129], v0 offset:3776\n\ts_lshr_b32 s65, s65, 16\n\tv_mfma_f32_16x16x32_bf16 v[168:171], a[36:39], v[100:103], v[168:171]\n\tds_read_b64 a[130:131], v0 offset:3784\n\ts_mul_i32 s56, s65, 48\n\tv_mfma_f32_16x16x32_bf16 v[172:175], a[36:39], v[104:107], v[172:175]\n\tds_read_b64 a[132:133], v0 offset:3264\n\t" \
    "s_sub_u32 s56, s51, s56\n\tv_mfma_f32_16x16x32_bf16 v[176:179], a[36:39], v[108:111], v[176:179]\n\tds_read_b64 a[134:135], v0 offset:3272\n\ts_lshl_b32 s56, s56, 6\n\tv_mfma_f32_16x16x32_bf16 v[180:183], a[36:39], v[112:115], v[180:183]\n\tds_read_b128 v[68:71], v1 offset:18816\n\ts_lshr_b32 s70, s65, 2\n\tv_mfma_f32_16x16x32_bf16 v[184:187], a[36:39], v[116:119], v[184:187]\n\t" \
    "ds_read_b128 v[72:75], v1 offset:19328\n\ts_mul_i32 s70, s70, 252\n\tv_mfma_f32_16x16x32_bf16 v[188:191], a[36:39], v[120:123], v[188:191]\n\tds_read_b128 v[76:79], v1 offset:19840\n\ts_add_u32 s65, s65, s70\n\tv_mfma_f32_16x16x32_bf16 v[192:195], a[36:39], v[124:127], v[192:195]\n\tds_read_b128 v[80:83], v1 offset:20352\n\ts_mul_i32 s65, s65, 3072\n\t" \
    "v_mfma_f32_16x16x32_bf16 v[172:175], a[40:43], v[100:103], v[172:175]\n\tds_read_b128 v[84:87], v1 offset:20864\n\ts_add_u32 s56, s56, s65\n\tv_mfma_f32_16x16x32_bf16 v[176:179], a[40:43], v[104:107], v[176:179]\n\tds_read_b128 v[88:91], v1 offset:21376\n\ts_add_u32 s56, s56, 4849664\n\tv_mfma_f32_16x16x32_bf16 v[180:183], a[40:43], v[108:111], v[180:183]\n\tds_read_b128 v[92:95], v1 offset:21888\n\t" \
    "s_add_u32 s51, s51, s38\n\tv_mfma_f32_16x16x32_bf16 v[184:187], a[40:43], v[112:115], v[184:187]\n\tds_read_b128 v[96:99], v1 offset:22400\n\ts_lshl_b32 s51, s51, 3\n\tv_mfma_f32_16x16x32_bf16 v[188:191], a[40:43], v[116:119], v[188:191]\n\tds_read_b64 a[136:137], v0 offset:2752\n\ts_add_u32 s51, s51, 50176\n\tds_read_b64_tr_b16 v[204:205], v4 offset:0\n\t" \
    "v_mfma_f32_16x16x32_bf16 v[192:195], a[40:43], v[120:123], v[192:195]\n\tds_read_b64 a[138:139], v0 offset:2760\n\tds_read_b64_tr_b16 v[206:207], v4 offset:8\n\tv_mfma_f32_16x16x32_bf16 v[176:179], a[44:47], v[100:103], v[176:179]\n\tds_read_b64 a[140:141], v0 offset:2240\n\tds_read_b64_tr_b16 v[208:209], v4 offset:64\n\tv_mfma_f32_16x16x32_bf16 v[180:183], a[44:47], v[104:107], v[180:183]\n\tds_read_b64 a[142:143], v0 offset:2248\n\t" \
    "ds_read_b64_tr_b16 v[210:211], v4 offset:72\n\tv_mfma_f32_16x16x32_bf16 v[184:187], a[44:47], v[108:111], v[184:187]\n\tds_read_b64 a[144:145], v0 offset:1728\n\tds_read_b64_tr_b16 v[212:213], v4 offset:512\n\tv_mfma_f32_16x16x32_bf16 v[188:191], a[44:47], v[112:115], v[188:191]\n\tds_read_b64 a[146:147], v0 offset:1736\n\tds_read_b64_tr_b16 v[214:215], v4 offset:520\n\tv_mfma_f32_16x16x32_bf16 v[192:195], a[44:47], v[116:119], v[192:195]\n\t" \
    "ds_read_b64 a[148:149], v0 offset:1216\n\tds_read_b64_tr_b16 v[216:217], v4 offset:576\n\tv_mfma_f32_16x16x32_bf16 v[180:183], a[48:51], v[100:103], v[180:183]\n\tds_read_b64 a[150:151], v0 offset:1224\n\tds_read_b64_tr_b16 v[218:219], v4 offset:584\n\tv_mfma_f32_16x16x32_bf16 v[184:187], a[48:51], v[104:107], v[184:187]\n\tds_read_b64 a[152:153], v0 offset:704\n\tv_mfma_f32_16x16x32_bf16 v[188:191], a[48:51], v[108:111], v[188:191]\n\t" \
    "ds_read_b64 a[154:155], v0 offset:712\n\tv_mfma_f32_16x16x32_bf16 v[192:195], a[48:51], v[112:115], v[192:195]\n\tds_read_b64 a[156:157], v0 offset:192\n\tv_mfma_f32_16x16x32_bf16 v[184:187], a[52:55], v[100:103], v[184:187]\n\tds_read_b64 a[158:159], v0 offset:200\n\tv_mfma_f32_16x16x32_bf16 v[188:191], a[52:55], v[104:107], v[188:191]\n\tv_mfma_f32_16x16x32_bf16 v[192:195], a[52:55], v[108:111], v[192:195]\n\ts_waitcnt lgkmcnt(15)\n\t" \
    "buffer_store_dwordx4 v[204:207], v5, s[8:11], s56 offen sc1\n\ts_add_u32 s56, s56, 49152\n\tv_mfma_f32_16x16x32_bf16 v[188:191], a[56:59], v[100:103], v[188:191]\n\tv_mfma_f32_16x16x32_bf16 v[192:195], a[56:59], v[104:107], v[192:195]\n\tv_mfma_f32_16x16x32_bf16 v[192:195], a[60:63], v[100:103], v[192:195]\n\tv_mfma_f32_16x16x32_bf16 v[164:167], a[64:67], v[68:71], v[164:167]\n\tds_read_b64 a[160:161], v0 offset:3712\n\tds_read_b64 a[162:163], v0 offset:3720\n\t" \
    "v_mfma_f32_16x16x32_bf16 v[168:171], a[64:67], v[72:75], v[168:171]\n\tds_read_b64 a[164:165], v0 offset:3200\n\tds_read_b64 a[166:167], v0 offset:3208\n\tv_mfma_f32_16x16x32_bf16 v[172:175], a[64:67], v[76:79], v[172:175]\n\tds_read_b64 a[168:169], v0 offset:2688\n\tds_read_b64 a[170:171], v0 offset:2696\n\tv_mfma_f32_16x16x32_bf16 v[176:179], a[64:67], v[80:83], v[176:179]\n\tds_read_b64 a[172:173], v0 offset:2176\n\t" \
    "ds_read_b64 a[174:175], v0 offset:2184\n\tv_mfma_f32_16x16x32_bf16 v[180:183], a[64:67], v[84:87], v[180:183]\n\tds_read_b64 a[176:177], v0 offset:1664\n\tds_read_b64 a[178:179], v0 offset:1672\n\tv_mfma_f32_16x16x32_bf16 v[184:187], a[64:67], v[88:91], v[184:187]\n\tds_read_b64 a[180:181], v0 offset:1152\n\tv_mfma_f32_16x16x32_bf16 v[188:191], a[64:67], v[92:95], v[188:191]\n\tds_read_b64 a[182:183], v0 offset:1160\n\t" \
    "v_mfma_f32_16x16x32_bf16 v[192:195], a[64:67], v[96:99], v[192:195]\n\tds_read_b64 a[184:185], v0 offset:640\n\tv_mfma_f32_16x16x32_bf16 v[168:171], a[68:71], v[68:71], v[168:171]\n\tds_read_b64 a[186:187], v0 offset:648\n\tv_mfma_f32_16x16x32_bf16 v[172:175], a[68:71], v[72:75], v[172:175]\n\tds_read_b64 a[188:189], v0 offset:128\n\tv_mfma_f32_16x16x32_bf16 v[176:179], a[68:71], v[76:79], v[176:179]\n\tds_read_b64 a[190:191], v0 offset:136\n\t" \
    "v_mfma_f32_16x16x32_bf16 v[180:183], a[68:71], v[80:83], v[180:183]\n\tds_read_b128 v[100:103], v1 offset:18752\n\tv_mfma_f32_16x16x32_bf16 v[184:187], a[68:71], v[84:87], v[184:187]\n\tds_read_b128 v[104:107], v1 offset:19264\n\tv_mfma_f32_16x16x32_bf16 v[188:191], a[68:71], v[88:91], v[188:191]\n\tds_read_b128 v[108:111], v1 offset:19776\n\tv_mfma_f32_16x16x32_bf16 v[192:195], a[68:71], v[92:95], v[192:195]\n\tds_read_b128 v[112:115], v1 offset:20288\n\t" \
    "s_waitcnt lgkmcnt(15)\n\tbuffer_store_dwordx4 v[208:211], v5, s[8:11], s56 offen sc1\n\tv_mfma_f32_16x16x32_bf16 v[172:175], a[72:75], v[68:71], v[172:175]\n\tds_read_b128 v[116:119], v1 offset:20800\n\ts_add_u32 s56, s56, 49152\n\tv_mfma_f32_16x16x32_bf16 v[176:179], a[72:75], v[72:75], v[176:179]\n\tds_read_b128 v[120:123], v1 offset:21312\n\tv_mfma_f32_16x16x32_bf16 v[180:183], a[72:75], v[76:79], v[180:183]\n\t" \
    "ds_read_b128 v[124:127], v1 offset:21824\n\tv_mfma_f32_16x16x32_bf16 v[184:187], a[72:75], v[80:83], v[184:187]\n\tds_read_b128 v[128:131], v1 offset:22336\n\tv_mfma_f32_16x16x32_bf16 v[188:191], a[72:75], v[84:87], v[188:191]\n\tds_read_b64 a[192:193], v0 offset:3648\n\tv_mfma_f32_16x16x32_bf16 v[192:195], a[72:75], v[88:91], v[192:195]\n\tds_read_b64 a[194:195], v0 offset:3656\n\tv_mfma_f32_16x16x32_bf16 v[176:179], a[76:79], v[68:71], v[176:179]\n\t" \
    "ds_read_b64 a[196:197], v0 offset:3136\n\tv_mfma_f32_16x16x32_bf16 v[180:183], a[76:79], v[72:75], v[180:183]\n\tds_read_b64 a[198:199], v0 offset:3144\n\tv_mfma_f32_16x16x32_bf16 v[184:187], a[76:79], v[76:79], v[184:187]\n\tds_read_b64 a[200:201], v0 offset:2624\n\tv_mfma_f32_16x16x32_bf16 v[188:191], a[76:79], v[80:83], v[188:191]\n\tds_read_b64 a[202:203], v0 offset:2632\n\tv_mfma_f32_16x16x32_bf16 v[192:195], a[76:79], v[84:87], v[192:195]\n\t" \
    "ds_read_b64 a[204:205], v0 offset:2112\n\tv_mfma_f32_16x16x32_bf16 v[180:183], a[80:83], v[68:71], v[180:183]\n\tds_read_b64 a[206:207], v0 offset:2120\n\tv_mfma_f32_16x16x32_bf16 v[184:187], a[80:83], v[72:75], v[184:187]\n\tds_read_b64 a[208:209], v0 offset:1600\n\tv_mfma_f32_16x16x32_bf16 v[188:191], a[80:83], v[76:79], v[188:191]\n\tds_read_b64 a[210:211], v0 offset:1608\n\tv_mfma_f32_16x16x32_bf16 v[192:195], a[80:83], v[80:83], v[192:195]\n\t" \
    "ds_read_b64 a[212:213], v0 offset:1088\n\tv_mfma_f32_16x16x32_bf16 v[184:187], a[84:87], v[68:71], v[184:187]\n\tds_read_b64 a[214:215], v0 offset:1096\n\tv_mfma_f32_16x16x32_bf16 v[188:191], a[84:87], v[72:75], v[188:191]\n\tbuffer_store_dwordx4 v[212:215], v5, s[8:11], s56 offen sc1\n\ts_add_u32 s56, s56, 49152\n\tv_mfma_f32_16x16x32_bf16 v[192:195], a[84:87], v[76:79], v[192:195]\n\tv_mfma_f32_16x16x32_bf16 v[188:191], a[88:91], v[68:71], v[188:191]\n\t" \
    "v_mfma_f32_16x16x32_bf16 v[192:195], a[88:91], v[72:75], v[192:195]\n\tv_mfma_f32_16x16x32_bf16 v[192:195], a[92:95], v[68:71], v[192:195]\n\ts_waitcnt lgkmcnt(12)\n\tv_mfma_f32_16x16x32_bf16 v[164:167], a[96:99], v[100:103], v[164:167]\n\tds_read_b64 a[216:217], v0 offset:576\n\tv_mfma_f32_16x16x32_bf16 v[168:171], a[96:99], v[104:107], v[168:171]\n\tds_read_b64 a[218:219], v0 offset:584\n\tv_mfma_f32_16x16x32_bf16 v[172:175], a[96:99], v[108:111], v[172:175]\n\t" \
    "ds_read_b64 a[220:221], v0 offset:64\n\tv_mfma_f32_16x16x32_bf16 v[176:179], a[96:99], v[112:115], v[176:179]\n\tds_read_b64 a[222:223], v0 offset:72\n\tv_mfma_f32_16x16x32_bf16 v[180:183], a[96:99], v[116:119], v[180:183]\n\tds_read_b64 a[224:225], v0 offset:3584\n\tv_mfma_f32_16x16x32_bf16 v[184:187], a[96:99], v[120:123], v[184:187]\n\tds_read_b64 a[226:227], v0 offset:3592\n\tv_mfma_f32_16x16x32_bf16 v[188:191], a[96:99], v[124:127], v[188:191]\n\t" \
    "ds_read_b64 a[228:229], v0 offset:3072\n\tv_mfma_f32_16x16x32_bf16 v[192:195], a[96:99], v[128:131], v[192:195]\n\tds_read_b64 a[230:231], v0 offset:3080\n\tbuffer_store_dwordx4 v[216:219], v5, s[8:11], s56 offen sc1\n\tv_mfma_f32_16x16x32_bf16 v[168:171], a[100:103], v[100:103], v[168:171]\n\tds_read_b64 a[232:233], v0 offset:2560\n\ts_add_u32 s56, s56, 49152\n\tv_mfma_f32_16x16x32_bf16 v[172:175], a[100:103], v[104:107], v[172:175]\n\t" \
    "ds_read_b64 a[234:235], v0 offset:2568\n\tv_mfma_f32_16x16x32_bf16 v[176:179], a[100:103], v[108:111], v[176:179]\n\tds_read_b64 a[236:237], v0 offset:2048\n\tv_mfma_f32_16x16x32_bf16 v[180:183], a[100:103], v[112:115], v[180:183]\n\tds_read_b64 a[238:239], v0 offset:2056\n\tv_mfma_f32_16x16x32_bf16 v[184:187], a[100:103], v[116:119], v[184:187]\n\tbuffer_load_dword v15, v7, s[16:19], s42 offen\n\tds_read_b64 a[240:241], v0 offset:1536\n\t" \
    "v_mfma_f32_16x16x32_bf16 v[188:191], a[100:103], v[120:123], v[188:191]\n\ts_add_u32 s43, s43, 1\n\tds_read_b64 a[242:243], v0 offset:1544\n\tv_mfma_f32_16x16x32_bf16 v[192:195], a[100:103], v[124:127], v[192:195]\n\tv_readlane_b32 s42, v237, s43\n\ts_nop 4\n\tds_read_b64 a[244:245], v0 offset:1024\n\tv_mfma_f32_16x16x32_bf16 v[172:175], a[104:107], v[100:103], v[172:175]\n\t" \
    "ds_read_b64 a[246:247], v0 offset:1032\n\tv_mfma_f32_16x16x32_bf16 v[176:179], a[104:107], v[104:107], v[176:179]\n\tds_read_b128 v[68:71], v1 offset:18688\n\tv_mfma_f32_16x16x32_bf16 v[180:183], a[104:107], v[108:111], v[180:183]\n\tds_read_b128 v[72:75], v1 offset:19200\n\tv_mfma_f32_16x16x32_bf16 v[184:187], a[104:107], v[112:115], v[184:187]\n\tds_read_b128 v[76:79], v1 offset:19712\n\tv_mfma_f32_16x16x32_bf16 v[188:191], a[104:107], v[116:119], v[188:191]\n\t" \
    "ds_read_b128 v[80:83], v1 offset:20224\n\tv_mfma_f32_16x16x32_bf16 v[192:195], a[104:107], v[120:123], v[192:195]\n\tds_read_b128 v[84:87], v1 offset:20736\n\tv_mfma_f32_16x16x32_bf16 v[176:179], a[108:111], v[100:103], v[176:179]\n\tds_read_b128 v[88:91], v1 offset:21248\n\tv_mfma_f32_16x16x32_bf16 v[180:183], a[108:111], v[104:107], v[180:183]\n\tds_read_b128 v[92:95], v1 offset:21760\n\tv_mfma_f32_16x16x32_bf16 v[184:187], a[108:111], v[108:111], v[184:187]\n\t" \
    "ds_read_b128 v[96:99], v1 offset:22272\n\tv_mfma_f32_16x16x32_bf16 v[188:191], a[108:111], v[112:115], v[188:191]\n\tds_read_b64 a[248:249], v0 offset:512\n\tv_mfma_f32_16x16x32_bf16 v[192:195], a[108:111], v[116:119], v[192:195]\n\tds_read_b64 a[250:251], v0 offset:520\n\tv_mfma_f32_16x16x32_bf16 v[180:183], a[112:115], v[100:103], v[180:183]\n\tds_read_b64 a[252:253], v0 offset:0\n\tv_mfma_f32_16x16x32_bf16 v[184:187], a[112:115], v[104:107], v[184:187]\n\t" \
    "ds_read_b64 a[254:255], v0 offset:8\n\tv_mfma_f32_16x16x32_bf16 v[188:191], a[112:115], v[108:111], v[188:191]\n\tv_mfma_f32_16x16x32_bf16 v[192:195], a[112:115], v[112:115], v[192:195]\n\tv_mfma_f32_16x16x32_bf16 v[184:187], a[116:119], v[100:103], v[184:187]\n\tv_mfma_f32_16x16x32_bf16 v[188:191], a[116:119], v[104:107], v[188:191]\n\tv_mfma_f32_16x16x32_bf16 v[192:195], a[116:119], v[108:111], v[192:195]\n\tv_mfma_f32_16x16x32_bf16 v[188:191], a[120:123], v[100:103], v[188:191]\n\tv_mfma_f32_16x16x32_bf16 v[192:195], a[120:123], v[104:107], v[192:195]\n\t" \
    "v_mfma_f32_16x16x32_bf16 v[192:195], a[124:127], v[100:103], v[192:195]\n\ts_waitcnt lgkmcnt(4)\n\tv_mfma_f32_16x16x32_bf16 v[164:167], a[128:131], v[68:71], v[164:167]\n\tds_read_b128 v[100:103], v1 offset:18624\n\ts_waitcnt vmcnt(8)\n\tv_alignbit_b32 v196, v19, v19, 16\n\tv_mfma_f32_16x16x32_bf16 v[168:171], a[128:131], v[72:75], v[168:171]\n\tds_read_b128 v[104:107], v1 offset:19136\n\t" \
    "v_alignbit_b32 v197, v18, v18, 16\n\tv_mfma_f32_16x16x32_bf16 v[172:175], a[128:131], v[76:79], v[172:175]\n\tds_read_b128 v[108:111], v1 offset:19648\n\tv_alignbit_b32 v198, v17, v17, 16\n\tv_mfma_f32_16x16x32_bf16 v[176:179], a[128:131], v[80:83], v[176:179]\n\tds_read_b128 v[112:115], v1 offset:20160\n\tv_alignbit_b32 v199, v16, v16, 16\n\tv_mfma_f32_16x16x32_bf16 v[180:183], a[128:131], v[84:87], v[180:183]\n\t" \
    "ds_write_b128 v2, v[196:199] offset:3072\n\tv_mfma_f32_16x16x32_bf16 v[184:187], a[128:131], v[88:91], v[184:187]\n\tds_read_b128 v[116:119], v1 offset:20672\n\tv_bfi_b32 v200, s46, v19, v18\n\tv_mfma_f32_16x16x32_bf16 v[188:191], a[128:131], v[92:95], v[188:191]\n\tds_read_b128 v[120:123], v1 offset:21184\n\tv_bfi_b32 v201, s46, v18, v17\n\tv_mfma_f32_16x16x32_bf16 v[192:195], a[128:131], v[96:99], v[192:195]\n\t" \
    "ds_read_b128 v[124:127], v1 offset:21696\n\tv_bfi_b32 v202, s46, v17, v16\n\tv_mfma_f32_16x16x32_bf16 v[168:171], a[132:135], v[68:71], v[168:171]\n\tds_read_b128 v[128:131], v1 offset:22208\n\tv_bfi_b32 v203, s46, v16, v33\n\tv_mfma_f32_16x16x32_bf16 v[172:175], a[132:135], v[72:75], v[172:175]\n\tds_write_b128 v2, v[200:203] offset:7232\n\tv_mfma_f32_16x16x32_bf16 v[176:179], a[132:135], v[76:79], v[176:179]\n\t" \
    "v_alignbit_b32 v196, v18, v18, 16\n\tv_alignbit_b32 v197, v17, v17, 16\n\tv_mfma_f32_16x16x32_bf16 v[180:183], a[132:135], v[80:83], v[180:183]\n\tv_alignbit_b32 v198, v16, v16, 16\n\tv_alignbit_b32 v199, v33, v33, 16\n\tv_mfma_f32_16x16x32_bf16 v[184:187], a[132:135], v[84:87], v[184:187]\n\tds_write_b128 v2, v[196:199] offset:11392\n\tv_mfma_f32_16x16x32_bf16 v[188:191], a[132:135], v[88:91], v[188:191]\n\t" \
    "v_bfi_b32 v200, s46, v18, v17\n\tv_bfi_b32 v201, s46, v17, v16\n\tv_mfma_f32_16x16x32_bf16 v[192:195], a[132:135], v[92:95], v[192:195]\n\tv_bfi_b32 v202, s46, v16, v33\n\tv_bfi_b32 v203, s46, v33, v32\n\tv_mfma_f32_16x16x32_bf16 v[172:175], a[136:139], v[68:71], v[172:175]\n\tds_write_b128 v2, v[200:203] offset:15552\n\tv_mfma_f32_16x16x32_bf16 v[176:179], a[136:139], v[72:75], v[176:179]\n\t" \
    "v_alignbit_b32 v196, v23, v23, 16\n\tv_alignbit_b32 v197, v22, v22, 16\n\tv_mfma_f32_16x16x32_bf16 v[180:183], a[136:139], v[76:79], v[180:183]\n\tv_alignbit_b32 v198, v21, v21, 16\n\tv_alignbit_b32 v199, v20, v20, 16\n\tv_mfma_f32_16x16x32_bf16 v[184:187], a[136:139], v[80:83], v[184:187]\n\tds_write_b128 v2, v[196:199] offset:2048\n\tv_mfma_f32_16x16x32_bf16 v[188:191], a[136:139], v[84:87], v[188:191]\n\t" \
    "v_bfi_b32 v200, s46, v23, v22\n\tv_bfi_b32 v201, s46, v22, v21\n\tv_mfma_f32_16x16x32_bf16 v[192:195], a[136:139], v[88:91], v[192:195]\n\tv_bfi_b32 v202, s46, v21, v20\n\tv_bfi_b32 v203, s46, v20, v35\n\tv_mfma_f32_16x16x32_bf16 v[176:179], a[140:143], v[68:71], v[176:179]\n\tds_write_b128 v2, v[200:203] offset:6208\n\tv_mfma_f32_16x16x32_bf16 v[180:183], a[140:143], v[72:75], v[180:183]\n\t" \
    "v_alignbit_b32 v196, v22, v22, 16\n\tv_alignbit_b32 v197, v21, v21, 16\n\tv_mfma_f32_16x16x32_bf16 v[184:187], a[140:143], v[76:79], v[184:187]\n\tv_alignbit_b32 v198, v20, v20, 16\n\tv_alignbit_b32 v199, v35, v35, 16\n\tv_mfma_f32_16x16x32_bf16 v[188:191], a[140:143], v[80:83], v[188:191]\n\tds_write_b128 v2, v[196:199] offset:10368\n\tv_mfma_f32_16x16x32_bf16 v[192:195], a[140:143], v[84:87], v[192:195]\n\t" \
    "v_bfi_b32 v200, s46, v22, v21\n\tv_bfi_b32 v201, s46, v21, v20\n\tv_mfma_f32_16x16x32_bf16 v[180:183], a[144:147], v[68:71], v[180:183]\n\tv_bfi_b32 v202, s46, v20, v35\n\tv_bfi_b32 v203, s46, v35, v34\n\tv_mfma_f32_16x16x32_bf16 v[184:187], a[144:147], v[72:75], v[184:187]\n\tds_write_b128 v2, v[200:203] offset:14528\n\tv_mfma_f32_16x16x32_bf16 v[188:191], a[144:147], v[76:79], v[188:191]\n\t" \
    "v_alignbit_b32 v196, v27, v27, 16\n\tv_alignbit_b32 v197, v26, v26, 16\n\tv_mfma_f32_16x16x32_bf16 v[192:195], a[144:147], v[80:83], v[192:195]\n\tv_alignbit_b32 v198, v25, v25, 16\n\tv_alignbit_b32 v199, v24, v24, 16\n\tv_mfma_f32_16x16x32_bf16 v[184:187], a[148:151], v[68:71], v[184:187]\n\tds_write_b128 v2, v[196:199] offset:1024\n\tv_mfma_f32_16x16x32_bf16 v[188:191], a[148:151], v[72:75], v[188:191]\n\t" \
    "v_bfi_b32 v200, s46, v27, v26\n\tv_bfi_b32 v201, s46, v26, v25\n\tv_mfma_f32_16x16x32_bf16 v[192:195], a[148:151], v[76:79], v[192:195]\n\tv_bfi_b32 v202, s46, v25, v24\n\tv_bfi_b32 v203, s46, v24, v37\n\tv_mfma_f32_16x16x32_bf16 v[188:191], a[152:155], v[68:71], v[188:191]\n\tds_write_b128 v2, v[200:203] offset:5184\n\tv_mfma_f32_16x16x32_bf16 v[192:195], a[152:155], v[72:75], v[192:195]\n\t" \
    "v_alignbit_b32 v196, v26, v26, 16\n\tv_alignbit_b32 v197, v25, v25, 16\n\tv_mfma_f32_16x16x32_bf16 v[192:195], a[156:159], v[68:71], v[192:195]\n\tv_alignbit_b32 v198, v24, v24, 16\n\tv_alignbit_b32 v199, v37, v37, 16\n\ts_waitcnt lgkmcnt(9)\n\tv_mfma_f32_16x16x32_bf16 v[164:167], a[160:163], v[100:103], v[164:167]\n\tds_write_b128 v2, v[196:199] offset:9344\n\t" \
    "v_mfma_f32_16x16x32_bf16 v[168:171], a[160:163], v[104:107], v[168:171]\n\tds_read_b128 v[68:71], v1 offset:18560\n\tv_bfi_b32 v200, s46, v26, v25\n\tv_mfma_f32_16x16x32_bf16 v[172:175], a[160:163], v[108:111], v[172:175]\n\tds_read_b128 v[72:75], v1 offset:19072\n\tv_bfi_b32 v201, s46, v25, v24\n\tv_mfma_f32_16x16x32_bf16 v[176:179], a[160:163], v[112:115], v[176:179]\n\tds_read_b128 v[76:79], v1 offset:19584\n\t" \
    "v_bfi_b32 v202, s46, v24, v37\n\tv_mfma_f32_16x16x32_bf16 v[180:183], a[160:163], v[116:119], v[180:183]\n\tds_read_b128 v[80:83], v1 offset:20096\n\tv_bfi_b32 v203, s46, v37, v36\n\tv_mfma_f32_16x16x32_bf16 v[184:187], a[160:163], v[120:123], v[184:187]\n\tds_write_b128 v2, v[200:203] offset:13504\n\tv_mfma_f32_16x16x32_bf16 v[188:191], a[160:163], v[124:127], v[188:191]\n\tds_read_b128 v[84:87], v1 offset:20608\n\t" \
    "v_alignbit_b32 v196, v31, v31, 16\n\tv_mfma_f32_16x16x32_bf16 v[192:195], a[160:163], v[128:131], v[192:195]\n\tds_read_b128 v[88:91], v1 offset:21120\n\tv_alignbit_b32 v197, v30, v30, 16\n\tv_mfma_f32_16x16x32_bf16 v[168:171], a[164:167], v[100:103], v[168:171]\n\tds_read_b128 v[92:95], v1 offset:21632\n\tv_alignbit_b32 v198, v29, v29, 16\n\tv_mfma_f32_16x16x32_bf16 v[172:175], a[164:167], v[104:107], v[172:175]\n\t" \
    "ds_read_b128 v[96:99], v1 offset:22144\n\tv_alignbit_b32 v199, v28, v28, 16\n\tv_mfma_f32_16x16x32_bf16 v[176:179], a[164:167], v[108:111], v[176:179]\n\tds_write_b128 v2, v[196:199] offset:0\n\tv_mfma_f32_16x16x32_bf16 v[180:183], a[164:167], v[112:115], v[180:183]\n\tv_bfi_b32 v200, s46, v31, v30\n\tv_bfi_b32 v201, s46, v30, v29\n\tv_mfma_f32_16x16x32_bf16 v[184:187], a[164:167], v[116:119], v[184:187]\n\t" \
    "v_bfi_b32 v202, s46, v29, v28\n\tv_bfi_b32 v203, s46, v28, v39\n\tv_mfma_f32_16x16x32_bf16 v[188:191], a[164:167], v[120:123], v[188:191]\n\tds_write_b128 v2, v[200:203] offset:4160\n\tv_mfma_f32_16x16x32_bf16 v[192:195], a[164:167], v[124:127], v[192:195]\n\tv_alignbit_b32 v196, v30, v30, 16\n\tv_alignbit_b32 v197, v29, v29, 16\n\tv_mfma_f32_16x16x32_bf16 v[172:175], a[168:171], v[100:103], v[172:175]\n\t" \
    "v_alignbit_b32 v198, v28, v28, 16\n\tv_alignbit_b32 v199, v39, v39, 16\n\tv_mfma_f32_16x16x32_bf16 v[176:179], a[168:171], v[104:107], v[176:179]\n\tds_write_b128 v2, v[196:199] offset:8320\n\tv_mfma_f32_16x16x32_bf16 v[180:183], a[168:171], v[108:111], v[180:183]\n\tv_bfi_b32 v200, s46, v30, v29\n\tv_bfi_b32 v201, s46, v29, v28\n\tv_mfma_f32_16x16x32_bf16 v[184:187], a[168:171], v[112:115], v[184:187]\n\t" \
    "v_bfi_b32 v202, s46, v28, v39\n\tv_bfi_b32 v203, s46, v39, v38\n\tv_mfma_f32_16x16x32_bf16 v[188:191], a[168:171], v[116:119], v[188:191]\n\tds_write_b128 v2, v[200:203] offset:12480\n\tv_mfma_f32_16x16x32_bf16 v[192:195], a[168:171], v[120:123], v[192:195]\n\tv_lshl_or_b32 v40, v40, 16, v40\n\tv_and_b32 v228, v40, v232\n\tv_mfma_f32_16x16x32_bf16 v[176:179], a[172:175], v[100:103], v[176:179]\n\t" \
    "v_and_b32 v229, v40, v233\n\tv_and_b32 v230, v40, v234\n\tv_mfma_f32_16x16x32_bf16 v[180:183], a[172:175], v[104:107], v[180:183]\n\tv_and_b32 v231, v40, v235\n\tv_mfma_f32_16x16x32_bf16 v[184:187], a[172:175], v[108:111], v[184:187]\n\tv_mfma_f32_16x16x32_bf16 v[188:191], a[172:175], v[112:115], v[188:191]\n\tv_mfma_f32_16x16x32_bf16 v[192:195], a[172:175], v[116:119], v[192:195]\n\tv_mfma_f32_16x16x32_bf16 v[180:183], a[176:179], v[100:103], v[180:183]\n\t" \
    "v_mfma_f32_16x16x32_bf16 v[184:187], a[176:179], v[104:107], v[184:187]\n\tv_mfma_f32_16x16x32_bf16 v[188:191], a[176:179], v[108:111], v[188:191]\n\tv_mfma_f32_16x16x32_bf16 v[192:195], a[176:179], v[112:115], v[192:195]\n\tv_mfma_f32_16x16x32_bf16 v[184:187], a[180:183], v[100:103], v[184:187]\n\tv_mfma_f32_16x16x32_bf16 v[188:191], a[180:183], v[104:107], v[188:191]\n\tv_mfma_f32_16x16x32_bf16 v[192:195], a[180:183], v[108:111], v[192:195]\n\tv_mfma_f32_16x16x32_bf16 v[188:191], a[184:187], v[100:103], v[188:191]\n\tv_mfma_f32_16x16x32_bf16 v[192:195], a[184:187], v[104:107], v[192:195]\n\t" \
    "v_mfma_f32_16x16x32_bf16 v[192:195], a[188:191], v[100:103], v[192:195]\n\ts_waitcnt lgkmcnt(4)\n\tv_mfma_f32_16x16x32_bf16 v[164:167], a[192:195], v[68:71], v[164:167]\n\ts_add_u32 s68, s44, s38\n\tds_read_b128 v[100:103], v1 offset:18496\n\tv_mfma_f32_16x16x32_bf16 v[168:171], a[192:195], v[72:75], v[168:171]\n\ts_add_u32 s68, s68, 12\n\tds_read_b128 v[104:107], v1 offset:19008\n\t" \
    "v_mfma_f32_16x16x32_bf16 v[172:175], a[192:195], v[76:79], v[172:175]\n\ts_lshl_b32 s66, s68, 1\n\tds_read_b128 v[108:111], v1 offset:19520\n\tv_mfma_f32_16x16x32_bf16 v[176:179], a[192:195], v[80:83], v[176:179]\n\ts_lshl_b32 s68, s68, 12\n\tds_read_b128 v[112:115], v1 offset:20032\n\tv_mfma_f32_16x16x32_bf16 v[180:183], a[192:195], v[84:87], v[180:183]\n\ts_or_b32 s68, s68, s45\n\t" \
    "ds_read_b128 v[116:119], v1 offset:20544\n\tv_mfma_f32_16x16x32_bf16 v[184:187], a[192:195], v[88:91], v[184:187]\n\ts_or_b32 s66, s66, s45\n\tbuffer_load_dwordx4 v[42:45], v8, s[76:79], s68 offen offset:0\n\tds_read_b128 v[120:123], v1 offset:21056\n\tv_mfma_f32_16x16x32_bf16 v[188:191], a[192:195], v[92:95], v[188:191]\n\tds_read_b128 v[124:127], v1 offset:21568\n\tv_mfma_f32_16x16x32_bf16 v[192:195], a[192:195], v[96:99], v[192:195]\n\t" \
    "ds_read_b128 v[128:131], v1 offset:22080\n\tv_mfma_f32_16x16x32_bf16 v[168:171], a[196:199], v[68:71], v[168:171]\n\tv_mfma_f32_16x16x32_bf16 v[172:175], a[196:199], v[72:75], v[172:175]\n\tv_mfma_f32_16x16x32_bf16 v[176:179], a[196:199], v[76:79], v[176:179]\n\tv_mfma_f32_16x16x32_bf16 v[180:183], a[196:199], v[80:83], v[180:183]\n\tbuffer_load_dwordx4 v[46:49], v8, s[76:79], s68 offen offset:1024\n\tv_mfma_f32_16x16x32_bf16 v[184:187], a[196:199], v[84:87], v[184:187]\n\tv_mfma_f32_16x16x32_bf16 v[188:191], a[196:199], v[88:91], v[188:191]\n\t" \
    "v_mfma_f32_16x16x32_bf16 v[192:195], a[196:199], v[92:95], v[192:195]\n\tv_mfma_f32_16x16x32_bf16 v[172:175], a[200:203], v[68:71], v[172:175]\n\tv_mfma_f32_16x16x32_bf16 v[176:179], a[200:203], v[72:75], v[176:179]\n\tv_mfma_f32_16x16x32_bf16 v[180:183], a[200:203], v[76:79], v[180:183]\n\tbuffer_load_dwordx4 v[50:53], v8, s[76:79], s68 offen offset:2048\n\tv_mfma_f32_16x16x32_bf16 v[184:187], a[200:203], v[80:83], v[184:187]\n\tv_mfma_f32_16x16x32_bf16 v[188:191], a[200:203], v[84:87], v[188:191]\n\tv_mfma_f32_16x16x32_bf16 v[192:195], a[200:203], v[88:91], v[192:195]\n\t" \
    "v_mfma_f32_16x16x32_bf16 v[176:179], a[204:207], v[68:71], v[176:179]\n\tv_mfma_f32_16x16x32_bf16 v[180:183], a[204:207], v[72:75], v[180:183]\n\tv_mfma_f32_16x16x32_bf16 v[184:187], a[204:207], v[76:79], v[184:187]\n\tbuffer_load_dwordx4 v[54:57], v8, s[76:79], s68 offen offset:3072\n\tv_mfma_f32_16x16x32_bf16 v[188:191], a[204:207], v[80:83], v[188:191]\n\tv_mfma_f32_16x16x32_bf16 v[192:195], a[204:207], v[84:87], v[192:195]\n\tv_mfma_f32_16x16x32_bf16 v[180:183], a[208:211], v[68:71], v[180:183]\n\tv_mfma_f32_16x16x32_bf16 v[184:187], a[208:211], v[72:75], v[184:187]\n\t" \
    "v_mfma_f32_16x16x32_bf16 v[188:191], a[208:211], v[76:79], v[188:191]\n\tv_mfma_f32_16x16x32_bf16 v[192:195], a[208:211], v[80:83], v[192:195]\n\tbuffer_load_dwordx2 v[58:59], v13, s[76:79], s68 offen\n\tv_mfma_f32_16x16x32_bf16 v[184:187], a[212:215], v[68:71], v[184:187]\n\tv_mfma_f32_16x16x32_bf16 v[188:191], a[212:215], v[72:75], v[188:191]\n\tv_mfma_f32_16x16x32_bf16 v[192:195], a[212:215], v[76:79], v[192:195]\n\tv_mfma_f32_16x16x32_bf16 v[188:191], a[216:219], v[68:71], v[188:191]\n\tv_mfma_f32_16x16x32_bf16 v[192:195], a[216:219], v[72:75], v[192:195]\n\t" \
    "v_mfma_f32_16x16x32_bf16 v[192:195], a[220:223], v[68:71], v[192:195]\n\tbuffer_load_dwordx2 v[60:61], v8, s[76:79], s68 offen offset:1016\n\ts_waitcnt lgkmcnt(0)\n\tv_mfma_f32_16x16x32_bf16 v[164:167], a[224:227], v[100:103], v[164:167]\n\tds_read_b64 a[0:1], v0 offset:4032\n\tv_mfma_f32_16x16x32_bf16 v[168:171], a[224:227], v[104:107], v[168:171]\n\tds_read_b64 a[2:3], v0 offset:4040\n\tv_mfma_f32_16x16x32_bf16 v[172:175], a[224:227], v[108:111], v[172:175]\n\t" \
    "ds_read_b64 a[4:5], v0 offset:3520\n\tv_mfma_f32_16x16x32_bf16 v[176:179], a[224:227], v[112:115], v[176:179]\n\tds_read_b64 a[6:7], v0 offset:3528\n\tv_mfma_f32_16x16x32_bf16 v[180:183], a[224:227], v[116:119], v[180:183]\n\tds_read_b64 a[8:9], v0 offset:3008\n\tv_mfma_f32_16x16x32_bf16 v[184:187], a[224:227], v[120:123], v[184:187]\n\tbuffer_load_dwordx2 v[62:63], v8, s[76:79], s68 offen offset:2040\n\tds_read_b64 a[10:11], v0 offset:3016\n\t" \
    "v_mfma_f32_16x16x32_bf16 v[188:191], a[224:227], v[124:127], v[188:191]\n\tds_read_b64 a[12:13], v0 offset:2496\n\tv_mfma_f32_16x16x32_bf16 v[192:195], a[224:227], v[128:131], v[192:195]\n\tds_read_b64 a[14:15], v0 offset:2504\n\tv_mfma_f32_16x16x32_bf16 v[168:171], a[228:231], v[100:103], v[168:171]\n\tds_read_b64 a[16:17], v0 offset:1984\n\tv_mfma_f32_16x16x32_bf16 v[172:175], a[228:231], v[104:107], v[172:175]\n\tds_read_b64 a[18:19], v0 offset:1992\n\t" \
    "v_mfma_f32_16x16x32_bf16 v[176:179], a[228:231], v[108:111], v[176:179]\n\tds_read_b64 a[20:21], v0 offset:1472\n\tv_mfma_f32_16x16x32_bf16 v[180:183], a[228:231], v[112:115], v[180:183]\n\tbuffer_load_dwordx2 v[64:65], v8, s[76:79], s68 offen offset:3064\n\tds_read_b64 a[22:23], v0 offset:1480\n\tv_mfma_f32_16x16x32_bf16 v[184:187], a[228:231], v[116:119], v[184:187]\n\tds_read_b64 a[24:25], v0 offset:960\n\tv_mfma_f32_16x16x32_bf16 v[188:191], a[228:231], v[120:123], v[188:191]\n\t" \
    "ds_read_b64 a[26:27], v0 offset:968\n\tv_mfma_f32_16x16x32_bf16 v[192:195], a[228:231], v[124:127], v[192:195]\n\tds_read_b64 a[28:29], v0 offset:448\n\tv_mfma_f32_16x16x32_bf16 v[172:175], a[232:235], v[100:103], v[172:175]\n\tds_read_b64 a[30:31], v0 offset:456\n\tv_mfma_f32_16x16x32_bf16 v[176:179], a[232:235], v[104:107], v[176:179]\n\tds_read_b128 v[68:71], v1 offset:37440\n\tv_mfma_f32_16x16x32_bf16 v[180:183], a[232:235], v[108:111], v[180:183]\n\t" \
    "buffer_load_ushort v66, off, s[80:83], s66\n\tds_read_b128 v[72:75], v1 offset:37952\n\tv_mfma_f32_16x16x32_bf16 v[184:187], a[232:235], v[112:115], v[184:187]\n\tds_read_b128 v[76:79], v1 offset:38464\n\tv_mfma_f32_16x16x32_bf16 v[188:191], a[232:235], v[116:119], v[188:191]\n\tds_read_b128 v[80:83], v1 offset:38976\n\tv_mfma_f32_16x16x32_bf16 v[192:195], a[232:235], v[120:123], v[192:195]\n\tds_read_b128 v[84:87], v1 offset:39488\n\t" \
    "v_mfma_f32_16x16x32_bf16 v[176:179], a[236:239], v[100:103], v[176:179]\n\tds_read_b128 v[88:91], v1 offset:40000\n\tv_mfma_f32_16x16x32_bf16 v[180:183], a[236:239], v[104:107], v[180:183]\n\tds_read_b128 v[92:95], v1 offset:40512\n\tv_mfma_f32_16x16x32_bf16 v[184:187], a[236:239], v[108:111], v[184:187]\n\ts_lshl_b32 s69, s67, 13\n\tbuffer_load_dword v15, v14, s[4:7], s69 offen\n\tds_read_b128 v[96:99], v1 offset:41024\n\t" \
    "v_mfma_f32_16x16x32_bf16 v[188:191], a[236:239], v[112:115], v[188:191]\n\ts_waitcnt vmcnt(11)\n\tbuffer_store_dwordx2 v[248:249], off, s[8:11], s51 sc1\n\tv_mfma_f32_16x16x32_bf16 v[192:195], a[236:239], v[116:119], v[192:195]\n\tv_mfma_f32_16x16x32_bf16 v[180:183], a[240:243], v[100:103], v[180:183]\n\tv_mfma_f32_16x16x32_bf16 v[184:187], a[240:243], v[104:107], v[184:187]\n\tv_mfma_f32_16x16x32_bf16 v[188:191], a[240:243], v[108:111], v[188:191]\n\tv_mfma_f32_16x16x32_bf16 v[192:195], a[240:243], v[112:115], v[192:195]\n\t" \
    "v_mfma_f32_16x16x32_bf16 v[184:187], a[244:247], v[100:103], v[184:187]\n\tv_mfma_f32_16x16x32_bf16 v[188:191], a[244:247], v[104:107], v[188:191]\n\tv_mfma_f32_16x16x32_bf16 v[192:195], a[244:247], v[108:111], v[192:195]\n\tv_mfma_f32_16x16x32_bf16 v[188:191], a[248:251], v[100:103], v[188:191]\n\tv_mfma_f32_16x16x32_bf16 v[192:195], a[248:251], v[104:107], v[192:195]\n\tv_mfma_f32_16x16x32_bf16 v[192:195], a[252:255], v[100:103], v[192:195]\n\ts_waitcnt lgkmcnt(0)\n\tv_mfma_f32_16x16x32_bf16 v[132:135], v[228:231], v[68:71], 0\n\t" \
    "ds_read_b64 a[32:33], v0 offset:3968\n\tds_read_b64 a[34:35], v0 offset:3976\n\tv_mfma_f32_16x16x32_bf16 v[136:139], v[228:231], v[72:75], 0\n\tds_read_b64 a[36:37], v0 offset:3456\n\tds_read_b64 a[38:39], v0 offset:3464\n\tv_mfma_f32_16x16x32_bf16 v[140:143], v[228:231], v[76:79], 0\n\tds_read_b64 a[40:41], v0 offset:2944\n\tds_read_b64 a[42:43], v0 offset:2952\n\t" \
    "v_mfma_f32_16x16x32_bf16 v[144:147], v[228:231], v[80:83], 0\n\tds_read_b64 a[44:45], v0 offset:2432\n\tds_read_b64 a[46:47], v0 offset:2440\n\tv_mfma_f32_16x16x32_bf16 v[148:151], v[228:231], v[84:87], 0\n\tds_read_b64 a[48:49], v0 offset:1920\n\tds_read_b64 a[50:51], v0 offset:1928\n\tv_mfma_f32_16x16x32_bf16 v[152:155], v[228:231], v[88:91], 0\n\tds_read_b64 a[52:53], v0 offset:1408\n\t" \
    "v_cvt_pk_bf16_f32 v164, v164, v165\n\tv_mfma_f32_16x16x32_bf16 v[156:159], v[228:231], v[92:95], 0\n\tds_read_b64 a[54:55], v0 offset:1416\n\tv_cvt_pk_bf16_f32 v165, v166, v167\n\tv_mfma_f32_16x16x32_bf16 v[160:163], v[228:231], v[96:99], 0\n\tds_read_b64 a[56:57], v0 offset:896\n\tds_write_b64 v3, v[164:165] offset:18496\n\tv_mfma_f32_16x16x32_bf16 v[132:135], a[0:3], v[68:71], v[132:135]\n\t" \
    "ds_read_b64 a[58:59], v0 offset:904\n\tv_cvt_pk_bf16_f32 v168, v168, v169\n\tv_mfma_f32_16x16x32_bf16 v[136:139], a[0:3], v[72:75], v[136:139]\n\tds_read_b64 a[60:61], v0 offset:384\n\tv_cvt_pk_bf16_f32 v169, v170, v171\n\tv_mfma_f32_16x16x32_bf16 v[140:143], a[0:3], v[76:79], v[140:143]\n\tds_read_b64 a[62:63], v0 offset:392\n\tds_write_b64 v3, v[168:169] offset:19008\n\t" \
    "v_mfma_f32_16x16x32_bf16 v[144:147], a[0:3], v[80:83], v[144:147]\n\tds_read_b128 v[100:103], v1 offset:37376\n\tv_cvt_pk_bf16_f32 v172, v172, v173\n\tv_mfma_f32_16x16x32_bf16 v[148:151], a[0:3], v[84:87], v[148:151]\n\tds_read_b128 v[104:107], v1 offset:37888\n\tv_cvt_pk_bf16_f32 v173, v174, v175\n\tv_mfma_f32_16x16x32_bf16 v[152:155], a[0:3], v[88:91], v[152:155]\n\tds_read_b128 v[108:111], v1 offset:38400\n\t" \
    "ds_write_b64 v3, v[172:173] offset:19520\n\tv_mfma_f32_16x16x32_bf16 v[156:159], a[0:3], v[92:95], v[156:159]\n\tds_read_b128 v[112:115], v1 offset:38912\n\tv_cvt_pk_bf16_f32 v176, v176, v177\n\tv_mfma_f32_16x16x32_bf16 v[160:163], a[0:3], v[96:99], v[160:163]\n\tds_read_b128 v[116:119], v1 offset:39424\n\tv_cvt_pk_bf16_f32 v177, v178, v179\n\tv_mfma_f32_16x16x32_bf16 v[136:139], a[4:7], v[68:71], v[136:139]\n\t" \
    "ds_read_b128 v[120:123], v1 offset:39936\n\tds_write_b64 v3, v[176:177] offset:20032\n\tv_mfma_f32_16x16x32_bf16 v[140:143], a[4:7], v[72:75], v[140:143]\n\tbuffer_load_dword v15, v7, s[16:19], s42 offen\n\tds_read_b128 v[124:127], v1 offset:40448\n\tv_mfma_f32_16x16x32_bf16 v[144:147], a[4:7], v[76:79], v[144:147]\n\ts_add_u32 s43, s43, 1\n\tds_read_b128 v[128:131], v1 offset:40960\n\t" \
    "v_cvt_pk_bf16_f32 v180, v180, v181\n\tv_mfma_f32_16x16x32_bf16 v[148:151], a[4:7], v[80:83], v[148:151]\n\tv_readlane_b32 s42, v237, s43\n\ts_nop 4\n\tds_read_b64 a[64:65], v0 offset:3904\n\tv_cvt_pk_bf16_f32 v181, v182, v183\n\tv_mfma_f32_16x16x32_bf16 v[152:155], a[4:7], v[84:87], v[152:155]\n\tds_read_b64 a[66:67], v0 offset:3912\n\t" \
    "ds_write_b64 v3, v[180:181] offset:20544\n\tv_mfma_f32_16x16x32_bf16 v[156:159], a[4:7], v[88:91], v[156:159]\n\tds_read_b64 a[68:69], v0 offset:3392\n\tv_cvt_pk_bf16_f32 v184, v184, v185\n\tv_mfma_f32_16x16x32_bf16 v[160:163], a[4:7], v[92:95], v[160:163]\n\tds_read_b64 a[70:71], v0 offset:3400\n\tv_cvt_pk_bf16_f32 v185, v186, v187\n\tv_mfma_f32_16x16x32_bf16 v[140:143], a[8:11], v[68:71], v[140:143]\n\t" \
    "ds_read_b64 a[72:73], v0 offset:2880\n\tds_write_b64 v3, v[184:185] offset:21056\n\tv_mfma_f32_16x16x32_bf16 v[144:147], a[8:11], v[72:75], v[144:147]\n\tds_read_b64 a[74:75], v0 offset:2888\n\tv_cvt_pk_bf16_f32 v188, v188, v189\n\tv_mfma_f32_16x16x32_bf16 v[148:151], a[8:11], v[76:79], v[148:151]\n\tds_read_b64 a[76:77], v0 offset:2368\n\tv_cvt_pk_bf16_f32 v189, v190, v191\n\t" \
    "v_mfma_f32_16x16x32_bf16 v[152:155], a[8:11], v[80:83], v[152:155]\n\tds_read_b64 a[78:79], v0 offset:2376\n\tds_write_b64 v3, v[188:189] offset:21568\n\tv_mfma_f32_16x16x32_bf16 v[156:159], a[8:11], v[84:87], v[156:159]\n\tds_read_b64 a[80:81], v0 offset:1856\n\tv_cvt_pk_bf16_f32 v192, v192, v193\n\tv_mfma_f32_16x16x32_bf16 v[160:163], a[8:11], v[88:91], v[160:163]\n\tds_read_b64 a[82:83], v0 offset:1864\n\t" \
    "v_cvt_pk_bf16_f32 v193, v194, v195\n\tv_mfma_f32_16x16x32_bf16 v[144:147], a[12:15], v[68:71], v[144:147]\n\tds_read_b64 a[84:85], v0 offset:1344\n\tds_write_b64 v3, v[192:193] offset:22080\n\tv_mfma_f32_16x16x32_bf16 v[148:151], a[12:15], v[72:75], v[148:151]\n\tds_read_b64 a[86:87], v0 offset:1352\n\tv_mfma_f32_16x16x32_bf16 v[152:155], a[12:15], v[76:79], v[152:155]\n\tds_read_b64 a[88:89], v0 offset:832\n\t" \
    "v_mfma_f32_16x16x32_bf16 v[156:159], a[12:15], v[80:83], v[156:159]\n\tds_read_b64 a[90:91], v0 offset:840\n\tv_mfma_f32_16x16x32_bf16 v[160:163], a[12:15], v[84:87], v[160:163]\n\tds_read_b64 a[92:93], v0 offset:320\n\tv_mfma_f32_16x16x32_bf16 v[148:151], a[16:19], v[68:71], v[148:151]\n\tds_read_b64 a[94:95], v0 offset:328\n\tv_mfma_f32_16x16x32_bf16 v[152:155], a[16:19], v[72:75], v[152:155]\n\tds_read_b64 a[96:97], v0 offset:3840\n\t" \
    "v_mfma_f32_16x16x32_bf16 v[156:159], a[16:19], v[76:79], v[156:159]\n\tds_read_b64 a[98:99], v0 offset:3848\n\tv_mfma_f32_16x16x32_bf16 v[160:163], a[16:19], v[80:83], v[160:163]\n\tds_read_b64 a[100:101], v0 offset:3328\n\tv_mfma_f32_16x16x32_bf16 v[152:155], a[20:23], v[68:71], v[152:155]\n\tds_read_b64 a[102:103], v0 offset:3336\n\tv_mfma_f32_16x16x32_bf16 v[156:159], a[20:23], v[72:75], v[156:159]\n\tv_mfma_f32_16x16x32_bf16 v[160:163], a[20:23], v[76:79], v[160:163]\n\t" \
    "v_mfma_f32_16x16x32_bf16 v[156:159], a[24:27], v[68:71], v[156:159]\n\tv_mfma_f32_16x16x32_bf16 v[160:163], a[24:27], v[72:75], v[160:163]\n\tv_mfma_f32_16x16x32_bf16 v[160:163], a[28:31], v[68:71], v[160:163]\n\ts_waitcnt lgkmcnt(9)\n\ts_barrier\n\tv_mfma_f32_16x16x32_bf16 v[132:135], a[32:35], v[100:103], v[132:135]\n\tds_read_b64 a[104:105], v0 offset:2816\n\tds_read_b64 a[106:107], v0 offset:2824\n\t" \
    "v_mfma_f32_16x16x32_bf16 v[136:139], a[32:35], v[104:107], v[136:139]\n\tds_read_b64 a[108:109], v0 offset:2304\n\tds_read_b64 a[110:111], v0 offset:2312\n\tv_mfma_f32_16x16x32_bf16 v[140:143], a[32:35], v[108:111], v[140:143]\n\tds_read_b64 a[112:113], v0 offset:1792\n\tds_read_b64 a[114:115], v0 offset:1800\n\tv_mfma_f32_16x16x32_bf16 v[144:147], a[32:35], v[112:115], v[144:147]\n\tds_read_b64 a[116:117], v0 offset:1280\n\t" \
    "ds_read_b64 a[118:119], v0 offset:1288\n\tv_mfma_f32_16x16x32_bf16 v[148:151], a[32:35], v[116:119], v[148:151]\n\tds_read_b64 a[120:121], v0 offset:768\n\tds_read_b64 a[122:123], v0 offset:776\n\tv_mfma_f32_16x16x32_bf16 v[152:155], a[32:35], v[120:123], v[152:155]\n\tds_read_b64 a[124:125], v0 offset:256\n\ts_add_u32 s51, s44, 4\n\tv_mfma_f32_16x16x32_bf16 v[156:159], a[32:35], v[124:127], v[156:159]\n\t" \
    "ds_read_b64 a[126:127], v0 offset:264\n\ts_mul_i32 s65, s51, 1366\n\tv_mfma_f32_16x16x32_bf16 v[160:163], a[32:35], v[128:131], v[160:163]\n\tds_read_b64 a[128:129], v0 offset:3776\n\ts_lshr_b32 s65, s65, 16\n\tv_mfma_f32_16x16x32_bf16 v[136:139], a[36:39], v[100:103], v[136:139]\n\tds_read_b64 a[130:131], v0 offset:3784\n\ts_mul_i32 s56, s65, 48\n\t" \
    "v_mfma_f32_16x16x32_bf16 v[140:143], a[36:39], v[104:107], v[140:143]\n\tds_read_b64 a[132:133], v0 offset:3264\n\ts_sub_u32 s56, s51, s56\n\tv_mfma_f32_16x16x32_bf16 v[144:147], a[36:39], v[108:111], v[144:147]\n\tds_read_b64 a[134:135], v0 offset:3272\n\ts_lshl_b32 s56, s56, 6\n\tv_mfma_f32_16x16x32_bf16 v[148:151], a[36:39], v[112:115], v[148:151]\n\tds_read_b128 v[68:71], v1 offset:37312\n\t" \
    "s_lshr_b32 s70, s65, 2\n\tv_mfma_f32_16x16x32_bf16 v[152:155], a[36:39], v[116:119], v[152:155]\n\tds_read_b128 v[72:75], v1 offset:37824\n\ts_mul_i32 s70, s70, 252\n\tv_mfma_f32_16x16x32_bf16 v[156:159], a[36:39], v[120:123], v[156:159]\n\tds_read_b128 v[76:79], v1 offset:38336\n\ts_add_u32 s65, s65, s70\n\tv_mfma_f32_16x16x32_bf16 v[160:163], a[36:39], v[124:127], v[160:163]\n\t" \
    "ds_read_b128 v[80:83], v1 offset:38848\n\ts_mul_i32 s65, s65, 3072\n\tv_mfma_f32_16x16x32_bf16 v[140:143], a[40:43], v[100:103], v[140:143]\n\tds_read_b128 v[84:87], v1 offset:39360\n\ts_add_u32 s56, s56, s65\n\tv_mfma_f32_16x16x32_bf16 v[144:147], a[40:43], v[104:107], v[144:147]\n\tds_read_b128 v[88:91], v1 offset:39872\n\ts_add_u32 s56, s56, 4849664\n\t" \
    "v_mfma_f32_16x16x32_bf16 v[148:151], a[40:43], v[108:111], v[148:151]\n\tds_read_b128 v[92:95], v1 offset:40384\n\ts_add_u32 s51, s51, s38\n\tv_mfma_f32_16x16x32_bf16 v[152:155], a[40:43], v[112:115], v[152:155]\n\tds_read_b128 v[96:99], v1 offset:40896\n\ts_lshl_b32 s51, s51, 3\n\tv_mfma_f32_16x16x32_bf16 v[156:159], a[40:43], v[116:119], v[156:159]\n\tds_read_b64 a[136:137], v0 offset:2752\n\t" \
    "s_add_u32 s51, s51, 50176\n\tds_read_b64_tr_b16 v[204:205], v4 offset:18496\n\tv_mfma_f32_16x16x32_bf16 v[160:163], a[40:43], v[120:123], v[160:163]\n\tds_read_b64 a[138:139], v0 offset:2760\n\tds_read_b64_tr_b16 v[206:207], v4 offset:18504\n\tv_mfma_f32_16x16x32_bf16 v[144:147], a[44:47], v[100:103], v[144:147]\n\tds_read_b64 a[140:141], v0 offset:2240\n\tds_read_b64_tr_b16 v[208:209], v4 offset:18560\n\t" \
    "v_mfma_f32_16x16x32_bf16 v[148:151], a[44:47], v[104:107], v[148:151]\n\tds_read_b64 a[142:143], v0 offset:2248\n\tds_read_b64_tr_b16 v[210:211], v4 offset:18568\n\tv_mfma_f32_16x16x32_bf16 v[152:155], a[44:47], v[108:111], v[152:155]\n\tds_read_b64 a[144:145], v0 offset:1728\n\tds_read_b64_tr_b16 v[212:213], v4 offset:19008\n\tv_mfma_f32_16x16x32_bf16 v[156:159], a[44:47], v[112:115], v[156:159]\n\tds_read_b64 a[146:147], v0 offset:1736\n\t" \
    "ds_read_b64_tr_b16 v[214:215], v4 offset:19016\n\tv_mfma_f32_16x16x32_bf16 v[160:163], a[44:47], v[116:119], v[160:163]\n\tds_read_b64 a[148:149], v0 offset:1216\n\tds_read_b64_tr_b16 v[216:217], v4 offset:19072\n\tv_mfma_f32_16x16x32_bf16 v[148:151], a[48:51], v[100:103], v[148:151]\n\tds_read_b64 a[150:151], v0 offset:1224\n\tds_read_b64_tr_b16 v[218:219], v4 offset:19080\n\tv_mfma_f32_16x16x32_bf16 v[152:155], a[48:51], v[104:107], v[152:155]\n\t" \
    "ds_read_b64 a[152:153], v0 offset:704\n\tv_mfma_f32_16x16x32_bf16 v[156:159], a[48:51], v[108:111], v[156:159]\n\tds_read_b64 a[154:155], v0 offset:712\n\tv_mfma_f32_16x16x32_bf16 v[160:163], a[48:51], v[112:115], v[160:163]\n\tds_read_b64 a[156:157], v0 offset:192\n\tv_mfma_f32_16x16x32_bf16 v[152:155], a[52:55], v[100:103], v[152:155]\n\tds_read_b64 a[158:159], v0 offset:200\n\tv_mfma_f32_16x16x32_bf16 v[156:159], a[52:55], v[104:107], v[156:159]\n\t" \
    "v_mfma_f32_16x16x32_bf16 v[160:163], a[52:55], v[108:111], v[160:163]\n\tbuffer_load_dword v15, v7, s[16:19], s42 offen\n\ts_waitcnt lgkmcnt(15)\n\tbuffer_store_dwordx4 v[204:207], v5, s[8:11], s56 offen sc1\n\tv_mfma_f32_16x16x32_bf16 v[156:159], a[56:59], v[100:103], v[156:159]\n\ts_add_u32 s43, s43, 1\n\ts_add_u32 s56, s56, 49152\n\tv_mfma_f32_16x16x32_bf16 v[160:163], a[56:59], v[104:107], v[160:163]\n\t" \
    "v_readlane_b32 s42, v237, s43\n\ts_nop 4\n\tv_mfma_f32_16x16x32_bf16 v[160:163], a[60:63], v[100:103], v[160:163]\n\tv_mfma_f32_16x16x32_bf16 v[132:135], a[64:67], v[68:71], v[132:135]\n\tds_read_b64 a[160:161], v0 offset:3712\n\tds_read_b64 a[162:163], v0 offset:3720\n\tv_mfma_f32_16x16x32_bf16 v[136:139], a[64:67], v[72:75], v[136:139]\n\tds_read_b64 a[164:165], v0 offset:3200\n\t" \
    "ds_read_b64 a[166:167], v0 offset:3208\n\tv_mfma_f32_16x16x32_bf16 v[140:143], a[64:67], v[76:79], v[140:143]\n\tds_read_b64 a[168:169], v0 offset:2688\n\tds_read_b64 a[170:171], v0 offset:2696\n\tv_mfma_f32_16x16x32_bf16 v[144:147], a[64:67], v[80:83], v[144:147]\n\tds_read_b64 a[172:173], v0 offset:2176\n\tds_read_b64 a[174:175], v0 offset:2184\n\tv_mfma_f32_16x16x32_bf16 v[148:151], a[64:67], v[84:87], v[148:151]\n\t" \
    "ds_read_b64 a[176:177], v0 offset:1664\n\tds_read_b64 a[178:179], v0 offset:1672\n\tv_mfma_f32_16x16x32_bf16 v[152:155], a[64:67], v[88:91], v[152:155]\n\tds_read_b64 a[180:181], v0 offset:1152\n\tv_mfma_f32_16x16x32_bf16 v[156:159], a[64:67], v[92:95], v[156:159]\n\tds_read_b64 a[182:183], v0 offset:1160\n\tv_mfma_f32_16x16x32_bf16 v[160:163], a[64:67], v[96:99], v[160:163]\n\tds_read_b64 a[184:185], v0 offset:640\n\t" \
    "v_mfma_f32_16x16x32_bf16 v[136:139], a[68:71], v[68:71], v[136:139]\n\tds_read_b64 a[186:187], v0 offset:648\n\tv_mfma_f32_16x16x32_bf16 v[140:143], a[68:71], v[72:75], v[140:143]\n\tds_read_b64 a[188:189], v0 offset:128\n\tv_mfma_f32_16x16x32_bf16 v[144:147], a[68:71], v[76:79], v[144:147]\n\tds_read_b64 a[190:191], v0 offset:136\n\tv_mfma_f32_16x16x32_bf16 v[148:151], a[68:71], v[80:83], v[148:151]\n\tds_read_b128 v[100:103], v1 offset:37248\n\t" \
    "v_mfma_f32_16x16x32_bf16 v[152:155], a[68:71], v[84:87], v[152:155]\n\tds_read_b128 v[104:107], v1 offset:37760\n\tv_mfma_f32_16x16x32_bf16 v[156:159], a[68:71], v[88:91], v[156:159]\n\tds_read_b128 v[108:111], v1 offset:38272\n\tv_mfma_f32_16x16x32_bf16 v[160:163], a[68:71], v[92:95], v[160:163]\n\tds_read_b128 v[112:115], v1 offset:38784\n\tv_mfma_f32_16x16x32_bf16 v[140:143], a[72:75], v[68:71], v[140:143]\n\tds_read_b128 v[116:119], v1 offset:39296\n\t" \
    "s_waitcnt lgkmcnt(15)\n\tbuffer_store_dwordx4 v[208:211], v5, s[8:11], s56 offen sc1\n\tv_mfma_f32_16x16x32_bf16 v[144:147], a[72:75], v[72:75], v[144:147]\n\tds_read_b128 v[120:123], v1 offset:39808\n\ts_add_u32 s56, s56, 49152\n\tv_mfma_f32_16x16x32_bf16 v[148:151], a[72:75], v[76:79], v[148:151]\n\tds_read_b128 v[124:127], v1 offset:40320\n\tv_mfma_f32_16x16x32_bf16 v[152:155], a[72:75], v[80:83], v[152:155]\n\t" \
    "ds_read_b128 v[128:131], v1 offset:40832\n\tv_mfma_f32_16x16x32_bf16 v[156:159], a[72:75], v[84:87], v[156:159]\n\tds_read_b64 a[192:193], v0 offset:3648\n\tv_mfma_f32_16x16x32_bf16 v[160:163], a[72:75], v[88:91], v[160:163]\n\tds_read_b64 a[194:195], v0 offset:3656\n\tv_mfma_f32_16x16x32_bf16 v[144:147], a[76:79], v[68:71], v[144:147]\n\tds_read_b64 a[196:197], v0 offset:3136\n\tv_mfma_f32_16x16x32_bf16 v[148:151], a[76:79], v[72:75], v[148:151]\n\t" \
    "ds_read_b64 a[198:199], v0 offset:3144\n\tv_mfma_f32_16x16x32_bf16 v[152:155], a[76:79], v[76:79], v[152:155]\n\tds_read_b64 a[200:201], v0 offset:2624\n\tv_mfma_f32_16x16x32_bf16 v[156:159], a[76:79], v[80:83], v[156:159]\n\tds_read_b64 a[202:203], v0 offset:2632\n\tv_mfma_f32_16x16x32_bf16 v[160:163], a[76:79], v[84:87], v[160:163]\n\tds_read_b64 a[204:205], v0 offset:2112\n\tv_mfma_f32_16x16x32_bf16 v[148:151], a[80:83], v[68:71], v[148:151]\n\t" \
    "ds_read_b64 a[206:207], v0 offset:2120\n\tv_mfma_f32_16x16x32_bf16 v[152:155], a[80:83], v[72:75], v[152:155]\n\tds_read_b64 a[208:209], v0 offset:1600\n\tv_mfma_f32_16x16x32_bf16 v[156:159], a[80:83], v[76:79], v[156:159]\n\tds_read_b64 a[210:211], v0 offset:1608\n\tv_mfma_f32_16x16x32_bf16 v[160:163], a[80:83], v[80:83], v[160:163]\n\tds_read_b64 a[212:213], v0 offset:1088\n\tv_mfma_f32_16x16x32_bf16 v[152:155], a[84:87], v[68:71], v[152:155]\n\t" \
    "ds_read_b64 a[214:215], v0 offset:1096\n\tv_mfma_f32_16x16x32_bf16 v[156:159], a[84:87], v[72:75], v[156:159]\n\tbuffer_store_dwordx4 v[212:215], v5, s[8:11], s56 offen sc1\n\tv_mfma_f32_16x16x32_bf16 v[160:163], a[84:87], v[76:79], v[160:163]\n\ts_add_u32 s56, s56, 49152\n\tv_mfma_f32_16x16x32_bf16 v[156:159], a[88:91], v[68:71], v[156:159]\n\tv_mfma_f32_16x16x32_bf16 v[160:163], a[88:91], v[72:75], v[160:163]\n\tv_mfma_f32_16x16x32_bf16 v[160:163], a[92:95], v[68:71], v[160:163]\n\t" \
    "s_waitcnt lgkmcnt(12)\n\tv_mfma_f32_16x16x32_bf16 v[132:135], a[96:99], v[100:103], v[132:135]\n\tds_read_b64 a[216:217], v0 offset:576\n\tv_mfma_f32_16x16x32_bf16 v[136:139], a[96:99], v[104:107], v[136:139]\n\tds_read_b64 a[218:219], v0 offset:584\n\tv_mfma_f32_16x16x32_bf16 v[140:143], a[96:99], v[108:111], v[140:143]\n\tds_read_b64 a[220:221], v0 offset:64\n\tv_mfma_f32_16x16x32_bf16 v[144:147], a[96:99], v[112:115], v[144:147]\n\t" \
    "ds_read_b64 a[222:223], v0 offset:72\n\tv_mfma_f32_16x16x32_bf16 v[148:151], a[96:99], v[116:119], v[148:151]\n\tds_read_b64 a[224:225], v0 offset:3584\n\tv_mfma_f32_16x16x32_bf16 v[152:155], a[96:99], v[120:123], v[152:155]\n\tds_read_b64 a[226:227], v0 offset:3592\n\tv_mfma_f32_16x16x32_bf16 v[156:159], a[96:99], v[124:127], v[156:159]\n\tds_read_b64 a[228:229], v0 offset:3072\n\tv_mfma_f32_16x16x32_bf16 v[160:163], a[96:99], v[128:131], v[160:163]\n\t" \
    "ds_read_b64 a[230:231], v0 offset:3080\n\tv_mfma_f32_16x16x32_bf16 v[136:139], a[100:103], v[100:103], v[136:139]\n\tds_read_b64 a[232:233], v0 offset:2560\n\tbuffer_store_dwordx4 v[216:219], v5, s[8:11], s56 offen sc1\n\tv_mfma_f32_16x16x32_bf16 v[140:143], a[100:103], v[104:107], v[140:143]\n\tds_read_b64 a[234:235], v0 offset:2568\n\ts_add_u32 s56, s56, 49152\n\tv_mfma_f32_16x16x32_bf16 v[144:147], a[100:103], v[108:111], v[144:147]\n\t" \
    "ds_read_b64 a[236:237], v0 offset:2048\n\tv_mfma_f32_16x16x32_bf16 v[148:151], a[100:103], v[112:115], v[148:151]\n\tds_read_b64 a[238:239], v0 offset:2056\n\tv_mfma_f32_16x16x32_bf16 v[152:155], a[100:103], v[116:119], v[152:155]\n\tds_read_b64 a[240:241], v0 offset:1536\n\tv_mfma_f32_16x16x32_bf16 v[156:159], a[100:103], v[120:123], v[156:159]\n\tds_read_b64 a[242:243], v0 offset:1544\n\tv_mfma_f32_16x16x32_bf16 v[160:163], a[100:103], v[124:127], v[160:163]\n\t" \
    "ds_read_b64 a[244:245], v0 offset:1024\n\tv_mfma_f32_16x16x32_bf16 v[140:143], a[104:107], v[100:103], v[140:143]\n\tds_read_b64 a[246:247], v0 offset:1032\n\tv_mfma_f32_16x16x32_bf16 v[144:147], a[104:107], v[104:107], v[144:147]\n\tds_read_b128 v[68:71], v1 offset:37184\n\tv_mfma_f32_16x16x32_bf16 v[148:151], a[104:107], v[108:111], v[148:151]\n\tds_read_b128 v[72:75], v1 offset:37696\n\tv_mfma_f32_16x16x32_bf16 v[152:155], a[104:107], v[112:115], v[152:155]\n\t" \
    "ds_read_b128 v[76:79], v1 offset:38208\n\tv_mfma_f32_16x16x32_bf16 v[156:159], a[104:107], v[116:119], v[156:159]\n\tds_read_b128 v[80:83], v1 offset:38720\n\tv_mfma_f32_16x16x32_bf16 v[160:163], a[104:107], v[120:123], v[160:163]\n\tbuffer_load_dword v15, v7, s[16:19], s42 offen\n\tds_read_b128 v[84:87], v1 offset:39232\n\tv_mfma_f32_16x16x32_bf16 v[144:147], a[108:111], v[100:103], v[144:147]\n\ts_add_u32 s43, s43, 1\n\t" \
    "ds_read_b128 v[88:91], v1 offset:39744\n\tv_mfma_f32_16x16x32_bf16 v[148:151], a[108:111], v[104:107], v[148:151]\n\tv_readlane_b32 s42, v237, s43\n\ts_nop 4\n\tds_read_b128 v[92:95], v1 offset:40256\n\tv_mfma_f32_16x16x32_bf16 v[152:155], a[108:111], v[108:111], v[152:155]\n\tds_read_b128 v[96:99], v1 offset:40768\n\tv_mfma_f32_16x16x32_bf16 v[156:159], a[108:111], v[112:115], v[156:159]\n\t" \
    "ds_read_b64 a[248:249], v0 offset:512\n\tv_mfma_f32_16x16x32_bf16 v[160:163], a[108:111], v[116:119], v[160:163]\n\tds_read_b64 a[250:251], v0 offset:520\n\tv_mfma_f32_16x16x32_bf16 v[148:151], a[112:115], v[100:103], v[148:151]\n\tds_read_b64 a[252:253], v0 offset:0\n\tv_mfma_f32_16x16x32_bf16 v[152:155], a[112:115], v[104:107], v[152:155]\n\tds_read_b64 a[254:255], v0 offset:8\n\tv_mfma_f32_16x16x32_bf16 v[156:159], a[112:115], v[108:111], v[156:159]\n\t" \
    "v_mfma_f32_16x16x32_bf16 v[160:163], a[112:115], v[112:115], v[160:163]\n\tv_mfma_f32_16x16x32_bf16 v[152:155], a[116:119], v[100:103], v[152:155]\n\tv_mfma_f32_16x16x32_bf16 v[156:159], a[116:119], v[104:107], v[156:159]\n\tv_mfma_f32_16x16x32_bf16 v[160:163], a[116:119], v[108:111], v[160:163]\n\tv_mfma_f32_16x16x32_bf16 v[156:159], a[120:123], v[100:103], v[156:159]\n\tv_mfma_f32_16x16x32_bf16 v[160:163], a[120:123], v[104:107], v[160:163]\n\tv_mfma_f32_16x16x32_bf16 v[160:163], a[124:127], v[100:103], v[160:163]\n\ts_waitcnt lgkmcnt(4)\n\t" \
    "v_mfma_f32_16x16x32_bf16 v[132:135], a[128:131], v[68:71], v[132:135]\n\tds_read_b128 v[100:103], v1 offset:37120\n\ts_waitcnt vmcnt(9)\n\tv_alignbit_b32 v196, v45, v45, 16\n\tv_mfma_f32_16x16x32_bf16 v[136:139], a[128:131], v[72:75], v[136:139]\n\tds_read_b128 v[104:107], v1 offset:37632\n\tv_alignbit_b32 v197, v44, v44, 16\n\tv_mfma_f32_16x16x32_bf16 v[140:143], a[128:131], v[76:79], v[140:143]\n\t" \
    "ds_read_b128 v[108:111], v1 offset:38144\n\tv_alignbit_b32 v198, v43, v43, 16\n\tv_mfma_f32_16x16x32_bf16 v[144:147], a[128:131], v[80:83], v[144:147]\n\tds_read_b128 v[112:115], v1 offset:38656\n\tv_alignbit_b32 v199, v42, v42, 16\n\tv_mfma_f32_16x16x32_bf16 v[148:151], a[128:131], v[84:87], v[148:151]\n\tds_write_b128 v2, v[196:199] offset:3072\n\tv_mfma_f32_16x16x32_bf16 v[152:155], a[128:131], v[88:91], v[152:155]\n\t" \
    "ds_read_b128 v[116:119], v1 offset:39168\n\tv_bfi_b32 v200, s46, v45, v44\n\tv_mfma_f32_16x16x32_bf16 v[156:159], a[128:131], v[92:95], v[156:159]\n\tds_read_b128 v[120:123], v1 offset:39680\n\tv_bfi_b32 v201, s46, v44, v43\n\tv_mfma_f32_16x16x32_bf16 v[160:163], a[128:131], v[96:99], v[160:163]\n\tds_read_b128 v[124:127], v1 offset:40192\n\tv_bfi_b32 v202, s46, v43, v42\n\t" \
    "v_mfma_f32_16x16x32_bf16 v[136:139], a[132:135], v[68:71], v[136:139]\n\tds_read_b128 v[128:131], v1 offset:40704\n\tv_bfi_b32 v203, s46, v42, v59\n\tv_mfma_f32_16x16x32_bf16 v[140:143], a[132:135], v[72:75], v[140:143]\n\tds_write_b128 v2, v[200:203] offset:7232\n\tv_mfma_f32_16x16x32_bf16 v[144:147], a[132:135], v[76:79], v[144:147]\n\tv_alignbit_b32 v196, v44, v44, 16\n\tv_alignbit_b32 v197, v43, v43, 16\n\t" \
    "v_mfma_f32_16x16x32_bf16 v[148:151], a[132:135], v[80:83], v[148:151]\n\tv_alignbit_b32 v198, v42, v42, 16\n\tv_alignbit_b32 v199, v59, v59, 16\n\tv_mfma_f32_16x16x32_bf16 v[152:155], a[132:135], v[84:87], v[152:155]\n\tds_write_b128 v2, v[196:199] offset:11392\n\tv_mfma_f32_16x16x32_bf16 v[156:159], a[132:135], v[88:91], v[156:159]\n\tv_bfi_b32 v200, s46, v44, v43\n\tv_bfi_b32 v201, s46, v43, v42\n\t" \
    "v_mfma_f32_16x16x32_bf16 v[160:163], a[132:135], v[92:95], v[160:163]\n\tv_bfi_b32 v202, s46, v42, v59\n\tv_bfi_b32 v203, s46, v59, v58\n\tv_mfma_f32_16x16x32_bf16 v[140:143], a[136:139], v[68:71], v[140:143]\n\tds_write_b128 v2, v[200:203] offset:15552\n\tv_mfma_f32_16x16x32_bf16 v[144:147], a[136:139], v[72:75], v[144:147]\n\tv_alignbit_b32 v196, v49, v49, 16\n\tv_alignbit_b32 v197, v48, v48, 16\n\t" \
    "v_mfma_f32_16x16x32_bf16 v[148:151], a[136:139], v[76:79], v[148:151]\n\tv_alignbit_b32 v198, v47, v47, 16\n\tv_alignbit_b32 v199, v46, v46, 16\n\tv_mfma_f32_16x16x32_bf16 v[152:155], a[136:139], v[80:83], v[152:155]\n\tds_write_b128 v2, v[196:199] offset:2048\n\tv_mfma_f32_16x16x32_bf16 v[156:159], a[136:139], v[84:87], v[156:159]\n\tv_bfi_b32 v200, s46, v49, v48\n\tv_bfi_b32 v201, s46, v48, v47\n\t" \
    "v_mfma_f32_16x16x32_bf16 v[160:163], a[136:139], v[88:91], v[160:163]\n\tv_bfi_b32 v202, s46, v47, v46\n\tv_bfi_b32 v203, s46, v46, v61\n\tv_mfma_f32_16x16x32_bf16 v[144:147], a[140:143], v[68:71], v[144:147]\n\tds_write_b128 v2, v[200:203] offset:6208\n\tv_mfma_f32_16x16x32_bf16 v[148:151], a[140:143], v[72:75], v[148:151]\n\tv_alignbit_b32 v196, v48, v48, 16\n\tv_alignbit_b32 v197, v47, v47, 16\n\t" \
    "v_mfma_f32_16x16x32_bf16 v[152:155], a[140:143], v[76:79], v[152:155]\n\tv_alignbit_b32 v198, v46, v46, 16\n\tv_alignbit_b32 v199, v61, v61, 16\n\tv_mfma_f32_16x16x32_bf16 v[156:159], a[140:143], v[80:83], v[156:159]\n\tds_write_b128 v2, v[196:199] offset:10368\n\tv_mfma_f32_16x16x32_bf16 v[160:163], a[140:143], v[84:87], v[160:163]\n\tv_bfi_b32 v200, s46, v48, v47\n\tv_bfi_b32 v201, s46, v47, v46\n\t" \
    "v_mfma_f32_16x16x32_bf16 v[148:151], a[144:147], v[68:71], v[148:151]\n\tv_bfi_b32 v202, s46, v46, v61\n\tv_bfi_b32 v203, s46, v61, v60\n\tv_mfma_f32_16x16x32_bf16 v[152:155], a[144:147], v[72:75], v[152:155]\n\tds_write_b128 v2, v[200:203] offset:14528\n\tv_mfma_f32_16x16x32_bf16 v[156:159], a[144:147], v[76:79], v[156:159]\n\tv_alignbit_b32 v196, v53, v53, 16\n\tv_alignbit_b32 v197, v52, v52, 16\n\t" \
    "v_mfma_f32_16x16x32_bf16 v[160:163], a[144:147], v[80:83], v[160:163]\n\tv_alignbit_b32 v198, v51, v51, 16\n\tv_alignbit_b32 v199, v50, v50, 16\n\tv_mfma_f32_16x16x32_bf16 v[152:155], a[148:151], v[68:71], v[152:155]\n\tds_write_b128 v2, v[196:199] offset:1024\n\tv_mfma_f32_16x16x32_bf16 v[156:159], a[148:151], v[72:75], v[156:159]\n\tv_bfi_b32 v200, s46, v53, v52\n\tv_bfi_b32 v201, s46, v52, v51\n\t" \
    "v_mfma_f32_16x16x32_bf16 v[160:163], a[148:151], v[76:79], v[160:163]\n\tv_bfi_b32 v202, s46, v51, v50\n\tv_bfi_b32 v203, s46, v50, v63\n\tv_mfma_f32_16x16x32_bf16 v[156:159], a[152:155], v[68:71], v[156:159]\n\tds_write_b128 v2, v[200:203] offset:5184\n\tv_mfma_f32_16x16x32_bf16 v[160:163], a[152:155], v[72:75], v[160:163]\n\tv_alignbit_b32 v196, v52, v52, 16\n\tv_alignbit_b32 v197, v51, v51, 16\n\t" \
    "v_mfma_f32_16x16x32_bf16 v[160:163], a[156:159], v[68:71], v[160:163]\n\tv_alignbit_b32 v198, v50, v50, 16\n\tv_alignbit_b32 v199, v63, v63, 16\n\ts_waitcnt lgkmcnt(9)\n\tv_mfma_f32_16x16x32_bf16 v[132:135], a[160:163], v[100:103], v[132:135]\n\tds_write_b128 v2, v[196:199] offset:9344\n\tv_mfma_f32_16x16x32_bf16 v[136:139], a[160:163], v[104:107], v[136:139]\n\tds_read_b128 v[68:71], v1 offset:37056\n\t" \
    "v_bfi_b32 v200, s46, v52, v51\n\tv_mfma_f32_16x16x32_bf16 v[140:143], a[160:163], v[108:111], v[140:143]\n\tds_read_b128 v[72:75], v1 offset:37568\n\tv_bfi_b32 v201, s46, v51, v50\n\tv_mfma_f32_16x16x32_bf16 v[144:147], a[160:163], v[112:115], v[144:147]\n\tds_read_b128 v[76:79], v1 offset:38080\n\tv_bfi_b32 v202, s46, v50, v63\n\tv_mfma_f32_16x16x32_bf16 v[148:151], a[160:163], v[116:119], v[148:151]\n\t" \
    "ds_read_b128 v[80:83], v1 offset:38592\n\tv_bfi_b32 v203, s46, v63, v62\n\tv_mfma_f32_16x16x32_bf16 v[152:155], a[160:163], v[120:123], v[152:155]\n\tds_write_b128 v2, v[200:203] offset:13504\n\tv_mfma_f32_16x16x32_bf16 v[156:159], a[160:163], v[124:127], v[156:159]\n\tds_read_b128 v[84:87], v1 offset:39104\n\tv_alignbit_b32 v196, v57, v57, 16\n\tv_mfma_f32_16x16x32_bf16 v[160:163], a[160:163], v[128:131], v[160:163]\n\t" \
    "buffer_load_dword v15, v7, s[16:19], s42 offen\n\tds_read_b128 v[88:91], v1 offset:39616\n\tv_mfma_f32_16x16x32_bf16 v[136:139], a[164:167], v[100:103], v[136:139]\n\ts_add_u32 s43, s43, 1\n\tds_read_b128 v[92:95], v1 offset:40128\n\tv_alignbit_b32 v197, v56, v56, 16\n\tv_mfma_f32_16x16x32_bf16 v[140:143], a[164:167], v[104:107], v[140:143]\n\tv_readlane_b32 s42, v237, s43\n\t" \
    "s_nop 4\n\tds_read_b128 v[96:99], v1 offset:40640\n\tv_alignbit_b32 v198, v55, v55, 16\n\tv_mfma_f32_16x16x32_bf16 v[144:147], a[164:167], v[108:111], v[144:147]\n\tv_alignbit_b32 v199, v54, v54, 16\n\tv_mfma_f32_16x16x32_bf16 v[148:151], a[164:167], v[112:115], v[148:151]\n\tds_write_b128 v2, v[196:199] offset:0\n\tv_mfma_f32_16x16x32_bf16 v[152:155], a[164:167], v[116:119], v[152:155]\n\t" \
    "v_bfi_b32 v200, s46, v57, v56\n\tv_bfi_b32 v201, s46, v56, v55\n\tv_mfma_f32_16x16x32_bf16 v[156:159], a[164:167], v[120:123], v[156:159]\n\tv_bfi_b32 v202, s46, v55, v54\n\tv_bfi_b32 v203, s46, v54, v65\n\tv_mfma_f32_16x16x32_bf16 v[160:163], a[164:167], v[124:127], v[160:163]\n\tds_write_b128 v2, v[200:203] offset:4160\n\tv_mfma_f32_16x16x32_bf16 v[140:143], a[168:171], v[100:103], v[140:143]\n\t" \
    "v_alignbit_b32 v196, v56, v56, 16\n\tv_alignbit_b32 v197, v55, v55, 16\n\tv_mfma_f32_16x16x32_bf16 v[144:147], a[168:171], v[104:107], v[144:147]\n\tv_alignbit_b32 v198, v54, v54, 16\n\tv_alignbit_b32 v199, v65, v65, 16\n\tv_mfma_f32_16x16x32_bf16 v[148:151], a[168:171], v[108:111], v[148:151]\n\tds_write_b128 v2, v[196:199] offset:8320\n\tv_mfma_f32_16x16x32_bf16 v[152:155], a[168:171], v[112:115], v[152:155]\n\t" \
    "v_bfi_b32 v200, s46, v56, v55\n\tv_bfi_b32 v201, s46, v55, v54\n\tv_mfma_f32_16x16x32_bf16 v[156:159], a[168:171], v[116:119], v[156:159]\n\tv_bfi_b32 v202, s46, v54, v65\n\tv_bfi_b32 v203, s46, v65, v64\n\tv_mfma_f32_16x16x32_bf16 v[160:163], a[168:171], v[120:123], v[160:163]\n\tds_write_b128 v2, v[200:203] offset:12480\n\tv_mfma_f32_16x16x32_bf16 v[144:147], a[172:175], v[100:103], v[144:147]\n\t" \
    "v_lshl_or_b32 v66, v66, 16, v66\n\tv_and_b32 v228, v66, v232\n\tv_mfma_f32_16x16x32_bf16 v[148:151], a[172:175], v[104:107], v[148:151]\n\tv_and_b32 v229, v66, v233\n\tv_and_b32 v230, v66, v234\n\tv_mfma_f32_16x16x32_bf16 v[152:155], a[172:175], v[108:111], v[152:155]\n\tv_and_b32 v231, v66, v235\n\tv_mfma_f32_16x16x32_bf16 v[156:159], a[172:175], v[112:115], v[156:159]\n\t" \
    "v_mfma_f32_16x16x32_bf16 v[160:163], a[172:175], v[116:119], v[160:163]\n\tv_mfma_f32_16x16x32_bf16 v[148:151], a[176:179], v[100:103], v[148:151]\n\tv_mfma_f32_16x16x32_bf16 v[152:155], a[176:179], v[104:107], v[152:155]\n\tv_mfma_f32_16x16x32_bf16 v[156:159], a[176:179], v[108:111], v[156:159]\n\tv_mfma_f32_16x16x32_bf16 v[160:163], a[176:179], v[112:115], v[160:163]\n\tv_mfma_f32_16x16x32_bf16 v[152:155], a[180:183], v[100:103], v[152:155]\n\tv_mfma_f32_16x16x32_bf16 v[156:159], a[180:183], v[104:107], v[156:159]\n\tv_mfma_f32_16x16x32_bf16 v[160:163], a[180:183], v[108:111], v[160:163]\n\t" \
    "v_mfma_f32_16x16x32_bf16 v[156:159], a[184:187], v[100:103], v[156:159]\n\tv_mfma_f32_16x16x32_bf16 v[160:163], a[184:187], v[104:107], v[160:163]\n\tv_mfma_f32_16x16x32_bf16 v[160:163], a[188:191], v[100:103], v[160:163]\n\ts_waitcnt lgkmcnt(4)\n\tv_mfma_f32_16x16x32_bf16 v[132:135], a[192:195], v[68:71], v[132:135]\n\tds_read_b128 v[100:103], v1 offset:36992\n\tv_mfma_f32_16x16x32_bf16 v[136:139], a[192:195], v[72:75], v[136:139]\n\tds_read_b128 v[104:107], v1 offset:37504\n\t" \
    "v_mfma_f32_16x16x32_bf16 v[140:143], a[192:195], v[76:79], v[140:143]\n\tds_read_b128 v[108:111], v1 offset:38016\n\tv_mfma_f32_16x16x32_bf16 v[144:147], a[192:195], v[80:83], v[144:147]\n\tds_read_b128 v[112:115], v1 offset:38528\n\tv_mfma_f32_16x16x32_bf16 v[148:151], a[192:195], v[84:87], v[148:151]\n\tds_read_b128 v[116:119], v1 offset:39040\n\tv_mfma_f32_16x16x32_bf16 v[152:155], a[192:195], v[88:91], v[152:155]\n\tds_read_b128 v[120:123], v1 offset:39552\n\t" \
    "v_mfma_f32_16x16x32_bf16 v[156:159], a[192:195], v[92:95], v[156:159]\n\tds_read_b128 v[124:127], v1 offset:40064\n\tv_mfma_f32_16x16x32_bf16 v[160:163], a[192:195], v[96:99], v[160:163]\n\tds_read_b128 v[128:131], v1 offset:40576\n\tv_mfma_f32_16x16x32_bf16 v[136:139], a[196:199], v[68:71], v[136:139]\n\tv_mfma_f32_16x16x32_bf16 v[140:143], a[196:199], v[72:75], v[140:143]\n\tv_mfma_f32_16x16x32_bf16 v[144:147], a[196:199], v[76:79], v[144:147]\n\tv_mfma_f32_16x16x32_bf16 v[148:151], a[196:199], v[80:83], v[148:151]\n\t" \
    "v_mfma_f32_16x16x32_bf16 v[152:155], a[196:199], v[84:87], v[152:155]\n\tv_mfma_f32_16x16x32_bf16 v[156:159], a[196:199], v[88:91], v[156:159]\n\tv_mfma_f32_16x16x32_bf16 v[160:163], a[196:199], v[92:95], v[160:163]\n\tv_mfma_f32_16x16x32_bf16 v[140:143], a[200:203], v[68:71], v[140:143]\n\tv_mfma_f32_16x16x32_bf16 v[144:147], a[200:203], v[72:75], v[144:147]\n\tv_mfma_f32_16x16x32_bf16 v[148:151], a[200:203], v[76:79], v[148:151]\n\tv_mfma_f32_16x16x32_bf16 v[152:155], a[200:203], v[80:83], v[152:155]\n\tv_mfma_f32_16x16x32_bf16 v[156:159], a[200:203], v[84:87], v[156:159]\n\t" \
    "v_mfma_f32_16x16x32_bf16 v[160:163], a[200:203], v[88:91], v[160:163]\n\tv_mfma_f32_16x16x32_bf16 v[144:147], a[204:207], v[68:71], v[144:147]\n\tv_mfma_f32_16x16x32_bf16 v[148:151], a[204:207], v[72:75], v[148:151]\n\tv_mfma_f32_16x16x32_bf16 v[152:155], a[204:207], v[76:79], v[152:155]\n\tv_mfma_f32_16x16x32_bf16 v[156:159], a[204:207], v[80:83], v[156:159]\n\tv_mfma_f32_16x16x32_bf16 v[160:163], a[204:207], v[84:87], v[160:163]\n\tv_mfma_f32_16x16x32_bf16 v[148:151], a[208:211], v[68:71], v[148:151]\n\tv_mfma_f32_16x16x32_bf16 v[152:155], a[208:211], v[72:75], v[152:155]\n\t" \
    "v_mfma_f32_16x16x32_bf16 v[156:159], a[208:211], v[76:79], v[156:159]\n\tv_mfma_f32_16x16x32_bf16 v[160:163], a[208:211], v[80:83], v[160:163]\n\tv_mfma_f32_16x16x32_bf16 v[152:155], a[212:215], v[68:71], v[152:155]\n\tbuffer_load_dword v15, v7, s[16:19], s42 offen\n\tv_mfma_f32_16x16x32_bf16 v[156:159], a[212:215], v[72:75], v[156:159]\n\ts_add_u32 s43, s43, 1\n\tv_mfma_f32_16x16x32_bf16 v[160:163], a[212:215], v[76:79], v[160:163]\n\tv_readlane_b32 s42, v237, s43\n\t" \
    "s_nop 4\n\tv_mfma_f32_16x16x32_bf16 v[156:159], a[216:219], v[68:71], v[156:159]\n\tv_mfma_f32_16x16x32_bf16 v[160:163], a[216:219], v[72:75], v[160:163]\n\tv_mfma_f32_16x16x32_bf16 v[160:163], a[220:223], v[68:71], v[160:163]\n\ts_waitcnt lgkmcnt(0)\n\tv_mfma_f32_16x16x32_bf16 v[132:135], a[224:227], v[100:103], v[132:135]\n\ts_waitcnt vmcnt(3)\n\tbuffer_store_dwordx2 v[248:249], off, s[8:11], s51 sc1\n\t" \
    "ds_read_b64 a[0:1], v0 offset:4032\n\tv_mfma_f32_16x16x32_bf16 v[136:139], a[224:227], v[104:107], v[136:139]\n\tds_read_b64 a[2:3], v0 offset:4040\n\tv_mfma_f32_16x16x32_bf16 v[140:143], a[224:227], v[108:111], v[140:143]\n\tds_read_b64 a[4:5], v0 offset:3520\n\tv_mfma_f32_16x16x32_bf16 v[144:147], a[224:227], v[112:115], v[144:147]\n\tds_read_b64 a[6:7], v0 offset:3528\n\tv_mfma_f32_16x16x32_bf16 v[148:151], a[224:227], v[116:119], v[148:151]\n\t" \
    "ds_read_b64 a[8:9], v0 offset:3008\n\tv_mfma_f32_16x16x32_bf16 v[152:155], a[224:227], v[120:123], v[152:155]\n\tds_read_b64 a[10:11], v0 offset:3016\n\tv_mfma_f32_16x16x32_bf16 v[156:159], a[224:227], v[124:127], v[156:159]\n\tds_read_b64 a[12:13], v0 offset:2496\n\tv_mfma_f32_16x16x32_bf16 v[160:163], a[224:227], v[128:131], v[160:163]\n\tds_read_b64 a[14:15], v0 offset:2504\n\tv_mfma_f32_16x16x32_bf16 v[136:139], a[228:231], v[100:103], v[136:139]\n\t" \
    "ds_read_b64 a[16:17], v0 offset:1984\n\tv_mfma_f32_16x16x32_bf16 v[140:143], a[228:231], v[104:107], v[140:143]\n\tds_read_b64 a[18:19], v0 offset:1992\n\tv_mfma_f32_16x16x32_bf16 v[144:147], a[228:231], v[108:111], v[144:147]\n\tds_read_b64 a[20:21], v0 offset:1472\n\tv_mfma_f32_16x16x32_bf16 v[148:151], a[228:231], v[112:115], v[148:151]\n\tds_read_b64 a[22:23], v0 offset:1480\n\tv_mfma_f32_16x16x32_bf16 v[152:155], a[228:231], v[116:119], v[152:155]\n\t" \
    "ds_read_b64 a[24:25], v0 offset:960\n\tv_mfma_f32_16x16x32_bf16 v[156:159], a[228:231], v[120:123], v[156:159]\n\tds_read_b64 a[26:27], v0 offset:968\n\tv_mfma_f32_16x16x32_bf16 v[160:163], a[228:231], v[124:127], v[160:163]\n\tds_read_b64 a[28:29], v0 offset:448\n\tv_mfma_f32_16x16x32_bf16 v[140:143], a[232:235], v[100:103], v[140:143]\n\tds_read_b64 a[30:31], v0 offset:456\n\tv_mfma_f32_16x16x32_bf16 v[144:147], a[232:235], v[104:107], v[144:147]\n\t" \
    "ds_read_b128 v[68:71], v1 offset:55936\n\tv_mfma_f32_16x16x32_bf16 v[148:151], a[232:235], v[108:111], v[148:151]\n\tds_read_b128 v[72:75], v1 offset:56448\n\tv_mfma_f32_16x16x32_bf16 v[152:155], a[232:235], v[112:115], v[152:155]\n\tds_read_b128 v[76:79], v1 offset:56960\n\tv_mfma_f32_16x16x32_bf16 v[156:159], a[232:235], v[116:119], v[156:159]\n\tds_read_b128 v[80:83], v1 offset:57472\n\tv_mfma_f32_16x16x32_bf16 v[160:163], a[232:235], v[120:123], v[160:163]\n\t" \
    "ds_read_b128 v[84:87], v1 offset:57984\n\tv_mfma_f32_16x16x32_bf16 v[144:147], a[236:239], v[100:103], v[144:147]\n\tds_read_b128 v[88:91], v1 offset:58496\n\tv_mfma_f32_16x16x32_bf16 v[148:151], a[236:239], v[104:107], v[148:151]\n\tds_read_b128 v[92:95], v1 offset:59008\n\tv_mfma_f32_16x16x32_bf16 v[152:155], a[236:239], v[108:111], v[152:155]\n\tds_read_b128 v[96:99], v1 offset:59520\n\tv_mfma_f32_16x16x32_bf16 v[156:159], a[236:239], v[112:115], v[156:159]\n\t" \
    "v_mfma_f32_16x16x32_bf16 v[160:163], a[236:239], v[116:119], v[160:163]\n\tv_mfma_f32_16x16x32_bf16 v[148:151], a[240:243], v[100:103], v[148:151]\n\tv_mfma_f32_16x16x32_bf16 v[152:155], a[240:243], v[104:107], v[152:155]\n\tv_mfma_f32_16x16x32_bf16 v[156:159], a[240:243], v[108:111], v[156:159]\n\tv_mfma_f32_16x16x32_bf16 v[160:163], a[240:243], v[112:115], v[160:163]\n\tv_mfma_f32_16x16x32_bf16 v[152:155], a[244:247], v[100:103], v[152:155]\n\tv_mfma_f32_16x16x32_bf16 v[156:159], a[244:247], v[104:107], v[156:159]\n\tv_mfma_f32_16x16x32_bf16 v[160:163], a[244:247], v[108:111], v[160:163]\n\t" \
    "v_mfma_f32_16x16x32_bf16 v[156:159], a[248:251], v[100:103], v[156:159]\n\tv_mfma_f32_16x16x32_bf16 v[160:163], a[248:251], v[104:107], v[160:163]\n\tv_mfma_f32_16x16x32_bf16 v[160:163], a[252:255], v[100:103], v[160:163]\n\ts_waitcnt lgkmcnt(0)\n\tv_mfma_f32_16x16x32_bf16 v[164:167], v[228:231], v[68:71], 0\n\tds_read_b64 a[32:33], v0 offset:3968\n\tds_read_b64 a[34:35], v0 offset:3976\n\tv_mfma_f32_16x16x32_bf16 v[168:171], v[228:231], v[72:75], 0\n\t" \
    "ds_read_b64 a[36:37], v0 offset:3456\n\tds_read_b64 a[38:39], v0 offset:3464\n\tv_mfma_f32_16x16x32_bf16 v[172:175], v[228:231], v[76:79], 0\n\tds_read_b64 a[40:41], v0 offset:2944\n\tds_read_b64 a[42:43], v0 offset:2952\n\tv_mfma_f32_16x16x32_bf16 v[176:179], v[228:231], v[80:83], 0\n\tds_read_b64 a[44:45], v0 offset:2432\n\tds_read_b64 a[46:47], v0 offset:2440\n\t" \
    "v_mfma_f32_16x16x32_bf16 v[180:183], v[228:231], v[84:87], 0\n\tds_read_b64 a[48:49], v0 offset:1920\n\tds_read_b64 a[50:51], v0 offset:1928\n\tv_mfma_f32_16x16x32_bf16 v[184:187], v[228:231], v[88:91], 0\n\tds_read_b64 a[52:53], v0 offset:1408\n\tv_cvt_pk_bf16_f32 v132, v132, v133\n\tv_mfma_f32_16x16x32_bf16 v[188:191], v[228:231], v[92:95], 0\n\tds_read_b64 a[54:55], v0 offset:1416\n\t" \
    "v_cvt_pk_bf16_f32 v133, v134, v135\n\tv_mfma_f32_16x16x32_bf16 v[192:195], v[228:231], v[96:99], 0\n\tds_read_b64 a[56:57], v0 offset:896\n\tds_write_b64 v3, v[132:133] offset:36992\n\tv_mfma_f32_16x16x32_bf16 v[164:167], a[0:3], v[68:71], v[164:167]\n\tds_read_b64 a[58:59], v0 offset:904\n\tv_cvt_pk_bf16_f32 v136, v136, v137\n\tv_mfma_f32_16x16x32_bf16 v[168:171], a[0:3], v[72:75], v[168:171]\n\t" \
    "ds_read_b64 a[60:61], v0 offset:384\n\tv_cvt_pk_bf16_f32 v137, v138, v139\n\tv_mfma_f32_16x16x32_bf16 v[172:175], a[0:3], v[76:79], v[172:175]\n\tds_read_b64 a[62:63], v0 offset:392\n\tds_write_b64 v3, v[136:137] offset:37504\n\tv_mfma_f32_16x16x32_bf16 v[176:179], a[0:3], v[80:83], v[176:179]\n\tds_read_b128 v[100:103], v1 offset:55872\n\tv_cvt_pk_bf16_f32 v140, v140, v141\n\t" \
    "v_mfma_f32_16x16x32_bf16 v[180:183], a[0:3], v[84:87], v[180:183]\n\tds_read_b128 v[104:107], v1 offset:56384\n\tv_cvt_pk_bf16_f32 v141, v142, v143\n\tv_mfma_f32_16x16x32_bf16 v[184:187], a[0:3], v[88:91], v[184:187]\n\tds_read_b128 v[108:111], v1 offset:56896\n\tds_write_b64 v3, v[140:141] offset:38016\n\tv_mfma_f32_16x16x32_bf16 v[188:191], a[0:3], v[92:95], v[188:191]\n\tds_read_b128 v[112:115], v1 offset:57408\n\t" \
    "v_cvt_pk_bf16_f32 v144, v144, v145\n\tv_mfma_f32_16x16x32_bf16 v[192:195], a[0:3], v[96:99], v[192:195]\n\tds_read_b128 v[116:119], v1 offset:57920\n\tv_cvt_pk_bf16_f32 v145, v146, v147\n\tv_mfma_f32_16x16x32_bf16 v[168:171], a[4:7], v[68:71], v[168:171]\n\tds_read_b128 v[120:123], v1 offset:58432\n\tds_write_b64 v3, v[144:145] offset:38528\n\tv_mfma_f32_16x16x32_bf16 v[172:175], a[4:7], v[72:75], v[172:175]\n\t" \
    "ds_read_b128 v[124:127], v1 offset:58944\n\tv_cvt_pk_bf16_f32 v148, v148, v149\n\tv_mfma_f32_16x16x32_bf16 v[176:179], a[4:7], v[76:79], v[176:179]\n\tds_read_b128 v[128:131], v1 offset:59456\n\tv_cvt_pk_bf16_f32 v149, v150, v151\n\tv_mfma_f32_16x16x32_bf16 v[180:183], a[4:7], v[80:83], v[180:183]\n\tds_read_b64 a[64:65], v0 offset:3904\n\tds_write_b64 v3, v[148:149] offset:39040\n\t" \
    "v_mfma_f32_16x16x32_bf16 v[184:187], a[4:7], v[84:87], v[184:187]\n\tds_read_b64 a[66:67], v0 offset:3912\n\tv_cvt_pk_bf16_f32 v152, v152, v153\n\tv_mfma_f32_16x16x32_bf16 v[188:191], a[4:7], v[88:91], v[188:191]\n\tds_read_b64 a[68:69], v0 offset:3392\n\tv_cvt_pk_bf16_f32 v153, v154, v155\n\tv_mfma_f32_16x16x32_bf16 v[192:195], a[4:7], v[92:95], v[192:195]\n\tbuffer_load_dword v15, v7, s[16:19], s42 offen\n\t" \
    "ds_read_b64 a[70:71], v0 offset:3400\n\tv_mfma_f32_16x16x32_bf16 v[172:175], a[8:11], v[68:71], v[172:175]\n\ts_add_u32 s43, s43, 1\n\tds_read_b64 a[72:73], v0 offset:2880\n\tds_write_b64 v3, v[152:153] offset:39552\n\tv_mfma_f32_16x16x32_bf16 v[176:179], a[8:11], v[72:75], v[176:179]\n\tv_readlane_b32 s42, v237, s43\n\ts_nop 4\n\t" \
    "ds_read_b64 a[74:75], v0 offset:2888\n\tv_cvt_pk_bf16_f32 v156, v156, v157\n\tv_mfma_f32_16x16x32_bf16 v[180:183], a[8:11], v[76:79], v[180:183]\n\tds_read_b64 a[76:77], v0 offset:2368\n\tv_cvt_pk_bf16_f32 v157, v158, v159\n\tv_mfma_f32_16x16x32_bf16 v[184:187], a[8:11], v[80:83], v[184:187]\n\tds_read_b64 a[78:79], v0 offset:2376\n\tds_write_b64 v3, v[156:157] offset:40064\n\t" \
    "v_mfma_f32_16x16x32_bf16 v[188:191], a[8:11], v[84:87], v[188:191]\n\tds_read_b64 a[80:81], v0 offset:1856\n\tv_cvt_pk_bf16_f32 v160, v160, v161\n\tv_mfma_f32_16x16x32_bf16 v[192:195], a[8:11], v[88:91], v[192:195]\n\tds_read_b64 a[82:83], v0 offset:1864\n\tv_cvt_pk_bf16_f32 v161, v162, v163\n\tv_mfma_f32_16x16x32_bf16 v[176:179], a[12:15], v[68:71], v[176:179]\n\tds_read_b64 a[84:85], v0 offset:1344\n\t" \
    "ds_write_b64 v3, v[160:161] offset:40576\n\tv_mfma_f32_16x16x32_bf16 v[180:183], a[12:15], v[72:75], v[180:183]\n\tds_read_b64 a[86:87], v0 offset:1352\n\tv_mfma_f32_16x16x32_bf16 v[184:187], a[12:15], v[76:79], v[184:187]\n\tds_read_b64 a[88:89], v0 offset:832\n\tv_mfma_f32_16x16x32_bf16 v[188:191], a[12:15], v[80:83], v[188:191]\n\tds_read_b64 a[90:91], v0 offset:840\n\tv_mfma_f32_16x16x32_bf16 v[192:195], a[12:15], v[84:87], v[192:195]\n\t" \
    "ds_read_b64 a[92:93], v0 offset:320\n\tv_mfma_f32_16x16x32_bf16 v[180:183], a[16:19], v[68:71], v[180:183]\n\tds_read_b64 a[94:95], v0 offset:328\n\tv_mfma_f32_16x16x32_bf16 v[184:187], a[16:19], v[72:75], v[184:187]\n\tds_read_b64 a[96:97], v0 offset:3840\n\tv_mfma_f32_16x16x32_bf16 v[188:191], a[16:19], v[76:79], v[188:191]\n\tds_read_b64 a[98:99], v0 offset:3848\n\tv_mfma_f32_16x16x32_bf16 v[192:195], a[16:19], v[80:83], v[192:195]\n\t" \
    "ds_read_b64 a[100:101], v0 offset:3328\n\tv_mfma_f32_16x16x32_bf16 v[184:187], a[20:23], v[68:71], v[184:187]\n\tds_read_b64 a[102:103], v0 offset:3336\n\tv_mfma_f32_16x16x32_bf16 v[188:191], a[20:23], v[72:75], v[188:191]\n\tv_mfma_f32_16x16x32_bf16 v[192:195], a[20:23], v[76:79], v[192:195]\n\tv_mfma_f32_16x16x32_bf16 v[188:191], a[24:27], v[68:71], v[188:191]\n\tv_mfma_f32_16x16x32_bf16 v[192:195], a[24:27], v[72:75], v[192:195]\n\tv_mfma_f32_16x16x32_bf16 v[192:195], a[28:31], v[68:71], v[192:195]\n\t" \
    "s_waitcnt lgkmcnt(9)\n\ts_barrier\n\tv_mfma_f32_16x16x32_bf16 v[164:167], a[32:35], v[100:103], v[164:167]\n\tds_read_b64 a[104:105], v0 offset:2816\n\tds_read_b64 a[106:107], v0 offset:2824\n\tv_mfma_f32_16x16x32_bf16 v[168:171], a[32:35], v[104:107], v[168:171]\n\tds_read_b64 a[108:109], v0 offset:2304\n\tds_read_b64 a[110:111], v0 offset:2312\n\t" \
    "v_mfma_f32_16x16x32_bf16 v[172:175], a[32:35], v[108:111], v[172:175]\n\tds_read_b64 a[112:113], v0 offset:1792\n\tds_read_b64 a[114:115], v0 offset:1800\n\tv_mfma_f32_16x16x32_bf16 v[176:179], a[32:35], v[112:115], v[176:179]\n\tds_read_b64 a[116:117], v0 offset:1280\n\tds_read_b64 a[118:119], v0 offset:1288\n\tv_mfma_f32_16x16x32_bf16 v[180:183], a[32:35], v[116:119], v[180:183]\n\tds_read_b64 a[120:121], v0 offset:768\n\t" \
    "ds_read_b64 a[122:123], v0 offset:776\n\tv_mfma_f32_16x16x32_bf16 v[184:187], a[32:35], v[120:123], v[184:187]\n\tds_read_b64 a[124:125], v0 offset:256\n\ts_add_u32 s51, s44, 8\n\tv_mfma_f32_16x16x32_bf16 v[188:191], a[32:35], v[124:127], v[188:191]\n\tds_read_b64 a[126:127], v0 offset:264\n\ts_mul_i32 s65, s51, 1366\n\tv_mfma_f32_16x16x32_bf16 v[192:195], a[32:35], v[128:131], v[192:195]\n\t" \
    "ds_read_b64 a[128:129], v0 offset:3776\n\ts_lshr_b32 s65, s65, 16\n\tv_mfma_f32_16x16x32_bf16 v[168:171], a[36:39], v[100:103], v[168:171]\n\tds_read_b64 a[130:131], v0 offset:3784\n\ts_mul_i32 s56, s65, 48\n\tv_mfma_f32_16x16x32_bf16 v[172:175], a[36:39], v[104:107], v[172:175]\n\tds_read_b64 a[132:133], v0 offset:3264\n\ts_sub_u32 s56, s51, s56\n\t" \
    "v_mfma_f32_16x16x32_bf16 v[176:179], a[36:39], v[108:111], v[176:179]\n\tds_read_b64 a[134:135], v0 offset:3272\n\ts_lshl_b32 s56, s56, 6\n\tv_mfma_f32_16x16x32_bf16 v[180:183], a[36:39], v[112:115], v[180:183]\n\tds_read_b128 v[68:71], v1 offset:55808\n\ts_lshr_b32 s70, s65, 2\n\tv_mfma_f32_16x16x32_bf16 v[184:187], a[36:39], v[116:119], v[184:187]\n\tds_read_b128 v[72:75], v1 offset:56320\n\t" \
    "s_mul_i32 s70, s70, 252\n\tv_mfma_f32_16x16x32_bf16 v[188:191], a[36:39], v[120:123], v[188:191]\n\tds_read_b128 v[76:79], v1 offset:56832\n\ts_add_u32 s65, s65, s70\n\tv_mfma_f32_16x16x32_bf16 v[192:195], a[36:39], v[124:127], v[192:195]\n\tds_read_b128 v[80:83], v1 offset:57344\n\ts_mul_i32 s65, s65, 3072\n\tv_mfma_f32_16x16x32_bf16 v[172:175], a[40:43], v[100:103], v[172:175]\n\t" \
    "ds_read_b128 v[84:87], v1 offset:57856\n\ts_add_u32 s56, s56, s65\n\tv_mfma_f32_16x16x32_bf16 v[176:179], a[40:43], v[104:107], v[176:179]\n\tds_read_b128 v[88:91], v1 offset:58368\n\ts_add_u32 s56, s56, 4849664\n\tv_mfma_f32_16x16x32_bf16 v[180:183], a[40:43], v[108:111], v[180:183]\n\tds_read_b128 v[92:95], v1 offset:58880\n\ts_add_u32 s51, s51, s38\n\t" \
    "v_mfma_f32_16x16x32_bf16 v[184:187], a[40:43], v[112:115], v[184:187]\n\tds_read_b128 v[96:99], v1 offset:59392\n\ts_lshl_b32 s51, s51, 3\n\tv_mfma_f32_16x16x32_bf16 v[188:191], a[40:43], v[116:119], v[188:191]\n\tds_read_b64 a[136:137], v0 offset:2752\n\ts_add_u32 s51, s51, 50176\n\tds_read_b64_tr_b16 v[204:205], v4 offset:36992\n\tv_mfma_f32_16x16x32_bf16 v[192:195], a[40:43], v[120:123], v[192:195]\n\t" \
    "ds_read_b64 a[138:139], v0 offset:2760\n\tds_read_b64_tr_b16 v[206:207], v4 offset:37000\n\tv_mfma_f32_16x16x32_bf16 v[176:179], a[44:47], v[100:103], v[176:179]\n\tds_read_b64 a[140:141], v0 offset:2240\n\tds_read_b64_tr_b16 v[208:209], v4 offset:37056\n\tv_mfma_f32_16x16x32_bf16 v[180:183], a[44:47], v[104:107], v[180:183]\n\tds_read_b64 a[142:143], v0 offset:2248\n\tds_read_b64_tr_b16 v[210:211], v4 offset:37064\n\t" \
    "v_mfma_f32_16x16x32_bf16 v[184:187], a[44:47], v[108:111], v[184:187]\n\tds_read_b64 a[144:145], v0 offset:1728\n\tds_read_b64_tr_b16 v[212:213], v4 offset:37504\n\tv_mfma_f32_16x16x32_bf16 v[188:191], a[44:47], v[112:115], v[188:191]\n\tds_read_b64 a[146:147], v0 offset:1736\n\tds_read_b64_tr_b16 v[214:215], v4 offset:37512\n\tv_mfma_f32_16x16x32_bf16 v[192:195], a[44:47], v[116:119], v[192:195]\n\tds_read_b64 a[148:149], v0 offset:1216\n\t" \
    "ds_read_b64_tr_b16 v[216:217], v4 offset:37568\n\tv_mfma_f32_16x16x32_bf16 v[180:183], a[48:51], v[100:103], v[180:183]\n\tds_read_b64 a[150:151], v0 offset:1224\n\tds_read_b64_tr_b16 v[218:219], v4 offset:37576\n\tv_mfma_f32_16x16x32_bf16 v[184:187], a[48:51], v[104:107], v[184:187]\n\tds_read_b64 a[152:153], v0 offset:704\n\tv_mfma_f32_16x16x32_bf16 v[188:191], a[48:51], v[108:111], v[188:191]\n\tds_read_b64 a[154:155], v0 offset:712\n\t" \
    "v_mfma_f32_16x16x32_bf16 v[192:195], a[48:51], v[112:115], v[192:195]\n\tds_read_b64 a[156:157], v0 offset:192\n\tv_mfma_f32_16x16x32_bf16 v[184:187], a[52:55], v[100:103], v[184:187]\n\tds_read_b64 a[158:159], v0 offset:200\n\tv_mfma_f32_16x16x32_bf16 v[188:191], a[52:55], v[104:107], v[188:191]\n\tv_mfma_f32_16x16x32_bf16 v[192:195], a[52:55], v[108:111], v[192:195]\n\ts_waitcnt lgkmcnt(15)\n\tbuffer_store_dwordx4 v[204:207], v5, s[8:11], s56 offen sc1\n\t" \
    "s_add_u32 s56, s56, 49152\n\tv_mfma_f32_16x16x32_bf16 v[188:191], a[56:59], v[100:103], v[188:191]\n\tv_mfma_f32_16x16x32_bf16 v[192:195], a[56:59], v[104:107], v[192:195]\n\tv_mfma_f32_16x16x32_bf16 v[192:195], a[60:63], v[100:103], v[192:195]\n\tv_mfma_f32_16x16x32_bf16 v[164:167], a[64:67], v[68:71], v[164:167]\n\tds_read_b64 a[160:161], v0 offset:3712\n\tds_read_b64 a[162:163], v0 offset:3720\n\tv_mfma_f32_16x16x32_bf16 v[168:171], a[64:67], v[72:75], v[168:171]\n\t" \
    "ds_read_b64 a[164:165], v0 offset:3200\n\tds_read_b64 a[166:167], v0 offset:3208\n\tv_mfma_f32_16x16x32_bf16 v[172:175], a[64:67], v[76:79], v[172:175]\n\tds_read_b64 a[168:169], v0 offset:2688\n\tds_read_b64 a[170:171], v0 offset:2696\n\tv_mfma_f32_16x16x32_bf16 v[176:179], a[64:67], v[80:83], v[176:179]\n\tds_read_b64 a[172:173], v0 offset:2176\n\tds_read_b64 a[174:175], v0 offset:2184\n\t" \
    "v_mfma_f32_16x16x32_bf16 v[180:183], a[64:67], v[84:87], v[180:183]\n\tds_read_b64 a[176:177], v0 offset:1664\n\tds_read_b64 a[178:179], v0 offset:1672\n\tv_mfma_f32_16x16x32_bf16 v[184:187], a[64:67], v[88:91], v[184:187]\n\tds_read_b64 a[180:181], v0 offset:1152\n\tv_mfma_f32_16x16x32_bf16 v[188:191], a[64:67], v[92:95], v[188:191]\n\tds_read_b64 a[182:183], v0 offset:1160\n\tv_mfma_f32_16x16x32_bf16 v[192:195], a[64:67], v[96:99], v[192:195]\n\t" \
    "ds_read_b64 a[184:185], v0 offset:640\n\tv_mfma_f32_16x16x32_bf16 v[168:171], a[68:71], v[68:71], v[168:171]\n\tds_read_b64 a[186:187], v0 offset:648\n\tv_mfma_f32_16x16x32_bf16 v[172:175], a[68:71], v[72:75], v[172:175]\n\tds_read_b64 a[188:189], v0 offset:128\n\tv_mfma_f32_16x16x32_bf16 v[176:179], a[68:71], v[76:79], v[176:179]\n\tds_read_b64 a[190:191], v0 offset:136\n\tv_mfma_f32_16x16x32_bf16 v[180:183], a[68:71], v[80:83], v[180:183]\n\t" \
    "ds_read_b128 v[100:103], v1 offset:55744\n\tv_mfma_f32_16x16x32_bf16 v[184:187], a[68:71], v[84:87], v[184:187]\n\tds_read_b128 v[104:107], v1 offset:56256\n\tv_mfma_f32_16x16x32_bf16 v[188:191], a[68:71], v[88:91], v[188:191]\n\tds_read_b128 v[108:111], v1 offset:56768\n\tv_mfma_f32_16x16x32_bf16 v[192:195], a[68:71], v[92:95], v[192:195]\n\tds_read_b128 v[112:115], v1 offset:57280\n\ts_waitcnt lgkmcnt(15)\n\t" \
    "buffer_store_dwordx4 v[208:211], v5, s[8:11], s56 offen sc1\n\tv_mfma_f32_16x16x32_bf16 v[172:175], a[72:75], v[68:71], v[172:175]\n\tds_read_b128 v[116:119], v1 offset:57792\n\ts_add_u32 s56, s56, 49152\n\tv_mfma_f32_16x16x32_bf16 v[176:179], a[72:75], v[72:75], v[176:179]\n\tds_read_b128 v[120:123], v1 offset:58304\n\tv_mfma_f32_16x16x32_bf16 v[180:183], a[72:75], v[76:79], v[180:183]\n\tds_read_b128 v[124:127], v1 offset:58816\n\t" \
    "v_mfma_f32_16x16x32_bf16 v[184:187], a[72:75], v[80:83], v[184:187]\n\tds_read_b128 v[128:131], v1 offset:59328\n\tv_mfma_f32_16x16x32_bf16 v[188:191], a[72:75], v[84:87], v[188:191]\n\tds_read_b64 a[192:193], v0 offset:3648\n\tv_mfma_f32_16x16x32_bf16 v[192:195], a[72:75], v[88:91], v[192:195]\n\tds_read_b64 a[194:195], v0 offset:3656\n\tv_mfma_f32_16x16x32_bf16 v[176:179], a[76:79], v[68:71], v[176:179]\n\tds_read_b64 a[196:197], v0 offset:3136\n\t" \
    "v_mfma_f32_16x16x32_bf16 v[180:183], a[76:79], v[72:75], v[180:183]\n\tds_read_b64 a[198:199], v0 offset:3144\n\tv_mfma_f32_16x16x32_bf16 v[184:187], a[76:79], v[76:79], v[184:187]\n\tds_read_b64 a[200:201], v0 offset:2624\n\tv_mfma_f32_16x16x32_bf16 v[188:191], a[76:79], v[80:83], v[188:191]\n\tds_read_b64 a[202:203], v0 offset:2632\n\tv_mfma_f32_16x16x32_bf16 v[192:195], a[76:79], v[84:87], v[192:195]\n\tds_read_b64 a[204:205], v0 offset:2112\n\t" \
    "v_mfma_f32_16x16x32_bf16 v[180:183], a[80:83], v[68:71], v[180:183]\n\tds_read_b64 a[206:207], v0 offset:2120\n\tv_mfma_f32_16x16x32_bf16 v[184:187], a[80:83], v[72:75], v[184:187]\n\tds_read_b64 a[208:209], v0 offset:1600\n\tv_mfma_f32_16x16x32_bf16 v[188:191], a[80:83], v[76:79], v[188:191]\n\tds_read_b64 a[210:211], v0 offset:1608\n\tv_mfma_f32_16x16x32_bf16 v[192:195], a[80:83], v[80:83], v[192:195]\n\tds_read_b64 a[212:213], v0 offset:1088\n\t" \
    "v_mfma_f32_16x16x32_bf16 v[184:187], a[84:87], v[68:71], v[184:187]\n\tds_read_b64 a[214:215], v0 offset:1096\n\tv_mfma_f32_16x16x32_bf16 v[188:191], a[84:87], v[72:75], v[188:191]\n\tbuffer_store_dwordx4 v[212:215], v5, s[8:11], s56 offen sc1\n\ts_add_u32 s56, s56, 49152\n\tv_mfma_f32_16x16x32_bf16 v[192:195], a[84:87], v[76:79], v[192:195]\n\tv_mfma_f32_16x16x32_bf16 v[188:191], a[88:91], v[68:71], v[188:191]\n\tv_mfma_f32_16x16x32_bf16 v[192:195], a[88:91], v[72:75], v[192:195]\n\t" \
    "v_mfma_f32_16x16x32_bf16 v[192:195], a[92:95], v[68:71], v[192:195]\n\ts_waitcnt lgkmcnt(12)\n\tv_mfma_f32_16x16x32_bf16 v[164:167], a[96:99], v[100:103], v[164:167]\n\tds_read_b64 a[216:217], v0 offset:576\n\tv_mfma_f32_16x16x32_bf16 v[168:171], a[96:99], v[104:107], v[168:171]\n\tds_read_b64 a[218:219], v0 offset:584\n\tv_mfma_f32_16x16x32_bf16 v[172:175], a[96:99], v[108:111], v[172:175]\n\tds_read_b64 a[220:221], v0 offset:64\n\t" \
    "v_mfma_f32_16x16x32_bf16 v[176:179], a[96:99], v[112:115], v[176:179]\n\tds_read_b64 a[222:223], v0 offset:72\n\tv_mfma_f32_16x16x32_bf16 v[180:183], a[96:99], v[116:119], v[180:183]\n\tds_read_b64 a[224:225], v0 offset:3584\n\tv_mfma_f32_16x16x32_bf16 v[184:187], a[96:99], v[120:123], v[184:187]\n\tds_read_b64 a[226:227], v0 offset:3592\n\tv_mfma_f32_16x16x32_bf16 v[188:191], a[96:99], v[124:127], v[188:191]\n\tds_read_b64 a[228:229], v0 offset:3072\n\t" \
    "v_mfma_f32_16x16x32_bf16 v[192:195], a[96:99], v[128:131], v[192:195]\n\tds_read_b64 a[230:231], v0 offset:3080\n\tbuffer_store_dwordx4 v[216:219], v5, s[8:11], s56 offen sc1\n\tv_mfma_f32_16x16x32_bf16 v[168:171], a[100:103], v[100:103], v[168:171]\n\tds_read_b64 a[232:233], v0 offset:2560\n\ts_add_u32 s56, s56, 49152\n\tv_mfma_f32_16x16x32_bf16 v[172:175], a[100:103], v[104:107], v[172:175]\n\tds_read_b64 a[234:235], v0 offset:2568\n\t" \
    "v_mfma_f32_16x16x32_bf16 v[176:179], a[100:103], v[108:111], v[176:179]\n\tds_read_b64 a[236:237], v0 offset:2048\n\tv_mfma_f32_16x16x32_bf16 v[180:183], a[100:103], v[112:115], v[180:183]\n\tds_read_b64 a[238:239], v0 offset:2056\n\tv_mfma_f32_16x16x32_bf16 v[184:187], a[100:103], v[116:119], v[184:187]\n\tds_read_b64 a[240:241], v0 offset:1536\n\tv_mfma_f32_16x16x32_bf16 v[188:191], a[100:103], v[120:123], v[188:191]\n\tds_read_b64 a[242:243], v0 offset:1544\n\t" \
    "v_mfma_f32_16x16x32_bf16 v[192:195], a[100:103], v[124:127], v[192:195]\n\tds_read_b64 a[244:245], v0 offset:1024\n\tv_mfma_f32_16x16x32_bf16 v[172:175], a[104:107], v[100:103], v[172:175]\n\tds_read_b64 a[246:247], v0 offset:1032\n\tv_mfma_f32_16x16x32_bf16 v[176:179], a[104:107], v[104:107], v[176:179]\n\tds_read_b128 v[68:71], v1 offset:55680\n\tv_mfma_f32_16x16x32_bf16 v[180:183], a[104:107], v[108:111], v[180:183]\n\tds_read_b128 v[72:75], v1 offset:56192\n\t" \
    "v_mfma_f32_16x16x32_bf16 v[184:187], a[104:107], v[112:115], v[184:187]\n\tds_read_b128 v[76:79], v1 offset:56704\n\tv_mfma_f32_16x16x32_bf16 v[188:191], a[104:107], v[116:119], v[188:191]\n\tds_read_b128 v[80:83], v1 offset:57216\n\tv_mfma_f32_16x16x32_bf16 v[192:195], a[104:107], v[120:123], v[192:195]\n\tds_read_b128 v[84:87], v1 offset:57728\n\tv_mfma_f32_16x16x32_bf16 v[176:179], a[108:111], v[100:103], v[176:179]\n\tds_read_b128 v[88:91], v1 offset:58240\n\t" \
    "v_mfma_f32_16x16x32_bf16 v[180:183], a[108:111], v[104:107], v[180:183]\n\tds_read_b128 v[92:95], v1 offset:58752\n\tv_mfma_f32_16x16x32_bf16 v[184:187], a[108:111], v[108:111], v[184:187]\n\tds_read_b128 v[96:99], v1 offset:59264\n\tv_mfma_f32_16x16x32_bf16 v[188:191], a[108:111], v[112:115], v[188:191]\n\tds_read_b64 a[248:249], v0 offset:512\n\tv_mfma_f32_16x16x32_bf16 v[192:195], a[108:111], v[116:119], v[192:195]\n\tds_read_b64 a[250:251], v0 offset:520\n\t" \
    "v_mfma_f32_16x16x32_bf16 v[180:183], a[112:115], v[100:103], v[180:183]\n\tds_read_b64 a[252:253], v0 offset:0\n\tv_mfma_f32_16x16x32_bf16 v[184:187], a[112:115], v[104:107], v[184:187]\n\tds_read_b64 a[254:255], v0 offset:8\n\tv_mfma_f32_16x16x32_bf16 v[188:191], a[112:115], v[108:111], v[188:191]\n\tv_mfma_f32_16x16x32_bf16 v[192:195], a[112:115], v[112:115], v[192:195]\n\tv_mfma_f32_16x16x32_bf16 v[184:187], a[116:119], v[100:103], v[184:187]\n\tv_mfma_f32_16x16x32_bf16 v[188:191], a[116:119], v[104:107], v[188:191]\n\t" \
    "v_mfma_f32_16x16x32_bf16 v[192:195], a[116:119], v[108:111], v[192:195]\n\tv_mfma_f32_16x16x32_bf16 v[188:191], a[120:123], v[100:103], v[188:191]\n\tv_mfma_f32_16x16x32_bf16 v[192:195], a[120:123], v[104:107], v[192:195]\n\tv_mfma_f32_16x16x32_bf16 v[192:195], a[124:127], v[100:103], v[192:195]\n\ts_waitcnt lgkmcnt(4)\n\tv_mfma_f32_16x16x32_bf16 v[164:167], a[128:131], v[68:71], v[164:167]\n\tds_read_b128 v[100:103], v1 offset:55616\n\tv_mfma_f32_16x16x32_bf16 v[168:171], a[128:131], v[72:75], v[168:171]\n\t" \
    "ds_read_b128 v[104:107], v1 offset:56128\n\tv_mfma_f32_16x16x32_bf16 v[172:175], a[128:131], v[76:79], v[172:175]\n\tds_read_b128 v[108:111], v1 offset:56640\n\tv_mfma_f32_16x16x32_bf16 v[176:179], a[128:131], v[80:83], v[176:179]\n\tds_read_b128 v[112:115], v1 offset:57152\n\tv_mfma_f32_16x16x32_bf16 v[180:183], a[128:131], v[84:87], v[180:183]\n\tds_read_b128 v[116:119], v1 offset:57664\n\tv_mfma_f32_16x16x32_bf16 v[184:187], a[128:131], v[88:91], v[184:187]\n\t" \
    "ds_read_b128 v[120:123], v1 offset:58176\n\tv_mfma_f32_16x16x32_bf16 v[188:191], a[128:131], v[92:95], v[188:191]\n\tds_read_b128 v[124:127], v1 offset:58688\n\tv_mfma_f32_16x16x32_bf16 v[192:195], a[128:131], v[96:99], v[192:195]\n\tds_read_b128 v[128:131], v1 offset:59200\n\tv_mfma_f32_16x16x32_bf16 v[168:171], a[132:135], v[68:71], v[168:171]\n\tv_mfma_f32_16x16x32_bf16 v[172:175], a[132:135], v[72:75], v[172:175]\n\tv_mfma_f32_16x16x32_bf16 v[176:179], a[132:135], v[76:79], v[176:179]\n\t" \
    "v_mfma_f32_16x16x32_bf16 v[180:183], a[132:135], v[80:83], v[180:183]\n\tv_mfma_f32_16x16x32_bf16 v[184:187], a[132:135], v[84:87], v[184:187]\n\tv_mfma_f32_16x16x32_bf16 v[188:191], a[132:135], v[88:91], v[188:191]\n\tv_mfma_f32_16x16x32_bf16 v[192:195], a[132:135], v[92:95], v[192:195]\n\tv_mfma_f32_16x16x32_bf16 v[172:175], a[136:139], v[68:71], v[172:175]\n\tv_mfma_f32_16x16x32_bf16 v[176:179], a[136:139], v[72:75], v[176:179]\n\tv_mfma_f32_16x16x32_bf16 v[180:183], a[136:139], v[76:79], v[180:183]\n\tv_mfma_f32_16x16x32_bf16 v[184:187], a[136:139], v[80:83], v[184:187]\n\t" \
    "v_mfma_f32_16x16x32_bf16 v[188:191], a[136:139], v[84:87], v[188:191]\n\tv_mfma_f32_16x16x32_bf16 v[192:195], a[136:139], v[88:91], v[192:195]\n\tv_mfma_f32_16x16x32_bf16 v[176:179], a[140:143], v[68:71], v[176:179]\n\tv_mfma_f32_16x16x32_bf16 v[180:183], a[140:143], v[72:75], v[180:183]\n\tv_mfma_f32_16x16x32_bf16 v[184:187], a[140:143], v[76:79], v[184:187]\n\tv_mfma_f32_16x16x32_bf16 v[188:191], a[140:143], v[80:83], v[188:191]\n\tv_mfma_f32_16x16x32_bf16 v[192:195], a[140:143], v[84:87], v[192:195]\n\tv_mfma_f32_16x16x32_bf16 v[180:183], a[144:147], v[68:71], v[180:183]\n\t" \
    "v_mfma_f32_16x16x32_bf16 v[184:187], a[144:147], v[72:75], v[184:187]\n\tv_mfma_f32_16x16x32_bf16 v[188:191], a[144:147], v[76:79], v[188:191]\n\tv_mfma_f32_16x16x32_bf16 v[192:195], a[144:147], v[80:83], v[192:195]\n\tv_mfma_f32_16x16x32_bf16 v[184:187], a[148:151], v[68:71], v[184:187]\n\tv_mfma_f32_16x16x32_bf16 v[188:191], a[148:151], v[72:75], v[188:191]\n\tv_mfma_f32_16x16x32_bf16 v[192:195], a[148:151], v[76:79], v[192:195]\n\tv_mfma_f32_16x16x32_bf16 v[188:191], a[152:155], v[68:71], v[188:191]\n\tv_mfma_f32_16x16x32_bf16 v[192:195], a[152:155], v[72:75], v[192:195]\n\t" \
    "v_mfma_f32_16x16x32_bf16 v[192:195], a[156:159], v[68:71], v[192:195]\n\ts_waitcnt lgkmcnt(0)\n\tv_mfma_f32_16x16x32_bf16 v[164:167], a[160:163], v[100:103], v[164:167]\n\tds_read_b128 v[68:71], v1 offset:55552\n\tv_mfma_f32_16x16x32_bf16 v[168:171], a[160:163], v[104:107], v[168:171]\n\tds_read_b128 v[72:75], v1 offset:56064\n\tv_mfma_f32_16x16x32_bf16 v[172:175], a[160:163], v[108:111], v[172:175]\n\tds_read_b128 v[76:79], v1 offset:56576\n\t" \
    "v_mfma_f32_16x16x32_bf16 v[176:179], a[160:163], v[112:115], v[176:179]\n\tds_read_b128 v[80:83], v1 offset:57088\n\tv_mfma_f32_16x16x32_bf16 v[180:183], a[160:163], v[116:119], v[180:183]\n\tds_read_b128 v[84:87], v1 offset:57600\n\tv_mfma_f32_16x16x32_bf16 v[184:187], a[160:163], v[120:123], v[184:187]\n\tds_read_b128 v[88:91], v1 offset:58112\n\tv_mfma_f32_16x16x32_bf16 v[188:191], a[160:163], v[124:127], v[188:191]\n\tds_read_b128 v[92:95], v1 offset:58624\n\t" \
    "v_mfma_f32_16x16x32_bf16 v[192:195], a[160:163], v[128:131], v[192:195]\n\tds_read_b128 v[96:99], v1 offset:59136\n\tv_mfma_f32_16x16x32_bf16 v[168:171], a[164:167], v[100:103], v[168:171]\n\tv_mfma_f32_16x16x32_bf16 v[172:175], a[164:167], v[104:107], v[172:175]\n\tv_mfma_f32_16x16x32_bf16 v[176:179], a[164:167], v[108:111], v[176:179]\n\tv_mfma_f32_16x16x32_bf16 v[180:183], a[164:167], v[112:115], v[180:183]\n\tv_mfma_f32_16x16x32_bf16 v[184:187], a[164:167], v[116:119], v[184:187]\n\tv_mfma_f32_16x16x32_bf16 v[188:191], a[164:167], v[120:123], v[188:191]\n\t" \
    "v_mfma_f32_16x16x32_bf16 v[192:195], a[164:167], v[124:127], v[192:195]\n\tv_mfma_f32_16x16x32_bf16 v[172:175], a[168:171], v[100:103], v[172:175]\n\tv_mfma_f32_16x16x32_bf16 v[176:179], a[168:171], v[104:107], v[176:179]\n\tv_mfma_f32_16x16x32_bf16 v[180:183], a[168:171], v[108:111], v[180:183]\n\tv_mfma_f32_16x16x32_bf16 v[184:187], a[168:171], v[112:115], v[184:187]\n\tv_mfma_f32_16x16x32_bf16 v[188:191], a[168:171], v[116:119], v[188:191]\n\tv_mfma_f32_16x16x32_bf16 v[192:195], a[168:171], v[120:123], v[192:195]\n\tv_mfma_f32_16x16x32_bf16 v[176:179], a[172:175], v[100:103], v[176:179]\n\t" \
    "v_mfma_f32_16x16x32_bf16 v[180:183], a[172:175], v[104:107], v[180:183]\n\tv_mfma_f32_16x16x32_bf16 v[184:187], a[172:175], v[108:111], v[184:187]\n\tv_mfma_f32_16x16x32_bf16 v[188:191], a[172:175], v[112:115], v[188:191]\n\tv_mfma_f32_16x16x32_bf16 v[192:195], a[172:175], v[116:119], v[192:195]\n\tv_mfma_f32_16x16x32_bf16 v[180:183], a[176:179], v[100:103], v[180:183]\n\tv_mfma_f32_16x16x32_bf16 v[184:187], a[176:179], v[104:107], v[184:187]\n\tv_mfma_f32_16x16x32_bf16 v[188:191], a[176:179], v[108:111], v[188:191]\n\tv_mfma_f32_16x16x32_bf16 v[192:195], a[176:179], v[112:115], v[192:195]\n\t" \
    "v_mfma_f32_16x16x32_bf16 v[184:187], a[180:183], v[100:103], v[184:187]\n\tv_mfma_f32_16x16x32_bf16 v[188:191], a[180:183], v[104:107], v[188:191]\n\tv_mfma_f32_16x16x32_bf16 v[192:195], a[180:183], v[108:111], v[192:195]\n\tv_mfma_f32_16x16x32_bf16 v[188:191], a[184:187], v[100:103], v[188:191]\n\tv_mfma_f32_16x16x32_bf16 v[192:195], a[184:187], v[104:107], v[192:195]\n\tv_mfma_f32_16x16x32_bf16 v[192:195], a[188:191], v[100:103], v[192:195]\n\ts_waitcnt lgkmcnt(0)\n\tv_mfma_f32_16x16x32_bf16 v[164:167], a[192:195], v[68:71], v[164:167]\n\t" \
    "ds_read_b128 v[100:103], v1 offset:55488\n\tv_mfma_f32_16x16x32_bf16 v[168:171], a[192:195], v[72:75], v[168:171]\n\tds_read_b128 v[104:107], v1 offset:56000\n\tv_mfma_f32_16x16x32_bf16 v[172:175], a[192:195], v[76:79], v[172:175]\n\tds_read_b128 v[108:111], v1 offset:56512\n\tv_mfma_f32_16x16x32_bf16 v[176:179], a[192:195], v[80:83], v[176:179]\n\tds_read_b128 v[112:115], v1 offset:57024\n\tv_mfma_f32_16x16x32_bf16 v[180:183], a[192:195], v[84:87], v[180:183]\n\t" \
    "ds_read_b128 v[116:119], v1 offset:57536\n\tv_mfma_f32_16x16x32_bf16 v[184:187], a[192:195], v[88:91], v[184:187]\n\tds_read_b128 v[120:123], v1 offset:58048\n\tv_mfma_f32_16x16x32_bf16 v[188:191], a[192:195], v[92:95], v[188:191]\n\tds_read_b128 v[124:127], v1 offset:58560\n\tv_mfma_f32_16x16x32_bf16 v[192:195], a[192:195], v[96:99], v[192:195]\n\tds_read_b128 v[128:131], v1 offset:59072\n\tv_mfma_f32_16x16x32_bf16 v[168:171], a[196:199], v[68:71], v[168:171]\n\t" \
    "v_mfma_f32_16x16x32_bf16 v[172:175], a[196:199], v[72:75], v[172:175]\n\tv_mfma_f32_16x16x32_bf16 v[176:179], a[196:199], v[76:79], v[176:179]\n\tv_mfma_f32_16x16x32_bf16 v[180:183], a[196:199], v[80:83], v[180:183]\n\tv_mfma_f32_16x16x32_bf16 v[184:187], a[196:199], v[84:87], v[184:187]\n\tv_mfma_f32_16x16x32_bf16 v[188:191], a[196:199], v[88:91], v[188:191]\n\tv_mfma_f32_16x16x32_bf16 v[192:195], a[196:199], v[92:95], v[192:195]\n\tv_mfma_f32_16x16x32_bf16 v[172:175], a[200:203], v[68:71], v[172:175]\n\tv_mfma_f32_16x16x32_bf16 v[176:179], a[200:203], v[72:75], v[176:179]\n\t" \
    "v_mfma_f32_16x16x32_bf16 v[180:183], a[200:203], v[76:79], v[180:183]\n\tv_mfma_f32_16x16x32_bf16 v[184:187], a[200:203], v[80:83], v[184:187]\n\tv_mfma_f32_16x16x32_bf16 v[188:191], a[200:203], v[84:87], v[188:191]\n\tv_mfma_f32_16x16x32_bf16 v[192:195], a[200:203], v[88:91], v[192:195]\n\tv_mfma_f32_16x16x32_bf16 v[176:179], a[204:207], v[68:71], v[176:179]\n\tv_mfma_f32_16x16x32_bf16 v[180:183], a[204:207], v[72:75], v[180:183]\n\tv_mfma_f32_16x16x32_bf16 v[184:187], a[204:207], v[76:79], v[184:187]\n\tv_mfma_f32_16x16x32_bf16 v[188:191], a[204:207], v[80:83], v[188:191]\n\t" \
    "v_mfma_f32_16x16x32_bf16 v[192:195], a[204:207], v[84:87], v[192:195]\n\tv_mfma_f32_16x16x32_bf16 v[180:183], a[208:211], v[68:71], v[180:183]\n\tv_mfma_f32_16x16x32_bf16 v[184:187], a[208:211], v[72:75], v[184:187]\n\tv_mfma_f32_16x16x32_bf16 v[188:191], a[208:211], v[76:79], v[188:191]\n\tv_mfma_f32_16x16x32_bf16 v[192:195], a[208:211], v[80:83], v[192:195]\n\tv_mfma_f32_16x16x32_bf16 v[184:187], a[212:215], v[68:71], v[184:187]\n\tv_mfma_f32_16x16x32_bf16 v[188:191], a[212:215], v[72:75], v[188:191]\n\tv_mfma_f32_16x16x32_bf16 v[192:195], a[212:215], v[76:79], v[192:195]\n\t" \
    "v_mfma_f32_16x16x32_bf16 v[188:191], a[216:219], v[68:71], v[188:191]\n\tv_mfma_f32_16x16x32_bf16 v[192:195], a[216:219], v[72:75], v[192:195]\n\tv_mfma_f32_16x16x32_bf16 v[192:195], a[220:223], v[68:71], v[192:195]\n\ts_waitcnt lgkmcnt(0)\n\tv_mfma_f32_16x16x32_bf16 v[164:167], a[224:227], v[100:103], v[164:167]\n\ts_waitcnt vmcnt(0)\n\tbuffer_store_dwordx2 v[248:249], off, s[8:11], s51 sc1\n\tv_mfma_f32_16x16x32_bf16 v[168:171], a[224:227], v[104:107], v[168:171]\n\t" \
    "v_mfma_f32_16x16x32_bf16 v[172:175], a[224:227], v[108:111], v[172:175]\n\tv_mfma_f32_16x16x32_bf16 v[176:179], a[224:227], v[112:115], v[176:179]\n\tv_mfma_f32_16x16x32_bf16 v[180:183], a[224:227], v[116:119], v[180:183]\n\tv_mfma_f32_16x16x32_bf16 v[184:187], a[224:227], v[120:123], v[184:187]\n\tv_mfma_f32_16x16x32_bf16 v[188:191], a[224:227], v[124:127], v[188:191]\n\tv_mfma_f32_16x16x32_bf16 v[192:195], a[224:227], v[128:131], v[192:195]\n\tv_mfma_f32_16x16x32_bf16 v[168:171], a[228:231], v[100:103], v[168:171]\n\tv_mfma_f32_16x16x32_bf16 v[172:175], a[228:231], v[104:107], v[172:175]\n\t" \
    "v_mfma_f32_16x16x32_bf16 v[176:179], a[228:231], v[108:111], v[176:179]\n\tv_mfma_f32_16x16x32_bf16 v[180:183], a[228:231], v[112:115], v[180:183]\n\tv_mfma_f32_16x16x32_bf16 v[184:187], a[228:231], v[116:119], v[184:187]\n\tv_mfma_f32_16x16x32_bf16 v[188:191], a[228:231], v[120:123], v[188:191]\n\tv_mfma_f32_16x16x32_bf16 v[192:195], a[228:231], v[124:127], v[192:195]\n\tv_mfma_f32_16x16x32_bf16 v[172:175], a[232:235], v[100:103], v[172:175]\n\tv_mfma_f32_16x16x32_bf16 v[176:179], a[232:235], v[104:107], v[176:179]\n\tv_mfma_f32_16x16x32_bf16 v[180:183], a[232:235], v[108:111], v[180:183]\n\t" \
    "v_mfma_f32_16x16x32_bf16 v[184:187], a[232:235], v[112:115], v[184:187]\n\tv_mfma_f32_16x16x32_bf16 v[188:191], a[232:235], v[116:119], v[188:191]\n\tv_mfma_f32_16x16x32_bf16 v[192:195], a[232:235], v[120:123], v[192:195]\n\tv_mfma_f32_16x16x32_bf16 v[176:179], a[236:239], v[100:103], v[176:179]\n\tv_mfma_f32_16x16x32_bf16 v[180:183], a[236:239], v[104:107], v[180:183]\n\tv_mfma_f32_16x16x32_bf16 v[184:187], a[236:239], v[108:111], v[184:187]\n\tv_mfma_f32_16x16x32_bf16 v[188:191], a[236:239], v[112:115], v[188:191]\n\tv_mfma_f32_16x16x32_bf16 v[192:195], a[236:239], v[116:119], v[192:195]\n\t" \
    "v_mfma_f32_16x16x32_bf16 v[180:183], a[240:243], v[100:103], v[180:183]\n\tv_mfma_f32_16x16x32_bf16 v[184:187], a[240:243], v[104:107], v[184:187]\n\tv_mfma_f32_16x16x32_bf16 v[188:191], a[240:243], v[108:111], v[188:191]\n\tv_mfma_f32_16x16x32_bf16 v[192:195], a[240:243], v[112:115], v[192:195]\n\tv_mfma_f32_16x16x32_bf16 v[184:187], a[244:247], v[100:103], v[184:187]\n\tv_mfma_f32_16x16x32_bf16 v[188:191], a[244:247], v[104:107], v[188:191]\n\tv_mfma_f32_16x16x32_bf16 v[192:195], a[244:247], v[108:111], v[192:195]\n\tv_mfma_f32_16x16x32_bf16 v[188:191], a[248:251], v[100:103], v[188:191]\n\t" \
    "v_mfma_f32_16x16x32_bf16 v[192:195], a[248:251], v[104:107], v[192:195]\n\tv_mfma_f32_16x16x32_bf16 v[192:195], a[252:255], v[100:103], v[192:195]\n\tv_cvt_pk_bf16_f32 v164, v164, v165\n\tv_cvt_pk_bf16_f32 v165, v166, v167\n\tds_write_b64 v3, v[164:165] offset:55488\n\tv_cvt_pk_bf16_f32 v168, v168, v169\n\tv_cvt_pk_bf16_f32 v169, v170, v171\n\tds_write_b64 v3, v[168:169] offset:56000\n\t" \
    "v_cvt_pk_bf16_f32 v172, v172, v173\n\tv_cvt_pk_bf16_f32 v173, v174, v175\n\tds_write_b64 v3, v[172:173] offset:56512\n\tv_cvt_pk_bf16_f32 v176, v176, v177\n\tv_cvt_pk_bf16_f32 v177, v178, v179\n\tds_write_b64 v3, v[176:177] offset:57024\n\tv_cvt_pk_bf16_f32 v180, v180, v181\n\tv_cvt_pk_bf16_f32 v181, v182, v183\n\t" \
    "ds_write_b64 v3, v[180:181] offset:57536\n\tv_cvt_pk_bf16_f32 v184, v184, v185\n\tv_cvt_pk_bf16_f32 v185, v186, v187\n\tds_write_b64 v3, v[184:185] offset:58048\n\tv_cvt_pk_bf16_f32 v188, v188, v189\n\tv_cvt_pk_bf16_f32 v189, v190, v191\n\tds_write_b64 v3, v[188:189] offset:58560\n\tv_cvt_pk_bf16_f32 v192, v192, v193\n\t" \
    "v_cvt_pk_bf16_f32 v193, v194, v195\n\tds_write_b64 v3, v[192:193] offset:59072\n\ts_waitcnt lgkmcnt(0)\n\ts_barrier\n\ts_add_u32 s51, s44, 12\n\ts_mul_i32 s65, s51, 1366\n\ts_lshr_b32 s65, s65, 16\n\ts_mul_i32 s56, s65, 48\n\t" \
    "s_sub_u32 s56, s51, s56\n\ts_lshl_b32 s56, s56, 6\n\ts_lshr_b32 s70, s65, 2\n\ts_mul_i32 s70, s70, 252\n\ts_add_u32 s65, s65, s70\n\ts_mul_i32 s65, s65, 3072\n\ts_add_u32 s56, s56, s65\n\ts_add_u32 s56, s56, 4849664\n\t" \
    "s_add_u32 s51, s51, s38\n\ts_lshl_b32 s51, s51, 3\n\ts_add_u32 s51, s51, 50176\n\ts_or_b32 s56, s56, s45\n\ts_or_b32 s51, s51, s45\n\tds_read_b64_tr_b16 v[204:205], v4 offset:55488\n\tds_read_b64_tr_b16 v[206:207], v4 offset:55496\n\tds_read_b64_tr_b16 v[208:209], v4 offset:55552\n\t" \
    "ds_read_b64_tr_b16 v[210:211], v4 offset:55560\n\tds_read_b64_tr_b16 v[212:213], v4 offset:56000\n\tds_read_b64_tr_b16 v[214:215], v4 offset:56008\n\tds_read_b64_tr_b16 v[216:217], v4 offset:56064\n\tds_read_b64_tr_b16 v[218:219], v4 offset:56072\n\ts_waitcnt lgkmcnt(6)\n\tbuffer_store_dwordx4 v[204:207], v5, s[8:11], s56 offen sc1\n\ts_add_u32 s56, s56, 49152\n\t" \
    "s_waitcnt lgkmcnt(4)\n\tbuffer_store_dwordx4 v[208:211], v5, s[8:11], s56 offen sc1\n\ts_add_u32 s56, s56, 49152\n\ts_waitcnt lgkmcnt(2)\n\tbuffer_store_dwordx4 v[212:215], v5, s[8:11], s56 offen sc1\n\ts_add_u32 s56, s56, 49152\n\ts_waitcnt lgkmcnt(0)\n\tbuffer_store_dwordx4 v[216:219], v5, s[8:11], s56 offen sc1\n\t" \
    "s_add_u32 s56, s56, 49152\n\ts_waitcnt vmcnt(0)\n\tbuffer_store_dwordx2 v[248:249], off, s[8:11], s51 sc1\n\ts_waitcnt vmcnt(0)\n\t103:\n\t98:\n\ts_nop 1\n\t" \
    ""
#define MAIN_CLOB "s2", "s3", "s4", "s5", "s6", "s7", "s8", "s9", "s10", "s11", "s12", "s13", "s14", "s15", "s16", "s17", "s18", "s19", "s20", "s21", "s22", "s23", "s24", "s25", "s26", "s27", "s28", "s29", "s30", "s31", "s33", "s34", "s35", "s36", "s37", "s39", "s41", "s42", "s43", "s44", "s45", "s46", "s47", "s48", "s49", "s50", "s51", "s52", "s53", "s54", "s55", "s56", "s57", "s58", "s59", "s60", "s61", "s62", "s63", "s64", "s65", "s66", "s67", "s68", "s69", "s70", "s71", "s72", "s73", "s74", "s75", "s76", "s77", "s78", "s79", "s80", "s81", "s82", "s83", "s84", "s85", "s86", "s87", "v0", "v1", "v2", "v3", "v4", "v5", "v6", "v7", "v8", "v9", "v10", "v11", "v12", "v13", "v14", "v15", "v16", "v17", "v18", "v19", "v20", "v21", "v22", "v23", "v24", "v25", "v26", "v27", "v28", "v29", "v30", "v31", "v32", "v33", "v34", "v35", "v36", "v37", "v38", "v39", "v40", "v41", "v42", "v43", "v44", "v45", "v46", "v47", "v48", "v49", "v50", "v51", "v52", "v53", "v54", "v55", "v56", "v57", "v58", "v59", "v60", "v61", "v62", "v63", "v64", "v65", "v66", "v67", "v68", "v69", "v70", "v71", "v72", "v73", "v74", "v75", "v76", "v77", "v78", "v79", "v80", "v81", "v82", "v83", "v84", "v85", "v86", "v87", "v88", "v89", "v90", "v91", "v92", "v93", "v94", "v95", "v96", "v97", "v98", "v99", "v100", "v101", "v102", "v103", "v104", "v105", "v106", "v107", "v108", "v109", "v110", "v111", "v112", "v113", "v114", "v115", "v116", "v117", "v118", "v119", "v120", "v121", "v122", "v123", "v124", "v125", "v126", "v127", "v128", "v129", "v130", "v131", "v132", "v133", "v134", "v135", "v136", "v137", "v138", "v139", "v140", "v141", "v142", "v143", "v144", "v145", "v146", "v147", "v148", "v149", "v150", "v151", "v152", "v153", "v154", "v155", "v156", "v157", "v158", "v159", "v160", "v161", "v162", "v163", "v164", "v165", "v166", "v167", "v168", "v169", "v170", "v171", "v172", "v173", "v174", "v175", "v176", "v177", "v178", "v179", "v180", "v181", "v182", "v183", "v184", "v185", "v186", "v187", "v188", "v189", "v190", "v191", "v192", "v193", "v194", "v195", "v196", "v197", "v198", "v199", "v200", "v201", "v202", "v203", "v204", "v205", "v206", "v207", "v208", "v209", "v210", "v211", "v212", "v213", "v214", "v215", "v216", "v217", "v218", "v219", "v220", "v221", "v222", "v223", "v224", "v225", "v226", "v227", "v228", "v229", "v230", "v231", "v232", "v233", "v234", "v235", "v236", "v237", "v238", "v239", "v241", "v242", "v243", "v244", "v245", "v246", "v247", "v248", "v249", "v250", "v251", "v252", "v253", "a0", "a1", "a2", "a3", "a4", "a5", "a6", "a7", "a8", "a9", "a10", "a11", "a12", "a13", "a14", "a15", "a16", "a17", "a18", "a19", "a20", "a21", "a22", "a23", "a24", "a25", "a26", "a27", "a28", "a29", "a30", "a31", "a32", "a33", "a34", "a35", "a36", "a37", "a38", "a39", "a40", "a41", "a42", "a43", "a44", "a45", "a46", "a47", "a48", "a49", "a50", "a51", "a52", "a53", "a54", "a55", "a56", "a57", "a58", "a59", "a60", "a61", "a62", "a63", "a64", "a65", "a66", "a67", "a68", "a69", "a70", "a71", "a72", "a73", "a74", "a75", "a76", "a77", "a78", "a79", "a80", "a81", "a82", "a83", "a84", "a85", "a86", "a87", "a88", "a89", "a90", "a91", "a92", "a93", "a94", "a95", "a96", "a97", "a98", "a99", "a100", "a101", "a102", "a103", "a104", "a105", "a106", "a107", "a108", "a109", "a110", "a111", "a112", "a113", "a114", "a115", "a116", "a117", "a118", "a119", "a120", "a121", "a122", "a123", "a124", "a125", "a126", "a127", "a128", "a129", "a130", "a131", "a132", "a133", "a134", "a135", "a136", "a137", "a138", "a139", "a140", "a141", "a142", "a143", "a144", "a145", "a146", "a147", "a148", "a149", "a150", "a151", "a152", "a153", "a154", "a155", "a156", "a157", "a158", "a159", "a160", "a161", "a162", "a163", "a164", "a165", "a166", "a167", "a168", "a169", "a170", "a171", "a172", "a173", "a174", "a175", "a176", "a177", "a178", "a179", "a180", "a181", "a182", "a183", "a184", "a185", "a186", "a187", "a188", "a189", "a190", "a191", "a192", "a193", "a194", "a195", "a196", "a197", "a198", "a199", "a200", "a201", "a202", "a203", "a204", "a205", "a206", "a207", "a208", "a209", "a210", "a211", "a212", "a213", "a214", "a215", "a216", "a217", "a218", "a219", "a220", "a221", "a222", "a223", "a224", "a225", "a226", "a227", "a228", "a229", "a230", "a231", "a232", "a233", "a234", "a235", "a236", "a237", "a238", "a239", "a240", "a241", "a242", "a243", "a244", "a245", "a246", "a247", "a248", "a249", "a250", "a251", "a252", "a253", "a254", "a255"

#define ARG(i) const void* p##i, unsigned n##i, unsigned f##i
__global__ __launch_bounds__(256, 1) void fused_kernel(ARG(0), ARG(1), ARG(2), ARG(3), ARG(4), ARG(5), ARG(6), ARG(7), ARG(8), ARG(9), ARG(10),
                                                       ARG(11)) {
#define MAIN_OUT
    const int b = blockIdx.x;
    unsigned tid = threadIdx.x;
    u64 ka = (u64)__builtin_amdgcn_kernarg_segment_ptr();
    int w;
    asm volatile(MAIN_ASM
                 : "=&{s38}"(w), "+{s[0:1]}"(ka), "+{v240}"(tid) MAIN_OUT
                 : "{s40}"(b)
                 : "memory", "scc", "vcc", MAIN_CLOB);
}

extern "C" void kernel_launch(void* const* d_in, const int* in_sizes, int n_in, void* d_out, int out_size,
                              void* d_ws, size_t ws_size, hipStream_t stream) {
    (void)hipFuncSetAttribute((const void*)fused_kernel, hipFuncAttributeMaxDynamicSharedMemorySize, LDS_BYTES);
    const unsigned F = 0x20000u, NX = S_LEN * D_DIM * 2, NW = S_LEN * F_DIM * 2;
    fused_kernel<<<256, 256, LDS_BYTES, stream>>>(d_ws, WS_END, F, d_in[7], NW, F, d_in[9], NW, F, d_in[0], NX, F, d_out, NX, F, d_in[8], F_DIM * 2, F,
                                                  d_in[10], S_LEN * 2, F, d_in[1], NX, F, d_in[2], NX, F, d_in[3], D_DIM * 8, F, d_in[4], NX, F,
                                                  d_in[5], D_DIM * 2, F);
}
```

```cpp
#include <hip/hip_runtime.h>

typedef unsigned long long u64;

#define S_LEN 2048
#define D_DIM 576
#define F_DIM 4608
#define LDS_BYTES 163840

#define WS_END 29163520u
#define MAIN_ASM \
    "s_load_dwordx16 s[8:23], s[0:1], 0\n\ts_getpc_b64 s[2:3]\n\t99:\n\tv_lshrrev_b32 v20, 5, v240\n\tv_mul_u32_u24 v20, 1152, v20\n\tv_and_b32 v21, 31, v240\n\tv_lshl_add_u32 v22, v21, 2, v20\n\tv_lshl_add_u32 v21, v21, 4, v20\n\t" \
    "s_mul_i32 s68, s40, 9216\n\tv_lshrrev_b32 v245, 6, v240\n\tv_and_b32 v240, 63, v240\n\ts_and_b32 s2, s2, 0xffffff80\n\ts_mov_b32 s4, s2\n\ts_mov_b32 s5, s3\n\ts_mov_b32 s6, 98f-99b\n\ts_mov_b32 s7, 0x20000\n\t" \
    "v_lshl_add_u32 v16, v245, 16, v240\n\tv_lshlrev_b32 v16, 7, v16\n\ts_lshr_b32 s65, s40, 3\n\ts_lshl_b32 s73, s65, 13\n\tbuffer_load_dword v57, v16, s[4:7], s73 offen\n\ts_waitcnt lgkmcnt(0)\n\tbuffer_load_dwordx4 v[48:51], v21, s[20:23], s68 offen\n\tbuffer_load_dwordx4 v[52:55], v21, s[20:23], s68 offen offset:512\n\t" \
    "buffer_load_dword v56, v22, s[20:23], s68 offen offset:1024\n\tbuffer_load_dwordx2 v[58:59], off, s[8:11], 0 sc1\n\ts_load_dwordx8 s[76:83], s[0:1], 112\n\ts_load_dwordx4 s[84:87], s[0:1], 144\n\ts_load_dwordx8 s[24:31], s[0:1], 64\n\ts_load_dwordx4 s[32:35], s[0:1], 96\n\tv_readfirstlane_b32 s38, v245\n\tv_lshlrev_b32 v8, 4, v240\n\t" \
    "v_and_b32 v244, 0xf0, v8\n\tv_lshrrev_b32 v245, 4, v240\n\ts_and_b32 s57, s40, 1\n\ts_and_b32 s58, s38, 1\n\ts_cmp_lt_u32 s65, 27\n\ts_cselect_b32 s39, 1, 0\n\ts_cmp_lt_u32 s65, 24\n\ts_cselect_b32 s39, 2, s39\n\t" \
    "s_min_u32 s65, s65, 26\n\ts_mul_i32 s42, s65, 43\n\ts_lshr_b32 s42, s42, 7\n\ts_mul_i32 s66, s42, 3\n\ts_sub_u32 s41, s65, s66\n\ts_lshl_b32 s69, s42, 8\n\ts_bfe_u32 s66, s40, 0x20001\n\ts_mul_i32 s66, s66, 9\n\t" \
    "s_add_u32 s42, s42, s66\n\ts_mul_i32 s43, s41, 903\n\ts_lshr_b32 s43, s43, 7\n\ts_sub_u32 s70, s43, 21\n\ts_mul_i32 s67, s57, 9437184\n\ts_lshl_b32 s66, s66, 8\n\ts_add_u32 s67, s67, s66\n\tv_lshrrev_b32 v16, 3, v240\n\t" \
    "s_lshl_b32 s73, s38, 3\n\tv_add_u32 v16, s73, v16\n\tv_mul_u32_u24 v16, 9216, v16\n\tv_and_b32 v17, 0x70, v8\n\tv_and_b32 v18, 16, v240\n\tv_lshlrev_b32 v18, 1, v18\n\tv_xor_b32 v17, v17, v18\n\ts_lshl_b32 s73, s57, 1\n\t" \
    "s_add_u32 s73, s73, s58\n\ts_lshl_b32 s73, s73, 6\n\tv_xor_b32 v17, s73, v17\n\tv_add3_u32 v11, v16, v17, s69\n\tv_xor_b32 v10, 128, v11\n\ts_lshl_b32 s54, s38, 10\n\tv_add_u32 v9, 4096, v8\n\tv_lshlrev_b32 v239, 3, v240\n\t" \
    "s_mul_i32 s73, s40, 9216\n\ts_add_u32 s73, s73, s54\n\tv_add_u32 v23, s73, v8\n\tv_subrev_u32 v19, 3456, v23\n\ts_add_u32 s12, s12, s67\n\ts_addc_u32 s13, s13, 0\n\ts_sub_u32 s14, s14, s67\n\ts_cmp_eq_u32 s39, 0\n\t" \
    "s_cselect_b32 s14, 0, s14\n\tv_and_b32 v88, 31, v240\n\tv_lshrrev_b32 v89, 5, v240\n\ts_lshl_b32 s73, s38, 1\n\tv_add_u32 v89, s73, v89\n\tv_mul_u32_u24 v90, 1168, v89\n\tv_add_u32 v90, 153600, v90\n\tv_lshl_add_u32 v91, v88, 2, v90\n\t" \
    "v_lshl_add_u32 v90, v88, 4, v90\n\tv_cmp_eq_u32 vcc, 0, v88\n\tv_lshlrev_b32 v89, 3, v89\n\tv_mov_b32 v92, 0x7ff00000\n\tv_cndmask_b32 v89, v92, v89, vcc\n\tv_bfe_u32 v88, v240, 2, 2\n\tv_and_b32 v92, 3, v240\n\tv_lshlrev_b32 v92, 3, v92\n\t" \
    "v_lshl_add_u32 v92, v245, 5, v92\n\ts_lshl_b32 s73, s38, 7\n\tv_add_u32 v93, s73, v92\n\tv_mul_u32_u24 v94, 1168, v88\n\tv_add_u32 v94, 153600, v94\n\tv_add_u32 v95, v94, v92\n\tv_add_u32 v94, v94, v93\n\tv_mul_u32_u24 v88, 1152, v88\n\t" \
    "v_add_u32 v88, 131072, v88\n\tv_add_u32 v92, v88, v92\n\tv_add_u32 v93, v88, v93\n\tv_lshl_add_u32 v88, v245, 10, v244\n\ts_waitcnt lgkmcnt(0)\n\ts_lshr_b32 s61, s40, 2\n\ts_mul_i32 s61, s61, 36864\n\ts_and_b32 s65, s40, 3\n\t" \
    "s_lshl_b32 s65, s65, 8\n\ts_add_u32 s61, s61, s65\n\ts_add_u32 s61, s61, 131072\n\ts_mul_i32 s62, s40, 9216\n\ts_add_u32 s62, s62, 2490368\n\ts_lshl_b32 s63, s40, 3\n\ts_lshl_b32 s48, s38, 11\n\ts_add_u32 s48, s48, s63\n\t" \
    "s_add_u32 s48, s48, 17408\n\ts_add_u32 s63, s63, 1024\n\ts_cmp_eq_u32 s38, 0\n\ts_cselect_b32 s63, s63, 2146435072\n\ts_lshl_b32 s64, s40, 6\n\ts_add_u32 s64, s64, 58368\n\ts_add_u32 m0, s54, 131072\n\ts_nop 0\n\t" \
    "buffer_load_dwordx4 v19, s[76:79], 0 offen lds\n\ts_and_b32 s4, s40, 7\n\ts_lshl_b32 s47, s4, 11\n\ts_lshl_b32 s65, s57, 10\n\ts_add_u32 s47, s47, s65\n\tv_add_u32 v19, 4096, v19\n\ts_add_u32 m0, s54, 135168\n\ts_nop 0\n\t" \
    "buffer_load_dwordx4 v19, s[76:79], 0 offen lds\n\ts_add_u32 s47, s47, 1024\n\ts_mul_i32 s44, s57, 1179648\n\ts_mul_i32 s65, s41, 12288\n\ts_add_u32 s44, s44, s65\n\tv_add_u32 v19, 4096, v19\n\ts_add_u32 m0, s54, 139264\n\ts_nop 0\n\t" \
    "buffer_load_dwordx4 v19, s[76:79], 0 offen lds\n\ts_mul_i32 s65, s38, 3072\n\ts_add_u32 s44, s44, s65\n\ts_add_u32 s44, s44, 131072\n\ts_lshr_b32 s5, s4, 1\n\tv_add_u32 v19, 4096, v19\n\ts_add_u32 m0, s54, 144384\n\ts_nop 0\n\t" \
    "buffer_load_dwordx4 v23, s[80:83], 0 offen lds\n\tv_bfe_u32 v16, v240, 2, 2\n\tv_and_b32 v17, 3, v240\n\tv_lshlrev_b32 v17, 3, v17\n\tv_lshl_add_u32 v16, v16, 7, v17\n\tv_add_u32 v23, 4096, v23\n\ts_add_u32 m0, s54, 148480\n\ts_nop 0\n\t" \
    "buffer_load_dwordx4 v23, s[80:83], 0 offen lds\n\tv_bfe_u32 v17, v240, 3, 2\n\tv_lshl_add_u32 v16, v17, 5, v16\n\tv_lshl_add_u32 v0, v245, 10, v16\n\ts_and_b32 s65, s5, 1\n\tv_add_u32 v23, 4096, v23\n\tv_subrev_u32 v19, s54, v19\n\tv_subrev_u32 v23, s54, v23\n\t" \
    "s_cmp_eq_u32 s58, 1\n\ts_cselect_b64 vcc, -1, 0\n\ts_cselect_b64 s[76:77], s[80:81], s[76:77]\n\ts_mov_b32 m0, 143360\n\ts_cselect_b32 m0, 152576, m0\n\tv_cndmask_b32 v19, v19, v23, vcc\n\tbuffer_load_dwordx4 v19, s[76:79], 0 offen lds\n\ts_xor_b32 s65, s65, 1\n\t" \
    "s_lshl_b32 s65, s65, 6\n\tv_xor_b32 v17, s65, v16\n\tv_lshl_add_u32 v4, v245, 9, v17\n\ts_lshr_b32 s71, s54, 1\n\tbuffer_load_dwordx2 v[60:61], v239, s[84:87], s71 offen\n\tv_xor_b32 v1, 32, v0\n\tv_xor_b32 v5, 32, v4\n\tv_xor_b32 v2, 64, v0\n\t" \
    "v_xor_b32 v6, 64, v4\n\tbuffer_load_dwordx2 v[62:63], v239, s[84:87], s71 offen offset:2048\n\tv_xor_b32 v3, 96, v0\n\tv_xor_b32 v7, 96, v4\n\tv_add_u32 v224, 0x10000, v4\n\tv_add_u32 v14, 0x10000, v0\n\ts_mov_b32 s71, 4096\n\tbuffer_load_dwordx2 v[64:65], v239, s[84:87], s71 offen\n\t" \
    "v_add_u32 v220, 0x20000, v4\n\tv_add_u32 v225, 0x10000, v5\n\tv_add_u32 v15, 0x10000, v1\n\tv_add_u32 v221, 0x20000, v5\n\ts_cmp_ge_u32 s43, 21\n\ts_cselect_b32 s69, s70, s43\n\ts_mul_i32 s69, s69, 294912\n\ts_add_u32 s69, s69, 0\n\t" \
    "s_add_u32 m0, s54, 0\n\ts_nop 0\n\tbuffer_load_dwordx4 v10, s[12:15], s69 offen lds\n\tv_add_u32 v226, 0x10000, v6\n\tv_add_u32 v237, 0x10000, v2\n\tv_add_u32 v222, 0x20000, v6\n\tv_add_u32 v227, 0x10000, v7\n\ts_cmp_ge_u32 s43, 20\n\t" \
    "s_cselect_b32 s69, s70, s43\n\ts_mul_i32 s69, s69, 294912\n\ts_add_u32 s69, s69, 294912\n\ts_add_u32 m0, s54, 4096\n\ts_nop 0\n\tbuffer_load_dwordx4 v10, s[12:15], s69 offen lds\n\tv_add_u32 v238, 0x10000, v3\n\tv_add_u32 v223, 0x20000, v7\n\t" \
    "s_lshl_b32 s72, s40, 2\n\ts_add_u32 s72, s72, s38\n\ts_waitcnt vmcnt(12)\n\ts_load_dwordx8 s[76:83], s[0:1], 160\n\tv_lshlrev_b32 v24, 16, v48\n\tv_and_b32 v25, 0xffff0000, v48\n\tv_lshlrev_b32 v26, 16, v49\n\tv_and_b32 v27, 0xffff0000, v49\n\t" \
    "v_lshlrev_b32 v28, 16, v50\n\tv_and_b32 v29, 0xffff0000, v50\n\tv_lshlrev_b32 v30, 16, v51\n\tv_and_b32 v31, 0xffff0000, v51\n\tv_lshlrev_b32 v32, 16, v52\n\tv_and_b32 v33, 0xffff0000, v52\n\tv_lshlrev_b32 v34, 16, v53\n\tv_and_b32 v35, 0xffff0000, v53\n\t" \
    "v_lshlrev_b32 v36, 16, v54\n\tv_and_b32 v37, 0xffff0000, v54\n\tv_lshlrev_b32 v38, 16, v55\n\tv_and_b32 v39, 0xffff0000, v55\n\tv_lshlrev_b32 v40, 16, v56\n\tv_and_b32 v41, 0xffff0000, v56\n\tv_pk_add_f32 v[70:71], v[24:25], v[26:27]\n\tv_pk_add_f32 v[72:73], v[28:29], v[30:31]\n\t" \
    "v_pk_add_f32 v[74:75], v[32:33], v[34:35]\n\tv_pk_add_f32 v[76:77], v[36:37], v[38:39]\n\tv_pk_add_f32 v[70:71], v[70:71], v[72:73]\n\tv_pk_add_f32 v[74:75], v[74:75], v[76:77]\n\tv_pk_add_f32 v[70:71], v[70:71], v[40:41]\n\tv_pk_add_f32 v[70:71], v[70:71], v[74:75]\n\tv_add_f32 v66, v70, v71\n\ts_nop 1\n\t" \
    "v_add_f32_dpp v66, v66, v66 row_ror:8 row_mask:0xf bank_mask:0xf\n\ts_nop 1\n\tv_add_f32_dpp v66, v66, v66 row_ror:4 row_mask:0xf bank_mask:0xf\n\ts_nop 1\n\tv_add_f32_dpp v66, v66, v66 row_ror:2 row_mask:0xf bank_mask:0xf\n\ts_nop 1\n\tv_add_f32_dpp v66, v66, v66 row_ror:1 row_mask:0xf bank_mask:0xf\n\tv_mov_b32 v67, v66\n\t" \
    "s_nop 1\n\tv_permlane16_swap_b32 v66, v67\n\tv_add_f32 v66, v66, v67\n\tv_mul_f32 v66, 0x3ae38e39, v66\n\tv_pk_add_f32 v[70:71], v[24:25], v[66:67] op_sel_hi:[1,0] neg_lo:[0,1] neg_hi:[0,1]\n\tv_pk_add_f32 v[72:73], v[26:27], v[66:67] op_sel_hi:[1,0] neg_lo:[0,1] neg_hi:[0,1]\n\tv_pk_add_f32 v[74:75], v[28:29], v[66:67] op_sel_hi:[1,0] neg_lo:[0,1] neg_hi:[0,1]\n\tv_pk_add_f32 v[76:77], v[30:31], v[66:67] op_sel_hi:[1,0] neg_lo:[0,1] neg_hi:[0,1]\n\t" \
    "v_pk_add_f32 v[78:79], v[32:33], v[66:67] op_sel_hi:[1,0] neg_lo:[0,1] neg_hi:[0,1]\n\tv_pk_add_f32 v[80:81], v[34:35], v[66:67] op_sel_hi:[1,0] neg_lo:[0,1] neg_hi:[0,1]\n\tv_pk_add_f32 v[82:83], v[36:37], v[66:67] op_sel_hi:[1,0] neg_lo:[0,1] neg_hi:[0,1]\n\tv_pk_add_f32 v[84:85], v[38:39], v[66:67] op_sel_hi:[1,0] neg_lo:[0,1] neg_hi:[0,1]\n\tv_pk_add_f32 v[86:87], v[40:41], v[66:67] op_sel_hi:[1,0] neg_lo:[0,1] neg_hi:[0,1]\n\tv_pk_mul_f32 v[44:45], v[70:71], v[70:71]\n\tv_pk_mul_f32 v[46:47], v[72:73], v[72:73]\n\tv_pk_fma_f32 v[44:45], v[74:75], v[74:75], v[44:45]\n\t" \
    "v_pk_fma_f32 v[46:47], v[76:77], v[76:77], v[46:47]\n\tv_pk_fma_f32 v[44:45], v[78:79], v[78:79], v[44:45]\n\tv_pk_fma_f32 v[46:47], v[80:81], v[80:81], v[46:47]\n\tv_pk_fma_f32 v[44:45], v[82:83], v[82:83], v[44:45]\n\tv_pk_fma_f32 v[46:47], v[84:85], v[84:85], v[46:47]\n\tv_pk_fma_f32 v[44:45], v[86:87], v[86:87], v[44:45]\n\tv_pk_add_f32 v[44:45], v[44:45], v[46:47]\n\tv_add_f32 v68, v44, v45\n\t" \
    "s_nop 1\n\tv_add_f32_dpp v68, v68, v68 row_ror:8 row_mask:0xf bank_mask:0xf\n\ts_nop 1\n\tv_add_f32_dpp v68, v68, v68 row_ror:4 row_mask:0xf bank_mask:0xf\n\ts_nop 1\n\tv_add_f32_dpp v68, v68, v68 row_ror:2 row_mask:0xf bank_mask:0xf\n\ts_nop 1\n\tv_add_f32_dpp v68, v68, v68 row_ror:1 row_mask:0xf bank_mask:0xf\n\t" \
    "v_mov_b32 v69, v68\n\ts_nop 1\n\tv_permlane16_swap_b32 v68, v69\n\tv_add_f32 v68, v68, v69\n\tv_mul_f32 v68, 0x3ae38e39, v68\n\tv_cvt_pk_bf16_f32 v68, v68, v68\n\tv_lshlrev_b32 v68, 16, v68\n\tv_add_f32 v68, 0x3727c5ac, v68\n\t" \
    "v_cvt_pk_bf16_f32 v68, v68, v68\n\tv_lshlrev_b32 v68, 16, v68\n\tv_sqrt_f32 v68, v68\n\tv_cvt_pk_bf16_f32 v66, v66, v66\n\tv_lshlrev_b32 v66, 16, v66\n\tv_cvt_pk_bf16_f32 v68, v68, v68\n\tv_lshlrev_b32 v68, 16, v68\n\tv_rcp_f32 v68, v68\n\t" \
    "s_nop 0\n\tv_mul_f32 v69, v66, v68\n\tv_xor_b32 v69, 0x80000000, v69\n\tv_pk_fma_f32 v[24:25], v[24:25], v[68:69], v[68:69] op_sel:[0,0,1] op_sel_hi:[1,0,1]\n\tv_pk_fma_f32 v[26:27], v[26:27], v[68:69], v[68:69] op_sel:[0,0,1] op_sel_hi:[1,0,1]\n\tv_pk_fma_f32 v[28:29], v[28:29], v[68:69], v[68:69] op_sel:[0,0,1] op_sel_hi:[1,0,1]\n\tv_pk_fma_f32 v[30:31], v[30:31], v[68:69], v[68:69] op_sel:[0,0,1] op_sel_hi:[1,0,1]\n\tv_pk_fma_f32 v[32:33], v[32:33], v[68:69], v[68:69] op_sel:[0,0,1] op_sel_hi:[1,0,1]\n\t" \
    "v_pk_fma_f32 v[34:35], v[34:35], v[68:69], v[68:69] op_sel:[0,0,1] op_sel_hi:[1,0,1]\n\tv_pk_fma_f32 v[36:37], v[36:37], v[68:69], v[68:69] op_sel:[0,0,1] op_sel_hi:[1,0,1]\n\tv_pk_fma_f32 v[38:39], v[38:39], v[68:69], v[68:69] op_sel:[0,0,1] op_sel_hi:[1,0,1]\n\tv_pk_fma_f32 v[40:41], v[40:41], v[68:69], v[68:69] op_sel:[0,0,1] op_sel_hi:[1,0,1]\n\tv_cvt_pk_bf16_f32 v48, v24, v25\n\tv_cvt_pk_bf16_f32 v49, v26, v27\n\tv_cvt_pk_bf16_f32 v50, v28, v29\n\tv_cvt_pk_bf16_f32 v51, v30, v31\n\t" \
    "v_cvt_pk_bf16_f32 v52, v32, v33\n\tv_cvt_pk_bf16_f32 v53, v34, v35\n\tv_cvt_pk_bf16_f32 v54, v36, v37\n\tv_cvt_pk_bf16_f32 v55, v38, v39\n\tv_cvt_pk_bf16_f32 v56, v40, v41\n\tv_lshlrev_b32 v24, 16, v48\n\tv_and_b32 v25, 0xffff0000, v48\n\tv_lshlrev_b32 v26, 16, v49\n\t" \
    "v_and_b32 v27, 0xffff0000, v49\n\tv_lshlrev_b32 v28, 16, v50\n\tv_and_b32 v29, 0xffff0000, v50\n\tv_lshlrev_b32 v30, 16, v51\n\tv_and_b32 v31, 0xffff0000, v51\n\tv_lshlrev_b32 v32, 16, v52\n\tv_and_b32 v33, 0xffff0000, v52\n\tv_lshlrev_b32 v34, 16, v53\n\t" \
    "v_and_b32 v35, 0xffff0000, v53\n\tv_lshlrev_b32 v36, 16, v54\n\tv_and_b32 v37, 0xffff0000, v54\n\tv_lshlrev_b32 v38, 16, v55\n\tv_and_b32 v39, 0xffff0000, v55\n\tv_lshlrev_b32 v40, 16, v56\n\tv_and_b32 v41, 0xffff0000, v56\n\tv_pk_add_f32 v[70:71], v[24:25], v[26:27]\n\t" \
    "v_pk_add_f32 v[72:73], v[28:29], v[30:31]\n\tv_pk_add_f32 v[74:75], v[32:33], v[34:35]\n\tv_pk_add_f32 v[76:77], v[36:37], v[38:39]\n\tv_pk_mul_f32 v[44:45], v[24:25], v[24:25]\n\tv_pk_mul_f32 v[46:47], v[26:27], v[26:27]\n\tv_pk_add_f32 v[70:71], v[70:71], v[72:73]\n\tv_pk_add_f32 v[74:75], v[74:75], v[76:77]\n\tv_pk_fma_f32 v[44:45], v[28:29], v[28:29], v[44:45]\n\t" \
    "v_pk_fma_f32 v[46:47], v[30:31], v[30:31], v[46:47]\n\tv_pk_add_f32 v[70:71], v[70:71], v[40:41]\n\tv_pk_fma_f32 v[44:45], v[32:33], v[32:33], v[44:45]\n\tv_pk_fma_f32 v[46:47], v[34:35], v[34:35], v[46:47]\n\tv_pk_add_f32 v[70:71], v[70:71], v[74:75]\n\tv_pk_fma_f32 v[44:45], v[36:37], v[36:37], v[44:45]\n\tv_pk_fma_f32 v[46:47], v[38:39], v[38:39], v[46:47]\n\tv_pk_fma_f32 v[44:45], v[40:41], v[40:41], v[44:45]\n\t" \
    "v_pk_add_f32 v[44:45], v[44:45], v[46:47]\n\tv_add_f32 v66, v70, v71\n\tv_add_f32 v42, v44, v45\n\ts_nop 0\n\tv_add_f32_dpp v66, v66, v66 row_ror:8 row_mask:0xf bank_mask:0xf\n\tv_add_f32_dpp v42, v42, v42 row_ror:8 row_mask:0xf bank_mask:0xf\n\ts_nop 0\n\tv_add_f32_dpp v66, v66, v66 row_ror:4 row_mask:0xf bank_mask:0xf\n\t" \
    "v_add_f32_dpp v42, v42, v42 row_ror:4 row_mask:0xf bank_mask:0xf\n\ts_nop 0\n\tv_add_f32_dpp v66, v66, v66 row_ror:2 row_mask:0xf bank_mask:0xf\n\tv_add_f32_dpp v42, v42, v42 row_ror:2 row_mask:0xf bank_mask:0xf\n\ts_nop 0\n\tv_add_f32_dpp v66, v66, v66 row_ror:1 row_mask:0xf bank_mask:0xf\n\tv_add_f32_dpp v42, v42, v42 row_ror:1 row_mask:0xf bank_mask:0xf\n\tv_mov_b32 v67, v66\n\t" \
    "v_mov_b32 v43, v42\n\ts_nop 0\n\tv_permlane16_swap_b32 v66, v67\n\tv_permlane16_swap_b32 v42, v43\n\tv_add_f32 v66, v66, v67\n\tv_add_f32 v42, v42, v43\n\tv_mul_f32 v66, 0x3ae38e39, v66\n\tv_mul_f32 v42, 0x3ae38e39, v42\n\t" \
    "v_fma_f32 v42, -v66, v66, v42\n\tv_max_f32 v42, 0, v42\n\tv_cvt_pk_bf16_f32 v42, v42, v42\n\tv_lshlrev_b32 v42, 16, v42\n\tv_add_f32 v42, 0x3727c5ac, v42\n\tv_cvt_pk_bf16_f32 v42, v42, v42\n\tv_lshlrev_b32 v42, 16, v42\n\tv_sqrt_f32 v42, v42\n\t" \
    "v_cvt_pk_bf16_f32 v66, v66, v66\n\tv_lshlrev_b32 v66, 16, v66\n\tv_cvt_pk_bf16_f32 v42, v42, v42\n\tv_lshlrev_b32 v42, 16, v42\n\tv_rcp_f32 v42, v42\n\ts_nop 0\n\tv_mul_f32 v43, v66, v42\n\tv_xor_b32 v43, 0x80000000, v43\n\t" \
    "v_pk_fma_f32 v[24:25], v[24:25], v[42:43], v[42:43] op_sel:[0,0,1] op_sel_hi:[1,0,1]\n\tv_pk_fma_f32 v[26:27], v[26:27], v[42:43], v[42:43] op_sel:[0,0,1] op_sel_hi:[1,0,1]\n\tv_pk_fma_f32 v[28:29], v[28:29], v[42:43], v[42:43] op_sel:[0,0,1] op_sel_hi:[1,0,1]\n\tv_pk_fma_f32 v[30:31], v[30:31], v[42:43], v[42:43] op_sel:[0,0,1] op_sel_hi:[1,0,1]\n\tv_pk_fma_f32 v[32:33], v[32:33], v[42:43], v[42:43] op_sel:[0,0,1] op_sel_hi:[1,0,1]\n\tv_pk_fma_f32 v[34:35], v[34:35], v[42:43], v[42:43] op_sel:[0,0,1] op_sel_hi:[1,0,1]\n\tv_pk_fma_f32 v[36:37], v[36:37], v[42:43], v[42:43] op_sel:[0,0,1] op_sel_hi:[1,0,1]\n\tv_pk_fma_f32 v[38:39], v[38:39], v[42:43], v[42:43] op_sel:[0,0,1] op_sel_hi:[1,0,1]\n\t" \
    "v_pk_fma_f32 v[40:41], v[40:41], v[42:43], v[42:43] op_sel:[0,0,1] op_sel_hi:[1,0,1]\n\tv_cvt_pk_bf16_f32 v48, v24, v25\n\tv_cvt_pk_bf16_f32 v49, v26, v27\n\tv_cvt_pk_bf16_f32 v50, v28, v29\n\tv_cvt_pk_bf16_f32 v51, v30, v31\n\tv_cvt_pk_bf16_f32 v52, v32, v33\n\tv_cvt_pk_bf16_f32 v53, v34, v35\n\tv_cvt_pk_bf16_f32 v54, v36, v37\n\t" \
    "v_cvt_pk_bf16_f32 v55, v38, v39\n\tv_cvt_pk_bf16_f32 v56, v40, v41\n\tds_write_b128 v90, v[48:51]\n\tds_write_b128 v90, v[52:55] offset:512\n\tds_write_b32 v91, v56 offset:1024\n\ts_waitcnt vmcnt(11) lgkmcnt(0)\n\ts_barrier\n\tds_read_b64_tr_b16 v[24:25], v94 offset:0\n\t" \
    "ds_read_b64_tr_b16 v[26:27], v94 offset:4672\n\tds_read_b64_tr_b16 v[28:29], v94 offset:512\n\tds_read_b64_tr_b16 v[30:31], v94 offset:5184\n\tds_read_b64_tr_b16 v[32:33], v95 offset:1024\n\tds_read_b64_tr_b16 v[34:35], v95 offset:5696\n\tbuffer_store_dwordx2 v[68:69], v89, s[8:11], s64 offen sc1\n\tv_readfirstlane_b32 s36, v58\n\tv_readfirstlane_b32 s37, v59\n\t" \
    "s_lshl_b32 s73, s38, 12\n\ts_add_u32 s68, s61, s73\n\ts_add_u32 s36, s36, 0x7f4a7c15\n\ts_addc_u32 s37, s37, 0x9e3779b9\n\ts_waitcnt lgkmcnt(4)\n\tbuffer_store_dwordx4 v[24:27], v88, s[8:11], s68 offen sc1\n\ts_add_u32 s68, s68, 16384\n\ts_waitcnt lgkmcnt(2)\n\t" \
    "buffer_store_dwordx4 v[28:31], v88, s[8:11], s68 offen sc1\n\ts_add_u32 s68, s61, 32768\n\ts_cmp_eq_u32 s38, 0\n\ts_cselect_b32 s68, s68, 0x7ff00000\n\ts_waitcnt lgkmcnt(0)\n\tbuffer_store_dwordx4 v[32:35], v88, s[8:11], s68 offen sc1\n\tv_mov_b64 v[248:249], s[36:37]\n\tv_and_b32 v89, 7, v240\n\t" \
    "v_lshlrev_b32 v89, 11, v89\n\ts_cmp_ge_u32 s43, 21\n\ts_cselect_b32 s69, s70, s43\n\ts_mul_i32 s69, s69, 294912\n\ts_add_u32 s69, s69, 0\n\ts_add_u32 m0, s54, 8192\n\ts_nop 0\n\tbuffer_load_dwordx4 v11, s[12:15], s69 offen lds\n\t" \
    "s_xor_b32 s52, s72, 4\n\ts_mul_i32 s72, s72, 6656\n\ts_add_u32 s72, s72, 22347776\n\ts_mul_i32 s52, s52, 6656\n\ts_add_u32 s52, s52, 22347776\n\tv_mov_b32 v246, 0xbdd2d3e8\n\tv_mov_b32 v247, 0xc0135761\n\ts_lshr_b32 s65, s40, 3\n\t" \
    "s_mul_i32 s66, s65, 43\n\ts_lshr_b32 s66, s66, 7\n\ts_mul_i32 s6, s66, 3\n\ts_sub_u32 s6, s65, s6\n\ts_and_b32 s66, s66, 7\n\ts_and_b32 s7, s4, 1\n\ts_lshl_b32 s7, s7, 3\n\ts_add_u32 s7, s7, s66\n\t" \
    "s_mul_i32 s65, s5, 4718592\n\ts_lshl_b32 s67, s7, 8\n\ts_add_u32 s65, s65, s67\n\ts_add_u32 s16, s16, s65\n\ts_addc_u32 s17, s17, 0\n\ts_sub_u32 s18, s18, s65\n\ts_cmp_eq_u32 s39, 2\n\ts_cselect_b32 s18, s18, 0\n\t" \
    "v_lshrrev_b32 v16, 3, v240\n\ts_lshl_b32 s65, s38, 3\n\tv_add_u32 v16, s65, v16\n\tv_and_b32 v17, 0x70, v8\n\tv_lshlrev_b32 v18, 5, v245\n\tv_xor_b32 v17, v17, v18\n\tv_lshl_add_u32 v16, v16, 12, v17\n\ts_lshr_b32 s65, s5, 1\n\t" \
    "s_cmp_ge_u32 s43, 20\n\ts_cselect_b32 s69, s70, s43\n\ts_mul_i32 s69, s69, 294912\n\ts_add_u32 s69, s69, 294912\n\ts_add_u32 m0, s54, 12288\n\ts_nop 0\n\tbuffer_load_dwordx4 v11, s[12:15], s69 offen lds\n\ts_lshl_b32 s65, s65, 7\n\t" \
    "v_add_u32 v13, s65, v16\n\tv_xor_b32 v12, 128, v13\n\ts_lshl_b32 s65, s7, 7\n\ts_lshl_b32 s66, s5, 5\n\ts_add_u32 s65, s65, s66\n\ts_lshl_b32 s59, s65, 3\n\ts_add_u32 s59, s59, 58368\n\ts_lshl_b32 s66, s65, 1\n\t" \
    "s_add_u32 s32, s32, s66\n\ts_addc_u32 s33, s33, 0\n\ts_sub_u32 s34, s34, s66\n\ts_lshl_b32 s66, s6, 2\n\ts_add_u32 s66, s66, s38\n\ts_mul_i32 s67, s66, 96\n\ts_mul_i32 s65, s65, 1152\n\ts_add_u32 s65, s65, s67\n\t" \
    "s_add_u32 s24, s24, s65\n\ts_addc_u32 s25, s25, 0\n\ts_sub_u32 s26, s26, s65\n\ts_add_u32 s20, s20, s65\n\ts_addc_u32 s21, s21, 0\n\ts_sub_u32 s22, s22, s65\n\ts_lshl_b32 s51, s67, 2\n\ts_add_u32 s51, s51, 50176\n\t" \
    "s_lshl_b32 s66, s66, 2\n\ts_add_u32 s66, s66, s5\n\ts_mul_i32 s46, s66, 110592\n\ts_add_u32 s46, s46, 16646144\n\ts_lshl_b32 s50, s66, 13\n\ts_add_u32 s50, s50, 21954560\n\ts_mul_i32 s65, s5, 9\n\ts_sub_u32 s65, s42, s65\n\t" \
    "s_cmp_ge_u32 s43, 19\n\ts_cselect_b32 s69, s70, s43\n\ts_mul_i32 s69, s69, 294912\n\ts_add_u32 s69, s69, 589824\n\ts_add_u32 m0, s54, 16384\n\ts_nop 0\n\tbuffer_load_dwordx4 v10, s[12:15], s69 offen lds\n\ts_lshl_b32 s65, s65, 1\n\t" \
    "s_add_u32 s65, s65, s57\n\ts_mov_b32 s4, s65\n\ts_lshl_b32 s49, s65, 7\n\ts_add_u32 s49, s49, s50\n\ts_mul_i32 s45, s65, 3072\n\ts_add_u32 s45, s45, s46\n\ts_cmp_eq_u32 s39, 2\n\ts_cselect_b32 s46, s46, 2146435072\n\t" \
    "v_and_b32 v16, 31, v240\n\tv_mul_u32_u24 v243, 1152, v16\n\tv_lshrrev_b32 v16, 5, v240\n\tv_lshl_add_u32 v243, v16, 4, v243\n\tv_lshrrev_b32 v242, 1, v244\n\tv_lshlrev_b32 v236, 2, v240\n\tv_min_u32 v241, 17, v240\n\tv_lshlrev_b32 v241, 7, v241\n\t" \
    "s_lshl_b32 s65, s6, 4\n\ts_add_u32 s65, s65, s7\n\ts_lshl_b32 s65, s65, 2\n\ts_add_u32 s65, s65, s5\n\ts_lshl_b32 s65, s65, 2\n\ts_add_u32 s53, s65, s38\n\ts_mul_i32 s55, s53, 9984\n\ts_add_u32 s55, s55, 7208960\n\t" \
    "s_cmp_eq_u32 s39, 2\n\ts_cselect_b32 s55, s55, 2146435072\n\ts_mul_i32 s56, s53, 3072\n\ts_add_u32 s56, s56, 4849664\n\ts_or_b32 s65, s40, s38\n\ts_cselect_b32 s60, 2146435072, 0\n\ts_cmp_ge_u32 s43, 18\n\ts_cselect_b32 s69, s70, s43\n\t" \
    "s_mul_i32 s69, s69, 294912\n\ts_add_u32 s69, s69, 884736\n\ts_add_u32 m0, s54, 20480\n\ts_nop 0\n\tbuffer_load_dwordx4 v10, s[12:15], s69 offen lds\n\ts_cmp_ge_u32 s43, 19\n\ts_cselect_b32 s69, s70, s43\n\ts_mul_i32 s69, s69, 294912\n\t" \
    "s_add_u32 s69, s69, 589824\n\ts_add_u32 m0, s54, 24576\n\ts_nop 0\n\tbuffer_load_dwordx4 v11, s[12:15], s69 offen lds\n\ts_cmp_ge_u32 s43, 18\n\ts_cselect_b32 s69, s70, s43\n\ts_mul_i32 s69, s69, 294912\n\ts_add_u32 s69, s69, 884736\n\t" \
    "s_add_u32 m0, s54, 28672\n\ts_nop 0\n\tbuffer_load_dwordx4 v11, s[12:15], s69 offen lds\n\ts_cmp_ge_u32 s43, 17\n\ts_cselect_b32 s69, s70, s43\n\ts_mul_i32 s69, s69, 294912\n\ts_add_u32 s69, s69, 1179648\n\ts_add_u32 m0, s54, 32768\n\t" \
    "s_nop 0\n\tbuffer_load_dwordx4 v10, s[12:15], s69 offen lds\n\ts_cmp_ge_u32 s43, 16\n\ts_cselect_b32 s69, s70, s43\n\ts_mul_i32 s69, s69, 294912\n\ts_add_u32 s69, s69, 1474560\n\ts_add_u32 m0, s54, 36864\n\ts_nop 0\n\t" \
    "buffer_load_dwordx4 v10, s[12:15], s69 offen lds\n\ts_waitcnt vmcnt(14)\n\ts_barrier\n\tds_read_b64_tr_b16 v[36:37], v93 offset:0\n\tds_read_b64_tr_b16 v[38:39], v93 offset:4608\n\tds_read_b64_tr_b16 v[40:41], v93 offset:9216\n\tds_read_b64_tr_b16 v[42:43], v93 offset:13312\n\tds_read_b64_tr_b16 v[44:45], v93 offset:17920\n\t" \
    "ds_read_b64_tr_b16 v[46:47], v93 offset:512\n\tds_read_b64_tr_b16 v[48:49], v93 offset:5120\n\tds_read_b64_tr_b16 v[50:51], v93 offset:9728\n\tds_read_b64_tr_b16 v[52:53], v93 offset:13824\n\tds_read_b64_tr_b16 v[54:55], v93 offset:18432\n\tds_read_b64_tr_b16 v[120:121], v92 offset:1024\n\tds_read_b64_tr_b16 v[122:123], v92 offset:5632\n\tds_read_b64_tr_b16 v[124:125], v92 offset:10240\n\t" \
    "ds_read_b64_tr_b16 v[126:127], v92 offset:14336\n\tds_read_b64_tr_b16 v[128:129], v92 offset:18944\n\tv_lshlrev_b32 v78, 16, v60\n\tv_and_b32 v79, 0xffff0000, v60\n\tv_lshlrev_b32 v80, 16, v61\n\tv_and_b32 v81, 0xffff0000, v61\n\ts_waitcnt lgkmcnt(10)\n\tv_lshlrev_b32 v66, 16, v36\n\t" \
    "v_and_b32 v67, 0xffff0000, v36\n\tv_lshlrev_b32 v68, 16, v37\n\tv_and_b32 v69, 0xffff0000, v37\n\tv_lshlrev_b32 v70, 16, v38\n\tv_and_b32 v71, 0xffff0000, v38\n\tv_lshlrev_b32 v72, 16, v39\n\tv_and_b32 v73, 0xffff0000, v39\n\tv_lshlrev_b32 v74, 16, v40\n\t" \
    "v_and_b32 v75, 0xffff0000, v40\n\tv_lshlrev_b32 v76, 16, v41\n\tv_mul_f32 v16, v66, v81\n\tv_mul_f32 v17, v67, v81\n\tv_mul_f32 v18, v68, v81\n\tv_mul_f32 v19, v69, v81\n\tv_mul_f32 v20, v70, v81\n\tv_mul_f32 v21, v71, v81\n\t" \
    "v_mul_f32 v22, v72, v81\n\tv_mul_f32 v23, v73, v81\n\tv_fmac_f32 v16, v67, v80\n\tv_fmac_f32 v17, v68, v80\n\tv_fmac_f32 v18, v69, v80\n\tv_fmac_f32 v19, v70, v80\n\tv_fmac_f32 v20, v71, v80\n\tv_fmac_f32 v21, v72, v80\n\t" \
    "v_fmac_f32 v22, v73, v80\n\tv_fmac_f32 v23, v74, v80\n\tv_fmac_f32 v16, v68, v79\n\tv_fmac_f32 v17, v69, v79\n\tv_fmac_f32 v18, v70, v79\n\tv_fmac_f32 v19, v71, v79\n\tv_fmac_f32 v20, v72, v79\n\tv_fmac_f32 v21, v73, v79\n\t" \
    "v_fmac_f32 v22, v74, v79\n\tv_fmac_f32 v23, v75, v79\n\tv_fmac_f32 v16, v69, v78\n\tv_fmac_f32 v17, v70, v78\n\tv_fmac_f32 v18, v71, v78\n\tv_fmac_f32 v19, v72, v78\n\tv_fmac_f32 v20, v73, v78\n\tv_fmac_f32 v21, v74, v78\n\t" \
    "v_fmac_f32 v22, v75, v78\n\tv_fmac_f32 v23, v76, v78\n\tv_cvt_pk_bf16_f32 v66, v16, v17\n\tv_cvt_pk_bf16_f32 v67, v18, v19\n\tv_cvt_pk_bf16_f32 v68, v20, v21\n\tv_cvt_pk_bf16_f32 v69, v22, v23\n\tv_lshlrev_b32 v16, 16, v66\n\tv_and_b32 v17, 0xffff0000, v66\n\t" \
    "v_lshlrev_b32 v18, 16, v67\n\tv_and_b32 v19, 0xffff0000, v67\n\tv_lshlrev_b32 v20, 16, v68\n\tv_and_b32 v21, 0xffff0000, v68\n\tv_lshlrev_b32 v22, 16, v69\n\tv_and_b32 v23, 0xffff0000, v69\n\tv_lshlrev_b32 v66, 16, v42\n\tv_and_b32 v67, 0xffff0000, v42\n\t" \
    "v_lshlrev_b32 v68, 16, v43\n\tv_and_b32 v69, 0xffff0000, v43\n\tv_lshlrev_b32 v70, 16, v44\n\tv_and_b32 v71, 0xffff0000, v44\n\tv_lshlrev_b32 v72, 16, v45\n\tv_and_b32 v73, 0xffff0000, v45\n\tv_pk_mul_f32 v[16:17], v[16:17], v[66:67]\n\tv_pk_mul_f32 v[18:19], v[18:19], v[68:69]\n\t" \
    "v_pk_mul_f32 v[20:21], v[20:21], v[70:71]\n\tv_pk_mul_f32 v[22:23], v[22:23], v[72:73]\n\tv_cvt_pk_bf16_f32 v24, v16, v17\n\tv_cvt_pk_bf16_f32 v25, v18, v19\n\tv_cvt_pk_bf16_f32 v26, v20, v21\n\tv_cvt_pk_bf16_f32 v27, v22, v23\n\ts_add_u32 s68, s62, s54\n\tbuffer_store_dwordx4 v[24:27], v8, s[8:11], s68 offen sc1\n\t" \
    "s_waitcnt vmcnt(9) lgkmcnt(0)\n\ts_barrier\n\tbuffer_store_dwordx2 v[248:249], v89, s[8:11], s63 offen sc1\n\ts_cmp_lg_u32 s39, 0\n\ts_cbranch_scc1 104f\n\ts_sub_u32 s73, s40, 216\n\ts_lshl_b32 s71, s73, 4\n\ts_mul_i32 s68, s73, 12\n\t" \
    "s_add_u32 s68, s68, 96\n\ts_cmp_lt_u32 s73, 24\n\ts_cselect_b32 s71, s71, s68\n\ts_cselect_b32 s73, 0, 2146435072\n\tv_cmp_eq_u32 vcc, 0, v240\n\tv_subrev_u32 v190, 8, v8\n\tv_mov_b32 v191, 2146435072\n\tv_cndmask_b32 v190, v190, v191, vcc\n\t" \
    "s_add_u32 s68, s71, s38\n\ts_add_u32 s68, s68, 0\n\ts_lshl_b32 s73, s68, 1\n\ts_lshl_b32 s68, s68, 12\n\tbuffer_load_dwordx4 v[164:167], v8, s[76:79], s68 offen offset:0\n\tbuffer_load_dwordx4 v[168:171], v8, s[76:79], s68 offen offset:1024\n\tbuffer_load_dwordx4 v[172:175], v8, s[76:79], s68 offen offset:2048\n\tbuffer_load_dwordx4 v[176:179], v8, s[76:79], s68 offen offset:3072\n\t" \
    "buffer_load_dwordx2 v[180:181], v190, s[76:79], s68 offen\n\tbuffer_load_dwordx2 v[182:183], v8, s[76:79], s68 offen offset:1016\n\tbuffer_load_dwordx2 v[184:185], v8, s[76:79], s68 offen offset:2040\n\tbuffer_load_dwordx2 v[186:187], v8, s[76:79], s68 offen offset:3064\n\tbuffer_load_ushort v188, off, s[80:83], s73\n\t104:\n\tv_lshlrev_b32 v78, 16, v62\n\ts_cmp_ge_u32 s43, 17\n\t" \
    "s_cselect_b32 s69, s70, s43\n\ts_mul_i32 s69, s69, 294912\n\ts_add_u32 s69, s69, 1179648\n\ts_add_u32 m0, s54, 40960\n\tv_and_b32 v79, 0xffff0000, v62\n\tv_lshlrev_b32 v80, 16, v63\n\tv_and_b32 v81, 0xffff0000, v63\n\tv_lshlrev_b32 v66, 16, v46\n\t" \
    "v_and_b32 v67, 0xffff0000, v46\n\tv_lshlrev_b32 v68, 16, v47\n\tv_and_b32 v69, 0xffff0000, v47\n\tv_lshlrev_b32 v70, 16, v48\n\tv_and_b32 v71, 0xffff0000, v48\n\tv_lshlrev_b32 v72, 16, v49\n\tv_and_b32 v73, 0xffff0000, v49\n\tv_lshlrev_b32 v74, 16, v50\n\t" \
    "v_and_b32 v75, 0xffff0000, v50\n\tv_lshlrev_b32 v76, 16, v51\n\tv_mul_f32 v16, v66, v81\n\tv_mul_f32 v17, v67, v81\n\tv_mul_f32 v18, v68, v81\n\tv_mul_f32 v19, v69, v81\n\tv_mul_f32 v20, v70, v81\n\tv_mul_f32 v21, v71, v81\n\t" \
    "v_mul_f32 v22, v72, v81\n\tv_mul_f32 v23, v73, v81\n\tv_fmac_f32 v16, v67, v80\n\tv_fmac_f32 v17, v68, v80\n\tv_fmac_f32 v18, v69, v80\n\tv_fmac_f32 v19, v70, v80\n\tv_fmac_f32 v20, v71, v80\n\tv_fmac_f32 v21, v72, v80\n\t" \
    "v_fmac_f32 v22, v73, v80\n\tv_fmac_f32 v23, v74, v80\n\tv_fmac_f32 v16, v68, v79\n\tv_fmac_f32 v17, v69, v79\n\tv_fmac_f32 v18, v70, v79\n\tv_fmac_f32 v19, v71, v79\n\tv_fmac_f32 v20, v72, v79\n\tv_fmac_f32 v21, v73, v79\n\t" \
    "v_fmac_f32 v22, v74, v79\n\tv_fmac_f32 v23, v75, v79\n\tv_fmac_f32 v16, v69, v78\n\tv_fmac_f32 v17, v70, v78\n\tv_fmac_f32 v18, v71, v78\n\tv_fmac_f32 v19, v72, v78\n\tv_fmac_f32 v20, v73, v78\n\tv_fmac_f32 v21, v74, v78\n\t" \
    "v_fmac_f32 v22, v75, v78\n\tv_fmac_f32 v23, v76, v78\n\tv_cvt_pk_bf16_f32 v66, v16, v17\n\tv_cvt_pk_bf16_f32 v67, v18, v19\n\tv_cvt_pk_bf16_f32 v68, v20, v21\n\tv_cvt_pk_bf16_f32 v69, v22, v23\n\tv_lshlrev_b32 v16, 16, v66\n\tv_and_b32 v17, 0xffff0000, v66\n\t" \
    "v_lshlrev_b32 v18, 16, v67\n\tv_and_b32 v19, 0xffff0000, v67\n\tv_lshlrev_b32 v20, 16, v68\n\tv_and_b32 v21, 0xffff0000, v68\n\tv_lshlrev_b32 v22, 16, v69\n\tv_and_b32 v23, 0xffff0000, v69\n\tv_lshlrev_b32 v66, 16, v52\n\tv_and_b32 v67, 0xffff0000, v52\n\t" \
    "v_lshlrev_b32 v68, 16, v53\n\tv_and_b32 v69, 0xffff0000, v53\n\tv_lshlrev_b32 v70, 16, v54\n\tv_and_b32 v71, 0xffff0000, v54\n\tv_lshlrev_b32 v72, 16, v55\n\tv_and_b32 v73, 0xffff0000, v55\n\tv_pk_mul_f32 v[16:17], v[16:17], v[66:67]\n\tv_pk_mul_f32 v[18:19], v[18:19], v[68:69]\n\t" \
    "v_pk_mul_f32 v[20:21], v[20:21], v[70:71]\n\tv_pk_mul_f32 v[22:23], v[22:23], v[72:73]\n\tv_cvt_pk_bf16_f32 v28, v16, v17\n\tv_cvt_pk_bf16_f32 v29, v18, v19\n\tv_cvt_pk_bf16_f32 v30, v20, v21\n\tv_cvt_pk_bf16_f32 v31, v22, v23\n\ts_add_u32 s68, s62, s54\n\tbuffer_store_dwordx4 v[28:31], v9, s[8:11], s68 offen sc1\n\t" \
    "v_lshlrev_b32 v78, 16, v64\n\tv_and_b32 v79, 0xffff0000, v64\n\tv_lshlrev_b32 v80, 16, v65\n\tv_and_b32 v81, 0xffff0000, v65\n\tv_lshlrev_b32 v66, 16, v120\n\tv_and_b32 v67, 0xffff0000, v120\n\tv_lshlrev_b32 v68, 16, v121\n\tv_and_b32 v69, 0xffff0000, v121\n\t" \
    "v_lshlrev_b32 v70, 16, v122\n\tv_and_b32 v71, 0xffff0000, v122\n\tv_lshlrev_b32 v72, 16, v123\n\tv_and_b32 v73, 0xffff0000, v123\n\tv_lshlrev_b32 v74, 16, v124\n\tv_and_b32 v75, 0xffff0000, v124\n\tv_lshlrev_b32 v76, 16, v125\n\tv_mul_f32 v16, v66, v81\n\t" \
    "v_mul_f32 v17, v67, v81\n\tv_mul_f32 v18, v68, v81\n\tv_mul_f32 v19, v69, v81\n\tv_mul_f32 v20, v70, v81\n\tv_mul_f32 v21, v71, v81\n\tv_mul_f32 v22, v72, v81\n\tv_mul_f32 v23, v73, v81\n\tv_fmac_f32 v16, v67, v80\n\t" \
    "v_fmac_f32 v17, v68, v80\n\tv_fmac_f32 v18, v69, v80\n\tv_fmac_f32 v19, v70, v80\n\tv_fmac_f32 v20, v71, v80\n\tv_fmac_f32 v21, v72, v80\n\tv_fmac_f32 v22, v73, v80\n\tv_fmac_f32 v23, v74, v80\n\tv_fmac_f32 v16, v68, v79\n\t" \
    "v_fmac_f32 v17, v69, v79\n\tv_fmac_f32 v18, v70, v79\n\tv_fmac_f32 v19, v71, v79\n\tv_fmac_f32 v20, v72, v79\n\tv_fmac_f32 v21, v73, v79\n\tv_fmac_f32 v22, v74, v79\n\tv_fmac_f32 v23, v75, v79\n\tv_fmac_f32 v16, v69, v78\n\t" \
    "v_fmac_f32 v17, v70, v78\n\tv_fmac_f32 v18, v71, v78\n\tv_fmac_f32 v19, v72, v78\n\tv_fmac_f32 v20, v73, v78\n\tv_fmac_f32 v21, v74, v78\n\tv_fmac_f32 v22, v75, v78\n\tv_fmac_f32 v23, v76, v78\n\tv_cvt_pk_bf16_f32 v66, v16, v17\n\t" \
    "v_cvt_pk_bf16_f32 v67, v18, v19\n\tv_cvt_pk_bf16_f32 v68, v20, v21\n\tv_cvt_pk_bf16_f32 v69, v22, v23\n\tv_lshlrev_b32 v16, 16, v66\n\tv_and_b32 v17, 0xffff0000, v66\n\tv_lshlrev_b32 v18, 16, v67\n\tv_and_b32 v19, 0xffff0000, v67\n\tv_lshlrev_b32 v20, 16, v68\n\t" \
    "v_and_b32 v21, 0xffff0000, v68\n\tv_lshlrev_b32 v22, 16, v69\n\tv_and_b32 v23, 0xffff0000, v69\n\tv_lshlrev_b32 v66, 16, v126\n\tv_and_b32 v67, 0xffff0000, v126\n\tv_lshlrev_b32 v68, 16, v127\n\tv_and_b32 v69, 0xffff0000, v127\n\tv_lshlrev_b32 v70, 16, v128\n\t" \
    "v_and_b32 v71, 0xffff0000, v128\n\tv_lshlrev_b32 v72, 16, v129\n\tv_and_b32 v73, 0xffff0000, v129\n\tv_pk_mul_f32 v[16:17], v[16:17], v[66:67]\n\tv_pk_mul_f32 v[18:19], v[18:19], v[68:69]\n\tv_pk_mul_f32 v[20:21], v[20:21], v[70:71]\n\tv_pk_mul_f32 v[22:23], v[22:23], v[72:73]\n\tv_cvt_pk_bf16_f32 v32, v16, v17\n\t" \
    "v_cvt_pk_bf16_f32 v33, v18, v19\n\tv_cvt_pk_bf16_f32 v34, v20, v21\n\tv_cvt_pk_bf16_f32 v35, v22, v23\n\ts_add_u32 s68, s62, 8192\n\ts_cmp_eq_u32 s38, 1\n\ts_cselect_b32 s68, s68, 0x7ff00000\n\tbuffer_store_dwordx4 v[32:35], v8, s[8:11], s68 offen sc1\n\ts_cmp_eq_u32 s39, 0\n\t" \
    "s_cbranch_scc1 101f\n\tbuffer_load_dwordx4 v11, s[12:15], s69 offen lds\n\ts_cmp_ge_u32 s43, 16\n\ts_cselect_b32 s69, s70, s43\n\ts_mul_i32 s69, s69, 294912\n\ts_add_u32 s69, s69, 1474560\n\ts_add_u32 m0, s54, 45056\n\ts_nop 0\n\t" \
    "buffer_load_dwordx4 v11, s[12:15], s69 offen lds\n\ts_cmp_ge_u32 s43, 15\n\ts_cselect_b32 s69, s70, s43\n\ts_mul_i32 s69, s69, 294912\n\ts_add_u32 s69, s69, 1769472\n\ts_add_u32 m0, s54, 49152\n\ts_nop 0\n\tbuffer_load_dwordx4 v10, s[12:15], s69 offen lds\n\t" \
    "s_waitcnt vmcnt(3)\n\tbuffer_store_dwordx2 v[248:249], off, s[8:11], s48 sc1\n\ts_cmp_ge_u32 s43, 14\n\ts_cselect_b32 s69, s70, s43\n\ts_mul_i32 s69, s69, 294912\n\ts_add_u32 s69, s69, 2064384\n\ts_add_u32 m0, s54, 53248\n\ts_nop 0\n\t" \
    "buffer_load_dwordx4 v10, s[12:15], s69 offen lds\n\ts_cmp_ge_u32 s43, 15\n\ts_cselect_b32 s69, s70, s43\n\ts_mul_i32 s69, s69, 294912\n\ts_add_u32 s69, s69, 1769472\n\ts_add_u32 m0, s54, 57344\n\ts_nop 0\n\tbuffer_load_dwordx4 v11, s[12:15], s69 offen lds\n\t" \
    "s_cmp_ge_u32 s43, 14\n\ts_cselect_b32 s69, s70, s43\n\ts_mul_i32 s69, s69, 294912\n\ts_add_u32 s69, s69, 2064384\n\ts_add_u32 m0, s54, 61440\n\ts_nop 0\n\tbuffer_load_dwordx4 v11, s[12:15], s69 offen lds\n\ts_cmp_ge_u32 s43, 13\n\t" \
    "s_cselect_b32 s69, s70, s43\n\ts_mul_i32 s69, s69, 294912\n\ts_add_u32 s69, s69, 2359296\n\ts_add_u32 m0, s54, 65536\n\ts_nop 0\n\tbuffer_load_dwordx4 v10, s[12:15], s69 offen lds\n\ts_cmp_ge_u32 s43, 12\n\ts_cselect_b32 s69, s70, s43\n\t" \
    "s_mul_i32 s69, s69, 294912\n\ts_add_u32 s69, s69, 2654208\n\ts_add_u32 m0, s54, 69632\n\ts_nop 0\n\tbuffer_load_dwordx4 v10, s[12:15], s69 offen lds\n\ts_cmp_ge_u32 s43, 13\n\ts_cselect_b32 s69, s70, s43\n\ts_mul_i32 s69, s69, 294912\n\t" \
    "s_add_u32 s69, s69, 2359296\n\ts_add_u32 m0, s54, 73728\n\ts_nop 0\n\tbuffer_load_dwordx4 v11, s[12:15], s69 offen lds\n\ts_cmp_ge_u32 s43, 12\n\ts_cselect_b32 s69, s70, s43\n\ts_mul_i32 s69, s69, 294912\n\ts_add_u32 s69, s69, 2654208\n\t" \
    "s_add_u32 m0, s54, 77824\n\ts_nop 0\n\tbuffer_load_dwordx4 v11, s[12:15], s69 offen lds\n\ts_cmp_ge_u32 s43, 11\n\ts_cselect_b32 s69, s70, s43\n\ts_mul_i32 s69, s69, 294912\n\ts_add_u32 s69, s69, 2949120\n\ts_add_u32 m0, s54, 81920\n\t" \
    "s_nop 0\n\tbuffer_load_dwordx4 v10, s[12:15], s69 offen lds\n\ts_lshl_b32 s71, s57, 7\n\ts_lshl_b32 s69, s42, 8\n\ts_add_u32 s71, s71, s69\n\tv_lshlrev_b32 v96, 3, v245\n\tbuffer_load_dwordx2 v[112:113], v96, s[28:31], s71 offen offset:0\n\tbuffer_load_dwordx2 v[114:115], v96, s[28:31], s71 offen offset:32\n\t" \
    "buffer_load_dwordx2 v[116:117], v96, s[28:31], s71 offen offset:64\n\tbuffer_load_dwordx2 v[118:119], v96, s[28:31], s71 offen offset:96\n\tv_lshrrev_b32 v229, 3, v244\n\tbuffer_load_ushort v228, v229, s[32:35], 0 offen offset:0\n\tbuffer_load_ushort v232, v229, s[32:35], 0 offen offset:32\n\ts_cmp_ge_u32 s43, 10\n\ts_cselect_b32 s69, s70, s43\n\ts_mul_i32 s69, s69, 294912\n\t" \
    "s_add_u32 s69, s69, 3244032\n\ts_add_u32 m0, s54, 86016\n\ts_nop 0\n\tbuffer_load_dwordx4 v10, s[12:15], s69 offen lds\n\tbuffer_load_dwordx4 v[250:253], v8, s[8:11], s47 offen sc1\n\ts_cmp_ge_u32 s43, 11\n\ts_cselect_b32 s69, s70, s43\n\ts_mul_i32 s69, s69, 294912\n\t" \
    "s_add_u32 s69, s69, 2949120\n\ts_add_u32 m0, s54, 90112\n\ts_nop 0\n\tbuffer_load_dwordx4 v11, s[12:15], s69 offen lds\n\ts_cmp_ge_u32 s43, 10\n\ts_cselect_b32 s69, s70, s43\n\ts_mul_i32 s69, s69, 294912\n\ts_add_u32 s69, s69, 3244032\n\t" \
    "s_add_u32 m0, s54, 94208\n\ts_nop 0\n\tbuffer_load_dwordx4 v11, s[12:15], s69 offen lds\n\ts_cmp_ge_u32 s43, 9\n\ts_cselect_b32 s69, s70, s43\n\ts_mul_i32 s69, s69, 294912\n\ts_add_u32 s69, s69, 3538944\n\ts_add_u32 m0, s54, 98304\n\t" \
    "s_nop 0\n\tbuffer_load_dwordx4 v10, s[12:15], s69 offen lds\n\ts_cmp_ge_u32 s43, 8\n\ts_cselect_b32 s69, s70, s43\n\ts_mul_i32 s69, s69, 294912\n\ts_add_u32 s69, s69, 3833856\n\ts_add_u32 m0, s54, 102400\n\ts_nop 0\n\t" \
    "buffer_load_dwordx4 v10, s[12:15], s69 offen lds\n\ts_cmp_ge_u32 s43, 7\n\ts_cselect_b32 s69, s70, s43\n\ts_mul_i32 s69, s69, 294912\n\ts_add_u32 s69, s69, 4128768\n\ts_add_u32 m0, s54, 106496\n\ts_nop 0\n\tbuffer_load_dwordx4 v10, s[12:15], s69 offen lds\n\t" \
    "s_cmp_ge_u32 s43, 6\n\ts_cselect_b32 s69, s70, s43\n\ts_mul_i32 s69, s69, 294912\n\ts_add_u32 s69, s69, 4423680\n\ts_add_u32 m0, s54, 110592\n\ts_nop 0\n\tbuffer_load_dwordx4 v10, s[12:15], s69 offen lds\n\ts_cmp_ge_u32 s43, 5\n\t" \
    "s_cselect_b32 s69, s70, s43\n\ts_mul_i32 s69, s69, 294912\n\ts_add_u32 s69, s69, 4718592\n\ts_add_u32 m0, s54, 114688\n\ts_nop 0\n\tbuffer_load_dwordx4 v10, s[12:15], s69 offen lds\n\ts_cmp_ge_u32 s43, 4\n\ts_cselect_b32 s69, s70, s43\n\t" \
    "s_mul_i32 s69, s69, 294912\n\ts_add_u32 s69, s69, 5013504\n\ts_add_u32 m0, s54, 118784\n\ts_nop 0\n\tbuffer_load_dwordx4 v10, s[12:15], s69 offen lds\n\ts_cmp_ge_u32 s43, 3\n\ts_cselect_b32 s69, s70, s43\n\ts_mul_i32 s69, s69, 294912\n\t" \
    "s_add_u32 s69, s69, 5308416\n\ts_add_u32 m0, s54, 122880\n\ts_nop 0\n\tbuffer_load_dwordx4 v10, s[12:15], s69 offen lds\n\ts_cmp_ge_u32 s43, 2\n\ts_cselect_b32 s69, s70, s43\n\ts_mul_i32 s69, s69, 294912\n\ts_add_u32 s69, s69, 5603328\n\t" \
    "s_add_u32 m0, s54, 126976\n\ts_nop 0\n\tbuffer_load_dwordx4 v10, s[12:15], s69 offen lds\n\ts_waitcnt vmcnt(10)\n\tv_cmp_ne_u64 vcc, s[36:37], v[250:251]\n\tv_cmp_ne_u64 s[74:75], s[36:37], v[252:253]\n\ts_or_b64 s[74:75], s[74:75], vcc\n\ts_cbranch_scc0 106f\n\t" \
    "s_mov_b32 s71, 1048576\n\t105:\n\ts_sleep 1\n\tbuffer_load_dwordx4 v[250:253], v8, s[8:11], s47 offen sc1\n\ts_waitcnt vmcnt(0)\n\tv_cmp_ne_u64 vcc, s[36:37], v[250:251]\n\tv_cmp_ne_u64 s[74:75], s[36:37], v[252:253]\n\ts_or_b64 s[74:75], s[74:75], vcc\n\t" \
    "s_cbranch_scc0 106f\n\ts_sub_u32 s71, s71, 1\n\ts_cbranch_scc0 105b\n\t106:\n\ts_cmp_ge_u32 s43, 21\n\ts_cselect_b32 s68, s70, s43\n\ts_mul_i32 s68, s68, 36864\n\ts_add_u32 s68, s68, s44\n\t" \
    "s_add_u32 s68, s68, 0\n\tbuffer_load_dwordx4 v[192:195], v8, s[8:11], s68 offen offset:0 sc1\n\tbuffer_load_dwordx4 v[196:199], v8, s[8:11], s68 offen offset:1024 sc1\n\tbuffer_load_dwordx4 v[200:203], v8, s[8:11], s68 offen offset:2048 sc1\n\ts_cmp_ge_u32 s43, 20\n\ts_cselect_b32 s68, s70, s43\n\ts_mul_i32 s68, s68, 36864\n\ts_add_u32 s68, s68, s44\n\t" \
    "s_add_u32 s68, s68, 36864\n\tbuffer_load_dwordx4 v[168:171], v8, s[8:11], s68 offen offset:0 sc1\n\tbuffer_load_dwordx4 v[172:175], v8, s[8:11], s68 offen offset:1024 sc1\n\tbuffer_load_dwordx4 v[176:179], v8, s[8:11], s68 offen offset:2048 sc1\n\ts_cmp_ge_u32 s43, 19\n\ts_cselect_b32 s68, s70, s43\n\ts_mul_i32 s68, s68, 36864\n\ts_add_u32 s68, s68, s44\n\t" \
    "s_add_u32 s68, s68, 73728\n\tbuffer_load_dwordx4 v[180:183], v8, s[8:11], s68 offen offset:0 sc1\n\tbuffer_load_dwordx4 v[184:187], v8, s[8:11], s68 offen offset:1024 sc1\n\tbuffer_load_dwordx4 v[188:191], v8, s[8:11], s68 offen offset:2048 sc1\n\ts_cmp_ge_u32 s43, 18\n\ts_cselect_b32 s68, s70, s43\n\ts_mul_i32 s68, s68, 36864\n\ts_add_u32 s68, s68, s44\n\t" \
    "s_add_u32 s68, s68, 110592\n\tbuffer_load_dwordx4 v[204:207], v8, s[8:11], s68 offen offset:0 sc1\n\tbuffer_load_dwordx4 v[208:211], v8, s[8:11], s68 offen offset:1024 sc1\n\tbuffer_load_dwordx4 v[212:215], v8, s[8:11], s68 offen offset:2048 sc1\n\tv_lshlrev_b32 v96, 16, v112\n\tv_and_b32 v97, 0xffff0000, v112\n\tv_lshlrev_b32 v98, 16, v113\n\tv_and_b32 v99, 0xffff0000, v113\n\t" \
    "v_lshlrev_b32 v100, 16, v114\n\tv_and_b32 v101, 0xffff0000, v114\n\tv_lshlrev_b32 v102, 16, v115\n\tv_and_b32 v103, 0xffff0000, v115\n\tv_lshlrev_b32 v104, 16, v116\n\tv_and_b32 v105, 0xffff0000, v116\n\tv_lshlrev_b32 v106, 16, v117\n\tv_and_b32 v107, 0xffff0000, v117\n\t" \
    "v_lshlrev_b32 v108, 16, v118\n\tv_and_b32 v109, 0xffff0000, v118\n\tv_lshlrev_b32 v110, 16, v119\n\tv_and_b32 v111, 0xffff0000, v119\n\tv_lshlrev_b32 v228, 16, v228\n\tv_mov_b32 v229, v228\n\tv_mov_b32 v230, v228\n\tv_mov_b32 v231, v228\n\t" \
    "v_lshlrev_b32 v232, 16, v232\n\tv_mov_b32 v233, v232\n\tv_mov_b32 v234, v232\n\tv_mov_b32 v235, v232\n\ts_barrier\n\tds_read_b64_tr_b16 v[16:17], v0 offset:0\n\tds_read_b64_tr_b16 v[18:19], v0 offset:512\n\tds_read_b64_tr_b16 v[20:21], v0 offset:4096\n\t" \
    "ds_read_b64_tr_b16 v[22:23], v0 offset:4608\n\tds_read_b64_tr_b16 v[24:25], v1 offset:0\n\tds_read_b64_tr_b16 v[26:27], v1 offset:512\n\tds_read_b64_tr_b16 v[28:29], v1 offset:4096\n\tds_read_b64_tr_b16 v[30:31], v1 offset:4608\n\tds_read_b64_tr_b16 v[32:33], v2 offset:0\n\tds_read_b64_tr_b16 v[34:35], v2 offset:512\n\tds_read_b64_tr_b16 v[36:37], v2 offset:4096\n\t" \
    "ds_read_b64_tr_b16 v[38:39], v2 offset:4608\n\ts_waitcnt vmcnt(9)\n\ts_cmp_ge_u32 s43, 17\n\ts_cselect_b32 s68, s70, s43\n\ts_mul_i32 s68, s68, 36864\n\ts_add_u32 s68, s68, s44\n\ts_add_u32 s68, s68, 147456\n\tbuffer_load_dwordx4 v[216:219], v8, s[8:11], s68 offen offset:0 sc1\n\t" \
    "buffer_load_dwordx4 a[0:3], v8, s[8:11], s68 offen offset:1024 sc1\n\tbuffer_load_dwordx4 a[4:7], v8, s[8:11], s68 offen offset:2048 sc1\n\ts_cmp_ge_u32 s43, 16\n\ts_cselect_b32 s68, s70, s43\n\ts_mul_i32 s68, s68, 36864\n\ts_add_u32 s68, s68, s44\n\ts_add_u32 s68, s68, 184320\n\tbuffer_load_dwordx4 a[8:11], v8, s[8:11], s68 offen offset:0 sc1\n\t" \
    "buffer_load_dwordx4 a[12:15], v8, s[8:11], s68 offen offset:1024 sc1\n\ts_waitcnt vmcnt(11) lgkmcnt(8)\n\tv_mfma_f32_16x16x32_bf16 v[120:123], v[16:19], v[192:195], 0\n\tds_read_b64_tr_b16 v[40:41], v3 offset:0\n\tds_read_b64_tr_b16 v[42:43], v3 offset:512\n\tv_mfma_f32_16x16x32_bf16 v[120:123], v[20:23], v[168:171], v[120:123]\n\tv_mfma_f32_16x16x32_bf16 v[128:131], v[16:19], v[196:199], 0\n\tv_mfma_f32_16x16x32_bf16 v[128:131], v[20:23], v[172:175], v[128:131]\n\t" \
    "ds_read_b64_tr_b16 v[44:45], v3 offset:4096\n\tds_read_b64_tr_b16 v[46:47], v3 offset:4608\n\tv_mfma_f32_16x16x32_bf16 v[136:139], v[16:19], v[200:203], 0\n\tbuffer_load_dwordx4 a[16:19], v8, s[8:11], s68 offen offset:2048 sc1\n\ts_cmp_ge_u32 s43, 15\n\ts_cselect_b32 s68, s70, s43\n\ts_mul_i32 s68, s68, 36864\n\tv_mfma_f32_16x16x32_bf16 v[136:139], v[20:23], v[176:179], v[136:139]\n\t" \
    "s_add_u32 s68, s68, s44\n\ts_add_u32 s68, s68, 221184\n\ts_waitcnt lgkmcnt(8)\n\tv_mfma_f32_16x16x32_bf16 v[124:127], v[24:27], v[192:195], 0\n\tds_read_b64_tr_b16 v[16:17], v0 offset:8192\n\tds_read_b64_tr_b16 v[18:19], v0 offset:8704\n\tv_mfma_f32_16x16x32_bf16 v[124:127], v[28:31], v[168:171], v[124:127]\n\tbuffer_load_dwordx4 a[20:23], v8, s[8:11], s68 offen offset:0 sc1\n\t" \
    "v_mfma_f32_16x16x32_bf16 v[132:135], v[24:27], v[196:199], 0\n\tv_mfma_f32_16x16x32_bf16 v[132:135], v[28:31], v[172:175], v[132:135]\n\tds_read_b64_tr_b16 v[20:21], v0 offset:12288\n\tds_read_b64_tr_b16 v[22:23], v0 offset:12800\n\tv_mfma_f32_16x16x32_bf16 v[140:143], v[24:27], v[200:203], 0\n\tbuffer_load_dwordx4 a[24:27], v8, s[8:11], s68 offen offset:1024 sc1\n\tv_mfma_f32_16x16x32_bf16 v[140:143], v[28:31], v[176:179], v[140:143]\n\ts_waitcnt lgkmcnt(8)\n\t" \
    "v_mfma_f32_16x16x32_bf16 v[144:147], v[32:35], v[192:195], 0\n\tds_read_b64_tr_b16 v[24:25], v1 offset:8192\n\tds_read_b64_tr_b16 v[26:27], v1 offset:8704\n\tv_mfma_f32_16x16x32_bf16 v[144:147], v[36:39], v[168:171], v[144:147]\n\tv_mfma_f32_16x16x32_bf16 v[152:155], v[32:35], v[196:199], 0\n\tv_mfma_f32_16x16x32_bf16 v[152:155], v[36:39], v[172:175], v[152:155]\n\tds_read_b64_tr_b16 v[28:29], v1 offset:12288\n\tds_read_b64_tr_b16 v[30:31], v1 offset:12800\n\t" \
    "v_mfma_f32_16x16x32_bf16 v[160:163], v[32:35], v[200:203], 0\n\tbuffer_load_dwordx4 a[28:31], v8, s[8:11], s68 offen offset:2048 sc1\n\ts_cmp_ge_u32 s43, 14\n\ts_cselect_b32 s68, s70, s43\n\ts_mul_i32 s68, s68, 36864\n\tv_mfma_f32_16x16x32_bf16 v[160:163], v[36:39], v[176:179], v[160:163]\n\ts_add_u32 s68, s68, s44\n\ts_add_u32 s68, s68, 258048\n\t" \
    "s_waitcnt lgkmcnt(8)\n\tv_mfma_f32_16x16x32_bf16 v[148:151], v[40:43], v[192:195], 0\n\tds_read_b64_tr_b16 v[32:33], v2 offset:8192\n\tds_read_b64_tr_b16 v[34:35], v2 offset:8704\n\tv_mfma_f32_16x16x32_bf16 v[148:151], v[44:47], v[168:171], v[148:151]\n\tbuffer_load_dwordx4 a[32:35], v8, s[8:11], s68 offen offset:0 sc1\n\tv_mfma_f32_16x16x32_bf16 v[156:159], v[40:43], v[196:199], 0\n\tv_mfma_f32_16x16x32_bf16 v[156:159], v[44:47], v[172:175], v[156:159]\n\t" \
    "ds_read_b64_tr_b16 v[36:37], v2 offset:12288\n\tds_read_b64_tr_b16 v[38:39], v2 offset:12800\n\tv_mfma_f32_16x16x32_bf16 v[164:167], v[40:43], v[200:203], 0\n\tbuffer_load_dwordx4 a[36:39], v8, s[8:11], s68 offen offset:1024 sc1\n\tv_mfma_f32_16x16x32_bf16 v[164:167], v[44:47], v[176:179], v[164:167]\n\ts_waitcnt lgkmcnt(8)\n\tv_mfma_f32_16x16x32_bf16 v[48:51], v[16:19], v[192:195], v[96:99]\n\tds_read_b64_tr_b16 v[40:41], v3 offset:8192\n\t" \
    "ds_read_b64_tr_b16 v[42:43], v3 offset:8704\n\tv_mfma_f32_16x16x32_bf16 v[48:51], v[20:23], v[168:171], v[48:51]\n\tv_mfma_f32_16x16x32_bf16 v[56:59], v[16:19], v[196:199], v[96:99]\n\tv_mfma_f32_16x16x32_bf16 v[56:59], v[20:23], v[172:175], v[56:59]\n\tds_read_b64_tr_b16 v[44:45], v3 offset:12288\n\tds_read_b64_tr_b16 v[46:47], v3 offset:12800\n\tv_mfma_f32_16x16x32_bf16 v[64:67], v[16:19], v[200:203], v[96:99]\n\tbuffer_load_dwordx4 a[40:43], v8, s[8:11], s68 offen offset:2048 sc1\n\t" \
    "s_cmp_ge_u32 s43, 13\n\ts_cselect_b32 s68, s70, s43\n\ts_mul_i32 s68, s68, 36864\n\tv_mfma_f32_16x16x32_bf16 v[64:67], v[20:23], v[176:179], v[64:67]\n\ts_add_u32 s68, s68, s44\n\ts_add_u32 s68, s68, 294912\n\ts_waitcnt lgkmcnt(8)\n\tv_mfma_f32_16x16x32_bf16 v[52:55], v[24:27], v[192:195], v[100:103]\n\t" \
    "ds_read_b64_tr_b16 v[16:17], v0 offset:16384\n\tds_read_b64_tr_b16 v[18:19], v0 offset:16896\n\tv_mfma_f32_16x16x32_bf16 v[52:55], v[28:31], v[168:171], v[52:55]\n\tbuffer_load_dwordx4 a[44:47], v8, s[8:11], s68 offen offset:0 sc1\n\tv_mfma_f32_16x16x32_bf16 v[60:63], v[24:27], v[196:199], v[100:103]\n\tv_mfma_f32_16x16x32_bf16 v[60:63], v[28:31], v[172:175], v[60:63]\n\tds_read_b64_tr_b16 v[20:21], v0 offset:20480\n\tds_read_b64_tr_b16 v[22:23], v0 offset:20992\n\t" \
    "v_mfma_f32_16x16x32_bf16 v[68:71], v[24:27], v[200:203], v[100:103]\n\tv_mfma_f32_16x16x32_bf16 v[68:71], v[28:31], v[176:179], v[68:71]\n\ts_waitcnt lgkmcnt(8)\n\tv_mfma_f32_16x16x32_bf16 v[72:75], v[32:35], v[192:195], v[104:107]\n\tds_read_b64_tr_b16 v[24:25], v1 offset:16384\n\tds_read_b64_tr_b16 v[26:27], v1 offset:16896\n\tv_mfma_f32_16x16x32_bf16 v[72:75], v[36:39], v[168:171], v[72:75]\n\tbuffer_load_dwordx4 a[48:51], v8, s[8:11], s68 offen offset:1024 sc1\n\t" \
    "v_mfma_f32_16x16x32_bf16 v[80:83], v[32:35], v[196:199], v[104:107]\n\tv_mfma_f32_16x16x32_bf16 v[80:83], v[36:39], v[172:175], v[80:83]\n\tds_read_b64_tr_b16 v[28:29], v1 offset:20480\n\tds_read_b64_tr_b16 v[30:31], v1 offset:20992\n\tv_mfma_f32_16x16x32_bf16 v[88:91], v[32:35], v[200:203], v[104:107]\n\tbuffer_load_dwordx4 a[52:55], v8, s[8:11], s68 offen offset:2048 sc1\n\ts_cmp_ge_u32 s43, 12\n\ts_cselect_b32 s68, s70, s43\n\t" \
    "s_mul_i32 s68, s68, 36864\n\tv_mfma_f32_16x16x32_bf16 v[88:91], v[36:39], v[176:179], v[88:91]\n\ts_add_u32 s68, s68, s44\n\ts_add_u32 s68, s68, 331776\n\ts_waitcnt lgkmcnt(8)\n\tv_mfma_f32_16x16x32_bf16 v[76:79], v[40:43], v[192:195], v[108:111]\n\tds_read_b64_tr_b16 v[32:33], v2 offset:16384\n\tds_read_b64_tr_b16 v[34:35], v2 offset:16896\n\t" \
    "v_mfma_f32_16x16x32_bf16 v[76:79], v[44:47], v[168:171], v[76:79]\n\tbuffer_load_dwordx4 a[56:59], v8, s[8:11], s68 offen offset:0 sc1\n\tv_mfma_f32_16x16x32_bf16 v[84:87], v[40:43], v[196:199], v[108:111]\n\tv_mfma_f32_16x16x32_bf16 v[84:87], v[44:47], v[172:175], v[84:87]\n\tds_read_b64_tr_b16 v[36:37], v2 offset:20480\n\tds_read_b64_tr_b16 v[38:39], v2 offset:20992\n\tv_mfma_f32_16x16x32_bf16 v[92:95], v[40:43], v[200:203], v[108:111]\n\tv_mfma_f32_16x16x32_bf16 v[92:95], v[44:47], v[176:179], v[92:95]\n\t" \
    "s_barrier\n\ts_waitcnt vmcnt(16) lgkmcnt(8)\n\tv_mfma_f32_16x16x32_bf16 v[120:123], v[16:19], v[180:183], v[120:123]\n\tds_read_b64_tr_b16 v[40:41], v3 offset:16384\n\tds_read_b64_tr_b16 v[42:43], v3 offset:16896\n\tv_mfma_f32_16x16x32_bf16 v[120:123], v[20:23], v[204:207], v[120:123]\n\tbuffer_load_dwordx4 a[60:63], v8, s[8:11], s68 offen offset:1024 sc1\n\tv_mfma_f32_16x16x32_bf16 v[128:131], v[20:23], v[208:211], v[128:131]\n\t" \
    "v_mfma_f32_16x16x32_bf16 v[128:131], v[16:19], v[184:187], v[128:131]\n\tds_read_b64_tr_b16 v[44:45], v3 offset:20480\n\tds_read_b64_tr_b16 v[46:47], v3 offset:20992\n\tv_mfma_f32_16x16x32_bf16 v[136:139], v[16:19], v[188:191], v[136:139]\n\tbuffer_load_dwordx4 a[64:67], v8, s[8:11], s68 offen offset:2048 sc1\n\ts_cmp_ge_u32 s43, 11\n\ts_cselect_b32 s68, s70, s43\n\ts_mul_i32 s68, s68, 36864\n\t" \
    "v_mfma_f32_16x16x32_bf16 v[136:139], v[20:23], v[212:215], v[136:139]\n\ts_add_u32 s68, s68, s44\n\ts_add_u32 s68, s68, 368640\n\ts_waitcnt lgkmcnt(8)\n\tv_mfma_f32_16x16x32_bf16 v[124:127], v[24:27], v[180:183], v[124:127]\n\tds_read_b64_tr_b16 v[16:17], v0 offset:24576\n\tds_read_b64_tr_b16 v[18:19], v0 offset:25088\n\tv_mfma_f32_16x16x32_bf16 v[124:127], v[28:31], v[204:207], v[124:127]\n\t" \
    "buffer_load_dwordx4 v[192:195], v8, s[8:11], s68 offen offset:0 sc1\n\tv_mfma_f32_16x16x32_bf16 v[132:135], v[28:31], v[208:211], v[132:135]\n\tv_mfma_f32_16x16x32_bf16 v[132:135], v[24:27], v[184:187], v[132:135]\n\tds_read_b64_tr_b16 v[20:21], v0 offset:28672\n\tds_read_b64_tr_b16 v[22:23], v0 offset:29184\n\tv_mfma_f32_16x16x32_bf16 v[140:143], v[24:27], v[188:191], v[140:143]\n\tbuffer_load_dwordx4 v[196:199], v8, s[8:11], s68 offen offset:1024 sc1\n\tv_mfma_f32_16x16x32_bf16 v[140:143], v[28:31], v[212:215], v[140:143]\n\t" \
    "s_waitcnt lgkmcnt(8)\n\tv_mfma_f32_16x16x32_bf16 v[144:147], v[32:35], v[180:183], v[144:147]\n\tds_read_b64_tr_b16 v[24:25], v1 offset:24576\n\tds_read_b64_tr_b16 v[26:27], v1 offset:25088\n\tv_mfma_f32_16x16x32_bf16 v[144:147], v[36:39], v[204:207], v[144:147]\n\tbuffer_load_dwordx4 v[200:203], v8, s[8:11], s68 offen offset:2048 sc1\n\ts_cmp_ge_u32 s43, 10\n\ts_cselect_b32 s68, s70, s43\n\t" \
    "s_mul_i32 s68, s68, 36864\n\tv_mfma_f32_16x16x32_bf16 v[152:155], v[36:39], v[208:211], v[152:155]\n\ts_add_u32 s68, s68, s44\n\ts_add_u32 s68, s68, 405504\n\tv_mfma_f32_16x16x32_bf16 v[152:155], v[32:35], v[184:187], v[152:155]\n\tds_read_b64_tr_b16 v[28:29], v1 offset:28672\n\tds_read_b64_tr_b16 v[30:31], v1 offset:29184\n\tv_mfma_f32_16x16x32_bf16 v[160:163], v[32:35], v[188:191], v[160:163]\n\t" \
    "buffer_load_dwordx4 v[168:171], v8, s[8:11], s68 offen offset:0 sc1\n\tv_mfma_f32_16x16x32_bf16 v[160:163], v[36:39], v[212:215], v[160:163]\n\ts_waitcnt lgkmcnt(8)\n\tv_mfma_f32_16x16x32_bf16 v[148:151], v[40:43], v[180:183], v[148:151]\n\tds_read_b64_tr_b16 v[32:33], v2 offset:24576\n\tds_read_b64_tr_b16 v[34:35], v2 offset:25088\n\tv_mfma_f32_16x16x32_bf16 v[148:151], v[44:47], v[204:207], v[148:151]\n\tv_mfma_f32_16x16x32_bf16 v[156:159], v[44:47], v[208:211], v[156:159]\n\t" \
    "v_mfma_f32_16x16x32_bf16 v[156:159], v[40:43], v[184:187], v[156:159]\n\tds_read_b64_tr_b16 v[36:37], v2 offset:28672\n\tds_read_b64_tr_b16 v[38:39], v2 offset:29184\n\tv_mfma_f32_16x16x32_bf16 v[164:167], v[40:43], v[188:191], v[164:167]\n\tbuffer_load_dwordx4 v[172:175], v8, s[8:11], s68 offen offset:1024 sc1\n\tv_mfma_f32_16x16x32_bf16 v[164:167], v[44:47], v[212:215], v[164:167]\n\ts_waitcnt lgkmcnt(8)\n\tv_mfma_f32_16x16x32_bf16 v[48:51], v[16:19], v[180:183], v[48:51]\n\t" \
    "ds_read_b64_tr_b16 v[40:41], v3 offset:24576\n\tds_read_b64_tr_b16 v[42:43], v3 offset:25088\n\tv_mfma_f32_16x16x32_bf16 v[48:51], v[20:23], v[204:207], v[48:51]\n\tbuffer_load_dwordx4 v[176:179], v8, s[8:11], s68 offen offset:2048 sc1\n\ts_cmp_ge_u32 s43, 9\n\ts_cselect_b32 s68, s70, s43\n\ts_mul_i32 s68, s68, 36864\n\tv_mfma_f32_16x16x32_bf16 v[56:59], v[20:23], v[208:211], v[56:59]\n\t" \
    "s_add_u32 s68, s68, s44\n\ts_add_u32 s68, s68, 442368\n\tv_mfma_f32_16x16x32_bf16 v[56:59], v[16:19], v[184:187], v[56:59]\n\tds_read_b64_tr_b16 v[44:45], v3 offset:28672\n\tds_read_b64_tr_b16 v[46:47], v3 offset:29184\n\tv_mfma_f32_16x16x32_bf16 v[64:67], v[16:19], v[188:191], v[64:67]\n\tv_mfma_f32_16x16x32_bf16 v[64:67], v[20:23], v[212:215], v[64:67]\n\ts_waitcnt lgkmcnt(8)\n\t" \
    "v_mfma_f32_16x16x32_bf16 v[52:55], v[24:27], v[180:183], v[52:55]\n\tds_read_b64_tr_b16 v[16:17], v0 offset:32768\n\tds_read_b64_tr_b16 v[18:19], v0 offset:33280\n\tv_mfma_f32_16x16x32_bf16 v[52:55], v[28:31], v[204:207], v[52:55]\n\tbuffer_load_dwordx4 a[68:71], v8, s[8:11], s68 offen offset:0 sc1\n\tv_mfma_f32_16x16x32_bf16 v[60:63], v[28:31], v[208:211], v[60:63]\n\tv_mfma_f32_16x16x32_bf16 v[60:63], v[24:27], v[184:187], v[60:63]\n\tds_read_b64_tr_b16 v[20:21], v0 offset:36864\n\t" \
    "ds_read_b64_tr_b16 v[22:23], v0 offset:37376\n\tv_mfma_f32_16x16x32_bf16 v[68:71], v[24:27], v[188:191], v[68:71]\n\tbuffer_load_dwordx4 a[72:75], v8, s[8:11], s68 offen offset:1024 sc1\n\tv_mfma_f32_16x16x32_bf16 v[68:71], v[28:31], v[212:215], v[68:71]\n\ts_waitcnt lgkmcnt(8)\n\tv_mfma_f32_16x16x32_bf16 v[72:75], v[32:35], v[180:183], v[72:75]\n\tds_read_b64_tr_b16 v[24:25], v1 offset:32768\n\tds_read_b64_tr_b16 v[26:27], v1 offset:33280\n\t" \
    "v_mfma_f32_16x16x32_bf16 v[72:75], v[36:39], v[204:207], v[72:75]\n\tbuffer_load_dwordx4 a[76:79], v8, s[8:11], s68 offen offset:2048 sc1\n\ts_cmp_ge_u32 s43, 8\n\ts_cselect_b32 s68, s70, s43\n\ts_mul_i32 s68, s68, 36864\n\tv_mfma_f32_16x16x32_bf16 v[80:83], v[36:39], v[208:211], v[80:83]\n\ts_add_u32 s68, s68, s44\n\ts_add_u32 s68, s68, 479232\n\t" \
    "v_mfma_f32_16x16x32_bf16 v[80:83], v[32:35], v[184:187], v[80:83]\n\tds_read_b64_tr_b16 v[28:29], v1 offset:36864\n\tds_read_b64_tr_b16 v[30:31], v1 offset:37376\n\tv_mfma_f32_16x16x32_bf16 v[88:91], v[32:35], v[188:191], v[88:91]\n\tv_mfma_f32_16x16x32_bf16 v[88:91], v[36:39], v[212:215], v[88:91]\n\ts_waitcnt lgkmcnt(8)\n\tv_mfma_f32_16x16x32_bf16 v[76:79], v[40:43], v[180:183], v[76:79]\n\tds_read_b64_tr_b16 v[32:33], v2 offset:32768\n\t" \
    "ds_read_b64_tr_b16 v[34:35], v2 offset:33280\n\tv_mfma_f32_16x16x32_bf16 v[76:79], v[44:47], v[204:207], v[76:79]\n\tbuffer_load_dwordx4 a[80:83], v8, s[8:11], s68 offen offset:0 sc1\n\tv_mfma_f32_16x16x32_bf16 v[84:87], v[44:47], v[208:211], v[84:87]\n\tv_mfma_f32_16x16x32_bf16 v[84:87], v[40:43], v[184:187], v[84:87]\n\tds_read_b64_tr_b16 v[36:37], v2 offset:36864\n\tds_read_b64_tr_b16 v[38:39], v2 offset:37376\n\tv_mfma_f32_16x16x32_bf16 v[92:95], v[40:43], v[188:191], v[92:95]\n\t" \
    "buffer_load_dwordx4 a[84:87], v8, s[8:11], s68 offen offset:1024 sc1\n\tv_mfma_f32_16x16x32_bf16 v[92:95], v[44:47], v[212:215], v[92:95]\n\ts_barrier\n\ts_waitcnt vmcnt(23) lgkmcnt(8)\n\tv_mfma_f32_16x16x32_bf16 v[120:123], v[16:19], v[216:219], v[120:123]\n\tds_read_b64_tr_b16 v[40:41], v3 offset:32768\n\tds_read_b64_tr_b16 v[42:43], v3 offset:33280\n\tv_mfma_f32_16x16x32_bf16 v[120:123], v[20:23], a[8:11], v[120:123]\n\t" \
    "buffer_load_dwordx4 a[88:91], v8, s[8:11], s68 offen offset:2048 sc1\n\ts_cmp_ge_u32 s43, 7\n\ts_cselect_b32 s68, s70, s43\n\ts_mul_i32 s68, s68, 36864\n\tv_mfma_f32_16x16x32_bf16 v[128:131], v[20:23], a[12:15], v[128:131]\n\ts_add_u32 s68, s68, s44\n\ts_add_u32 s68, s68, 516096\n\tv_mfma_f32_16x16x32_bf16 v[128:131], v[16:19], a[0:3], v[128:131]\n\t" \
    "ds_read_b64_tr_b16 v[44:45], v3 offset:36864\n\tds_read_b64_tr_b16 v[46:47], v3 offset:37376\n\tv_mfma_f32_16x16x32_bf16 v[136:139], v[16:19], a[4:7], v[136:139]\n\tbuffer_load_dwordx4 v[180:183], v8, s[8:11], s68 offen offset:0 sc1\n\tv_mfma_f32_16x16x32_bf16 v[136:139], v[20:23], a[16:19], v[136:139]\n\ts_waitcnt lgkmcnt(8)\n\tv_mfma_f32_16x16x32_bf16 v[124:127], v[24:27], v[216:219], v[124:127]\n\tds_read_b64_tr_b16 v[16:17], v0 offset:40960\n\t" \
    "ds_read_b64_tr_b16 v[18:19], v0 offset:41472\n\tv_mfma_f32_16x16x32_bf16 v[124:127], v[28:31], a[8:11], v[124:127]\n\tbuffer_load_dwordx4 v[184:187], v8, s[8:11], s68 offen offset:1024 sc1\n\tv_mfma_f32_16x16x32_bf16 v[132:135], v[28:31], a[12:15], v[132:135]\n\tv_mfma_f32_16x16x32_bf16 v[132:135], v[24:27], a[0:3], v[132:135]\n\tds_read_b64_tr_b16 v[20:21], v0 offset:45056\n\tds_read_b64_tr_b16 v[22:23], v0 offset:45568\n\tv_mfma_f32_16x16x32_bf16 v[140:143], v[24:27], a[4:7], v[140:143]\n\t" \
    "v_mfma_f32_16x16x32_bf16 v[140:143], v[28:31], a[16:19], v[140:143]\n\ts_waitcnt lgkmcnt(8)\n\tv_mfma_f32_16x16x32_bf16 v[144:147], v[32:35], v[216:219], v[144:147]\n\tds_read_b64_tr_b16 v[24:25], v1 offset:40960\n\tds_read_b64_tr_b16 v[26:27], v1 offset:41472\n\tv_mfma_f32_16x16x32_bf16 v[144:147], v[36:39], a[8:11], v[144:147]\n\tbuffer_load_dwordx4 v[188:191], v8, s[8:11], s68 offen offset:2048 sc1\n\ts_cmp_ge_u32 s43, 6\n\t" \
    "s_cselect_b32 s68, s70, s43\n\ts_mul_i32 s68, s68, 36864\n\tv_mfma_f32_16x16x32_bf16 v[152:155], v[36:39], a[12:15], v[152:155]\n\ts_add_u32 s68, s68, s44\n\ts_add_u32 s68, s68, 552960\n\tv_mfma_f32_16x16x32_bf16 v[152:155], v[32:35], a[0:3], v[152:155]\n\tds_read_b64_tr_b16 v[28:29], v1 offset:45056\n\tds_read_b64_tr_b16 v[30:31], v1 offset:45568\n\t" \
    "v_mfma_f32_16x16x32_bf16 v[160:163], v[32:35], a[4:7], v[160:163]\n\tbuffer_load_dwordx4 v[204:207], v8, s[8:11], s68 offen offset:0 sc1\n\tv_mfma_f32_16x16x32_bf16 v[160:163], v[36:39], a[16:19], v[160:163]\n\ts_waitcnt lgkmcnt(8)\n\tv_mfma_f32_16x16x32_bf16 v[148:151], v[40:43], v[216:219], v[148:151]\n\tds_read_b64_tr_b16 v[32:33], v2 offset:40960\n\tds_read_b64_tr_b16 v[34:35], v2 offset:41472\n\tv_mfma_f32_16x16x32_bf16 v[148:151], v[44:47], a[8:11], v[148:151]\n\t" \
    "buffer_load_dwordx4 v[208:211], v8, s[8:11], s68 offen offset:1024 sc1\n\tv_mfma_f32_16x16x32_bf16 v[156:159], v[44:47], a[12:15], v[156:159]\n\tv_mfma_f32_16x16x32_bf16 v[156:159], v[40:43], a[0:3], v[156:159]\n\tds_read_b64_tr_b16 v[36:37], v2 offset:45056\n\tds_read_b64_tr_b16 v[38:39], v2 offset:45568\n\tv_mfma_f32_16x16x32_bf16 v[164:167], v[40:43], a[4:7], v[164:167]\n\tv_mfma_f32_16x16x32_bf16 v[164:167], v[44:47], a[16:19], v[164:167]\n\ts_waitcnt lgkmcnt(8)\n\t" \
    "v_mfma_f32_16x16x32_bf16 v[48:51], v[16:19], v[216:219], v[48:51]\n\tds_read_b64_tr_b16 v[40:41], v3 offset:40960\n\tds_read_b64_tr_b16 v[42:43], v3 offset:41472\n\tv_mfma_f32_16x16x32_bf16 v[48:51], v[20:23], a[8:11], v[48:51]\n\tbuffer_load_dwordx4 v[212:215], v8, s[8:11], s68 offen offset:2048 sc1\n\ts_cmp_ge_u32 s43, 1\n\ts_cselect_b32 s69, s70, s43\n\ts_mul_i32 s69, s69, 294912\n\t" \
    "v_mfma_f32_16x16x32_bf16 v[56:59], v[20:23], a[12:15], v[56:59]\n\ts_add_u32 s69, s69, 5898240\n\ts_add_u32 m0, s54, 0\n\tv_mfma_f32_16x16x32_bf16 v[56:59], v[16:19], a[0:3], v[56:59]\n\tds_read_b64_tr_b16 v[44:45], v3 offset:45056\n\tds_read_b64_tr_b16 v[46:47], v3 offset:45568\n\tv_mfma_f32_16x16x32_bf16 v[64:67], v[16:19], a[4:7], v[64:67]\n\tbuffer_load_dwordx4 v10, s[12:15], s69 offen lds\n\t" \
    "s_mov_b32 s69, 6193152\n\ts_add_u32 m0, s54, 4096\n\tv_mfma_f32_16x16x32_bf16 v[64:67], v[20:23], a[16:19], v[64:67]\n\ts_waitcnt lgkmcnt(8)\n\tv_mfma_f32_16x16x32_bf16 v[52:55], v[24:27], v[216:219], v[52:55]\n\tds_read_b64_tr_b16 v[16:17], v0 offset:49152\n\tds_read_b64_tr_b16 v[18:19], v0 offset:49664\n\tv_mfma_f32_16x16x32_bf16 v[52:55], v[28:31], a[8:11], v[52:55]\n\t" \
    "buffer_load_dwordx4 v10, s[12:15], s69 offen lds\n\ts_cmp_ge_u32 s43, 5\n\ts_cselect_b32 s68, s70, s43\n\ts_mul_i32 s68, s68, 36864\n\tv_mfma_f32_16x16x32_bf16 v[60:63], v[28:31], a[12:15], v[60:63]\n\ts_add_u32 s68, s68, s44\n\ts_add_u32 s68, s68, 589824\n\tv_mfma_f32_16x16x32_bf16 v[60:63], v[24:27], a[0:3], v[60:63]\n\t" \
    "ds_read_b64_tr_b16 v[20:21], v0 offset:53248\n\tds_read_b64_tr_b16 v[22:23], v0 offset:53760\n\tv_mfma_f32_16x16x32_bf16 v[68:71], v[24:27], a[4:7], v[68:71]\n\tv_mfma_f32_16x16x32_bf16 v[68:71], v[28:31], a[16:19], v[68:71]\n\ts_waitcnt lgkmcnt(8)\n\tv_mfma_f32_16x16x32_bf16 v[72:75], v[32:35], v[216:219], v[72:75]\n\tds_read_b64_tr_b16 v[24:25], v1 offset:49152\n\tds_read_b64_tr_b16 v[26:27], v1 offset:49664\n\t" \
    "v_mfma_f32_16x16x32_bf16 v[72:75], v[36:39], a[8:11], v[72:75]\n\tbuffer_load_dwordx4 a[92:95], v8, s[8:11], s68 offen offset:0 sc1\n\tv_mfma_f32_16x16x32_bf16 v[80:83], v[36:39], a[12:15], v[80:83]\n\tv_mfma_f32_16x16x32_bf16 v[80:83], v[32:35], a[0:3], v[80:83]\n\tds_read_b64_tr_b16 v[28:29], v1 offset:53248\n\tds_read_b64_tr_b16 v[30:31], v1 offset:53760\n\tv_mfma_f32_16x16x32_bf16 v[88:91], v[32:35], a[4:7], v[88:91]\n\tbuffer_load_dwordx4 a[96:99], v8, s[8:11], s68 offen offset:1024 sc1\n\t" \
    "v_mfma_f32_16x16x32_bf16 v[88:91], v[36:39], a[16:19], v[88:91]\n\ts_waitcnt lgkmcnt(8)\n\tv_mfma_f32_16x16x32_bf16 v[76:79], v[40:43], v[216:219], v[76:79]\n\tds_read_b64_tr_b16 v[32:33], v2 offset:49152\n\tds_read_b64_tr_b16 v[34:35], v2 offset:49664\n\tv_mfma_f32_16x16x32_bf16 v[76:79], v[44:47], a[8:11], v[76:79]\n\tbuffer_load_dwordx4 a[100:103], v8, s[8:11], s68 offen offset:2048 sc1\n\ts_cmp_ge_u32 s43, 4\n\t" \
    "s_cselect_b32 s68, s70, s43\n\ts_mul_i32 s68, s68, 36864\n\tv_mfma_f32_16x16x32_bf16 v[84:87], v[44:47], a[12:15], v[84:87]\n\ts_add_u32 s68, s68, s44\n\ts_add_u32 s68, s68, 626688\n\tv_mfma_f32_16x16x32_bf16 v[84:87], v[40:43], a[0:3], v[84:87]\n\tds_read_b64_tr_b16 v[36:37], v2 offset:53248\n\tds_read_b64_tr_b16 v[38:39], v2 offset:53760\n\t" \
    "v_mfma_f32_16x16x32_bf16 v[92:95], v[40:43], a[4:7], v[92:95]\n\tv_mfma_f32_16x16x32_bf16 v[92:95], v[44:47], a[16:19], v[92:95]\n\ts_barrier\n\ts_waitcnt vmcnt(29) lgkmcnt(8)\n\tv_mfma_f32_16x16x32_bf16 v[120:123], v[16:19], a[20:23], v[120:123]\n\tds_read_b64_tr_b16 v[40:41], v3 offset:49152\n\tds_read_b64_tr_b16 v[42:43], v3 offset:49664\n\tv_mfma_f32_16x16x32_bf16 v[120:123], v[20:23], a[32:35], v[120:123]\n\t" \
    "buffer_load_dwordx4 a[104:107], v8, s[8:11], s68 offen offset:0 sc1\n\tv_mfma_f32_16x16x32_bf16 v[128:131], v[20:23], a[36:39], v[128:131]\n\tv_mfma_f32_16x16x32_bf16 v[128:131], v[16:19], a[24:27], v[128:131]\n\tds_read_b64_tr_b16 v[44:45], v3 offset:53248\n\tds_read_b64_tr_b16 v[46:47], v3 offset:53760\n\tv_mfma_f32_16x16x32_bf16 v[136:139], v[16:19], a[28:31], v[136:139]\n\tbuffer_load_dwordx4 a[108:111], v8, s[8:11], s68 offen offset:1024 sc1\n\tv_mfma_f32_16x16x32_bf16 v[136:139], v[20:23], a[40:43], v[136:139]\n\t" \
    "s_waitcnt lgkmcnt(8)\n\tv_mfma_f32_16x16x32_bf16 v[124:127], v[24:27], a[20:23], v[124:127]\n\tds_read_b64_tr_b16 v[16:17], v0 offset:57344\n\tds_read_b64_tr_b16 v[18:19], v0 offset:57856\n\tv_mfma_f32_16x16x32_bf16 v[124:127], v[28:31], a[32:35], v[124:127]\n\tbuffer_load_dwordx4 a[112:115], v8, s[8:11], s68 offen offset:2048 sc1\n\ts_cmp_ge_u32 s43, 3\n\ts_cselect_b32 s68, s70, s43\n\t" \
    "s_mul_i32 s68, s68, 36864\n\tv_mfma_f32_16x16x32_bf16 v[132:135], v[28:31], a[36:39], v[132:135]\n\ts_add_u32 s68, s68, s44\n\ts_add_u32 s68, s68, 663552\n\tv_mfma_f32_16x16x32_bf16 v[132:135], v[24:27], a[24:27], v[132:135]\n\tds_read_b64_tr_b16 v[20:21], v0 offset:61440\n\tds_read_b64_tr_b16 v[22:23], v0 offset:61952\n\tv_mfma_f32_16x16x32_bf16 v[140:143], v[24:27], a[28:31], v[140:143]\n\t" \
    "buffer_load_dwordx4 v[216:219], v8, s[8:11], s68 offen offset:0 sc1\n\tv_mfma_f32_16x16x32_bf16 v[140:143], v[28:31], a[40:43], v[140:143]\n\ts_waitcnt lgkmcnt(8)\n\tv_mfma_f32_16x16x32_bf16 v[144:147], v[32:35], a[20:23], v[144:147]\n\tds_read_b64_tr_b16 v[24:25], v1 offset:57344\n\tds_read_b64_tr_b16 v[26:27], v1 offset:57856\n\tv_mfma_f32_16x16x32_bf16 v[144:147], v[36:39], a[32:35], v[144:147]\n\tbuffer_load_dwordx4 a[0:3], v8, s[8:11], s68 offen offset:1024 sc1\n\t" \
    "v_mfma_f32_16x16x32_bf16 v[152:155], v[36:39], a[36:39], v[152:155]\n\tv_mfma_f32_16x16x32_bf16 v[152:155], v[32:35], a[24:27], v[152:155]\n\tds_read_b64_tr_b16 v[28:29], v1 offset:61440\n\tds_read_b64_tr_b16 v[30:31], v1 offset:61952\n\tv_mfma_f32_16x16x32_bf16 v[160:163], v[32:35], a[28:31], v[160:163]\n\tv_mfma_f32_16x16x32_bf16 v[160:163], v[36:39], a[40:43], v[160:163]\n\ts_waitcnt lgkmcnt(8)\n\tv_mfma_f32_16x16x32_bf16 v[148:151], v[40:43], a[20:23], v[148:151]\n\t" \
    "ds_read_b64_tr_b16 v[32:33], v2 offset:57344\n\tds_read_b64_tr_b16 v[34:35], v2 offset:57856\n\tv_mfma_f32_16x16x32_bf16 v[148:151], v[44:47], a[32:35], v[148:151]\n\tbuffer_load_dwordx4 a[4:7], v8, s[8:11], s68 offen offset:2048 sc1\n\ts_cmp_ge_u32 s43, 2\n\ts_cselect_b32 s68, s70, s43\n\ts_mul_i32 s68, s68, 36864\n\tv_mfma_f32_16x16x32_bf16 v[156:159], v[44:47], a[36:39], v[156:159]\n\t" \
    "s_add_u32 s68, s68, s44\n\ts_add_u32 s68, s68, 700416\n\tv_mfma_f32_16x16x32_bf16 v[156:159], v[40:43], a[24:27], v[156:159]\n\tds_read_b64_tr_b16 v[36:37], v2 offset:61440\n\tds_read_b64_tr_b16 v[38:39], v2 offset:61952\n\tv_mfma_f32_16x16x32_bf16 v[164:167], v[40:43], a[28:31], v[164:167]\n\tbuffer_load_dwordx4 a[8:11], v8, s[8:11], s68 offen offset:0 sc1\n\tv_mfma_f32_16x16x32_bf16 v[164:167], v[44:47], a[40:43], v[164:167]\n\t" \
    "s_waitcnt lgkmcnt(8)\n\tv_mfma_f32_16x16x32_bf16 v[48:51], v[16:19], a[20:23], v[48:51]\n\tds_read_b64_tr_b16 v[40:41], v3 offset:57344\n\tds_read_b64_tr_b16 v[42:43], v3 offset:57856\n\tv_mfma_f32_16x16x32_bf16 v[48:51], v[20:23], a[32:35], v[48:51]\n\tbuffer_load_dwordx4 a[12:15], v8, s[8:11], s68 offen offset:1024 sc1\n\tv_mfma_f32_16x16x32_bf16 v[56:59], v[20:23], a[36:39], v[56:59]\n\tv_mfma_f32_16x16x32_bf16 v[56:59], v[16:19], a[24:27], v[56:59]\n\t" \
    "ds_read_b64_tr_b16 v[44:45], v3 offset:61440\n\tds_read_b64_tr_b16 v[46:47], v3 offset:61952\n\tv_mfma_f32_16x16x32_bf16 v[64:67], v[16:19], a[28:31], v[64:67]\n\tv_mfma_f32_16x16x32_bf16 v[64:67], v[20:23], a[40:43], v[64:67]\n\ts_waitcnt lgkmcnt(8)\n\tv_mfma_f32_16x16x32_bf16 v[52:55], v[24:27], a[20:23], v[52:55]\n\tds_read_b64_tr_b16 v[16:17], v14 offset:0\n\tds_read_b64_tr_b16 v[18:19], v14 offset:512\n\t" \
    "v_mfma_f32_16x16x32_bf16 v[52:55], v[28:31], a[32:35], v[52:55]\n\tbuffer_load_dwordx4 a[16:19], v8, s[8:11], s68 offen offset:2048 sc1\n\ts_mov_b32 s69, 6488064\n\ts_add_u32 m0, s54, 8192\n\tv_mfma_f32_16x16x32_bf16 v[60:63], v[28:31], a[36:39], v[60:63]\n\tv_mfma_f32_16x16x32_bf16 v[60:63], v[24:27], a[24:27], v[60:63]\n\tds_read_b64_tr_b16 v[20:21], v14 offset:4096\n\tds_read_b64_tr_b16 v[22:23], v14 offset:4608\n\t" \
    "v_mfma_f32_16x16x32_bf16 v[68:71], v[24:27], a[28:31], v[68:71]\n\tbuffer_load_dwordx4 v10, s[12:15], s69 offen lds\n\ts_mov_b32 s69, 6782976\n\ts_add_u32 m0, s54, 12288\n\tv_mfma_f32_16x16x32_bf16 v[68:71], v[28:31], a[40:43], v[68:71]\n\ts_waitcnt lgkmcnt(8)\n\tv_mfma_f32_16x16x32_bf16 v[72:75], v[32:35], a[20:23], v[72:75]\n\tds_read_b64_tr_b16 v[24:25], v15 offset:0\n\t" \
    "ds_read_b64_tr_b16 v[26:27], v15 offset:512\n\tv_mfma_f32_16x16x32_bf16 v[72:75], v[36:39], a[32:35], v[72:75]\n\tbuffer_load_dwordx4 v10, s[12:15], s69 offen lds\n\ts_mov_b32 s69, 7077888\n\ts_add_u32 m0, s54, 16384\n\tv_mfma_f32_16x16x32_bf16 v[80:83], v[36:39], a[36:39], v[80:83]\n\tv_mfma_f32_16x16x32_bf16 v[80:83], v[32:35], a[24:27], v[80:83]\n\tds_read_b64_tr_b16 v[28:29], v15 offset:4096\n\t" \
    "ds_read_b64_tr_b16 v[30:31], v15 offset:4608\n\tv_mfma_f32_16x16x32_bf16 v[88:91], v[32:35], a[28:31], v[88:91]\n\tv_mfma_f32_16x16x32_bf16 v[88:91], v[36:39], a[40:43], v[88:91]\n\ts_waitcnt lgkmcnt(8)\n\tv_mfma_f32_16x16x32_bf16 v[76:79], v[40:43], a[20:23], v[76:79]\n\tds_read_b64_tr_b16 v[32:33], v237 offset:0\n\tds_read_b64_tr_b16 v[34:35], v237 offset:512\n\tv_mfma_f32_16x16x32_bf16 v[76:79], v[44:47], a[32:35], v[76:79]\n\t" \
    "buffer_load_dwordx4 v10, s[12:15], s69 offen lds\n\ts_mov_b32 s69, 7372800\n\ts_add_u32 m0, s54, 20480\n\tv_mfma_f32_16x16x32_bf16 v[84:87], v[44:47], a[36:39], v[84:87]\n\tv_mfma_f32_16x16x32_bf16 v[84:87], v[40:43], a[24:27], v[84:87]\n\tds_read_b64_tr_b16 v[36:37], v237 offset:4096\n\tds_read_b64_tr_b16 v[38:39], v237 offset:4608\n\tv_mfma_f32_16x16x32_bf16 v[92:95], v[40:43], a[28:31], v[92:95]\n\t" \
    "buffer_load_dwordx4 v10, s[12:15], s69 offen lds\n\ts_mov_b32 s69, 7667712\n\ts_add_u32 m0, s54, 24576\n\tv_mfma_f32_16x16x32_bf16 v[92:95], v[44:47], a[40:43], v[92:95]\n\ts_barrier\n\ts_waitcnt vmcnt(36) lgkmcnt(8)\n\tv_mfma_f32_16x16x32_bf16 v[120:123], v[16:19], a[44:47], v[120:123]\n\tds_read_b64_tr_b16 v[40:41], v238 offset:0\n\t" \
    "ds_read_b64_tr_b16 v[42:43], v238 offset:512\n\tv_mfma_f32_16x16x32_bf16 v[120:123], v[20:23], a[56:59], v[120:123]\n\tbuffer_load_dwordx4 v10, s[12:15], s69 offen lds\n\ts_mov_b32 s69, 7962624\n\ts_add_u32 m0, s54, 28672\n\tv_mfma_f32_16x16x32_bf16 v[128:131], v[20:23], a[60:63], v[128:131]\n\tv_mfma_f32_16x16x32_bf16 v[128:131], v[16:19], a[48:51], v[128:131]\n\tds_read_b64_tr_b16 v[44:45], v238 offset:4096\n\t" \
    "ds_read_b64_tr_b16 v[46:47], v238 offset:4608\n\tv_mfma_f32_16x16x32_bf16 v[136:139], v[16:19], a[52:55], v[136:139]\n\tbuffer_load_dwordx4 v10, s[12:15], s69 offen lds\n\ts_mov_b32 s69, 8257536\n\ts_add_u32 m0, s54, 32768\n\tv_mfma_f32_16x16x32_bf16 v[136:139], v[20:23], a[64:67], v[136:139]\n\ts_waitcnt lgkmcnt(8)\n\tv_mfma_f32_16x16x32_bf16 v[124:127], v[24:27], a[44:47], v[124:127]\n\t" \
    "ds_read_b64_tr_b16 v[16:17], v14 offset:8192\n\tds_read_b64_tr_b16 v[18:19], v14 offset:8704\n\tv_mfma_f32_16x16x32_bf16 v[124:127], v[28:31], a[56:59], v[124:127]\n\tbuffer_load_dwordx4 v10, s[12:15], s69 offen lds\n\ts_mov_b32 s69, 8552448\n\ts_add_u32 m0, s54, 36864\n\tv_mfma_f32_16x16x32_bf16 v[132:135], v[28:31], a[60:63], v[132:135]\n\tv_mfma_f32_16x16x32_bf16 v[132:135], v[24:27], a[48:51], v[132:135]\n\t" \
    "ds_read_b64_tr_b16 v[20:21], v14 offset:12288\n\tds_read_b64_tr_b16 v[22:23], v14 offset:12800\n\tv_mfma_f32_16x16x32_bf16 v[140:143], v[24:27], a[52:55], v[140:143]\n\tv_mfma_f32_16x16x32_bf16 v[140:143], v[28:31], a[64:67], v[140:143]\n\ts_waitcnt lgkmcnt(8)\n\tv_mfma_f32_16x16x32_bf16 v[144:147], v[32:35], a[44:47], v[144:147]\n\tds_read_b64_tr_b16 v[24:25], v15 offset:8192\n\tds_read_b64_tr_b16 v[26:27], v15 offset:8704\n\t" \
    "v_mfma_f32_16x16x32_bf16 v[144:147], v[36:39], a[56:59], v[144:147]\n\tbuffer_load_dwordx4 v10, s[12:15], s69 offen lds\n\ts_cmp_ge_u32 s43, 1\n\ts_cselect_b32 s68, s70, s43\n\ts_mul_i32 s68, s68, 36864\n\tv_mfma_f32_16x16x32_bf16 v[152:155], v[36:39], a[60:63], v[152:155]\n\ts_add_u32 s68, s68, s44\n\ts_add_u32 s68, s68, 737280\n\t" \
    "v_mfma_f32_16x16x32_bf16 v[152:155], v[32:35], a[48:51], v[152:155]\n\tds_read_b64_tr_b16 v[28:29], v15 offset:12288\n\tds_read_b64_tr_b16 v[30:31], v15 offset:12800\n\tv_mfma_f32_16x16x32_bf16 v[160:163], v[32:35], a[52:55], v[160:163]\n\tbuffer_load_dwordx4 a[20:23], v8, s[8:11], s68 offen offset:0 sc1\n\tv_mfma_f32_16x16x32_bf16 v[160:163], v[36:39], a[64:67], v[160:163]\n\ts_waitcnt lgkmcnt(8)\n\tv_mfma_f32_16x16x32_bf16 v[148:151], v[40:43], a[44:47], v[148:151]\n\t" \
    "ds_read_b64_tr_b16 v[32:33], v237 offset:8192\n\tds_read_b64_tr_b16 v[34:35], v237 offset:8704\n\tv_mfma_f32_16x16x32_bf16 v[148:151], v[44:47], a[56:59], v[148:151]\n\tbuffer_load_dwordx4 a[24:27], v8, s[8:11], s68 offen offset:1024 sc1\n\tv_mfma_f32_16x16x32_bf16 v[156:159], v[44:47], a[60:63], v[156:159]\n\tv_mfma_f32_16x16x32_bf16 v[156:159], v[40:43], a[48:51], v[156:159]\n\tds_read_b64_tr_b16 v[36:37], v237 offset:12288\n\tds_read_b64_tr_b16 v[38:39], v237 offset:12800\n\t" \
    "v_mfma_f32_16x16x32_bf16 v[164:167], v[40:43], a[52:55], v[164:167]\n\tv_mfma_f32_16x16x32_bf16 v[164:167], v[44:47], a[64:67], v[164:167]\n\ts_waitcnt lgkmcnt(8)\n\tv_mfma_f32_16x16x32_bf16 v[48:51], v[16:19], a[44:47], v[48:51]\n\tds_read_b64_tr_b16 v[40:41], v238 offset:8192\n\tds_read_b64_tr_b16 v[42:43], v238 offset:8704\n\tv_mfma_f32_16x16x32_bf16 v[48:51], v[20:23], a[56:59], v[48:51]\n\tbuffer_load_dwordx4 a[28:31], v8, s[8:11], s68 offen offset:2048 sc1\n\t" \
    "s_add_u32 s68, s44, 774144\n\tv_mfma_f32_16x16x32_bf16 v[56:59], v[20:23], a[60:63], v[56:59]\n\tv_mfma_f32_16x16x32_bf16 v[56:59], v[16:19], a[48:51], v[56:59]\n\tds_read_b64_tr_b16 v[44:45], v238 offset:12288\n\tds_read_b64_tr_b16 v[46:47], v238 offset:12800\n\tv_mfma_f32_16x16x32_bf16 v[64:67], v[16:19], a[52:55], v[64:67]\n\tbuffer_load_dwordx4 a[32:35], v8, s[8:11], s68 offen offset:0 sc1\n\tv_mfma_f32_16x16x32_bf16 v[64:67], v[20:23], a[64:67], v[64:67]\n\t" \
    "s_waitcnt lgkmcnt(8)\n\tv_mfma_f32_16x16x32_bf16 v[52:55], v[24:27], a[44:47], v[52:55]\n\tds_read_b64_tr_b16 v[16:17], v14 offset:16384\n\tds_read_b64_tr_b16 v[18:19], v14 offset:16896\n\tv_mfma_f32_16x16x32_bf16 v[52:55], v[28:31], a[56:59], v[52:55]\n\tv_mfma_f32_16x16x32_bf16 v[60:63], v[28:31], a[60:63], v[60:63]\n\tv_mfma_f32_16x16x32_bf16 v[60:63], v[24:27], a[48:51], v[60:63]\n\tds_read_b64_tr_b16 v[20:21], v14 offset:20480\n\t" \
    "ds_read_b64_tr_b16 v[22:23], v14 offset:20992\n\tv_mfma_f32_16x16x32_bf16 v[68:71], v[24:27], a[52:55], v[68:71]\n\tbuffer_load_dwordx4 a[36:39], v8, s[8:11], s68 offen offset:1024 sc1\n\tv_mfma_f32_16x16x32_bf16 v[68:71], v[28:31], a[64:67], v[68:71]\n\ts_waitcnt lgkmcnt(8)\n\tv_mfma_f32_16x16x32_bf16 v[72:75], v[32:35], a[44:47], v[72:75]\n\tds_read_b64_tr_b16 v[24:25], v15 offset:16384\n\tds_read_b64_tr_b16 v[26:27], v15 offset:16896\n\t" \
    "v_mfma_f32_16x16x32_bf16 v[72:75], v[36:39], a[56:59], v[72:75]\n\tbuffer_load_dwordx4 a[40:43], v8, s[8:11], s68 offen offset:2048 sc1\n\ts_add_u32 s68, s44, 811008\n\tv_mfma_f32_16x16x32_bf16 v[80:83], v[36:39], a[60:63], v[80:83]\n\tv_mfma_f32_16x16x32_bf16 v[80:83], v[32:35], a[48:51], v[80:83]\n\tds_read_b64_tr_b16 v[28:29], v15 offset:20480\n\tds_read_b64_tr_b16 v[30:31], v15 offset:20992\n\tv_mfma_f32_16x16x32_bf16 v[88:91], v[32:35], a[52:55], v[88:91]\n\t" \
    "buffer_load_dwordx4 a[116:119], v8, s[8:11], s68 offen offset:0 sc1\n\tv_mfma_f32_16x16x32_bf16 v[88:91], v[36:39], a[64:67], v[88:91]\n\ts_waitcnt lgkmcnt(8)\n\tv_mfma_f32_16x16x32_bf16 v[76:79], v[40:43], a[44:47], v[76:79]\n\tds_read_b64_tr_b16 v[32:33], v237 offset:16384\n\tds_read_b64_tr_b16 v[34:35], v237 offset:16896\n\tv_mfma_f32_16x16x32_bf16 v[76:79], v[44:47], a[56:59], v[76:79]\n\tv_mfma_f32_16x16x32_bf16 v[84:87], v[44:47], a[60:63], v[84:87]\n\t" \
    "v_mfma_f32_16x16x32_bf16 v[84:87], v[40:43], a[48:51], v[84:87]\n\tds_read_b64_tr_b16 v[36:37], v237 offset:20480\n\tds_read_b64_tr_b16 v[38:39], v237 offset:20992\n\tv_mfma_f32_16x16x32_bf16 v[92:95], v[40:43], a[52:55], v[92:95]\n\tbuffer_load_dwordx4 a[120:123], v8, s[8:11], s68 offen offset:1024 sc1\n\tv_mfma_f32_16x16x32_bf16 v[92:95], v[44:47], a[64:67], v[92:95]\n\ts_barrier\n\ts_waitcnt vmcnt(42) lgkmcnt(8)\n\t" \
    "v_mfma_f32_16x16x32_bf16 v[120:123], v[16:19], v[192:195], v[120:123]\n\tds_read_b64_tr_b16 v[40:41], v238 offset:16384\n\tds_read_b64_tr_b16 v[42:43], v238 offset:16896\n\tv_mfma_f32_16x16x32_bf16 v[120:123], v[20:23], v[168:171], v[120:123]\n\tbuffer_load_dwordx4 a[124:127], v8, s[8:11], s68 offen offset:2048 sc1\n\ts_add_u32 s68, s44, 847872\n\tv_mfma_f32_16x16x32_bf16 v[128:131], v[20:23], v[172:175], v[128:131]\n\tv_mfma_f32_16x16x32_bf16 v[128:131], v[16:19], v[196:199], v[128:131]\n\t" \
    "ds_read_b64_tr_b16 v[44:45], v238 offset:20480\n\tds_read_b64_tr_b16 v[46:47], v238 offset:20992\n\tv_mfma_f32_16x16x32_bf16 v[136:139], v[16:19], v[200:203], v[136:139]\n\tbuffer_load_dwordx4 a[44:47], v8, s[8:11], s68 offen offset:0 sc1\n\tv_mfma_f32_16x16x32_bf16 v[136:139], v[20:23], v[176:179], v[136:139]\n\ts_waitcnt lgkmcnt(8)\n\tv_mfma_f32_16x16x32_bf16 v[124:127], v[24:27], v[192:195], v[124:127]\n\tds_read_b64_tr_b16 v[16:17], v14 offset:24576\n\t" \
    "ds_read_b64_tr_b16 v[18:19], v14 offset:25088\n\tv_mfma_f32_16x16x32_bf16 v[124:127], v[28:31], v[168:171], v[124:127]\n\tbuffer_load_dwordx4 a[48:51], v8, s[8:11], s68 offen offset:1024 sc1\n\tv_mfma_f32_16x16x32_bf16 v[132:135], v[28:31], v[172:175], v[132:135]\n\tv_mfma_f32_16x16x32_bf16 v[132:135], v[24:27], v[196:199], v[132:135]\n\tds_read_b64_tr_b16 v[20:21], v14 offset:28672\n\tds_read_b64_tr_b16 v[22:23], v14 offset:29184\n\tv_mfma_f32_16x16x32_bf16 v[140:143], v[24:27], v[200:203], v[140:143]\n\t" \
    "buffer_load_dwordx4 a[52:55], v8, s[8:11], s68 offen offset:2048 sc1\n\ts_add_u32 s68, s44, 884736\n\tv_mfma_f32_16x16x32_bf16 v[140:143], v[28:31], v[176:179], v[140:143]\n\ts_waitcnt lgkmcnt(8)\n\tv_mfma_f32_16x16x32_bf16 v[144:147], v[32:35], v[192:195], v[144:147]\n\tds_read_b64_tr_b16 v[24:25], v15 offset:24576\n\tds_read_b64_tr_b16 v[26:27], v15 offset:25088\n\tv_mfma_f32_16x16x32_bf16 v[144:147], v[36:39], v[168:171], v[144:147]\n\t" \
    "v_mfma_f32_16x16x32_bf16 v[152:155], v[36:39], v[172:175], v[152:155]\n\tv_mfma_f32_16x16x32_bf16 v[152:155], v[32:35], v[196:199], v[152:155]\n\tds_read_b64_tr_b16 v[28:29], v15 offset:28672\n\tds_read_b64_tr_b16 v[30:31], v15 offset:29184\n\tv_mfma_f32_16x16x32_bf16 v[160:163], v[32:35], v[200:203], v[160:163]\n\tbuffer_load_dwordx4 a[56:59], v8, s[8:11], s68 offen offset:0 sc1\n\tv_mfma_f32_16x16x32_bf16 v[160:163], v[36:39], v[176:179], v[160:163]\n\ts_waitcnt lgkmcnt(8)\n\t" \
    "v_mfma_f32_16x16x32_bf16 v[148:151], v[40:43], v[192:195], v[148:151]\n\tds_read_b64_tr_b16 v[32:33], v237 offset:24576\n\tds_read_b64_tr_b16 v[34:35], v237 offset:25088\n\tv_mfma_f32_16x16x32_bf16 v[148:151], v[44:47], v[168:171], v[148:151]\n\tbuffer_load_dwordx4 a[60:63], v8, s[8:11], s68 offen offset:1024 sc1\n\tv_mfma_f32_16x16x32_bf16 v[156:159], v[44:47], v[172:175], v[156:159]\n\tv_mfma_f32_16x16x32_bf16 v[156:159], v[40:43], v[196:199], v[156:159]\n\tds_read_b64_tr_b16 v[36:37], v237 offset:28672\n\t" \
    "ds_read_b64_tr_b16 v[38:39], v237 offset:29184\n\tv_mfma_f32_16x16x32_bf16 v[164:167], v[40:43], v[200:203], v[164:167]\n\tbuffer_load_dwordx4 a[64:67], v8, s[8:11], s68 offen offset:2048 sc1\n\ts_add_u32 s68, s44, 921600\n\tv_mfma_f32_16x16x32_bf16 v[164:167], v[44:47], v[176:179], v[164:167]\n\ts_waitcnt lgkmcnt(8)\n\tv_mfma_f32_16x16x32_bf16 v[48:51], v[16:19], v[192:195], v[48:51]\n\tds_read_b64_tr_b16 v[40:41], v238 offset:24576\n\t" \
    "ds_read_b64_tr_b16 v[42:43], v238 offset:25088\n\tv_mfma_f32_16x16x32_bf16 v[48:51], v[20:23], v[168:171], v[48:51]\n\tv_mfma_f32_16x16x32_bf16 v[56:59], v[20:23], v[172:175], v[56:59]\n\tv_mfma_f32_16x16x32_bf16 v[56:59], v[16:19], v[196:199], v[56:59]\n\tds_read_b64_tr_b16 v[44:45], v238 offset:28672\n\tds_read_b64_tr_b16 v[46:47], v238 offset:29184\n\tv_mfma_f32_16x16x32_bf16 v[64:67], v[16:19], v[200:203], v[64:67]\n\tbuffer_load_dwordx4 a[128:131], v8, s[8:11], s68 offen offset:0 sc1\n\t" \
    "v_mfma_f32_16x16x32_bf16 v[64:67], v[20:23], v[176:179], v[64:67]\n\ts_waitcnt lgkmcnt(8)\n\tv_mfma_f32_16x16x32_bf16 v[52:55], v[24:27], v[192:195], v[52:55]\n\tds_read_b64_tr_b16 v[16:17], v14 offset:32768\n\tds_read_b64_tr_b16 v[18:19], v14 offset:33280\n\tv_mfma_f32_16x16x32_bf16 v[52:55], v[28:31], v[168:171], v[52:55]\n\tbuffer_load_dwordx4 a[132:135], v8, s[8:11], s68 offen offset:1024 sc1\n\tv_mfma_f32_16x16x32_bf16 v[60:63], v[28:31], v[172:175], v[60:63]\n\t" \
    "v_mfma_f32_16x16x32_bf16 v[60:63], v[24:27], v[196:199], v[60:63]\n\tds_read_b64_tr_b16 v[20:21], v14 offset:36864\n\tds_read_b64_tr_b16 v[22:23], v14 offset:37376\n\tv_mfma_f32_16x16x32_bf16 v[68:71], v[24:27], v[200:203], v[68:71]\n\tv_mfma_f32_16x16x32_bf16 v[68:71], v[28:31], v[176:179], v[68:71]\n\ts_waitcnt lgkmcnt(8)\n\tv_mfma_f32_16x16x32_bf16 v[72:75], v[32:35], v[192:195], v[72:75]\n\tds_read_b64_tr_b16 v[24:25], v15 offset:32768\n\t" \
    "ds_read_b64_tr_b16 v[26:27], v15 offset:33280\n\tv_mfma_f32_16x16x32_bf16 v[72:75], v[36:39], v[168:171], v[72:75]\n\tbuffer_load_dwordx4 a[136:139], v8, s[8:11], s68 offen offset:2048 sc1\n\ts_mov_b32 s69, 8847360\n\ts_add_u32 m0, s54, 40960\n\tv_mfma_f32_16x16x32_bf16 v[80:83], v[36:39], v[172:175], v[80:83]\n\tv_mfma_f32_16x16x32_bf16 v[80:83], v[32:35], v[196:199], v[80:83]\n\tds_read_b64_tr_b16 v[28:29], v15 offset:36864\n\t" \
    "ds_read_b64_tr_b16 v[30:31], v15 offset:37376\n\tv_mfma_f32_16x16x32_bf16 v[88:91], v[32:35], v[200:203], v[88:91]\n\tbuffer_load_dwordx4 v10, s[12:15], s69 offen lds\n\ts_mov_b32 s69, 9142272\n\ts_add_u32 m0, s54, 45056\n\tv_mfma_f32_16x16x32_bf16 v[88:91], v[36:39], v[176:179], v[88:91]\n\ts_waitcnt lgkmcnt(8)\n\tv_mfma_f32_16x16x32_bf16 v[76:79], v[40:43], v[192:195], v[76:79]\n\t" \
    "ds_read_b64_tr_b16 v[32:33], v237 offset:32768\n\tds_read_b64_tr_b16 v[34:35], v237 offset:33280\n\tv_mfma_f32_16x16x32_bf16 v[76:79], v[44:47], v[168:171], v[76:79]\n\tbuffer_load_dwordx4 v10, s[12:15], s69 offen lds\n\ts_cmp_ge_u32 s43, 9\n\ts_cselect_b32 s69, s70, s43\n\ts_mul_i32 s69, s69, 294912\n\tv_mfma_f32_16x16x32_bf16 v[84:87], v[44:47], v[172:175], v[84:87]\n\t" \
    "s_add_u32 s69, s69, 3538944\n\ts_add_u32 m0, s54, 49152\n\tv_mfma_f32_16x16x32_bf16 v[84:87], v[40:43], v[196:199], v[84:87]\n\tds_read_b64_tr_b16 v[36:37], v237 offset:36864\n\tds_read_b64_tr_b16 v[38:39], v237 offset:37376\n\tv_mfma_f32_16x16x32_bf16 v[92:95], v[40:43], v[200:203], v[92:95]\n\tv_mfma_f32_16x16x32_bf16 v[92:95], v[44:47], v[176:179], v[92:95]\n\ts_barrier\n\t" \
    "s_waitcnt vmcnt(48) lgkmcnt(8)\n\tv_mfma_f32_16x16x32_bf16 v[120:123], v[16:19], a[68:71], v[120:123]\n\tds_read_b64_tr_b16 v[40:41], v238 offset:32768\n\tds_read_b64_tr_b16 v[42:43], v238 offset:33280\n\tv_mfma_f32_16x16x32_bf16 v[120:123], v[20:23], a[80:83], v[120:123]\n\tbuffer_load_dwordx4 v11, s[12:15], s69 offen lds\n\ts_cmp_ge_u32 s43, 8\n\ts_cselect_b32 s69, s70, s43\n\t" \
    "s_mul_i32 s69, s69, 294912\n\tv_mfma_f32_16x16x32_bf16 v[128:131], v[20:23], a[84:87], v[128:131]\n\ts_add_u32 s69, s69, 3833856\n\ts_add_u32 m0, s54, 53248\n\tv_mfma_f32_16x16x32_bf16 v[128:131], v[16:19], a[72:75], v[128:131]\n\tds_read_b64_tr_b16 v[44:45], v238 offset:36864\n\tds_read_b64_tr_b16 v[46:47], v238 offset:37376\n\tv_mfma_f32_16x16x32_bf16 v[136:139], v[16:19], a[76:79], v[136:139]\n\t" \
    "buffer_load_dwordx4 v11, s[12:15], s69 offen lds\n\ts_cmp_ge_u32 s43, 7\n\ts_cselect_b32 s69, s70, s43\n\ts_mul_i32 s69, s69, 294912\n\tv_mfma_f32_16x16x32_bf16 v[136:139], v[20:23], a[88:91], v[136:139]\n\ts_add_u32 s69, s69, 4128768\n\ts_add_u32 m0, s54, 57344\n\ts_waitcnt lgkmcnt(8)\n\t" \
    "v_mfma_f32_16x16x32_bf16 v[124:127], v[24:27], a[68:71], v[124:127]\n\tds_read_b64_tr_b16 v[16:17], v14 offset:40960\n\tds_read_b64_tr_b16 v[18:19], v14 offset:41472\n\tv_mfma_f32_16x16x32_bf16 v[124:127], v[28:31], a[80:83], v[124:127]\n\tbuffer_load_dwordx4 v11, s[12:15], s69 offen lds\n\ts_cmp_ge_u32 s43, 6\n\ts_cselect_b32 s69, s70, s43\n\ts_mul_i32 s69, s69, 294912\n\t" \
    "v_mfma_f32_16x16x32_bf16 v[132:135], v[28:31], a[84:87], v[132:135]\n\ts_add_u32 s69, s69, 4423680\n\ts_add_u32 m0, s54, 61440\n\tv_mfma_f32_16x16x32_bf16 v[132:135], v[24:27], a[72:75], v[132:135]\n\tds_read_b64_tr_b16 v[20:21], v14 offset:45056\n\tds_read_b64_tr_b16 v[22:23], v14 offset:45568\n\tv_mfma_f32_16x16x32_bf16 v[140:143], v[24:27], a[76:79], v[140:143]\n\tbuffer_load_dwordx4 v11, s[12:15], s69 offen lds\n\t" \
    "s_cmp_ge_u32 s43, 5\n\ts_cselect_b32 s69, s70, s43\n\ts_mul_i32 s69, s69, 294912\n\tv_mfma_f32_16x16x32_bf16 v[140:143], v[28:31], a[88:91], v[140:143]\n\ts_add_u32 s69, s69, 4718592\n\ts_add_u32 m0, s54, 65536\n\ts_waitcnt lgkmcnt(8)\n\tv_mfma_f32_16x16x32_bf16 v[144:147], v[32:35], a[68:71], v[144:147]\n\t" \
    "ds_read_b64_tr_b16 v[24:25], v15 offset:40960\n\tds_read_b64_tr_b16 v[26:27], v15 offset:41472\n\tv_mfma_f32_16x16x32_bf16 v[144:147], v[36:39], a[80:83], v[144:147]\n\tbuffer_load_dwordx4 v11, s[12:15], s69 offen lds\n\ts_cmp_ge_u32 s43, 4\n\ts_cselect_b32 s69, s70, s43\n\ts_mul_i32 s69, s69, 294912\n\tv_mfma_f32_16x16x32_bf16 v[152:155], v[36:39], a[84:87], v[152:155]\n\t" \
    "s_add_u32 s69, s69, 5013504\n\ts_add_u32 m0, s54, 69632\n\tv_mfma_f32_16x16x32_bf16 v[152:155], v[32:35], a[72:75], v[152:155]\n\tds_read_b64_tr_b16 v[28:29], v15 offset:45056\n\tds_read_b64_tr_b16 v[30:31], v15 offset:45568\n\tv_mfma_f32_16x16x32_bf16 v[160:163], v[32:35], a[76:79], v[160:163]\n\tbuffer_load_dwordx4 v11, s[12:15], s69 offen lds\n\ts_add_u32 s68, s44, 958464\n\t" \
    "v_mfma_f32_16x16x32_bf16 v[160:163], v[36:39], a[88:91], v[160:163]\n\ts_waitcnt lgkmcnt(8)\n\tv_mfma_f32_16x16x32_bf16 v[148:151], v[40:43], a[68:71], v[148:151]\n\tds_read_b64_tr_b16 v[32:33], v237 offset:40960\n\tds_read_b64_tr_b16 v[34:35], v237 offset:41472\n\tv_mfma_f32_16x16x32_bf16 v[148:151], v[44:47], a[80:83], v[148:151]\n\tv_mfma_f32_16x16x32_bf16 v[156:159], v[44:47], a[84:87], v[156:159]\n\tv_mfma_f32_16x16x32_bf16 v[156:159], v[40:43], a[72:75], v[156:159]\n\t" \
    "ds_read_b64_tr_b16 v[36:37], v237 offset:45056\n\tds_read_b64_tr_b16 v[38:39], v237 offset:45568\n\tv_mfma_f32_16x16x32_bf16 v[164:167], v[40:43], a[76:79], v[164:167]\n\tbuffer_load_dwordx4 v[168:171], v8, s[8:11], s68 offen offset:0 sc1\n\tv_mfma_f32_16x16x32_bf16 v[164:167], v[44:47], a[88:91], v[164:167]\n\ts_waitcnt vmcnt(49) lgkmcnt(8)\n\tv_mfma_f32_16x16x32_bf16 v[120:123], v[16:19], v[180:183], v[120:123]\n\tds_read_b64_tr_b16 v[40:41], v238 offset:40960\n\t" \
    "ds_read_b64_tr_b16 v[42:43], v238 offset:41472\n\tv_mfma_f32_16x16x32_bf16 v[120:123], v[20:23], v[204:207], v[120:123]\n\tbuffer_load_dwordx4 v[172:175], v8, s[8:11], s68 offen offset:1024 sc1\n\tv_mfma_f32_16x16x32_bf16 v[128:131], v[20:23], v[208:211], v[128:131]\n\tv_mfma_f32_16x16x32_bf16 v[128:131], v[16:19], v[184:187], v[128:131]\n\tds_read_b64_tr_b16 v[44:45], v238 offset:45056\n\tds_read_b64_tr_b16 v[46:47], v238 offset:45568\n\tv_mfma_f32_16x16x32_bf16 v[136:139], v[16:19], v[188:191], v[136:139]\n\t" \
    "buffer_load_dwordx4 v[176:179], v8, s[8:11], s68 offen offset:2048 sc1\n\ts_add_u32 s68, s44, 995328\n\tv_mfma_f32_16x16x32_bf16 v[136:139], v[20:23], v[212:215], v[136:139]\n\ts_waitcnt lgkmcnt(8)\n\tv_mfma_f32_16x16x32_bf16 v[124:127], v[24:27], v[180:183], v[124:127]\n\tds_read_b64_tr_b16 v[16:17], v14 offset:49152\n\tds_read_b64_tr_b16 v[18:19], v14 offset:49664\n\tv_mfma_f32_16x16x32_bf16 v[124:127], v[28:31], v[204:207], v[124:127]\n\t" \
    "v_mfma_f32_16x16x32_bf16 v[132:135], v[28:31], v[208:211], v[132:135]\n\tv_mfma_f32_16x16x32_bf16 v[132:135], v[24:27], v[184:187], v[132:135]\n\tds_read_b64_tr_b16 v[20:21], v14 offset:53248\n\tds_read_b64_tr_b16 v[22:23], v14 offset:53760\n\tv_mfma_f32_16x16x32_bf16 v[140:143], v[24:27], v[188:191], v[140:143]\n\tbuffer_load_dwordx4 a[140:143], v8, s[8:11], s68 offen offset:0 sc1\n\tv_mfma_f32_16x16x32_bf16 v[140:143], v[28:31], v[212:215], v[140:143]\n\ts_waitcnt lgkmcnt(8)\n\t" \
    "v_mfma_f32_16x16x32_bf16 v[144:147], v[32:35], v[180:183], v[144:147]\n\tds_read_b64_tr_b16 v[24:25], v15 offset:49152\n\tds_read_b64_tr_b16 v[26:27], v15 offset:49664\n\tv_mfma_f32_16x16x32_bf16 v[144:147], v[36:39], v[204:207], v[144:147]\n\tbuffer_load_dwordx4 a[144:147], v8, s[8:11], s68 offen offset:1024 sc1\n\tv_mfma_f32_16x16x32_bf16 v[152:155], v[36:39], v[208:211], v[152:155]\n\tv_mfma_f32_16x16x32_bf16 v[152:155], v[32:35], v[184:187], v[152:155]\n\tds_read_b64_tr_b16 v[28:29], v15 offset:53248\n\t" \
    "ds_read_b64_tr_b16 v[30:31], v15 offset:53760\n\tv_mfma_f32_16x16x32_bf16 v[160:163], v[32:35], v[188:191], v[160:163]\n\tbuffer_load_dwordx4 a[148:151], v8, s[8:11], s68 offen offset:2048 sc1\n\ts_add_u32 s68, s44, 1032192\n\tv_mfma_f32_16x16x32_bf16 v[160:163], v[36:39], v[212:215], v[160:163]\n\ts_waitcnt lgkmcnt(8)\n\tv_mfma_f32_16x16x32_bf16 v[148:151], v[40:43], v[180:183], v[148:151]\n\tds_read_b64_tr_b16 v[32:33], v237 offset:49152\n\t" \
    "ds_read_b64_tr_b16 v[34:35], v237 offset:49664\n\tv_mfma_f32_16x16x32_bf16 v[148:151], v[44:47], v[204:207], v[148:151]\n\tv_mfma_f32_16x16x32_bf16 v[156:159], v[44:47], v[208:211], v[156:159]\n\tv_mfma_f32_16x16x32_bf16 v[156:159], v[40:43], v[184:187], v[156:159]\n\tds_read_b64_tr_b16 v[36:37], v237 offset:53248\n\tds_read_b64_tr_b16 v[38:39], v237 offset:53760\n\tv_mfma_f32_16x16x32_bf16 v[164:167], v[40:43], v[188:191], v[164:167]\n\tbuffer_load_dwordx4 a[152:155], v8, s[8:11], s68 offen offset:0 sc1\n\t" \
    "v_mfma_f32_16x16x32_bf16 v[164:167], v[44:47], v[212:215], v[164:167]\n\ts_waitcnt vmcnt(53)\n\ts_barrier\n\ts_waitcnt vmcnt(47) lgkmcnt(8)\n\tv_mfma_f32_16x16x32_bf16 v[120:123], v[16:19], a[92:95], v[120:123]\n\tds_read_b64_tr_b16 v[40:41], v238 offset:49152\n\tds_read_b64_tr_b16 v[42:43], v238 offset:49664\n\tv_mfma_f32_16x16x32_bf16 v[120:123], v[20:23], a[104:107], v[120:123]\n\t" \
    "buffer_load_dwordx4 a[156:159], v8, s[8:11], s68 offen offset:1024 sc1\n\tv_mfma_f32_16x16x32_bf16 v[128:131], v[20:23], a[108:111], v[128:131]\n\tv_mfma_f32_16x16x32_bf16 v[128:131], v[16:19], a[96:99], v[128:131]\n\tds_read_b64_tr_b16 v[44:45], v238 offset:53248\n\tds_read_b64_tr_b16 v[46:47], v238 offset:53760\n\tv_mfma_f32_16x16x32_bf16 v[136:139], v[16:19], a[100:103], v[136:139]\n\tbuffer_load_dwordx4 a[160:163], v8, s[8:11], s68 offen offset:2048 sc1\n\ts_add_u32 s68, s44, 1069056\n\t" \
    "v_mfma_f32_16x16x32_bf16 v[136:139], v[20:23], a[112:115], v[136:139]\n\ts_waitcnt lgkmcnt(8)\n\tv_mfma_f32_16x16x32_bf16 v[124:127], v[24:27], a[92:95], v[124:127]\n\tds_read_b64_tr_b16 v[16:17], v14 offset:57344\n\tds_read_b64_tr_b16 v[18:19], v14 offset:57856\n\tv_mfma_f32_16x16x32_bf16 v[124:127], v[28:31], a[104:107], v[124:127]\n\tbuffer_load_dwordx4 a[164:167], v8, s[8:11], s68 offen offset:0 sc1\n\tv_mfma_f32_16x16x32_bf16 v[132:135], v[28:31], a[108:111], v[132:135]\n\t" \
    "v_mfma_f32_16x16x32_bf16 v[132:135], v[24:27], a[96:99], v[132:135]\n\tds_read_b64_tr_b16 v[20:21], v14 offset:61440\n\tds_read_b64_tr_b16 v[22:23], v14 offset:61952\n\tv_mfma_f32_16x16x32_bf16 v[140:143], v[24:27], a[100:103], v[140:143]\n\tv_mfma_f32_16x16x32_bf16 v[140:143], v[28:31], a[112:115], v[140:143]\n\ts_waitcnt lgkmcnt(8)\n\tv_mfma_f32_16x16x32_bf16 v[144:147], v[32:35], a[92:95], v[144:147]\n\tds_read_b64_tr_b16 v[24:25], v15 offset:57344\n\t" \
    "ds_read_b64_tr_b16 v[26:27], v15 offset:57856\n\tv_mfma_f32_16x16x32_bf16 v[144:147], v[36:39], a[104:107], v[144:147]\n\tbuffer_load_dwordx4 a[168:171], v8, s[8:11], s68 offen offset:1024 sc1\n\tv_mfma_f32_16x16x32_bf16 v[152:155], v[36:39], a[108:111], v[152:155]\n\tv_mfma_f32_16x16x32_bf16 v[152:155], v[32:35], a[96:99], v[152:155]\n\tds_read_b64_tr_b16 v[28:29], v15 offset:61440\n\tds_read_b64_tr_b16 v[30:31], v15 offset:61952\n\tv_mfma_f32_16x16x32_bf16 v[160:163], v[32:35], a[100:103], v[160:163]\n\t" \
    "buffer_load_dwordx4 a[172:175], v8, s[8:11], s68 offen offset:2048 sc1\n\ts_add_u32 s68, s44, 1105920\n\tv_mfma_f32_16x16x32_bf16 v[160:163], v[36:39], a[112:115], v[160:163]\n\ts_waitcnt lgkmcnt(8)\n\tv_mfma_f32_16x16x32_bf16 v[148:151], v[40:43], a[92:95], v[148:151]\n\tds_read_b64_tr_b16 v[32:33], v237 offset:57344\n\tds_read_b64_tr_b16 v[34:35], v237 offset:57856\n\tv_mfma_f32_16x16x32_bf16 v[148:151], v[44:47], a[104:107], v[148:151]\n\t" \
    "buffer_load_dwordx4 a[176:179], v8, s[8:11], s68 offen offset:0 sc1\n\tv_mfma_f32_16x16x32_bf16 v[156:159], v[44:47], a[108:111], v[156:159]\n\tv_mfma_f32_16x16x32_bf16 v[156:159], v[40:43], a[96:99], v[156:159]\n\tds_read_b64_tr_b16 v[36:37], v237 offset:61440\n\tds_read_b64_tr_b16 v[38:39], v237 offset:61952\n\tv_mfma_f32_16x16x32_bf16 v[164:167], v[40:43], a[100:103], v[164:167]\n\tv_mfma_f32_16x16x32_bf16 v[164:167], v[44:47], a[112:115], v[164:167]\n\ts_waitcnt vmcnt(47) lgkmcnt(8)\n\t" \
    "v_mfma_f32_16x16x32_bf16 v[120:123], v[16:19], v[216:219], v[120:123]\n\tds_read_b64_tr_b16 v[40:41], v238 offset:57344\n\tds_read_b64_tr_b16 v[42:43], v238 offset:57856\n\tv_mfma_f32_16x16x32_bf16 v[120:123], v[20:23], a[8:11], v[120:123]\n\tbuffer_load_dwordx4 a[180:183], v8, s[8:11], s68 offen offset:1024 sc1\n\tv_mfma_f32_16x16x32_bf16 v[128:131], v[20:23], a[12:15], v[128:131]\n\tv_mfma_f32_16x16x32_bf16 v[128:131], v[16:19], a[0:3], v[128:131]\n\tds_read_b64_tr_b16 v[44:45], v238 offset:61440\n\t" \
    "ds_read_b64_tr_b16 v[46:47], v238 offset:61952\n\tv_mfma_f32_16x16x32_bf16 v[136:139], v[16:19], a[4:7], v[136:139]\n\tbuffer_load_dwordx4 a[184:187], v8, s[8:11], s68 offen offset:2048 sc1\n\ts_add_u32 s68, s44, 1142784\n\tv_mfma_f32_16x16x32_bf16 v[136:139], v[20:23], a[16:19], v[136:139]\n\ts_waitcnt lgkmcnt(8)\n\tv_mfma_f32_16x16x32_bf16 v[124:127], v[24:27], v[216:219], v[124:127]\n\tds_read_b64_tr_b16 v[16:17], v0 offset:0\n\t" \
    "ds_read_b64_tr_b16 v[18:19], v0 offset:512\n\tv_mfma_f32_16x16x32_bf16 v[124:127], v[28:31], a[8:11], v[124:127]\n\tbuffer_load_dwordx4 a[188:191], v8, s[8:11], s68 offen offset:0 sc1\n\tv_mfma_f32_16x16x32_bf16 v[132:135], v[28:31], a[12:15], v[132:135]\n\tv_mfma_f32_16x16x32_bf16 v[132:135], v[24:27], a[0:3], v[132:135]\n\tds_read_b64_tr_b16 v[20:21], v0 offset:4096\n\tds_read_b64_tr_b16 v[22:23], v0 offset:4608\n\tv_mfma_f32_16x16x32_bf16 v[140:143], v[24:27], a[4:7], v[140:143]\n\t" \
    "v_mfma_f32_16x16x32_bf16 v[140:143], v[28:31], a[16:19], v[140:143]\n\ts_waitcnt lgkmcnt(8)\n\tv_mfma_f32_16x16x32_bf16 v[144:147], v[32:35], v[216:219], v[144:147]\n\tds_read_b64_tr_b16 v[24:25], v1 offset:0\n\tds_read_b64_tr_b16 v[26:27], v1 offset:512\n\tv_mfma_f32_16x16x32_bf16 v[144:147], v[36:39], a[8:11], v[144:147]\n\tbuffer_load_dwordx4 a[192:195], v8, s[8:11], s68 offen offset:1024 sc1\n\tv_mfma_f32_16x16x32_bf16 v[152:155], v[36:39], a[12:15], v[152:155]\n\t" \
    "v_mfma_f32_16x16x32_bf16 v[152:155], v[32:35], a[0:3], v[152:155]\n\tds_read_b64_tr_b16 v[28:29], v1 offset:4096\n\tds_read_b64_tr_b16 v[30:31], v1 offset:4608\n\tv_mfma_f32_16x16x32_bf16 v[160:163], v[32:35], a[4:7], v[160:163]\n\tbuffer_load_dwordx4 a[196:199], v8, s[8:11], s68 offen offset:2048 sc1\n\ts_cmp_ge_u32 s43, 3\n\ts_cselect_b32 s69, s70, s43\n\ts_mul_i32 s69, s69, 294912\n\t" \
    "v_mfma_f32_16x16x32_bf16 v[160:163], v[36:39], a[16:19], v[160:163]\n\ts_add_u32 s69, s69, 5308416\n\ts_add_u32 m0, s54, 73728\n\ts_waitcnt lgkmcnt(8)\n\tv_mfma_f32_16x16x32_bf16 v[148:151], v[40:43], v[216:219], v[148:151]\n\tds_read_b64_tr_b16 v[32:33], v2 offset:0\n\tds_read_b64_tr_b16 v[34:35], v2 offset:512\n\tv_mfma_f32_16x16x32_bf16 v[148:151], v[44:47], a[8:11], v[148:151]\n\t" \
    "buffer_load_dwordx4 v11, s[12:15], s69 offen lds\n\ts_cmp_ge_u32 s43, 2\n\ts_cselect_b32 s69, s70, s43\n\ts_mul_i32 s69, s69, 294912\n\tv_mfma_f32_16x16x32_bf16 v[156:159], v[44:47], a[12:15], v[156:159]\n\ts_add_u32 s69, s69, 5603328\n\ts_add_u32 m0, s54, 77824\n\tv_mfma_f32_16x16x32_bf16 v[156:159], v[40:43], a[0:3], v[156:159]\n\t" \
    "ds_read_b64_tr_b16 v[36:37], v2 offset:4096\n\tds_read_b64_tr_b16 v[38:39], v2 offset:4608\n\tv_mfma_f32_16x16x32_bf16 v[164:167], v[40:43], a[4:7], v[164:167]\n\tv_mfma_f32_16x16x32_bf16 v[164:167], v[44:47], a[16:19], v[164:167]\n\ts_waitcnt vmcnt(45)\n\ts_barrier\n\ts_waitcnt vmcnt(39) lgkmcnt(8)\n\tv_mfma_f32_16x16x32_bf16 v[120:123], v[16:19], a[20:23], v[120:123]\n\t" \
    "ds_read_b64_tr_b16 v[40:41], v3 offset:0\n\tds_read_b64_tr_b16 v[42:43], v3 offset:512\n\tv_mfma_f32_16x16x32_bf16 v[120:123], v[20:23], a[32:35], v[120:123]\n\tbuffer_load_dwordx4 v11, s[12:15], s69 offen lds\n\ts_cmp_ge_u32 s43, 1\n\ts_cselect_b32 s69, s70, s43\n\ts_mul_i32 s69, s69, 294912\n\tv_mfma_f32_16x16x32_bf16 v[128:131], v[20:23], a[36:39], v[128:131]\n\t" \
    "s_add_u32 s69, s69, 5898240\n\ts_add_u32 m0, s54, 81920\n\tv_mfma_f32_16x16x32_bf16 v[128:131], v[16:19], a[24:27], v[128:131]\n\tds_read_b64_tr_b16 v[44:45], v3 offset:4096\n\tds_read_b64_tr_b16 v[46:47], v3 offset:4608\n\tv_mfma_f32_16x16x32_bf16 v[136:139], v[16:19], a[28:31], v[136:139]\n\tbuffer_load_dwordx4 v11, s[12:15], s69 offen lds\n\ts_mov_b32 s69, 6193152\n\t" \
    "s_add_u32 m0, s54, 86016\n\tv_mfma_f32_16x16x32_bf16 v[136:139], v[20:23], a[40:43], v[136:139]\n\ts_waitcnt lgkmcnt(8)\n\tv_mfma_f32_16x16x32_bf16 v[124:127], v[24:27], a[20:23], v[124:127]\n\tds_read_b64_tr_b16 v[16:17], v0 offset:8192\n\tds_read_b64_tr_b16 v[18:19], v0 offset:8704\n\tv_mfma_f32_16x16x32_bf16 v[124:127], v[28:31], a[32:35], v[124:127]\n\tbuffer_load_dwordx4 v11, s[12:15], s69 offen lds\n\t" \
    "s_mov_b32 s69, 6488064\n\ts_add_u32 m0, s54, 90112\n\tv_mfma_f32_16x16x32_bf16 v[132:135], v[28:31], a[36:39], v[132:135]\n\tv_mfma_f32_16x16x32_bf16 v[132:135], v[24:27], a[24:27], v[132:135]\n\tds_read_b64_tr_b16 v[20:21], v0 offset:12288\n\tds_read_b64_tr_b16 v[22:23], v0 offset:12800\n\tv_mfma_f32_16x16x32_bf16 v[140:143], v[24:27], a[28:31], v[140:143]\n\tbuffer_load_dwordx4 v11, s[12:15], s69 offen lds\n\t" \
    "s_mov_b32 s69, 6782976\n\ts_add_u32 m0, s54, 94208\n\tv_mfma_f32_16x16x32_bf16 v[140:143], v[28:31], a[40:43], v[140:143]\n\ts_waitcnt lgkmcnt(8)\n\tv_mfma_f32_16x16x32_bf16 v[144:147], v[32:35], a[20:23], v[144:147]\n\tds_read_b64_tr_b16 v[24:25], v1 offset:8192\n\tds_read_b64_tr_b16 v[26:27], v1 offset:8704\n\tv_mfma_f32_16x16x32_bf16 v[144:147], v[36:39], a[32:35], v[144:147]\n\t" \
    "buffer_load_dwordx4 v11, s[12:15], s69 offen lds\n\ts_mov_b32 s69, 7077888\n\ts_add_u32 m0, s54, 98304\n\tv_mfma_f32_16x16x32_bf16 v[152:155], v[36:39], a[36:39], v[152:155]\n\tv_mfma_f32_16x16x32_bf16 v[152:155], v[32:35], a[24:27], v[152:155]\n\tds_read_b64_tr_b16 v[28:29], v1 offset:12288\n\tds_read_b64_tr_b16 v[30:31], v1 offset:12800\n\tv_mfma_f32_16x16x32_bf16 v[160:163], v[32:35], a[28:31], v[160:163]\n\t" \
    "v_mfma_f32_16x16x32_bf16 v[160:163], v[36:39], a[40:43], v[160:163]\n\ts_waitcnt lgkmcnt(8)\n\tv_mfma_f32_16x16x32_bf16 v[148:151], v[40:43], a[20:23], v[148:151]\n\tds_read_b64_tr_b16 v[32:33], v0 offset:16384\n\tds_read_b64_tr_b16 v[34:35], v0 offset:16896\n\tv_mfma_f32_16x16x32_bf16 v[148:151], v[44:47], a[32:35], v[148:151]\n\tbuffer_load_dwordx4 v11, s[12:15], s69 offen lds\n\ts_mov_b32 s69, 7372800\n\t" \
    "s_add_u32 m0, s54, 102400\n\tv_mfma_f32_16x16x32_bf16 v[156:159], v[44:47], a[36:39], v[156:159]\n\tv_mfma_f32_16x16x32_bf16 v[156:159], v[40:43], a[24:27], v[156:159]\n\tds_read_b64_tr_b16 v[36:37], v0 offset:20480\n\tds_read_b64_tr_b16 v[38:39], v0 offset:20992\n\tv_mfma_f32_16x16x32_bf16 v[164:167], v[40:43], a[28:31], v[164:167]\n\tbuffer_load_dwordx4 v11, s[12:15], s69 offen lds\n\ts_mov_b32 s69, 7667712\n\t" \
    "s_add_u32 m0, s54, 106496\n\tv_mfma_f32_16x16x32_bf16 v[164:167], v[44:47], a[40:43], v[164:167]\n\ts_waitcnt vmcnt(40) lgkmcnt(8)\n\tv_mfma_f32_16x16x32_bf16 v[120:123], v[16:19], a[116:119], v[120:123]\n\tds_read_b64_tr_b16 v[40:41], v1 offset:16384\n\tds_read_b64_tr_b16 v[42:43], v1 offset:16896\n\tv_mfma_f32_16x16x32_bf16 v[120:123], v[20:23], a[44:47], v[120:123]\n\tbuffer_load_dwordx4 v11, s[12:15], s69 offen lds\n\t" \
    "s_mov_b32 s69, 7962624\n\ts_add_u32 m0, s54, 110592\n\tv_mfma_f32_16x16x32_bf16 v[128:131], v[20:23], a[48:51], v[128:131]\n\tv_mfma_f32_16x16x32_bf16 v[128:131], v[16:19], a[120:123], v[128:131]\n\tds_read_b64_tr_b16 v[44:45], v1 offset:20480\n\tds_read_b64_tr_b16 v[46:47], v1 offset:20992\n\tv_mfma_f32_16x16x32_bf16 v[136:139], v[16:19], a[124:127], v[136:139]\n\tv_mfma_f32_16x16x32_bf16 v[136:139], v[20:23], a[52:55], v[136:139]\n\t" \
    "s_waitcnt lgkmcnt(8)\n\tv_mfma_f32_16x16x32_bf16 v[124:127], v[24:27], a[116:119], v[124:127]\n\tds_read_b64_tr_b16 v[16:17], v0 offset:24576\n\tds_read_b64_tr_b16 v[18:19], v0 offset:25088\n\tv_mfma_f32_16x16x32_bf16 v[124:127], v[28:31], a[44:47], v[124:127]\n\tbuffer_load_dwordx4 v11, s[12:15], s69 offen lds\n\ts_mov_b32 s69, 8257536\n\ts_add_u32 m0, s54, 114688\n\t" \
    "v_mfma_f32_16x16x32_bf16 v[132:135], v[28:31], a[48:51], v[132:135]\n\tv_mfma_f32_16x16x32_bf16 v[132:135], v[24:27], a[120:123], v[132:135]\n\tds_read_b64_tr_b16 v[20:21], v0 offset:28672\n\tds_read_b64_tr_b16 v[22:23], v0 offset:29184\n\tv_mfma_f32_16x16x32_bf16 v[140:143], v[24:27], a[124:127], v[140:143]\n\tbuffer_load_dwordx4 v11, s[12:15], s69 offen lds\n\ts_mov_b32 s69, 8552448\n\ts_add_u32 m0, s54, 118784\n\t" \
    "v_mfma_f32_16x16x32_bf16 v[140:143], v[28:31], a[52:55], v[140:143]\n\ts_waitcnt vmcnt(37) lgkmcnt(8)\n\tv_mfma_f32_16x16x32_bf16 v[120:123], v[32:35], a[56:59], v[120:123]\n\tds_read_b64_tr_b16 v[24:25], v1 offset:24576\n\tds_read_b64_tr_b16 v[26:27], v1 offset:25088\n\tv_mfma_f32_16x16x32_bf16 v[120:123], v[36:39], a[128:131], v[120:123]\n\tv_mfma_f32_16x16x32_bf16 v[128:131], v[36:39], a[132:135], v[128:131]\n\tv_mfma_f32_16x16x32_bf16 v[128:131], v[32:35], a[60:63], v[128:131]\n\t" \
    "ds_read_b64_tr_b16 v[28:29], v1 offset:28672\n\tds_read_b64_tr_b16 v[30:31], v1 offset:29184\n\tv_mfma_f32_16x16x32_bf16 v[136:139], v[32:35], a[64:67], v[136:139]\n\tbuffer_load_dwordx4 v11, s[12:15], s69 offen lds\n\ts_mov_b32 s69, 8847360\n\ts_add_u32 m0, s54, 122880\n\tv_mfma_f32_16x16x32_bf16 v[136:139], v[36:39], a[136:139], v[136:139]\n\ts_waitcnt lgkmcnt(8)\n\t" \
    "v_mfma_f32_16x16x32_bf16 v[124:127], v[40:43], a[56:59], v[124:127]\n\tds_read_b64_tr_b16 v[32:33], v0 offset:32768\n\tds_read_b64_tr_b16 v[34:35], v0 offset:33280\n\tv_mfma_f32_16x16x32_bf16 v[124:127], v[44:47], a[128:131], v[124:127]\n\tbuffer_load_dwordx4 v11, s[12:15], s69 offen lds\n\ts_mov_b32 s69, 9142272\n\ts_add_u32 m0, s54, 126976\n\tv_mfma_f32_16x16x32_bf16 v[132:135], v[44:47], a[132:135], v[132:135]\n\t" \
    "v_mfma_f32_16x16x32_bf16 v[132:135], v[40:43], a[60:63], v[132:135]\n\tds_read_b64_tr_b16 v[36:37], v0 offset:36864\n\tds_read_b64_tr_b16 v[38:39], v0 offset:37376\n\tv_mfma_f32_16x16x32_bf16 v[140:143], v[40:43], a[64:67], v[140:143]\n\tbuffer_load_dwordx4 v11, s[12:15], s69 offen lds\n\tv_mfma_f32_16x16x32_bf16 v[140:143], v[44:47], a[136:139], v[140:143]\n\ts_waitcnt vmcnt(32)\n\ts_barrier\n\t" \
    "s_waitcnt vmcnt(26) lgkmcnt(8)\n\tv_mfma_f32_16x16x32_bf16 v[120:123], v[16:19], v[168:171], v[120:123]\n\tds_read_b64_tr_b16 v[40:41], v1 offset:32768\n\tds_read_b64_tr_b16 v[42:43], v1 offset:33280\n\tv_mfma_f32_16x16x32_bf16 v[120:123], v[20:23], a[140:143], v[120:123]\n\tv_mfma_f32_16x16x32_bf16 v[128:131], v[20:23], a[144:147], v[128:131]\n\tv_mfma_f32_16x16x32_bf16 v[128:131], v[16:19], v[172:175], v[128:131]\n\tds_read_b64_tr_b16 v[44:45], v1 offset:36864\n\t" \
    "ds_read_b64_tr_b16 v[46:47], v1 offset:37376\n\tv_mfma_f32_16x16x32_bf16 v[136:139], v[16:19], v[176:179], v[136:139]\n\tv_mfma_f32_16x16x32_bf16 v[136:139], v[20:23], a[148:151], v[136:139]\n\ts_waitcnt lgkmcnt(8)\n\tv_mfma_f32_16x16x32_bf16 v[124:127], v[24:27], v[168:171], v[124:127]\n\tds_read_b64_tr_b16 v[16:17], v0 offset:40960\n\tds_read_b64_tr_b16 v[18:19], v0 offset:41472\n\tv_mfma_f32_16x16x32_bf16 v[124:127], v[28:31], a[140:143], v[124:127]\n\t" \
    "v_mfma_f32_16x16x32_bf16 v[132:135], v[28:31], a[144:147], v[132:135]\n\tv_mfma_f32_16x16x32_bf16 v[132:135], v[24:27], v[172:175], v[132:135]\n\tds_read_b64_tr_b16 v[20:21], v0 offset:45056\n\tds_read_b64_tr_b16 v[22:23], v0 offset:45568\n\tv_mfma_f32_16x16x32_bf16 v[140:143], v[24:27], v[176:179], v[140:143]\n\tv_mfma_f32_16x16x32_bf16 v[140:143], v[28:31], a[148:151], v[140:143]\n\ts_waitcnt vmcnt(20) lgkmcnt(8)\n\tv_mfma_f32_16x16x32_bf16 v[120:123], v[32:35], a[152:155], v[120:123]\n\t" \
    "ds_read_b64_tr_b16 v[24:25], v1 offset:40960\n\tds_read_b64_tr_b16 v[26:27], v1 offset:41472\n\tv_mfma_f32_16x16x32_bf16 v[120:123], v[36:39], a[164:167], v[120:123]\n\tv_mfma_f32_16x16x32_bf16 v[128:131], v[36:39], a[168:171], v[128:131]\n\tv_mfma_f32_16x16x32_bf16 v[128:131], v[32:35], a[156:159], v[128:131]\n\tds_read_b64_tr_b16 v[28:29], v1 offset:45056\n\tds_read_b64_tr_b16 v[30:31], v1 offset:45568\n\tv_mfma_f32_16x16x32_bf16 v[136:139], v[32:35], a[160:163], v[136:139]\n\t" \
    "v_mfma_f32_16x16x32_bf16 v[136:139], v[36:39], a[172:175], v[136:139]\n\ts_waitcnt lgkmcnt(8)\n\tv_mfma_f32_16x16x32_bf16 v[124:127], v[40:43], a[152:155], v[124:127]\n\tds_read_b64_tr_b16 v[32:33], v0 offset:49152\n\tds_read_b64_tr_b16 v[34:35], v0 offset:49664\n\tv_mfma_f32_16x16x32_bf16 v[124:127], v[44:47], a[164:167], v[124:127]\n\tv_mfma_f32_16x16x32_bf16 v[132:135], v[44:47], a[168:171], v[132:135]\n\tv_mfma_f32_16x16x32_bf16 v[132:135], v[40:43], a[156:159], v[132:135]\n\t" \
    "ds_read_b64_tr_b16 v[36:37], v0 offset:53248\n\tds_read_b64_tr_b16 v[38:39], v0 offset:53760\n\tv_mfma_f32_16x16x32_bf16 v[140:143], v[40:43], a[160:163], v[140:143]\n\tv_mfma_f32_16x16x32_bf16 v[140:143], v[44:47], a[172:175], v[140:143]\n\ts_waitcnt vmcnt(14) lgkmcnt(8)\n\tv_mfma_f32_16x16x32_bf16 v[120:123], v[16:19], a[176:179], v[120:123]\n\tds_read_b64_tr_b16 v[40:41], v1 offset:49152\n\tds_read_b64_tr_b16 v[42:43], v1 offset:49664\n\t" \
    "v_mfma_f32_16x16x32_bf16 v[120:123], v[20:23], a[188:191], v[120:123]\n\tv_mfma_f32_16x16x32_bf16 v[128:131], v[20:23], a[192:195], v[128:131]\n\tv_mfma_f32_16x16x32_bf16 v[128:131], v[16:19], a[180:183], v[128:131]\n\tds_read_b64_tr_b16 v[44:45], v1 offset:53248\n\tds_read_b64_tr_b16 v[46:47], v1 offset:53760\n\tv_mfma_f32_16x16x32_bf16 v[136:139], v[16:19], a[184:187], v[136:139]\n\tv_mfma_f32_16x16x32_bf16 v[136:139], v[20:23], a[196:199], v[136:139]\n\ts_waitcnt lgkmcnt(8)\n\t" \
    "v_mfma_f32_16x16x32_bf16 v[124:127], v[24:27], a[176:179], v[124:127]\n\tds_read_b64_tr_b16 v[16:17], v0 offset:57344\n\tds_read_b64_tr_b16 v[18:19], v0 offset:57856\n\tv_mfma_f32_16x16x32_bf16 v[124:127], v[28:31], a[188:191], v[124:127]\n\tv_mfma_f32_16x16x32_bf16 v[132:135], v[28:31], a[192:195], v[132:135]\n\tv_mfma_f32_16x16x32_bf16 v[132:135], v[24:27], a[180:183], v[132:135]\n\tds_read_b64_tr_b16 v[20:21], v0 offset:61440\n\tds_read_b64_tr_b16 v[22:23], v0 offset:61952\n\t" \
    "v_mfma_f32_16x16x32_bf16 v[140:143], v[24:27], a[184:187], v[140:143]\n\tv_mfma_f32_16x16x32_bf16 v[140:143], v[28:31], a[196:199], v[140:143]\n\ts_add_u32 s68, s52, 0\n\tv_cvt_pk_bf16_f32 v120, v120, v121\n\tv_cvt_pk_bf16_f32 v121, v122, v123\n\tv_cvt_pk_bf16_f32 v122, v124, v125\n\tv_cvt_pk_bf16_f32 v123, v126, v127\n\tbuffer_store_dwordx4 v[120:123], v8, s[8:11], s68 offen offset:0 sc1\n\t" \
    "v_cvt_pk_bf16_f32 v124, v128, v129\n\tv_cvt_pk_bf16_f32 v125, v130, v131\n\tv_cvt_pk_bf16_f32 v126, v132, v133\n\tv_cvt_pk_bf16_f32 v127, v134, v135\n\tbuffer_store_dwordx4 v[124:127], v8, s[8:11], s68 offen offset:1024 sc1\n\tv_cvt_pk_bf16_f32 v128, v136, v137\n\tv_cvt_pk_bf16_f32 v129, v138, v139\n\tv_cvt_pk_bf16_f32 v130, v140, v141\n\t" \
    "v_cvt_pk_bf16_f32 v131, v142, v143\n\tbuffer_store_dwordx4 v[128:131], v8, s[8:11], s68 offen offset:2048 sc1\n\tv_add3_u32 v132, s36, v120, v121\n\tv_add3_u32 v133, v122, v123, v124\n\tv_add3_u32 v132, v132, v125, v126\n\tv_add3_u32 v133, v133, v127, v128\n\tv_add3_u32 v132, v132, v129, v130\n\tv_add3_u32 v132, v132, v133, v131\n\t" \
    "buffer_store_dword v132, v236, s[8:11], s68 offen offset:3072 sc1\n\ts_waitcnt lgkmcnt(8)\n\tv_mfma_f32_16x16x32_bf16 v[48:51], v[32:35], a[68:71], v[48:51]\n\tds_read_b64_tr_b16 v[24:25], v1 offset:57344\n\tds_read_b64_tr_b16 v[26:27], v1 offset:57856\n\tv_mfma_f32_16x16x32_bf16 v[48:51], v[36:39], a[80:83], v[48:51]\n\tv_mfma_f32_16x16x32_bf16 v[56:59], v[36:39], a[84:87], v[56:59]\n\tv_mfma_f32_16x16x32_bf16 v[56:59], v[32:35], a[72:75], v[56:59]\n\t" \
    "ds_read_b64_tr_b16 v[28:29], v1 offset:61440\n\tds_read_b64_tr_b16 v[30:31], v1 offset:61952\n\tv_mfma_f32_16x16x32_bf16 v[64:67], v[32:35], a[76:79], v[64:67]\n\tv_mfma_f32_16x16x32_bf16 v[64:67], v[36:39], a[88:91], v[64:67]\n\ts_waitcnt lgkmcnt(8)\n\tv_mfma_f32_16x16x32_bf16 v[52:55], v[40:43], a[68:71], v[52:55]\n\tds_read_b64_tr_b16 v[32:33], v14 offset:0\n\tds_read_b64_tr_b16 v[34:35], v14 offset:512\n\t" \
    "v_mfma_f32_16x16x32_bf16 v[52:55], v[44:47], a[80:83], v[52:55]\n\tv_mfma_f32_16x16x32_bf16 v[60:63], v[44:47], a[84:87], v[60:63]\n\tv_mfma_f32_16x16x32_bf16 v[60:63], v[40:43], a[72:75], v[60:63]\n\tds_read_b64_tr_b16 v[36:37], v14 offset:4096\n\tds_read_b64_tr_b16 v[38:39], v14 offset:4608\n\tv_mfma_f32_16x16x32_bf16 v[68:71], v[40:43], a[76:79], v[68:71]\n\tv_mfma_f32_16x16x32_bf16 v[68:71], v[44:47], a[88:91], v[68:71]\n\ts_waitcnt vmcnt(10)\n\t" \
    "s_barrier\n\ts_waitcnt lgkmcnt(8)\n\tv_mfma_f32_16x16x32_bf16 v[48:51], v[16:19], v[180:183], v[48:51]\n\tds_read_b64_tr_b16 v[40:41], v15 offset:0\n\tds_read_b64_tr_b16 v[42:43], v15 offset:512\n\ts_lshl_b32 s69, s4, 18\n\tv_mfma_f32_16x16x32_bf16 v[48:51], v[20:23], v[204:207], v[48:51]\n\ts_add_u32 s69, s69, 0\n\t" \
    "s_add_u32 m0, s54, 131072\n\tv_mfma_f32_16x16x32_bf16 v[56:59], v[20:23], v[208:211], v[56:59]\n\tv_mfma_f32_16x16x32_bf16 v[56:59], v[16:19], v[184:187], v[56:59]\n\tds_read_b64_tr_b16 v[44:45], v15 offset:4096\n\tds_read_b64_tr_b16 v[46:47], v15 offset:4608\n\tv_mfma_f32_16x16x32_bf16 v[64:67], v[16:19], v[188:191], v[64:67]\n\tbuffer_load_dwordx4 v12, s[16:19], s69 offen lds\n\ts_lshl_b32 s69, s4, 18\n\t" \
    "s_add_u32 s69, s69, 0\n\tv_mfma_f32_16x16x32_bf16 v[64:67], v[20:23], v[212:215], v[64:67]\n\ts_add_u32 m0, s54, 135168\n\ts_waitcnt lgkmcnt(8)\n\tv_mfma_f32_16x16x32_bf16 v[52:55], v[24:27], v[180:183], v[52:55]\n\tds_read_b64_tr_b16 v[16:17], v14 offset:8192\n\tds_read_b64_tr_b16 v[18:19], v14 offset:8704\n\tv_mfma_f32_16x16x32_bf16 v[52:55], v[28:31], v[204:207], v[52:55]\n\t" \
    "buffer_load_dwordx4 v13, s[16:19], s69 offen lds\n\ts_lshl_b32 s69, s4, 18\n\ts_add_u32 s69, s69, 131072\n\tv_mfma_f32_16x16x32_bf16 v[60:63], v[28:31], v[208:211], v[60:63]\n\ts_add_u32 m0, s54, 139264\n\tv_mfma_f32_16x16x32_bf16 v[60:63], v[24:27], v[184:187], v[60:63]\n\tds_read_b64_tr_b16 v[20:21], v14 offset:12288\n\tds_read_b64_tr_b16 v[22:23], v14 offset:12800\n\t" \
    "v_mfma_f32_16x16x32_bf16 v[68:71], v[24:27], v[188:191], v[68:71]\n\tbuffer_load_dwordx4 v12, s[16:19], s69 offen lds\n\ts_lshl_b32 s69, s4, 18\n\ts_add_u32 s69, s69, 131072\n\tv_mfma_f32_16x16x32_bf16 v[68:71], v[28:31], v[212:215], v[68:71]\n\ts_add_u32 m0, s54, 143360\n\ts_waitcnt lgkmcnt(8)\n\tv_mfma_f32_16x16x32_bf16 v[48:51], v[32:35], a[92:95], v[48:51]\n\t" \
    "ds_read_b64_tr_b16 v[24:25], v15 offset:8192\n\tds_read_b64_tr_b16 v[26:27], v15 offset:8704\n\tv_mfma_f32_16x16x32_bf16 v[48:51], v[36:39], a[104:107], v[48:51]\n\tbuffer_load_dwordx4 v13, s[16:19], s69 offen lds\n\tv_mfma_f32_16x16x32_bf16 v[56:59], v[36:39], a[108:111], v[56:59]\n\tv_mfma_f32_16x16x32_bf16 v[56:59], v[32:35], a[96:99], v[56:59]\n\tds_read_b64_tr_b16 v[28:29], v15 offset:12288\n\tds_read_b64_tr_b16 v[30:31], v15 offset:12800\n\t" \
    "v_mfma_f32_16x16x32_bf16 v[64:67], v[32:35], a[100:103], v[64:67]\n\tv_mfma_f32_16x16x32_bf16 v[64:67], v[36:39], a[112:115], v[64:67]\n\ts_waitcnt lgkmcnt(8)\n\tv_mfma_f32_16x16x32_bf16 v[52:55], v[40:43], a[92:95], v[52:55]\n\tds_read_b64_tr_b16 v[32:33], v14 offset:16384\n\tds_read_b64_tr_b16 v[34:35], v14 offset:16896\n\tv_mfma_f32_16x16x32_bf16 v[52:55], v[44:47], a[104:107], v[52:55]\n\tv_mfma_f32_16x16x32_bf16 v[60:63], v[44:47], a[108:111], v[60:63]\n\t" \
    "v_mfma_f32_16x16x32_bf16 v[60:63], v[40:43], a[96:99], v[60:63]\n\tds_read_b64_tr_b16 v[36:37], v14 offset:20480\n\tds_read_b64_tr_b16 v[38:39], v14 offset:20992\n\tv_mfma_f32_16x16x32_bf16 v[68:71], v[40:43], a[100:103], v[68:71]\n\tv_mfma_f32_16x16x32_bf16 v[68:71], v[44:47], a[112:115], v[68:71]\n\ts_waitcnt lgkmcnt(8)\n\tv_mfma_f32_16x16x32_bf16 v[48:51], v[16:19], v[216:219], v[48:51]\n\tds_read_b64_tr_b16 v[40:41], v15 offset:16384\n\t" \
    "ds_read_b64_tr_b16 v[42:43], v15 offset:16896\n\tv_mfma_f32_16x16x32_bf16 v[48:51], v[20:23], a[8:11], v[48:51]\n\tv_mfma_f32_16x16x32_bf16 v[56:59], v[20:23], a[12:15], v[56:59]\n\tv_mfma_f32_16x16x32_bf16 v[56:59], v[16:19], a[0:3], v[56:59]\n\tds_read_b64_tr_b16 v[44:45], v15 offset:20480\n\tds_read_b64_tr_b16 v[46:47], v15 offset:20992\n\tv_mfma_f32_16x16x32_bf16 v[64:67], v[16:19], a[4:7], v[64:67]\n\tv_mfma_f32_16x16x32_bf16 v[64:67], v[20:23], a[16:19], v[64:67]\n\t" \
    "s_waitcnt lgkmcnt(8)\n\tv_mfma_f32_16x16x32_bf16 v[52:55], v[24:27], v[216:219], v[52:55]\n\tds_read_b64_tr_b16 v[16:17], v14 offset:24576\n\tds_read_b64_tr_b16 v[18:19], v14 offset:25088\n\tv_mfma_f32_16x16x32_bf16 v[52:55], v[28:31], a[8:11], v[52:55]\n\tv_mfma_f32_16x16x32_bf16 v[60:63], v[28:31], a[12:15], v[60:63]\n\tv_mfma_f32_16x16x32_bf16 v[60:63], v[24:27], a[0:3], v[60:63]\n\tds_read_b64_tr_b16 v[20:21], v14 offset:28672\n\t" \
    "ds_read_b64_tr_b16 v[22:23], v14 offset:29184\n\tv_mfma_f32_16x16x32_bf16 v[68:71], v[24:27], a[4:7], v[68:71]\n\tv_mfma_f32_16x16x32_bf16 v[68:71], v[28:31], a[16:19], v[68:71]\n\ts_waitcnt lgkmcnt(8)\n\tv_mfma_f32_16x16x32_bf16 v[48:51], v[32:35], a[20:23], v[48:51]\n\tds_read_b64_tr_b16 v[24:25], v15 offset:24576\n\tds_read_b64_tr_b16 v[26:27], v15 offset:25088\n\tv_mfma_f32_16x16x32_bf16 v[48:51], v[36:39], a[32:35], v[48:51]\n\t" \
    "v_mfma_f32_16x16x32_bf16 v[56:59], v[36:39], a[36:39], v[56:59]\n\tv_mfma_f32_16x16x32_bf16 v[56:59], v[32:35], a[24:27], v[56:59]\n\tds_read_b64_tr_b16 v[28:29], v15 offset:28672\n\tds_read_b64_tr_b16 v[30:31], v15 offset:29184\n\tv_mfma_f32_16x16x32_bf16 v[64:67], v[32:35], a[28:31], v[64:67]\n\tv_mfma_f32_16x16x32_bf16 v[64:67], v[36:39], a[40:43], v[64:67]\n\ts_waitcnt lgkmcnt(8)\n\tv_mfma_f32_16x16x32_bf16 v[52:55], v[40:43], a[20:23], v[52:55]\n\t" \
    "ds_read_b64_tr_b16 v[32:33], v14 offset:32768\n\tds_read_b64_tr_b16 v[34:35], v14 offset:33280\n\ts_add_u32 s68, s72, 0\n\tv_mfma_f32_16x16x32_bf16 v[52:55], v[44:47], a[32:35], v[52:55]\n\tbuffer_load_dwordx4 v[120:123], v8, s[8:11], s68 offen offset:0 sc1\n\tv_mfma_f32_16x16x32_bf16 v[60:63], v[44:47], a[36:39], v[60:63]\n\tv_mfma_f32_16x16x32_bf16 v[60:63], v[40:43], a[24:27], v[60:63]\n\tds_read_b64_tr_b16 v[36:37], v14 offset:36864\n\t" \
    "ds_read_b64_tr_b16 v[38:39], v14 offset:37376\n\tv_mfma_f32_16x16x32_bf16 v[68:71], v[40:43], a[28:31], v[68:71]\n\tbuffer_load_dwordx4 v[124:127], v8, s[8:11], s68 offen offset:1024 sc1\n\tv_mfma_f32_16x16x32_bf16 v[68:71], v[44:47], a[40:43], v[68:71]\n\ts_waitcnt vmcnt(10)\n\ts_barrier\n\ts_waitcnt lgkmcnt(8)\n\tv_mfma_f32_16x16x32_bf16 v[48:51], v[16:19], a[116:119], v[48:51]\n\t" \
    "ds_read_b64_tr_b16 v[40:41], v15 offset:32768\n\tds_read_b64_tr_b16 v[42:43], v15 offset:33280\n\tv_mfma_f32_16x16x32_bf16 v[48:51], v[20:23], a[44:47], v[48:51]\n\tbuffer_load_dwordx4 v[128:131], v8, s[8:11], s68 offen offset:2048 sc1\n\tv_mfma_f32_16x16x32_bf16 v[56:59], v[20:23], a[48:51], v[56:59]\n\tv_mfma_f32_16x16x32_bf16 v[56:59], v[16:19], a[120:123], v[56:59]\n\tds_read_b64_tr_b16 v[44:45], v15 offset:36864\n\tds_read_b64_tr_b16 v[46:47], v15 offset:37376\n\t" \
    "v_mfma_f32_16x16x32_bf16 v[64:67], v[16:19], a[124:127], v[64:67]\n\tbuffer_load_dword v132, v236, s[8:11], s68 offen offset:3072 sc1\n\ts_lshl_b32 s65, s41, 2\n\ts_add_u32 s65, s65, s38\n\tv_mfma_f32_16x16x32_bf16 v[64:67], v[20:23], a[52:55], v[64:67]\n\ts_lshr_b32 s65, s65, 1\n\ts_add_u32 s65, s65, 5\n\ts_waitcnt lgkmcnt(8)\n\t" \
    "v_mfma_f32_16x16x32_bf16 v[52:55], v[24:27], a[116:119], v[52:55]\n\tds_read_b64_tr_b16 v[16:17], v14 offset:40960\n\tds_read_b64_tr_b16 v[18:19], v14 offset:41472\n\ts_cmp_ge_u32 s65, 18\n\ts_cselect_b32 s66, 1120, 0\n\tv_mfma_f32_16x16x32_bf16 v[52:55], v[28:31], a[44:47], v[52:55]\n\ts_lshl_b32 s65, s65, 6\n\ts_sub_u32 s65, s65, s66\n\t" \
    "s_lshl_b32 s66, s58, 4\n\tv_mfma_f32_16x16x32_bf16 v[60:63], v[28:31], a[48:51], v[60:63]\n\ts_add_u32 s65, s65, s66\n\ts_lshl_b32 s65, s65, 12\n\tv_bfe_u32 v252, v240, 1, 4\n\tv_mfma_f32_16x16x32_bf16 v[60:63], v[24:27], a[120:123], v[60:63]\n\tds_read_b64_tr_b16 v[20:21], v14 offset:45056\n\tds_read_b64_tr_b16 v[22:23], v14 offset:45568\n\t" \
    "v_and_b32 v251, 1, v240\n\tv_mfma_f32_16x16x32_bf16 v[68:71], v[24:27], a[124:127], v[68:71]\n\tv_lshlrev_b32 v252, 12, v252\n\tv_lshl_add_u32 v252, v251, 7, v252\n\tv_mfma_f32_16x16x32_bf16 v[68:71], v[28:31], a[52:55], v[68:71]\n\ts_waitcnt lgkmcnt(8)\n\tv_mfma_f32_16x16x32_bf16 v[48:51], v[32:35], a[56:59], v[48:51]\n\tds_read_b64_tr_b16 v[24:25], v15 offset:40960\n\t" \
    "ds_read_b64_tr_b16 v[26:27], v15 offset:41472\n\tv_mfma_f32_16x16x32_bf16 v[48:51], v[36:39], a[128:131], v[48:51]\n\tbuffer_load_dword v253, v252, s[16:19], s65 offen\n\tv_mfma_f32_16x16x32_bf16 v[56:59], v[36:39], a[132:135], v[56:59]\n\tv_mfma_f32_16x16x32_bf16 v[56:59], v[32:35], a[60:63], v[56:59]\n\tds_read_b64_tr_b16 v[28:29], v15 offset:45056\n\tds_read_b64_tr_b16 v[30:31], v15 offset:45568\n\tv_mfma_f32_16x16x32_bf16 v[64:67], v[32:35], a[64:67], v[64:67]\n\t" \
    "v_mfma_f32_16x16x32_bf16 v[64:67], v[36:39], a[136:139], v[64:67]\n\ts_waitcnt lgkmcnt(8)\n\tv_mfma_f32_16x16x32_bf16 v[52:55], v[40:43], a[56:59], v[52:55]\n\tds_read_b64_tr_b16 v[32:33], v14 offset:49152\n\tds_read_b64_tr_b16 v[34:35], v14 offset:49664\n\tv_mfma_f32_16x16x32_bf16 v[52:55], v[44:47], a[128:131], v[52:55]\n\tv_mfma_f32_16x16x32_bf16 v[60:63], v[44:47], a[132:135], v[60:63]\n\tv_mfma_f32_16x16x32_bf16 v[60:63], v[40:43], a[60:63], v[60:63]\n\t" \
    "ds_read_b64_tr_b16 v[36:37], v14 offset:53248\n\tds_read_b64_tr_b16 v[38:39], v14 offset:53760\n\tv_mfma_f32_16x16x32_bf16 v[68:71], v[40:43], a[64:67], v[68:71]\n\tv_mfma_f32_16x16x32_bf16 v[68:71], v[44:47], a[136:139], v[68:71]\n\ts_waitcnt lgkmcnt(8)\n\tv_mfma_f32_16x16x32_bf16 v[48:51], v[16:19], v[168:171], v[48:51]\n\tds_read_b64_tr_b16 v[40:41], v15 offset:49152\n\tds_read_b64_tr_b16 v[42:43], v15 offset:49664\n\t" \
    "v_mfma_f32_16x16x32_bf16 v[48:51], v[20:23], a[140:143], v[48:51]\n\tv_mfma_f32_16x16x32_bf16 v[56:59], v[20:23], a[144:147], v[56:59]\n\tv_mfma_f32_16x16x32_bf16 v[56:59], v[16:19], v[172:175], v[56:59]\n\tds_read_b64_tr_b16 v[44:45], v15 offset:53248\n\tds_read_b64_tr_b16 v[46:47], v15 offset:53760\n\tv_mfma_f32_16x16x32_bf16 v[64:67], v[16:19], v[176:179], v[64:67]\n\tv_mfma_f32_16x16x32_bf16 v[64:67], v[20:23], a[148:151], v[64:67]\n\ts_waitcnt lgkmcnt(8)\n\t" \
    "v_mfma_f32_16x16x32_bf16 v[52:55], v[24:27], v[168:171], v[52:55]\n\tds_read_b64_tr_b16 v[16:17], v14 offset:57344\n\tds_read_b64_tr_b16 v[18:19], v14 offset:57856\n\tv_mfma_f32_16x16x32_bf16 v[52:55], v[28:31], a[140:143], v[52:55]\n\tv_mfma_f32_16x16x32_bf16 v[60:63], v[28:31], a[144:147], v[60:63]\n\tv_mfma_f32_16x16x32_bf16 v[60:63], v[24:27], v[172:175], v[60:63]\n\tds_read_b64_tr_b16 v[20:21], v14 offset:61440\n\tds_read_b64_tr_b16 v[22:23], v14 offset:61952\n\t" \
    "v_mfma_f32_16x16x32_bf16 v[68:71], v[24:27], v[176:179], v[68:71]\n\tv_mfma_f32_16x16x32_bf16 v[68:71], v[28:31], a[148:151], v[68:71]\n\ts_waitcnt lgkmcnt(8)\n\tv_mfma_f32_16x16x32_bf16 v[48:51], v[32:35], a[152:155], v[48:51]\n\tds_read_b64_tr_b16 v[24:25], v15 offset:57344\n\tds_read_b64_tr_b16 v[26:27], v15 offset:57856\n\tv_mfma_f32_16x16x32_bf16 v[48:51], v[36:39], a[164:167], v[48:51]\n\tv_mfma_f32_16x16x32_bf16 v[56:59], v[36:39], a[168:171], v[56:59]\n\t" \
    "v_mfma_f32_16x16x32_bf16 v[56:59], v[32:35], a[156:159], v[56:59]\n\tds_read_b64_tr_b16 v[28:29], v15 offset:61440\n\tds_read_b64_tr_b16 v[30:31], v15 offset:61952\n\tv_mfma_f32_16x16x32_bf16 v[64:67], v[32:35], a[160:163], v[64:67]\n\tv_mfma_f32_16x16x32_bf16 v[64:67], v[36:39], a[172:175], v[64:67]\n\ts_waitcnt lgkmcnt(8)\n\tv_mfma_f32_16x16x32_bf16 v[52:55], v[40:43], a[152:155], v[52:55]\n\tds_read_b64_tr_b16 v[32:33], v2 offset:8192\n\t" \
    "ds_read_b64_tr_b16 v[34:35], v2 offset:8704\n\tv_mfma_f32_16x16x32_bf16 v[52:55], v[44:47], a[164:167], v[52:55]\n\tv_mfma_f32_16x16x32_bf16 v[60:63], v[44:47], a[168:171], v[60:63]\n\tv_mfma_f32_16x16x32_bf16 v[60:63], v[40:43], a[156:159], v[60:63]\n\tds_read_b64_tr_b16 v[36:37], v2 offset:12288\n\tds_read_b64_tr_b16 v[38:39], v2 offset:12800\n\tv_mfma_f32_16x16x32_bf16 v[68:71], v[40:43], a[160:163], v[68:71]\n\tv_mfma_f32_16x16x32_bf16 v[68:71], v[44:47], a[172:175], v[68:71]\n\t" \
    "s_barrier\n\ts_waitcnt lgkmcnt(8)\n\tv_mfma_f32_16x16x32_bf16 v[48:51], v[16:19], a[176:179], v[48:51]\n\tds_read_b64_tr_b16 v[40:41], v3 offset:8192\n\tds_read_b64_tr_b16 v[42:43], v3 offset:8704\n\tv_mfma_f32_16x16x32_bf16 v[48:51], v[20:23], a[188:191], v[48:51]\n\tv_mfma_f32_16x16x32_bf16 v[56:59], v[20:23], a[192:195], v[56:59]\n\tv_mfma_f32_16x16x32_bf16 v[56:59], v[16:19], a[180:183], v[56:59]\n\t" \
    "ds_read_b64_tr_b16 v[44:45], v3 offset:12288\n\tds_read_b64_tr_b16 v[46:47], v3 offset:12800\n\tv_mfma_f32_16x16x32_bf16 v[64:67], v[16:19], a[184:187], v[64:67]\n\tv_mfma_f32_16x16x32_bf16 v[64:67], v[20:23], a[196:199], v[64:67]\n\ts_waitcnt lgkmcnt(8)\n\tv_mfma_f32_16x16x32_bf16 v[52:55], v[24:27], a[176:179], v[52:55]\n\tds_read_b64_tr_b16 v[16:17], v2 offset:16384\n\tds_read_b64_tr_b16 v[18:19], v2 offset:16896\n\t" \
    "v_mfma_f32_16x16x32_bf16 v[52:55], v[28:31], a[188:191], v[52:55]\n\tv_mfma_f32_16x16x32_bf16 v[60:63], v[28:31], a[192:195], v[60:63]\n\tv_mfma_f32_16x16x32_bf16 v[60:63], v[24:27], a[180:183], v[60:63]\n\tds_read_b64_tr_b16 v[20:21], v2 offset:20480\n\tds_read_b64_tr_b16 v[22:23], v2 offset:20992\n\tv_mfma_f32_16x16x32_bf16 v[68:71], v[24:27], a[184:187], v[68:71]\n\tv_mfma_f32_16x16x32_bf16 v[68:71], v[28:31], a[196:199], v[68:71]\n\ts_mov_b32 s71, 1048576\n\t" \
    "s_waitcnt vmcnt(1)\n\t107:\n\tv_add3_u32 v134, s36, v120, v121\n\tv_add3_u32 v135, v122, v123, v124\n\tv_add3_u32 v134, v134, v125, v126\n\tv_add3_u32 v135, v135, v127, v128\n\tv_add3_u32 v134, v134, v129, v130\n\tv_add3_u32 v134, v134, v135, v131\n\t" \
    "v_cmp_ne_u32 vcc, v134, v132\n\ts_cbranch_vccz 108f\n\ts_sub_u32 s71, s71, 1\n\ts_cbranch_scc1 108f\n\ts_sleep 6\n\ts_add_u32 s73, s72, 0\n\tbuffer_load_dwordx4 v[120:123], v8, s[8:11], s73 offen offset:0 sc1\n\tbuffer_load_dwordx4 v[124:127], v8, s[8:11], s73 offen offset:1024 sc1\n\t" \
    "buffer_load_dwordx4 v[128:131], v8, s[8:11], s73 offen offset:2048 sc1\n\tbuffer_load_dword v132, v236, s[8:11], s73 offen offset:3072 sc1\n\ts_waitcnt vmcnt(0)\n\ts_branch 107b\n\t108:\n\tv_lshlrev_b32 v96, 16, v120\n\tv_and_b32 v97, 0xffff0000, v120\n\tv_lshlrev_b32 v98, 16, v121\n\t" \
    "v_and_b32 v99, 0xffff0000, v121\n\tv_lshlrev_b32 v100, 16, v122\n\tv_and_b32 v101, 0xffff0000, v122\n\tv_lshlrev_b32 v102, 16, v123\n\tv_and_b32 v103, 0xffff0000, v123\n\tv_pk_add_f32 v[48:49], v[48:49], v[96:97]\n\tv_pk_add_f32 v[50:51], v[50:51], v[98:99]\n\tv_pk_add_f32 v[52:53], v[52:53], v[100:101]\n\t" \
    "v_pk_add_f32 v[54:55], v[54:55], v[102:103]\n\tv_pk_mul_f32 v[96:97], v[48:49], v[48:49]\n\tv_pk_mul_f32 v[98:99], v[50:51], v[50:51]\n\tv_pk_mul_f32 v[100:101], v[52:53], v[52:53]\n\tv_pk_mul_f32 v[102:103], v[54:55], v[54:55]\n\tv_pk_fma_f32 v[96:97], v[96:97], v[246:247], v[246:247] op_sel:[0,0,1] op_sel_hi:[1,0,1]\n\tv_pk_fma_f32 v[98:99], v[98:99], v[246:247], v[246:247] op_sel:[0,0,1] op_sel_hi:[1,0,1]\n\tv_pk_fma_f32 v[100:101], v[100:101], v[246:247], v[246:247] op_sel:[0,0,1] op_sel_hi:[1,0,1]\n\t" \
    "v_pk_fma_f32 v[102:103], v[102:103], v[246:247], v[246:247] op_sel:[0,0,1] op_sel_hi:[1,0,1]\n\tv_pk_mul_f32 v[96:97], v[96:97], v[48:49]\n\tv_pk_mul_f32 v[98:99], v[98:99], v[50:51]\n\tv_pk_mul_f32 v[100:101], v[100:101], v[52:53]\n\tv_pk_mul_f32 v[102:103], v[102:103], v[54:55]\n\tv_lshlrev_b32 v104, 16, v124\n\tv_exp_f32 v96, v96\n\tv_and_b32 v105, 0xffff0000, v124\n\t" \
    "v_exp_f32 v97, v97\n\tv_lshlrev_b32 v106, 16, v125\n\tv_exp_f32 v98, v98\n\tv_and_b32 v107, 0xffff0000, v125\n\tv_exp_f32 v99, v99\n\tv_lshlrev_b32 v108, 16, v126\n\tv_exp_f32 v100, v100\n\tv_and_b32 v109, 0xffff0000, v126\n\t" \
    "v_exp_f32 v101, v101\n\tv_lshlrev_b32 v110, 16, v127\n\tv_exp_f32 v102, v102\n\tv_and_b32 v111, 0xffff0000, v127\n\tv_exp_f32 v103, v103\n\tv_pk_add_f32 v[56:57], v[56:57], v[104:105]\n\tv_pk_add_f32 v[96:97], v[96:97], 1.0 op_sel_hi:[1,0]\n\tv_pk_add_f32 v[98:99], v[98:99], 1.0 op_sel_hi:[1,0]\n\t" \
    "v_pk_add_f32 v[100:101], v[100:101], 1.0 op_sel_hi:[1,0]\n\tv_pk_add_f32 v[102:103], v[102:103], 1.0 op_sel_hi:[1,0]\n\tv_pk_add_f32 v[58:59], v[58:59], v[106:107]\n\tv_rcp_f32 v96, v96\n\tv_pk_add_f32 v[60:61], v[60:61], v[108:109]\n\tv_rcp_f32 v97, v97\n\tv_pk_add_f32 v[62:63], v[62:63], v[110:111]\n\tv_rcp_f32 v98, v98\n\t" \
    "v_pk_mul_f32 v[104:105], v[56:57], v[56:57]\n\tv_rcp_f32 v99, v99\n\tv_pk_mul_f32 v[106:107], v[58:59], v[58:59]\n\tv_rcp_f32 v100, v100\n\tv_pk_mul_f32 v[108:109], v[60:61], v[60:61]\n\tv_rcp_f32 v101, v101\n\tv_pk_mul_f32 v[110:111], v[62:63], v[62:63]\n\tv_rcp_f32 v102, v102\n\t" \
    "v_pk_fma_f32 v[104:105], v[104:105], v[246:247], v[246:247] op_sel:[0,0,1] op_sel_hi:[1,0,1]\n\tv_rcp_f32 v103, v103\n\tv_pk_fma_f32 v[106:107], v[106:107], v[246:247], v[246:247] op_sel:[0,0,1] op_sel_hi:[1,0,1]\n\tv_pk_mul_f32 v[96:97], v[96:97], v[48:49]\n\tv_pk_mul_f32 v[98:99], v[98:99], v[50:51]\n\tv_pk_mul_f32 v[100:101], v[100:101], v[52:53]\n\tv_pk_mul_f32 v[102:103], v[102:103], v[54:55]\n\tv_cvt_pk_bf16_f32 v192, v96, v97\n\t" \
    "v_cvt_pk_bf16_f32 v193, v98, v99\n\tv_cvt_pk_bf16_f32 v194, v100, v101\n\tv_cvt_pk_bf16_f32 v195, v102, v103\n\tbuffer_store_dwordx4 v[192:195], v8, s[8:11], s45 offen offset:0 sc1\n\tv_pk_fma_f32 v[108:109], v[108:109], v[246:247], v[246:247] op_sel:[0,0,1] op_sel_hi:[1,0,1]\n\tv_pk_fma_f32 v[110:111], v[110:111], v[246:247], v[246:247] op_sel:[0,0,1] op_sel_hi:[1,0,1]\n\tv_pk_mul_f32 v[104:105], v[104:105], v[56:57]\n\tv_pk_mul_f32 v[106:107], v[106:107], v[58:59]\n\t" \
    "v_pk_mul_f32 v[108:109], v[108:109], v[60:61]\n\tv_pk_mul_f32 v[110:111], v[110:111], v[62:63]\n\tv_lshlrev_b32 v112, 16, v128\n\tv_exp_f32 v104, v104\n\tv_and_b32 v113, 0xffff0000, v128\n\tv_exp_f32 v105, v105\n\tv_lshlrev_b32 v114, 16, v129\n\tv_exp_f32 v106, v106\n\t" \
    "v_and_b32 v115, 0xffff0000, v129\n\tv_exp_f32 v107, v107\n\tv_lshlrev_b32 v116, 16, v130\n\tv_exp_f32 v108, v108\n\tv_and_b32 v117, 0xffff0000, v130\n\tv_exp_f32 v109, v109\n\tv_lshlrev_b32 v118, 16, v131\n\tv_exp_f32 v110, v110\n\t" \
    "v_and_b32 v119, 0xffff0000, v131\n\tv_exp_f32 v111, v111\n\tv_pk_add_f32 v[64:65], v[64:65], v[112:113]\n\tv_pk_add_f32 v[104:105], v[104:105], 1.0 op_sel_hi:[1,0]\n\tv_pk_add_f32 v[106:107], v[106:107], 1.0 op_sel_hi:[1,0]\n\tv_pk_add_f32 v[108:109], v[108:109], 1.0 op_sel_hi:[1,0]\n\tv_pk_add_f32 v[110:111], v[110:111], 1.0 op_sel_hi:[1,0]\n\tv_pk_add_f32 v[66:67], v[66:67], v[114:115]\n\t" \
    "v_rcp_f32 v104, v104\n\tv_pk_add_f32 v[68:69], v[68:69], v[116:117]\n\tv_rcp_f32 v105, v105\n\tv_pk_add_f32 v[70:71], v[70:71], v[118:119]\n\tv_rcp_f32 v106, v106\n\tv_pk_mul_f32 v[112:113], v[64:65], v[64:65]\n\tv_rcp_f32 v107, v107\n\tv_pk_mul_f32 v[114:115], v[66:67], v[66:67]\n\t" \
    "v_rcp_f32 v108, v108\n\tv_pk_mul_f32 v[116:117], v[68:69], v[68:69]\n\tv_rcp_f32 v109, v109\n\tv_pk_mul_f32 v[118:119], v[70:71], v[70:71]\n\tv_rcp_f32 v110, v110\n\tv_pk_fma_f32 v[112:113], v[112:113], v[246:247], v[246:247] op_sel:[0,0,1] op_sel_hi:[1,0,1]\n\tv_rcp_f32 v111, v111\n\tv_pk_fma_f32 v[114:115], v[114:115], v[246:247], v[246:247] op_sel:[0,0,1] op_sel_hi:[1,0,1]\n\t" \
    "v_pk_mul_f32 v[104:105], v[104:105], v[56:57]\n\tv_pk_mul_f32 v[106:107], v[106:107], v[58:59]\n\tv_pk_mul_f32 v[108:109], v[108:109], v[60:61]\n\tv_pk_mul_f32 v[110:111], v[110:111], v[62:63]\n\tv_cvt_pk_bf16_f32 v196, v104, v105\n\tv_cvt_pk_bf16_f32 v197, v106, v107\n\tv_cvt_pk_bf16_f32 v198, v108, v109\n\tv_cvt_pk_bf16_f32 v199, v110, v111\n\t" \
    "buffer_store_dwordx4 v[196:199], v8, s[8:11], s45 offen offset:1024 sc1\n\tv_pk_fma_f32 v[116:117], v[116:117], v[246:247], v[246:247] op_sel:[0,0,1] op_sel_hi:[1,0,1]\n\tv_pk_fma_f32 v[118:119], v[118:119], v[246:247], v[246:247] op_sel:[0,0,1] op_sel_hi:[1,0,1]\n\tv_pk_mul_f32 v[112:113], v[112:113], v[64:65]\n\tv_pk_mul_f32 v[114:115], v[114:115], v[66:67]\n\tv_pk_mul_f32 v[116:117], v[116:117], v[68:69]\n\tv_pk_mul_f32 v[118:119], v[118:119], v[70:71]\n\tv_exp_f32 v112, v112\n\t" \
    "v_exp_f32 v113, v113\n\tv_exp_f32 v114, v114\n\tv_exp_f32 v115, v115\n\tv_exp_f32 v116, v116\n\tv_exp_f32 v117, v117\n\tv_exp_f32 v118, v118\n\tv_exp_f32 v119, v119\n\tv_pk_add_f32 v[112:113], v[112:113], 1.0 op_sel_hi:[1,0]\n\t" \
    "v_pk_add_f32 v[114:115], v[114:115], 1.0 op_sel_hi:[1,0]\n\tv_pk_add_f32 v[116:117], v[116:117], 1.0 op_sel_hi:[1,0]\n\tv_pk_add_f32 v[118:119], v[118:119], 1.0 op_sel_hi:[1,0]\n\tv_rcp_f32 v112, v112\n\tv_rcp_f32 v113, v113\n\tv_rcp_f32 v114, v114\n\tv_rcp_f32 v115, v115\n\tv_rcp_f32 v116, v116\n\t" \
    "v_rcp_f32 v117, v117\n\tv_rcp_f32 v118, v118\n\tv_rcp_f32 v119, v119\n\tv_pk_mul_f32 v[112:113], v[112:113], v[64:65]\n\tv_pk_mul_f32 v[114:115], v[114:115], v[66:67]\n\tv_pk_mul_f32 v[116:117], v[116:117], v[68:69]\n\tv_pk_mul_f32 v[118:119], v[118:119], v[70:71]\n\tv_cvt_pk_bf16_f32 v200, v112, v113\n\t" \
    "v_cvt_pk_bf16_f32 v201, v114, v115\n\tv_cvt_pk_bf16_f32 v202, v116, v117\n\tv_cvt_pk_bf16_f32 v203, v118, v119\n\tbuffer_store_dwordx4 v[200:203], v8, s[8:11], s45 offen offset:2048 sc1\n\ts_waitcnt lgkmcnt(8)\n\tv_mfma_f32_16x16x32_bf16 v[144:147], v[32:35], a[116:119], v[144:147]\n\tds_read_b64_tr_b16 v[24:25], v3 offset:16384\n\tds_read_b64_tr_b16 v[26:27], v3 offset:16896\n\t" \
    "v_mfma_f32_16x16x32_bf16 v[144:147], v[36:39], a[44:47], v[144:147]\n\tbuffer_store_dwordx2 v[248:249], off, s[8:11], s60 sc1\n\tv_mfma_f32_16x16x32_bf16 v[152:155], v[36:39], a[48:51], v[152:155]\n\tv_mfma_f32_16x16x32_bf16 v[152:155], v[32:35], a[120:123], v[152:155]\n\tds_read_b64_tr_b16 v[28:29], v3 offset:20480\n\tds_read_b64_tr_b16 v[30:31], v3 offset:20992\n\tv_mfma_f32_16x16x32_bf16 v[160:163], v[32:35], a[124:127], v[160:163]\n\tv_mfma_f32_16x16x32_bf16 v[160:163], v[36:39], a[52:55], v[160:163]\n\t" \
    "s_waitcnt lgkmcnt(8)\n\tv_mfma_f32_16x16x32_bf16 v[148:151], v[40:43], a[116:119], v[148:151]\n\tds_read_b64_tr_b16 v[32:33], v2 offset:24576\n\tds_read_b64_tr_b16 v[34:35], v2 offset:25088\n\tv_mfma_f32_16x16x32_bf16 v[148:151], v[44:47], a[44:47], v[148:151]\n\tv_mfma_f32_16x16x32_bf16 v[156:159], v[44:47], a[48:51], v[156:159]\n\tv_mfma_f32_16x16x32_bf16 v[156:159], v[40:43], a[120:123], v[156:159]\n\tds_read_b64_tr_b16 v[36:37], v2 offset:28672\n\t" \
    "ds_read_b64_tr_b16 v[38:39], v2 offset:29184\n\tv_mfma_f32_16x16x32_bf16 v[164:167], v[40:43], a[124:127], v[164:167]\n\tv_mfma_f32_16x16x32_bf16 v[164:167], v[44:47], a[52:55], v[164:167]\n\ts_waitcnt lgkmcnt(8)\n\tv_mfma_f32_16x16x32_bf16 v[144:147], v[16:19], a[56:59], v[144:147]\n\tds_read_b64_tr_b16 v[40:41], v3 offset:24576\n\tds_read_b64_tr_b16 v[42:43], v3 offset:25088\n\tv_mfma_f32_16x16x32_bf16 v[144:147], v[20:23], a[128:131], v[144:147]\n\t" \
    "v_mfma_f32_16x16x32_bf16 v[152:155], v[20:23], a[132:135], v[152:155]\n\tv_mfma_f32_16x16x32_bf16 v[152:155], v[16:19], a[60:63], v[152:155]\n\tds_read_b64_tr_b16 v[44:45], v3 offset:28672\n\tds_read_b64_tr_b16 v[46:47], v3 offset:29184\n\tv_mfma_f32_16x16x32_bf16 v[160:163], v[16:19], a[64:67], v[160:163]\n\tv_mfma_f32_16x16x32_bf16 v[160:163], v[20:23], a[136:139], v[160:163]\n\ts_waitcnt lgkmcnt(8)\n\tv_mfma_f32_16x16x32_bf16 v[148:151], v[24:27], a[56:59], v[148:151]\n\t" \
    "ds_read_b64_tr_b16 v[16:17], v2 offset:32768\n\tds_read_b64_tr_b16 v[18:19], v2 offset:33280\n\tv_mfma_f32_16x16x32_bf16 v[148:151], v[28:31], a[128:131], v[148:151]\n\tv_mfma_f32_16x16x32_bf16 v[156:159], v[28:31], a[132:135], v[156:159]\n\tv_mfma_f32_16x16x32_bf16 v[156:159], v[24:27], a[60:63], v[156:159]\n\tds_read_b64_tr_b16 v[20:21], v2 offset:36864\n\tds_read_b64_tr_b16 v[22:23], v2 offset:37376\n\tv_mfma_f32_16x16x32_bf16 v[164:167], v[24:27], a[64:67], v[164:167]\n\t" \
    "v_mfma_f32_16x16x32_bf16 v[164:167], v[28:31], a[136:139], v[164:167]\n\ts_waitcnt lgkmcnt(8)\n\tv_mfma_f32_16x16x32_bf16 v[144:147], v[32:35], v[168:171], v[144:147]\n\tds_read_b64_tr_b16 v[24:25], v3 offset:32768\n\tds_read_b64_tr_b16 v[26:27], v3 offset:33280\n\tv_mfma_f32_16x16x32_bf16 v[144:147], v[36:39], a[140:143], v[144:147]\n\tv_mfma_f32_16x16x32_bf16 v[152:155], v[36:39], a[144:147], v[152:155]\n\tv_mfma_f32_16x16x32_bf16 v[152:155], v[32:35], v[172:175], v[152:155]\n\t" \
    "ds_read_b64_tr_b16 v[28:29], v3 offset:36864\n\tds_read_b64_tr_b16 v[30:31], v3 offset:37376\n\tv_mfma_f32_16x16x32_bf16 v[160:163], v[32:35], v[176:179], v[160:163]\n\tv_mfma_f32_16x16x32_bf16 v[160:163], v[36:39], a[148:151], v[160:163]\n\ts_waitcnt lgkmcnt(8)\n\tv_mfma_f32_16x16x32_bf16 v[148:151], v[40:43], v[168:171], v[148:151]\n\tds_read_b64_tr_b16 v[32:33], v2 offset:40960\n\tds_read_b64_tr_b16 v[34:35], v2 offset:41472\n\t" \
    "v_mfma_f32_16x16x32_bf16 v[148:151], v[44:47], a[140:143], v[148:151]\n\tv_mfma_f32_16x16x32_bf16 v[156:159], v[44:47], a[144:147], v[156:159]\n\tv_mfma_f32_16x16x32_bf16 v[156:159], v[40:43], v[172:175], v[156:159]\n\tds_read_b64_tr_b16 v[36:37], v2 offset:45056\n\tds_read_b64_tr_b16 v[38:39], v2 offset:45568\n\tv_mfma_f32_16x16x32_bf16 v[164:167], v[40:43], v[176:179], v[164:167]\n\tv_mfma_f32_16x16x32_bf16 v[164:167], v[44:47], a[148:151], v[164:167]\n\ts_barrier\n\t" \
    "s_waitcnt lgkmcnt(8)\n\tv_mfma_f32_16x16x32_bf16 v[144:147], v[16:19], a[152:155], v[144:147]\n\tds_read_b64_tr_b16 v[40:41], v3 offset:40960\n\tds_read_b64_tr_b16 v[42:43], v3 offset:41472\n\tv_mfma_f32_16x16x32_bf16 v[144:147], v[20:23], a[164:167], v[144:147]\n\tv_mfma_f32_16x16x32_bf16 v[152:155], v[20:23], a[168:171], v[152:155]\n\tv_mfma_f32_16x16x32_bf16 v[152:155], v[16:19], a[156:159], v[152:155]\n\tds_read_b64_tr_b16 v[44:45], v3 offset:45056\n\t" \
    "ds_read_b64_tr_b16 v[46:47], v3 offset:45568\n\tv_mfma_f32_16x16x32_bf16 v[160:163], v[16:19], a[160:163], v[160:163]\n\tv_mfma_f32_16x16x32_bf16 v[160:163], v[20:23], a[172:175], v[160:163]\n\ts_waitcnt lgkmcnt(8)\n\tv_mfma_f32_16x16x32_bf16 v[148:151], v[24:27], a[152:155], v[148:151]\n\tds_read_b64_tr_b16 v[16:17], v2 offset:49152\n\tds_read_b64_tr_b16 v[18:19], v2 offset:49664\n\tv_mfma_f32_16x16x32_bf16 v[148:151], v[28:31], a[164:167], v[148:151]\n\t" \
    "v_mfma_f32_16x16x32_bf16 v[156:159], v[28:31], a[168:171], v[156:159]\n\tv_mfma_f32_16x16x32_bf16 v[156:159], v[24:27], a[156:159], v[156:159]\n\tds_read_b64_tr_b16 v[20:21], v2 offset:53248\n\tds_read_b64_tr_b16 v[22:23], v2 offset:53760\n\tv_mfma_f32_16x16x32_bf16 v[164:167], v[24:27], a[160:163], v[164:167]\n\tv_mfma_f32_16x16x32_bf16 v[164:167], v[28:31], a[172:175], v[164:167]\n\ts_waitcnt lgkmcnt(8)\n\tv_mfma_f32_16x16x32_bf16 v[144:147], v[32:35], a[176:179], v[144:147]\n\t" \
    "ds_read_b64_tr_b16 v[24:25], v3 offset:49152\n\tds_read_b64_tr_b16 v[26:27], v3 offset:49664\n\tv_mfma_f32_16x16x32_bf16 v[144:147], v[36:39], a[188:191], v[144:147]\n\tv_mfma_f32_16x16x32_bf16 v[152:155], v[36:39], a[192:195], v[152:155]\n\tv_mfma_f32_16x16x32_bf16 v[152:155], v[32:35], a[180:183], v[152:155]\n\tds_read_b64_tr_b16 v[28:29], v3 offset:53248\n\tds_read_b64_tr_b16 v[30:31], v3 offset:53760\n\tv_mfma_f32_16x16x32_bf16 v[160:163], v[32:35], a[184:187], v[160:163]\n\t" \
    "v_mfma_f32_16x16x32_bf16 v[160:163], v[36:39], a[196:199], v[160:163]\n\ts_waitcnt vmcnt(1)\n\tbuffer_store_dwordx2 v[248:249], off, s[8:11], s49 sc1\n\ts_waitcnt lgkmcnt(8)\n\tv_mfma_f32_16x16x32_bf16 v[148:151], v[40:43], a[176:179], v[148:151]\n\tds_read_b64_tr_b16 v[32:33], v2 offset:57344\n\tds_read_b64_tr_b16 v[34:35], v2 offset:57856\n\tv_mfma_f32_16x16x32_bf16 v[148:151], v[44:47], a[188:191], v[148:151]\n\t" \
    "v_mfma_f32_16x16x32_bf16 v[156:159], v[44:47], a[192:195], v[156:159]\n\tv_mfma_f32_16x16x32_bf16 v[156:159], v[40:43], a[180:183], v[156:159]\n\tds_read_b64_tr_b16 v[36:37], v2 offset:61440\n\tds_read_b64_tr_b16 v[38:39], v2 offset:61952\n\tv_mfma_f32_16x16x32_bf16 v[164:167], v[40:43], a[184:187], v[164:167]\n\tv_mfma_f32_16x16x32_bf16 v[164:167], v[44:47], a[196:199], v[164:167]\n\ts_waitcnt lgkmcnt(8)\n\tv_mfma_f32_16x16x32_bf16 v[72:75], v[16:19], a[68:71], v[72:75]\n\t" \
    "ds_read_b64_tr_b16 v[40:41], v3 offset:57344\n\tds_read_b64_tr_b16 v[42:43], v3 offset:57856\n\ts_add_u32 s68, s52, 3328\n\tv_mfma_f32_16x16x32_bf16 v[72:75], v[20:23], a[80:83], v[72:75]\n\tv_cvt_pk_bf16_f32 v144, v144, v145\n\tv_cvt_pk_bf16_f32 v145, v146, v147\n\tv_cvt_pk_bf16_f32 v146, v148, v149\n\tv_mfma_f32_16x16x32_bf16 v[80:83], v[20:23], a[84:87], v[80:83]\n\t" \
    "v_cvt_pk_bf16_f32 v147, v150, v151\n\tv_mfma_f32_16x16x32_bf16 v[80:83], v[16:19], a[72:75], v[80:83]\n\tds_read_b64_tr_b16 v[44:45], v3 offset:61440\n\tds_read_b64_tr_b16 v[46:47], v3 offset:61952\n\tv_mfma_f32_16x16x32_bf16 v[88:91], v[16:19], a[76:79], v[88:91]\n\tbuffer_store_dwordx4 v[144:147], v8, s[8:11], s68 offen offset:0 sc1\n\tv_cvt_pk_bf16_f32 v148, v152, v153\n\tv_cvt_pk_bf16_f32 v149, v154, v155\n\t" \
    "v_mfma_f32_16x16x32_bf16 v[88:91], v[20:23], a[88:91], v[88:91]\n\tv_cvt_pk_bf16_f32 v150, v156, v157\n\tv_cvt_pk_bf16_f32 v151, v158, v159\n\ts_waitcnt lgkmcnt(8)\n\tv_mfma_f32_16x16x32_bf16 v[76:79], v[24:27], a[68:71], v[76:79]\n\tds_read_b64_tr_b16 v[16:17], v237 offset:0\n\tds_read_b64_tr_b16 v[18:19], v237 offset:512\n\tv_mfma_f32_16x16x32_bf16 v[76:79], v[28:31], a[80:83], v[76:79]\n\t" \
    "buffer_store_dwordx4 v[148:151], v8, s[8:11], s68 offen offset:1024 sc1\n\tv_cvt_pk_bf16_f32 v152, v160, v161\n\tv_cvt_pk_bf16_f32 v153, v162, v163\n\tv_mfma_f32_16x16x32_bf16 v[84:87], v[28:31], a[84:87], v[84:87]\n\tv_cvt_pk_bf16_f32 v154, v164, v165\n\tv_cvt_pk_bf16_f32 v155, v166, v167\n\tv_mfma_f32_16x16x32_bf16 v[84:87], v[24:27], a[72:75], v[84:87]\n\tds_read_b64_tr_b16 v[20:21], v237 offset:4096\n\t" \
    "ds_read_b64_tr_b16 v[22:23], v237 offset:4608\n\tv_mfma_f32_16x16x32_bf16 v[92:95], v[24:27], a[76:79], v[92:95]\n\tbuffer_store_dwordx4 v[152:155], v8, s[8:11], s68 offen offset:2048 sc1\n\tv_add3_u32 v156, s36, v144, v145\n\tv_add3_u32 v157, v146, v147, v148\n\tv_mfma_f32_16x16x32_bf16 v[92:95], v[28:31], a[88:91], v[92:95]\n\tv_add3_u32 v156, v156, v149, v150\n\tv_add3_u32 v157, v157, v151, v152\n\t" \
    "s_waitcnt lgkmcnt(8)\n\tv_mfma_f32_16x16x32_bf16 v[72:75], v[32:35], v[180:183], v[72:75]\n\tds_read_b64_tr_b16 v[24:25], v238 offset:0\n\tds_read_b64_tr_b16 v[26:27], v238 offset:512\n\tv_add3_u32 v156, v156, v153, v154\n\tv_mfma_f32_16x16x32_bf16 v[72:75], v[36:39], v[204:207], v[72:75]\n\tv_add3_u32 v156, v156, v157, v155\n\tv_mfma_f32_16x16x32_bf16 v[80:83], v[36:39], v[208:211], v[80:83]\n\t" \
    "v_mfma_f32_16x16x32_bf16 v[80:83], v[32:35], v[184:187], v[80:83]\n\tds_read_b64_tr_b16 v[28:29], v238 offset:4096\n\tds_read_b64_tr_b16 v[30:31], v238 offset:4608\n\tv_mfma_f32_16x16x32_bf16 v[88:91], v[32:35], v[188:191], v[88:91]\n\tbuffer_store_dword v156, v236, s[8:11], s68 offen offset:3072 sc1\n\ts_cmp_le_u32 s4, 0\n\ts_cselect_b32 s69, 262144, 0\n\ts_add_u32 s69, s69, 0\n\t" \
    "v_mfma_f32_16x16x32_bf16 v[88:91], v[36:39], v[212:215], v[88:91]\n\ts_add_u32 m0, s54, 147456\n\ts_waitcnt lgkmcnt(8)\n\tv_mfma_f32_16x16x32_bf16 v[76:79], v[40:43], v[180:183], v[76:79]\n\tds_read_b64_tr_b16 v[32:33], v237 offset:8192\n\tds_read_b64_tr_b16 v[34:35], v237 offset:8704\n\tv_mfma_f32_16x16x32_bf16 v[76:79], v[44:47], v[204:207], v[76:79]\n\tbuffer_load_dwordx4 v12, s[16:19], s69 offen lds\n\t" \
    "s_cmp_le_u32 s4, 1\n\ts_cselect_b32 s69, 262144, 0\n\ts_add_u32 s69, s69, 262144\n\tv_mfma_f32_16x16x32_bf16 v[84:87], v[44:47], v[208:211], v[84:87]\n\ts_add_u32 m0, s54, 151552\n\tv_mfma_f32_16x16x32_bf16 v[84:87], v[40:43], v[184:187], v[84:87]\n\tds_read_b64_tr_b16 v[36:37], v237 offset:12288\n\tds_read_b64_tr_b16 v[38:39], v237 offset:12800\n\t" \
    "v_mfma_f32_16x16x32_bf16 v[92:95], v[40:43], v[188:191], v[92:95]\n\tbuffer_load_dwordx4 v12, s[16:19], s69 offen lds\n\ts_cmp_le_u32 s4, 0\n\ts_cselect_b32 s69, 262144, 0\n\ts_add_u32 s69, s69, 0\n\tv_mfma_f32_16x16x32_bf16 v[92:95], v[44:47], v[212:215], v[92:95]\n\ts_add_u32 m0, s54, 155648\n\ts_barrier\n\t" \
    "s_waitcnt lgkmcnt(8)\n\tv_mfma_f32_16x16x32_bf16 v[72:75], v[16:19], a[92:95], v[72:75]\n\tds_read_b64_tr_b16 v[40:41], v238 offset:8192\n\tds_read_b64_tr_b16 v[42:43], v238 offset:8704\n\tv_mfma_f32_16x16x32_bf16 v[72:75], v[20:23], a[104:107], v[72:75]\n\tbuffer_load_dwordx4 v13, s[16:19], s69 offen lds\n\ts_cmp_le_u32 s4, 1\n\ts_cselect_b32 s69, 262144, 0\n\t" \
    "s_add_u32 s69, s69, 262144\n\tv_mfma_f32_16x16x32_bf16 v[80:83], v[20:23], a[108:111], v[80:83]\n\ts_add_u32 m0, s54, 159744\n\tv_mfma_f32_16x16x32_bf16 v[80:83], v[16:19], a[96:99], v[80:83]\n\tds_read_b64_tr_b16 v[44:45], v238 offset:12288\n\tds_read_b64_tr_b16 v[46:47], v238 offset:12800\n\tv_mfma_f32_16x16x32_bf16 v[88:91], v[16:19], a[100:103], v[88:91]\n\tbuffer_load_dwordx4 v13, s[16:19], s69 offen lds\n\t" \
    "s_cmp_le_u32 s4, 2\n\ts_cselect_b32 s69, 262144, 0\n\ts_add_u32 s69, s69, 524288\n\tv_mfma_f32_16x16x32_bf16 v[88:91], v[20:23], a[112:115], v[88:91]\n\ts_add_u32 m0, s54, 0\n\ts_waitcnt lgkmcnt(8)\n\tv_mfma_f32_16x16x32_bf16 v[76:79], v[24:27], a[92:95], v[76:79]\n\tds_read_b64_tr_b16 v[16:17], v237 offset:16384\n\t" \
    "ds_read_b64_tr_b16 v[18:19], v237 offset:16896\n\tv_mfma_f32_16x16x32_bf16 v[76:79], v[28:31], a[104:107], v[76:79]\n\tbuffer_load_dwordx4 v12, s[16:19], s69 offen lds\n\ts_cmp_le_u32 s4, 3\n\ts_cselect_b32 s69, 262144, 0\n\ts_add_u32 s69, s69, 786432\n\tv_mfma_f32_16x16x32_bf16 v[84:87], v[28:31], a[108:111], v[84:87]\n\ts_add_u32 m0, s54, 4096\n\t" \
    "v_mfma_f32_16x16x32_bf16 v[84:87], v[24:27], a[96:99], v[84:87]\n\tds_read_b64_tr_b16 v[20:21], v237 offset:20480\n\tds_read_b64_tr_b16 v[22:23], v237 offset:20992\n\tv_mfma_f32_16x16x32_bf16 v[92:95], v[24:27], a[100:103], v[92:95]\n\tbuffer_load_dwordx4 v12, s[16:19], s69 offen lds\n\ts_cmp_le_u32 s4, 2\n\ts_cselect_b32 s69, 262144, 0\n\ts_add_u32 s69, s69, 524288\n\t" \
    "v_mfma_f32_16x16x32_bf16 v[92:95], v[28:31], a[112:115], v[92:95]\n\ts_add_u32 m0, s54, 8192\n\ts_waitcnt lgkmcnt(8)\n\tv_mfma_f32_16x16x32_bf16 v[72:75], v[32:35], v[216:219], v[72:75]\n\tds_read_b64_tr_b16 v[24:25], v238 offset:16384\n\tds_read_b64_tr_b16 v[26:27], v238 offset:16896\n\tv_mfma_f32_16x16x32_bf16 v[72:75], v[36:39], a[8:11], v[72:75]\n\tbuffer_load_dwordx4 v13, s[16:19], s69 offen lds\n\t" \
    "s_cmp_le_u32 s4, 3\n\ts_cselect_b32 s69, 262144, 0\n\ts_add_u32 s69, s69, 786432\n\tv_mfma_f32_16x16x32_bf16 v[80:83], v[36:39], a[12:15], v[80:83]\n\ts_add_u32 m0, s54, 12288\n\tv_mfma_f32_16x16x32_bf16 v[80:83], v[32:35], a[0:3], v[80:83]\n\tds_read_b64_tr_b16 v[28:29], v238 offset:20480\n\tds_read_b64_tr_b16 v[30:31], v238 offset:20992\n\t" \
    "v_mfma_f32_16x16x32_bf16 v[88:91], v[32:35], a[4:7], v[88:91]\n\tbuffer_load_dwordx4 v13, s[16:19], s69 offen lds\n\ts_cmp_le_u32 s4, 4\n\ts_cselect_b32 s69, 262144, 0\n\ts_add_u32 s69, s69, 1048576\n\tv_mfma_f32_16x16x32_bf16 v[88:91], v[36:39], a[16:19], v[88:91]\n\ts_add_u32 m0, s54, 16384\n\ts_waitcnt lgkmcnt(8)\n\t" \
    "v_mfma_f32_16x16x32_bf16 v[76:79], v[40:43], v[216:219], v[76:79]\n\tds_read_b64_tr_b16 v[32:33], v237 offset:24576\n\tds_read_b64_tr_b16 v[34:35], v237 offset:25088\n\tv_mfma_f32_16x16x32_bf16 v[76:79], v[44:47], a[8:11], v[76:79]\n\tbuffer_load_dwordx4 v12, s[16:19], s69 offen lds\n\ts_cmp_le_u32 s4, 5\n\ts_cselect_b32 s69, 262144, 0\n\ts_add_u32 s69, s69, 1310720\n\t" \
    "v_mfma_f32_16x16x32_bf16 v[84:87], v[44:47], a[12:15], v[84:87]\n\ts_add_u32 m0, s54, 20480\n\tv_mfma_f32_16x16x32_bf16 v[84:87], v[40:43], a[0:3], v[84:87]\n\tds_read_b64_tr_b16 v[36:37], v237 offset:28672\n\tds_read_b64_tr_b16 v[38:39], v237 offset:29184\n\tv_mfma_f32_16x16x32_bf16 v[92:95], v[40:43], a[4:7], v[92:95]\n\tbuffer_load_dwordx4 v12, s[16:19], s69 offen lds\n\ts_cmp_le_u32 s4, 4\n\t" \
    "s_cselect_b32 s69, 262144, 0\n\ts_add_u32 s69, s69, 1048576\n\tv_mfma_f32_16x16x32_bf16 v[92:95], v[44:47], a[16:19], v[92:95]\n\ts_add_u32 m0, s54, 24576\n\ts_waitcnt lgkmcnt(8)\n\tv_mfma_f32_16x16x32_bf16 v[72:75], v[16:19], a[20:23], v[72:75]\n\tds_read_b64_tr_b16 v[40:41], v238 offset:24576\n\tds_read_b64_tr_b16 v[42:43], v238 offset:25088\n\t" \
    "v_mfma_f32_16x16x32_bf16 v[72:75], v[20:23], a[32:35], v[72:75]\n\tv_mfma_f32_16x16x32_bf16 v[80:83], v[20:23], a[36:39], v[80:83]\n\tv_mfma_f32_16x16x32_bf16 v[80:83], v[16:19], a[24:27], v[80:83]\n\tds_read_b64_tr_b16 v[44:45], v238 offset:28672\n\tds_read_b64_tr_b16 v[46:47], v238 offset:29184\n\tv_mfma_f32_16x16x32_bf16 v[88:91], v[16:19], a[28:31], v[88:91]\n\tbuffer_load_dwordx4 v13, s[16:19], s69 offen lds\n\ts_cmp_le_u32 s4, 5\n\t" \
    "s_cselect_b32 s69, 262144, 0\n\ts_add_u32 s69, s69, 1310720\n\tv_mfma_f32_16x16x32_bf16 v[88:91], v[20:23], a[40:43], v[88:91]\n\ts_add_u32 m0, s54, 28672\n\ts_waitcnt lgkmcnt(8)\n\tv_mfma_f32_16x16x32_bf16 v[76:79], v[24:27], a[20:23], v[76:79]\n\tds_read_b64_tr_b16 v[16:17], v237 offset:32768\n\tds_read_b64_tr_b16 v[18:19], v237 offset:33280\n\t" \
    "v_mfma_f32_16x16x32_bf16 v[76:79], v[28:31], a[32:35], v[76:79]\n\tbuffer_load_dwordx4 v13, s[16:19], s69 offen lds\n\ts_cmp_le_u32 s4, 6\n\ts_cselect_b32 s69, 262144, 0\n\ts_add_u32 s69, s69, 1572864\n\tv_mfma_f32_16x16x32_bf16 v[84:87], v[28:31], a[36:39], v[84:87]\n\ts_add_u32 m0, s54, 32768\n\tv_mfma_f32_16x16x32_bf16 v[84:87], v[24:27], a[24:27], v[84:87]\n\t" \
    "ds_read_b64_tr_b16 v[20:21], v237 offset:36864\n\tds_read_b64_tr_b16 v[22:23], v237 offset:37376\n\tv_mfma_f32_16x16x32_bf16 v[92:95], v[24:27], a[28:31], v[92:95]\n\tv_mfma_f32_16x16x32_bf16 v[92:95], v[28:31], a[40:43], v[92:95]\n\ts_waitcnt lgkmcnt(8)\n\tv_mfma_f32_16x16x32_bf16 v[72:75], v[32:35], a[116:119], v[72:75]\n\tds_read_b64_tr_b16 v[24:25], v238 offset:32768\n\tds_read_b64_tr_b16 v[26:27], v238 offset:33280\n\t" \
    "v_mfma_f32_16x16x32_bf16 v[72:75], v[36:39], a[44:47], v[72:75]\n\tbuffer_load_dwordx4 v12, s[16:19], s69 offen lds\n\ts_cmp_le_u32 s4, 7\n\ts_cselect_b32 s69, 262144, 0\n\ts_add_u32 s69, s69, 1835008\n\tv_mfma_f32_16x16x32_bf16 v[80:83], v[36:39], a[48:51], v[80:83]\n\ts_add_u32 m0, s54, 36864\n\tv_mfma_f32_16x16x32_bf16 v[80:83], v[32:35], a[120:123], v[80:83]\n\t" \
    "ds_read_b64_tr_b16 v[28:29], v238 offset:36864\n\tds_read_b64_tr_b16 v[30:31], v238 offset:37376\n\tv_mfma_f32_16x16x32_bf16 v[88:91], v[32:35], a[124:127], v[88:91]\n\tbuffer_load_dwordx4 v12, s[16:19], s69 offen lds\n\ts_cmp_le_u32 s4, 6\n\ts_cselect_b32 s69, 262144, 0\n\ts_add_u32 s69, s69, 1572864\n\tv_mfma_f32_16x16x32_bf16 v[88:91], v[36:39], a[52:55], v[88:91]\n\t" \
    "s_add_u32 m0, s54, 40960\n\ts_waitcnt lgkmcnt(8)\n\tv_mfma_f32_16x16x32_bf16 v[76:79], v[40:43], a[116:119], v[76:79]\n\tds_read_b64_tr_b16 v[32:33], v237 offset:40960\n\tds_read_b64_tr_b16 v[34:35], v237 offset:41472\n\tv_mfma_f32_16x16x32_bf16 v[76:79], v[44:47], a[44:47], v[76:79]\n\tbuffer_load_dwordx4 v13, s[16:19], s69 offen lds\n\ts_add_u32 s68, s72, 3328\n\t" \
    "v_mfma_f32_16x16x32_bf16 v[84:87], v[44:47], a[48:51], v[84:87]\n\tv_mfma_f32_16x16x32_bf16 v[84:87], v[40:43], a[120:123], v[84:87]\n\tds_read_b64_tr_b16 v[36:37], v237 offset:45056\n\tds_read_b64_tr_b16 v[38:39], v237 offset:45568\n\tv_mfma_f32_16x16x32_bf16 v[92:95], v[40:43], a[124:127], v[92:95]\n\tbuffer_load_dwordx4 v[144:147], v8, s[8:11], s68 offen offset:0 sc1\n\tv_mfma_f32_16x16x32_bf16 v[92:95], v[44:47], a[52:55], v[92:95]\n\ts_barrier\n\t" \
    "s_waitcnt lgkmcnt(8)\n\tv_mfma_f32_16x16x32_bf16 v[72:75], v[16:19], a[56:59], v[72:75]\n\tds_read_b64_tr_b16 v[40:41], v238 offset:40960\n\tds_read_b64_tr_b16 v[42:43], v238 offset:41472\n\tv_mfma_f32_16x16x32_bf16 v[72:75], v[20:23], a[128:131], v[72:75]\n\tbuffer_load_dwordx4 v[148:151], v8, s[8:11], s68 offen offset:1024 sc1\n\tv_mfma_f32_16x16x32_bf16 v[80:83], v[20:23], a[132:135], v[80:83]\n\tv_mfma_f32_16x16x32_bf16 v[80:83], v[16:19], a[60:63], v[80:83]\n\t" \
    "ds_read_b64_tr_b16 v[44:45], v238 offset:45056\n\tds_read_b64_tr_b16 v[46:47], v238 offset:45568\n\tv_mfma_f32_16x16x32_bf16 v[88:91], v[16:19], a[64:67], v[88:91]\n\tbuffer_load_dwordx4 v[152:155], v8, s[8:11], s68 offen offset:2048 sc1\n\tv_mfma_f32_16x16x32_bf16 v[88:91], v[20:23], a[136:139], v[88:91]\n\ts_waitcnt lgkmcnt(8)\n\tv_mfma_f32_16x16x32_bf16 v[76:79], v[24:27], a[56:59], v[76:79]\n\tds_read_b64_tr_b16 v[16:17], v237 offset:49152\n\t" \
    "ds_read_b64_tr_b16 v[18:19], v237 offset:49664\n\tv_mfma_f32_16x16x32_bf16 v[76:79], v[28:31], a[128:131], v[76:79]\n\tv_mfma_f32_16x16x32_bf16 v[84:87], v[28:31], a[132:135], v[84:87]\n\tv_mfma_f32_16x16x32_bf16 v[84:87], v[24:27], a[60:63], v[84:87]\n\tds_read_b64_tr_b16 v[20:21], v237 offset:53248\n\tds_read_b64_tr_b16 v[22:23], v237 offset:53760\n\tv_mfma_f32_16x16x32_bf16 v[92:95], v[24:27], a[64:67], v[92:95]\n\tbuffer_load_dword v156, v236, s[8:11], s68 offen offset:3072 sc1\n\t" \
    "s_cmp_le_u32 s4, 7\n\ts_cselect_b32 s69, 262144, 0\n\ts_add_u32 s69, s69, 1835008\n\tv_mfma_f32_16x16x32_bf16 v[92:95], v[28:31], a[136:139], v[92:95]\n\ts_add_u32 m0, s54, 45056\n\ts_waitcnt lgkmcnt(8)\n\tv_mfma_f32_16x16x32_bf16 v[72:75], v[32:35], v[168:171], v[72:75]\n\tds_read_b64_tr_b16 v[24:25], v238 offset:49152\n\t" \
    "ds_read_b64_tr_b16 v[26:27], v238 offset:49664\n\tv_mfma_f32_16x16x32_bf16 v[72:75], v[36:39], a[140:143], v[72:75]\n\tbuffer_load_dwordx4 v13, s[16:19], s69 offen lds\n\ts_cmp_le_u32 s4, 8\n\ts_cselect_b32 s69, 262144, 0\n\ts_add_u32 s69, s69, 2097152\n\tv_mfma_f32_16x16x32_bf16 v[80:83], v[36:39], a[144:147], v[80:83]\n\ts_add_u32 m0, s54, 49152\n\t" \
    "v_mfma_f32_16x16x32_bf16 v[80:83], v[32:35], v[172:175], v[80:83]\n\tds_read_b64_tr_b16 v[28:29], v238 offset:53248\n\tds_read_b64_tr_b16 v[30:31], v238 offset:53760\n\tv_mfma_f32_16x16x32_bf16 v[88:91], v[32:35], v[176:179], v[88:91]\n\tbuffer_load_dwordx4 v12, s[16:19], s69 offen lds\n\ts_cmp_le_u32 s4, 9\n\ts_cselect_b32 s69, 262144, 0\n\ts_add_u32 s69, s69, 2359296\n\t" \
    "v_mfma_f32_16x16x32_bf16 v[88:91], v[36:39], a[148:151], v[88:91]\n\ts_add_u32 m0, s54, 53248\n\ts_waitcnt lgkmcnt(8)\n\tv_mfma_f32_16x16x32_bf16 v[76:79], v[40:43], v[168:171], v[76:79]\n\tds_read_b64_tr_b16 v[32:33], v237 offset:57344\n\tds_read_b64_tr_b16 v[34:35], v237 offset:57856\n\tv_mfma_f32_16x16x32_bf16 v[76:79], v[44:47], a[140:143], v[76:79]\n\tv_mfma_f32_16x16x32_bf16 v[84:87], v[44:47], a[144:147], v[84:87]\n\t" \
    "v_mfma_f32_16x16x32_bf16 v[84:87], v[40:43], v[172:175], v[84:87]\n\tds_read_b64_tr_b16 v[36:37], v237 offset:61440\n\tds_read_b64_tr_b16 v[38:39], v237 offset:61952\n\tv_mfma_f32_16x16x32_bf16 v[92:95], v[40:43], v[176:179], v[92:95]\n\tbuffer_load_dwordx4 v12, s[16:19], s69 offen lds\n\tv_mfma_f32_16x16x32_bf16 v[92:95], v[44:47], a[148:151], v[92:95]\n\ts_waitcnt lgkmcnt(8)\n\tv_mfma_f32_16x16x32_bf16 v[72:75], v[16:19], a[152:155], v[72:75]\n\t" \
    "ds_read_b64_tr_b16 v[40:41], v238 offset:57344\n\tds_read_b64_tr_b16 v[42:43], v238 offset:57856\n\tv_mfma_f32_16x16x32_bf16 v[72:75], v[20:23], a[164:167], v[72:75]\n\tv_mfma_f32_16x16x32_bf16 v[80:83], v[20:23], a[168:171], v[80:83]\n\tv_mfma_f32_16x16x32_bf16 v[80:83], v[16:19], a[156:159], v[80:83]\n\tds_read_b64_tr_b16 v[44:45], v238 offset:61440\n\tds_read_b64_tr_b16 v[46:47], v238 offset:61952\n\tv_mfma_f32_16x16x32_bf16 v[88:91], v[16:19], a[160:163], v[88:91]\n\t" \
    "v_mfma_f32_16x16x32_bf16 v[88:91], v[20:23], a[172:175], v[88:91]\n\tbuffer_load_dwordx2 v[250:251], v241, s[8:11], s50 offen sc1\n\ts_waitcnt lgkmcnt(8)\n\tv_mfma_f32_16x16x32_bf16 v[76:79], v[24:27], a[152:155], v[76:79]\n\tds_read_b64_tr_b16 v[16:17], v220 offset:0\n\tds_read_b64_tr_b16 v[18:19], v220 offset:2048\n\tv_mfma_f32_16x16x32_bf16 v[76:79], v[28:31], a[164:167], v[76:79]\n\tv_mfma_f32_16x16x32_bf16 v[84:87], v[28:31], a[168:171], v[84:87]\n\t" \
    "v_mfma_f32_16x16x32_bf16 v[84:87], v[24:27], a[156:159], v[84:87]\n\tds_read_b64_tr_b16 v[20:21], v220 offset:8192\n\tds_read_b64_tr_b16 v[22:23], v220 offset:10240\n\tv_mfma_f32_16x16x32_bf16 v[92:95], v[24:27], a[160:163], v[92:95]\n\tv_mfma_f32_16x16x32_bf16 v[92:95], v[28:31], a[172:175], v[92:95]\n\ts_waitcnt lgkmcnt(8)\n\tv_mfma_f32_16x16x32_bf16 v[72:75], v[32:35], a[176:179], v[72:75]\n\tds_read_b64_tr_b16 v[24:25], v221 offset:0\n\t" \
    "ds_read_b64_tr_b16 v[26:27], v221 offset:2048\n\tv_mfma_f32_16x16x32_bf16 v[72:75], v[36:39], a[188:191], v[72:75]\n\tv_mfma_f32_16x16x32_bf16 v[80:83], v[36:39], a[192:195], v[80:83]\n\tv_mfma_f32_16x16x32_bf16 v[80:83], v[32:35], a[180:183], v[80:83]\n\tds_read_b64_tr_b16 v[28:29], v221 offset:8192\n\tds_read_b64_tr_b16 v[30:31], v221 offset:10240\n\tv_mfma_f32_16x16x32_bf16 v[88:91], v[32:35], a[184:187], v[88:91]\n\tv_mfma_f32_16x16x32_bf16 v[88:91], v[36:39], a[196:199], v[88:91]\n\t" \
    "s_waitcnt lgkmcnt(8)\n\tv_mfma_f32_16x16x32_bf16 v[76:79], v[40:43], a[176:179], v[76:79]\n\tds_read_b64_tr_b16 v[32:33], v222 offset:0\n\tds_read_b64_tr_b16 v[34:35], v222 offset:2048\n\tv_mfma_f32_16x16x32_bf16 v[76:79], v[44:47], a[188:191], v[76:79]\n\tv_mfma_f32_16x16x32_bf16 v[84:87], v[44:47], a[192:195], v[84:87]\n\tv_mfma_f32_16x16x32_bf16 v[84:87], v[40:43], a[180:183], v[84:87]\n\tds_read_b64_tr_b16 v[36:37], v222 offset:8192\n\t" \
    "ds_read_b64_tr_b16 v[38:39], v222 offset:10240\n\tv_mfma_f32_16x16x32_bf16 v[92:95], v[40:43], a[184:187], v[92:95]\n\tv_mfma_f32_16x16x32_bf16 v[92:95], v[44:47], a[196:199], v[92:95]\n\ts_mov_b32 s71, 1048576\n\ts_waitcnt vmcnt(4)\n\t109:\n\tv_add3_u32 v158, s36, v144, v145\n\tv_add3_u32 v159, v146, v147, v148\n\t" \
    "v_add3_u32 v158, v158, v149, v150\n\tv_add3_u32 v159, v159, v151, v152\n\tv_add3_u32 v158, v158, v153, v154\n\tv_add3_u32 v158, v158, v159, v155\n\tv_cmp_ne_u32 vcc, v158, v156\n\ts_cbranch_vccz 110f\n\ts_sub_u32 s71, s71, 1\n\ts_cbranch_scc1 110f\n\t" \
    "s_sleep 6\n\ts_add_u32 s73, s72, 3328\n\tbuffer_load_dwordx4 v[144:147], v8, s[8:11], s73 offen offset:0 sc1\n\tbuffer_load_dwordx4 v[148:151], v8, s[8:11], s73 offen offset:1024 sc1\n\tbuffer_load_dwordx4 v[152:155], v8, s[8:11], s73 offen offset:2048 sc1\n\tbuffer_load_dword v156, v236, s[8:11], s73 offen offset:3072 sc1\n\ts_waitcnt vmcnt(0)\n\ts_branch 109b\n\t" \
    "110:\n\ts_add_u32 s45, s45, 55296\n\tv_lshlrev_b32 v96, 16, v144\n\tv_and_b32 v97, 0xffff0000, v144\n\tv_lshlrev_b32 v98, 16, v145\n\tv_and_b32 v99, 0xffff0000, v145\n\tv_lshlrev_b32 v100, 16, v146\n\tv_and_b32 v101, 0xffff0000, v146\n\t" \
    "v_lshlrev_b32 v102, 16, v147\n\tv_and_b32 v103, 0xffff0000, v147\n\tv_pk_add_f32 v[72:73], v[72:73], v[96:97]\n\tv_pk_add_f32 v[74:75], v[74:75], v[98:99]\n\tv_pk_add_f32 v[76:77], v[76:77], v[100:101]\n\tv_pk_add_f32 v[78:79], v[78:79], v[102:103]\n\tv_pk_mul_f32 v[96:97], v[72:73], v[72:73]\n\tv_pk_mul_f32 v[98:99], v[74:75], v[74:75]\n\t" \
    "v_pk_mul_f32 v[100:101], v[76:77], v[76:77]\n\tv_pk_mul_f32 v[102:103], v[78:79], v[78:79]\n\tv_pk_fma_f32 v[96:97], v[96:97], v[246:247], v[246:247] op_sel:[0,0,1] op_sel_hi:[1,0,1]\n\tv_pk_fma_f32 v[98:99], v[98:99], v[246:247], v[246:247] op_sel:[0,0,1] op_sel_hi:[1,0,1]\n\tv_pk_fma_f32 v[100:101], v[100:101], v[246:247], v[246:247] op_sel:[0,0,1] op_sel_hi:[1,0,1]\n\tv_pk_fma_f32 v[102:103], v[102:103], v[246:247], v[246:247] op_sel:[0,0,1] op_sel_hi:[1,0,1]\n\tv_pk_mul_f32 v[96:97], v[96:97], v[72:73]\n\tv_pk_mul_f32 v[98:99], v[98:99], v[74:75]\n\t" \
    "v_pk_mul_f32 v[100:101], v[100:101], v[76:77]\n\tv_pk_mul_f32 v[102:103], v[102:103], v[78:79]\n\tv_lshlrev_b32 v104, 16, v148\n\tv_exp_f32 v96, v96\n\tv_and_b32 v105, 0xffff0000, v148\n\tv_exp_f32 v97, v97\n\tv_lshlrev_b32 v106, 16, v149\n\tv_exp_f32 v98, v98\n\t" \
    "v_and_b32 v107, 0xffff0000, v149\n\tv_exp_f32 v99, v99\n\tv_lshlrev_b32 v108, 16, v150\n\tv_exp_f32 v100, v100\n\tv_and_b32 v109, 0xffff0000, v150\n\tv_exp_f32 v101, v101\n\tv_lshlrev_b32 v110, 16, v151\n\tv_exp_f32 v102, v102\n\t" \
    "v_and_b32 v111, 0xffff0000, v151\n\tv_exp_f32 v103, v103\n\tv_pk_add_f32 v[80:81], v[80:81], v[104:105]\n\tv_pk_add_f32 v[96:97], v[96:97], 1.0 op_sel_hi:[1,0]\n\tv_pk_add_f32 v[98:99], v[98:99], 1.0 op_sel_hi:[1,0]\n\tv_pk_add_f32 v[100:101], v[100:101], 1.0 op_sel_hi:[1,0]\n\tv_pk_add_f32 v[102:103], v[102:103], 1.0 op_sel_hi:[1,0]\n\tv_pk_add_f32 v[82:83], v[82:83], v[106:107]\n\t" \
    "v_rcp_f32 v96, v96\n\tv_pk_add_f32 v[84:85], v[84:85], v[108:109]\n\tv_rcp_f32 v97, v97\n\tv_pk_add_f32 v[86:87], v[86:87], v[110:111]\n\tv_rcp_f32 v98, v98\n\tv_pk_mul_f32 v[104:105], v[80:81], v[80:81]\n\tv_rcp_f32 v99, v99\n\tv_pk_mul_f32 v[106:107], v[82:83], v[82:83]\n\t" \
    "v_rcp_f32 v100, v100\n\tv_pk_mul_f32 v[108:109], v[84:85], v[84:85]\n\tv_rcp_f32 v101, v101\n\tv_pk_mul_f32 v[110:111], v[86:87], v[86:87]\n\tv_rcp_f32 v102, v102\n\tv_pk_fma_f32 v[104:105], v[104:105], v[246:247], v[246:247] op_sel:[0,0,1] op_sel_hi:[1,0,1]\n\tv_rcp_f32 v103, v103\n\tv_pk_fma_f32 v[106:107], v[106:107], v[246:247], v[246:247] op_sel:[0,0,1] op_sel_hi:[1,0,1]\n\t" \
    "v_pk_mul_f32 v[96:97], v[96:97], v[72:73]\n\tv_pk_mul_f32 v[98:99], v[98:99], v[74:75]\n\tv_pk_mul_f32 v[100:101], v[100:101], v[76:77]\n\tv_pk_mul_f32 v[102:103], v[102:103], v[78:79]\n\tv_cvt_pk_bf16_f32 v72, v96, v97\n\tv_cvt_pk_bf16_f32 v73, v98, v99\n\tv_cvt_pk_bf16_f32 v74, v100, v101\n\tv_cvt_pk_bf16_f32 v75, v102, v103\n\t" \
    "s_cmp_lg_u32 s39, 2\n\ts_cbranch_scc1 111f\n\ts_waitcnt vmcnt(0)\n\tv_cmp_ne_u64 vcc, s[36:37], v[250:251]\n\ts_cbranch_vccz 113f\n\ts_mov_b32 s71, 1048576\n\t112:\n\ts_sleep 4\n\t" \
    "buffer_load_dwordx2 v[250:251], v241, s[8:11], s50 offen sc1\n\ts_waitcnt vmcnt(0)\n\tv_cmp_ne_u64 vcc, s[36:37], v[250:251]\n\ts_cbranch_vccz 113f\n\ts_sub_u32 s71, s71, 1\n\ts_cbranch_scc0 112b\n\t113:\n\t111:\n\t" \
    "s_cmp_le_u32 s4, 0\n\ts_cselect_b32 s68, 3072, 0\n\ts_add_u32 s68, s68, s46\n\ts_add_u32 s68, s68, 0\n\tbuffer_load_dwordx4 v[204:207], v8, s[8:11], s68 offen offset:0 sc1\n\tbuffer_load_dwordx4 v[208:211], v8, s[8:11], s68 offen offset:1024 sc1\n\tbuffer_load_dwordx4 v[212:215], v8, s[8:11], s68 offen offset:2048 sc1\n\tv_pk_fma_f32 v[108:109], v[108:109], v[246:247], v[246:247] op_sel:[0,0,1] op_sel_hi:[1,0,1]\n\t" \
    "s_cmp_le_u32 s4, 1\n\ts_cselect_b32 s68, 3072, 0\n\tv_pk_fma_f32 v[110:111], v[110:111], v[246:247], v[246:247] op_sel:[0,0,1] op_sel_hi:[1,0,1]\n\ts_add_u32 s68, s68, s46\n\tv_pk_mul_f32 v[104:105], v[104:105], v[80:81]\n\ts_add_u32 s68, s68, 3072\n\tv_pk_mul_f32 v[106:107], v[106:107], v[82:83]\n\tv_pk_mul_f32 v[108:109], v[108:109], v[84:85]\n\t" \
    "v_pk_mul_f32 v[110:111], v[110:111], v[86:87]\n\tv_lshlrev_b32 v112, 16, v152\n\tv_exp_f32 v104, v104\n\tv_and_b32 v113, 0xffff0000, v152\n\tv_exp_f32 v105, v105\n\tv_lshlrev_b32 v114, 16, v153\n\tv_exp_f32 v106, v106\n\tv_and_b32 v115, 0xffff0000, v153\n\t" \
    "v_exp_f32 v107, v107\n\tv_lshlrev_b32 v116, 16, v154\n\tbuffer_load_dwordx4 v[216:219], v8, s[8:11], s68 offen offset:0 sc1\n\tv_exp_f32 v108, v108\n\tv_and_b32 v117, 0xffff0000, v154\n\tv_exp_f32 v109, v109\n\tv_lshlrev_b32 v118, 16, v155\n\tv_exp_f32 v110, v110\n\t" \
    "v_and_b32 v119, 0xffff0000, v155\n\tv_exp_f32 v111, v111\n\tv_pk_add_f32 v[88:89], v[88:89], v[112:113]\n\tv_pk_add_f32 v[104:105], v[104:105], 1.0 op_sel_hi:[1,0]\n\tv_pk_add_f32 v[106:107], v[106:107], 1.0 op_sel_hi:[1,0]\n\tv_pk_add_f32 v[108:109], v[108:109], 1.0 op_sel_hi:[1,0]\n\tbuffer_load_dwordx4 a[0:3], v8, s[8:11], s68 offen offset:1024 sc1\n\tv_pk_add_f32 v[110:111], v[110:111], 1.0 op_sel_hi:[1,0]\n\t" \
    "v_pk_add_f32 v[90:91], v[90:91], v[114:115]\n\tv_rcp_f32 v104, v104\n\tv_pk_add_f32 v[92:93], v[92:93], v[116:117]\n\tv_rcp_f32 v105, v105\n\tv_pk_add_f32 v[94:95], v[94:95], v[118:119]\n\tv_rcp_f32 v106, v106\n\tv_pk_mul_f32 v[112:113], v[88:89], v[88:89]\n\tv_rcp_f32 v107, v107\n\t" \
    "v_pk_mul_f32 v[114:115], v[90:91], v[90:91]\n\tv_rcp_f32 v108, v108\n\tbuffer_load_dwordx4 a[4:7], v8, s[8:11], s68 offen offset:2048 sc1\n\tv_pk_mul_f32 v[116:117], v[92:93], v[92:93]\n\ts_cmp_le_u32 s4, 2\n\ts_cselect_b32 s68, 3072, 0\n\tv_rcp_f32 v109, v109\n\ts_add_u32 s68, s68, s46\n\t" \
    "v_pk_mul_f32 v[118:119], v[94:95], v[94:95]\n\ts_add_u32 s68, s68, 6144\n\tv_rcp_f32 v110, v110\n\tv_pk_fma_f32 v[112:113], v[112:113], v[246:247], v[246:247] op_sel:[0,0,1] op_sel_hi:[1,0,1]\n\tv_rcp_f32 v111, v111\n\tv_pk_fma_f32 v[114:115], v[114:115], v[246:247], v[246:247] op_sel:[0,0,1] op_sel_hi:[1,0,1]\n\tv_pk_mul_f32 v[104:105], v[104:105], v[80:81]\n\tbuffer_load_dwordx4 a[8:11], v8, s[8:11], s68 offen offset:0 sc1\n\t" \
    "v_pk_mul_f32 v[106:107], v[106:107], v[82:83]\n\tv_pk_mul_f32 v[108:109], v[108:109], v[84:85]\n\tv_pk_mul_f32 v[110:111], v[110:111], v[86:87]\n\tv_cvt_pk_bf16_f32 v80, v104, v105\n\tv_cvt_pk_bf16_f32 v81, v106, v107\n\tv_cvt_pk_bf16_f32 v82, v108, v109\n\tv_cvt_pk_bf16_f32 v83, v110, v111\n\tv_pk_fma_f32 v[116:117], v[116:117], v[246:247], v[246:247] op_sel:[0,0,1] op_sel_hi:[1,0,1]\n\t" \
    "v_pk_fma_f32 v[118:119], v[118:119], v[246:247], v[246:247] op_sel:[0,0,1] op_sel_hi:[1,0,1]\n\tv_pk_mul_f32 v[112:113], v[112:113], v[88:89]\n\tv_pk_mul_f32 v[114:115], v[114:115], v[90:91]\n\tv_pk_mul_f32 v[116:117], v[116:117], v[92:93]\n\tv_pk_mul_f32 v[118:119], v[118:119], v[94:95]\n\tv_exp_f32 v112, v112\n\tv_exp_f32 v113, v113\n\tbuffer_load_dwordx4 a[12:15], v8, s[8:11], s68 offen offset:1024 sc1\n\t" \
    "v_exp_f32 v114, v114\n\tv_exp_f32 v115, v115\n\tv_exp_f32 v116, v116\n\tv_exp_f32 v117, v117\n\tv_exp_f32 v118, v118\n\tv_exp_f32 v119, v119\n\tv_pk_add_f32 v[112:113], v[112:113], 1.0 op_sel_hi:[1,0]\n\tv_pk_add_f32 v[114:115], v[114:115], 1.0 op_sel_hi:[1,0]\n\t" \
    "buffer_load_dwordx4 a[16:19], v8, s[8:11], s68 offen offset:2048 sc1\n\tv_pk_add_f32 v[116:117], v[116:117], 1.0 op_sel_hi:[1,0]\n\ts_cmp_le_u32 s4, 3\n\ts_cselect_b32 s68, 3072, 0\n\tv_pk_add_f32 v[118:119], v[118:119], 1.0 op_sel_hi:[1,0]\n\ts_add_u32 s68, s68, s46\n\tv_rcp_f32 v112, v112\n\ts_add_u32 s68, s68, 9216\n\t" \
    "v_rcp_f32 v113, v113\n\tv_rcp_f32 v114, v114\n\tv_rcp_f32 v115, v115\n\tv_rcp_f32 v116, v116\n\tbuffer_load_dwordx4 a[20:23], v8, s[8:11], s68 offen offset:0 sc1\n\tv_rcp_f32 v117, v117\n\tv_rcp_f32 v118, v118\n\tv_rcp_f32 v119, v119\n\t" \
    "v_pk_mul_f32 v[112:113], v[112:113], v[88:89]\n\tv_pk_mul_f32 v[114:115], v[114:115], v[90:91]\n\tv_pk_mul_f32 v[116:117], v[116:117], v[92:93]\n\tv_pk_mul_f32 v[118:119], v[118:119], v[94:95]\n\tv_cvt_pk_bf16_f32 v88, v112, v113\n\tv_cvt_pk_bf16_f32 v89, v114, v115\n\tv_cvt_pk_bf16_f32 v90, v116, v117\n\tv_cvt_pk_bf16_f32 v91, v118, v119\n\t" \
    "s_cmp_lg_u32 s39, 2\n\ts_cbranch_scc1 102f\n\ts_barrier\n\ts_waitcnt lgkmcnt(8)\n\tv_mfma_f32_16x16x32_bf16 v[120:123], v[192:195], v[16:19], 0\n\tds_read_b64_tr_b16 v[40:41], v223 offset:0\n\tds_read_b64_tr_b16 v[42:43], v223 offset:2048\n\tv_mfma_f32_16x16x32_bf16 v[120:123], v[72:75], v[20:23], v[120:123]\n\t" \
    "buffer_load_dwordx4 a[24:27], v8, s[8:11], s68 offen offset:1024 sc1\n\tv_mfma_f32_16x16x32_bf16 v[128:131], v[196:199], v[16:19], 0\n\tv_mfma_f32_16x16x32_bf16 v[128:131], v[80:83], v[20:23], v[128:131]\n\tds_read_b64_tr_b16 v[44:45], v223 offset:8192\n\tds_read_b64_tr_b16 v[46:47], v223 offset:10240\n\tv_mfma_f32_16x16x32_bf16 v[136:139], v[200:203], v[16:19], 0\n\tbuffer_load_dwordx4 a[28:31], v8, s[8:11], s68 offen offset:2048 sc1\n\ts_cmp_le_u32 s4, 4\n\t" \
    "s_cselect_b32 s68, 3072, 0\n\ts_add_u32 s68, s68, s46\n\tv_mfma_f32_16x16x32_bf16 v[136:139], v[88:91], v[20:23], v[136:139]\n\ts_add_u32 s68, s68, 12288\n\ts_waitcnt lgkmcnt(8)\n\tv_mfma_f32_16x16x32_bf16 v[124:127], v[192:195], v[24:27], 0\n\tds_read_b64_tr_b16 v[16:17], v220 offset:4096\n\tds_read_b64_tr_b16 v[18:19], v220 offset:6144\n\t" \
    "v_mfma_f32_16x16x32_bf16 v[124:127], v[72:75], v[28:31], v[124:127]\n\tbuffer_load_dwordx4 a[32:35], v8, s[8:11], s68 offen offset:0 sc1\n\tv_mfma_f32_16x16x32_bf16 v[132:135], v[196:199], v[24:27], 0\n\tv_mfma_f32_16x16x32_bf16 v[132:135], v[80:83], v[28:31], v[132:135]\n\tds_read_b64_tr_b16 v[20:21], v220 offset:12288\n\tds_read_b64_tr_b16 v[22:23], v220 offset:14336\n\tv_mfma_f32_16x16x32_bf16 v[140:143], v[200:203], v[24:27], 0\n\tbuffer_load_dwordx4 a[36:39], v8, s[8:11], s68 offen offset:1024 sc1\n\t" \
    "v_mfma_f32_16x16x32_bf16 v[140:143], v[88:91], v[28:31], v[140:143]\n\ts_waitcnt lgkmcnt(8)\n\tv_mfma_f32_16x16x32_bf16 v[144:147], v[192:195], v[32:35], 0\n\tds_read_b64_tr_b16 v[24:25], v221 offset:4096\n\tds_read_b64_tr_b16 v[26:27], v221 offset:6144\n\tv_mfma_f32_16x16x32_bf16 v[144:147], v[72:75], v[36:39], v[144:147]\n\tbuffer_load_dwordx4 a[40:43], v8, s[8:11], s68 offen offset:2048 sc1\n\tv_mfma_f32_16x16x32_bf16 v[152:155], v[196:199], v[32:35], 0\n\t" \
    "v_mfma_f32_16x16x32_bf16 v[152:155], v[80:83], v[36:39], v[152:155]\n\tds_read_b64_tr_b16 v[28:29], v221 offset:12288\n\tds_read_b64_tr_b16 v[30:31], v221 offset:14336\n\tv_mfma_f32_16x16x32_bf16 v[160:163], v[200:203], v[32:35], 0\n\tv_mfma_f32_16x16x32_bf16 v[160:163], v[88:91], v[36:39], v[160:163]\n\ts_waitcnt lgkmcnt(8)\n\tv_mfma_f32_16x16x32_bf16 v[148:151], v[192:195], v[40:43], 0\n\tds_read_b64_tr_b16 v[32:33], v222 offset:4096\n\t" \
    "ds_read_b64_tr_b16 v[34:35], v222 offset:6144\n\tv_mfma_f32_16x16x32_bf16 v[148:151], v[72:75], v[44:47], v[148:151]\n\tbuffer_store_dwordx4 v[72:75], v8, s[8:11], s45 offen offset:0 sc1\n\tv_mfma_f32_16x16x32_bf16 v[156:159], v[196:199], v[40:43], 0\n\tv_mfma_f32_16x16x32_bf16 v[156:159], v[80:83], v[44:47], v[156:159]\n\tds_read_b64_tr_b16 v[36:37], v222 offset:12288\n\tds_read_b64_tr_b16 v[38:39], v222 offset:14336\n\tv_mfma_f32_16x16x32_bf16 v[164:167], v[200:203], v[40:43], 0\n\t" \
    "buffer_store_dwordx4 v[80:83], v8, s[8:11], s45 offen offset:1024 sc1\n\tv_mfma_f32_16x16x32_bf16 v[164:167], v[88:91], v[44:47], v[164:167]\n\ts_waitcnt lgkmcnt(8)\n\tv_mfma_f32_16x16x32_bf16 v[168:171], v[192:195], v[16:19], 0\n\tds_read_b64_tr_b16 v[40:41], v223 offset:4096\n\tds_read_b64_tr_b16 v[42:43], v223 offset:6144\n\tv_mfma_f32_16x16x32_bf16 v[168:171], v[72:75], v[20:23], v[168:171]\n\tbuffer_store_dwordx4 v[88:91], v8, s[8:11], s45 offen offset:2048 sc1\n\t" \
    "s_cmp_le_u32 s4, 5\n\ts_cselect_b32 s68, 3072, 0\n\ts_add_u32 s68, s68, s46\n\tv_mfma_f32_16x16x32_bf16 v[176:179], v[196:199], v[16:19], 0\n\ts_add_u32 s68, s68, 15360\n\tv_mfma_f32_16x16x32_bf16 v[176:179], v[80:83], v[20:23], v[176:179]\n\tds_read_b64_tr_b16 v[44:45], v223 offset:12288\n\tds_read_b64_tr_b16 v[46:47], v223 offset:14336\n\t" \
    "v_mfma_f32_16x16x32_bf16 v[184:187], v[200:203], v[16:19], 0\n\tv_mfma_f32_16x16x32_bf16 v[184:187], v[88:91], v[20:23], v[184:187]\n\ts_waitcnt lgkmcnt(8)\n\tv_mfma_f32_16x16x32_bf16 v[172:175], v[192:195], v[24:27], 0\n\tds_read_b64_tr_b16 v[16:17], v220 offset:16384\n\tds_read_b64_tr_b16 v[18:19], v220 offset:18432\n\tv_mfma_f32_16x16x32_bf16 v[172:175], v[72:75], v[28:31], v[172:175]\n\tbuffer_load_dwordx4 a[44:47], v8, s[8:11], s68 offen offset:0 sc1\n\t" \
    "v_mfma_f32_16x16x32_bf16 v[180:183], v[196:199], v[24:27], 0\n\tv_mfma_f32_16x16x32_bf16 v[180:183], v[80:83], v[28:31], v[180:183]\n\tds_read_b64_tr_b16 v[20:21], v220 offset:20480\n\tds_read_b64_tr_b16 v[22:23], v220 offset:22528\n\tv_mfma_f32_16x16x32_bf16 v[188:191], v[200:203], v[24:27], 0\n\tbuffer_load_dwordx4 a[48:51], v8, s[8:11], s68 offen offset:1024 sc1\n\tv_mfma_f32_16x16x32_bf16 v[188:191], v[88:91], v[28:31], v[188:191]\n\ts_waitcnt lgkmcnt(8)\n\t" \
    "v_mfma_f32_16x16x32_bf16 v[48:51], v[192:195], v[32:35], v[228:231]\n\tds_read_b64_tr_b16 v[24:25], v221 offset:16384\n\tds_read_b64_tr_b16 v[26:27], v221 offset:18432\n\tv_mfma_f32_16x16x32_bf16 v[48:51], v[72:75], v[36:39], v[48:51]\n\tv_mfma_f32_16x16x32_bf16 v[56:59], v[196:199], v[32:35], v[228:231]\n\tv_mfma_f32_16x16x32_bf16 v[56:59], v[80:83], v[36:39], v[56:59]\n\tds_read_b64_tr_b16 v[28:29], v221 offset:20480\n\tds_read_b64_tr_b16 v[30:31], v221 offset:22528\n\t" \
    "v_mfma_f32_16x16x32_bf16 v[64:67], v[200:203], v[32:35], v[228:231]\n\tbuffer_load_dwordx4 a[52:55], v8, s[8:11], s68 offen offset:2048 sc1\n\ts_cmp_le_u32 s4, 6\n\ts_cselect_b32 s68, 3072, 0\n\ts_add_u32 s68, s68, s46\n\tv_mfma_f32_16x16x32_bf16 v[64:67], v[88:91], v[36:39], v[64:67]\n\ts_add_u32 s68, s68, 18432\n\ts_waitcnt lgkmcnt(8)\n\t" \
    "v_mfma_f32_16x16x32_bf16 v[52:55], v[192:195], v[40:43], v[232:235]\n\tds_read_b64_tr_b16 v[32:33], v222 offset:16384\n\tds_read_b64_tr_b16 v[34:35], v222 offset:18432\n\tv_mfma_f32_16x16x32_bf16 v[52:55], v[72:75], v[44:47], v[52:55]\n\tbuffer_load_dwordx4 a[56:59], v8, s[8:11], s68 offen offset:0 sc1\n\tv_mfma_f32_16x16x32_bf16 v[60:63], v[196:199], v[40:43], v[232:235]\n\tv_mfma_f32_16x16x32_bf16 v[60:63], v[80:83], v[44:47], v[60:63]\n\tds_read_b64_tr_b16 v[36:37], v222 offset:20480\n\t" \
    "ds_read_b64_tr_b16 v[38:39], v222 offset:22528\n\tv_mfma_f32_16x16x32_bf16 v[68:71], v[200:203], v[40:43], v[232:235]\n\tbuffer_load_dwordx4 a[60:63], v8, s[8:11], s68 offen offset:1024 sc1\n\tv_mfma_f32_16x16x32_bf16 v[68:71], v[88:91], v[44:47], v[68:71]\n\ts_barrier\n\ts_waitcnt vmcnt(17) lgkmcnt(8)\n\tv_mfma_f32_16x16x32_bf16 v[120:123], v[204:207], v[16:19], v[120:123]\n\tds_read_b64_tr_b16 v[40:41], v223 offset:16384\n\t" \
    "ds_read_b64_tr_b16 v[42:43], v223 offset:18432\n\tv_mfma_f32_16x16x32_bf16 v[120:123], v[216:219], v[20:23], v[120:123]\n\tbuffer_load_dwordx4 a[64:67], v8, s[8:11], s68 offen offset:2048 sc1\n\ts_cmp_le_u32 s4, 7\n\ts_cselect_b32 s68, 3072, 0\n\ts_add_u32 s68, s68, s46\n\tv_mfma_f32_16x16x32_bf16 v[128:131], a[0:3], v[20:23], v[128:131]\n\ts_add_u32 s68, s68, 21504\n\t" \
    "v_mfma_f32_16x16x32_bf16 v[128:131], v[208:211], v[16:19], v[128:131]\n\tds_read_b64_tr_b16 v[44:45], v223 offset:20480\n\tds_read_b64_tr_b16 v[46:47], v223 offset:22528\n\tv_mfma_f32_16x16x32_bf16 v[136:139], v[212:215], v[16:19], v[136:139]\n\tbuffer_load_dwordx4 a[68:71], v8, s[8:11], s68 offen offset:0 sc1\n\tv_mfma_f32_16x16x32_bf16 v[136:139], a[4:7], v[20:23], v[136:139]\n\ts_waitcnt lgkmcnt(8)\n\tv_mfma_f32_16x16x32_bf16 v[124:127], v[204:207], v[24:27], v[124:127]\n\t" \
    "ds_read_b64_tr_b16 v[16:17], v220 offset:24576\n\tds_read_b64_tr_b16 v[18:19], v220 offset:26624\n\tv_mfma_f32_16x16x32_bf16 v[124:127], v[216:219], v[28:31], v[124:127]\n\tv_mfma_f32_16x16x32_bf16 v[132:135], a[0:3], v[28:31], v[132:135]\n\tv_mfma_f32_16x16x32_bf16 v[132:135], v[208:211], v[24:27], v[132:135]\n\tds_read_b64_tr_b16 v[20:21], v220 offset:28672\n\tds_read_b64_tr_b16 v[22:23], v220 offset:30720\n\tv_mfma_f32_16x16x32_bf16 v[140:143], v[212:215], v[24:27], v[140:143]\n\t" \
    "buffer_load_dwordx4 a[72:75], v8, s[8:11], s68 offen offset:1024 sc1\n\tv_mfma_f32_16x16x32_bf16 v[140:143], a[4:7], v[28:31], v[140:143]\n\ts_waitcnt lgkmcnt(8)\n\tv_mfma_f32_16x16x32_bf16 v[144:147], v[204:207], v[32:35], v[144:147]\n\tds_read_b64_tr_b16 v[24:25], v221 offset:24576\n\tds_read_b64_tr_b16 v[26:27], v221 offset:26624\n\tv_mfma_f32_16x16x32_bf16 v[144:147], v[216:219], v[36:39], v[144:147]\n\tbuffer_load_dwordx4 a[76:79], v8, s[8:11], s68 offen offset:2048 sc1\n\t" \
    "s_cmp_le_u32 s4, 8\n\ts_cselect_b32 s69, 262144, 0\n\ts_add_u32 s69, s69, 2097152\n\tv_mfma_f32_16x16x32_bf16 v[152:155], a[0:3], v[36:39], v[152:155]\n\ts_add_u32 m0, s54, 131072\n\tv_mfma_f32_16x16x32_bf16 v[152:155], v[208:211], v[32:35], v[152:155]\n\tds_read_b64_tr_b16 v[28:29], v221 offset:28672\n\tds_read_b64_tr_b16 v[30:31], v221 offset:30720\n\t" \
    "v_mfma_f32_16x16x32_bf16 v[160:163], v[212:215], v[32:35], v[160:163]\n\tbuffer_load_dwordx4 v13, s[16:19], s69 offen lds\n\ts_cmp_le_u32 s4, 9\n\ts_cselect_b32 s69, 262144, 0\n\ts_add_u32 s69, s69, 2359296\n\tv_mfma_f32_16x16x32_bf16 v[160:163], a[4:7], v[36:39], v[160:163]\n\ts_add_u32 m0, s54, 135168\n\ts_waitcnt lgkmcnt(8)\n\t" \
    "v_mfma_f32_16x16x32_bf16 v[148:151], v[204:207], v[40:43], v[148:151]\n\tds_read_b64_tr_b16 v[32:33], v222 offset:24576\n\tds_read_b64_tr_b16 v[34:35], v222 offset:26624\n\tv_mfma_f32_16x16x32_bf16 v[148:151], v[216:219], v[44:47], v[148:151]\n\tv_mfma_f32_16x16x32_bf16 v[156:159], a[0:3], v[44:47], v[156:159]\n\tv_mfma_f32_16x16x32_bf16 v[156:159], v[208:211], v[40:43], v[156:159]\n\tds_read_b64_tr_b16 v[36:37], v222 offset:28672\n\tds_read_b64_tr_b16 v[38:39], v222 offset:30720\n\t" \
    "v_mfma_f32_16x16x32_bf16 v[164:167], v[212:215], v[40:43], v[164:167]\n\tbuffer_load_dwordx4 v13, s[16:19], s69 offen lds\n\ts_cmp_le_u32 s4, 10\n\ts_cselect_b32 s69, 262144, 0\n\ts_add_u32 s69, s69, 2621440\n\tv_mfma_f32_16x16x32_bf16 v[164:167], a[4:7], v[44:47], v[164:167]\n\ts_add_u32 m0, s54, 139264\n\ts_waitcnt lgkmcnt(8)\n\t" \
    "v_mfma_f32_16x16x32_bf16 v[168:171], v[204:207], v[16:19], v[168:171]\n\tds_read_b64_tr_b16 v[40:41], v223 offset:24576\n\tds_read_b64_tr_b16 v[42:43], v223 offset:26624\n\tv_mfma_f32_16x16x32_bf16 v[168:171], v[216:219], v[20:23], v[168:171]\n\tbuffer_load_dwordx4 v12, s[16:19], s69 offen lds\n\ts_cmp_le_u32 s4, 11\n\ts_cselect_b32 s69, 262144, 0\n\ts_add_u32 s69, s69, 2883584\n\t" \
    "v_mfma_f32_16x16x32_bf16 v[176:179], a[0:3], v[20:23], v[176:179]\n\ts_add_u32 m0, s54, 143360\n\tv_mfma_f32_16x16x32_bf16 v[176:179], v[208:211], v[16:19], v[176:179]\n\tds_read_b64_tr_b16 v[44:45], v223 offset:28672\n\tds_read_b64_tr_b16 v[46:47], v223 offset:30720\n\tv_mfma_f32_16x16x32_bf16 v[184:187], v[212:215], v[16:19], v[184:187]\n\tbuffer_load_dwordx4 v12, s[16:19], s69 offen lds\n\ts_cmp_le_u32 s4, 8\n\t" \
    "s_cselect_b32 s68, 3072, 0\n\ts_add_u32 s68, s68, s46\n\tv_mfma_f32_16x16x32_bf16 v[184:187], a[4:7], v[20:23], v[184:187]\n\ts_add_u32 s68, s68, 24576\n\ts_waitcnt lgkmcnt(8)\n\tv_mfma_f32_16x16x32_bf16 v[172:175], v[204:207], v[24:27], v[172:175]\n\tds_read_b64_tr_b16 v[16:17], v4 offset:0\n\tds_read_b64_tr_b16 v[18:19], v4 offset:2048\n\t" \
    "v_mfma_f32_16x16x32_bf16 v[172:175], v[216:219], v[28:31], v[172:175]\n\tv_mfma_f32_16x16x32_bf16 v[180:183], a[0:3], v[28:31], v[180:183]\n\tv_mfma_f32_16x16x32_bf16 v[180:183], v[208:211], v[24:27], v[180:183]\n\tds_read_b64_tr_b16 v[20:21], v4 offset:4096\n\tds_read_b64_tr_b16 v[22:23], v4 offset:6144\n\tv_mfma_f32_16x16x32_bf16 v[188:191], v[212:215], v[24:27], v[188:191]\n\tbuffer_load_dwordx4 a[80:83], v8, s[8:11], s68 offen offset:0 sc1\n\tv_mfma_f32_16x16x32_bf16 v[188:191], a[4:7], v[28:31], v[188:191]\n\t" \
    "s_waitcnt lgkmcnt(8)\n\tv_mfma_f32_16x16x32_bf16 v[48:51], v[204:207], v[32:35], v[48:51]\n\tds_read_b64_tr_b16 v[24:25], v5 offset:0\n\tds_read_b64_tr_b16 v[26:27], v5 offset:2048\n\tv_mfma_f32_16x16x32_bf16 v[48:51], v[216:219], v[36:39], v[48:51]\n\tbuffer_load_dwordx4 a[84:87], v8, s[8:11], s68 offen offset:1024 sc1\n\tv_mfma_f32_16x16x32_bf16 v[56:59], a[0:3], v[36:39], v[56:59]\n\tv_mfma_f32_16x16x32_bf16 v[56:59], v[208:211], v[32:35], v[56:59]\n\t" \
    "ds_read_b64_tr_b16 v[28:29], v5 offset:4096\n\tds_read_b64_tr_b16 v[30:31], v5 offset:6144\n\tv_mfma_f32_16x16x32_bf16 v[64:67], v[212:215], v[32:35], v[64:67]\n\tv_mfma_f32_16x16x32_bf16 v[64:67], a[4:7], v[36:39], v[64:67]\n\ts_waitcnt lgkmcnt(8)\n\tv_mfma_f32_16x16x32_bf16 v[52:55], v[204:207], v[40:43], v[52:55]\n\tds_read_b64_tr_b16 v[32:33], v6 offset:0\n\tds_read_b64_tr_b16 v[34:35], v6 offset:2048\n\t" \
    "v_mfma_f32_16x16x32_bf16 v[52:55], v[216:219], v[44:47], v[52:55]\n\tbuffer_load_dwordx4 a[88:91], v8, s[8:11], s68 offen offset:2048 sc1\n\ts_cmp_le_u32 s4, 9\n\ts_cselect_b32 s68, 3072, 0\n\ts_add_u32 s68, s68, s46\n\tv_mfma_f32_16x16x32_bf16 v[60:63], a[0:3], v[44:47], v[60:63]\n\ts_add_u32 s68, s68, 27648\n\tv_mfma_f32_16x16x32_bf16 v[60:63], v[208:211], v[40:43], v[60:63]\n\t" \
    "ds_read_b64_tr_b16 v[36:37], v6 offset:4096\n\tds_read_b64_tr_b16 v[38:39], v6 offset:6144\n\tv_mfma_f32_16x16x32_bf16 v[68:71], v[212:215], v[40:43], v[68:71]\n\tbuffer_load_dwordx4 a[92:95], v8, s[8:11], s68 offen offset:0 sc1\n\tv_mfma_f32_16x16x32_bf16 v[68:71], a[4:7], v[44:47], v[68:71]\n\ts_waitcnt vmcnt(17)\n\ts_add_u32 s73, s49, 4096\n\tbuffer_store_dwordx2 v[248:249], off, s[8:11], s73 sc1\n\t" \
    "s_barrier\n\ts_waitcnt lgkmcnt(8)\n\tv_mfma_f32_16x16x32_bf16 v[120:123], a[8:11], v[16:19], v[120:123]\n\tds_read_b64_tr_b16 v[40:41], v7 offset:0\n\tds_read_b64_tr_b16 v[42:43], v7 offset:2048\n\tv_mfma_f32_16x16x32_bf16 v[120:123], a[20:23], v[20:23], v[120:123]\n\tbuffer_load_dwordx4 a[96:99], v8, s[8:11], s68 offen offset:1024 sc1\n\tv_mfma_f32_16x16x32_bf16 v[128:131], a[24:27], v[20:23], v[128:131]\n\t" \
    "v_mfma_f32_16x16x32_bf16 v[128:131], a[12:15], v[16:19], v[128:131]\n\tds_read_b64_tr_b16 v[44:45], v7 offset:4096\n\tds_read_b64_tr_b16 v[46:47], v7 offset:6144\n\tv_mfma_f32_16x16x32_bf16 v[136:139], a[16:19], v[16:19], v[136:139]\n\tbuffer_load_dwordx4 a[100:103], v8, s[8:11], s68 offen offset:2048 sc1\n\ts_cmp_le_u32 s4, 10\n\ts_cselect_b32 s69, 262144, 0\n\ts_add_u32 s69, s69, 2621440\n\t" \
    "v_mfma_f32_16x16x32_bf16 v[136:139], a[28:31], v[20:23], v[136:139]\n\ts_add_u32 m0, s54, 147456\n\ts_waitcnt lgkmcnt(8)\n\tv_mfma_f32_16x16x32_bf16 v[124:127], a[8:11], v[24:27], v[124:127]\n\tds_read_b64_tr_b16 v[16:17], v4 offset:8192\n\tds_read_b64_tr_b16 v[18:19], v4 offset:10240\n\tv_mfma_f32_16x16x32_bf16 v[124:127], a[20:23], v[28:31], v[124:127]\n\tbuffer_load_dwordx4 v13, s[16:19], s69 offen lds\n\t" \
    "s_cmp_le_u32 s4, 11\n\ts_cselect_b32 s69, 262144, 0\n\ts_add_u32 s69, s69, 2883584\n\tv_mfma_f32_16x16x32_bf16 v[132:135], a[24:27], v[28:31], v[132:135]\n\ts_add_u32 m0, s54, 151552\n\tv_mfma_f32_16x16x32_bf16 v[132:135], a[12:15], v[24:27], v[132:135]\n\tds_read_b64_tr_b16 v[20:21], v4 offset:12288\n\tds_read_b64_tr_b16 v[22:23], v4 offset:14336\n\t" \
    "v_mfma_f32_16x16x32_bf16 v[140:143], a[16:19], v[24:27], v[140:143]\n\tbuffer_load_dwordx4 v13, s[16:19], s69 offen lds\n\ts_cmp_le_u32 s4, 12\n\ts_cselect_b32 s69, 262144, 0\n\ts_add_u32 s69, s69, 3145728\n\tv_mfma_f32_16x16x32_bf16 v[140:143], a[28:31], v[28:31], v[140:143]\n\ts_add_u32 m0, s54, 155648\n\ts_waitcnt lgkmcnt(8)\n\t" \
    "v_mfma_f32_16x16x32_bf16 v[144:147], a[8:11], v[32:35], v[144:147]\n\tds_read_b64_tr_b16 v[24:25], v5 offset:8192\n\tds_read_b64_tr_b16 v[26:27], v5 offset:10240\n\tv_mfma_f32_16x16x32_bf16 v[144:147], a[20:23], v[36:39], v[144:147]\n\tbuffer_load_dwordx4 v12, s[16:19], s69 offen lds\n\ts_cmp_le_u32 s4, 13\n\ts_cselect_b32 s69, 262144, 0\n\ts_add_u32 s69, s69, 3407872\n\t" \
    "v_mfma_f32_16x16x32_bf16 v[152:155], a[24:27], v[36:39], v[152:155]\n\ts_add_u32 m0, s54, 159744\n\tv_mfma_f32_16x16x32_bf16 v[152:155], a[12:15], v[32:35], v[152:155]\n\tds_read_b64_tr_b16 v[28:29], v5 offset:12288\n\tds_read_b64_tr_b16 v[30:31], v5 offset:14336\n\tv_mfma_f32_16x16x32_bf16 v[160:163], a[16:19], v[32:35], v[160:163]\n\tbuffer_load_dwordx4 v12, s[16:19], s69 offen lds\n\ts_cmp_le_u32 s4, 10\n\t" \
    "s_cselect_b32 s68, 3072, 0\n\ts_add_u32 s68, s68, s46\n\tv_mfma_f32_16x16x32_bf16 v[160:163], a[28:31], v[36:39], v[160:163]\n\ts_add_u32 s68, s68, 30720\n\ts_waitcnt lgkmcnt(8)\n\tv_mfma_f32_16x16x32_bf16 v[148:151], a[8:11], v[40:43], v[148:151]\n\tds_read_b64_tr_b16 v[32:33], v6 offset:8192\n\tds_read_b64_tr_b16 v[34:35], v6 offset:10240\n\t" \
    "v_mfma_f32_16x16x32_bf16 v[148:151], a[20:23], v[44:47], v[148:151]\n\tbuffer_load_dwordx4 v[192:195], v8, s[8:11], s68 offen offset:0 sc1\n\tv_mfma_f32_16x16x32_bf16 v[156:159], a[24:27], v[44:47], v[156:159]\n\tv_mfma_f32_16x16x32_bf16 v[156:159], a[12:15], v[40:43], v[156:159]\n\tds_read_b64_tr_b16 v[36:37], v6 offset:12288\n\tds_read_b64_tr_b16 v[38:39], v6 offset:14336\n\tv_mfma_f32_16x16x32_bf16 v[164:167], a[16:19], v[40:43], v[164:167]\n\tv_mfma_f32_16x16x32_bf16 v[164:167], a[28:31], v[44:47], v[164:167]\n\t" \
    "s_waitcnt lgkmcnt(8)\n\tv_mfma_f32_16x16x32_bf16 v[168:171], a[8:11], v[16:19], v[168:171]\n\tds_read_b64_tr_b16 v[40:41], v7 offset:8192\n\tds_read_b64_tr_b16 v[42:43], v7 offset:10240\n\tv_mfma_f32_16x16x32_bf16 v[168:171], a[20:23], v[20:23], v[168:171]\n\tbuffer_load_dwordx4 v[196:199], v8, s[8:11], s68 offen offset:1024 sc1\n\tv_mfma_f32_16x16x32_bf16 v[176:179], a[24:27], v[20:23], v[176:179]\n\tv_mfma_f32_16x16x32_bf16 v[176:179], a[12:15], v[16:19], v[176:179]\n\t" \
    "ds_read_b64_tr_b16 v[44:45], v7 offset:12288\n\tds_read_b64_tr_b16 v[46:47], v7 offset:14336\n\tv_mfma_f32_16x16x32_bf16 v[184:187], a[16:19], v[16:19], v[184:187]\n\tbuffer_load_dwordx4 v[200:203], v8, s[8:11], s68 offen offset:2048 sc1\n\ts_cmp_le_u32 s4, 11\n\ts_cselect_b32 s68, 3072, 0\n\ts_add_u32 s68, s68, s46\n\tv_mfma_f32_16x16x32_bf16 v[184:187], a[28:31], v[20:23], v[184:187]\n\t" \
    "s_add_u32 s68, s68, 33792\n\ts_waitcnt lgkmcnt(8)\n\tv_mfma_f32_16x16x32_bf16 v[172:175], a[8:11], v[24:27], v[172:175]\n\tds_read_b64_tr_b16 v[16:17], v4 offset:16384\n\tds_read_b64_tr_b16 v[18:19], v4 offset:18432\n\tv_mfma_f32_16x16x32_bf16 v[172:175], a[20:23], v[28:31], v[172:175]\n\tbuffer_load_dwordx4 v[216:219], v8, s[8:11], s68 offen offset:0 sc1\n\tv_mfma_f32_16x16x32_bf16 v[180:183], a[24:27], v[28:31], v[180:183]\n\t" \
    "v_mfma_f32_16x16x32_bf16 v[180:183], a[12:15], v[24:27], v[180:183]\n\tds_read_b64_tr_b16 v[20:21], v4 offset:20480\n\tds_read_b64_tr_b16 v[22:23], v4 offset:22528\n\tv_mfma_f32_16x16x32_bf16 v[188:191], a[16:19], v[24:27], v[188:191]\n\tv_mfma_f32_16x16x32_bf16 v[188:191], a[28:31], v[28:31], v[188:191]\n\ts_waitcnt lgkmcnt(8)\n\tv_mfma_f32_16x16x32_bf16 v[48:51], a[8:11], v[32:35], v[48:51]\n\tds_read_b64_tr_b16 v[24:25], v5 offset:16384\n\t" \
    "ds_read_b64_tr_b16 v[26:27], v5 offset:18432\n\tv_mfma_f32_16x16x32_bf16 v[48:51], a[20:23], v[36:39], v[48:51]\n\tbuffer_load_dwordx4 v[204:207], v8, s[8:11], s68 offen offset:1024 sc1\n\tv_mfma_f32_16x16x32_bf16 v[56:59], a[24:27], v[36:39], v[56:59]\n\tv_mfma_f32_16x16x32_bf16 v[56:59], a[12:15], v[32:35], v[56:59]\n\tds_read_b64_tr_b16 v[28:29], v5 offset:20480\n\tds_read_b64_tr_b16 v[30:31], v5 offset:22528\n\tv_mfma_f32_16x16x32_bf16 v[64:67], a[16:19], v[32:35], v[64:67]\n\t" \
    "buffer_load_dwordx4 v[208:211], v8, s[8:11], s68 offen offset:2048 sc1\n\ts_cmp_le_u32 s4, 12\n\ts_cselect_b32 s69, 262144, 0\n\ts_add_u32 s69, s69, 3145728\n\tv_mfma_f32_16x16x32_bf16 v[64:67], a[28:31], v[36:39], v[64:67]\n\ts_add_u32 m0, s54, 57344\n\ts_waitcnt lgkmcnt(8)\n\tv_mfma_f32_16x16x32_bf16 v[52:55], a[8:11], v[40:43], v[52:55]\n\t" \
    "ds_read_b64_tr_b16 v[32:33], v6 offset:16384\n\tds_read_b64_tr_b16 v[34:35], v6 offset:18432\n\tv_mfma_f32_16x16x32_bf16 v[52:55], a[20:23], v[44:47], v[52:55]\n\tbuffer_load_dwordx4 v13, s[16:19], s69 offen lds\n\ts_cmp_le_u32 s4, 13\n\ts_cselect_b32 s69, 262144, 0\n\ts_add_u32 s69, s69, 3407872\n\tv_mfma_f32_16x16x32_bf16 v[60:63], a[24:27], v[44:47], v[60:63]\n\t" \
    "s_add_u32 m0, s54, 61440\n\tv_mfma_f32_16x16x32_bf16 v[60:63], a[12:15], v[40:43], v[60:63]\n\tds_read_b64_tr_b16 v[36:37], v6 offset:20480\n\tds_read_b64_tr_b16 v[38:39], v6 offset:22528\n\tv_mfma_f32_16x16x32_bf16 v[68:71], a[16:19], v[40:43], v[68:71]\n\tv_mfma_f32_16x16x32_bf16 v[68:71], a[28:31], v[44:47], v[68:71]\n\ts_barrier\n\ts_waitcnt vmcnt(28) lgkmcnt(8)\n\t" \
    "v_mfma_f32_16x16x32_bf16 v[120:123], a[32:35], v[16:19], v[120:123]\n\tds_read_b64_tr_b16 v[40:41], v7 offset:16384\n\tds_read_b64_tr_b16 v[42:43], v7 offset:18432\n\tv_mfma_f32_16x16x32_bf16 v[120:123], a[44:47], v[20:23], v[120:123]\n\tbuffer_load_dwordx4 v13, s[16:19], s69 offen lds\n\ts_cmp_le_u32 s4, 14\n\ts_cselect_b32 s69, 262144, 0\n\ts_add_u32 s69, s69, 3670016\n\t" \
    "v_mfma_f32_16x16x32_bf16 v[128:131], a[48:51], v[20:23], v[128:131]\n\ts_add_u32 m0, s54, 65536\n\tv_mfma_f32_16x16x32_bf16 v[128:131], a[36:39], v[16:19], v[128:131]\n\tds_read_b64_tr_b16 v[44:45], v7 offset:20480\n\tds_read_b64_tr_b16 v[46:47], v7 offset:22528\n\tv_mfma_f32_16x16x32_bf16 v[136:139], a[40:43], v[16:19], v[136:139]\n\tbuffer_load_dwordx4 v12, s[16:19], s69 offen lds\n\ts_cmp_le_u32 s4, 15\n\t" \
    "s_cselect_b32 s69, 262144, 0\n\ts_add_u32 s69, s69, 3932160\n\tv_mfma_f32_16x16x32_bf16 v[136:139], a[52:55], v[20:23], v[136:139]\n\ts_add_u32 m0, s54, 69632\n\ts_waitcnt lgkmcnt(8)\n\tv_mfma_f32_16x16x32_bf16 v[124:127], a[32:35], v[24:27], v[124:127]\n\tds_read_b64_tr_b16 v[16:17], v4 offset:24576\n\tds_read_b64_tr_b16 v[18:19], v4 offset:26624\n\t" \
    "v_mfma_f32_16x16x32_bf16 v[124:127], a[44:47], v[28:31], v[124:127]\n\tbuffer_load_dwordx4 v12, s[16:19], s69 offen lds\n\ts_cmp_le_u32 s4, 12\n\ts_cselect_b32 s68, 3072, 0\n\ts_add_u32 s68, s68, s46\n\tv_mfma_f32_16x16x32_bf16 v[132:135], a[48:51], v[28:31], v[132:135]\n\ts_add_u32 s68, s68, 36864\n\tv_mfma_f32_16x16x32_bf16 v[132:135], a[36:39], v[24:27], v[132:135]\n\t" \
    "ds_read_b64_tr_b16 v[20:21], v4 offset:28672\n\tds_read_b64_tr_b16 v[22:23], v4 offset:30720\n\tv_mfma_f32_16x16x32_bf16 v[140:143], a[40:43], v[24:27], v[140:143]\n\tbuffer_load_dwordx4 v[212:215], v8, s[8:11], s68 offen offset:0 sc1\n\tv_mfma_f32_16x16x32_bf16 v[140:143], a[52:55], v[28:31], v[140:143]\n\ts_waitcnt lgkmcnt(8)\n\tv_mfma_f32_16x16x32_bf16 v[144:147], a[32:35], v[32:35], v[144:147]\n\tds_read_b64_tr_b16 v[24:25], v5 offset:24576\n\t" \
    "ds_read_b64_tr_b16 v[26:27], v5 offset:26624\n\tv_mfma_f32_16x16x32_bf16 v[144:147], a[44:47], v[36:39], v[144:147]\n\tbuffer_load_dwordx4 v[72:75], v8, s[8:11], s68 offen offset:1024 sc1\n\tv_mfma_f32_16x16x32_bf16 v[152:155], a[48:51], v[36:39], v[152:155]\n\tv_mfma_f32_16x16x32_bf16 v[152:155], a[36:39], v[32:35], v[152:155]\n\tds_read_b64_tr_b16 v[28:29], v5 offset:28672\n\tds_read_b64_tr_b16 v[30:31], v5 offset:30720\n\tv_mfma_f32_16x16x32_bf16 v[160:163], a[40:43], v[32:35], v[160:163]\n\t" \
    "v_mfma_f32_16x16x32_bf16 v[160:163], a[52:55], v[36:39], v[160:163]\n\ts_waitcnt lgkmcnt(8)\n\tv_mfma_f32_16x16x32_bf16 v[148:151], a[32:35], v[40:43], v[148:151]\n\tds_read_b64_tr_b16 v[32:33], v6 offset:24576\n\tds_read_b64_tr_b16 v[34:35], v6 offset:26624\n\tv_mfma_f32_16x16x32_bf16 v[148:151], a[44:47], v[44:47], v[148:151]\n\tbuffer_load_dwordx4 v[76:79], v8, s[8:11], s68 offen offset:2048 sc1\n\ts_cmp_le_u32 s4, 13\n\t" \
    "s_cselect_b32 s68, 3072, 0\n\ts_add_u32 s68, s68, s46\n\tv_mfma_f32_16x16x32_bf16 v[156:159], a[48:51], v[44:47], v[156:159]\n\ts_add_u32 s68, s68, 39936\n\tv_mfma_f32_16x16x32_bf16 v[156:159], a[36:39], v[40:43], v[156:159]\n\tds_read_b64_tr_b16 v[36:37], v6 offset:28672\n\tds_read_b64_tr_b16 v[38:39], v6 offset:30720\n\tv_mfma_f32_16x16x32_bf16 v[164:167], a[40:43], v[40:43], v[164:167]\n\t" \
    "buffer_load_dwordx4 v[80:83], v8, s[8:11], s68 offen offset:0 sc1\n\tv_mfma_f32_16x16x32_bf16 v[164:167], a[52:55], v[44:47], v[164:167]\n\ts_waitcnt lgkmcnt(8)\n\tv_mfma_f32_16x16x32_bf16 v[168:171], a[32:35], v[16:19], v[168:171]\n\tds_read_b64_tr_b16 v[40:41], v7 offset:24576\n\tds_read_b64_tr_b16 v[42:43], v7 offset:26624\n\tv_mfma_f32_16x16x32_bf16 v[168:171], a[44:47], v[20:23], v[168:171]\n\tv_mfma_f32_16x16x32_bf16 v[176:179], a[48:51], v[20:23], v[176:179]\n\t" \
    "v_mfma_f32_16x16x32_bf16 v[176:179], a[36:39], v[16:19], v[176:179]\n\tds_read_b64_tr_b16 v[44:45], v7 offset:28672\n\tds_read_b64_tr_b16 v[46:47], v7 offset:30720\n\tv_mfma_f32_16x16x32_bf16 v[184:187], a[40:43], v[16:19], v[184:187]\n\tbuffer_load_dwordx4 v[84:87], v8, s[8:11], s68 offen offset:1024 sc1\n\tv_mfma_f32_16x16x32_bf16 v[184:187], a[52:55], v[20:23], v[184:187]\n\ts_waitcnt lgkmcnt(8)\n\tv_mfma_f32_16x16x32_bf16 v[172:175], a[32:35], v[24:27], v[172:175]\n\t" \
    "ds_read_b64_tr_b16 v[16:17], v4 offset:32768\n\tds_read_b64_tr_b16 v[18:19], v4 offset:34816\n\tv_mfma_f32_16x16x32_bf16 v[172:175], a[44:47], v[28:31], v[172:175]\n\tbuffer_load_dwordx4 v[88:91], v8, s[8:11], s68 offen offset:2048 sc1\n\ts_cmp_le_u32 s4, 14\n\ts_cselect_b32 s69, 262144, 0\n\ts_add_u32 s69, s69, 3670016\n\tv_mfma_f32_16x16x32_bf16 v[180:183], a[48:51], v[28:31], v[180:183]\n\t" \
    "s_add_u32 m0, s54, 73728\n\tv_mfma_f32_16x16x32_bf16 v[180:183], a[36:39], v[24:27], v[180:183]\n\tds_read_b64_tr_b16 v[20:21], v4 offset:36864\n\tds_read_b64_tr_b16 v[22:23], v4 offset:38912\n\tv_mfma_f32_16x16x32_bf16 v[188:191], a[40:43], v[24:27], v[188:191]\n\tbuffer_load_dwordx4 v13, s[16:19], s69 offen lds\n\ts_cmp_le_u32 s4, 15\n\ts_cselect_b32 s69, 262144, 0\n\t" \
    "s_add_u32 s69, s69, 3932160\n\tv_mfma_f32_16x16x32_bf16 v[188:191], a[52:55], v[28:31], v[188:191]\n\ts_add_u32 m0, s54, 77824\n\ts_waitcnt lgkmcnt(8)\n\tv_mfma_f32_16x16x32_bf16 v[48:51], a[32:35], v[32:35], v[48:51]\n\tds_read_b64_tr_b16 v[24:25], v5 offset:32768\n\tds_read_b64_tr_b16 v[26:27], v5 offset:34816\n\tv_mfma_f32_16x16x32_bf16 v[48:51], a[44:47], v[36:39], v[48:51]\n\t" \
    "v_mfma_f32_16x16x32_bf16 v[56:59], a[48:51], v[36:39], v[56:59]\n\tv_mfma_f32_16x16x32_bf16 v[56:59], a[36:39], v[32:35], v[56:59]\n\tds_read_b64_tr_b16 v[28:29], v5 offset:36864\n\tds_read_b64_tr_b16 v[30:31], v5 offset:38912\n\tv_mfma_f32_16x16x32_bf16 v[64:67], a[40:43], v[32:35], v[64:67]\n\tbuffer_load_dwordx4 v13, s[16:19], s69 offen lds\n\ts_cmp_le_u32 s4, 16\n\ts_cselect_b32 s69, 262144, 0\n\t" \
    "s_add_u32 s69, s69, 4194304\n\tv_mfma_f32_16x16x32_bf16 v[64:67], a[52:55], v[36:39], v[64:67]\n\ts_add_u32 m0, s54, 81920\n\ts_waitcnt lgkmcnt(8)\n\tv_mfma_f32_16x16x32_bf16 v[52:55], a[32:35], v[40:43], v[52:55]\n\tds_read_b64_tr_b16 v[32:33], v6 offset:32768\n\tds_read_b64_tr_b16 v[34:35], v6 offset:34816\n\tv_mfma_f32_16x16x32_bf16 v[52:55], a[44:47], v[44:47], v[52:55]\n\t" \
    "buffer_load_dwordx4 v12, s[16:19], s69 offen lds\n\ts_cmp_le_u32 s4, 0\n\ts_cselect_b32 s69, 262144, 0\n\ts_add_u32 s69, s69, 131072\n\tv_mfma_f32_16x16x32_bf16 v[60:63], a[48:51], v[44:47], v[60:63]\n\ts_add_u32 m0, s54, 86016\n\tv_mfma_f32_16x16x32_bf16 v[60:63], a[36:39], v[40:43], v[60:63]\n\tds_read_b64_tr_b16 v[36:37], v6 offset:36864\n\t" \
    "ds_read_b64_tr_b16 v[38:39], v6 offset:38912\n\tv_mfma_f32_16x16x32_bf16 v[68:71], a[40:43], v[40:43], v[68:71]\n\tbuffer_load_dwordx4 v12, s[16:19], s69 offen lds\n\ts_cmp_le_u32 s4, 16\n\ts_cselect_b32 s69, 262144, 0\n\ts_add_u32 s69, s69, 4194304\n\tv_mfma_f32_16x16x32_bf16 v[68:71], a[52:55], v[44:47], v[68:71]\n\ts_add_u32 m0, s54, 90112\n\t" \
    "s_barrier\n\ts_waitcnt vmcnt(35) lgkmcnt(8)\n\tv_mfma_f32_16x16x32_bf16 v[120:123], a[56:59], v[16:19], v[120:123]\n\tds_read_b64_tr_b16 v[40:41], v7 offset:32768\n\tds_read_b64_tr_b16 v[42:43], v7 offset:34816\n\tv_mfma_f32_16x16x32_bf16 v[120:123], a[68:71], v[20:23], v[120:123]\n\tbuffer_load_dwordx4 v13, s[16:19], s69 offen lds\n\ts_cmp_le_u32 s4, 0\n\t" \
    "s_cselect_b32 s69, 262144, 0\n\ts_add_u32 s69, s69, 131072\n\tv_mfma_f32_16x16x32_bf16 v[128:131], a[72:75], v[20:23], v[128:131]\n\ts_add_u32 m0, s54, 94208\n\tv_mfma_f32_16x16x32_bf16 v[128:131], a[60:63], v[16:19], v[128:131]\n\tds_read_b64_tr_b16 v[44:45], v7 offset:36864\n\tds_read_b64_tr_b16 v[46:47], v7 offset:38912\n\tv_mfma_f32_16x16x32_bf16 v[136:139], a[64:67], v[16:19], v[136:139]\n\t" \
    "buffer_load_dwordx4 v13, s[16:19], s69 offen lds\n\ts_cmp_le_u32 s4, 14\n\ts_cselect_b32 s68, 3072, 0\n\ts_add_u32 s68, s68, s46\n\tv_mfma_f32_16x16x32_bf16 v[136:139], a[76:79], v[20:23], v[136:139]\n\ts_add_u32 s68, s68, 43008\n\ts_waitcnt lgkmcnt(8)\n\tv_mfma_f32_16x16x32_bf16 v[124:127], a[56:59], v[24:27], v[124:127]\n\t" \
    "ds_read_b64_tr_b16 v[16:17], v4 offset:40960\n\tds_read_b64_tr_b16 v[18:19], v4 offset:43008\n\tv_mfma_f32_16x16x32_bf16 v[124:127], a[68:71], v[28:31], v[124:127]\n\tbuffer_load_dwordx4 v[92:95], v8, s[8:11], s68 offen offset:0 sc1\n\tv_mfma_f32_16x16x32_bf16 v[132:135], a[72:75], v[28:31], v[132:135]\n\tv_mfma_f32_16x16x32_bf16 v[132:135], a[60:63], v[24:27], v[132:135]\n\tds_read_b64_tr_b16 v[20:21], v4 offset:45056\n\tds_read_b64_tr_b16 v[22:23], v4 offset:47104\n\t" \
    "v_mfma_f32_16x16x32_bf16 v[140:143], a[64:67], v[24:27], v[140:143]\n\tv_mfma_f32_16x16x32_bf16 v[140:143], a[76:79], v[28:31], v[140:143]\n\ts_waitcnt lgkmcnt(8)\n\tv_mfma_f32_16x16x32_bf16 v[144:147], a[56:59], v[32:35], v[144:147]\n\tds_read_b64_tr_b16 v[24:25], v5 offset:40960\n\tds_read_b64_tr_b16 v[26:27], v5 offset:43008\n\tv_mfma_f32_16x16x32_bf16 v[144:147], a[68:71], v[36:39], v[144:147]\n\tbuffer_load_dwordx4 v[96:99], v8, s[8:11], s68 offen offset:1024 sc1\n\t" \
    "v_mfma_f32_16x16x32_bf16 v[152:155], a[72:75], v[36:39], v[152:155]\n\tv_mfma_f32_16x16x32_bf16 v[152:155], a[60:63], v[32:35], v[152:155]\n\tds_read_b64_tr_b16 v[28:29], v5 offset:45056\n\tds_read_b64_tr_b16 v[30:31], v5 offset:47104\n\tv_mfma_f32_16x16x32_bf16 v[160:163], a[64:67], v[32:35], v[160:163]\n\tbuffer_load_dwordx4 v[100:103], v8, s[8:11], s68 offen offset:2048 sc1\n\ts_cmp_le_u32 s4, 15\n\ts_cselect_b32 s68, 3072, 0\n\t" \
    "s_add_u32 s68, s68, s46\n\tv_mfma_f32_16x16x32_bf16 v[160:163], a[76:79], v[36:39], v[160:163]\n\ts_add_u32 s68, s68, 46080\n\ts_waitcnt lgkmcnt(8)\n\tv_mfma_f32_16x16x32_bf16 v[148:151], a[56:59], v[40:43], v[148:151]\n\tds_read_b64_tr_b16 v[32:33], v6 offset:40960\n\tds_read_b64_tr_b16 v[34:35], v6 offset:43008\n\tv_mfma_f32_16x16x32_bf16 v[148:151], a[68:71], v[44:47], v[148:151]\n\t" \
    "v_mfma_f32_16x16x32_bf16 v[156:159], a[72:75], v[44:47], v[156:159]\n\tv_mfma_f32_16x16x32_bf16 v[156:159], a[60:63], v[40:43], v[156:159]\n\tds_read_b64_tr_b16 v[36:37], v6 offset:45056\n\tds_read_b64_tr_b16 v[38:39], v6 offset:47104\n\tv_mfma_f32_16x16x32_bf16 v[164:167], a[64:67], v[40:43], v[164:167]\n\tbuffer_load_dwordx4 v[104:107], v8, s[8:11], s68 offen offset:0 sc1\n\tv_mfma_f32_16x16x32_bf16 v[164:167], a[76:79], v[44:47], v[164:167]\n\ts_waitcnt lgkmcnt(8)\n\t" \
    "v_mfma_f32_16x16x32_bf16 v[168:171], a[56:59], v[16:19], v[168:171]\n\tds_read_b64_tr_b16 v[40:41], v7 offset:40960\n\tds_read_b64_tr_b16 v[42:43], v7 offset:43008\n\tv_mfma_f32_16x16x32_bf16 v[168:171], a[68:71], v[20:23], v[168:171]\n\tbuffer_load_dwordx4 v[108:111], v8, s[8:11], s68 offen offset:1024 sc1\n\tv_mfma_f32_16x16x32_bf16 v[176:179], a[72:75], v[20:23], v[176:179]\n\tv_mfma_f32_16x16x32_bf16 v[176:179], a[60:63], v[16:19], v[176:179]\n\tds_read_b64_tr_b16 v[44:45], v7 offset:45056\n\t" \
    "ds_read_b64_tr_b16 v[46:47], v7 offset:47104\n\tv_mfma_f32_16x16x32_bf16 v[184:187], a[64:67], v[16:19], v[184:187]\n\tbuffer_load_dwordx4 v[112:115], v8, s[8:11], s68 offen offset:2048 sc1\n\ts_cmp_le_u32 s4, 16\n\ts_cselect_b32 s68, 3072, 0\n\ts_add_u32 s68, s68, s46\n\tv_mfma_f32_16x16x32_bf16 v[184:187], a[76:79], v[20:23], v[184:187]\n\ts_add_u32 s68, s68, 49152\n\t" \
    "s_waitcnt lgkmcnt(8)\n\tv_mfma_f32_16x16x32_bf16 v[172:175], a[56:59], v[24:27], v[172:175]\n\tds_read_b64_tr_b16 v[16:17], v4 offset:49152\n\tds_read_b64_tr_b16 v[18:19], v4 offset:51200\n\tv_mfma_f32_16x16x32_bf16 v[172:175], a[68:71], v[28:31], v[172:175]\n\tv_mfma_f32_16x16x32_bf16 v[180:183], a[72:75], v[28:31], v[180:183]\n\tv_mfma_f32_16x16x32_bf16 v[180:183], a[60:63], v[24:27], v[180:183]\n\tds_read_b64_tr_b16 v[20:21], v4 offset:53248\n\t" \
    "ds_read_b64_tr_b16 v[22:23], v4 offset:55296\n\tv_mfma_f32_16x16x32_bf16 v[188:191], a[64:67], v[24:27], v[188:191]\n\tbuffer_load_dwordx4 a[0:3], v8, s[8:11], s68 offen offset:0 sc1\n\tv_mfma_f32_16x16x32_bf16 v[188:191], a[76:79], v[28:31], v[188:191]\n\ts_waitcnt lgkmcnt(8)\n\tv_mfma_f32_16x16x32_bf16 v[48:51], a[56:59], v[32:35], v[48:51]\n\tds_read_b64_tr_b16 v[24:25], v5 offset:49152\n\tds_read_b64_tr_b16 v[26:27], v5 offset:51200\n\t" \
    "v_mfma_f32_16x16x32_bf16 v[48:51], a[68:71], v[36:39], v[48:51]\n\tbuffer_load_dwordx4 a[4:7], v8, s[8:11], s68 offen offset:1024 sc1\n\tv_mfma_f32_16x16x32_bf16 v[56:59], a[72:75], v[36:39], v[56:59]\n\tv_mfma_f32_16x16x32_bf16 v[56:59], a[60:63], v[32:35], v[56:59]\n\tds_read_b64_tr_b16 v[28:29], v5 offset:53248\n\tds_read_b64_tr_b16 v[30:31], v5 offset:55296\n\tv_mfma_f32_16x16x32_bf16 v[64:67], a[64:67], v[32:35], v[64:67]\n\tv_mfma_f32_16x16x32_bf16 v[64:67], a[76:79], v[36:39], v[64:67]\n\t" \
    "s_add_u32 s50, s50, 4096\n\tbuffer_load_dwordx2 v[250:251], v241, s[8:11], s50 offen sc1\n\ts_waitcnt lgkmcnt(8)\n\tv_mfma_f32_16x16x32_bf16 v[52:55], a[56:59], v[40:43], v[52:55]\n\tds_read_b64_tr_b16 v[32:33], v6 offset:49152\n\tds_read_b64_tr_b16 v[34:35], v6 offset:51200\n\tv_mfma_f32_16x16x32_bf16 v[52:55], a[68:71], v[44:47], v[52:55]\n\tv_mfma_f32_16x16x32_bf16 v[60:63], a[72:75], v[44:47], v[60:63]\n\t" \
    "v_mfma_f32_16x16x32_bf16 v[60:63], a[60:63], v[40:43], v[60:63]\n\tds_read_b64_tr_b16 v[36:37], v6 offset:53248\n\tds_read_b64_tr_b16 v[38:39], v6 offset:55296\n\tv_mfma_f32_16x16x32_bf16 v[68:71], a[64:67], v[40:43], v[68:71]\n\tbuffer_load_dwordx4 a[8:11], v8, s[8:11], s68 offen offset:2048 sc1\n\ts_cmp_le_u32 s4, 1\n\ts_cselect_b32 s69, 262144, 0\n\ts_add_u32 s69, s69, 393216\n\t" \
    "v_mfma_f32_16x16x32_bf16 v[68:71], a[76:79], v[44:47], v[68:71]\n\ts_add_u32 m0, s54, 98304\n\ts_waitcnt vmcnt(43)\n\ts_barrier\n\ts_waitcnt vmcnt(36) lgkmcnt(8)\n\tv_mfma_f32_16x16x32_bf16 v[120:123], a[80:83], v[16:19], v[120:123]\n\tds_read_b64_tr_b16 v[40:41], v7 offset:49152\n\tds_read_b64_tr_b16 v[42:43], v7 offset:51200\n\t" \
    "v_mfma_f32_16x16x32_bf16 v[120:123], a[92:95], v[20:23], v[120:123]\n\tbuffer_load_dwordx4 v12, s[16:19], s69 offen lds\n\ts_cmp_le_u32 s4, 2\n\ts_cselect_b32 s69, 262144, 0\n\ts_add_u32 s69, s69, 655360\n\tv_mfma_f32_16x16x32_bf16 v[128:131], a[96:99], v[20:23], v[128:131]\n\ts_add_u32 m0, s54, 102400\n\tv_mfma_f32_16x16x32_bf16 v[128:131], a[84:87], v[16:19], v[128:131]\n\t" \
    "ds_read_b64_tr_b16 v[44:45], v7 offset:53248\n\tds_read_b64_tr_b16 v[46:47], v7 offset:55296\n\tv_mfma_f32_16x16x32_bf16 v[136:139], a[88:91], v[16:19], v[136:139]\n\tbuffer_load_dwordx4 v12, s[16:19], s69 offen lds\n\ts_cmp_le_u32 s4, 1\n\ts_cselect_b32 s69, 262144, 0\n\ts_add_u32 s69, s69, 393216\n\tv_mfma_f32_16x16x32_bf16 v[136:139], a[100:103], v[20:23], v[136:139]\n\t" \
    "s_add_u32 m0, s54, 106496\n\ts_waitcnt lgkmcnt(8)\n\tv_mfma_f32_16x16x32_bf16 v[124:127], a[80:83], v[24:27], v[124:127]\n\tds_read_b64_tr_b16 v[16:17], v220 offset:0\n\tds_read_b64_tr_b16 v[18:19], v220 offset:2048\n\tv_mfma_f32_16x16x32_bf16 v[124:127], a[92:95], v[28:31], v[124:127]\n\tbuffer_load_dwordx4 v13, s[16:19], s69 offen lds\n\ts_cmp_le_u32 s4, 2\n\t" \
    "s_cselect_b32 s69, 262144, 0\n\ts_add_u32 s69, s69, 655360\n\tv_mfma_f32_16x16x32_bf16 v[132:135], a[96:99], v[28:31], v[132:135]\n\ts_add_u32 m0, s54, 110592\n\tv_mfma_f32_16x16x32_bf16 v[132:135], a[84:87], v[24:27], v[132:135]\n\tds_read_b64_tr_b16 v[20:21], v220 offset:4096\n\tds_read_b64_tr_b16 v[22:23], v220 offset:6144\n\tv_mfma_f32_16x16x32_bf16 v[140:143], a[88:91], v[24:27], v[140:143]\n\t" \
    "buffer_load_dwordx4 v13, s[16:19], s69 offen lds\n\ts_cmp_le_u32 s4, 3\n\ts_cselect_b32 s69, 262144, 0\n\ts_add_u32 s69, s69, 917504\n\tv_mfma_f32_16x16x32_bf16 v[140:143], a[100:103], v[28:31], v[140:143]\n\ts_add_u32 m0, s54, 114688\n\ts_waitcnt lgkmcnt(8)\n\tv_mfma_f32_16x16x32_bf16 v[144:147], a[80:83], v[32:35], v[144:147]\n\t" \
    "ds_read_b64_tr_b16 v[24:25], v221 offset:0\n\tds_read_b64_tr_b16 v[26:27], v221 offset:2048\n\tv_mfma_f32_16x16x32_bf16 v[144:147], a[92:95], v[36:39], v[144:147]\n\tv_mfma_f32_16x16x32_bf16 v[152:155], a[96:99], v[36:39], v[152:155]\n\tv_mfma_f32_16x16x32_bf16 v[152:155], a[84:87], v[32:35], v[152:155]\n\tds_read_b64_tr_b16 v[28:29], v221 offset:4096\n\tds_read_b64_tr_b16 v[30:31], v221 offset:6144\n\tv_mfma_f32_16x16x32_bf16 v[160:163], a[88:91], v[32:35], v[160:163]\n\t" \
    "buffer_load_dwordx4 v12, s[16:19], s69 offen lds\n\ts_cmp_le_u32 s4, 4\n\ts_cselect_b32 s69, 262144, 0\n\ts_add_u32 s69, s69, 1179648\n\tv_mfma_f32_16x16x32_bf16 v[160:163], a[100:103], v[36:39], v[160:163]\n\ts_add_u32 m0, s54, 118784\n\ts_waitcnt lgkmcnt(8)\n\tv_mfma_f32_16x16x32_bf16 v[148:151], a[80:83], v[40:43], v[148:151]\n\t" \
    "ds_read_b64_tr_b16 v[32:33], v222 offset:0\n\tds_read_b64_tr_b16 v[34:35], v222 offset:2048\n\tv_mfma_f32_16x16x32_bf16 v[148:151], a[92:95], v[44:47], v[148:151]\n\tbuffer_load_dwordx4 v12, s[16:19], s69 offen lds\n\ts_cmp_le_u32 s4, 3\n\ts_cselect_b32 s69, 262144, 0\n\ts_add_u32 s69, s69, 917504\n\tv_mfma_f32_16x16x32_bf16 v[156:159], a[96:99], v[44:47], v[156:159]\n\t" \
    "s_add_u32 m0, s54, 122880\n\tv_mfma_f32_16x16x32_bf16 v[156:159], a[84:87], v[40:43], v[156:159]\n\tds_read_b64_tr_b16 v[36:37], v222 offset:4096\n\tds_read_b64_tr_b16 v[38:39], v222 offset:6144\n\tv_mfma_f32_16x16x32_bf16 v[164:167], a[88:91], v[40:43], v[164:167]\n\tbuffer_load_dwordx4 v13, s[16:19], s69 offen lds\n\ts_cmp_le_u32 s4, 4\n\ts_cselect_b32 s69, 262144, 0\n\t" \
    "s_add_u32 s69, s69, 1179648\n\tv_mfma_f32_16x16x32_bf16 v[164:167], a[100:103], v[44:47], v[164:167]\n\ts_add_u32 m0, s54, 126976\n\ts_waitcnt lgkmcnt(8)\n\tv_mfma_f32_16x16x32_bf16 v[168:171], a[80:83], v[16:19], v[168:171]\n\tds_read_b64_tr_b16 v[40:41], v223 offset:0\n\tds_read_b64_tr_b16 v[42:43], v223 offset:2048\n\tv_mfma_f32_16x16x32_bf16 v[168:171], a[92:95], v[20:23], v[168:171]\n\t" \
    "v_mfma_f32_16x16x32_bf16 v[176:179], a[96:99], v[20:23], v[176:179]\n\tv_mfma_f32_16x16x32_bf16 v[176:179], a[84:87], v[16:19], v[176:179]\n\tds_read_b64_tr_b16 v[44:45], v223 offset:4096\n\tds_read_b64_tr_b16 v[46:47], v223 offset:6144\n\tv_mfma_f32_16x16x32_bf16 v[184:187], a[88:91], v[16:19], v[184:187]\n\tbuffer_load_dwordx4 v13, s[16:19], s69 offen lds\n\ts_cmp_le_u32 s4, 5\n\ts_cselect_b32 s69, 262144, 0\n\t" \
    "s_add_u32 s69, s69, 1441792\n\tv_mfma_f32_16x16x32_bf16 v[184:187], a[100:103], v[20:23], v[184:187]\n\ts_add_u32 m0, s54, 0\n\ts_waitcnt lgkmcnt(8)\n\tv_mfma_f32_16x16x32_bf16 v[172:175], a[80:83], v[24:27], v[172:175]\n\tds_read_b64_tr_b16 v[16:17], v220 offset:8192\n\tds_read_b64_tr_b16 v[18:19], v220 offset:10240\n\tv_mfma_f32_16x16x32_bf16 v[172:175], a[92:95], v[28:31], v[172:175]\n\t" \
    "buffer_load_dwordx4 v12, s[16:19], s69 offen lds\n\ts_cmp_le_u32 s4, 6\n\ts_cselect_b32 s69, 262144, 0\n\ts_add_u32 s69, s69, 1703936\n\tv_mfma_f32_16x16x32_bf16 v[180:183], a[96:99], v[28:31], v[180:183]\n\ts_add_u32 m0, s54, 4096\n\tv_mfma_f32_16x16x32_bf16 v[180:183], a[84:87], v[24:27], v[180:183]\n\tds_read_b64_tr_b16 v[20:21], v220 offset:12288\n\t" \
    "ds_read_b64_tr_b16 v[22:23], v220 offset:14336\n\tv_mfma_f32_16x16x32_bf16 v[188:191], a[88:91], v[24:27], v[188:191]\n\tbuffer_load_dwordx4 v12, s[16:19], s69 offen lds\n\ts_cmp_le_u32 s4, 5\n\ts_cselect_b32 s69, 262144, 0\n\ts_add_u32 s69, s69, 1441792\n\tv_mfma_f32_16x16x32_bf16 v[188:191], a[100:103], v[28:31], v[188:191]\n\ts_add_u32 m0, s54, 8192\n\t" \
    "s_waitcnt lgkmcnt(8)\n\tv_mfma_f32_16x16x32_bf16 v[48:51], a[80:83], v[32:35], v[48:51]\n\tds_read_b64_tr_b16 v[24:25], v221 offset:8192\n\tds_read_b64_tr_b16 v[26:27], v221 offset:10240\n\tv_mfma_f32_16x16x32_bf16 v[48:51], a[92:95], v[36:39], v[48:51]\n\tbuffer_load_dwordx4 v13, s[16:19], s69 offen lds\n\ts_cmp_le_u32 s4, 6\n\ts_cselect_b32 s69, 262144, 0\n\t" \
    "s_add_u32 s69, s69, 1703936\n\tv_mfma_f32_16x16x32_bf16 v[56:59], a[96:99], v[36:39], v[56:59]\n\ts_add_u32 m0, s54, 12288\n\tv_mfma_f32_16x16x32_bf16 v[56:59], a[84:87], v[32:35], v[56:59]\n\tds_read_b64_tr_b16 v[28:29], v221 offset:12288\n\tds_read_b64_tr_b16 v[30:31], v221 offset:14336\n\tv_mfma_f32_16x16x32_bf16 v[64:67], a[88:91], v[32:35], v[64:67]\n\tbuffer_load_dwordx4 v13, s[16:19], s69 offen lds\n\t" \
    "s_cmp_le_u32 s4, 7\n\ts_cselect_b32 s69, 262144, 0\n\ts_add_u32 s69, s69, 1966080\n\tv_mfma_f32_16x16x32_bf16 v[64:67], a[100:103], v[36:39], v[64:67]\n\ts_add_u32 m0, s54, 16384\n\ts_waitcnt lgkmcnt(8)\n\tv_mfma_f32_16x16x32_bf16 v[52:55], a[80:83], v[40:43], v[52:55]\n\tds_read_b64_tr_b16 v[32:33], v222 offset:8192\n\t" \
    "ds_read_b64_tr_b16 v[34:35], v222 offset:10240\n\tv_mfma_f32_16x16x32_bf16 v[52:55], a[92:95], v[44:47], v[52:55]\n\tbuffer_load_dwordx4 v12, s[16:19], s69 offen lds\n\ts_cmp_le_u32 s4, 8\n\ts_cselect_b32 s69, 262144, 0\n\ts_add_u32 s69, s69, 2228224\n\tv_mfma_f32_16x16x32_bf16 v[60:63], a[96:99], v[44:47], v[60:63]\n\ts_add_u32 m0, s54, 20480\n\t" \
    "v_mfma_f32_16x16x32_bf16 v[60:63], a[84:87], v[40:43], v[60:63]\n\tds_read_b64_tr_b16 v[36:37], v222 offset:12288\n\tds_read_b64_tr_b16 v[38:39], v222 offset:14336\n\tv_mfma_f32_16x16x32_bf16 v[68:71], a[88:91], v[40:43], v[68:71]\n\tv_mfma_f32_16x16x32_bf16 v[68:71], a[100:103], v[44:47], v[68:71]\n\ts_waitcnt vmcnt(45)\n\ts_barrier\n\ts_waitcnt vmcnt(39) lgkmcnt(8)\n\t" \
    "v_mfma_f32_16x16x32_bf16 v[120:123], v[192:195], v[16:19], v[120:123]\n\tds_read_b64_tr_b16 v[40:41], v223 offset:8192\n\tds_read_b64_tr_b16 v[42:43], v223 offset:10240\n\tv_mfma_f32_16x16x32_bf16 v[120:123], v[216:219], v[20:23], v[120:123]\n\tbuffer_load_dwordx4 v12, s[16:19], s69 offen lds\n\ts_cmp_le_u32 s4, 9\n\ts_cselect_b32 s69, 262144, 0\n\ts_add_u32 s69, s69, 2490368\n\t" \
    "v_mfma_f32_16x16x32_bf16 v[128:131], v[204:207], v[20:23], v[128:131]\n\ts_add_u32 m0, s54, 131072\n\tv_mfma_f32_16x16x32_bf16 v[128:131], v[196:199], v[16:19], v[128:131]\n\tds_read_b64_tr_b16 v[44:45], v223 offset:12288\n\tds_read_b64_tr_b16 v[46:47], v223 offset:14336\n\tv_mfma_f32_16x16x32_bf16 v[136:139], v[200:203], v[16:19], v[136:139]\n\tbuffer_load_dwordx4 v12, s[16:19], s69 offen lds\n\ts_cmp_le_u32 s4, 10\n\t" \
    "s_cselect_b32 s69, 262144, 0\n\ts_add_u32 s69, s69, 2752512\n\tv_mfma_f32_16x16x32_bf16 v[136:139], v[208:211], v[20:23], v[136:139]\n\ts_add_u32 m0, s54, 135168\n\ts_waitcnt lgkmcnt(8)\n\tv_mfma_f32_16x16x32_bf16 v[124:127], v[192:195], v[24:27], v[124:127]\n\tds_read_b64_tr_b16 v[16:17], v220 offset:16384\n\tds_read_b64_tr_b16 v[18:19], v220 offset:18432\n\t" \
    "v_mfma_f32_16x16x32_bf16 v[124:127], v[216:219], v[28:31], v[124:127]\n\tbuffer_load_dwordx4 v12, s[16:19], s69 offen lds\n\ts_cmp_le_u32 s4, 11\n\ts_cselect_b32 s69, 262144, 0\n\ts_add_u32 s69, s69, 3014656\n\tv_mfma_f32_16x16x32_bf16 v[132:135], v[204:207], v[28:31], v[132:135]\n\ts_add_u32 m0, s54, 24576\n\tv_mfma_f32_16x16x32_bf16 v[132:135], v[196:199], v[24:27], v[132:135]\n\t" \
    "ds_read_b64_tr_b16 v[20:21], v220 offset:20480\n\tds_read_b64_tr_b16 v[22:23], v220 offset:22528\n\tv_mfma_f32_16x16x32_bf16 v[140:143], v[200:203], v[24:27], v[140:143]\n\tbuffer_load_dwordx4 v12, s[16:19], s69 offen lds\n\ts_cmp_le_u32 s4, 12\n\ts_cselect_b32 s69, 262144, 0\n\ts_add_u32 s69, s69, 3276800\n\tv_mfma_f32_16x16x32_bf16 v[140:143], v[208:211], v[28:31], v[140:143]\n\t" \
    "s_add_u32 m0, s54, 28672\n\ts_waitcnt vmcnt(18)\n\tv_cmp_ne_u64 vcc, s[36:37], v[250:251]\n\ts_cbranch_vccz 115f\n\ts_mov_b32 s71, 1048576\n\t114:\n\ts_sleep 1\n\tbuffer_load_dwordx2 v[250:251], v241, s[8:11], s50 offen sc1\n\t" \
    "s_waitcnt vmcnt(0)\n\tv_cmp_ne_u64 vcc, s[36:37], v[250:251]\n\ts_cbranch_vccz 115f\n\ts_sub_u32 s71, s71, 1\n\ts_cbranch_scc0 114b\n\t115:\n\ts_waitcnt lgkmcnt(8)\n\tv_mfma_f32_16x16x32_bf16 v[144:147], v[192:195], v[32:35], v[144:147]\n\t" \
    "ds_read_b64_tr_b16 v[24:25], v221 offset:16384\n\tds_read_b64_tr_b16 v[26:27], v221 offset:18432\n\tv_mfma_f32_16x16x32_bf16 v[144:147], v[216:219], v[36:39], v[144:147]\n\tv_mfma_f32_16x16x32_bf16 v[152:155], v[204:207], v[36:39], v[152:155]\n\tv_mfma_f32_16x16x32_bf16 v[152:155], v[196:199], v[32:35], v[152:155]\n\tds_read_b64_tr_b16 v[28:29], v221 offset:20480\n\tds_read_b64_tr_b16 v[30:31], v221 offset:22528\n\tv_mfma_f32_16x16x32_bf16 v[160:163], v[200:203], v[32:35], v[160:163]\n\t" \
    "buffer_load_dwordx4 v12, s[16:19], s69 offen lds\n\ts_cmp_le_u32 s4, 0\n\ts_cselect_b32 s68, 3072, 0\n\ts_add_u32 s68, s68, s46\n\tv_mfma_f32_16x16x32_bf16 v[160:163], v[208:211], v[36:39], v[160:163]\n\ts_add_u32 s68, s68, 55296\n\ts_waitcnt lgkmcnt(8)\n\tv_mfma_f32_16x16x32_bf16 v[148:151], v[192:195], v[40:43], v[148:151]\n\t" \
    "ds_read_b64_tr_b16 v[32:33], v222 offset:16384\n\tds_read_b64_tr_b16 v[34:35], v222 offset:18432\n\tv_mfma_f32_16x16x32_bf16 v[148:151], v[216:219], v[44:47], v[148:151]\n\tbuffer_load_dwordx4 a[12:15], v8, s[8:11], s68 offen offset:0 sc1\n\tv_mfma_f32_16x16x32_bf16 v[156:159], v[204:207], v[44:47], v[156:159]\n\tv_mfma_f32_16x16x32_bf16 v[156:159], v[196:199], v[40:43], v[156:159]\n\tds_read_b64_tr_b16 v[36:37], v222 offset:20480\n\tds_read_b64_tr_b16 v[38:39], v222 offset:22528\n\t" \
    "v_mfma_f32_16x16x32_bf16 v[164:167], v[200:203], v[40:43], v[164:167]\n\tbuffer_load_dwordx4 a[16:19], v8, s[8:11], s68 offen offset:1024 sc1\n\tv_mfma_f32_16x16x32_bf16 v[164:167], v[208:211], v[44:47], v[164:167]\n\ts_waitcnt lgkmcnt(8)\n\tv_mfma_f32_16x16x32_bf16 v[168:171], v[192:195], v[16:19], v[168:171]\n\tds_read_b64_tr_b16 v[40:41], v223 offset:16384\n\tds_read_b64_tr_b16 v[42:43], v223 offset:18432\n\tv_mfma_f32_16x16x32_bf16 v[168:171], v[216:219], v[20:23], v[168:171]\n\t" \
    "buffer_load_dwordx4 a[20:23], v8, s[8:11], s68 offen offset:2048 sc1\n\ts_cmp_le_u32 s4, 1\n\ts_cselect_b32 s68, 3072, 0\n\ts_add_u32 s68, s68, s46\n\tv_mfma_f32_16x16x32_bf16 v[176:179], v[204:207], v[20:23], v[176:179]\n\ts_add_u32 s68, s68, 58368\n\tv_mfma_f32_16x16x32_bf16 v[176:179], v[196:199], v[16:19], v[176:179]\n\tds_read_b64_tr_b16 v[44:45], v223 offset:20480\n\t" \
    "ds_read_b64_tr_b16 v[46:47], v223 offset:22528\n\tv_mfma_f32_16x16x32_bf16 v[184:187], v[200:203], v[16:19], v[184:187]\n\tv_mfma_f32_16x16x32_bf16 v[184:187], v[208:211], v[20:23], v[184:187]\n\ts_waitcnt lgkmcnt(8)\n\tv_mfma_f32_16x16x32_bf16 v[172:175], v[192:195], v[24:27], v[172:175]\n\tds_read_b64_tr_b16 v[16:17], v220 offset:24576\n\tds_read_b64_tr_b16 v[18:19], v220 offset:26624\n\tv_mfma_f32_16x16x32_bf16 v[172:175], v[216:219], v[28:31], v[172:175]\n\t" \
    "buffer_load_dwordx4 a[24:27], v8, s[8:11], s68 offen offset:0 sc1\n\tv_mfma_f32_16x16x32_bf16 v[180:183], v[204:207], v[28:31], v[180:183]\n\tv_mfma_f32_16x16x32_bf16 v[180:183], v[196:199], v[24:27], v[180:183]\n\tds_read_b64_tr_b16 v[20:21], v220 offset:28672\n\tds_read_b64_tr_b16 v[22:23], v220 offset:30720\n\tv_mfma_f32_16x16x32_bf16 v[188:191], v[200:203], v[24:27], v[188:191]\n\tbuffer_load_dwordx4 a[28:31], v8, s[8:11], s68 offen offset:1024 sc1\n\tv_mfma_f32_16x16x32_bf16 v[188:191], v[208:211], v[28:31], v[188:191]\n\t" \
    "s_waitcnt lgkmcnt(8)\n\tv_mfma_f32_16x16x32_bf16 v[48:51], v[192:195], v[32:35], v[48:51]\n\tds_read_b64_tr_b16 v[24:25], v221 offset:24576\n\tds_read_b64_tr_b16 v[26:27], v221 offset:26624\n\tv_mfma_f32_16x16x32_bf16 v[48:51], v[216:219], v[36:39], v[48:51]\n\tbuffer_load_dwordx4 a[32:35], v8, s[8:11], s68 offen offset:2048 sc1\n\ts_cmp_le_u32 s4, 2\n\ts_cselect_b32 s68, 3072, 0\n\t" \
    "s_add_u32 s68, s68, s46\n\tv_mfma_f32_16x16x32_bf16 v[56:59], v[204:207], v[36:39], v[56:59]\n\ts_add_u32 s68, s68, 61440\n\tv_mfma_f32_16x16x32_bf16 v[56:59], v[196:199], v[32:35], v[56:59]\n\tds_read_b64_tr_b16 v[28:29], v221 offset:28672\n\tds_read_b64_tr_b16 v[30:31], v221 offset:30720\n\tv_mfma_f32_16x16x32_bf16 v[64:67], v[200:203], v[32:35], v[64:67]\n\tv_mfma_f32_16x16x32_bf16 v[64:67], v[208:211], v[36:39], v[64:67]\n\t" \
    "s_waitcnt lgkmcnt(8)\n\tv_mfma_f32_16x16x32_bf16 v[52:55], v[192:195], v[40:43], v[52:55]\n\tds_read_b64_tr_b16 v[32:33], v222 offset:24576\n\tds_read_b64_tr_b16 v[34:35], v222 offset:26624\n\tv_mfma_f32_16x16x32_bf16 v[52:55], v[216:219], v[44:47], v[52:55]\n\tbuffer_load_dwordx4 a[36:39], v8, s[8:11], s68 offen offset:0 sc1\n\tv_mfma_f32_16x16x32_bf16 v[60:63], v[204:207], v[44:47], v[60:63]\n\tv_mfma_f32_16x16x32_bf16 v[60:63], v[196:199], v[40:43], v[60:63]\n\t" \
    "ds_read_b64_tr_b16 v[36:37], v222 offset:28672\n\tds_read_b64_tr_b16 v[38:39], v222 offset:30720\n\tv_mfma_f32_16x16x32_bf16 v[68:71], v[200:203], v[40:43], v[68:71]\n\tbuffer_load_dwordx4 a[40:43], v8, s[8:11], s68 offen offset:1024 sc1\n\tv_mfma_f32_16x16x32_bf16 v[68:71], v[208:211], v[44:47], v[68:71]\n\ts_barrier\n\ts_waitcnt lgkmcnt(8)\n\tv_mfma_f32_16x16x32_bf16 v[120:123], v[212:215], v[16:19], v[120:123]\n\t" \
    "ds_read_b64_tr_b16 v[40:41], v223 offset:24576\n\tds_read_b64_tr_b16 v[42:43], v223 offset:26624\n\tv_mfma_f32_16x16x32_bf16 v[120:123], v[80:83], v[20:23], v[120:123]\n\tbuffer_load_dwordx4 a[44:47], v8, s[8:11], s68 offen offset:2048 sc1\n\ts_cmp_le_u32 s4, 3\n\ts_cselect_b32 s68, 3072, 0\n\ts_add_u32 s68, s68, s46\n\tv_mfma_f32_16x16x32_bf16 v[128:131], v[84:87], v[20:23], v[128:131]\n\t" \
    "s_add_u32 s68, s68, 64512\n\tv_mfma_f32_16x16x32_bf16 v[128:131], v[72:75], v[16:19], v[128:131]\n\tds_read_b64_tr_b16 v[44:45], v223 offset:28672\n\tds_read_b64_tr_b16 v[46:47], v223 offset:30720\n\tv_mfma_f32_16x16x32_bf16 v[136:139], v[76:79], v[16:19], v[136:139]\n\tbuffer_load_dwordx4 a[48:51], v8, s[8:11], s68 offen offset:0 sc1\n\tv_mfma_f32_16x16x32_bf16 v[136:139], v[88:91], v[20:23], v[136:139]\n\ts_waitcnt lgkmcnt(8)\n\t" \
    "v_mfma_f32_16x16x32_bf16 v[124:127], v[212:215], v[24:27], v[124:127]\n\tds_read_b64_tr_b16 v[16:17], v4 offset:57344\n\tds_read_b64_tr_b16 v[18:19], v4 offset:59392\n\tv_mfma_f32_16x16x32_bf16 v[124:127], v[80:83], v[28:31], v[124:127]\n\tbuffer_load_dwordx4 a[52:55], v8, s[8:11], s68 offen offset:1024 sc1\n\tv_mfma_f32_16x16x32_bf16 v[132:135], v[84:87], v[28:31], v[132:135]\n\tv_mfma_f32_16x16x32_bf16 v[132:135], v[72:75], v[24:27], v[132:135]\n\tds_read_b64_tr_b16 v[20:21], v4 offset:61440\n\t" \
    "ds_read_b64_tr_b16 v[22:23], v4 offset:63488\n\tv_mfma_f32_16x16x32_bf16 v[140:143], v[76:79], v[24:27], v[140:143]\n\tv_mfma_f32_16x16x32_bf16 v[140:143], v[88:91], v[28:31], v[140:143]\n\ts_waitcnt lgkmcnt(8)\n\tv_mfma_f32_16x16x32_bf16 v[144:147], v[212:215], v[32:35], v[144:147]\n\tds_read_b64_tr_b16 v[24:25], v5 offset:57344\n\tds_read_b64_tr_b16 v[26:27], v5 offset:59392\n\tv_mfma_f32_16x16x32_bf16 v[144:147], v[80:83], v[36:39], v[144:147]\n\t" \
    "buffer_load_dwordx4 a[56:59], v8, s[8:11], s68 offen offset:2048 sc1\n\ts_cmp_le_u32 s4, 4\n\ts_cselect_b32 s68, 3072, 0\n\ts_add_u32 s68, s68, s46\n\tv_mfma_f32_16x16x32_bf16 v[152:155], v[84:87], v[36:39], v[152:155]\n\ts_add_u32 s68, s68, 67584\n\tv_mfma_f32_16x16x32_bf16 v[152:155], v[72:75], v[32:35], v[152:155]\n\tds_read_b64_tr_b16 v[28:29], v5 offset:61440\n\t" \
    "ds_read_b64_tr_b16 v[30:31], v5 offset:63488\n\tv_mfma_f32_16x16x32_bf16 v[160:163], v[76:79], v[32:35], v[160:163]\n\tbuffer_load_dwordx4 a[60:63], v8, s[8:11], s68 offen offset:0 sc1\n\tv_mfma_f32_16x16x32_bf16 v[160:163], v[88:91], v[36:39], v[160:163]\n\ts_waitcnt lgkmcnt(8)\n\tv_mfma_f32_16x16x32_bf16 v[148:151], v[212:215], v[40:43], v[148:151]\n\tds_read_b64_tr_b16 v[32:33], v6 offset:57344\n\tds_read_b64_tr_b16 v[34:35], v6 offset:59392\n\t" \
    "v_mfma_f32_16x16x32_bf16 v[148:151], v[80:83], v[44:47], v[148:151]\n\tv_mfma_f32_16x16x32_bf16 v[156:159], v[84:87], v[44:47], v[156:159]\n\tv_mfma_f32_16x16x32_bf16 v[156:159], v[72:75], v[40:43], v[156:159]\n\tds_read_b64_tr_b16 v[36:37], v6 offset:61440\n\tds_read_b64_tr_b16 v[38:39], v6 offset:63488\n\tv_mfma_f32_16x16x32_bf16 v[164:167], v[76:79], v[40:43], v[164:167]\n\tbuffer_load_dwordx4 a[64:67], v8, s[8:11], s68 offen offset:1024 sc1\n\tv_mfma_f32_16x16x32_bf16 v[164:167], v[88:91], v[44:47], v[164:167]\n\t" \
    "s_waitcnt lgkmcnt(8)\n\tv_mfma_f32_16x16x32_bf16 v[168:171], v[212:215], v[16:19], v[168:171]\n\tds_read_b64_tr_b16 v[40:41], v7 offset:57344\n\tds_read_b64_tr_b16 v[42:43], v7 offset:59392\n\tv_mfma_f32_16x16x32_bf16 v[168:171], v[80:83], v[20:23], v[168:171]\n\tbuffer_load_dwordx4 a[68:71], v8, s[8:11], s68 offen offset:2048 sc1\n\ts_cmp_le_u32 s4, 5\n\ts_cselect_b32 s68, 3072, 0\n\t" \
    "s_add_u32 s68, s68, s46\n\tv_mfma_f32_16x16x32_bf16 v[176:179], v[84:87], v[20:23], v[176:179]\n\ts_add_u32 s68, s68, 70656\n\tv_mfma_f32_16x16x32_bf16 v[176:179], v[72:75], v[16:19], v[176:179]\n\tds_read_b64_tr_b16 v[44:45], v7 offset:61440\n\tds_read_b64_tr_b16 v[46:47], v7 offset:63488\n\tv_mfma_f32_16x16x32_bf16 v[184:187], v[76:79], v[16:19], v[184:187]\n\tbuffer_load_dwordx4 a[72:75], v8, s[8:11], s68 offen offset:0 sc1\n\t" \
    "v_mfma_f32_16x16x32_bf16 v[184:187], v[88:91], v[20:23], v[184:187]\n\ts_waitcnt lgkmcnt(8)\n\tv_mfma_f32_16x16x32_bf16 v[172:175], v[212:215], v[24:27], v[172:175]\n\tds_read_b64_tr_b16 v[16:17], v224 offset:0\n\tds_read_b64_tr_b16 v[18:19], v224 offset:2048\n\tv_mfma_f32_16x16x32_bf16 v[172:175], v[80:83], v[28:31], v[172:175]\n\tv_mfma_f32_16x16x32_bf16 v[180:183], v[84:87], v[28:31], v[180:183]\n\tv_mfma_f32_16x16x32_bf16 v[180:183], v[72:75], v[24:27], v[180:183]\n\t" \
    "ds_read_b64_tr_b16 v[20:21], v224 offset:4096\n\tds_read_b64_tr_b16 v[22:23], v224 offset:6144\n\tv_mfma_f32_16x16x32_bf16 v[188:191], v[76:79], v[24:27], v[188:191]\n\tbuffer_load_dwordx4 a[76:79], v8, s[8:11], s68 offen offset:1024 sc1\n\tv_mfma_f32_16x16x32_bf16 v[188:191], v[88:91], v[28:31], v[188:191]\n\ts_waitcnt lgkmcnt(8)\n\tv_mfma_f32_16x16x32_bf16 v[48:51], v[212:215], v[32:35], v[48:51]\n\tds_read_b64_tr_b16 v[24:25], v225 offset:0\n\t" \
    "ds_read_b64_tr_b16 v[26:27], v225 offset:2048\n\tv_mfma_f32_16x16x32_bf16 v[48:51], v[80:83], v[36:39], v[48:51]\n\tbuffer_load_dwordx4 a[80:83], v8, s[8:11], s68 offen offset:2048 sc1\n\ts_cmp_le_u32 s4, 6\n\ts_cselect_b32 s68, 3072, 0\n\ts_add_u32 s68, s68, s46\n\tv_mfma_f32_16x16x32_bf16 v[56:59], v[84:87], v[36:39], v[56:59]\n\ts_add_u32 s68, s68, 73728\n\t" \
    "v_mfma_f32_16x16x32_bf16 v[56:59], v[72:75], v[32:35], v[56:59]\n\tds_read_b64_tr_b16 v[28:29], v225 offset:4096\n\tds_read_b64_tr_b16 v[30:31], v225 offset:6144\n\tv_mfma_f32_16x16x32_bf16 v[64:67], v[76:79], v[32:35], v[64:67]\n\tv_mfma_f32_16x16x32_bf16 v[64:67], v[88:91], v[36:39], v[64:67]\n\ts_waitcnt lgkmcnt(8)\n\tv_mfma_f32_16x16x32_bf16 v[52:55], v[212:215], v[40:43], v[52:55]\n\tds_read_b64_tr_b16 v[32:33], v226 offset:0\n\t" \
    "ds_read_b64_tr_b16 v[34:35], v226 offset:2048\n\tv_mfma_f32_16x16x32_bf16 v[52:55], v[80:83], v[44:47], v[52:55]\n\tbuffer_load_dwordx4 a[84:87], v8, s[8:11], s68 offen offset:0 sc1\n\tv_mfma_f32_16x16x32_bf16 v[60:63], v[84:87], v[44:47], v[60:63]\n\tv_mfma_f32_16x16x32_bf16 v[60:63], v[72:75], v[40:43], v[60:63]\n\tds_read_b64_tr_b16 v[36:37], v226 offset:4096\n\tds_read_b64_tr_b16 v[38:39], v226 offset:6144\n\tv_mfma_f32_16x16x32_bf16 v[68:71], v[76:79], v[40:43], v[68:71]\n\t" \
    "buffer_load_dwordx4 a[88:91], v8, s[8:11], s68 offen offset:1024 sc1\n\tv_mfma_f32_16x16x32_bf16 v[68:71], v[88:91], v[44:47], v[68:71]\n\ts_barrier\n\ts_waitcnt lgkmcnt(8)\n\tv_mfma_f32_16x16x32_bf16 v[120:123], v[92:95], v[16:19], v[120:123]\n\tds_read_b64_tr_b16 v[40:41], v227 offset:0\n\tds_read_b64_tr_b16 v[42:43], v227 offset:2048\n\tv_mfma_f32_16x16x32_bf16 v[120:123], v[104:107], v[20:23], v[120:123]\n\t" \
    "buffer_load_dwordx4 a[92:95], v8, s[8:11], s68 offen offset:2048 sc1\n\ts_cmp_le_u32 s4, 7\n\ts_cselect_b32 s68, 3072, 0\n\ts_add_u32 s68, s68, s46\n\tv_mfma_f32_16x16x32_bf16 v[128:131], v[108:111], v[20:23], v[128:131]\n\ts_add_u32 s68, s68, 76800\n\tv_mfma_f32_16x16x32_bf16 v[128:131], v[96:99], v[16:19], v[128:131]\n\tds_read_b64_tr_b16 v[44:45], v227 offset:4096\n\t" \
    "ds_read_b64_tr_b16 v[46:47], v227 offset:6144\n\tv_mfma_f32_16x16x32_bf16 v[136:139], v[100:103], v[16:19], v[136:139]\n\tbuffer_load_dwordx4 a[96:99], v8, s[8:11], s68 offen offset:0 sc1\n\tv_mfma_f32_16x16x32_bf16 v[136:139], v[112:115], v[20:23], v[136:139]\n\ts_waitcnt lgkmcnt(8)\n\tv_mfma_f32_16x16x32_bf16 v[124:127], v[92:95], v[24:27], v[124:127]\n\tds_read_b64_tr_b16 v[16:17], v224 offset:8192\n\tds_read_b64_tr_b16 v[18:19], v224 offset:10240\n\t" \
    "v_mfma_f32_16x16x32_bf16 v[124:127], v[104:107], v[28:31], v[124:127]\n\tbuffer_load_dwordx4 a[100:103], v8, s[8:11], s68 offen offset:1024 sc1\n\tv_mfma_f32_16x16x32_bf16 v[132:135], v[108:111], v[28:31], v[132:135]\n\tv_mfma_f32_16x16x32_bf16 v[132:135], v[96:99], v[24:27], v[132:135]\n\tds_read_b64_tr_b16 v[20:21], v224 offset:12288\n\tds_read_b64_tr_b16 v[22:23], v224 offset:14336\n\tv_mfma_f32_16x16x32_bf16 v[140:143], v[100:103], v[24:27], v[140:143]\n\tv_mfma_f32_16x16x32_bf16 v[140:143], v[112:115], v[28:31], v[140:143]\n\t" \
    "s_waitcnt lgkmcnt(8)\n\tv_mfma_f32_16x16x32_bf16 v[144:147], v[92:95], v[32:35], v[144:147]\n\tds_read_b64_tr_b16 v[24:25], v225 offset:8192\n\tds_read_b64_tr_b16 v[26:27], v225 offset:10240\n\tv_mfma_f32_16x16x32_bf16 v[144:147], v[104:107], v[36:39], v[144:147]\n\tbuffer_load_dwordx4 a[104:107], v8, s[8:11], s68 offen offset:2048 sc1\n\ts_cmp_le_u32 s4, 8\n\ts_cselect_b32 s68, 3072, 0\n\t" \
    "s_add_u32 s68, s68, s46\n\tv_mfma_f32_16x16x32_bf16 v[152:155], v[108:111], v[36:39], v[152:155]\n\ts_add_u32 s68, s68, 79872\n\tv_mfma_f32_16x16x32_bf16 v[152:155], v[96:99], v[32:35], v[152:155]\n\tds_read_b64_tr_b16 v[28:29], v225 offset:12288\n\tds_read_b64_tr_b16 v[30:31], v225 offset:14336\n\tv_mfma_f32_16x16x32_bf16 v[160:163], v[100:103], v[32:35], v[160:163]\n\tbuffer_load_dwordx4 a[108:111], v8, s[8:11], s68 offen offset:0 sc1\n\t" \
    "v_mfma_f32_16x16x32_bf16 v[160:163], v[112:115], v[36:39], v[160:163]\n\ts_waitcnt lgkmcnt(8)\n\tv_mfma_f32_16x16x32_bf16 v[148:151], v[92:95], v[40:43], v[148:151]\n\tds_read_b64_tr_b16 v[32:33], v224 offset:16384\n\tds_read_b64_tr_b16 v[34:35], v224 offset:18432\n\tv_mfma_f32_16x16x32_bf16 v[148:151], v[104:107], v[44:47], v[148:151]\n\tbuffer_load_dwordx4 a[112:115], v8, s[8:11], s68 offen offset:1024 sc1\n\tv_mfma_f32_16x16x32_bf16 v[156:159], v[108:111], v[44:47], v[156:159]\n\t" \
    "v_mfma_f32_16x16x32_bf16 v[156:159], v[96:99], v[40:43], v[156:159]\n\tds_read_b64_tr_b16 v[36:37], v224 offset:20480\n\tds_read_b64_tr_b16 v[38:39], v224 offset:22528\n\tv_mfma_f32_16x16x32_bf16 v[164:167], v[100:103], v[40:43], v[164:167]\n\tv_mfma_f32_16x16x32_bf16 v[164:167], v[112:115], v[44:47], v[164:167]\n\ts_waitcnt lgkmcnt(8)\n\tv_mfma_f32_16x16x32_bf16 v[168:171], v[92:95], v[16:19], v[168:171]\n\tds_read_b64_tr_b16 v[40:41], v225 offset:16384\n\t" \
    "ds_read_b64_tr_b16 v[42:43], v225 offset:18432\n\tv_mfma_f32_16x16x32_bf16 v[168:171], v[104:107], v[20:23], v[168:171]\n\tbuffer_load_dwordx4 a[116:119], v8, s[8:11], s68 offen offset:2048 sc1\n\ts_cmp_le_u32 s4, 13\n\ts_cselect_b32 s69, 262144, 0\n\ts_add_u32 s69, s69, 3538944\n\tv_mfma_f32_16x16x32_bf16 v[176:179], v[108:111], v[20:23], v[176:179]\n\ts_add_u32 m0, s54, 139264\n\t" \
    "v_mfma_f32_16x16x32_bf16 v[176:179], v[96:99], v[16:19], v[176:179]\n\tds_read_b64_tr_b16 v[44:45], v225 offset:20480\n\tds_read_b64_tr_b16 v[46:47], v225 offset:22528\n\tv_mfma_f32_16x16x32_bf16 v[184:187], v[100:103], v[16:19], v[184:187]\n\tbuffer_load_dwordx4 v12, s[16:19], s69 offen lds\n\ts_cmp_le_u32 s4, 14\n\ts_cselect_b32 s69, 262144, 0\n\ts_add_u32 s69, s69, 3801088\n\t" \
    "v_mfma_f32_16x16x32_bf16 v[184:187], v[112:115], v[20:23], v[184:187]\n\ts_add_u32 m0, s54, 143360\n\ts_waitcnt lgkmcnt(8)\n\tv_mfma_f32_16x16x32_bf16 v[172:175], v[92:95], v[24:27], v[172:175]\n\tds_read_b64_tr_b16 v[16:17], v226 offset:16384\n\tds_read_b64_tr_b16 v[18:19], v226 offset:18432\n\tv_mfma_f32_16x16x32_bf16 v[172:175], v[104:107], v[28:31], v[172:175]\n\tv_mfma_f32_16x16x32_bf16 v[180:183], v[108:111], v[28:31], v[180:183]\n\t" \
    "v_mfma_f32_16x16x32_bf16 v[180:183], v[96:99], v[24:27], v[180:183]\n\tds_read_b64_tr_b16 v[20:21], v226 offset:20480\n\tds_read_b64_tr_b16 v[22:23], v226 offset:22528\n\tv_mfma_f32_16x16x32_bf16 v[188:191], v[100:103], v[24:27], v[188:191]\n\tbuffer_load_dwordx4 v12, s[16:19], s69 offen lds\n\ts_cmp_le_u32 s4, 15\n\ts_cselect_b32 s69, 262144, 0\n\ts_add_u32 s69, s69, 4063232\n\t" \
    "v_mfma_f32_16x16x32_bf16 v[188:191], v[112:115], v[28:31], v[188:191]\n\ts_add_u32 m0, s54, 147456\n\ts_waitcnt vmcnt(26) lgkmcnt(8)\n\tv_mfma_f32_16x16x32_bf16 v[120:123], a[0:3], v[32:35], v[120:123]\n\tds_read_b64_tr_b16 v[24:25], v227 offset:16384\n\tds_read_b64_tr_b16 v[26:27], v227 offset:18432\n\tv_mfma_f32_16x16x32_bf16 v[120:123], a[12:15], v[36:39], v[120:123]\n\tbuffer_load_dwordx4 v12, s[16:19], s69 offen lds\n\t" \
    "s_cmp_le_u32 s4, 16\n\ts_cselect_b32 s69, 262144, 0\n\ts_add_u32 s69, s69, 4325376\n\tv_mfma_f32_16x16x32_bf16 v[128:131], a[16:19], v[36:39], v[128:131]\n\ts_add_u32 m0, s54, 151552\n\tv_mfma_f32_16x16x32_bf16 v[128:131], a[4:7], v[32:35], v[128:131]\n\tds_read_b64_tr_b16 v[28:29], v227 offset:20480\n\tds_read_b64_tr_b16 v[30:31], v227 offset:22528\n\t" \
    "v_mfma_f32_16x16x32_bf16 v[136:139], a[8:11], v[32:35], v[136:139]\n\tbuffer_load_dwordx4 v12, s[16:19], s69 offen lds\n\ts_cmp_le_u32 s4, 9\n\ts_cselect_b32 s68, 3072, 0\n\ts_add_u32 s68, s68, s46\n\tv_mfma_f32_16x16x32_bf16 v[136:139], a[20:23], v[36:39], v[136:139]\n\ts_add_u32 s68, s68, 82944\n\ts_waitcnt lgkmcnt(8)\n\t" \
    "v_mfma_f32_16x16x32_bf16 v[124:127], a[0:3], v[40:43], v[124:127]\n\tds_read_b64_tr_b16 v[32:33], v224 offset:24576\n\tds_read_b64_tr_b16 v[34:35], v224 offset:26624\n\tv_mfma_f32_16x16x32_bf16 v[124:127], a[12:15], v[44:47], v[124:127]\n\tv_mfma_f32_16x16x32_bf16 v[132:135], a[16:19], v[44:47], v[132:135]\n\tv_mfma_f32_16x16x32_bf16 v[132:135], a[4:7], v[40:43], v[132:135]\n\tds_read_b64_tr_b16 v[36:37], v224 offset:28672\n\tds_read_b64_tr_b16 v[38:39], v224 offset:30720\n\t" \
    "v_mfma_f32_16x16x32_bf16 v[140:143], a[8:11], v[40:43], v[140:143]\n\tbuffer_load_dwordx4 a[120:123], v8, s[8:11], s68 offen offset:0 sc1\n\tv_mfma_f32_16x16x32_bf16 v[140:143], a[20:23], v[44:47], v[140:143]\n\ts_barrier\n\ts_waitcnt lgkmcnt(8)\n\tv_mfma_f32_16x16x32_bf16 v[144:147], a[0:3], v[16:19], v[144:147]\n\tds_read_b64_tr_b16 v[40:41], v225 offset:24576\n\tds_read_b64_tr_b16 v[42:43], v225 offset:26624\n\t" \
    "v_mfma_f32_16x16x32_bf16 v[144:147], a[12:15], v[20:23], v[144:147]\n\tbuffer_load_dwordx4 a[124:127], v8, s[8:11], s68 offen offset:1024 sc1\n\tv_mfma_f32_16x16x32_bf16 v[152:155], a[16:19], v[20:23], v[152:155]\n\tv_mfma_f32_16x16x32_bf16 v[152:155], a[4:7], v[16:19], v[152:155]\n\tds_read_b64_tr_b16 v[44:45], v225 offset:28672\n\tds_read_b64_tr_b16 v[46:47], v225 offset:30720\n\tv_mfma_f32_16x16x32_bf16 v[160:163], a[8:11], v[16:19], v[160:163]\n\tbuffer_load_dwordx4 a[128:131], v8, s[8:11], s68 offen offset:2048 sc1\n\t" \
    "s_cmp_le_u32 s4, 10\n\ts_cselect_b32 s68, 3072, 0\n\ts_add_u32 s68, s68, s46\n\tv_mfma_f32_16x16x32_bf16 v[160:163], a[20:23], v[20:23], v[160:163]\n\ts_add_u32 s68, s68, 86016\n\ts_waitcnt lgkmcnt(8)\n\tv_mfma_f32_16x16x32_bf16 v[148:151], a[0:3], v[24:27], v[148:151]\n\tds_read_b64_tr_b16 v[16:17], v224 offset:32768\n\t" \
    "ds_read_b64_tr_b16 v[18:19], v224 offset:34816\n\tv_mfma_f32_16x16x32_bf16 v[148:151], a[12:15], v[28:31], v[148:151]\n\tbuffer_load_dwordx4 a[132:135], v8, s[8:11], s68 offen offset:0 sc1\n\tv_mfma_f32_16x16x32_bf16 v[156:159], a[16:19], v[28:31], v[156:159]\n\tv_mfma_f32_16x16x32_bf16 v[156:159], a[4:7], v[24:27], v[156:159]\n\tds_read_b64_tr_b16 v[20:21], v224 offset:36864\n\tds_read_b64_tr_b16 v[22:23], v224 offset:38912\n\tv_mfma_f32_16x16x32_bf16 v[164:167], a[8:11], v[24:27], v[164:167]\n\t" \
    "buffer_load_dwordx4 a[136:139], v8, s[8:11], s68 offen offset:1024 sc1\n\tv_mfma_f32_16x16x32_bf16 v[164:167], a[20:23], v[28:31], v[164:167]\n\ts_waitcnt lgkmcnt(8)\n\tv_mfma_f32_16x16x32_bf16 v[168:171], a[0:3], v[32:35], v[168:171]\n\tds_read_b64_tr_b16 v[24:25], v225 offset:32768\n\tds_read_b64_tr_b16 v[26:27], v225 offset:34816\n\tv_mfma_f32_16x16x32_bf16 v[168:171], a[12:15], v[36:39], v[168:171]\n\tv_mfma_f32_16x16x32_bf16 v[176:179], a[16:19], v[36:39], v[176:179]\n\t" \
    "v_mfma_f32_16x16x32_bf16 v[176:179], a[4:7], v[32:35], v[176:179]\n\tds_read_b64_tr_b16 v[28:29], v225 offset:36864\n\tds_read_b64_tr_b16 v[30:31], v225 offset:38912\n\tv_mfma_f32_16x16x32_bf16 v[184:187], a[8:11], v[32:35], v[184:187]\n\tbuffer_load_dwordx4 a[140:143], v8, s[8:11], s68 offen offset:2048 sc1\n\ts_cmp_le_u32 s4, 11\n\ts_cselect_b32 s68, 3072, 0\n\ts_add_u32 s68, s68, s46\n\t" \
    "v_mfma_f32_16x16x32_bf16 v[184:187], a[20:23], v[36:39], v[184:187]\n\ts_add_u32 s68, s68, 89088\n\ts_waitcnt lgkmcnt(8)\n\tv_mfma_f32_16x16x32_bf16 v[172:175], a[0:3], v[40:43], v[172:175]\n\tds_read_b64_tr_b16 v[32:33], v226 offset:32768\n\tds_read_b64_tr_b16 v[34:35], v226 offset:34816\n\tv_mfma_f32_16x16x32_bf16 v[172:175], a[12:15], v[44:47], v[172:175]\n\tbuffer_load_dwordx4 a[144:147], v8, s[8:11], s68 offen offset:0 sc1\n\t" \
    "v_mfma_f32_16x16x32_bf16 v[180:183], a[16:19], v[44:47], v[180:183]\n\tv_mfma_f32_16x16x32_bf16 v[180:183], a[4:7], v[40:43], v[180:183]\n\tds_read_b64_tr_b16 v[36:37], v226 offset:36864\n\tds_read_b64_tr_b16 v[38:39], v226 offset:38912\n\tv_mfma_f32_16x16x32_bf16 v[188:191], a[8:11], v[40:43], v[188:191]\n\tbuffer_load_dwordx4 a[148:151], v8, s[8:11], s68 offen offset:1024 sc1\n\tv_mfma_f32_16x16x32_bf16 v[188:191], a[20:23], v[44:47], v[188:191]\n\ts_waitcnt vmcnt(30) lgkmcnt(8)\n\t" \
    "v_mfma_f32_16x16x32_bf16 v[120:123], a[24:27], v[16:19], v[120:123]\n\tds_read_b64_tr_b16 v[40:41], v227 offset:32768\n\tds_read_b64_tr_b16 v[42:43], v227 offset:34816\n\tv_mfma_f32_16x16x32_bf16 v[120:123], a[36:39], v[20:23], v[120:123]\n\tv_mfma_f32_16x16x32_bf16 v[128:131], a[40:43], v[20:23], v[128:131]\n\tv_mfma_f32_16x16x32_bf16 v[128:131], a[28:31], v[16:19], v[128:131]\n\tds_read_b64_tr_b16 v[44:45], v227 offset:36864\n\tds_read_b64_tr_b16 v[46:47], v227 offset:38912\n\t" \
    "v_mfma_f32_16x16x32_bf16 v[136:139], a[32:35], v[16:19], v[136:139]\n\tbuffer_load_dwordx4 a[152:155], v8, s[8:11], s68 offen offset:2048 sc1\n\ts_cmp_le_u32 s4, 12\n\ts_cselect_b32 s68, 3072, 0\n\ts_add_u32 s68, s68, s46\n\tv_mfma_f32_16x16x32_bf16 v[136:139], a[44:47], v[20:23], v[136:139]\n\ts_add_u32 s68, s68, 92160\n\ts_waitcnt lgkmcnt(8)\n\t" \
    "v_mfma_f32_16x16x32_bf16 v[124:127], a[24:27], v[24:27], v[124:127]\n\tds_read_b64_tr_b16 v[16:17], v224 offset:40960\n\tds_read_b64_tr_b16 v[18:19], v224 offset:43008\n\tv_mfma_f32_16x16x32_bf16 v[124:127], a[36:39], v[28:31], v[124:127]\n\tbuffer_load_dwordx4 a[156:159], v8, s[8:11], s68 offen offset:0 sc1\n\tv_mfma_f32_16x16x32_bf16 v[132:135], a[40:43], v[28:31], v[132:135]\n\tv_mfma_f32_16x16x32_bf16 v[132:135], a[28:31], v[24:27], v[132:135]\n\tds_read_b64_tr_b16 v[20:21], v224 offset:45056\n\t" \
    "ds_read_b64_tr_b16 v[22:23], v224 offset:47104\n\tv_mfma_f32_16x16x32_bf16 v[140:143], a[32:35], v[24:27], v[140:143]\n\tv_mfma_f32_16x16x32_bf16 v[140:143], a[44:47], v[28:31], v[140:143]\n\ts_waitcnt lgkmcnt(8)\n\tv_mfma_f32_16x16x32_bf16 v[144:147], a[24:27], v[32:35], v[144:147]\n\tds_read_b64_tr_b16 v[24:25], v225 offset:40960\n\tds_read_b64_tr_b16 v[26:27], v225 offset:43008\n\tv_mfma_f32_16x16x32_bf16 v[144:147], a[36:39], v[36:39], v[144:147]\n\t" \
    "buffer_load_dwordx4 a[160:163], v8, s[8:11], s68 offen offset:1024 sc1\n\tv_mfma_f32_16x16x32_bf16 v[152:155], a[40:43], v[36:39], v[152:155]\n\tv_mfma_f32_16x16x32_bf16 v[152:155], a[28:31], v[32:35], v[152:155]\n\tds_read_b64_tr_b16 v[28:29], v225 offset:45056\n\tds_read_b64_tr_b16 v[30:31], v225 offset:47104\n\tv_mfma_f32_16x16x32_bf16 v[160:163], a[32:35], v[32:35], v[160:163]\n\tbuffer_load_dwordx4 a[164:167], v8, s[8:11], s68 offen offset:2048 sc1\n\ts_cmp_le_u32 s4, 13\n\t" \
    "s_cselect_b32 s68, 3072, 0\n\ts_add_u32 s68, s68, s46\n\tv_mfma_f32_16x16x32_bf16 v[160:163], a[44:47], v[36:39], v[160:163]\n\ts_add_u32 s68, s68, 95232\n\ts_waitcnt lgkmcnt(8)\n\tv_mfma_f32_16x16x32_bf16 v[148:151], a[24:27], v[40:43], v[148:151]\n\tds_read_b64_tr_b16 v[32:33], v224 offset:49152\n\tds_read_b64_tr_b16 v[34:35], v224 offset:51200\n\t" \
    "v_mfma_f32_16x16x32_bf16 v[148:151], a[36:39], v[44:47], v[148:151]\n\tbuffer_load_dwordx4 a[168:171], v8, s[8:11], s68 offen offset:0 sc1\n\tv_mfma_f32_16x16x32_bf16 v[156:159], a[40:43], v[44:47], v[156:159]\n\tv_mfma_f32_16x16x32_bf16 v[156:159], a[28:31], v[40:43], v[156:159]\n\tds_read_b64_tr_b16 v[36:37], v224 offset:53248\n\tds_read_b64_tr_b16 v[38:39], v224 offset:55296\n\tv_mfma_f32_16x16x32_bf16 v[164:167], a[32:35], v[40:43], v[164:167]\n\tv_mfma_f32_16x16x32_bf16 v[164:167], a[44:47], v[44:47], v[164:167]\n\t" \
    "s_barrier\n\ts_waitcnt lgkmcnt(8)\n\tv_mfma_f32_16x16x32_bf16 v[168:171], a[24:27], v[16:19], v[168:171]\n\tds_read_b64_tr_b16 v[40:41], v225 offset:49152\n\tds_read_b64_tr_b16 v[42:43], v225 offset:51200\n\tv_mfma_f32_16x16x32_bf16 v[168:171], a[36:39], v[20:23], v[168:171]\n\tbuffer_load_dwordx4 a[172:175], v8, s[8:11], s68 offen offset:1024 sc1\n\tv_mfma_f32_16x16x32_bf16 v[176:179], a[40:43], v[20:23], v[176:179]\n\t" \
    "v_mfma_f32_16x16x32_bf16 v[176:179], a[28:31], v[16:19], v[176:179]\n\tds_read_b64_tr_b16 v[44:45], v225 offset:53248\n\tds_read_b64_tr_b16 v[46:47], v225 offset:55296\n\tv_mfma_f32_16x16x32_bf16 v[184:187], a[32:35], v[16:19], v[184:187]\n\tbuffer_load_dwordx4 a[176:179], v8, s[8:11], s68 offen offset:2048 sc1\n\ts_cmp_le_u32 s4, 14\n\ts_cselect_b32 s68, 3072, 0\n\ts_add_u32 s68, s68, s46\n\t" \
    "v_mfma_f32_16x16x32_bf16 v[184:187], a[44:47], v[20:23], v[184:187]\n\ts_add_u32 s68, s68, 98304\n\ts_waitcnt lgkmcnt(8)\n\tv_mfma_f32_16x16x32_bf16 v[172:175], a[24:27], v[24:27], v[172:175]\n\tds_read_b64_tr_b16 v[16:17], v226 offset:49152\n\tds_read_b64_tr_b16 v[18:19], v226 offset:51200\n\tv_mfma_f32_16x16x32_bf16 v[172:175], a[36:39], v[28:31], v[172:175]\n\tbuffer_load_dwordx4 a[180:183], v8, s[8:11], s68 offen offset:0 sc1\n\t" \
    "v_mfma_f32_16x16x32_bf16 v[180:183], a[40:43], v[28:31], v[180:183]\n\tv_mfma_f32_16x16x32_bf16 v[180:183], a[28:31], v[24:27], v[180:183]\n\tds_read_b64_tr_b16 v[20:21], v226 offset:53248\n\tds_read_b64_tr_b16 v[22:23], v226 offset:55296\n\tv_mfma_f32_16x16x32_bf16 v[188:191], a[32:35], v[24:27], v[188:191]\n\tbuffer_load_dwordx4 a[184:187], v8, s[8:11], s68 offen offset:1024 sc1\n\tv_mfma_f32_16x16x32_bf16 v[188:191], a[44:47], v[28:31], v[188:191]\n\ts_waitcnt vmcnt(33) lgkmcnt(8)\n\t" \
    "v_mfma_f32_16x16x32_bf16 v[120:123], a[48:51], v[32:35], v[120:123]\n\tds_read_b64_tr_b16 v[24:25], v227 offset:49152\n\tds_read_b64_tr_b16 v[26:27], v227 offset:51200\n\tv_mfma_f32_16x16x32_bf16 v[120:123], a[60:63], v[36:39], v[120:123]\n\tv_mfma_f32_16x16x32_bf16 v[128:131], a[64:67], v[36:39], v[128:131]\n\tv_mfma_f32_16x16x32_bf16 v[128:131], a[52:55], v[32:35], v[128:131]\n\tds_read_b64_tr_b16 v[28:29], v227 offset:53248\n\tds_read_b64_tr_b16 v[30:31], v227 offset:55296\n\t" \
    "v_mfma_f32_16x16x32_bf16 v[136:139], a[56:59], v[32:35], v[136:139]\n\tbuffer_load_dwordx4 a[188:191], v8, s[8:11], s68 offen offset:2048 sc1\n\ts_cmp_le_u32 s4, 15\n\ts_cselect_b32 s68, 3072, 0\n\ts_add_u32 s68, s68, s46\n\tv_mfma_f32_16x16x32_bf16 v[136:139], a[68:71], v[36:39], v[136:139]\n\ts_add_u32 s68, s68, 101376\n\ts_waitcnt lgkmcnt(8)\n\t" \
    "v_mfma_f32_16x16x32_bf16 v[124:127], a[48:51], v[40:43], v[124:127]\n\tds_read_b64_tr_b16 v[32:33], v224 offset:57344\n\tds_read_b64_tr_b16 v[34:35], v224 offset:59392\n\tv_mfma_f32_16x16x32_bf16 v[124:127], a[60:63], v[44:47], v[124:127]\n\tbuffer_load_dwordx4 a[192:195], v8, s[8:11], s68 offen offset:0 sc1\n\tv_mfma_f32_16x16x32_bf16 v[132:135], a[64:67], v[44:47], v[132:135]\n\tv_mfma_f32_16x16x32_bf16 v[132:135], a[52:55], v[40:43], v[132:135]\n\tds_read_b64_tr_b16 v[36:37], v224 offset:61440\n\t" \
    "ds_read_b64_tr_b16 v[38:39], v224 offset:63488\n\tv_mfma_f32_16x16x32_bf16 v[140:143], a[56:59], v[40:43], v[140:143]\n\tbuffer_load_dwordx4 a[196:199], v8, s[8:11], s68 offen offset:1024 sc1\n\tv_mfma_f32_16x16x32_bf16 v[140:143], a[68:71], v[44:47], v[140:143]\n\ts_waitcnt lgkmcnt(8)\n\tv_mfma_f32_16x16x32_bf16 v[144:147], a[48:51], v[16:19], v[144:147]\n\tds_read_b64_tr_b16 v[40:41], v225 offset:57344\n\tds_read_b64_tr_b16 v[42:43], v225 offset:59392\n\t" \
    "v_mfma_f32_16x16x32_bf16 v[144:147], a[60:63], v[20:23], v[144:147]\n\tv_mfma_f32_16x16x32_bf16 v[152:155], a[64:67], v[20:23], v[152:155]\n\tv_mfma_f32_16x16x32_bf16 v[152:155], a[52:55], v[16:19], v[152:155]\n\tds_read_b64_tr_b16 v[44:45], v225 offset:61440\n\tds_read_b64_tr_b16 v[46:47], v225 offset:63488\n\tv_mfma_f32_16x16x32_bf16 v[160:163], a[56:59], v[16:19], v[160:163]\n\tbuffer_load_dwordx4 a[200:203], v8, s[8:11], s68 offen offset:2048 sc1\n\ts_cmp_le_u32 s4, 16\n\t" \
    "s_cselect_b32 s68, 3072, 0\n\ts_add_u32 s68, s68, s46\n\tv_mfma_f32_16x16x32_bf16 v[160:163], a[68:71], v[20:23], v[160:163]\n\ts_add_u32 s68, s68, 104448\n\ts_waitcnt lgkmcnt(8)\n\tv_mfma_f32_16x16x32_bf16 v[148:151], a[48:51], v[24:27], v[148:151]\n\tds_read_b64_tr_b16 v[16:17], v4 offset:0\n\tds_read_b64_tr_b16 v[18:19], v4 offset:2048\n\t" \
    "v_mfma_f32_16x16x32_bf16 v[148:151], a[60:63], v[28:31], v[148:151]\n\tbuffer_load_dwordx4 a[204:207], v8, s[8:11], s68 offen offset:0 sc1\n\tv_mfma_f32_16x16x32_bf16 v[156:159], a[64:67], v[28:31], v[156:159]\n\tv_mfma_f32_16x16x32_bf16 v[156:159], a[52:55], v[24:27], v[156:159]\n\tds_read_b64_tr_b16 v[20:21], v4 offset:4096\n\tds_read_b64_tr_b16 v[22:23], v4 offset:6144\n\tv_mfma_f32_16x16x32_bf16 v[164:167], a[56:59], v[24:27], v[164:167]\n\tbuffer_load_dwordx4 a[208:211], v8, s[8:11], s68 offen offset:1024 sc1\n\t" \
    "v_mfma_f32_16x16x32_bf16 v[164:167], a[68:71], v[28:31], v[164:167]\n\ts_waitcnt lgkmcnt(8)\n\tv_mfma_f32_16x16x32_bf16 v[168:171], a[48:51], v[32:35], v[168:171]\n\tds_read_b64_tr_b16 v[24:25], v5 offset:0\n\tds_read_b64_tr_b16 v[26:27], v5 offset:2048\n\tv_mfma_f32_16x16x32_bf16 v[168:171], a[60:63], v[36:39], v[168:171]\n\tv_mfma_f32_16x16x32_bf16 v[176:179], a[64:67], v[36:39], v[176:179]\n\tv_mfma_f32_16x16x32_bf16 v[176:179], a[52:55], v[32:35], v[176:179]\n\t" \
    "ds_read_b64_tr_b16 v[28:29], v5 offset:4096\n\tds_read_b64_tr_b16 v[30:31], v5 offset:6144\n\tv_mfma_f32_16x16x32_bf16 v[184:187], a[56:59], v[32:35], v[184:187]\n\tbuffer_load_dwordx4 a[212:215], v8, s[8:11], s68 offen offset:2048 sc1\n\ts_cmp_le_u32 s4, 7\n\ts_cselect_b32 s69, 262144, 0\n\ts_add_u32 s69, s69, 1966080\n\tv_mfma_f32_16x16x32_bf16 v[184:187], a[68:71], v[36:39], v[184:187]\n\t" \
    "s_add_u32 m0, s54, 155648\n\ts_waitcnt lgkmcnt(8)\n\tv_mfma_f32_16x16x32_bf16 v[172:175], a[48:51], v[40:43], v[172:175]\n\tds_read_b64_tr_b16 v[32:33], v6 offset:0\n\tds_read_b64_tr_b16 v[34:35], v6 offset:2048\n\tv_mfma_f32_16x16x32_bf16 v[172:175], a[60:63], v[44:47], v[172:175]\n\tbuffer_load_dwordx4 v13, s[16:19], s69 offen lds\n\ts_cmp_le_u32 s4, 8\n\t" \
    "s_cselect_b32 s69, 262144, 0\n\ts_add_u32 s69, s69, 2228224\n\tv_mfma_f32_16x16x32_bf16 v[180:183], a[64:67], v[44:47], v[180:183]\n\ts_add_u32 m0, s54, 159744\n\tv_mfma_f32_16x16x32_bf16 v[180:183], a[52:55], v[40:43], v[180:183]\n\tds_read_b64_tr_b16 v[36:37], v6 offset:4096\n\tds_read_b64_tr_b16 v[38:39], v6 offset:6144\n\tv_mfma_f32_16x16x32_bf16 v[188:191], a[56:59], v[40:43], v[188:191]\n\t" \
    "v_mfma_f32_16x16x32_bf16 v[188:191], a[68:71], v[44:47], v[188:191]\n\ts_barrier\n\ts_waitcnt vmcnt(35) lgkmcnt(8)\n\tv_mfma_f32_16x16x32_bf16 v[120:123], a[72:75], v[16:19], v[120:123]\n\tds_read_b64_tr_b16 v[40:41], v7 offset:0\n\tds_read_b64_tr_b16 v[42:43], v7 offset:2048\n\tv_mfma_f32_16x16x32_bf16 v[120:123], a[84:87], v[20:23], v[120:123]\n\tbuffer_load_dwordx4 v13, s[16:19], s69 offen lds\n\t" \
    "s_cmp_le_u32 s4, 9\n\ts_cselect_b32 s69, 262144, 0\n\ts_add_u32 s69, s69, 2490368\n\tv_mfma_f32_16x16x32_bf16 v[128:131], a[88:91], v[20:23], v[128:131]\n\ts_add_u32 m0, s54, 32768\n\tv_mfma_f32_16x16x32_bf16 v[128:131], a[76:79], v[16:19], v[128:131]\n\tds_read_b64_tr_b16 v[44:45], v7 offset:4096\n\tds_read_b64_tr_b16 v[46:47], v7 offset:6144\n\t" \
    "v_mfma_f32_16x16x32_bf16 v[136:139], a[80:83], v[16:19], v[136:139]\n\tbuffer_load_dwordx4 v13, s[16:19], s69 offen lds\n\ts_cmp_le_u32 s4, 10\n\ts_cselect_b32 s69, 262144, 0\n\ts_add_u32 s69, s69, 2752512\n\tv_mfma_f32_16x16x32_bf16 v[136:139], a[92:95], v[20:23], v[136:139]\n\ts_add_u32 m0, s54, 36864\n\ts_waitcnt lgkmcnt(8)\n\t" \
    "v_mfma_f32_16x16x32_bf16 v[124:127], a[72:75], v[24:27], v[124:127]\n\tds_read_b64_tr_b16 v[16:17], v4 offset:8192\n\tds_read_b64_tr_b16 v[18:19], v4 offset:10240\n\tv_mfma_f32_16x16x32_bf16 v[124:127], a[84:87], v[28:31], v[124:127]\n\tbuffer_load_dwordx4 v13, s[16:19], s69 offen lds\n\ts_cmp_le_u32 s4, 11\n\ts_cselect_b32 s69, 262144, 0\n\ts_add_u32 s69, s69, 3014656\n\t" \
    "v_mfma_f32_16x16x32_bf16 v[132:135], a[88:91], v[28:31], v[132:135]\n\ts_add_u32 m0, s54, 40960\n\tv_mfma_f32_16x16x32_bf16 v[132:135], a[76:79], v[24:27], v[132:135]\n\tds_read_b64_tr_b16 v[20:21], v4 offset:12288\n\tds_read_b64_tr_b16 v[22:23], v4 offset:14336\n\tv_mfma_f32_16x16x32_bf16 v[140:143], a[80:83], v[24:27], v[140:143]\n\tbuffer_load_dwordx4 v13, s[16:19], s69 offen lds\n\ts_cmp_le_u32 s4, 12\n\t" \
    "s_cselect_b32 s69, 262144, 0\n\ts_add_u32 s69, s69, 3276800\n\tv_mfma_f32_16x16x32_bf16 v[140:143], a[92:95], v[28:31], v[140:143]\n\ts_add_u32 m0, s54, 45056\n\ts_waitcnt lgkmcnt(8)\n\tv_mfma_f32_16x16x32_bf16 v[144:147], a[72:75], v[32:35], v[144:147]\n\tds_read_b64_tr_b16 v[24:25], v5 offset:8192\n\tds_read_b64_tr_b16 v[26:27], v5 offset:10240\n\t" \
    "v_mfma_f32_16x16x32_bf16 v[144:147], a[84:87], v[36:39], v[144:147]\n\tbuffer_load_dwordx4 v13, s[16:19], s69 offen lds\n\ts_cmp_le_u32 s4, 13\n\ts_cselect_b32 s69, 262144, 0\n\ts_add_u32 s69, s69, 3538944\n\tv_mfma_f32_16x16x32_bf16 v[152:155], a[88:91], v[36:39], v[152:155]\n\ts_add_u32 m0, s54, 49152\n\tv_mfma_f32_16x16x32_bf16 v[152:155], a[76:79], v[32:35], v[152:155]\n\t" \
    "ds_read_b64_tr_b16 v[28:29], v5 offset:12288\n\tds_read_b64_tr_b16 v[30:31], v5 offset:14336\n\tv_mfma_f32_16x16x32_bf16 v[160:163], a[80:83], v[32:35], v[160:163]\n\tbuffer_load_dwordx4 v13, s[16:19], s69 offen lds\n\ts_cmp_le_u32 s4, 14\n\ts_cselect_b32 s69, 262144, 0\n\ts_add_u32 s69, s69, 3801088\n\tv_mfma_f32_16x16x32_bf16 v[160:163], a[92:95], v[36:39], v[160:163]\n\t" \
    "s_add_u32 m0, s54, 53248\n\ts_waitcnt lgkmcnt(8)\n\tv_mfma_f32_16x16x32_bf16 v[148:151], a[72:75], v[40:43], v[148:151]\n\tds_read_b64_tr_b16 v[32:33], v4 offset:16384\n\tds_read_b64_tr_b16 v[34:35], v4 offset:18432\n\tv_mfma_f32_16x16x32_bf16 v[148:151], a[84:87], v[44:47], v[148:151]\n\tv_mfma_f32_16x16x32_bf16 v[156:159], a[88:91], v[44:47], v[156:159]\n\tv_mfma_f32_16x16x32_bf16 v[156:159], a[76:79], v[40:43], v[156:159]\n\t" \
    "ds_read_b64_tr_b16 v[36:37], v4 offset:20480\n\tds_read_b64_tr_b16 v[38:39], v4 offset:22528\n\tv_mfma_f32_16x16x32_bf16 v[164:167], a[80:83], v[40:43], v[164:167]\n\tbuffer_load_dwordx4 v13, s[16:19], s69 offen lds\n\ts_cmp_le_u32 s4, 15\n\ts_cselect_b32 s69, 262144, 0\n\ts_add_u32 s69, s69, 4063232\n\tv_mfma_f32_16x16x32_bf16 v[164:167], a[92:95], v[44:47], v[164:167]\n\t" \
    "s_add_u32 m0, s54, 57344\n\ts_waitcnt lgkmcnt(8)\n\tv_mfma_f32_16x16x32_bf16 v[168:171], a[72:75], v[16:19], v[168:171]\n\tds_read_b64_tr_b16 v[40:41], v5 offset:16384\n\tds_read_b64_tr_b16 v[42:43], v5 offset:18432\n\tv_mfma_f32_16x16x32_bf16 v[168:171], a[84:87], v[20:23], v[168:171]\n\tbuffer_load_dwordx4 v13, s[16:19], s69 offen lds\n\ts_cmp_le_u32 s4, 16\n\t" \
    "s_cselect_b32 s69, 262144, 0\n\ts_add_u32 s69, s69, 4325376\n\tv_mfma_f32_16x16x32_bf16 v[176:179], a[88:91], v[20:23], v[176:179]\n\ts_add_u32 m0, s54, 61440\n\tv_mfma_f32_16x16x32_bf16 v[176:179], a[76:79], v[16:19], v[176:179]\n\tds_read_b64_tr_b16 v[44:45], v5 offset:20480\n\tds_read_b64_tr_b16 v[46:47], v5 offset:22528\n\tv_mfma_f32_16x16x32_bf16 v[184:187], a[80:83], v[16:19], v[184:187]\n\t" \
    "buffer_load_dwordx4 v13, s[16:19], s69 offen lds\n\tv_mfma_f32_16x16x32_bf16 v[184:187], a[92:95], v[20:23], v[184:187]\n\ts_waitcnt lgkmcnt(8)\n\tv_mfma_f32_16x16x32_bf16 v[172:175], a[72:75], v[24:27], v[172:175]\n\tds_read_b64_tr_b16 v[16:17], v220 offset:0\n\tds_read_b64_tr_b16 v[18:19], v220 offset:2048\n\tv_mfma_f32_16x16x32_bf16 v[172:175], a[84:87], v[28:31], v[172:175]\n\tv_mfma_f32_16x16x32_bf16 v[180:183], a[88:91], v[28:31], v[180:183]\n\t" \
    "v_mfma_f32_16x16x32_bf16 v[180:183], a[76:79], v[24:27], v[180:183]\n\tds_read_b64_tr_b16 v[20:21], v220 offset:4096\n\tds_read_b64_tr_b16 v[22:23], v220 offset:6144\n\tv_mfma_f32_16x16x32_bf16 v[188:191], a[80:83], v[24:27], v[188:191]\n\tbuffer_load_dwordx4 v[192:195], v243, s[20:23], 0 offen offset:0\n\tv_mfma_f32_16x16x32_bf16 v[188:191], a[92:95], v[28:31], v[188:191]\n\ts_waitcnt vmcnt(39) lgkmcnt(8)\n\tv_mfma_f32_16x16x32_bf16 v[120:123], a[96:99], v[32:35], v[120:123]\n\t" \
    "ds_read_b64_tr_b16 v[24:25], v221 offset:0\n\tds_read_b64_tr_b16 v[26:27], v221 offset:2048\n\tv_mfma_f32_16x16x32_bf16 v[120:123], a[108:111], v[36:39], v[120:123]\n\tbuffer_load_dwordx4 v[196:199], v243, s[20:23], 0 offen offset:32\n\tv_mfma_f32_16x16x32_bf16 v[128:131], a[112:115], v[36:39], v[128:131]\n\tv_mfma_f32_16x16x32_bf16 v[128:131], a[100:103], v[32:35], v[128:131]\n\tds_read_b64_tr_b16 v[28:29], v221 offset:4096\n\tds_read_b64_tr_b16 v[30:31], v221 offset:6144\n\t" \
    "v_mfma_f32_16x16x32_bf16 v[136:139], a[104:107], v[32:35], v[136:139]\n\tv_mfma_f32_16x16x32_bf16 v[136:139], a[116:119], v[36:39], v[136:139]\n\ts_waitcnt lgkmcnt(8)\n\tv_mfma_f32_16x16x32_bf16 v[124:127], a[96:99], v[40:43], v[124:127]\n\tds_read_b64_tr_b16 v[32:33], v4 offset:24576\n\tds_read_b64_tr_b16 v[34:35], v4 offset:26624\n\tv_mfma_f32_16x16x32_bf16 v[124:127], a[108:111], v[44:47], v[124:127]\n\tbuffer_load_dwordx4 v[200:203], v243, s[20:23], 0 offen offset:64\n\t" \
    "v_mfma_f32_16x16x32_bf16 v[132:135], a[112:115], v[44:47], v[132:135]\n\tv_mfma_f32_16x16x32_bf16 v[132:135], a[100:103], v[40:43], v[132:135]\n\tds_read_b64_tr_b16 v[36:37], v4 offset:28672\n\tds_read_b64_tr_b16 v[38:39], v4 offset:30720\n\tv_mfma_f32_16x16x32_bf16 v[140:143], a[104:107], v[40:43], v[140:143]\n\tbuffer_load_dwordx2 v[216:217], v242, s[8:11], s59 offen offset:0 sc1\n\tv_mfma_f32_16x16x32_bf16 v[140:143], a[116:119], v[44:47], v[140:143]\n\ts_waitcnt vmcnt(12)\n\t" \
    "s_barrier\n\ts_waitcnt lgkmcnt(8)\n\tv_mfma_f32_16x16x32_bf16 v[120:123], a[120:123], v[16:19], v[120:123]\n\tds_read_b64_tr_b16 v[40:41], v5 offset:24576\n\tds_read_b64_tr_b16 v[42:43], v5 offset:26624\n\tv_mfma_f32_16x16x32_bf16 v[120:123], a[132:135], v[20:23], v[120:123]\n\tbuffer_load_dwordx2 v[218:219], v242, s[8:11], s59 offen offset:128 sc1\n\tv_mfma_f32_16x16x32_bf16 v[128:131], a[136:139], v[20:23], v[128:131]\n\t" \
    "v_mfma_f32_16x16x32_bf16 v[128:131], a[124:127], v[16:19], v[128:131]\n\tds_read_b64_tr_b16 v[44:45], v5 offset:28672\n\tds_read_b64_tr_b16 v[46:47], v5 offset:30720\n\tv_mfma_f32_16x16x32_bf16 v[136:139], a[128:131], v[16:19], v[136:139]\n\tv_mfma_f32_16x16x32_bf16 v[136:139], a[140:143], v[20:23], v[136:139]\n\ts_waitcnt lgkmcnt(8)\n\tv_mfma_f32_16x16x32_bf16 v[124:127], a[120:123], v[24:27], v[124:127]\n\tds_read_b64_tr_b16 v[16:17], v220 offset:8192\n\t" \
    "ds_read_b64_tr_b16 v[18:19], v220 offset:10240\n\tv_mfma_f32_16x16x32_bf16 v[124:127], a[132:135], v[28:31], v[124:127]\n\tv_mfma_f32_16x16x32_bf16 v[132:135], a[136:139], v[28:31], v[132:135]\n\tv_mfma_f32_16x16x32_bf16 v[132:135], a[124:127], v[24:27], v[132:135]\n\tds_read_b64_tr_b16 v[20:21], v220 offset:12288\n\tds_read_b64_tr_b16 v[22:23], v220 offset:14336\n\tv_mfma_f32_16x16x32_bf16 v[140:143], a[128:131], v[24:27], v[140:143]\n\tv_mfma_f32_16x16x32_bf16 v[140:143], a[140:143], v[28:31], v[140:143]\n\t" \
    "s_waitcnt lgkmcnt(8)\n\tv_mfma_f32_16x16x32_bf16 v[120:123], a[144:147], v[32:35], v[120:123]\n\tds_read_b64_tr_b16 v[24:25], v221 offset:8192\n\tds_read_b64_tr_b16 v[26:27], v221 offset:10240\n\tv_mfma_f32_16x16x32_bf16 v[120:123], a[156:159], v[36:39], v[120:123]\n\tv_mfma_f32_16x16x32_bf16 v[128:131], a[160:163], v[36:39], v[128:131]\n\tv_mfma_f32_16x16x32_bf16 v[128:131], a[148:151], v[32:35], v[128:131]\n\tds_read_b64_tr_b16 v[28:29], v221 offset:12288\n\t" \
    "ds_read_b64_tr_b16 v[30:31], v221 offset:14336\n\tv_mfma_f32_16x16x32_bf16 v[136:139], a[152:155], v[32:35], v[136:139]\n\tv_mfma_f32_16x16x32_bf16 v[136:139], a[164:167], v[36:39], v[136:139]\n\ts_waitcnt lgkmcnt(8)\n\tv_mfma_f32_16x16x32_bf16 v[124:127], a[144:147], v[40:43], v[124:127]\n\tds_read_b64_tr_b16 v[32:33], v220 offset:16384\n\tds_read_b64_tr_b16 v[34:35], v220 offset:18432\n\tv_mfma_f32_16x16x32_bf16 v[124:127], a[156:159], v[44:47], v[124:127]\n\t" \
    "v_mfma_f32_16x16x32_bf16 v[132:135], a[160:163], v[44:47], v[132:135]\n\tv_mfma_f32_16x16x32_bf16 v[132:135], a[148:151], v[40:43], v[132:135]\n\tds_read_b64_tr_b16 v[36:37], v220 offset:20480\n\tds_read_b64_tr_b16 v[38:39], v220 offset:22528\n\tv_mfma_f32_16x16x32_bf16 v[140:143], a[152:155], v[40:43], v[140:143]\n\tv_mfma_f32_16x16x32_bf16 v[140:143], a[164:167], v[44:47], v[140:143]\n\ts_waitcnt lgkmcnt(8)\n\tv_mfma_f32_16x16x32_bf16 v[120:123], a[168:171], v[16:19], v[120:123]\n\t" \
    "ds_read_b64_tr_b16 v[40:41], v221 offset:16384\n\tds_read_b64_tr_b16 v[42:43], v221 offset:18432\n\tv_mfma_f32_16x16x32_bf16 v[120:123], a[180:183], v[20:23], v[120:123]\n\tv_mfma_f32_16x16x32_bf16 v[128:131], a[184:187], v[20:23], v[128:131]\n\tv_mfma_f32_16x16x32_bf16 v[128:131], a[172:175], v[16:19], v[128:131]\n\tds_read_b64_tr_b16 v[44:45], v221 offset:20480\n\tds_read_b64_tr_b16 v[46:47], v221 offset:22528\n\tv_mfma_f32_16x16x32_bf16 v[136:139], a[176:179], v[16:19], v[136:139]\n\t" \
    "v_mfma_f32_16x16x32_bf16 v[136:139], a[188:191], v[20:23], v[136:139]\n\ts_waitcnt lgkmcnt(8)\n\tv_mfma_f32_16x16x32_bf16 v[124:127], a[168:171], v[24:27], v[124:127]\n\tds_read_b64_tr_b16 v[16:17], v6 offset:16384\n\tds_read_b64_tr_b16 v[18:19], v6 offset:18432\n\tv_mfma_f32_16x16x32_bf16 v[124:127], a[180:183], v[28:31], v[124:127]\n\tv_mfma_f32_16x16x32_bf16 v[132:135], a[184:187], v[28:31], v[132:135]\n\tv_mfma_f32_16x16x32_bf16 v[132:135], a[172:175], v[24:27], v[132:135]\n\t" \
    "ds_read_b64_tr_b16 v[20:21], v6 offset:20480\n\tds_read_b64_tr_b16 v[22:23], v6 offset:22528\n\tv_mfma_f32_16x16x32_bf16 v[140:143], a[176:179], v[24:27], v[140:143]\n\tv_mfma_f32_16x16x32_bf16 v[140:143], a[188:191], v[28:31], v[140:143]\n\ts_waitcnt lgkmcnt(8)\n\tv_mfma_f32_16x16x32_bf16 v[120:123], a[192:195], v[32:35], v[120:123]\n\tds_read_b64_tr_b16 v[24:25], v7 offset:16384\n\tds_read_b64_tr_b16 v[26:27], v7 offset:18432\n\t" \
    "v_mfma_f32_16x16x32_bf16 v[120:123], a[204:207], v[36:39], v[120:123]\n\tv_mfma_f32_16x16x32_bf16 v[128:131], a[208:211], v[36:39], v[128:131]\n\tv_mfma_f32_16x16x32_bf16 v[128:131], a[196:199], v[32:35], v[128:131]\n\tds_read_b64_tr_b16 v[28:29], v7 offset:20480\n\tds_read_b64_tr_b16 v[30:31], v7 offset:22528\n\tv_mfma_f32_16x16x32_bf16 v[136:139], a[200:203], v[32:35], v[136:139]\n\tv_mfma_f32_16x16x32_bf16 v[136:139], a[212:215], v[36:39], v[136:139]\n\ts_waitcnt lgkmcnt(8)\n\t" \
    "v_mfma_f32_16x16x32_bf16 v[124:127], a[192:195], v[40:43], v[124:127]\n\tds_read_b64_tr_b16 v[32:33], v222 offset:0\n\tds_read_b64_tr_b16 v[34:35], v222 offset:2048\n\tv_mfma_f32_16x16x32_bf16 v[124:127], a[204:207], v[44:47], v[124:127]\n\tv_mfma_f32_16x16x32_bf16 v[132:135], a[208:211], v[44:47], v[132:135]\n\tv_mfma_f32_16x16x32_bf16 v[132:135], a[196:199], v[40:43], v[132:135]\n\tds_read_b64_tr_b16 v[36:37], v222 offset:4096\n\tds_read_b64_tr_b16 v[38:39], v222 offset:6144\n\t" \
    "v_mfma_f32_16x16x32_bf16 v[140:143], a[200:203], v[40:43], v[140:143]\n\tv_mfma_f32_16x16x32_bf16 v[140:143], a[212:215], v[44:47], v[140:143]\n\ts_waitcnt lgkmcnt(8)\n\tv_mfma_f32_16x16x32_bf16 v[144:147], a[96:99], v[16:19], v[144:147]\n\tds_read_b64_tr_b16 v[40:41], v223 offset:0\n\tds_read_b64_tr_b16 v[42:43], v223 offset:2048\n\ts_xor_b32 s68, s53, 12\n\tv_mfma_f32_16x16x32_bf16 v[144:147], a[108:111], v[20:23], v[144:147]\n\t" \
    "s_mul_i32 s68, s68, 9984\n\ts_add_u32 s68, s68, 7208960\n\tv_cvt_pk_bf16_f32 v120, v120, v121\n\tv_mfma_f32_16x16x32_bf16 v[152:155], a[112:115], v[20:23], v[152:155]\n\tv_cvt_pk_bf16_f32 v121, v122, v123\n\tv_cvt_pk_bf16_f32 v122, v124, v125\n\tv_cvt_pk_bf16_f32 v123, v126, v127\n\tv_mfma_f32_16x16x32_bf16 v[152:155], a[100:103], v[16:19], v[152:155]\n\t" \
    "ds_read_b64_tr_b16 v[44:45], v223 offset:4096\n\tds_read_b64_tr_b16 v[46:47], v223 offset:6144\n\tv_mfma_f32_16x16x32_bf16 v[160:163], a[104:107], v[16:19], v[160:163]\n\tbuffer_store_dwordx4 v[120:123], v8, s[8:11], s68 offen offset:0 sc1\n\tv_cvt_pk_bf16_f32 v124, v128, v129\n\tv_cvt_pk_bf16_f32 v125, v130, v131\n\tv_mfma_f32_16x16x32_bf16 v[160:163], a[116:119], v[20:23], v[160:163]\n\tv_cvt_pk_bf16_f32 v126, v132, v133\n\t" \
    "v_cvt_pk_bf16_f32 v127, v134, v135\n\ts_waitcnt lgkmcnt(8)\n\tv_mfma_f32_16x16x32_bf16 v[148:151], a[96:99], v[24:27], v[148:151]\n\tds_read_b64_tr_b16 v[16:17], v6 offset:24576\n\tds_read_b64_tr_b16 v[18:19], v6 offset:26624\n\tv_mfma_f32_16x16x32_bf16 v[148:151], a[108:111], v[28:31], v[148:151]\n\tbuffer_store_dwordx4 v[124:127], v8, s[8:11], s68 offen offset:1024 sc1\n\tv_cvt_pk_bf16_f32 v128, v136, v137\n\t" \
    "v_cvt_pk_bf16_f32 v129, v138, v139\n\tv_mfma_f32_16x16x32_bf16 v[156:159], a[112:115], v[28:31], v[156:159]\n\tv_cvt_pk_bf16_f32 v130, v140, v141\n\tv_cvt_pk_bf16_f32 v131, v142, v143\n\tv_mfma_f32_16x16x32_bf16 v[156:159], a[100:103], v[24:27], v[156:159]\n\tds_read_b64_tr_b16 v[20:21], v6 offset:28672\n\tds_read_b64_tr_b16 v[22:23], v6 offset:30720\n\tv_mfma_f32_16x16x32_bf16 v[164:167], a[104:107], v[24:27], v[164:167]\n\t" \
    "buffer_store_dwordx4 v[128:131], v8, s[8:11], s68 offen offset:2048 sc1\n\tv_add3_u32 v132, s36, v120, v121\n\tv_add3_u32 v133, v122, v123, v124\n\tv_mfma_f32_16x16x32_bf16 v[164:167], a[116:119], v[28:31], v[164:167]\n\tv_add3_u32 v132, v132, v125, v126\n\tv_add3_u32 v133, v133, v127, v128\n\ts_waitcnt lgkmcnt(8)\n\tv_mfma_f32_16x16x32_bf16 v[144:147], a[120:123], v[32:35], v[144:147]\n\t" \
    "ds_read_b64_tr_b16 v[24:25], v7 offset:24576\n\tds_read_b64_tr_b16 v[26:27], v7 offset:26624\n\tv_add3_u32 v132, v132, v129, v130\n\tv_mfma_f32_16x16x32_bf16 v[144:147], a[132:135], v[36:39], v[144:147]\n\tv_add3_u32 v132, v132, v133, v131\n\tv_mfma_f32_16x16x32_bf16 v[152:155], a[136:139], v[36:39], v[152:155]\n\tv_mfma_f32_16x16x32_bf16 v[152:155], a[124:127], v[32:35], v[152:155]\n\tds_read_b64_tr_b16 v[28:29], v7 offset:28672\n\t" \
    "ds_read_b64_tr_b16 v[30:31], v7 offset:30720\n\tv_mfma_f32_16x16x32_bf16 v[160:163], a[128:131], v[32:35], v[160:163]\n\tbuffer_store_dword v132, v236, s[8:11], s68 offen offset:3072 sc1\n\tv_mfma_f32_16x16x32_bf16 v[160:163], a[140:143], v[36:39], v[160:163]\n\ts_waitcnt lgkmcnt(8)\n\tv_mfma_f32_16x16x32_bf16 v[148:151], a[120:123], v[40:43], v[148:151]\n\tds_read_b64_tr_b16 v[32:33], v222 offset:8192\n\tds_read_b64_tr_b16 v[34:35], v222 offset:10240\n\t" \
    "v_mfma_f32_16x16x32_bf16 v[148:151], a[132:135], v[44:47], v[148:151]\n\tv_mfma_f32_16x16x32_bf16 v[156:159], a[136:139], v[44:47], v[156:159]\n\tv_mfma_f32_16x16x32_bf16 v[156:159], a[124:127], v[40:43], v[156:159]\n\tds_read_b64_tr_b16 v[36:37], v222 offset:12288\n\tds_read_b64_tr_b16 v[38:39], v222 offset:14336\n\tv_mfma_f32_16x16x32_bf16 v[164:167], a[128:131], v[40:43], v[164:167]\n\tv_mfma_f32_16x16x32_bf16 v[164:167], a[140:143], v[44:47], v[164:167]\n\ts_waitcnt lgkmcnt(8)\n\t" \
    "v_mfma_f32_16x16x32_bf16 v[144:147], a[144:147], v[16:19], v[144:147]\n\tds_read_b64_tr_b16 v[40:41], v223 offset:8192\n\tds_read_b64_tr_b16 v[42:43], v223 offset:10240\n\tv_mfma_f32_16x16x32_bf16 v[144:147], a[156:159], v[20:23], v[144:147]\n\tv_mfma_f32_16x16x32_bf16 v[152:155], a[160:163], v[20:23], v[152:155]\n\tv_mfma_f32_16x16x32_bf16 v[152:155], a[148:151], v[16:19], v[152:155]\n\tds_read_b64_tr_b16 v[44:45], v223 offset:12288\n\tds_read_b64_tr_b16 v[46:47], v223 offset:14336\n\t" \
    "v_mfma_f32_16x16x32_bf16 v[160:163], a[152:155], v[16:19], v[160:163]\n\tv_mfma_f32_16x16x32_bf16 v[160:163], a[164:167], v[20:23], v[160:163]\n\ts_waitcnt lgkmcnt(8)\n\tv_mfma_f32_16x16x32_bf16 v[148:151], a[144:147], v[24:27], v[148:151]\n\tds_read_b64_tr_b16 v[16:17], v222 offset:16384\n\tds_read_b64_tr_b16 v[18:19], v222 offset:18432\n\tv_mfma_f32_16x16x32_bf16 v[148:151], a[156:159], v[28:31], v[148:151]\n\tv_mfma_f32_16x16x32_bf16 v[156:159], a[160:163], v[28:31], v[156:159]\n\t" \
    "v_mfma_f32_16x16x32_bf16 v[156:159], a[148:151], v[24:27], v[156:159]\n\tds_read_b64_tr_b16 v[20:21], v222 offset:20480\n\tds_read_b64_tr_b16 v[22:23], v222 offset:22528\n\tv_mfma_f32_16x16x32_bf16 v[164:167], a[152:155], v[24:27], v[164:167]\n\tv_mfma_f32_16x16x32_bf16 v[164:167], a[164:167], v[28:31], v[164:167]\n\ts_waitcnt lgkmcnt(8)\n\tv_mfma_f32_16x16x32_bf16 v[144:147], a[168:171], v[32:35], v[144:147]\n\tds_read_b64_tr_b16 v[24:25], v223 offset:16384\n\t" \
    "ds_read_b64_tr_b16 v[26:27], v223 offset:18432\n\tv_mfma_f32_16x16x32_bf16 v[144:147], a[180:183], v[36:39], v[144:147]\n\tv_mfma_f32_16x16x32_bf16 v[152:155], a[184:187], v[36:39], v[152:155]\n\tv_mfma_f32_16x16x32_bf16 v[152:155], a[172:175], v[32:35], v[152:155]\n\tds_read_b64_tr_b16 v[28:29], v223 offset:20480\n\tds_read_b64_tr_b16 v[30:31], v223 offset:22528\n\tv_mfma_f32_16x16x32_bf16 v[160:163], a[176:179], v[32:35], v[160:163]\n\tv_mfma_f32_16x16x32_bf16 v[160:163], a[188:191], v[36:39], v[160:163]\n\t" \
    "s_waitcnt lgkmcnt(8)\n\tv_mfma_f32_16x16x32_bf16 v[148:151], a[168:171], v[40:43], v[148:151]\n\tds_read_b64_tr_b16 v[32:33], v220 offset:24576\n\tds_read_b64_tr_b16 v[34:35], v220 offset:26624\n\tv_mfma_f32_16x16x32_bf16 v[148:151], a[180:183], v[44:47], v[148:151]\n\tv_mfma_f32_16x16x32_bf16 v[156:159], a[184:187], v[44:47], v[156:159]\n\tv_mfma_f32_16x16x32_bf16 v[156:159], a[172:175], v[40:43], v[156:159]\n\tds_read_b64_tr_b16 v[36:37], v220 offset:28672\n\t" \
    "ds_read_b64_tr_b16 v[38:39], v220 offset:30720\n\tv_mfma_f32_16x16x32_bf16 v[164:167], a[176:179], v[40:43], v[164:167]\n\tv_mfma_f32_16x16x32_bf16 v[164:167], a[188:191], v[44:47], v[164:167]\n\ts_waitcnt vmcnt(9)\n\ts_barrier\n\ts_waitcnt lgkmcnt(8)\n\tv_mfma_f32_16x16x32_bf16 v[144:147], a[192:195], v[16:19], v[144:147]\n\tds_read_b64_tr_b16 v[40:41], v221 offset:24576\n\t" \
    "ds_read_b64_tr_b16 v[42:43], v221 offset:26624\n\tv_mfma_f32_16x16x32_bf16 v[144:147], a[204:207], v[20:23], v[144:147]\n\tv_mfma_f32_16x16x32_bf16 v[152:155], a[208:211], v[20:23], v[152:155]\n\tv_mfma_f32_16x16x32_bf16 v[152:155], a[196:199], v[16:19], v[152:155]\n\tds_read_b64_tr_b16 v[44:45], v221 offset:28672\n\tds_read_b64_tr_b16 v[46:47], v221 offset:30720\n\tv_mfma_f32_16x16x32_bf16 v[160:163], a[200:203], v[16:19], v[160:163]\n\tv_mfma_f32_16x16x32_bf16 v[160:163], a[212:215], v[20:23], v[160:163]\n\t" \
    "v_min_u32 v241, 376, v239\n\tbuffer_load_dwordx2 v[250:251], v241, s[8:11], s51 offen sc1\n\ts_waitcnt lgkmcnt(8)\n\tv_mfma_f32_16x16x32_bf16 v[148:151], a[192:195], v[24:27], v[148:151]\n\tds_read_b64_tr_b16 v[16:17], v4 offset:32768\n\tds_read_b64_tr_b16 v[18:19], v4 offset:34816\n\tv_mfma_f32_16x16x32_bf16 v[148:151], a[204:207], v[28:31], v[148:151]\n\tv_mfma_f32_16x16x32_bf16 v[156:159], a[208:211], v[28:31], v[156:159]\n\t" \
    "v_mfma_f32_16x16x32_bf16 v[156:159], a[196:199], v[24:27], v[156:159]\n\tds_read_b64_tr_b16 v[20:21], v4 offset:36864\n\tds_read_b64_tr_b16 v[22:23], v4 offset:38912\n\tv_mfma_f32_16x16x32_bf16 v[164:167], a[200:203], v[24:27], v[164:167]\n\tv_mfma_f32_16x16x32_bf16 v[164:167], a[212:215], v[28:31], v[164:167]\n\ts_waitcnt lgkmcnt(8)\n\tv_mfma_f32_16x16x32_bf16 v[168:171], a[96:99], v[32:35], v[168:171]\n\tds_read_b64_tr_b16 v[24:25], v5 offset:32768\n\t" \
    "ds_read_b64_tr_b16 v[26:27], v5 offset:34816\n\ts_xor_b32 s68, s53, 8\n\tv_mfma_f32_16x16x32_bf16 v[168:171], a[108:111], v[36:39], v[168:171]\n\ts_mul_i32 s68, s68, 9984\n\ts_add_u32 s68, s68, 7212288\n\tv_cvt_pk_bf16_f32 v144, v144, v145\n\tv_mfma_f32_16x16x32_bf16 v[176:179], a[112:115], v[36:39], v[176:179]\n\tv_cvt_pk_bf16_f32 v145, v146, v147\n\t" \
    "v_cvt_pk_bf16_f32 v146, v148, v149\n\tv_cvt_pk_bf16_f32 v147, v150, v151\n\tv_mfma_f32_16x16x32_bf16 v[176:179], a[100:103], v[32:35], v[176:179]\n\tds_read_b64_tr_b16 v[28:29], v5 offset:36864\n\tds_read_b64_tr_b16 v[30:31], v5 offset:38912\n\tv_mfma_f32_16x16x32_bf16 v[184:187], a[104:107], v[32:35], v[184:187]\n\tbuffer_store_dwordx4 v[144:147], v8, s[8:11], s68 offen offset:0 sc1\n\tv_cvt_pk_bf16_f32 v148, v152, v153\n\t" \
    "v_cvt_pk_bf16_f32 v149, v154, v155\n\tv_mfma_f32_16x16x32_bf16 v[184:187], a[116:119], v[36:39], v[184:187]\n\tv_cvt_pk_bf16_f32 v150, v156, v157\n\tv_cvt_pk_bf16_f32 v151, v158, v159\n\ts_waitcnt lgkmcnt(8)\n\tv_mfma_f32_16x16x32_bf16 v[172:175], a[96:99], v[40:43], v[172:175]\n\tds_read_b64_tr_b16 v[32:33], v4 offset:40960\n\tds_read_b64_tr_b16 v[34:35], v4 offset:43008\n\t" \
    "v_mfma_f32_16x16x32_bf16 v[172:175], a[108:111], v[44:47], v[172:175]\n\tbuffer_store_dwordx4 v[148:151], v8, s[8:11], s68 offen offset:1024 sc1\n\tv_cvt_pk_bf16_f32 v152, v160, v161\n\tv_cvt_pk_bf16_f32 v153, v162, v163\n\tv_mfma_f32_16x16x32_bf16 v[180:183], a[112:115], v[44:47], v[180:183]\n\tv_cvt_pk_bf16_f32 v154, v164, v165\n\tv_cvt_pk_bf16_f32 v155, v166, v167\n\tv_mfma_f32_16x16x32_bf16 v[180:183], a[100:103], v[40:43], v[180:183]\n\t" \
    "ds_read_b64_tr_b16 v[36:37], v4 offset:45056\n\tds_read_b64_tr_b16 v[38:39], v4 offset:47104\n\tv_mfma_f32_16x16x32_bf16 v[188:191], a[104:107], v[40:43], v[188:191]\n\tbuffer_store_dwordx4 v[152:155], v8, s[8:11], s68 offen offset:2048 sc1\n\tv_add3_u32 v156, s36, v144, v145\n\tv_add3_u32 v157, v146, v147, v148\n\tv_mfma_f32_16x16x32_bf16 v[188:191], a[116:119], v[44:47], v[188:191]\n\tv_add3_u32 v156, v156, v149, v150\n\t" \
    "v_add3_u32 v157, v157, v151, v152\n\ts_waitcnt lgkmcnt(8)\n\tv_mfma_f32_16x16x32_bf16 v[168:171], a[120:123], v[16:19], v[168:171]\n\tds_read_b64_tr_b16 v[40:41], v5 offset:40960\n\tds_read_b64_tr_b16 v[42:43], v5 offset:43008\n\tv_add3_u32 v156, v156, v153, v154\n\tv_mfma_f32_16x16x32_bf16 v[168:171], a[132:135], v[20:23], v[168:171]\n\tv_add3_u32 v156, v156, v157, v155\n\t" \
    "v_mfma_f32_16x16x32_bf16 v[176:179], a[136:139], v[20:23], v[176:179]\n\tv_mfma_f32_16x16x32_bf16 v[176:179], a[124:127], v[16:19], v[176:179]\n\tds_read_b64_tr_b16 v[44:45], v5 offset:45056\n\tds_read_b64_tr_b16 v[46:47], v5 offset:47104\n\tv_mfma_f32_16x16x32_bf16 v[184:187], a[128:131], v[16:19], v[184:187]\n\tbuffer_store_dword v156, v236, s[8:11], s68 offen offset:3072 sc1\n\tv_mfma_f32_16x16x32_bf16 v[184:187], a[140:143], v[20:23], v[184:187]\n\ts_waitcnt lgkmcnt(8)\n\t" \
    "v_mfma_f32_16x16x32_bf16 v[172:175], a[120:123], v[24:27], v[172:175]\n\tds_read_b64_tr_b16 v[16:17], v4 offset:49152\n\tds_read_b64_tr_b16 v[18:19], v4 offset:51200\n\tv_mfma_f32_16x16x32_bf16 v[172:175], a[132:135], v[28:31], v[172:175]\n\tv_mfma_f32_16x16x32_bf16 v[180:183], a[136:139], v[28:31], v[180:183]\n\tv_mfma_f32_16x16x32_bf16 v[180:183], a[124:127], v[24:27], v[180:183]\n\tds_read_b64_tr_b16 v[20:21], v4 offset:53248\n\tds_read_b64_tr_b16 v[22:23], v4 offset:55296\n\t" \
    "v_mfma_f32_16x16x32_bf16 v[188:191], a[128:131], v[24:27], v[188:191]\n\tv_mfma_f32_16x16x32_bf16 v[188:191], a[140:143], v[28:31], v[188:191]\n\ts_waitcnt lgkmcnt(8)\n\tv_mfma_f32_16x16x32_bf16 v[168:171], a[144:147], v[32:35], v[168:171]\n\tds_read_b64_tr_b16 v[24:25], v5 offset:49152\n\tds_read_b64_tr_b16 v[26:27], v5 offset:51200\n\tv_mfma_f32_16x16x32_bf16 v[168:171], a[156:159], v[36:39], v[168:171]\n\tv_mfma_f32_16x16x32_bf16 v[176:179], a[160:163], v[36:39], v[176:179]\n\t" \
    "v_mfma_f32_16x16x32_bf16 v[176:179], a[148:151], v[32:35], v[176:179]\n\tds_read_b64_tr_b16 v[28:29], v5 offset:53248\n\tds_read_b64_tr_b16 v[30:31], v5 offset:55296\n\tv_mfma_f32_16x16x32_bf16 v[184:187], a[152:155], v[32:35], v[184:187]\n\tv_mfma_f32_16x16x32_bf16 v[184:187], a[164:167], v[36:39], v[184:187]\n\ts_waitcnt lgkmcnt(8)\n\tv_mfma_f32_16x16x32_bf16 v[172:175], a[144:147], v[40:43], v[172:175]\n\tds_read_b64_tr_b16 v[32:33], v4 offset:57344\n\t" \
    "ds_read_b64_tr_b16 v[34:35], v4 offset:59392\n\tv_mfma_f32_16x16x32_bf16 v[172:175], a[156:159], v[44:47], v[172:175]\n\tv_mfma_f32_16x16x32_bf16 v[180:183], a[160:163], v[44:47], v[180:183]\n\tv_mfma_f32_16x16x32_bf16 v[180:183], a[148:151], v[40:43], v[180:183]\n\tds_read_b64_tr_b16 v[36:37], v4 offset:61440\n\tds_read_b64_tr_b16 v[38:39], v4 offset:63488\n\tv_mfma_f32_16x16x32_bf16 v[188:191], a[152:155], v[40:43], v[188:191]\n\tv_mfma_f32_16x16x32_bf16 v[188:191], a[164:167], v[44:47], v[188:191]\n\t" \
    "s_waitcnt lgkmcnt(8)\n\tv_mfma_f32_16x16x32_bf16 v[168:171], a[168:171], v[16:19], v[168:171]\n\tds_read_b64_tr_b16 v[40:41], v5 offset:57344\n\tds_read_b64_tr_b16 v[42:43], v5 offset:59392\n\tv_mfma_f32_16x16x32_bf16 v[168:171], a[180:183], v[20:23], v[168:171]\n\tv_mfma_f32_16x16x32_bf16 v[176:179], a[184:187], v[20:23], v[176:179]\n\tv_mfma_f32_16x16x32_bf16 v[176:179], a[172:175], v[16:19], v[176:179]\n\tds_read_b64_tr_b16 v[44:45], v5 offset:61440\n\t" \
    "ds_read_b64_tr_b16 v[46:47], v5 offset:63488\n\tv_mfma_f32_16x16x32_bf16 v[184:187], a[176:179], v[16:19], v[184:187]\n\tv_mfma_f32_16x16x32_bf16 v[184:187], a[188:191], v[20:23], v[184:187]\n\ts_waitcnt lgkmcnt(8)\n\tv_mfma_f32_16x16x32_bf16 v[172:175], a[168:171], v[24:27], v[172:175]\n\tds_read_b64_tr_b16 v[16:17], v226 offset:8192\n\tds_read_b64_tr_b16 v[18:19], v226 offset:10240\n\tv_mfma_f32_16x16x32_bf16 v[172:175], a[180:183], v[28:31], v[172:175]\n\t" \
    "v_mfma_f32_16x16x32_bf16 v[180:183], a[184:187], v[28:31], v[180:183]\n\tv_mfma_f32_16x16x32_bf16 v[180:183], a[172:175], v[24:27], v[180:183]\n\tds_read_b64_tr_b16 v[20:21], v226 offset:12288\n\tds_read_b64_tr_b16 v[22:23], v226 offset:14336\n\tv_mfma_f32_16x16x32_bf16 v[188:191], a[176:179], v[24:27], v[188:191]\n\tv_mfma_f32_16x16x32_bf16 v[188:191], a[188:191], v[28:31], v[188:191]\n\ts_waitcnt lgkmcnt(8)\n\tv_mfma_f32_16x16x32_bf16 v[168:171], a[192:195], v[32:35], v[168:171]\n\t" \
    "ds_read_b64_tr_b16 v[24:25], v227 offset:8192\n\tds_read_b64_tr_b16 v[26:27], v227 offset:10240\n\tv_mfma_f32_16x16x32_bf16 v[168:171], a[204:207], v[36:39], v[168:171]\n\tv_mfma_f32_16x16x32_bf16 v[176:179], a[208:211], v[36:39], v[176:179]\n\tv_mfma_f32_16x16x32_bf16 v[176:179], a[196:199], v[32:35], v[176:179]\n\tds_read_b64_tr_b16 v[28:29], v227 offset:12288\n\tds_read_b64_tr_b16 v[30:31], v227 offset:14336\n\tv_mfma_f32_16x16x32_bf16 v[184:187], a[200:203], v[32:35], v[184:187]\n\t" \
    "v_mfma_f32_16x16x32_bf16 v[184:187], a[212:215], v[36:39], v[184:187]\n\ts_waitcnt vmcnt(4)\n\tv_cmp_ne_u64 vcc, s[36:37], v[250:251]\n\ts_cmp_lg_u64 vcc, 0\n\ts_cselect_b32 s70, 1, 0\n\ts_waitcnt lgkmcnt(8)\n\tv_mfma_f32_16x16x32_bf16 v[172:175], a[192:195], v[40:43], v[172:175]\n\tds_read_b64_tr_b16 v[32:33], v226 offset:24576\n\t" \
    "ds_read_b64_tr_b16 v[34:35], v226 offset:26624\n\tv_mfma_f32_16x16x32_bf16 v[172:175], a[204:207], v[44:47], v[172:175]\n\tbuffer_load_dwordx4 v[204:207], v8, s[8:11], s56 offen offset:0 sc1\n\tv_mfma_f32_16x16x32_bf16 v[180:183], a[208:211], v[44:47], v[180:183]\n\tv_mfma_f32_16x16x32_bf16 v[180:183], a[196:199], v[40:43], v[180:183]\n\tds_read_b64_tr_b16 v[36:37], v226 offset:28672\n\tds_read_b64_tr_b16 v[38:39], v226 offset:30720\n\tv_mfma_f32_16x16x32_bf16 v[188:191], a[200:203], v[40:43], v[188:191]\n\t" \
    "buffer_load_dwordx4 v[208:211], v8, s[8:11], s56 offen offset:1024 sc1\n\tv_mfma_f32_16x16x32_bf16 v[188:191], a[212:215], v[44:47], v[188:191]\n\ts_waitcnt lgkmcnt(8)\n\tv_mfma_f32_16x16x32_bf16 v[48:51], v[92:95], v[16:19], v[48:51]\n\tds_read_b64_tr_b16 v[40:41], v227 offset:24576\n\tds_read_b64_tr_b16 v[42:43], v227 offset:26624\n\tv_mfma_f32_16x16x32_bf16 v[48:51], v[104:107], v[20:23], v[48:51]\n\tbuffer_load_dwordx4 v[212:215], v8, s[8:11], s56 offen offset:2048 sc1\n\t" \
    "s_xor_b32 s68, s53, 4\n\ts_mul_i32 s68, s68, 9984\n\tv_mfma_f32_16x16x32_bf16 v[56:59], v[108:111], v[20:23], v[56:59]\n\ts_add_u32 s68, s68, 7215616\n\tv_cvt_pk_bf16_f32 v168, v168, v169\n\tv_cvt_pk_bf16_f32 v169, v170, v171\n\tv_mfma_f32_16x16x32_bf16 v[56:59], v[96:99], v[16:19], v[56:59]\n\tds_read_b64_tr_b16 v[44:45], v227 offset:28672\n\t" \
    "ds_read_b64_tr_b16 v[46:47], v227 offset:30720\n\tv_cvt_pk_bf16_f32 v170, v172, v173\n\tv_mfma_f32_16x16x32_bf16 v[64:67], v[100:103], v[16:19], v[64:67]\n\tv_cvt_pk_bf16_f32 v171, v174, v175\n\tv_mfma_f32_16x16x32_bf16 v[64:67], v[112:115], v[20:23], v[64:67]\n\ts_waitcnt lgkmcnt(8)\n\tv_mfma_f32_16x16x32_bf16 v[52:55], v[92:95], v[24:27], v[52:55]\n\tds_read_b64_tr_b16 v[16:17], v226 offset:40960\n\t" \
    "ds_read_b64_tr_b16 v[18:19], v226 offset:43008\n\tv_mfma_f32_16x16x32_bf16 v[52:55], v[104:107], v[28:31], v[52:55]\n\tbuffer_store_dwordx4 v[168:171], v8, s[8:11], s68 offen offset:0 sc1\n\tv_cvt_pk_bf16_f32 v172, v176, v177\n\tv_cvt_pk_bf16_f32 v173, v178, v179\n\tv_mfma_f32_16x16x32_bf16 v[60:63], v[108:111], v[28:31], v[60:63]\n\tv_cvt_pk_bf16_f32 v174, v180, v181\n\tv_cvt_pk_bf16_f32 v175, v182, v183\n\t" \
    "v_mfma_f32_16x16x32_bf16 v[60:63], v[96:99], v[24:27], v[60:63]\n\tds_read_b64_tr_b16 v[20:21], v226 offset:45056\n\tds_read_b64_tr_b16 v[22:23], v226 offset:47104\n\tv_mfma_f32_16x16x32_bf16 v[68:71], v[100:103], v[24:27], v[68:71]\n\tbuffer_store_dwordx4 v[172:175], v8, s[8:11], s68 offen offset:1024 sc1\n\tv_cvt_pk_bf16_f32 v176, v184, v185\n\tv_cvt_pk_bf16_f32 v177, v186, v187\n\tv_mfma_f32_16x16x32_bf16 v[68:71], v[112:115], v[28:31], v[68:71]\n\t" \
    "v_cvt_pk_bf16_f32 v178, v188, v189\n\tv_cvt_pk_bf16_f32 v179, v190, v191\n\ts_waitcnt lgkmcnt(8)\n\tv_mfma_f32_16x16x32_bf16 v[48:51], a[0:3], v[32:35], v[48:51]\n\tds_read_b64_tr_b16 v[24:25], v227 offset:40960\n\tds_read_b64_tr_b16 v[26:27], v227 offset:43008\n\tv_mfma_f32_16x16x32_bf16 v[48:51], a[12:15], v[36:39], v[48:51]\n\tbuffer_store_dwordx4 v[176:179], v8, s[8:11], s68 offen offset:2048 sc1\n\t" \
    "v_add3_u32 v180, s36, v168, v169\n\tv_add3_u32 v181, v170, v171, v172\n\tv_mfma_f32_16x16x32_bf16 v[56:59], a[16:19], v[36:39], v[56:59]\n\tv_add3_u32 v180, v180, v173, v174\n\tv_add3_u32 v181, v181, v175, v176\n\tv_add3_u32 v180, v180, v177, v178\n\tv_mfma_f32_16x16x32_bf16 v[56:59], a[4:7], v[32:35], v[56:59]\n\tds_read_b64_tr_b16 v[28:29], v227 offset:45056\n\t" \
    "ds_read_b64_tr_b16 v[30:31], v227 offset:47104\n\tv_add3_u32 v180, v180, v181, v179\n\tv_mfma_f32_16x16x32_bf16 v[64:67], a[8:11], v[32:35], v[64:67]\n\tbuffer_store_dword v180, v236, s[8:11], s68 offen offset:3072 sc1\n\tv_mfma_f32_16x16x32_bf16 v[64:67], a[20:23], v[36:39], v[64:67]\n\ts_waitcnt lgkmcnt(8)\n\tv_mfma_f32_16x16x32_bf16 v[52:55], a[0:3], v[40:43], v[52:55]\n\tds_read_b64_tr_b16 v[32:33], v226 offset:57344\n\t" \
    "ds_read_b64_tr_b16 v[34:35], v226 offset:59392\n\tv_mfma_f32_16x16x32_bf16 v[52:55], a[12:15], v[44:47], v[52:55]\n\tv_permlane16_swap_b32 v192, v194\n\tv_permlane16_swap_b32 v193, v195\n\tv_mfma_f32_16x16x32_bf16 v[60:63], a[16:19], v[44:47], v[60:63]\n\tv_permlane16_swap_b32 v196, v198\n\tv_permlane16_swap_b32 v197, v199\n\tv_mfma_f32_16x16x32_bf16 v[60:63], a[4:7], v[40:43], v[60:63]\n\t" \
    "ds_read_b64_tr_b16 v[36:37], v226 offset:61440\n\tds_read_b64_tr_b16 v[38:39], v226 offset:63488\n\tv_mfma_f32_16x16x32_bf16 v[68:71], a[8:11], v[40:43], v[68:71]\n\tv_permlane16_swap_b32 v200, v202\n\tv_permlane16_swap_b32 v201, v203\n\tv_mfma_f32_16x16x32_bf16 v[68:71], a[20:23], v[44:47], v[68:71]\n\tv_lshlrev_b32 v72, 16, v192\n\tv_and_b32 v73, 0xffff0000, v192\n\t" \
    "s_waitcnt lgkmcnt(8)\n\tv_mfma_f32_16x16x32_bf16 v[48:51], a[24:27], v[16:19], v[48:51]\n\tds_read_b64_tr_b16 v[40:41], v227 offset:57344\n\tds_read_b64_tr_b16 v[42:43], v227 offset:59392\n\tv_mfma_f32_16x16x32_bf16 v[48:51], a[36:39], v[20:23], v[48:51]\n\tv_lshlrev_b32 v74, 16, v193\n\tv_and_b32 v75, 0xffff0000, v193\n\tv_mfma_f32_16x16x32_bf16 v[56:59], a[40:43], v[20:23], v[56:59]\n\t" \
    "v_lshlrev_b32 v76, 16, v194\n\tv_and_b32 v77, 0xffff0000, v194\n\tv_mfma_f32_16x16x32_bf16 v[56:59], a[28:31], v[16:19], v[56:59]\n\tds_read_b64_tr_b16 v[44:45], v227 offset:61440\n\tds_read_b64_tr_b16 v[46:47], v227 offset:63488\n\tv_mfma_f32_16x16x32_bf16 v[64:67], a[32:35], v[16:19], v[64:67]\n\tv_lshlrev_b32 v78, 16, v195\n\tv_and_b32 v79, 0xffff0000, v195\n\t" \
    "v_mfma_f32_16x16x32_bf16 v[64:67], a[44:47], v[20:23], v[64:67]\n\tv_lshlrev_b32 v80, 16, v196\n\tv_and_b32 v81, 0xffff0000, v196\n\ts_waitcnt lgkmcnt(8)\n\tv_mfma_f32_16x16x32_bf16 v[52:55], a[24:27], v[24:27], v[52:55]\n\tds_read_b64_tr_b16 v[16:17], v6 offset:8192\n\tds_read_b64_tr_b16 v[18:19], v6 offset:10240\n\tv_mfma_f32_16x16x32_bf16 v[52:55], a[36:39], v[28:31], v[52:55]\n\t" \
    "v_lshlrev_b32 v82, 16, v197\n\tv_and_b32 v83, 0xffff0000, v197\n\tv_mfma_f32_16x16x32_bf16 v[60:63], a[40:43], v[28:31], v[60:63]\n\tv_lshlrev_b32 v84, 16, v198\n\tv_and_b32 v85, 0xffff0000, v198\n\tv_mfma_f32_16x16x32_bf16 v[60:63], a[28:31], v[24:27], v[60:63]\n\tds_read_b64_tr_b16 v[20:21], v6 offset:12288\n\tds_read_b64_tr_b16 v[22:23], v6 offset:14336\n\t" \
    "v_mfma_f32_16x16x32_bf16 v[68:71], a[32:35], v[24:27], v[68:71]\n\tv_lshlrev_b32 v86, 16, v199\n\tv_and_b32 v87, 0xffff0000, v199\n\tv_mfma_f32_16x16x32_bf16 v[68:71], a[44:47], v[28:31], v[68:71]\n\tv_lshlrev_b32 v88, 16, v200\n\tv_and_b32 v89, 0xffff0000, v200\n\ts_waitcnt lgkmcnt(8)\n\tv_mfma_f32_16x16x32_bf16 v[48:51], a[48:51], v[32:35], v[48:51]\n\t" \
    "ds_read_b64_tr_b16 v[24:25], v7 offset:8192\n\tds_read_b64_tr_b16 v[26:27], v7 offset:10240\n\tv_mfma_f32_16x16x32_bf16 v[48:51], a[60:63], v[36:39], v[48:51]\n\tv_lshlrev_b32 v90, 16, v201\n\tv_and_b32 v91, 0xffff0000, v201\n\tv_mfma_f32_16x16x32_bf16 v[56:59], a[64:67], v[36:39], v[56:59]\n\tv_lshlrev_b32 v92, 16, v202\n\tv_and_b32 v93, 0xffff0000, v202\n\t" \
    "v_mfma_f32_16x16x32_bf16 v[56:59], a[52:55], v[32:35], v[56:59]\n\tds_read_b64_tr_b16 v[28:29], v7 offset:12288\n\tds_read_b64_tr_b16 v[30:31], v7 offset:14336\n\tv_mfma_f32_16x16x32_bf16 v[64:67], a[56:59], v[32:35], v[64:67]\n\tv_lshlrev_b32 v94, 16, v203\n\tv_and_b32 v95, 0xffff0000, v203\n\tv_mfma_f32_16x16x32_bf16 v[64:67], a[68:71], v[36:39], v[64:67]\n\tv_fma_f32 v96, v72, v216, v217\n\t" \
    "v_fma_f32 v97, v73, v216, v217\n\ts_waitcnt lgkmcnt(8)\n\tv_mfma_f32_16x16x32_bf16 v[52:55], a[48:51], v[40:43], v[52:55]\n\tds_read_b64_tr_b16 v[32:33], v222 offset:24576\n\tds_read_b64_tr_b16 v[34:35], v222 offset:26624\n\tv_mfma_f32_16x16x32_bf16 v[52:55], a[60:63], v[44:47], v[52:55]\n\tv_fma_f32 v98, v74, v216, v217\n\tv_fma_f32 v99, v75, v216, v217\n\t" \
    "v_mfma_f32_16x16x32_bf16 v[60:63], a[64:67], v[44:47], v[60:63]\n\tv_fma_f32 v100, v76, v218, v219\n\tv_fma_f32 v101, v77, v218, v219\n\tv_mfma_f32_16x16x32_bf16 v[60:63], a[52:55], v[40:43], v[60:63]\n\tds_read_b64_tr_b16 v[36:37], v222 offset:28672\n\tds_read_b64_tr_b16 v[38:39], v222 offset:30720\n\tv_mfma_f32_16x16x32_bf16 v[68:71], a[56:59], v[40:43], v[68:71]\n\tv_fma_f32 v102, v78, v218, v219\n\t" \
    "v_fma_f32 v103, v79, v218, v219\n\tv_mfma_f32_16x16x32_bf16 v[68:71], a[68:71], v[44:47], v[68:71]\n\tv_fma_f32 v104, v80, v216, v217\n\tv_fma_f32 v105, v81, v216, v217\n\ts_waitcnt lgkmcnt(8)\n\tv_mfma_f32_16x16x32_bf16 v[48:51], a[72:75], v[16:19], v[48:51]\n\tds_read_b64_tr_b16 v[40:41], v223 offset:24576\n\tds_read_b64_tr_b16 v[42:43], v223 offset:26624\n\t" \
    "v_mfma_f32_16x16x32_bf16 v[48:51], a[84:87], v[20:23], v[48:51]\n\tv_fma_f32 v106, v82, v216, v217\n\tv_fma_f32 v107, v83, v216, v217\n\tv_mfma_f32_16x16x32_bf16 v[56:59], a[88:91], v[20:23], v[56:59]\n\tv_fma_f32 v108, v84, v218, v219\n\tv_fma_f32 v109, v85, v218, v219\n\tv_mfma_f32_16x16x32_bf16 v[56:59], a[76:79], v[16:19], v[56:59]\n\tds_read_b64_tr_b16 v[44:45], v223 offset:28672\n\t" \
    "ds_read_b64_tr_b16 v[46:47], v223 offset:30720\n\tv_mfma_f32_16x16x32_bf16 v[64:67], a[80:83], v[16:19], v[64:67]\n\tv_fma_f32 v110, v86, v218, v219\n\tv_fma_f32 v111, v87, v218, v219\n\tv_mfma_f32_16x16x32_bf16 v[64:67], a[92:95], v[20:23], v[64:67]\n\tv_fma_f32 v112, v88, v216, v217\n\tv_fma_f32 v113, v89, v216, v217\n\ts_waitcnt lgkmcnt(8)\n\t" \
    "v_mfma_f32_16x16x32_bf16 v[52:55], a[72:75], v[24:27], v[52:55]\n\tds_read_b64_tr_b16 v[16:17], v6 offset:32768\n\tds_read_b64_tr_b16 v[18:19], v6 offset:34816\n\tv_mfma_f32_16x16x32_bf16 v[52:55], a[84:87], v[28:31], v[52:55]\n\tv_fma_f32 v114, v90, v216, v217\n\tv_fma_f32 v115, v91, v216, v217\n\tv_mfma_f32_16x16x32_bf16 v[60:63], a[88:91], v[28:31], v[60:63]\n\tv_fma_f32 v116, v92, v218, v219\n\t" \
    "v_fma_f32 v117, v93, v218, v219\n\tv_mfma_f32_16x16x32_bf16 v[60:63], a[76:79], v[24:27], v[60:63]\n\tds_read_b64_tr_b16 v[20:21], v6 offset:36864\n\tds_read_b64_tr_b16 v[22:23], v6 offset:38912\n\tv_mfma_f32_16x16x32_bf16 v[68:71], a[80:83], v[24:27], v[68:71]\n\tv_fma_f32 v118, v94, v218, v219\n\tv_fma_f32 v119, v95, v218, v219\n\tv_mfma_f32_16x16x32_bf16 v[68:71], a[92:95], v[28:31], v[68:71]\n\t" \
    "s_waitcnt lgkmcnt(8)\n\tv_mfma_f32_16x16x32_bf16 v[48:51], a[96:99], v[32:35], v[48:51]\n\tds_read_b64_tr_b16 v[24:25], v7 offset:32768\n\tds_read_b64_tr_b16 v[26:27], v7 offset:34816\n\tv_mfma_f32_16x16x32_bf16 v[48:51], a[108:111], v[36:39], v[48:51]\n\tv_mfma_f32_16x16x32_bf16 v[56:59], a[112:115], v[36:39], v[56:59]\n\tv_mfma_f32_16x16x32_bf16 v[56:59], a[100:103], v[32:35], v[56:59]\n\tds_read_b64_tr_b16 v[28:29], v7 offset:36864\n\t" \
    "ds_read_b64_tr_b16 v[30:31], v7 offset:38912\n\tv_mfma_f32_16x16x32_bf16 v[64:67], a[104:107], v[32:35], v[64:67]\n\tv_mfma_f32_16x16x32_bf16 v[64:67], a[116:119], v[36:39], v[64:67]\n\ts_waitcnt lgkmcnt(8)\n\tv_mfma_f32_16x16x32_bf16 v[52:55], a[96:99], v[40:43], v[52:55]\n\tds_read_b64_tr_b16 v[32:33], v6 offset:40960\n\tds_read_b64_tr_b16 v[34:35], v6 offset:43008\n\tv_mfma_f32_16x16x32_bf16 v[52:55], a[108:111], v[44:47], v[52:55]\n\t" \
    "v_mfma_f32_16x16x32_bf16 v[60:63], a[112:115], v[44:47], v[60:63]\n\tv_mfma_f32_16x16x32_bf16 v[60:63], a[100:103], v[40:43], v[60:63]\n\tds_read_b64_tr_b16 v[36:37], v6 offset:45056\n\tds_read_b64_tr_b16 v[38:39], v6 offset:47104\n\tv_mfma_f32_16x16x32_bf16 v[68:71], a[104:107], v[40:43], v[68:71]\n\tv_mfma_f32_16x16x32_bf16 v[68:71], a[116:119], v[44:47], v[68:71]\n\ts_waitcnt lgkmcnt(8)\n\tv_mfma_f32_16x16x32_bf16 v[48:51], a[120:123], v[16:19], v[48:51]\n\t" \
    "ds_read_b64_tr_b16 v[40:41], v7 offset:40960\n\tds_read_b64_tr_b16 v[42:43], v7 offset:43008\n\tv_mfma_f32_16x16x32_bf16 v[48:51], a[132:135], v[20:23], v[48:51]\n\tv_mfma_f32_16x16x32_bf16 v[56:59], a[136:139], v[20:23], v[56:59]\n\tv_mfma_f32_16x16x32_bf16 v[56:59], a[124:127], v[16:19], v[56:59]\n\tds_read_b64_tr_b16 v[44:45], v7 offset:45056\n\tds_read_b64_tr_b16 v[46:47], v7 offset:47104\n\tv_mfma_f32_16x16x32_bf16 v[64:67], a[128:131], v[16:19], v[64:67]\n\t" \
    "v_mfma_f32_16x16x32_bf16 v[64:67], a[140:143], v[20:23], v[64:67]\n\ts_waitcnt lgkmcnt(8)\n\tv_mfma_f32_16x16x32_bf16 v[52:55], a[120:123], v[24:27], v[52:55]\n\tds_read_b64_tr_b16 v[16:17], v6 offset:49152\n\tds_read_b64_tr_b16 v[18:19], v6 offset:51200\n\ts_add_u32 s68, s55, 0\n\tv_mfma_f32_16x16x32_bf16 v[52:55], a[132:135], v[28:31], v[52:55]\n\tbuffer_load_dwordx4 v[120:123], v8, s[8:11], s68 offen offset:0 sc1\n\t" \
    "v_mfma_f32_16x16x32_bf16 v[60:63], a[136:139], v[28:31], v[60:63]\n\tv_mfma_f32_16x16x32_bf16 v[60:63], a[124:127], v[24:27], v[60:63]\n\tds_read_b64_tr_b16 v[20:21], v6 offset:53248\n\tds_read_b64_tr_b16 v[22:23], v6 offset:55296\n\tv_mfma_f32_16x16x32_bf16 v[68:71], a[128:131], v[24:27], v[68:71]\n\tbuffer_load_dwordx4 v[124:127], v8, s[8:11], s68 offen offset:1024 sc1\n\tv_mfma_f32_16x16x32_bf16 v[68:71], a[140:143], v[28:31], v[68:71]\n\ts_waitcnt lgkmcnt(8)\n\t" \
    "v_mfma_f32_16x16x32_bf16 v[48:51], a[144:147], v[32:35], v[48:51]\n\tds_read_b64_tr_b16 v[24:25], v7 offset:49152\n\tds_read_b64_tr_b16 v[26:27], v7 offset:51200\n\tv_mfma_f32_16x16x32_bf16 v[48:51], a[156:159], v[36:39], v[48:51]\n\tbuffer_load_dwordx4 v[128:131], v8, s[8:11], s68 offen offset:2048 sc1\n\tv_mfma_f32_16x16x32_bf16 v[56:59], a[160:163], v[36:39], v[56:59]\n\tv_mfma_f32_16x16x32_bf16 v[56:59], a[148:151], v[32:35], v[56:59]\n\tds_read_b64_tr_b16 v[28:29], v7 offset:53248\n\t" \
    "ds_read_b64_tr_b16 v[30:31], v7 offset:55296\n\tv_mfma_f32_16x16x32_bf16 v[64:67], a[152:155], v[32:35], v[64:67]\n\tbuffer_load_dword v132, v236, s[8:11], s68 offen offset:3072 sc1\n\tv_mfma_f32_16x16x32_bf16 v[64:67], a[164:167], v[36:39], v[64:67]\n\ts_waitcnt lgkmcnt(8)\n\tv_mfma_f32_16x16x32_bf16 v[52:55], a[144:147], v[40:43], v[52:55]\n\tds_read_b64_tr_b16 v[32:33], v6 offset:57344\n\tds_read_b64_tr_b16 v[34:35], v6 offset:59392\n\t" \
    "v_mfma_f32_16x16x32_bf16 v[52:55], a[156:159], v[44:47], v[52:55]\n\tv_mfma_f32_16x16x32_bf16 v[60:63], a[160:163], v[44:47], v[60:63]\n\tv_mfma_f32_16x16x32_bf16 v[60:63], a[148:151], v[40:43], v[60:63]\n\tds_read_b64_tr_b16 v[36:37], v6 offset:61440\n\tds_read_b64_tr_b16 v[38:39], v6 offset:63488\n\tv_mfma_f32_16x16x32_bf16 v[68:71], a[152:155], v[40:43], v[68:71]\n\tv_mfma_f32_16x16x32_bf16 v[68:71], a[164:167], v[44:47], v[68:71]\n\ts_waitcnt lgkmcnt(8)\n\t" \
    "v_mfma_f32_16x16x32_bf16 v[48:51], a[168:171], v[16:19], v[48:51]\n\tds_read_b64_tr_b16 v[40:41], v7 offset:57344\n\tds_read_b64_tr_b16 v[42:43], v7 offset:59392\n\ts_add_u32 s68, s55, 3328\n\tv_mfma_f32_16x16x32_bf16 v[48:51], a[180:183], v[20:23], v[48:51]\n\tbuffer_load_dwordx4 v[144:147], v8, s[8:11], s68 offen offset:0 sc1\n\tv_mfma_f32_16x16x32_bf16 v[56:59], a[184:187], v[20:23], v[56:59]\n\tv_mfma_f32_16x16x32_bf16 v[56:59], a[172:175], v[16:19], v[56:59]\n\t" \
    "ds_read_b64_tr_b16 v[44:45], v7 offset:61440\n\tds_read_b64_tr_b16 v[46:47], v7 offset:63488\n\tv_mfma_f32_16x16x32_bf16 v[64:67], a[176:179], v[16:19], v[64:67]\n\tbuffer_load_dwordx4 v[148:151], v8, s[8:11], s68 offen offset:1024 sc1\n\tv_mfma_f32_16x16x32_bf16 v[64:67], a[188:191], v[20:23], v[64:67]\n\ts_waitcnt lgkmcnt(8)\n\tv_mfma_f32_16x16x32_bf16 v[52:55], a[168:171], v[24:27], v[52:55]\n\tv_mfma_f32_16x16x32_bf16 v[52:55], a[180:183], v[28:31], v[52:55]\n\t" \
    "buffer_load_dwordx4 v[152:155], v8, s[8:11], s68 offen offset:2048 sc1\n\tv_mfma_f32_16x16x32_bf16 v[60:63], a[184:187], v[28:31], v[60:63]\n\tv_mfma_f32_16x16x32_bf16 v[60:63], a[172:175], v[24:27], v[60:63]\n\tv_mfma_f32_16x16x32_bf16 v[68:71], a[176:179], v[24:27], v[68:71]\n\tbuffer_load_dword v156, v236, s[8:11], s68 offen offset:3072 sc1\n\tv_mfma_f32_16x16x32_bf16 v[68:71], a[188:191], v[28:31], v[68:71]\n\ts_waitcnt lgkmcnt(4)\n\tv_mfma_f32_16x16x32_bf16 v[48:51], a[192:195], v[32:35], v[48:51]\n\t" \
    "s_add_u32 s68, s55, 6656\n\tv_mfma_f32_16x16x32_bf16 v[48:51], a[204:207], v[36:39], v[48:51]\n\tbuffer_load_dwordx4 v[168:171], v8, s[8:11], s68 offen offset:0 sc1\n\tv_mfma_f32_16x16x32_bf16 v[56:59], a[208:211], v[36:39], v[56:59]\n\tv_mfma_f32_16x16x32_bf16 v[56:59], a[196:199], v[32:35], v[56:59]\n\tv_mfma_f32_16x16x32_bf16 v[64:67], a[200:203], v[32:35], v[64:67]\n\tbuffer_load_dwordx4 v[172:175], v8, s[8:11], s68 offen offset:1024 sc1\n\tv_mfma_f32_16x16x32_bf16 v[64:67], a[212:215], v[36:39], v[64:67]\n\t" \
    "s_waitcnt lgkmcnt(0)\n\tv_mfma_f32_16x16x32_bf16 v[52:55], a[192:195], v[40:43], v[52:55]\n\tv_mfma_f32_16x16x32_bf16 v[52:55], a[204:207], v[44:47], v[52:55]\n\tbuffer_load_dwordx4 v[176:179], v8, s[8:11], s68 offen offset:2048 sc1\n\tv_mfma_f32_16x16x32_bf16 v[60:63], a[208:211], v[44:47], v[60:63]\n\tv_mfma_f32_16x16x32_bf16 v[60:63], a[196:199], v[40:43], v[60:63]\n\tv_mfma_f32_16x16x32_bf16 v[68:71], a[200:203], v[40:43], v[68:71]\n\tbuffer_load_dword v180, v236, s[8:11], s68 offen offset:3072 sc1\n\t" \
    "v_mfma_f32_16x16x32_bf16 v[68:71], a[212:215], v[44:47], v[68:71]\n\ts_cmp_eq_u32 s70, 0\n\ts_cbranch_scc1 116f\n\tv_cmp_ne_u64 vcc, s[36:37], v[250:251]\n\ts_cbranch_vccz 118f\n\ts_mov_b32 s71, 1048576\n\t117:\n\ts_sleep 1\n\t" \
    "buffer_load_dwordx2 v[250:251], v241, s[8:11], s51 offen sc1\n\ts_waitcnt vmcnt(0)\n\tv_cmp_ne_u64 vcc, s[36:37], v[250:251]\n\ts_cbranch_vccz 118f\n\ts_sub_u32 s71, s71, 1\n\ts_cbranch_scc0 117b\n\t118:\n\tbuffer_load_dwordx4 v[204:207], v8, s[8:11], s56 offen offset:0 sc1\n\t" \
    "buffer_load_dwordx4 v[208:211], v8, s[8:11], s56 offen offset:1024 sc1\n\tbuffer_load_dwordx4 v[212:215], v8, s[8:11], s56 offen offset:2048 sc1\n\ts_waitcnt vmcnt(0)\n\t116:\n\ts_waitcnt vmcnt(16)\n\tv_lshlrev_b32 v16, 16, v204\n\tv_and_b32 v17, 0xffff0000, v204\n\tv_lshlrev_b32 v18, 16, v205\n\t" \
    "v_and_b32 v19, 0xffff0000, v205\n\tv_lshlrev_b32 v20, 16, v206\n\tv_and_b32 v21, 0xffff0000, v206\n\tv_lshlrev_b32 v22, 16, v207\n\tv_and_b32 v23, 0xffff0000, v207\n\tv_lshlrev_b32 v24, 16, v208\n\tv_and_b32 v25, 0xffff0000, v208\n\tv_lshlrev_b32 v26, 16, v209\n\t" \
    "v_and_b32 v27, 0xffff0000, v209\n\tv_lshlrev_b32 v28, 16, v210\n\tv_and_b32 v29, 0xffff0000, v210\n\tv_lshlrev_b32 v30, 16, v211\n\tv_and_b32 v31, 0xffff0000, v211\n\tv_lshlrev_b32 v32, 16, v212\n\tv_and_b32 v33, 0xffff0000, v212\n\tv_lshlrev_b32 v34, 16, v213\n\t" \
    "v_and_b32 v35, 0xffff0000, v213\n\tv_lshlrev_b32 v36, 16, v214\n\tv_and_b32 v37, 0xffff0000, v214\n\tv_lshlrev_b32 v38, 16, v215\n\tv_and_b32 v39, 0xffff0000, v215\n\tv_pk_mul_f32 v[96:97], v[96:97], v[16:17]\n\tv_pk_mul_f32 v[98:99], v[98:99], v[18:19]\n\tv_pk_mul_f32 v[100:101], v[100:101], v[20:21]\n\t" \
    "v_pk_mul_f32 v[102:103], v[102:103], v[22:23]\n\tv_pk_mul_f32 v[104:105], v[104:105], v[24:25]\n\tv_pk_mul_f32 v[106:107], v[106:107], v[26:27]\n\tv_pk_mul_f32 v[108:109], v[108:109], v[28:29]\n\tv_pk_mul_f32 v[110:111], v[110:111], v[30:31]\n\tv_pk_mul_f32 v[112:113], v[112:113], v[32:33]\n\tv_pk_mul_f32 v[114:115], v[114:115], v[34:35]\n\tv_pk_mul_f32 v[116:117], v[116:117], v[36:37]\n\t" \
    "v_pk_mul_f32 v[118:119], v[118:119], v[38:39]\n\ts_mov_b32 s71, 1048576\n\ts_waitcnt vmcnt(8)\n\t119:\n\tv_add3_u32 v134, s36, v120, v121\n\tv_add3_u32 v135, v122, v123, v124\n\tv_add3_u32 v134, v134, v125, v126\n\tv_add3_u32 v135, v135, v127, v128\n\t" \
    "v_add3_u32 v134, v134, v129, v130\n\tv_add3_u32 v134, v134, v135, v131\n\tv_cmp_ne_u32 vcc, v134, v132\n\ts_cbranch_vccz 120f\n\ts_sub_u32 s71, s71, 1\n\ts_cbranch_scc1 120f\n\ts_sleep 6\n\ts_add_u32 s73, s55, 0\n\t" \
    "buffer_load_dwordx4 v[120:123], v8, s[8:11], s73 offen offset:0 sc1\n\tbuffer_load_dwordx4 v[124:127], v8, s[8:11], s73 offen offset:1024 sc1\n\tbuffer_load_dwordx4 v[128:131], v8, s[8:11], s73 offen offset:2048 sc1\n\tbuffer_load_dword v132, v236, s[8:11], s73 offen offset:3072 sc1\n\ts_waitcnt vmcnt(0)\n\ts_branch 119b\n\t120:\n\tv_lshlrev_b32 v16, 16, v120\n\t" \
    "v_and_b32 v17, 0xffff0000, v120\n\tv_lshlrev_b32 v18, 16, v121\n\tv_and_b32 v19, 0xffff0000, v121\n\tv_lshlrev_b32 v20, 16, v122\n\tv_and_b32 v21, 0xffff0000, v122\n\tv_lshlrev_b32 v22, 16, v123\n\tv_and_b32 v23, 0xffff0000, v123\n\tv_lshlrev_b32 v24, 16, v124\n\t" \
    "v_and_b32 v25, 0xffff0000, v124\n\tv_lshlrev_b32 v26, 16, v125\n\tv_and_b32 v27, 0xffff0000, v125\n\tv_lshlrev_b32 v28, 16, v126\n\tv_and_b32 v29, 0xffff0000, v126\n\tv_lshlrev_b32 v30, 16, v127\n\tv_and_b32 v31, 0xffff0000, v127\n\tv_lshlrev_b32 v32, 16, v128\n\t" \
    "v_and_b32 v33, 0xffff0000, v128\n\tv_lshlrev_b32 v34, 16, v129\n\tv_and_b32 v35, 0xffff0000, v129\n\tv_lshlrev_b32 v36, 16, v130\n\tv_and_b32 v37, 0xffff0000, v130\n\tv_lshlrev_b32 v38, 16, v131\n\tv_and_b32 v39, 0xffff0000, v131\n\tv_pk_add_f32 v[48:49], v[48:49], v[16:17]\n\t" \
    "v_pk_add_f32 v[50:51], v[50:51], v[18:19]\n\tv_pk_add_f32 v[52:53], v[52:53], v[20:21]\n\tv_pk_add_f32 v[54:55], v[54:55], v[22:23]\n\tv_pk_add_f32 v[56:57], v[56:57], v[24:25]\n\tv_pk_add_f32 v[58:59], v[58:59], v[26:27]\n\tv_pk_add_f32 v[60:61], v[60:61], v[28:29]\n\tv_pk_add_f32 v[62:63], v[62:63], v[30:31]\n\tv_pk_add_f32 v[64:65], v[64:65], v[32:33]\n\t" \
    "v_pk_add_f32 v[66:67], v[66:67], v[34:35]\n\tv_pk_add_f32 v[68:69], v[68:69], v[36:37]\n\tv_pk_add_f32 v[70:71], v[70:71], v[38:39]\n\ts_mov_b32 s71, 1048576\n\ts_waitcnt vmcnt(4)\n\t121:\n\tv_add3_u32 v158, s36, v144, v145\n\tv_add3_u32 v159, v146, v147, v148\n\t" \
    "v_add3_u32 v158, v158, v149, v150\n\tv_add3_u32 v159, v159, v151, v152\n\tv_add3_u32 v158, v158, v153, v154\n\tv_add3_u32 v158, v158, v159, v155\n\tv_cmp_ne_u32 vcc, v158, v156\n\ts_cbranch_vccz 122f\n\ts_sub_u32 s71, s71, 1\n\ts_cbranch_scc1 122f\n\t" \
    "s_sleep 6\n\ts_add_u32 s73, s55, 3328\n\tbuffer_load_dwordx4 v[144:147], v8, s[8:11], s73 offen offset:0 sc1\n\tbuffer_load_dwordx4 v[148:151], v8, s[8:11], s73 offen offset:1024 sc1\n\tbuffer_load_dwordx4 v[152:155], v8, s[8:11], s73 offen offset:2048 sc1\n\tbuffer_load_dword v156, v236, s[8:11], s73 offen offset:3072 sc1\n\ts_waitcnt vmcnt(0)\n\ts_branch 121b\n\t" \
    "122:\n\tv_lshlrev_b32 v16, 16, v144\n\tv_and_b32 v17, 0xffff0000, v144\n\tv_lshlrev_b32 v18, 16, v145\n\tv_and_b32 v19, 0xffff0000, v145\n\tv_lshlrev_b32 v20, 16, v146\n\tv_and_b32 v21, 0xffff0000, v146\n\tv_lshlrev_b32 v22, 16, v147\n\t" \
    "v_and_b32 v23, 0xffff0000, v147\n\tv_lshlrev_b32 v24, 16, v148\n\tv_and_b32 v25, 0xffff0000, v148\n\tv_lshlrev_b32 v26, 16, v149\n\tv_and_b32 v27, 0xffff0000, v149\n\tv_lshlrev_b32 v28, 16, v150\n\tv_and_b32 v29, 0xffff0000, v150\n\tv_lshlrev_b32 v30, 16, v151\n\t" \
    "v_and_b32 v31, 0xffff0000, v151\n\tv_lshlrev_b32 v32, 16, v152\n\tv_and_b32 v33, 0xffff0000, v152\n\tv_lshlrev_b32 v34, 16, v153\n\tv_and_b32 v35, 0xffff0000, v153\n\tv_lshlrev_b32 v36, 16, v154\n\tv_and_b32 v37, 0xffff0000, v154\n\tv_lshlrev_b32 v38, 16, v155\n\t" \
    "v_and_b32 v39, 0xffff0000, v155\n\tv_pk_add_f32 v[48:49], v[48:49], v[16:17]\n\tv_pk_add_f32 v[50:51], v[50:51], v[18:19]\n\tv_pk_add_f32 v[52:53], v[52:53], v[20:21]\n\tv_pk_add_f32 v[54:55], v[54:55], v[22:23]\n\tv_pk_add_f32 v[56:57], v[56:57], v[24:25]\n\tv_pk_add_f32 v[58:59], v[58:59], v[26:27]\n\tv_pk_add_f32 v[60:61], v[60:61], v[28:29]\n\t" \
    "v_pk_add_f32 v[62:63], v[62:63], v[30:31]\n\tv_pk_add_f32 v[64:65], v[64:65], v[32:33]\n\tv_pk_add_f32 v[66:67], v[66:67], v[34:35]\n\tv_pk_add_f32 v[68:69], v[68:69], v[36:37]\n\tv_pk_add_f32 v[70:71], v[70:71], v[38:39]\n\ts_mov_b32 s71, 1048576\n\ts_waitcnt vmcnt(0)\n\t123:\n\t" \
    "v_add3_u32 v182, s36, v168, v169\n\tv_add3_u32 v183, v170, v171, v172\n\tv_add3_u32 v182, v182, v173, v174\n\tv_add3_u32 v183, v183, v175, v176\n\tv_add3_u32 v182, v182, v177, v178\n\tv_add3_u32 v182, v182, v183, v179\n\tv_cmp_ne_u32 vcc, v182, v180\n\ts_cbranch_vccz 124f\n\t" \
    "s_sub_u32 s71, s71, 1\n\ts_cbranch_scc1 124f\n\ts_sleep 6\n\ts_add_u32 s73, s55, 6656\n\tbuffer_load_dwordx4 v[168:171], v8, s[8:11], s73 offen offset:0 sc1\n\tbuffer_load_dwordx4 v[172:175], v8, s[8:11], s73 offen offset:1024 sc1\n\tbuffer_load_dwordx4 v[176:179], v8, s[8:11], s73 offen offset:2048 sc1\n\tbuffer_load_dword v180, v236, s[8:11], s73 offen offset:3072 sc1\n\t" \
    "s_waitcnt vmcnt(0)\n\ts_branch 123b\n\t124:\n\ts_mov_b32 s68, 0xbfb8aa3b\n\ts_mov_b32 s69, 0xbfb8aa3b\n\tv_lshlrev_b32 v16, 16, v168\n\tv_and_b32 v17, 0xffff0000, v168\n\tv_lshlrev_b32 v18, 16, v169\n\t" \
    "v_and_b32 v19, 0xffff0000, v169\n\tv_lshlrev_b32 v20, 16, v170\n\tv_and_b32 v21, 0xffff0000, v170\n\tv_lshlrev_b32 v22, 16, v171\n\tv_and_b32 v23, 0xffff0000, v171\n\tv_pk_add_f32 v[48:49], v[48:49], v[16:17]\n\tv_pk_add_f32 v[50:51], v[50:51], v[18:19]\n\tv_pk_add_f32 v[52:53], v[52:53], v[20:21]\n\t" \
    "v_pk_add_f32 v[54:55], v[54:55], v[22:23]\n\tv_pk_mul_f32 v[16:17], v[48:49], s[68:69]\n\tv_pk_mul_f32 v[18:19], v[50:51], s[68:69]\n\tv_pk_mul_f32 v[20:21], v[52:53], s[68:69]\n\tv_pk_mul_f32 v[22:23], v[54:55], s[68:69]\n\tv_lshlrev_b32 v24, 16, v172\n\tv_exp_f32 v16, v16\n\tv_and_b32 v25, 0xffff0000, v172\n\t" \
    "v_exp_f32 v17, v17\n\tv_lshlrev_b32 v26, 16, v173\n\tv_exp_f32 v18, v18\n\tv_and_b32 v27, 0xffff0000, v173\n\tv_exp_f32 v19, v19\n\tv_lshlrev_b32 v28, 16, v174\n\tv_exp_f32 v20, v20\n\tv_and_b32 v29, 0xffff0000, v174\n\t" \
    "v_exp_f32 v21, v21\n\tv_lshlrev_b32 v30, 16, v175\n\tv_exp_f32 v22, v22\n\tv_and_b32 v31, 0xffff0000, v175\n\tv_exp_f32 v23, v23\n\tv_pk_add_f32 v[56:57], v[56:57], v[24:25]\n\tv_pk_add_f32 v[16:17], v[16:17], 1.0 op_sel_hi:[1,0]\n\tv_pk_add_f32 v[18:19], v[18:19], 1.0 op_sel_hi:[1,0]\n\t" \
    "v_pk_add_f32 v[20:21], v[20:21], 1.0 op_sel_hi:[1,0]\n\tv_pk_add_f32 v[22:23], v[22:23], 1.0 op_sel_hi:[1,0]\n\tv_pk_add_f32 v[58:59], v[58:59], v[26:27]\n\tv_rcp_f32 v16, v16\n\tv_pk_add_f32 v[60:61], v[60:61], v[28:29]\n\tv_rcp_f32 v17, v17\n\tv_pk_add_f32 v[62:63], v[62:63], v[30:31]\n\tv_rcp_f32 v18, v18\n\t" \
    "v_pk_mul_f32 v[24:25], v[56:57], s[68:69]\n\tv_rcp_f32 v19, v19\n\tv_pk_mul_f32 v[26:27], v[58:59], s[68:69]\n\tv_rcp_f32 v20, v20\n\tv_pk_mul_f32 v[28:29], v[60:61], s[68:69]\n\tv_rcp_f32 v21, v21\n\tv_pk_mul_f32 v[30:31], v[62:63], s[68:69]\n\tv_rcp_f32 v22, v22\n\t" \
    "v_lshlrev_b32 v32, 16, v176\n\tv_rcp_f32 v23, v23\n\tv_and_b32 v33, 0xffff0000, v176\n\tv_exp_f32 v24, v24\n\tv_pk_mul_f32 v[48:49], v[48:49], v[96:97]\n\tv_exp_f32 v25, v25\n\tv_pk_mul_f32 v[50:51], v[50:51], v[98:99]\n\tv_exp_f32 v26, v26\n\t" \
    "v_pk_mul_f32 v[52:53], v[52:53], v[100:101]\n\tv_exp_f32 v27, v27\n\tv_pk_mul_f32 v[54:55], v[54:55], v[102:103]\n\tv_exp_f32 v28, v28\n\tv_pk_fma_f32 v[16:17], v[16:17], v[48:49], v[72:73]\n\tv_exp_f32 v29, v29\n\tv_pk_fma_f32 v[18:19], v[18:19], v[50:51], v[74:75]\n\tv_exp_f32 v30, v30\n\t" \
    "v_pk_fma_f32 v[20:21], v[20:21], v[52:53], v[76:77]\n\tv_exp_f32 v31, v31\n\tv_pk_fma_f32 v[22:23], v[22:23], v[54:55], v[78:79]\n\tv_cvt_pk_bf16_f32 v48, v16, v17\n\tv_cvt_pk_bf16_f32 v49, v18, v19\n\tv_cvt_pk_bf16_f32 v50, v20, v21\n\tv_cvt_pk_bf16_f32 v51, v22, v23\n\ts_nop 0\n\t" \
    "v_permlane16_swap_b32 v48, v50\n\tv_permlane16_swap_b32 v49, v51\n\tbuffer_store_dwordx4 v[48:51], v243, s[24:27], 0 offen offset:0\n\tv_pk_add_f32 v[24:25], v[24:25], 1.0 op_sel_hi:[1,0]\n\tv_pk_add_f32 v[26:27], v[26:27], 1.0 op_sel_hi:[1,0]\n\tv_pk_add_f32 v[28:29], v[28:29], 1.0 op_sel_hi:[1,0]\n\tv_pk_add_f32 v[30:31], v[30:31], 1.0 op_sel_hi:[1,0]\n\tv_lshlrev_b32 v34, 16, v177\n\t" \
    "v_rcp_f32 v24, v24\n\tv_and_b32 v35, 0xffff0000, v177\n\tv_rcp_f32 v25, v25\n\tv_lshlrev_b32 v36, 16, v178\n\tv_rcp_f32 v26, v26\n\tv_and_b32 v37, 0xffff0000, v178\n\tv_rcp_f32 v27, v27\n\tv_lshlrev_b32 v38, 16, v179\n\t" \
    "v_rcp_f32 v28, v28\n\tv_and_b32 v39, 0xffff0000, v179\n\tv_rcp_f32 v29, v29\n\tv_pk_add_f32 v[64:65], v[64:65], v[32:33]\n\tv_rcp_f32 v30, v30\n\tv_pk_add_f32 v[66:67], v[66:67], v[34:35]\n\tv_rcp_f32 v31, v31\n\tv_pk_add_f32 v[68:69], v[68:69], v[36:37]\n\t" \
    "v_pk_mul_f32 v[56:57], v[56:57], v[104:105]\n\tv_pk_mul_f32 v[58:59], v[58:59], v[106:107]\n\tv_pk_mul_f32 v[60:61], v[60:61], v[108:109]\n\tv_pk_mul_f32 v[62:63], v[62:63], v[110:111]\n\tv_pk_fma_f32 v[24:25], v[24:25], v[56:57], v[80:81]\n\tv_pk_fma_f32 v[26:27], v[26:27], v[58:59], v[82:83]\n\tv_pk_fma_f32 v[28:29], v[28:29], v[60:61], v[84:85]\n\tv_pk_fma_f32 v[30:31], v[30:31], v[62:63], v[86:87]\n\t" \
    "v_cvt_pk_bf16_f32 v56, v24, v25\n\tv_cvt_pk_bf16_f32 v57, v26, v27\n\tv_cvt_pk_bf16_f32 v58, v28, v29\n\tv_cvt_pk_bf16_f32 v59, v30, v31\n\ts_nop 0\n\tv_permlane16_swap_b32 v56, v58\n\tv_permlane16_swap_b32 v57, v59\n\tbuffer_store_dwordx4 v[56:59], v243, s[24:27], 0 offen offset:32\n\t" \
    "v_pk_add_f32 v[70:71], v[70:71], v[38:39]\n\tv_pk_mul_f32 v[32:33], v[64:65], s[68:69]\n\tv_pk_mul_f32 v[34:35], v[66:67], s[68:69]\n\tv_pk_mul_f32 v[36:37], v[68:69], s[68:69]\n\tv_pk_mul_f32 v[38:39], v[70:71], s[68:69]\n\tv_exp_f32 v32, v32\n\tv_exp_f32 v33, v33\n\tv_exp_f32 v34, v34\n\t" \
    "v_exp_f32 v35, v35\n\tv_exp_f32 v36, v36\n\tv_exp_f32 v37, v37\n\tv_exp_f32 v38, v38\n\tv_exp_f32 v39, v39\n\tv_pk_add_f32 v[32:33], v[32:33], 1.0 op_sel_hi:[1,0]\n\tv_pk_add_f32 v[34:35], v[34:35], 1.0 op_sel_hi:[1,0]\n\tv_pk_add_f32 v[36:37], v[36:37], 1.0 op_sel_hi:[1,0]\n\t" \
    "v_pk_add_f32 v[38:39], v[38:39], 1.0 op_sel_hi:[1,0]\n\tv_rcp_f32 v32, v32\n\tv_rcp_f32 v33, v33\n\tv_rcp_f32 v34, v34\n\tv_rcp_f32 v35, v35\n\tv_rcp_f32 v36, v36\n\tv_rcp_f32 v37, v37\n\tv_rcp_f32 v38, v38\n\t" \
    "v_rcp_f32 v39, v39\n\tv_pk_mul_f32 v[64:65], v[64:65], v[112:113]\n\tv_pk_mul_f32 v[66:67], v[66:67], v[114:115]\n\tv_pk_mul_f32 v[68:69], v[68:69], v[116:117]\n\tv_pk_mul_f32 v[70:71], v[70:71], v[118:119]\n\tv_pk_fma_f32 v[32:33], v[32:33], v[64:65], v[88:89]\n\tv_pk_fma_f32 v[34:35], v[34:35], v[66:67], v[90:91]\n\tv_pk_fma_f32 v[36:37], v[36:37], v[68:69], v[92:93]\n\t" \
    "v_pk_fma_f32 v[38:39], v[38:39], v[70:71], v[94:95]\n\tv_cvt_pk_bf16_f32 v64, v32, v33\n\tv_cvt_pk_bf16_f32 v65, v34, v35\n\tv_cvt_pk_bf16_f32 v66, v36, v37\n\tv_cvt_pk_bf16_f32 v67, v38, v39\n\ts_nop 0\n\tv_permlane16_swap_b32 v64, v66\n\tv_permlane16_swap_b32 v65, v67\n\t" \
    "buffer_store_dwordx4 v[64:67], v243, s[24:27], 0 offen offset:64\n\ts_branch 103f\n\t102:\n\tbuffer_store_dwordx4 v[72:75], v8, s[8:11], s45 offen offset:0 sc1\n\tbuffer_store_dwordx4 v[80:83], v8, s[8:11], s45 offen offset:1024 sc1\n\tbuffer_store_dwordx4 v[88:91], v8, s[8:11], s45 offen offset:2048 sc1\n\ts_add_u32 s49, s49, 4096\n\ts_waitcnt vmcnt(0) lgkmcnt(0)\n\t" \
    "buffer_store_dwordx2 v[248:249], off, s[8:11], s49 sc1\n\ts_load_dwordx8 s[12:19], s[0:1], 16\n\ts_lshr_b32 s67, s40, 3\n\ts_sub_u32 s67, s67, 24\n\ts_lshl_b32 s67, s67, 2\n\ts_add_u32 s67, s67, s38\n\ts_lshl_b32 s65, s67, 6\n\tv_add_u32 v6, s65, v240\n\t" \
    "v_mul_u32_u24 v7, 7282, v6\n\tv_lshrrev_b32 v7, 16, v7\n\tv_mul_u32_u24 v5, 9, v7\n\tv_sub_u32 v6, v6, v5\n\tv_mul_u32_u24 v7, 9216, v7\n\tv_lshl_add_u32 v6, v6, 8, v7\n\tv_lshlrev_b32 v7, 3, v244\n\tv_lshl_add_u32 v7, v245, 12, v7\n\t" \
    "s_and_b32 s65, s40, 7\n\ts_lshr_b32 s73, s65, 1\n\ts_mul_i32 s66, s73, 2304\n\ts_mul_i32 s69, s57, 9437184\n\ts_add_u32 s66, s66, s69\n\ts_mul_i32 s73, s73, 4718592\n\ts_and_b32 s65, s65, 1\n\ts_lshl_b32 s65, s65, 11\n\t" \
    "s_add_u32 s65, s65, s73\n\ts_waitcnt lgkmcnt(0)\n\ts_add_u32 s12, s12, s66\n\ts_addc_u32 s13, s13, 0\n\ts_sub_u32 s14, s14, s66\n\ts_add_u32 s16, s16, s65\n\ts_addc_u32 s17, s17, 0\n\ts_sub_u32 s18, s18, s65\n\t" \
    "v_mov_b32 v238, 2146435072\n\tv_mov_b32 v236, 2146435072\n\ts_lshl_b32 s69, s57, 7\n\tv_mov_b32 v237, v240\n\tv_mul_u32_u24 v237, 12, v237\n\ts_add_u32 s73, s67, 196\n\tv_add_u32 v237, s73, v237\n\tv_lshrrev_b32 v5, 3, v237\n\t" \
    "v_lshlrev_b32 v5, 6, v5\n\tv_cmp_le_u32 vcc, 144, v237\n\tv_mov_b32 v4, 1120\n\tv_and_b32 v3, 7, v237\n\tv_cndmask_b32 v4, 0, v4, vcc\n\tv_lshl_add_u32 v5, v3, 2, v5\n\tv_sub_u32 v5, v5, v4\n\tv_subrev_u32 v237, 196, v237\n\t" \
    "v_cmp_gt_u32 vcc, 92, v237\n\tv_lshlrev_b32 v5, 12, v5\n\ts_mov_b32 s43, 0\n\tv_cndmask_b32 v237, v238, v5, vcc\n\ts_nop 0\n\tv_readlane_b32 s41, v236, 0\n\tv_readlane_b32 s42, v237, 0\n\ts_nop 4\n\t" \
    "buffer_load_dword v15, v7, s[16:19], s42 offen\n\ts_add_u32 s43, s43, 1\n\tv_readlane_b32 s42, v237, s43\n\ts_nop 4\n\tbuffer_load_dword v15, v7, s[16:19], s42 offen\n\ts_add_u32 s43, s43, 1\n\tv_readlane_b32 s42, v237, s43\n\ts_nop 4\n\t" \
    "buffer_load_dword v15, v7, s[16:19], s42 offen\n\ts_add_u32 s43, s43, 1\n\tv_readlane_b32 s42, v237, s43\n\ts_nop 4\n\tbuffer_load_dword v15, v7, s[16:19], s42 offen\n\ts_add_u32 s43, s43, 1\n\tv_readlane_b32 s42, v237, s43\n\ts_nop 4\n\t" \
    "buffer_load_dword v15, v7, s[16:19], s42 offen\n\ts_add_u32 s43, s43, 1\n\tv_readlane_b32 s42, v237, s43\n\ts_nop 4\n\tbuffer_load_dword v15, v7, s[16:19], s42 offen\n\ts_add_u32 s43, s43, 1\n\tv_readlane_b32 s42, v237, s43\n\ts_nop 4\n\t" \
    "buffer_load_dword v15, v7, s[16:19], s42 offen\n\ts_add_u32 s43, s43, 1\n\tv_readlane_b32 s42, v237, s43\n\ts_nop 4\n\tbuffer_load_dword v15, v7, s[16:19], s42 offen\n\ts_add_u32 s43, s43, 1\n\tv_readlane_b32 s42, v237, s43\n\ts_nop 4\n\t" \
    "s_waitcnt vmcnt(0)\n\ts_branch 103f\n\t101:\n\ts_load_dwordx8 s[12:19], s[0:1], 16\n\ts_lshr_b32 s67, s40, 3\n\ts_sub_u32 s67, s67, 27\n\ts_lshl_b32 s67, s67, 2\n\ts_add_u32 s67, s67, s38\n\t" \
    "s_lshl_b32 s65, s67, 6\n\tv_add_u32 v6, s65, v240\n\tv_mul_u32_u24 v7, 7282, v6\n\tv_lshrrev_b32 v7, 16, v7\n\tv_mul_u32_u24 v5, 9, v7\n\tv_sub_u32 v6, v6, v5\n\tv_mul_u32_u24 v7, 9216, v7\n\tv_lshl_add_u32 v6, v6, 8, v7\n\t" \
    "v_lshlrev_b32 v7, 3, v244\n\tv_lshl_add_u32 v7, v245, 12, v7\n\ts_and_b32 s65, s40, 7\n\ts_lshr_b32 s73, s65, 1\n\ts_mul_i32 s66, s73, 2304\n\ts_mul_i32 s69, s57, 9437184\n\ts_add_u32 s66, s66, s69\n\ts_mul_i32 s73, s73, 4718592\n\t" \
    "s_and_b32 s65, s65, 1\n\ts_lshl_b32 s65, s65, 11\n\ts_add_u32 s65, s65, s73\n\ts_waitcnt lgkmcnt(0)\n\ts_add_u32 s12, s12, s66\n\ts_addc_u32 s13, s13, 0\n\ts_sub_u32 s14, s14, s66\n\ts_add_u32 s16, s16, s65\n\t" \
    "s_addc_u32 s17, s17, 0\n\ts_sub_u32 s18, s18, s65\n\tv_mov_b32 v238, 2146435072\n\tv_mov_b32 v236, 2146435072\n\ts_lshl_b32 s69, s57, 7\n\tv_mov_b32 v237, v240\n\ts_xor_b32 s73, s69, 6193280\n\tv_writelane_b32 v236, s73, 0\n\t" \
    "v_writelane_b32 v237, 0, 0\n\ts_xor_b32 s73, s69, 7501952\n\tv_writelane_b32 v236, s73, 1\n\tv_writelane_b32 v237, 0, 1\n\ts_xor_b32 s73, s69, 8810624\n\tv_writelane_b32 v236, s73, 2\n\tv_writelane_b32 v237, 0, 2\n\tv_writelane_b32 v237, 6, 3\n\t" \
    "s_xor_b32 s73, s69, 6193152\n\tv_writelane_b32 v236, s73, 4\n\tv_writelane_b32 v237, 0, 4\n\ts_xor_b32 s73, s69, 7501824\n\tv_writelane_b32 v236, s73, 5\n\tv_writelane_b32 v237, 0, 5\n\ts_xor_b32 s73, s69, 8810496\n\tv_writelane_b32 v236, s73, 6\n\t" \
    "v_writelane_b32 v237, 0, 6\n\tv_mul_u32_u24 v237, 20, v237\n\ts_add_u32 s73, s67, -120\n\tv_add_u32 v237, s73, v237\n\tv_cmp_le_i32 vcc, 40, v237\n\tv_add_u32 v5, 48, v237\n\ts_nop 0\n\tv_cndmask_b32 v237, v237, v5, vcc\n\t" \
    "v_lshrrev_b32 v5, 3, v237\n\tv_lshlrev_b32 v5, 6, v5\n\tv_cmp_le_u32 vcc, 144, v237\n\tv_mov_b32 v4, 1120\n\tv_and_b32 v3, 7, v237\n\tv_cndmask_b32 v4, 0, v4, vcc\n\tv_lshl_add_u32 v5, v3, 2, v5\n\tv_sub_u32 v5, v5, v4\n\t" \
    "v_subrev_u32 v237, 0, v237\n\tv_cmp_gt_u32 vcc, 196, v237\n\tv_lshlrev_b32 v5, 12, v5\n\ts_mov_b32 s43, 0\n\tv_cndmask_b32 v237, v238, v5, vcc\n\ts_nop 0\n\tv_readlane_b32 s41, v236, 0\n\tv_readlane_b32 s42, v237, 0\n\t" \
    "s_nop 4\n\ts_sub_u32 s65, s40, 216\n\ts_lshl_b32 s44, s65, 4\n\ts_mul_i32 s66, s65, 12\n\ts_add_u32 s66, s66, 96\n\ts_cmp_lt_u32 s65, 24\n\ts_cselect_b32 s44, s44, s66\n\ts_cselect_b32 s45, 0, 2146435072\n\t" \
    "v_cmp_eq_u32 vcc, 0, v240\n\tv_subrev_u32 v13, 8, v8\n\tv_mov_b32 v12, 2146435072\n\tv_cndmask_b32 v13, v13, v12, vcc\n\ts_mov_b32 s4, s2\n\ts_mov_b32 s5, s3\n\ts_mov_b32 s6, 98f-99b\n\ts_mov_b32 s7, 0x20000\n\t" \
    "v_lshlrev_b32 v14, 7, v240\n\ts_mov_b32 s46, 0xffff\n\tv_and_b32 v204, 15, v240\n\tv_lshlrev_b32 v205, 3, v245\n\tv_sub_u32 v206, v205, v204\n\tv_add_u32 v206, 31, v206\n\tv_and_b32 v207, 3, v206\n\tv_mul_u32_u24 v207, 4160, v207\n\t" \
    "v_and_b32 v206, 0xfc, v206\n\tv_lshl_add_u32 v0, v206, 1, v207\n\ts_mul_i32 s65, s38, 16640\n\ts_add_u32 s65, s65, 73984\n\tv_add_u32 v0, s65, v0\n\ts_add_u32 s66, s65, 1040\n\tv_sub_u32 v2, s66, v8\n\tv_bfe_u32 v206, v240, 1, 2\n\t" \
    "v_mul_u32_u24 v206, 4160, v206\n\tv_and_b32 v207, 16, v8\n\tv_add3_u32 v9, v206, v207, s65\n\ts_mul_i32 s66, s38, 4624\n\tv_lshl_add_u32 v3, v204, 5, v205\n\tv_add_u32 v3, s66, v3\n\tv_add3_u32 v1, v3, v205, 32\n\tv_add_u32 v3, s66, v239\n\t" \
    "s_lshl_b32 s73, s38, 4\n\tv_add_u32 v206, s73, v245\n\tv_bfe_u32 v207, v240, 2, 2\n\tv_mul_u32_u24 v4, 4624, v207\n\tv_lshl_add_u32 v4, v245, 4, v4\n\tv_and_b32 v207, 3, v240\n\tv_lshl_add_u32 v4, v207, 7, v4\n\tv_add_u32 v4, s54, v4\n\t" \
    "v_mul_u32_u24 v5, 12288, v206\n\tv_add_u32 v5, v5, v244\n\tv_lshrrev_b32 v206, 5, v240\n\ts_lshl_b32 s73, s38, 2\n\tv_add_u32 v206, s73, v206\n\tv_mul_u32_u24 v206, 4624, v206\n\tv_and_b32 v207, 0x1f0, v8\n\tv_add_u32 v12, v206, v207\n\t" \
    "v_mul_u32_u24 v10, 9216, v245\n\tv_add_u32 v10, v10, v244\n\tv_mul_u32_u24 v11, 4624, v204\n\tv_lshl_add_u32 v11, v245, 4, v11\n\tv_add_u32 v11, s54, v11\n\ts_mul_i32 s62, s38, 589824\n\ts_lshl_b32 s73, s44, 4\n\ts_add_u32 s62, s62, s73\n\t" \
    "s_add_u32 s62, s62, 2490368\n\tv_lshrrev_b32 v206, 3, v204\n\tv_add_u32 v206, 2, v206\n\tv_cmp_eq_u32 vcc, v206, v245\n\tv_and_b32 v207, 1, v204\n\tv_lshlrev_b32 v207, 4, v207\n\tv_lshlrev_b32 v207, v207, s46\n\tv_cndmask_b32 v207, 0, v207, vcc\n\t" \
    "v_bfe_u32 v206, v204, 1, 2\n\tv_cmp_eq_u32 vcc, 0, v206\n\tv_mov_b32 v196, 0\n\ts_nop 0\n\tv_cndmask_b32 v232, 0, v207, vcc\n\tv_cmp_eq_u32 vcc, 1, v206\n\tv_mov_b32 v197, 0\n\ts_nop 0\n\t" \
    "v_cndmask_b32 v233, 0, v207, vcc\n\tv_cmp_eq_u32 vcc, 2, v206\n\tv_mov_b32 v198, 0\n\ts_nop 0\n\tv_cndmask_b32 v234, 0, v207, vcc\n\tv_cmp_eq_u32 vcc, 3, v206\n\tv_mov_b32 v199, 0\n\ts_nop 0\n\t" \
    "v_cndmask_b32 v235, 0, v207, vcc\n\ts_add_u32 s65, s44, 0\n\ts_lshr_b32 s65, s65, 6\n\ts_cmp_eq_u32 s65, 8\n\ts_cselect_b32 s65, 1, s65\n\ts_and_b32 s65, s65, 3\n\ts_lshl_b32 s47, s65, 11\n\ts_lshr_b32 s65, s54, 1\n\t" \
    "s_add_u32 s47, s47, s65\n\ts_add_u32 s47, s47, 17408\n\ts_add_u32 s65, s44, 11\n\ts_lshr_b32 s65, s65, 6\n\ts_cmp_eq_u32 s65, 8\n\ts_cselect_b32 s65, 1, s65\n\ts_and_b32 s65, s65, 3\n\ts_lshl_b32 s50, s65, 11\n\t" \
    "s_lshr_b32 s65, s54, 1\n\ts_add_u32 s50, s50, s65\n\ts_add_u32 s50, s50, 17408\n\tds_write_b128 v9, v[196:199] offset:4128\n\tbuffer_load_dword v15, v6, s[12:15], s41 offen\n\tbuffer_load_dword v15, v7, s[16:19], s42 offen\n\ts_add_u32 s43, s43, 1\n\tv_readlane_b32 s41, v236, s43\n\t" \
    "v_readlane_b32 s42, v237, s43\n\ts_nop 4\n\ts_waitcnt vmcnt(2)\n\tbuffer_store_dwordx2 v[248:249], off, s[8:11], s48 sc1\n\tv_alignbit_b32 v196, v167, v167, 16\n\tv_alignbit_b32 v197, v166, v166, 16\n\tv_alignbit_b32 v198, v165, v165, 16\n\tv_alignbit_b32 v199, v164, v164, 16\n\t" \
    "ds_write_b128 v2, v[196:199] offset:3072\n\tv_bfi_b32 v200, s46, v167, v166\n\tv_bfi_b32 v201, s46, v166, v165\n\tv_bfi_b32 v202, s46, v165, v164\n\tv_bfi_b32 v203, s46, v164, v181\n\tds_write_b128 v2, v[200:203] offset:7232\n\tv_alignbit_b32 v196, v166, v166, 16\n\tv_alignbit_b32 v197, v165, v165, 16\n\t" \
    "v_alignbit_b32 v198, v164, v164, 16\n\tv_alignbit_b32 v199, v181, v181, 16\n\tds_write_b128 v2, v[196:199] offset:11392\n\tv_bfi_b32 v200, s46, v166, v165\n\tv_bfi_b32 v201, s46, v165, v164\n\tv_bfi_b32 v202, s46, v164, v181\n\tv_bfi_b32 v203, s46, v181, v180\n\tds_write_b128 v2, v[200:203] offset:15552\n\t" \
    "v_alignbit_b32 v196, v171, v171, 16\n\tv_alignbit_b32 v197, v170, v170, 16\n\tv_alignbit_b32 v198, v169, v169, 16\n\tv_alignbit_b32 v199, v168, v168, 16\n\tds_write_b128 v2, v[196:199] offset:2048\n\tv_bfi_b32 v200, s46, v171, v170\n\tv_bfi_b32 v201, s46, v170, v169\n\tv_bfi_b32 v202, s46, v169, v168\n\t" \
    "v_bfi_b32 v203, s46, v168, v183\n\tds_write_b128 v2, v[200:203] offset:6208\n\tv_alignbit_b32 v196, v170, v170, 16\n\tv_alignbit_b32 v197, v169, v169, 16\n\tv_alignbit_b32 v198, v168, v168, 16\n\tv_alignbit_b32 v199, v183, v183, 16\n\tds_write_b128 v2, v[196:199] offset:10368\n\tv_bfi_b32 v200, s46, v170, v169\n\t" \
    "v_bfi_b32 v201, s46, v169, v168\n\tv_bfi_b32 v202, s46, v168, v183\n\tv_bfi_b32 v203, s46, v183, v182\n\tds_write_b128 v2, v[200:203] offset:14528\n\tv_alignbit_b32 v196, v175, v175, 16\n\tv_alignbit_b32 v197, v174, v174, 16\n\tv_alignbit_b32 v198, v173, v173, 16\n\tv_alignbit_b32 v199, v172, v172, 16\n\t" \
    "ds_write_b128 v2, v[196:199] offset:1024\n\tv_bfi_b32 v200, s46, v175, v174\n\tv_bfi_b32 v201, s46, v174, v173\n\tv_bfi_b32 v202, s46, v173, v172\n\tv_bfi_b32 v203, s46, v172, v185\n\tds_write_b128 v2, v[200:203] offset:5184\n\tv_alignbit_b32 v196, v174, v174, 16\n\tv_alignbit_b32 v197, v173, v173, 16\n\t" \
    "v_alignbit_b32 v198, v172, v172, 16\n\tv_alignbit_b32 v199, v185, v185, 16\n\tds_write_b128 v2, v[196:199] offset:9344\n\tv_bfi_b32 v200, s46, v174, v173\n\tv_bfi_b32 v201, s46, v173, v172\n\tv_bfi_b32 v202, s46, v172, v185\n\tv_bfi_b32 v203, s46, v185, v184\n\tds_write_b128 v2, v[200:203] offset:13504\n\t" \
    "v_alignbit_b32 v196, v179, v179, 16\n\tv_alignbit_b32 v197, v178, v178, 16\n\tv_alignbit_b32 v198, v177, v177, 16\n\tv_alignbit_b32 v199, v176, v176, 16\n\tds_write_b128 v2, v[196:199] offset:0\n\tv_bfi_b32 v200, s46, v179, v178\n\tv_bfi_b32 v201, s46, v178, v177\n\tv_bfi_b32 v202, s46, v177, v176\n\t" \
    "v_bfi_b32 v203, s46, v176, v187\n\tds_write_b128 v2, v[200:203] offset:4160\n\tv_alignbit_b32 v196, v178, v178, 16\n\tv_alignbit_b32 v197, v177, v177, 16\n\tv_alignbit_b32 v198, v176, v176, 16\n\tv_alignbit_b32 v199, v187, v187, 16\n\tds_write_b128 v2, v[196:199] offset:8320\n\tv_bfi_b32 v200, s46, v178, v177\n\t" \
    "v_bfi_b32 v201, s46, v177, v176\n\tv_bfi_b32 v202, s46, v176, v187\n\tv_bfi_b32 v203, s46, v187, v186\n\tds_write_b128 v2, v[200:203] offset:12480\n\tv_lshl_or_b32 v188, v188, 16, v188\n\tv_and_b32 v228, v188, v232\n\tv_and_b32 v229, v188, v233\n\tv_and_b32 v230, v188, v234\n\t" \
    "v_and_b32 v231, v188, v235\n\tds_read_b64 a[0:1], v0 offset:4032\n\tds_read_b64 a[2:3], v0 offset:4040\n\tds_read_b64 a[4:5], v0 offset:3520\n\tds_read_b64 a[6:7], v0 offset:3528\n\tds_read_b64 a[8:9], v0 offset:3008\n\tds_read_b64 a[10:11], v0 offset:3016\n\tds_read_b64 a[12:13], v0 offset:2496\n\t" \
    "ds_read_b64 a[14:15], v0 offset:2504\n\tds_read_b64 a[16:17], v0 offset:1984\n\tds_read_b64 a[18:19], v0 offset:1992\n\tds_read_b64 a[20:21], v0 offset:1472\n\tds_read_b64 a[22:23], v0 offset:1480\n\tds_read_b64 a[24:25], v0 offset:960\n\tds_read_b64 a[26:27], v0 offset:968\n\tds_read_b64 a[28:29], v0 offset:448\n\t" \
    "ds_read_b64 a[30:31], v0 offset:456\n\ts_barrier\n\tv_mov_b32 v196, 0\n\tv_mov_b32 v197, 0\n\tv_mov_b32 v198, 0\n\tv_mov_b32 v199, 0\n\tds_write_b128 v12, v[196:199]\n\tds_write_b128 v12, v[196:199] offset:9248\n\t" \
    "s_sleep 30\n\ts_mov_b32 s71, 1048576\n\t125:\n\tbuffer_load_dwordx2 v[250:251], v239, s[8:11], s47 offen sc1\n\tbuffer_load_dwordx2 v[252:253], v239, s[8:11], s50 offen sc1\n\ts_waitcnt vmcnt(0)\n\tv_cmp_ne_u64 vcc, s[36:37], v[250:251]\n\tv_cmp_ne_u64 s[74:75], s[36:37], v[252:253]\n\t" \
    "s_or_b64 s[74:75], s[74:75], vcc\n\ts_cbranch_scc0 126f\n\ts_sleep 16\n\ts_sub_u32 s71, s71, 1\n\ts_cbranch_scc0 125b\n\t126:\n\tbuffer_load_dwordx4 v[68:71], v10, s[8:11], s62 offen sc1\n\ts_add_u32 s62, s62, 36864\n\t" \
    "buffer_load_dwordx4 v[72:75], v10, s[8:11], s62 offen sc1\n\ts_add_u32 s62, s62, 36864\n\tbuffer_load_dwordx4 v[76:79], v10, s[8:11], s62 offen sc1\n\ts_add_u32 s62, s62, 36864\n\tbuffer_load_dwordx4 v[80:83], v10, s[8:11], s62 offen sc1\n\ts_add_u32 s62, s62, 36864\n\tbuffer_load_dwordx4 v[84:87], v10, s[8:11], s62 offen sc1\n\ts_add_u32 s62, s62, 36864\n\t" \
    "buffer_load_dwordx4 v[88:91], v10, s[8:11], s62 offen sc1\n\ts_add_u32 s62, s62, 36864\n\tbuffer_load_dwordx4 v[92:95], v10, s[8:11], s62 offen sc1\n\ts_add_u32 s62, s62, 36864\n\tbuffer_load_dwordx4 v[96:99], v10, s[8:11], s62 offen sc1\n\ts_add_u32 s62, s62, 36864\n\tbuffer_load_dwordx4 v[100:103], v10, s[8:11], s62 offen sc1\n\ts_add_u32 s62, s62, 36864\n\t" \
    "buffer_load_dwordx4 v[104:107], v10, s[8:11], s62 offen sc1\n\ts_add_u32 s62, s62, 36864\n\tbuffer_load_dwordx4 v[108:111], v10, s[8:11], s62 offen sc1\n\ts_add_u32 s62, s62, 36864\n\tbuffer_load_dwordx4 v[112:115], v10, s[8:11], s62 offen sc1\n\ts_add_u32 s62, s62, 36864\n\tbuffer_load_dwordx4 v[116:119], v10, s[8:11], s62 offen sc1\n\ts_add_u32 s62, s62, 36864\n\t" \
    "buffer_load_dwordx4 v[120:123], v10, s[8:11], s62 offen sc1\n\ts_add_u32 s62, s62, 36864\n\tbuffer_load_dwordx4 v[124:127], v10, s[8:11], s62 offen sc1\n\ts_add_u32 s62, s62, 36864\n\tbuffer_load_dwordx4 v[128:131], v10, s[8:11], s62 offen sc1\n\ts_add_u32 s62, s62, 36864\n\ts_waitcnt vmcnt(15)\n\tds_write_b128 v11, v[68:71] offset:512\n\t" \
    "s_waitcnt vmcnt(14)\n\tds_write_b128 v11, v[72:75] offset:576\n\ts_waitcnt vmcnt(13)\n\tds_write_b128 v11, v[76:79] offset:640\n\ts_waitcnt vmcnt(12)\n\tds_write_b128 v11, v[80:83] offset:704\n\ts_waitcnt vmcnt(11)\n\tds_write_b128 v11, v[84:87] offset:768\n\t" \
    "s_waitcnt vmcnt(10)\n\tds_write_b128 v11, v[88:91] offset:832\n\ts_waitcnt vmcnt(9)\n\tds_write_b128 v11, v[92:95] offset:896\n\ts_waitcnt vmcnt(8)\n\tds_write_b128 v11, v[96:99] offset:960\n\ts_waitcnt vmcnt(7)\n\tds_write_b128 v11, v[100:103] offset:1024\n\t" \
    "s_waitcnt vmcnt(6)\n\tds_write_b128 v11, v[104:107] offset:1088\n\ts_waitcnt vmcnt(5)\n\tds_write_b128 v11, v[108:111] offset:1152\n\ts_waitcnt vmcnt(4)\n\tds_write_b128 v11, v[112:115] offset:1216\n\ts_waitcnt vmcnt(3)\n\tds_write_b128 v11, v[116:119] offset:1280\n\t" \
    "s_waitcnt vmcnt(2)\n\tds_write_b128 v11, v[120:123] offset:1344\n\ts_waitcnt vmcnt(1)\n\tds_write_b128 v11, v[124:127] offset:1408\n\ts_waitcnt vmcnt(0)\n\tds_write_b128 v11, v[128:131] offset:1472\n\ts_waitcnt lgkmcnt(0)\n\ts_barrier\n\t" \
    "ds_read_b128 v[68:71], v1 offset:448\n\tds_read_b128 v[72:75], v1 offset:960\n\tds_read_b128 v[76:79], v1 offset:1472\n\tds_read_b128 v[80:83], v1 offset:1984\n\tds_read_b128 v[84:87], v1 offset:2496\n\tds_read_b128 v[88:91], v1 offset:3008\n\tds_read_b128 v[92:95], v1 offset:3520\n\tds_read_b128 v[96:99], v1 offset:4032\n\t" \
    "s_add_u32 s68, s44, s38\n\ts_add_u32 s68, s68, 4\n\ts_lshl_b32 s66, s68, 1\n\ts_lshl_b32 s68, s68, 12\n\tbuffer_load_dwordx4 v[42:45], v8, s[76:79], s68 offen offset:0\n\tbuffer_load_dwordx4 v[46:49], v8, s[76:79], s68 offen offset:1024\n\tbuffer_load_dwordx4 v[50:53], v8, s[76:79], s68 offen offset:2048\n\tbuffer_load_dwordx4 v[54:57], v8, s[76:79], s68 offen offset:3072\n\t" \
    "buffer_load_dwordx2 v[58:59], v13, s[76:79], s68 offen\n\tbuffer_load_dwordx2 v[60:61], v8, s[76:79], s68 offen offset:1016\n\tbuffer_load_dwordx2 v[62:63], v8, s[76:79], s68 offen offset:2040\n\tbuffer_load_dwordx2 v[64:65], v8, s[76:79], s68 offen offset:3064\n\tbuffer_load_ushort v66, off, s[80:83], s66\n\tbuffer_load_dword v15, v6, s[12:15], s41 offen\n\tbuffer_load_dword v15, v7, s[16:19], s42 offen\n\ts_add_u32 s43, s43, 1\n\t" \
    "v_readlane_b32 s41, v236, s43\n\tv_readlane_b32 s42, v237, s43\n\ts_nop 4\n\ts_waitcnt lgkmcnt(0)\n\tv_mfma_f32_16x16x32_bf16 v[132:135], v[228:231], v[68:71], 0\n\tds_read_b64 a[32:33], v0 offset:3968\n\tds_read_b64 a[34:35], v0 offset:3976\n\tv_mfma_f32_16x16x32_bf16 v[132:135], a[0:3], v[68:71], v[132:135]\n\t" \
    "ds_read_b64 a[36:37], v0 offset:3456\n\tds_read_b64 a[38:39], v0 offset:3464\n\tv_mfma_f32_16x16x32_bf16 v[136:139], v[228:231], v[72:75], 0\n\tds_read_b64 a[40:41], v0 offset:2944\n\tds_read_b64 a[42:43], v0 offset:2952\n\tv_mfma_f32_16x16x32_bf16 v[136:139], a[0:3], v[72:75], v[136:139]\n\tds_read_b64 a[44:45], v0 offset:2432\n\tds_read_b64 a[46:47], v0 offset:2440\n\t" \
    "v_mfma_f32_16x16x32_bf16 v[136:139], a[4:7], v[68:71], v[136:139]\n\tds_read_b64 a[48:49], v0 offset:1920\n\tds_read_b64 a[50:51], v0 offset:1928\n\tv_mfma_f32_16x16x32_bf16 v[140:143], v[228:231], v[76:79], 0\n\tds_read_b64 a[52:53], v0 offset:1408\n\tv_mfma_f32_16x16x32_bf16 v[140:143], a[0:3], v[76:79], v[140:143]\n\tds_read_b64 a[54:55], v0 offset:1416\n\tv_mfma_f32_16x16x32_bf16 v[140:143], a[4:7], v[72:75], v[140:143]\n\t" \
    "ds_read_b64 a[56:57], v0 offset:896\n\tv_mfma_f32_16x16x32_bf16 v[140:143], a[8:11], v[68:71], v[140:143]\n\tds_read_b64 a[58:59], v0 offset:904\n\tv_mfma_f32_16x16x32_bf16 v[144:147], v[228:231], v[80:83], 0\n\tds_read_b64 a[60:61], v0 offset:384\n\tv_mfma_f32_16x16x32_bf16 v[144:147], a[0:3], v[80:83], v[144:147]\n\tds_read_b64 a[62:63], v0 offset:392\n\tv_mfma_f32_16x16x32_bf16 v[144:147], a[4:7], v[76:79], v[144:147]\n\t" \
    "ds_read_b128 v[100:103], v1 offset:384\n\tv_mfma_f32_16x16x32_bf16 v[144:147], a[8:11], v[72:75], v[144:147]\n\tds_read_b128 v[104:107], v1 offset:896\n\tv_mfma_f32_16x16x32_bf16 v[144:147], a[12:15], v[68:71], v[144:147]\n\tds_read_b128 v[108:111], v1 offset:1408\n\tv_mfma_f32_16x16x32_bf16 v[148:151], v[228:231], v[84:87], 0\n\tds_read_b128 v[112:115], v1 offset:1920\n\tv_mfma_f32_16x16x32_bf16 v[148:151], a[0:3], v[84:87], v[148:151]\n\t" \
    "ds_read_b128 v[116:119], v1 offset:2432\n\tv_mfma_f32_16x16x32_bf16 v[148:151], a[4:7], v[80:83], v[148:151]\n\tds_read_b128 v[120:123], v1 offset:2944\n\tv_mfma_f32_16x16x32_bf16 v[148:151], a[8:11], v[76:79], v[148:151]\n\tbuffer_load_dword v15, v6, s[12:15], s41 offen\n\tds_read_b128 v[124:127], v1 offset:3456\n\tv_mfma_f32_16x16x32_bf16 v[148:151], a[12:15], v[72:75], v[148:151]\n\tbuffer_load_dword v15, v7, s[16:19], s42 offen\n\t" \
    "ds_read_b128 v[128:131], v1 offset:3968\n\tv_mfma_f32_16x16x32_bf16 v[148:151], a[16:19], v[68:71], v[148:151]\n\ts_add_u32 s43, s43, 1\n\tds_read_b64 a[64:65], v0 offset:3904\n\tv_mfma_f32_16x16x32_bf16 v[152:155], v[228:231], v[88:91], 0\n\tv_readlane_b32 s41, v236, s43\n\tv_readlane_b32 s42, v237, s43\n\ts_nop 4\n\t" \
    "ds_read_b64 a[66:67], v0 offset:3912\n\tv_mfma_f32_16x16x32_bf16 v[152:155], a[0:3], v[88:91], v[152:155]\n\tds_read_b64 a[68:69], v0 offset:3392\n\tv_mfma_f32_16x16x32_bf16 v[152:155], a[4:7], v[84:87], v[152:155]\n\tds_read_b64 a[70:71], v0 offset:3400\n\tv_mfma_f32_16x16x32_bf16 v[152:155], a[8:11], v[80:83], v[152:155]\n\tds_read_b64 a[72:73], v0 offset:2880\n\tv_mfma_f32_16x16x32_bf16 v[152:155], a[12:15], v[76:79], v[152:155]\n\t" \
    "ds_read_b64 a[74:75], v0 offset:2888\n\tv_mfma_f32_16x16x32_bf16 v[152:155], a[16:19], v[72:75], v[152:155]\n\tds_read_b64 a[76:77], v0 offset:2368\n\tv_mfma_f32_16x16x32_bf16 v[152:155], a[20:23], v[68:71], v[152:155]\n\tds_read_b64 a[78:79], v0 offset:2376\n\tv_mfma_f32_16x16x32_bf16 v[156:159], v[228:231], v[92:95], 0\n\tds_read_b64 a[80:81], v0 offset:1856\n\tv_mfma_f32_16x16x32_bf16 v[156:159], a[0:3], v[92:95], v[156:159]\n\t" \
    "ds_read_b64 a[82:83], v0 offset:1864\n\tv_mfma_f32_16x16x32_bf16 v[156:159], a[4:7], v[88:91], v[156:159]\n\tds_read_b64 a[84:85], v0 offset:1344\n\tv_mfma_f32_16x16x32_bf16 v[156:159], a[8:11], v[84:87], v[156:159]\n\tds_read_b64 a[86:87], v0 offset:1352\n\tv_mfma_f32_16x16x32_bf16 v[156:159], a[12:15], v[80:83], v[156:159]\n\tds_read_b64 a[88:89], v0 offset:832\n\tv_mfma_f32_16x16x32_bf16 v[156:159], a[16:19], v[76:79], v[156:159]\n\t" \
    "ds_read_b64 a[90:91], v0 offset:840\n\tv_mfma_f32_16x16x32_bf16 v[156:159], a[20:23], v[72:75], v[156:159]\n\tds_read_b64 a[92:93], v0 offset:320\n\tv_mfma_f32_16x16x32_bf16 v[156:159], a[24:27], v[68:71], v[156:159]\n\tds_read_b64 a[94:95], v0 offset:328\n\tv_mfma_f32_16x16x32_bf16 v[160:163], v[228:231], v[96:99], 0\n\tds_read_b64 a[96:97], v0 offset:3840\n\tv_mfma_f32_16x16x32_bf16 v[160:163], a[0:3], v[96:99], v[160:163]\n\t" \
    "ds_read_b64 a[98:99], v0 offset:3848\n\tv_mfma_f32_16x16x32_bf16 v[160:163], a[4:7], v[92:95], v[160:163]\n\tds_read_b64 a[100:101], v0 offset:3328\n\tv_mfma_f32_16x16x32_bf16 v[160:163], a[8:11], v[88:91], v[160:163]\n\tds_read_b64 a[102:103], v0 offset:3336\n\tv_mfma_f32_16x16x32_bf16 v[160:163], a[12:15], v[84:87], v[160:163]\n\tv_mfma_f32_16x16x32_bf16 v[160:163], a[16:19], v[80:83], v[160:163]\n\tv_mfma_f32_16x16x32_bf16 v[160:163], a[20:23], v[76:79], v[160:163]\n\t" \
    "v_mfma_f32_16x16x32_bf16 v[160:163], a[24:27], v[72:75], v[160:163]\n\tv_mfma_f32_16x16x32_bf16 v[160:163], a[28:31], v[68:71], v[160:163]\n\ts_waitcnt lgkmcnt(15)\n\tv_mfma_f32_16x16x32_bf16 v[160:163], a[32:35], v[128:131], v[160:163]\n\tds_read_b64 a[104:105], v0 offset:2816\n\tds_read_b64 a[106:107], v0 offset:2824\n\tv_mfma_f32_16x16x32_bf16 v[160:163], a[36:39], v[124:127], v[160:163]\n\tds_read_b64 a[108:109], v0 offset:2304\n\t" \
    "ds_read_b64 a[110:111], v0 offset:2312\n\tv_mfma_f32_16x16x32_bf16 v[160:163], a[40:43], v[120:123], v[160:163]\n\tds_read_b64 a[112:113], v0 offset:1792\n\tds_read_b64 a[114:115], v0 offset:1800\n\tv_mfma_f32_16x16x32_bf16 v[160:163], a[44:47], v[116:119], v[160:163]\n\tds_read_b64 a[116:117], v0 offset:1280\n\tds_read_b64 a[118:119], v0 offset:1288\n\tv_mfma_f32_16x16x32_bf16 v[160:163], a[48:51], v[112:115], v[160:163]\n\t" \
    "ds_read_b64 a[120:121], v0 offset:768\n\tds_read_b64 a[122:123], v0 offset:776\n\tv_mfma_f32_16x16x32_bf16 v[160:163], a[52:55], v[108:111], v[160:163]\n\tds_read_b64 a[124:125], v0 offset:256\n\tv_mfma_f32_16x16x32_bf16 v[160:163], a[56:59], v[104:107], v[160:163]\n\tds_read_b64 a[126:127], v0 offset:264\n\tv_mfma_f32_16x16x32_bf16 v[160:163], a[60:63], v[100:103], v[160:163]\n\tds_read_b64 a[128:129], v0 offset:3776\n\t" \
    "v_mfma_f32_16x16x32_bf16 v[156:159], a[32:35], v[124:127], v[156:159]\n\tds_read_b64 a[130:131], v0 offset:3784\n\tv_mfma_f32_16x16x32_bf16 v[156:159], a[36:39], v[120:123], v[156:159]\n\tds_read_b64 a[132:133], v0 offset:3264\n\tv_mfma_f32_16x16x32_bf16 v[156:159], a[40:43], v[116:119], v[156:159]\n\tds_read_b64 a[134:135], v0 offset:3272\n\tv_mfma_f32_16x16x32_bf16 v[156:159], a[44:47], v[112:115], v[156:159]\n\tds_read_b128 v[68:71], v1 offset:320\n\t" \
    "v_mfma_f32_16x16x32_bf16 v[156:159], a[48:51], v[108:111], v[156:159]\n\tds_read_b128 v[72:75], v1 offset:832\n\tv_mfma_f32_16x16x32_bf16 v[156:159], a[52:55], v[104:107], v[156:159]\n\tds_read_b128 v[76:79], v1 offset:1344\n\tv_mfma_f32_16x16x32_bf16 v[156:159], a[56:59], v[100:103], v[156:159]\n\tds_read_b128 v[80:83], v1 offset:1856\n\tv_mfma_f32_16x16x32_bf16 v[152:155], a[32:35], v[120:123], v[152:155]\n\tds_read_b128 v[84:87], v1 offset:2368\n\t" \
    "v_mfma_f32_16x16x32_bf16 v[152:155], a[36:39], v[116:119], v[152:155]\n\tds_read_b128 v[88:91], v1 offset:2880\n\tv_mfma_f32_16x16x32_bf16 v[152:155], a[40:43], v[112:115], v[152:155]\n\tds_read_b128 v[92:95], v1 offset:3392\n\tv_mfma_f32_16x16x32_bf16 v[152:155], a[44:47], v[108:111], v[152:155]\n\tds_read_b128 v[96:99], v1 offset:3904\n\tv_mfma_f32_16x16x32_bf16 v[152:155], a[48:51], v[104:107], v[152:155]\n\tds_read_b64 a[136:137], v0 offset:2752\n\t" \
    "v_mfma_f32_16x16x32_bf16 v[152:155], a[52:55], v[100:103], v[152:155]\n\tds_read_b64 a[138:139], v0 offset:2760\n\tv_mfma_f32_16x16x32_bf16 v[148:151], a[32:35], v[116:119], v[148:151]\n\tds_read_b64 a[140:141], v0 offset:2240\n\tv_mfma_f32_16x16x32_bf16 v[148:151], a[36:39], v[112:115], v[148:151]\n\tds_read_b64 a[142:143], v0 offset:2248\n\tv_mfma_f32_16x16x32_bf16 v[148:151], a[40:43], v[108:111], v[148:151]\n\tds_read_b64 a[144:145], v0 offset:1728\n\t" \
    "v_mfma_f32_16x16x32_bf16 v[148:151], a[44:47], v[104:107], v[148:151]\n\tds_read_b64 a[146:147], v0 offset:1736\n\tv_mfma_f32_16x16x32_bf16 v[148:151], a[48:51], v[100:103], v[148:151]\n\tds_read_b64 a[148:149], v0 offset:1216\n\tv_mfma_f32_16x16x32_bf16 v[144:147], a[32:35], v[112:115], v[144:147]\n\tds_read_b64 a[150:151], v0 offset:1224\n\tv_mfma_f32_16x16x32_bf16 v[144:147], a[36:39], v[108:111], v[144:147]\n\tds_read_b64 a[152:153], v0 offset:704\n\t" \
    "v_mfma_f32_16x16x32_bf16 v[144:147], a[40:43], v[104:107], v[144:147]\n\tds_read_b64 a[154:155], v0 offset:712\n\tv_mfma_f32_16x16x32_bf16 v[144:147], a[44:47], v[100:103], v[144:147]\n\tds_read_b64 a[156:157], v0 offset:192\n\tv_mfma_f32_16x16x32_bf16 v[140:143], a[32:35], v[108:111], v[140:143]\n\tds_read_b64 a[158:159], v0 offset:200\n\tv_mfma_f32_16x16x32_bf16 v[140:143], a[36:39], v[104:107], v[140:143]\n\tv_mfma_f32_16x16x32_bf16 v[140:143], a[40:43], v[100:103], v[140:143]\n\t" \
    "buffer_load_dword v15, v6, s[12:15], s41 offen\n\tv_mfma_f32_16x16x32_bf16 v[136:139], a[32:35], v[104:107], v[136:139]\n\tbuffer_load_dword v15, v7, s[16:19], s42 offen\n\tv_mfma_f32_16x16x32_bf16 v[136:139], a[36:39], v[100:103], v[136:139]\n\ts_add_u32 s43, s43, 1\n\tv_mfma_f32_16x16x32_bf16 v[132:135], a[32:35], v[100:103], v[132:135]\n\tv_readlane_b32 s41, v236, s43\n\tv_readlane_b32 s42, v237, s43\n\t" \
    "s_nop 4\n\ts_waitcnt lgkmcnt(12)\n\tv_mfma_f32_16x16x32_bf16 v[132:135], a[64:67], v[68:71], v[132:135]\n\tds_read_b64 a[160:161], v0 offset:3712\n\tds_read_b64 a[162:163], v0 offset:3720\n\tv_mfma_f32_16x16x32_bf16 v[136:139], a[64:67], v[72:75], v[136:139]\n\tds_read_b64 a[164:165], v0 offset:3200\n\tds_read_b64 a[166:167], v0 offset:3208\n\t" \
    "v_mfma_f32_16x16x32_bf16 v[136:139], a[68:71], v[68:71], v[136:139]\n\tds_read_b64 a[168:169], v0 offset:2688\n\tds_read_b64 a[170:171], v0 offset:2696\n\tv_mfma_f32_16x16x32_bf16 v[140:143], a[64:67], v[76:79], v[140:143]\n\tds_read_b64 a[172:173], v0 offset:2176\n\tds_read_b64 a[174:175], v0 offset:2184\n\tv_mfma_f32_16x16x32_bf16 v[140:143], a[68:71], v[72:75], v[140:143]\n\tds_read_b64 a[176:177], v0 offset:1664\n\t" \
    "ds_read_b64 a[178:179], v0 offset:1672\n\tv_mfma_f32_16x16x32_bf16 v[140:143], a[72:75], v[68:71], v[140:143]\n\tds_read_b64 a[180:181], v0 offset:1152\n\tv_mfma_f32_16x16x32_bf16 v[144:147], a[64:67], v[80:83], v[144:147]\n\tds_read_b64 a[182:183], v0 offset:1160\n\tv_mfma_f32_16x16x32_bf16 v[144:147], a[68:71], v[76:79], v[144:147]\n\tds_read_b64 a[184:185], v0 offset:640\n\tv_mfma_f32_16x16x32_bf16 v[144:147], a[72:75], v[72:75], v[144:147]\n\t" \
    "ds_read_b64 a[186:187], v0 offset:648\n\tv_mfma_f32_16x16x32_bf16 v[144:147], a[76:79], v[68:71], v[144:147]\n\tds_read_b64 a[188:189], v0 offset:128\n\tv_mfma_f32_16x16x32_bf16 v[148:151], a[64:67], v[84:87], v[148:151]\n\tds_read_b64 a[190:191], v0 offset:136\n\tv_mfma_f32_16x16x32_bf16 v[148:151], a[68:71], v[80:83], v[148:151]\n\tds_read_b128 v[100:103], v1 offset:256\n\tv_mfma_f32_16x16x32_bf16 v[148:151], a[72:75], v[76:79], v[148:151]\n\t" \
    "ds_read_b128 v[104:107], v1 offset:768\n\tv_mfma_f32_16x16x32_bf16 v[148:151], a[76:79], v[72:75], v[148:151]\n\tds_read_b128 v[108:111], v1 offset:1280\n\tv_mfma_f32_16x16x32_bf16 v[148:151], a[80:83], v[68:71], v[148:151]\n\tds_read_b128 v[112:115], v1 offset:1792\n\tv_mfma_f32_16x16x32_bf16 v[152:155], a[64:67], v[88:91], v[152:155]\n\tds_read_b128 v[116:119], v1 offset:2304\n\tv_mfma_f32_16x16x32_bf16 v[152:155], a[68:71], v[84:87], v[152:155]\n\t" \
    "ds_read_b128 v[120:123], v1 offset:2816\n\tv_mfma_f32_16x16x32_bf16 v[152:155], a[72:75], v[80:83], v[152:155]\n\tds_read_b128 v[124:127], v1 offset:3328\n\tv_mfma_f32_16x16x32_bf16 v[152:155], a[76:79], v[76:79], v[152:155]\n\tds_read_b128 v[128:131], v1 offset:3840\n\tv_mfma_f32_16x16x32_bf16 v[152:155], a[80:83], v[72:75], v[152:155]\n\tds_read_b64 a[192:193], v0 offset:3648\n\tv_mfma_f32_16x16x32_bf16 v[152:155], a[84:87], v[68:71], v[152:155]\n\t" \
    "ds_read_b64 a[194:195], v0 offset:3656\n\tv_mfma_f32_16x16x32_bf16 v[156:159], a[64:67], v[92:95], v[156:159]\n\tds_read_b64 a[196:197], v0 offset:3136\n\tv_mfma_f32_16x16x32_bf16 v[156:159], a[68:71], v[88:91], v[156:159]\n\tds_read_b64 a[198:199], v0 offset:3144\n\tv_mfma_f32_16x16x32_bf16 v[156:159], a[72:75], v[84:87], v[156:159]\n\tds_read_b64 a[200:201], v0 offset:2624\n\tv_mfma_f32_16x16x32_bf16 v[156:159], a[76:79], v[80:83], v[156:159]\n\t" \
    "ds_read_b64 a[202:203], v0 offset:2632\n\tv_mfma_f32_16x16x32_bf16 v[156:159], a[80:83], v[76:79], v[156:159]\n\tds_read_b64 a[204:205], v0 offset:2112\n\tv_mfma_f32_16x16x32_bf16 v[156:159], a[84:87], v[72:75], v[156:159]\n\tds_read_b64 a[206:207], v0 offset:2120\n\tv_mfma_f32_16x16x32_bf16 v[156:159], a[88:91], v[68:71], v[156:159]\n\tds_read_b64 a[208:209], v0 offset:1600\n\tv_mfma_f32_16x16x32_bf16 v[160:163], a[64:67], v[96:99], v[160:163]\n\t" \
    "ds_read_b64 a[210:211], v0 offset:1608\n\tv_mfma_f32_16x16x32_bf16 v[160:163], a[68:71], v[92:95], v[160:163]\n\tds_read_b64 a[212:213], v0 offset:1088\n\tv_mfma_f32_16x16x32_bf16 v[160:163], a[72:75], v[88:91], v[160:163]\n\tds_read_b64 a[214:215], v0 offset:1096\n\tv_mfma_f32_16x16x32_bf16 v[160:163], a[76:79], v[84:87], v[160:163]\n\tv_mfma_f32_16x16x32_bf16 v[160:163], a[80:83], v[80:83], v[160:163]\n\tv_mfma_f32_16x16x32_bf16 v[160:163], a[84:87], v[76:79], v[160:163]\n\t" \
    "v_mfma_f32_16x16x32_bf16 v[160:163], a[88:91], v[72:75], v[160:163]\n\tv_mfma_f32_16x16x32_bf16 v[160:163], a[92:95], v[68:71], v[160:163]\n\ts_waitcnt lgkmcnt(12)\n\tv_mfma_f32_16x16x32_bf16 v[160:163], a[96:99], v[128:131], v[160:163]\n\tds_read_b64 a[216:217], v0 offset:576\n\tv_mfma_f32_16x16x32_bf16 v[160:163], a[100:103], v[124:127], v[160:163]\n\tds_read_b64 a[218:219], v0 offset:584\n\tv_mfma_f32_16x16x32_bf16 v[160:163], a[104:107], v[120:123], v[160:163]\n\t" \
    "ds_read_b64 a[220:221], v0 offset:64\n\tv_mfma_f32_16x16x32_bf16 v[160:163], a[108:111], v[116:119], v[160:163]\n\tds_read_b64 a[222:223], v0 offset:72\n\tv_mfma_f32_16x16x32_bf16 v[160:163], a[112:115], v[112:115], v[160:163]\n\tds_read_b64 a[224:225], v0 offset:3584\n\tv_mfma_f32_16x16x32_bf16 v[160:163], a[116:119], v[108:111], v[160:163]\n\tds_read_b64 a[226:227], v0 offset:3592\n\tv_mfma_f32_16x16x32_bf16 v[160:163], a[120:123], v[104:107], v[160:163]\n\t" \
    "ds_read_b64 a[228:229], v0 offset:3072\n\tv_mfma_f32_16x16x32_bf16 v[160:163], a[124:127], v[100:103], v[160:163]\n\tds_read_b64 a[230:231], v0 offset:3080\n\tv_mfma_f32_16x16x32_bf16 v[156:159], a[96:99], v[124:127], v[156:159]\n\tds_read_b64 a[232:233], v0 offset:2560\n\tv_mfma_f32_16x16x32_bf16 v[156:159], a[100:103], v[120:123], v[156:159]\n\tds_read_b64 a[234:235], v0 offset:2568\n\tv_mfma_f32_16x16x32_bf16 v[156:159], a[104:107], v[116:119], v[156:159]\n\t" \
    "ds_read_b64 a[236:237], v0 offset:2048\n\tv_mfma_f32_16x16x32_bf16 v[156:159], a[108:111], v[112:115], v[156:159]\n\tds_read_b64 a[238:239], v0 offset:2056\n\tv_mfma_f32_16x16x32_bf16 v[156:159], a[112:115], v[108:111], v[156:159]\n\tds_read_b64 a[240:241], v0 offset:1536\n\tv_mfma_f32_16x16x32_bf16 v[156:159], a[116:119], v[104:107], v[156:159]\n\tds_read_b64 a[242:243], v0 offset:1544\n\tv_mfma_f32_16x16x32_bf16 v[156:159], a[120:123], v[100:103], v[156:159]\n\t" \
    "ds_read_b64 a[244:245], v0 offset:1024\n\tv_mfma_f32_16x16x32_bf16 v[152:155], a[96:99], v[120:123], v[152:155]\n\tds_read_b64 a[246:247], v0 offset:1032\n\tv_mfma_f32_16x16x32_bf16 v[152:155], a[100:103], v[116:119], v[152:155]\n\tds_read_b128 v[68:71], v1 offset:192\n\tv_mfma_f32_16x16x32_bf16 v[152:155], a[104:107], v[112:115], v[152:155]\n\tds_read_b128 v[72:75], v1 offset:704\n\tv_mfma_f32_16x16x32_bf16 v[152:155], a[108:111], v[108:111], v[152:155]\n\t" \
    "ds_read_b128 v[76:79], v1 offset:1216\n\tv_mfma_f32_16x16x32_bf16 v[152:155], a[112:115], v[104:107], v[152:155]\n\tds_read_b128 v[80:83], v1 offset:1728\n\tv_mfma_f32_16x16x32_bf16 v[152:155], a[116:119], v[100:103], v[152:155]\n\tbuffer_load_dword v15, v6, s[12:15], s41 offen\n\tds_read_b128 v[84:87], v1 offset:2240\n\tv_mfma_f32_16x16x32_bf16 v[148:151], a[96:99], v[116:119], v[148:151]\n\tbuffer_load_dword v15, v7, s[16:19], s42 offen\n\t" \
    "ds_read_b128 v[88:91], v1 offset:2752\n\tv_mfma_f32_16x16x32_bf16 v[148:151], a[100:103], v[112:115], v[148:151]\n\ts_add_u32 s43, s43, 1\n\tds_read_b128 v[92:95], v1 offset:3264\n\tv_mfma_f32_16x16x32_bf16 v[148:151], a[104:107], v[108:111], v[148:151]\n\tv_readlane_b32 s41, v236, s43\n\tv_readlane_b32 s42, v237, s43\n\ts_nop 4\n\t" \
    "ds_read_b128 v[96:99], v1 offset:3776\n\tv_mfma_f32_16x16x32_bf16 v[148:151], a[108:111], v[104:107], v[148:151]\n\tds_read_b64 a[248:249], v0 offset:512\n\tv_mfma_f32_16x16x32_bf16 v[148:151], a[112:115], v[100:103], v[148:151]\n\tds_read_b64 a[250:251], v0 offset:520\n\tv_mfma_f32_16x16x32_bf16 v[144:147], a[96:99], v[112:115], v[144:147]\n\tds_read_b64 a[252:253], v0 offset:0\n\tv_mfma_f32_16x16x32_bf16 v[144:147], a[100:103], v[108:111], v[144:147]\n\t" \
    "ds_read_b64 a[254:255], v0 offset:8\n\tv_mfma_f32_16x16x32_bf16 v[144:147], a[104:107], v[104:107], v[144:147]\n\tv_mfma_f32_16x16x32_bf16 v[144:147], a[108:111], v[100:103], v[144:147]\n\tv_mfma_f32_16x16x32_bf16 v[140:143], a[96:99], v[108:111], v[140:143]\n\tv_mfma_f32_16x16x32_bf16 v[140:143], a[100:103], v[104:107], v[140:143]\n\tv_mfma_f32_16x16x32_bf16 v[140:143], a[104:107], v[100:103], v[140:143]\n\tv_mfma_f32_16x16x32_bf16 v[136:139], a[96:99], v[104:107], v[136:139]\n\tv_mfma_f32_16x16x32_bf16 v[136:139], a[100:103], v[100:103], v[136:139]\n\t" \
    "v_mfma_f32_16x16x32_bf16 v[132:135], a[96:99], v[100:103], v[132:135]\n\ts_waitcnt lgkmcnt(4)\n\tv_mfma_f32_16x16x32_bf16 v[132:135], a[128:131], v[68:71], v[132:135]\n\tds_read_b128 v[100:103], v1 offset:128\n\ts_waitcnt vmcnt(8)\n\tv_alignbit_b32 v196, v45, v45, 16\n\tv_mfma_f32_16x16x32_bf16 v[136:139], a[128:131], v[72:75], v[136:139]\n\tds_read_b128 v[104:107], v1 offset:640\n\t" \
    "v_alignbit_b32 v197, v44, v44, 16\n\tv_mfma_f32_16x16x32_bf16 v[136:139], a[132:135], v[68:71], v[136:139]\n\tds_read_b128 v[108:111], v1 offset:1152\n\tv_alignbit_b32 v198, v43, v43, 16\n\tv_mfma_f32_16x16x32_bf16 v[140:143], a[128:131], v[76:79], v[140:143]\n\tds_read_b128 v[112:115], v1 offset:1664\n\tv_alignbit_b32 v199, v42, v42, 16\n\tv_mfma_f32_16x16x32_bf16 v[140:143], a[132:135], v[72:75], v[140:143]\n\t" \
    "ds_write_b128 v2, v[196:199] offset:3072\n\tv_mfma_f32_16x16x32_bf16 v[140:143], a[136:139], v[68:71], v[140:143]\n\tds_read_b128 v[116:119], v1 offset:2176\n\tv_bfi_b32 v200, s46, v45, v44\n\tv_mfma_f32_16x16x32_bf16 v[144:147], a[128:131], v[80:83], v[144:147]\n\tds_read_b128 v[120:123], v1 offset:2688\n\tv_bfi_b32 v201, s46, v44, v43\n\tv_mfma_f32_16x16x32_bf16 v[144:147], a[132:135], v[76:79], v[144:147]\n\t" \
    "ds_read_b128 v[124:127], v1 offset:3200\n\tv_bfi_b32 v202, s46, v43, v42\n\tv_mfma_f32_16x16x32_bf16 v[144:147], a[136:139], v[72:75], v[144:147]\n\tds_read_b128 v[128:131], v1 offset:3712\n\tv_bfi_b32 v203, s46, v42, v59\n\tv_mfma_f32_16x16x32_bf16 v[144:147], a[140:143], v[68:71], v[144:147]\n\tds_write_b128 v2, v[200:203] offset:7232\n\tv_mfma_f32_16x16x32_bf16 v[148:151], a[128:131], v[84:87], v[148:151]\n\t" \
    "v_alignbit_b32 v196, v44, v44, 16\n\tv_alignbit_b32 v197, v43, v43, 16\n\tv_mfma_f32_16x16x32_bf16 v[148:151], a[132:135], v[80:83], v[148:151]\n\tv_alignbit_b32 v198, v42, v42, 16\n\tv_alignbit_b32 v199, v59, v59, 16\n\tv_mfma_f32_16x16x32_bf16 v[148:151], a[136:139], v[76:79], v[148:151]\n\tds_write_b128 v2, v[196:199] offset:11392\n\tv_mfma_f32_16x16x32_bf16 v[148:151], a[140:143], v[72:75], v[148:151]\n\t" \
    "v_bfi_b32 v200, s46, v44, v43\n\tv_bfi_b32 v201, s46, v43, v42\n\tv_mfma_f32_16x16x32_bf16 v[148:151], a[144:147], v[68:71], v[148:151]\n\tv_bfi_b32 v202, s46, v42, v59\n\tv_bfi_b32 v203, s46, v59, v58\n\tv_mfma_f32_16x16x32_bf16 v[152:155], a[128:131], v[88:91], v[152:155]\n\tds_write_b128 v2, v[200:203] offset:15552\n\tv_mfma_f32_16x16x32_bf16 v[152:155], a[132:135], v[84:87], v[152:155]\n\t" \
    "v_alignbit_b32 v196, v49, v49, 16\n\tv_alignbit_b32 v197, v48, v48, 16\n\tv_mfma_f32_16x16x32_bf16 v[152:155], a[136:139], v[80:83], v[152:155]\n\tv_alignbit_b32 v198, v47, v47, 16\n\tv_alignbit_b32 v199, v46, v46, 16\n\tv_mfma_f32_16x16x32_bf16 v[152:155], a[140:143], v[76:79], v[152:155]\n\tds_write_b128 v2, v[196:199] offset:2048\n\tv_mfma_f32_16x16x32_bf16 v[152:155], a[144:147], v[72:75], v[152:155]\n\t" \
    "v_bfi_b32 v200, s46, v49, v48\n\tv_bfi_b32 v201, s46, v48, v47\n\tv_mfma_f32_16x16x32_bf16 v[152:155], a[148:151], v[68:71], v[152:155]\n\tv_bfi_b32 v202, s46, v47, v46\n\tv_bfi_b32 v203, s46, v46, v61\n\tv_mfma_f32_16x16x32_bf16 v[156:159], a[128:131], v[92:95], v[156:159]\n\tds_write_b128 v2, v[200:203] offset:6208\n\tv_mfma_f32_16x16x32_bf16 v[156:159], a[132:135], v[88:91], v[156:159]\n\t" \
    "v_alignbit_b32 v196, v48, v48, 16\n\tv_alignbit_b32 v197, v47, v47, 16\n\tv_mfma_f32_16x16x32_bf16 v[156:159], a[136:139], v[84:87], v[156:159]\n\tv_alignbit_b32 v198, v46, v46, 16\n\tv_alignbit_b32 v199, v61, v61, 16\n\tv_mfma_f32_16x16x32_bf16 v[156:159], a[140:143], v[80:83], v[156:159]\n\tds_write_b128 v2, v[196:199] offset:10368\n\tv_mfma_f32_16x16x32_bf16 v[156:159], a[144:147], v[76:79], v[156:159]\n\t" \
    "v_bfi_b32 v200, s46, v48, v47\n\tv_bfi_b32 v201, s46, v47, v46\n\tv_mfma_f32_16x16x32_bf16 v[156:159], a[148:151], v[72:75], v[156:159]\n\tv_bfi_b32 v202, s46, v46, v61\n\tv_bfi_b32 v203, s46, v61, v60\n\tv_mfma_f32_16x16x32_bf16 v[156:159], a[152:155], v[68:71], v[156:159]\n\tds_write_b128 v2, v[200:203] offset:14528\n\tv_mfma_f32_16x16x32_bf16 v[160:163], a[128:131], v[96:99], v[160:163]\n\t" \
    "v_alignbit_b32 v196, v53, v53, 16\n\tv_alignbit_b32 v197, v52, v52, 16\n\tv_mfma_f32_16x16x32_bf16 v[160:163], a[132:135], v[92:95], v[160:163]\n\tv_alignbit_b32 v198, v51, v51, 16\n\tv_alignbit_b32 v199, v50, v50, 16\n\tv_mfma_f32_16x16x32_bf16 v[160:163], a[136:139], v[88:91], v[160:163]\n\tds_write_b128 v2, v[196:199] offset:1024\n\tv_mfma_f32_16x16x32_bf16 v[160:163], a[140:143], v[84:87], v[160:163]\n\t" \
    "v_bfi_b32 v200, s46, v53, v52\n\tv_bfi_b32 v201, s46, v52, v51\n\tv_mfma_f32_16x16x32_bf16 v[160:163], a[144:147], v[80:83], v[160:163]\n\tv_bfi_b32 v202, s46, v51, v50\n\tv_bfi_b32 v203, s46, v50, v63\n\tv_mfma_f32_16x16x32_bf16 v[160:163], a[148:151], v[76:79], v[160:163]\n\tds_write_b128 v2, v[200:203] offset:5184\n\tv_mfma_f32_16x16x32_bf16 v[160:163], a[152:155], v[72:75], v[160:163]\n\t" \
    "v_alignbit_b32 v196, v52, v52, 16\n\tv_alignbit_b32 v197, v51, v51, 16\n\tv_mfma_f32_16x16x32_bf16 v[160:163], a[156:159], v[68:71], v[160:163]\n\tv_alignbit_b32 v198, v50, v50, 16\n\tv_alignbit_b32 v199, v63, v63, 16\n\ts_waitcnt lgkmcnt(9)\n\tv_mfma_f32_16x16x32_bf16 v[160:163], a[160:163], v[128:131], v[160:163]\n\tds_write_b128 v2, v[196:199] offset:9344\n\t" \
    "v_mfma_f32_16x16x32_bf16 v[160:163], a[164:167], v[124:127], v[160:163]\n\tds_read_b128 v[68:71], v1 offset:64\n\tv_bfi_b32 v200, s46, v52, v51\n\tv_mfma_f32_16x16x32_bf16 v[160:163], a[168:171], v[120:123], v[160:163]\n\tds_read_b128 v[72:75], v1 offset:576\n\tv_bfi_b32 v201, s46, v51, v50\n\tv_mfma_f32_16x16x32_bf16 v[160:163], a[172:175], v[116:119], v[160:163]\n\tds_read_b128 v[76:79], v1 offset:1088\n\t" \
    "v_bfi_b32 v202, s46, v50, v63\n\tv_mfma_f32_16x16x32_bf16 v[160:163], a[176:179], v[112:115], v[160:163]\n\tds_read_b128 v[80:83], v1 offset:1600\n\tv_bfi_b32 v203, s46, v63, v62\n\tv_mfma_f32_16x16x32_bf16 v[160:163], a[180:183], v[108:111], v[160:163]\n\tds_write_b128 v2, v[200:203] offset:13504\n\tv_mfma_f32_16x16x32_bf16 v[160:163], a[184:187], v[104:107], v[160:163]\n\tds_read_b128 v[84:87], v1 offset:2112\n\t" \
    "v_alignbit_b32 v196, v57, v57, 16\n\tv_mfma_f32_16x16x32_bf16 v[160:163], a[188:191], v[100:103], v[160:163]\n\tbuffer_load_dword v15, v6, s[12:15], s41 offen\n\tds_read_b128 v[88:91], v1 offset:2624\n\tv_mfma_f32_16x16x32_bf16 v[156:159], a[160:163], v[124:127], v[156:159]\n\tbuffer_load_dword v15, v7, s[16:19], s42 offen\n\tds_read_b128 v[92:95], v1 offset:3136\n\tv_mfma_f32_16x16x32_bf16 v[156:159], a[164:167], v[120:123], v[156:159]\n\t" \
    "s_add_u32 s43, s43, 1\n\tds_read_b128 v[96:99], v1 offset:3648\n\tv_alignbit_b32 v197, v56, v56, 16\n\tv_mfma_f32_16x16x32_bf16 v[156:159], a[168:171], v[116:119], v[156:159]\n\tv_readlane_b32 s41, v236, s43\n\tv_readlane_b32 s42, v237, s43\n\ts_nop 4\n\tv_alignbit_b32 v198, v55, v55, 16\n\t" \
    "v_alignbit_b32 v199, v54, v54, 16\n\tv_mfma_f32_16x16x32_bf16 v[156:159], a[172:175], v[112:115], v[156:159]\n\tds_write_b128 v2, v[196:199] offset:0\n\tv_mfma_f32_16x16x32_bf16 v[156:159], a[176:179], v[108:111], v[156:159]\n\tv_bfi_b32 v200, s46, v57, v56\n\tv_bfi_b32 v201, s46, v56, v55\n\tv_mfma_f32_16x16x32_bf16 v[156:159], a[180:183], v[104:107], v[156:159]\n\tv_bfi_b32 v202, s46, v55, v54\n\t" \
    "v_bfi_b32 v203, s46, v54, v65\n\tv_mfma_f32_16x16x32_bf16 v[156:159], a[184:187], v[100:103], v[156:159]\n\tds_write_b128 v2, v[200:203] offset:4160\n\tv_mfma_f32_16x16x32_bf16 v[152:155], a[160:163], v[120:123], v[152:155]\n\tv_alignbit_b32 v196, v56, v56, 16\n\tv_alignbit_b32 v197, v55, v55, 16\n\tv_mfma_f32_16x16x32_bf16 v[152:155], a[164:167], v[116:119], v[152:155]\n\tv_alignbit_b32 v198, v54, v54, 16\n\t" \
    "v_alignbit_b32 v199, v65, v65, 16\n\tv_mfma_f32_16x16x32_bf16 v[152:155], a[168:171], v[112:115], v[152:155]\n\tds_write_b128 v2, v[196:199] offset:8320\n\tv_mfma_f32_16x16x32_bf16 v[152:155], a[172:175], v[108:111], v[152:155]\n\tv_bfi_b32 v200, s46, v56, v55\n\tv_bfi_b32 v201, s46, v55, v54\n\tv_mfma_f32_16x16x32_bf16 v[152:155], a[176:179], v[104:107], v[152:155]\n\tv_bfi_b32 v202, s46, v54, v65\n\t" \
    "v_bfi_b32 v203, s46, v65, v64\n\tv_mfma_f32_16x16x32_bf16 v[152:155], a[180:183], v[100:103], v[152:155]\n\tds_write_b128 v2, v[200:203] offset:12480\n\tv_mfma_f32_16x16x32_bf16 v[148:151], a[160:163], v[116:119], v[148:151]\n\tv_lshl_or_b32 v66, v66, 16, v66\n\tv_and_b32 v228, v66, v232\n\tv_mfma_f32_16x16x32_bf16 v[148:151], a[164:167], v[112:115], v[148:151]\n\tv_and_b32 v229, v66, v233\n\t" \
    "v_and_b32 v230, v66, v234\n\tv_mfma_f32_16x16x32_bf16 v[148:151], a[168:171], v[108:111], v[148:151]\n\tv_and_b32 v231, v66, v235\n\tv_mfma_f32_16x16x32_bf16 v[148:151], a[172:175], v[104:107], v[148:151]\n\tv_mfma_f32_16x16x32_bf16 v[148:151], a[176:179], v[100:103], v[148:151]\n\tv_mfma_f32_16x16x32_bf16 v[144:147], a[160:163], v[112:115], v[144:147]\n\tv_mfma_f32_16x16x32_bf16 v[144:147], a[164:167], v[108:111], v[144:147]\n\tv_mfma_f32_16x16x32_bf16 v[144:147], a[168:171], v[104:107], v[144:147]\n\t" \
    "v_mfma_f32_16x16x32_bf16 v[144:147], a[172:175], v[100:103], v[144:147]\n\tv_mfma_f32_16x16x32_bf16 v[140:143], a[160:163], v[108:111], v[140:143]\n\tv_mfma_f32_16x16x32_bf16 v[140:143], a[164:167], v[104:107], v[140:143]\n\tv_mfma_f32_16x16x32_bf16 v[140:143], a[168:171], v[100:103], v[140:143]\n\tv_mfma_f32_16x16x32_bf16 v[136:139], a[160:163], v[104:107], v[136:139]\n\tv_mfma_f32_16x16x32_bf16 v[136:139], a[164:167], v[100:103], v[136:139]\n\tv_mfma_f32_16x16x32_bf16 v[132:135], a[160:163], v[100:103], v[132:135]\n\ts_waitcnt lgkmcnt(4)\n\t" \
    "v_mfma_f32_16x16x32_bf16 v[132:135], a[192:195], v[68:71], v[132:135]\n\ts_add_u32 s68, s44, s38\n\tds_read_b128 v[100:103], v1 offset:0\n\tv_mfma_f32_16x16x32_bf16 v[136:139], a[192:195], v[72:75], v[136:139]\n\ts_add_u32 s68, s68, 8\n\tds_read_b128 v[104:107], v1 offset:512\n\tv_mfma_f32_16x16x32_bf16 v[136:139], a[196:199], v[68:71], v[136:139]\n\ts_lshl_b32 s66, s68, 1\n\t" \
    "ds_read_b128 v[108:111], v1 offset:1024\n\tv_mfma_f32_16x16x32_bf16 v[140:143], a[192:195], v[76:79], v[140:143]\n\ts_lshl_b32 s68, s68, 12\n\tbuffer_load_dwordx4 v[16:19], v8, s[76:79], s68 offen offset:0\n\tds_read_b128 v[112:115], v1 offset:1536\n\tv_mfma_f32_16x16x32_bf16 v[140:143], a[196:199], v[72:75], v[140:143]\n\tds_read_b128 v[116:119], v1 offset:2048\n\tv_mfma_f32_16x16x32_bf16 v[140:143], a[200:203], v[68:71], v[140:143]\n\t" \
    "ds_read_b128 v[120:123], v1 offset:2560\n\tv_mfma_f32_16x16x32_bf16 v[144:147], a[192:195], v[80:83], v[144:147]\n\tds_read_b128 v[124:127], v1 offset:3072\n\tv_mfma_f32_16x16x32_bf16 v[144:147], a[196:199], v[76:79], v[144:147]\n\tds_read_b128 v[128:131], v1 offset:3584\n\tv_mfma_f32_16x16x32_bf16 v[144:147], a[200:203], v[72:75], v[144:147]\n\tv_mfma_f32_16x16x32_bf16 v[144:147], a[204:207], v[68:71], v[144:147]\n\tbuffer_load_dwordx4 v[20:23], v8, s[76:79], s68 offen offset:1024\n\t" \
    "v_mfma_f32_16x16x32_bf16 v[148:151], a[192:195], v[84:87], v[148:151]\n\tv_mfma_f32_16x16x32_bf16 v[148:151], a[196:199], v[80:83], v[148:151]\n\tv_mfma_f32_16x16x32_bf16 v[148:151], a[200:203], v[76:79], v[148:151]\n\tv_mfma_f32_16x16x32_bf16 v[148:151], a[204:207], v[72:75], v[148:151]\n\tv_mfma_f32_16x16x32_bf16 v[148:151], a[208:211], v[68:71], v[148:151]\n\tv_mfma_f32_16x16x32_bf16 v[152:155], a[192:195], v[88:91], v[152:155]\n\tbuffer_load_dwordx4 v[24:27], v8, s[76:79], s68 offen offset:2048\n\tv_mfma_f32_16x16x32_bf16 v[152:155], a[196:199], v[84:87], v[152:155]\n\t" \
    "v_mfma_f32_16x16x32_bf16 v[152:155], a[200:203], v[80:83], v[152:155]\n\tv_mfma_f32_16x16x32_bf16 v[152:155], a[204:207], v[76:79], v[152:155]\n\tv_mfma_f32_16x16x32_bf16 v[152:155], a[208:211], v[72:75], v[152:155]\n\tv_mfma_f32_16x16x32_bf16 v[152:155], a[212:215], v[68:71], v[152:155]\n\tv_mfma_f32_16x16x32_bf16 v[156:159], a[192:195], v[92:95], v[156:159]\n\tbuffer_load_dwordx4 v[28:31], v8, s[76:79], s68 offen offset:3072\n\tv_mfma_f32_16x16x32_bf16 v[156:159], a[196:199], v[88:91], v[156:159]\n\tv_mfma_f32_16x16x32_bf16 v[156:159], a[200:203], v[84:87], v[156:159]\n\t" \
    "v_mfma_f32_16x16x32_bf16 v[156:159], a[204:207], v[80:83], v[156:159]\n\tv_mfma_f32_16x16x32_bf16 v[156:159], a[208:211], v[76:79], v[156:159]\n\tv_mfma_f32_16x16x32_bf16 v[156:159], a[212:215], v[72:75], v[156:159]\n\tv_mfma_f32_16x16x32_bf16 v[156:159], a[216:219], v[68:71], v[156:159]\n\tbuffer_load_dwordx2 v[32:33], v13, s[76:79], s68 offen\n\tv_mfma_f32_16x16x32_bf16 v[160:163], a[192:195], v[96:99], v[160:163]\n\tv_mfma_f32_16x16x32_bf16 v[160:163], a[196:199], v[92:95], v[160:163]\n\tv_mfma_f32_16x16x32_bf16 v[160:163], a[200:203], v[88:91], v[160:163]\n\t" \
    "v_mfma_f32_16x16x32_bf16 v[160:163], a[204:207], v[84:87], v[160:163]\n\tv_mfma_f32_16x16x32_bf16 v[160:163], a[208:211], v[80:83], v[160:163]\n\tv_mfma_f32_16x16x32_bf16 v[160:163], a[212:215], v[76:79], v[160:163]\n\tbuffer_load_dwordx2 v[34:35], v8, s[76:79], s68 offen offset:1016\n\tv_mfma_f32_16x16x32_bf16 v[160:163], a[216:219], v[72:75], v[160:163]\n\tv_mfma_f32_16x16x32_bf16 v[160:163], a[220:223], v[68:71], v[160:163]\n\ts_waitcnt lgkmcnt(0)\n\tv_mfma_f32_16x16x32_bf16 v[160:163], a[224:227], v[128:131], v[160:163]\n\t" \
    "ds_read_b64 a[0:1], v0 offset:4032\n\tv_mfma_f32_16x16x32_bf16 v[160:163], a[228:231], v[124:127], v[160:163]\n\tds_read_b64 a[2:3], v0 offset:4040\n\tv_mfma_f32_16x16x32_bf16 v[160:163], a[232:235], v[120:123], v[160:163]\n\tds_read_b64 a[4:5], v0 offset:3520\n\tv_mfma_f32_16x16x32_bf16 v[160:163], a[236:239], v[116:119], v[160:163]\n\tbuffer_load_dwordx2 v[36:37], v8, s[76:79], s68 offen offset:2040\n\tds_read_b64 a[6:7], v0 offset:3528\n\t" \
    "v_mfma_f32_16x16x32_bf16 v[160:163], a[240:243], v[112:115], v[160:163]\n\tds_read_b64 a[8:9], v0 offset:3008\n\tv_mfma_f32_16x16x32_bf16 v[160:163], a[244:247], v[108:111], v[160:163]\n\tds_read_b64 a[10:11], v0 offset:3016\n\tv_mfma_f32_16x16x32_bf16 v[160:163], a[248:251], v[104:107], v[160:163]\n\tds_read_b64 a[12:13], v0 offset:2496\n\tv_mfma_f32_16x16x32_bf16 v[160:163], a[252:255], v[100:103], v[160:163]\n\tds_read_b64 a[14:15], v0 offset:2504\n\t" \
    "v_mfma_f32_16x16x32_bf16 v[156:159], a[224:227], v[124:127], v[156:159]\n\tds_read_b64 a[16:17], v0 offset:1984\n\tv_mfma_f32_16x16x32_bf16 v[156:159], a[228:231], v[120:123], v[156:159]\n\tbuffer_load_dwordx2 v[38:39], v8, s[76:79], s68 offen offset:3064\n\tds_read_b64 a[18:19], v0 offset:1992\n\tv_mfma_f32_16x16x32_bf16 v[156:159], a[232:235], v[116:119], v[156:159]\n\tds_read_b64 a[20:21], v0 offset:1472\n\tv_mfma_f32_16x16x32_bf16 v[156:159], a[236:239], v[112:115], v[156:159]\n\t" \
    "ds_read_b64 a[22:23], v0 offset:1480\n\tv_mfma_f32_16x16x32_bf16 v[156:159], a[240:243], v[108:111], v[156:159]\n\tds_read_b64 a[24:25], v0 offset:960\n\tv_mfma_f32_16x16x32_bf16 v[156:159], a[244:247], v[104:107], v[156:159]\n\tds_read_b64 a[26:27], v0 offset:968\n\tv_mfma_f32_16x16x32_bf16 v[156:159], a[248:251], v[100:103], v[156:159]\n\tds_read_b64 a[28:29], v0 offset:448\n\tv_mfma_f32_16x16x32_bf16 v[152:155], a[224:227], v[120:123], v[152:155]\n\t" \
    "buffer_load_ushort v40, off, s[80:83], s66\n\tds_read_b64 a[30:31], v0 offset:456\n\tv_mfma_f32_16x16x32_bf16 v[152:155], a[228:231], v[116:119], v[152:155]\n\tds_read_b128 v[68:71], v1 offset:18944\n\tv_mfma_f32_16x16x32_bf16 v[152:155], a[232:235], v[112:115], v[152:155]\n\tds_read_b128 v[72:75], v1 offset:19456\n\tv_mfma_f32_16x16x32_bf16 v[152:155], a[236:239], v[108:111], v[152:155]\n\tds_read_b128 v[76:79], v1 offset:19968\n\t" \
    "v_mfma_f32_16x16x32_bf16 v[152:155], a[240:243], v[104:107], v[152:155]\n\tds_read_b128 v[80:83], v1 offset:20480\n\tv_mfma_f32_16x16x32_bf16 v[152:155], a[244:247], v[100:103], v[152:155]\n\tds_read_b128 v[84:87], v1 offset:20992\n\tv_mfma_f32_16x16x32_bf16 v[148:151], a[224:227], v[116:119], v[148:151]\n\tbuffer_load_dword v15, v6, s[12:15], s41 offen\n\tds_read_b128 v[88:91], v1 offset:21504\n\tv_mfma_f32_16x16x32_bf16 v[148:151], a[228:231], v[112:115], v[148:151]\n\t" \
    "buffer_load_dword v15, v7, s[16:19], s42 offen\n\tds_read_b128 v[92:95], v1 offset:22016\n\tv_mfma_f32_16x16x32_bf16 v[148:151], a[232:235], v[108:111], v[148:151]\n\ts_add_u32 s43, s43, 1\n\tds_read_b128 v[96:99], v1 offset:22528\n\tv_mfma_f32_16x16x32_bf16 v[148:151], a[236:239], v[104:107], v[148:151]\n\tv_readlane_b32 s41, v236, s43\n\tv_readlane_b32 s42, v237, s43\n\t" \
    "s_nop 4\n\tv_mfma_f32_16x16x32_bf16 v[148:151], a[240:243], v[100:103], v[148:151]\n\tv_mfma_f32_16x16x32_bf16 v[144:147], a[224:227], v[112:115], v[144:147]\n\tv_mfma_f32_16x16x32_bf16 v[144:147], a[228:231], v[108:111], v[144:147]\n\tv_mfma_f32_16x16x32_bf16 v[144:147], a[232:235], v[104:107], v[144:147]\n\tv_mfma_f32_16x16x32_bf16 v[144:147], a[236:239], v[100:103], v[144:147]\n\tv_mfma_f32_16x16x32_bf16 v[140:143], a[224:227], v[108:111], v[140:143]\n\tv_mfma_f32_16x16x32_bf16 v[140:143], a[228:231], v[104:107], v[140:143]\n\t" \
    "v_mfma_f32_16x16x32_bf16 v[140:143], a[232:235], v[100:103], v[140:143]\n\tv_mfma_f32_16x16x32_bf16 v[136:139], a[224:227], v[104:107], v[136:139]\n\tv_mfma_f32_16x16x32_bf16 v[136:139], a[228:231], v[100:103], v[136:139]\n\tv_mfma_f32_16x16x32_bf16 v[132:135], a[224:227], v[100:103], v[132:135]\n\ts_waitcnt lgkmcnt(0)\n\tv_mfma_f32_16x16x32_bf16 v[164:167], v[228:231], v[68:71], 0\n\tds_read_b64 a[32:33], v0 offset:3968\n\tds_read_b64 a[34:35], v0 offset:3976\n\t" \
    "v_mfma_f32_16x16x32_bf16 v[164:167], a[0:3], v[68:71], v[164:167]\n\tds_read_b64 a[36:37], v0 offset:3456\n\tds_read_b64 a[38:39], v0 offset:3464\n\tv_mfma_f32_16x16x32_bf16 v[168:171], v[228:231], v[72:75], 0\n\tds_read_b64 a[40:41], v0 offset:2944\n\tds_read_b64 a[42:43], v0 offset:2952\n\tv_mfma_f32_16x16x32_bf16 v[168:171], a[0:3], v[72:75], v[168:171]\n\tds_read_b64 a[44:45], v0 offset:2432\n\t" \
    "ds_read_b64 a[46:47], v0 offset:2440\n\tv_mfma_f32_16x16x32_bf16 v[168:171], a[4:7], v[68:71], v[168:171]\n\tds_read_b64 a[48:49], v0 offset:1920\n\tds_read_b64 a[50:51], v0 offset:1928\n\tv_mfma_f32_16x16x32_bf16 v[172:175], v[228:231], v[76:79], 0\n\tds_read_b64 a[52:53], v0 offset:1408\n\tv_cvt_pk_bf16_f32 v132, v132, v133\n\tv_mfma_f32_16x16x32_bf16 v[172:175], a[0:3], v[76:79], v[172:175]\n\t" \
    "ds_read_b64 a[54:55], v0 offset:1416\n\tv_cvt_pk_bf16_f32 v133, v134, v135\n\tv_mfma_f32_16x16x32_bf16 v[172:175], a[4:7], v[72:75], v[172:175]\n\tds_read_b64 a[56:57], v0 offset:896\n\tds_write_b64 v3, v[132:133] offset:0\n\tv_mfma_f32_16x16x32_bf16 v[172:175], a[8:11], v[68:71], v[172:175]\n\tds_read_b64 a[58:59], v0 offset:904\n\tv_cvt_pk_bf16_f32 v136, v136, v137\n\t" \
    "v_mfma_f32_16x16x32_bf16 v[176:179], v[228:231], v[80:83], 0\n\tds_read_b64 a[60:61], v0 offset:384\n\tv_cvt_pk_bf16_f32 v137, v138, v139\n\tv_mfma_f32_16x16x32_bf16 v[176:179], a[0:3], v[80:83], v[176:179]\n\tds_read_b64 a[62:63], v0 offset:392\n\tds_write_b64 v3, v[136:137] offset:512\n\tv_mfma_f32_16x16x32_bf16 v[176:179], a[4:7], v[76:79], v[176:179]\n\tds_read_b128 v[100:103], v1 offset:18880\n\t" \
    "v_cvt_pk_bf16_f32 v140, v140, v141\n\tv_mfma_f32_16x16x32_bf16 v[176:179], a[8:11], v[72:75], v[176:179]\n\tds_read_b128 v[104:107], v1 offset:19392\n\tv_cvt_pk_bf16_f32 v141, v142, v143\n\tv_mfma_f32_16x16x32_bf16 v[176:179], a[12:15], v[68:71], v[176:179]\n\tds_read_b128 v[108:111], v1 offset:19904\n\tds_write_b64 v3, v[140:141] offset:1024\n\tv_mfma_f32_16x16x32_bf16 v[180:183], v[228:231], v[84:87], 0\n\t" \
    "ds_read_b128 v[112:115], v1 offset:20416\n\tv_cvt_pk_bf16_f32 v144, v144, v145\n\tv_mfma_f32_16x16x32_bf16 v[180:183], a[0:3], v[84:87], v[180:183]\n\tds_read_b128 v[116:119], v1 offset:20928\n\tv_cvt_pk_bf16_f32 v145, v146, v147\n\tv_mfma_f32_16x16x32_bf16 v[180:183], a[4:7], v[80:83], v[180:183]\n\tds_read_b128 v[120:123], v1 offset:21440\n\tds_write_b64 v3, v[144:145] offset:1536\n\t" \
    "v_mfma_f32_16x16x32_bf16 v[180:183], a[8:11], v[76:79], v[180:183]\n\tds_read_b128 v[124:127], v1 offset:21952\n\tv_cvt_pk_bf16_f32 v148, v148, v149\n\tv_mfma_f32_16x16x32_bf16 v[180:183], a[12:15], v[72:75], v[180:183]\n\tds_read_b128 v[128:131], v1 offset:22464\n\tv_cvt_pk_bf16_f32 v149, v150, v151\n\tv_mfma_f32_16x16x32_bf16 v[180:183], a[16:19], v[68:71], v[180:183]\n\tds_read_b64 a[64:65], v0 offset:3904\n\t" \
    "ds_write_b64 v3, v[148:149] offset:2048\n\tv_mfma_f32_16x16x32_bf16 v[184:187], v[228:231], v[88:91], 0\n\tds_read_b64 a[66:67], v0 offset:3912\n\tv_cvt_pk_bf16_f32 v152, v152, v153\n\tv_mfma_f32_16x16x32_bf16 v[184:187], a[0:3], v[88:91], v[184:187]\n\tds_read_b64 a[68:69], v0 offset:3392\n\tv_cvt_pk_bf16_f32 v153, v154, v155\n\tv_mfma_f32_16x16x32_bf16 v[184:187], a[4:7], v[84:87], v[184:187]\n\t" \
    "ds_read_b64 a[70:71], v0 offset:3400\n\tds_write_b64 v3, v[152:153] offset:2560\n\tv_mfma_f32_16x16x32_bf16 v[184:187], a[8:11], v[80:83], v[184:187]\n\tds_read_b64 a[72:73], v0 offset:2880\n\tv_cvt_pk_bf16_f32 v156, v156, v157\n\tv_mfma_f32_16x16x32_bf16 v[184:187], a[12:15], v[76:79], v[184:187]\n\tds_read_b64 a[74:75], v0 offset:2888\n\tv_cvt_pk_bf16_f32 v157, v158, v159\n\t" \
    "v_mfma_f32_16x16x32_bf16 v[184:187], a[16:19], v[72:75], v[184:187]\n\tds_read_b64 a[76:77], v0 offset:2368\n\tds_write_b64 v3, v[156:157] offset:3072\n\tv_mfma_f32_16x16x32_bf16 v[184:187], a[20:23], v[68:71], v[184:187]\n\tds_read_b64 a[78:79], v0 offset:2376\n\tv_cvt_pk_bf16_f32 v160, v160, v161\n\tv_mfma_f32_16x16x32_bf16 v[188:191], v[228:231], v[92:95], 0\n\tds_read_b64 a[80:81], v0 offset:1856\n\t" \
    "v_cvt_pk_bf16_f32 v161, v162, v163\n\tv_mfma_f32_16x16x32_bf16 v[188:191], a[0:3], v[92:95], v[188:191]\n\tds_read_b64 a[82:83], v0 offset:1864\n\tds_write_b64 v3, v[160:161] offset:3584\n\tv_mfma_f32_16x16x32_bf16 v[188:191], a[4:7], v[88:91], v[188:191]\n\tbuffer_load_dword v15, v7, s[16:19], s42 offen\n\tds_read_b64 a[84:85], v0 offset:1344\n\tv_mfma_f32_16x16x32_bf16 v[188:191], a[8:11], v[84:87], v[188:191]\n\t" \
    "s_add_u32 s43, s43, 1\n\tds_read_b64 a[86:87], v0 offset:1352\n\tv_mfma_f32_16x16x32_bf16 v[188:191], a[12:15], v[80:83], v[188:191]\n\tv_readlane_b32 s42, v237, s43\n\ts_nop 4\n\tds_read_b64 a[88:89], v0 offset:832\n\tv_mfma_f32_16x16x32_bf16 v[188:191], a[16:19], v[76:79], v[188:191]\n\tds_read_b64 a[90:91], v0 offset:840\n\t" \
    "v_mfma_f32_16x16x32_bf16 v[188:191], a[20:23], v[72:75], v[188:191]\n\tds_read_b64 a[92:93], v0 offset:320\n\tv_mfma_f32_16x16x32_bf16 v[188:191], a[24:27], v[68:71], v[188:191]\n\tds_read_b64 a[94:95], v0 offset:328\n\tv_mfma_f32_16x16x32_bf16 v[192:195], v[228:231], v[96:99], 0\n\tds_read_b64 a[96:97], v0 offset:3840\n\tv_mfma_f32_16x16x32_bf16 v[192:195], a[0:3], v[96:99], v[192:195]\n\tds_read_b64 a[98:99], v0 offset:3848\n\t" \
    "v_mfma_f32_16x16x32_bf16 v[192:195], a[4:7], v[92:95], v[192:195]\n\tds_read_b64 a[100:101], v0 offset:3328\n\tv_mfma_f32_16x16x32_bf16 v[192:195], a[8:11], v[88:91], v[192:195]\n\tds_read_b64 a[102:103], v0 offset:3336\n\tv_mfma_f32_16x16x32_bf16 v[192:195], a[12:15], v[84:87], v[192:195]\n\tv_mfma_f32_16x16x32_bf16 v[192:195], a[16:19], v[80:83], v[192:195]\n\tv_mfma_f32_16x16x32_bf16 v[192:195], a[20:23], v[76:79], v[192:195]\n\tv_mfma_f32_16x16x32_bf16 v[192:195], a[24:27], v[72:75], v[192:195]\n\t" \
    "v_mfma_f32_16x16x32_bf16 v[192:195], a[28:31], v[68:71], v[192:195]\n\ts_waitcnt lgkmcnt(10)\n\ts_barrier\n\tv_mfma_f32_16x16x32_bf16 v[192:195], a[32:35], v[128:131], v[192:195]\n\tds_read_b64 a[104:105], v0 offset:2816\n\tds_read_b64 a[106:107], v0 offset:2824\n\tv_mfma_f32_16x16x32_bf16 v[192:195], a[36:39], v[124:127], v[192:195]\n\tds_read_b64 a[108:109], v0 offset:2304\n\t" \
    "ds_read_b64 a[110:111], v0 offset:2312\n\tv_mfma_f32_16x16x32_bf16 v[192:195], a[40:43], v[120:123], v[192:195]\n\tds_read_b64 a[112:113], v0 offset:1792\n\tds_read_b64 a[114:115], v0 offset:1800\n\tv_mfma_f32_16x16x32_bf16 v[192:195], a[44:47], v[116:119], v[192:195]\n\tds_read_b64 a[116:117], v0 offset:1280\n\tds_read_b64 a[118:119], v0 offset:1288\n\tv_mfma_f32_16x16x32_bf16 v[192:195], a[48:51], v[112:115], v[192:195]\n\t" \
    "ds_read_b64 a[120:121], v0 offset:768\n\tds_read_b64 a[122:123], v0 offset:776\n\tv_mfma_f32_16x16x32_bf16 v[192:195], a[52:55], v[108:111], v[192:195]\n\tds_read_b64 a[124:125], v0 offset:256\n\ts_add_u32 s51, s44, 0\n\tv_mfma_f32_16x16x32_bf16 v[192:195], a[56:59], v[104:107], v[192:195]\n\tds_read_b64 a[126:127], v0 offset:264\n\ts_mul_i32 s65, s51, 1366\n\t" \
    "v_mfma_f32_16x16x32_bf16 v[192:195], a[60:63], v[100:103], v[192:195]\n\tds_read_b64 a[128:129], v0 offset:3776\n\ts_lshr_b32 s65, s65, 16\n\tv_mfma_f32_16x16x32_bf16 v[188:191], a[32:35], v[124:127], v[188:191]\n\tds_read_b64 a[130:131], v0 offset:3784\n\ts_mul_i32 s56, s65, 48\n\tv_mfma_f32_16x16x32_bf16 v[188:191], a[36:39], v[120:123], v[188:191]\n\tds_read_b64 a[132:133], v0 offset:3264\n\t" \
    "s_sub_u32 s56, s51, s56\n\tv_mfma_f32_16x16x32_bf16 v[188:191], a[40:43], v[116:119], v[188:191]\n\tds_read_b64 a[134:135], v0 offset:3272\n\ts_lshl_b32 s56, s56, 6\n\tv_mfma_f32_16x16x32_bf16 v[188:191], a[44:47], v[112:115], v[188:191]\n\tds_read_b128 v[68:71], v1 offset:18816\n\ts_lshr_b32 s70, s65, 2\n\tv_mfma_f32_16x16x32_bf16 v[188:191], a[48:51], v[108:111], v[188:191]\n\t" \
    "ds_read_b128 v[72:75], v1 offset:19328\n\ts_mul_i32 s70, s70, 252\n\tv_mfma_f32_16x16x32_bf16 v[188:191], a[52:55], v[104:107], v[188:191]\n\tds_read_b128 v[76:79], v1 offset:19840\n\ts_add_u32 s65, s65, s70\n\tv_mfma_f32_16x16x32_bf16 v[188:191], a[56:59], v[100:103], v[188:191]\n\tds_read_b128 v[80:83], v1 offset:20352\n\ts_mul_i32 s65, s65, 3072\n\t" \
    "v_mfma_f32_16x16x32_bf16 v[184:187], a[32:35], v[120:123], v[184:187]\n\tds_read_b128 v[84:87], v1 offset:20864\n\ts_add_u32 s56, s56, s65\n\tv_mfma_f32_16x16x32_bf16 v[184:187], a[36:39], v[116:119], v[184:187]\n\tds_read_b128 v[88:91], v1 offset:21376\n\ts_add_u32 s56, s56, 4849664\n\tv_mfma_f32_16x16x32_bf16 v[184:187], a[40:43], v[112:115], v[184:187]\n\tds_read_b128 v[92:95], v1 offset:21888\n\t" \
    "s_add_u32 s51, s51, s38\n\tv_mfma_f32_16x16x32_bf16 v[184:187], a[44:47], v[108:111], v[184:187]\n\tds_read_b128 v[96:99], v1 offset:22400\n\ts_lshl_b32 s51, s51, 3\n\tv_mfma_f32_16x16x32_bf16 v[184:187], a[48:51], v[104:107], v[184:187]\n\tds_read_b64 a[136:137], v0 offset:2752\n\ts_add_u32 s51, s51, 50176\n\tds_read_b64_tr_b16 v[204:205], v4 offset:0\n\t" \
    "v_mfma_f32_16x16x32_bf16 v[184:187], a[52:55], v[100:103], v[184:187]\n\tds_read_b64 a[138:139], v0 offset:2760\n\tds_read_b64_tr_b16 v[206:207], v4 offset:8\n\tv_mfma_f32_16x16x32_bf16 v[180:183], a[32:35], v[116:119], v[180:183]\n\tds_read_b64 a[140:141], v0 offset:2240\n\tds_read_b64_tr_b16 v[208:209], v4 offset:64\n\tv_mfma_f32_16x16x32_bf16 v[180:183], a[36:39], v[112:115], v[180:183]\n\tds_read_b64 a[142:143], v0 offset:2248\n\t" \
    "ds_read_b64_tr_b16 v[210:211], v4 offset:72\n\tv_mfma_f32_16x16x32_bf16 v[180:183], a[40:43], v[108:111], v[180:183]\n\tds_read_b64 a[144:145], v0 offset:1728\n\tds_read_b64_tr_b16 v[212:213], v4 offset:512\n\tv_mfma_f32_16x16x32_bf16 v[180:183], a[44:47], v[104:107], v[180:183]\n\tds_read_b64 a[146:147], v0 offset:1736\n\tds_read_b64_tr_b16 v[214:215], v4 offset:520\n\tv_mfma_f32_16x16x32_bf16 v[180:183], a[48:51], v[100:103], v[180:183]\n\t" \
    "ds_read_b64 a[148:149], v0 offset:1216\n\tds_read_b64_tr_b16 v[216:217], v4 offset:576\n\tv_mfma_f32_16x16x32_bf16 v[176:179], a[32:35], v[112:115], v[176:179]\n\tds_read_b64 a[150:151], v0 offset:1224\n\tds_read_b64_tr_b16 v[218:219], v4 offset:584\n\tv_mfma_f32_16x16x32_bf16 v[176:179], a[36:39], v[108:111], v[176:179]\n\tds_read_b64 a[152:153], v0 offset:704\n\tv_mfma_f32_16x16x32_bf16 v[176:179], a[40:43], v[104:107], v[176:179]\n\t" \
    "ds_read_b64 a[154:155], v0 offset:712\n\tv_mfma_f32_16x16x32_bf16 v[176:179], a[44:47], v[100:103], v[176:179]\n\tds_read_b64 a[156:157], v0 offset:192\n\tv_mfma_f32_16x16x32_bf16 v[172:175], a[32:35], v[108:111], v[172:175]\n\tds_read_b64 a[158:159], v0 offset:200\n\tv_mfma_f32_16x16x32_bf16 v[172:175], a[36:39], v[104:107], v[172:175]\n\tv_mfma_f32_16x16x32_bf16 v[172:175], a[40:43], v[100:103], v[172:175]\n\ts_waitcnt lgkmcnt(15)\n\t" \
    "buffer_store_dwordx4 v[204:207], v5, s[8:11], s56 offen sc1\n\ts_add_u32 s56, s56, 49152\n\tv_mfma_f32_16x16x32_bf16 v[168:171], a[32:35], v[104:107], v[168:171]\n\tv_mfma_f32_16x16x32_bf16 v[168:171], a[36:39], v[100:103], v[168:171]\n\tv_mfma_f32_16x16x32_bf16 v[164:167], a[32:35], v[100:103], v[164:167]\n\tv_mfma_f32_16x16x32_bf16 v[164:167], a[64:67], v[68:71], v[164:167]\n\tds_read_b64 a[160:161], v0 offset:3712\n\tds_read_b64 a[162:163], v0 offset:3720\n\t" \
    "v_mfma_f32_16x16x32_bf16 v[168:171], a[64:67], v[72:75], v[168:171]\n\tds_read_b64 a[164:165], v0 offset:3200\n\tds_read_b64 a[166:167], v0 offset:3208\n\tv_mfma_f32_16x16x32_bf16 v[168:171], a[68:71], v[68:71], v[168:171]\n\tds_read_b64 a[168:169], v0 offset:2688\n\tds_read_b64 a[170:171], v0 offset:2696\n\tv_mfma_f32_16x16x32_bf16 v[172:175], a[64:67], v[76:79], v[172:175]\n\tds_read_b64 a[172:173], v0 offset:2176\n\t" \
    "ds_read_b64 a[174:175], v0 offset:2184\n\tv_mfma_f32_16x16x32_bf16 v[172:175], a[68:71], v[72:75], v[172:175]\n\tds_read_b64 a[176:177], v0 offset:1664\n\tds_read_b64 a[178:179], v0 offset:1672\n\tv_mfma_f32_16x16x32_bf16 v[172:175], a[72:75], v[68:71], v[172:175]\n\tds_read_b64 a[180:181], v0 offset:1152\n\tv_mfma_f32_16x16x32_bf16 v[176:179], a[64:67], v[80:83], v[176:179]\n\tds_read_b64 a[182:183], v0 offset:1160\n\t" \
    "v_mfma_f32_16x16x32_bf16 v[176:179], a[68:71], v[76:79], v[176:179]\n\tds_read_b64 a[184:185], v0 offset:640\n\tv_mfma_f32_16x16x32_bf16 v[176:179], a[72:75], v[72:75], v[176:179]\n\tds_read_b64 a[186:187], v0 offset:648\n\tv_mfma_f32_16x16x32_bf16 v[176:179], a[76:79], v[68:71], v[176:179]\n\tds_read_b64 a[188:189], v0 offset:128\n\tv_mfma_f32_16x16x32_bf16 v[180:183], a[64:67], v[84:87], v[180:183]\n\tds_read_b64 a[190:191], v0 offset:136\n\t" \
    "v_mfma_f32_16x16x32_bf16 v[180:183], a[68:71], v[80:83], v[180:183]\n\tds_read_b128 v[100:103], v1 offset:18752\n\tv_mfma_f32_16x16x32_bf16 v[180:183], a[72:75], v[76:79], v[180:183]\n\tds_read_b128 v[104:107], v1 offset:19264\n\tv_mfma_f32_16x16x32_bf16 v[180:183], a[76:79], v[72:75], v[180:183]\n\tds_read_b128 v[108:111], v1 offset:19776\n\tv_mfma_f32_16x16x32_bf16 v[180:183], a[80:83], v[68:71], v[180:183]\n\tds_read_b128 v[112:115], v1 offset:20288\n\t" \
    "s_waitcnt lgkmcnt(15)\n\tbuffer_store_dwordx4 v[208:211], v5, s[8:11], s56 offen sc1\n\tv_mfma_f32_16x16x32_bf16 v[184:187], a[64:67], v[88:91], v[184:187]\n\tds_read_b128 v[116:119], v1 offset:20800\n\ts_add_u32 s56, s56, 49152\n\tv_mfma_f32_16x16x32_bf16 v[184:187], a[68:71], v[84:87], v[184:187]\n\tds_read_b128 v[120:123], v1 offset:21312\n\tv_mfma_f32_16x16x32_bf16 v[184:187], a[72:75], v[80:83], v[184:187]\n\t" \
    "ds_read_b128 v[124:127], v1 offset:21824\n\tv_mfma_f32_16x16x32_bf16 v[184:187], a[76:79], v[76:79], v[184:187]\n\tds_read_b128 v[128:131], v1 offset:22336\n\tv_mfma_f32_16x16x32_bf16 v[184:187], a[80:83], v[72:75], v[184:187]\n\tds_read_b64 a[192:193], v0 offset:3648\n\tv_mfma_f32_16x16x32_bf16 v[184:187], a[84:87], v[68:71], v[184:187]\n\tds_read_b64 a[194:195], v0 offset:3656\n\tv_mfma_f32_16x16x32_bf16 v[188:191], a[64:67], v[92:95], v[188:191]\n\t" \
    "ds_read_b64 a[196:197], v0 offset:3136\n\tv_mfma_f32_16x16x32_bf16 v[188:191], a[68:71], v[88:91], v[188:191]\n\tds_read_b64 a[198:199], v0 offset:3144\n\tv_mfma_f32_16x16x32_bf16 v[188:191], a[72:75], v[84:87], v[188:191]\n\tds_read_b64 a[200:201], v0 offset:2624\n\tv_mfma_f32_16x16x32_bf16 v[188:191], a[76:79], v[80:83], v[188:191]\n\tds_read_b64 a[202:203], v0 offset:2632\n\tv_mfma_f32_16x16x32_bf16 v[188:191], a[80:83], v[76:79], v[188:191]\n\t" \
    "ds_read_b64 a[204:205], v0 offset:2112\n\tv_mfma_f32_16x16x32_bf16 v[188:191], a[84:87], v[72:75], v[188:191]\n\tds_read_b64 a[206:207], v0 offset:2120\n\tv_mfma_f32_16x16x32_bf16 v[188:191], a[88:91], v[68:71], v[188:191]\n\tds_read_b64 a[208:209], v0 offset:1600\n\tv_mfma_f32_16x16x32_bf16 v[192:195], a[64:67], v[96:99], v[192:195]\n\tds_read_b64 a[210:211], v0 offset:1608\n\tv_mfma_f32_16x16x32_bf16 v[192:195], a[68:71], v[92:95], v[192:195]\n\t" \
    "ds_read_b64 a[212:213], v0 offset:1088\n\tv_mfma_f32_16x16x32_bf16 v[192:195], a[72:75], v[88:91], v[192:195]\n\tds_read_b64 a[214:215], v0 offset:1096\n\tv_mfma_f32_16x16x32_bf16 v[192:195], a[76:79], v[84:87], v[192:195]\n\tbuffer_store_dwordx4 v[212:215], v5, s[8:11], s56 offen sc1\n\ts_add_u32 s56, s56, 49152\n\tv_mfma_f32_16x16x32_bf16 v[192:195], a[80:83], v[80:83], v[192:195]\n\tv_mfma_f32_16x16x32_bf16 v[192:195], a[84:87], v[76:79], v[192:195]\n\t" \
    "v_mfma_f32_16x16x32_bf16 v[192:195], a[88:91], v[72:75], v[192:195]\n\tv_mfma_f32_16x16x32_bf16 v[192:195], a[92:95], v[68:71], v[192:195]\n\ts_waitcnt lgkmcnt(12)\n\tv_mfma_f32_16x16x32_bf16 v[192:195], a[96:99], v[128:131], v[192:195]\n\tds_read_b64 a[216:217], v0 offset:576\n\tv_mfma_f32_16x16x32_bf16 v[192:195], a[100:103], v[124:127], v[192:195]\n\tds_read_b64 a[218:219], v0 offset:584\n\tv_mfma_f32_16x16x32_bf16 v[192:195], a[104:107], v[120:123], v[192:195]\n\t" \
    "ds_read_b64 a[220:221], v0 offset:64\n\tv_mfma_f32_16x16x32_bf16 v[192:195], a[108:111], v[116:119], v[192:195]\n\tds_read_b64 a[222:223], v0 offset:72\n\tv_mfma_f32_16x16x32_bf16 v[192:195], a[112:115], v[112:115], v[192:195]\n\tds_read_b64 a[224:225], v0 offset:3584\n\tv_mfma_f32_16x16x32_bf16 v[192:195], a[116:119], v[108:111], v[192:195]\n\tds_read_b64 a[226:227], v0 offset:3592\n\tv_mfma_f32_16x16x32_bf16 v[192:195], a[120:123], v[104:107], v[192:195]\n\t" \
    "ds_read_b64 a[228:229], v0 offset:3072\n\tv_mfma_f32_16x16x32_bf16 v[192:195], a[124:127], v[100:103], v[192:195]\n\tds_read_b64 a[230:231], v0 offset:3080\n\tbuffer_store_dwordx4 v[216:219], v5, s[8:11], s56 offen sc1\n\tv_mfma_f32_16x16x32_bf16 v[188:191], a[96:99], v[124:127], v[188:191]\n\tds_read_b64 a[232:233], v0 offset:2560\n\ts_add_u32 s56, s56, 49152\n\tv_mfma_f32_16x16x32_bf16 v[188:191], a[100:103], v[120:123], v[188:191]\n\t" \
    "ds_read_b64 a[234:235], v0 offset:2568\n\tv_mfma_f32_16x16x32_bf16 v[188:191], a[104:107], v[116:119], v[188:191]\n\tds_read_b64 a[236:237], v0 offset:2048\n\tv_mfma_f32_16x16x32_bf16 v[188:191], a[108:111], v[112:115], v[188:191]\n\tds_read_b64 a[238:239], v0 offset:2056\n\tv_mfma_f32_16x16x32_bf16 v[188:191], a[112:115], v[108:111], v[188:191]\n\tbuffer_load_dword v15, v7, s[16:19], s42 offen\n\tds_read_b64 a[240:241], v0 offset:1536\n\t" \
    "v_mfma_f32_16x16x32_bf16 v[188:191], a[116:119], v[104:107], v[188:191]\n\ts_add_u32 s43, s43, 1\n\tds_read_b64 a[242:243], v0 offset:1544\n\tv_mfma_f32_16x16x32_bf16 v[188:191], a[120:123], v[100:103], v[188:191]\n\tv_readlane_b32 s42, v237, s43\n\ts_nop 4\n\tds_read_b64 a[244:245], v0 offset:1024\n\tv_mfma_f32_16x16x32_bf16 v[184:187], a[96:99], v[120:123], v[184:187]\n\t" \
    "ds_read_b64 a[246:247], v0 offset:1032\n\tv_mfma_f32_16x16x32_bf16 v[184:187], a[100:103], v[116:119], v[184:187]\n\tds_read_b128 v[68:71], v1 offset:18688\n\tv_mfma_f32_16x16x32_bf16 v[184:187], a[104:107], v[112:115], v[184:187]\n\tds_read_b128 v[72:75], v1 offset:19200\n\tv_mfma_f32_16x16x32_bf16 v[184:187], a[108:111], v[108:111], v[184:187]\n\tds_read_b128 v[76:79], v1 offset:19712\n\tv_mfma_f32_16x16x32_bf16 v[184:187], a[112:115], v[104:107], v[184:187]\n\t" \
    "ds_read_b128 v[80:83], v1 offset:20224\n\tv_mfma_f32_16x16x32_bf16 v[184:187], a[116:119], v[100:103], v[184:187]\n\tds_read_b128 v[84:87], v1 offset:20736\n\tv_mfma_f32_16x16x32_bf16 v[180:183], a[96:99], v[116:119], v[180:183]\n\tds_read_b128 v[88:91], v1 offset:21248\n\tv_mfma_f32_16x16x32_bf16 v[180:183], a[100:103], v[112:115], v[180:183]\n\tds_read_b128 v[92:95], v1 offset:21760\n\tv_mfma_f32_16x16x32_bf16 v[180:183], a[104:107], v[108:111], v[180:183]\n\t" \
    "ds_read_b128 v[96:99], v1 offset:22272\n\tv_mfma_f32_16x16x32_bf16 v[180:183], a[108:111], v[104:107], v[180:183]\n\tds_read_b64 a[248:249], v0 offset:512\n\tv_mfma_f32_16x16x32_bf16 v[180:183], a[112:115], v[100:103], v[180:183]\n\tds_read_b64 a[250:251], v0 offset:520\n\tv_mfma_f32_16x16x32_bf16 v[176:179], a[96:99], v[112:115], v[176:179]\n\tds_read_b64 a[252:253], v0 offset:0\n\tv_mfma_f32_16x16x32_bf16 v[176:179], a[100:103], v[108:111], v[176:179]\n\t" \
    "ds_read_b64 a[254:255], v0 offset:8\n\tv_mfma_f32_16x16x32_bf16 v[176:179], a[104:107], v[104:107], v[176:179]\n\tv_mfma_f32_16x16x32_bf16 v[176:179], a[108:111], v[100:103], v[176:179]\n\tv_mfma_f32_16x16x32_bf16 v[172:175], a[96:99], v[108:111], v[172:175]\n\tv_mfma_f32_16x16x32_bf16 v[172:175], a[100:103], v[104:107], v[172:175]\n\tv_mfma_f32_16x16x32_bf16 v[172:175], a[104:107], v[100:103], v[172:175]\n\tv_mfma_f32_16x16x32_bf16 v[168:171], a[96:99], v[104:107], v[168:171]\n\tv_mfma_f32_16x16x32_bf16 v[168:171], a[100:103], v[100:103], v[168:171]\n\t" \
    "v_mfma_f32_16x16x32_bf16 v[164:167], a[96:99], v[100:103], v[164:167]\n\ts_waitcnt lgkmcnt(4)\n\tv_mfma_f32_16x16x32_bf16 v[164:167], a[128:131], v[68:71], v[164:167]\n\tds_read_b128 v[100:103], v1 offset:18624\n\ts_waitcnt vmcnt(8)\n\tv_alignbit_b32 v196, v19, v19, 16\n\tv_mfma_f32_16x16x32_bf16 v[168:171], a[128:131], v[72:75], v[168:171]\n\tds_read_b128 v[104:107], v1 offset:19136\n\t" \
    "v_alignbit_b32 v197, v18, v18, 16\n\tv_mfma_f32_16x16x32_bf16 v[168:171], a[132:135], v[68:71], v[168:171]\n\tds_read_b128 v[108:111], v1 offset:19648\n\tv_alignbit_b32 v198, v17, v17, 16\n\tv_mfma_f32_16x16x32_bf16 v[172:175], a[128:131], v[76:79], v[172:175]\n\tds_read_b128 v[112:115], v1 offset:20160\n\tv_alignbit_b32 v199, v16, v16, 16\n\tv_mfma_f32_16x16x32_bf16 v[172:175], a[132:135], v[72:75], v[172:175]\n\t" \
    "ds_write_b128 v2, v[196:199] offset:3072\n\tv_mfma_f32_16x16x32_bf16 v[172:175], a[136:139], v[68:71], v[172:175]\n\tds_read_b128 v[116:119], v1 offset:20672\n\tv_bfi_b32 v200, s46, v19, v18\n\tv_mfma_f32_16x16x32_bf16 v[176:179], a[128:131], v[80:83], v[176:179]\n\tds_read_b128 v[120:123], v1 offset:21184\n\tv_bfi_b32 v201, s46, v18, v17\n\tv_mfma_f32_16x16x32_bf16 v[176:179], a[132:135], v[76:79], v[176:179]\n\t" \
    "ds_read_b128 v[124:127], v1 offset:21696\n\tv_bfi_b32 v202, s46, v17, v16\n\tv_mfma_f32_16x16x32_bf16 v[176:179], a[136:139], v[72:75], v[176:179]\n\tds_read_b128 v[128:131], v1 offset:22208\n\tv_bfi_b32 v203, s46, v16, v33\n\tv_mfma_f32_16x16x32_bf16 v[176:179], a[140:143], v[68:71], v[176:179]\n\tds_write_b128 v2, v[200:203] offset:7232\n\tv_mfma_f32_16x16x32_bf16 v[180:183], a[128:131], v[84:87], v[180:183]\n\t" \
    "v_alignbit_b32 v196, v18, v18, 16\n\tv_alignbit_b32 v197, v17, v17, 16\n\tv_mfma_f32_16x16x32_bf16 v[180:183], a[132:135], v[80:83], v[180:183]\n\tv_alignbit_b32 v198, v16, v16, 16\n\tv_alignbit_b32 v199, v33, v33, 16\n\tv_mfma_f32_16x16x32_bf16 v[180:183], a[136:139], v[76:79], v[180:183]\n\tds_write_b128 v2, v[196:199] offset:11392\n\tv_mfma_f32_16x16x32_bf16 v[180:183], a[140:143], v[72:75], v[180:183]\n\t" \
    "v_bfi_b32 v200, s46, v18, v17\n\tv_bfi_b32 v201, s46, v17, v16\n\tv_mfma_f32_16x16x32_bf16 v[180:183], a[144:147], v[68:71], v[180:183]\n\tv_bfi_b32 v202, s46, v16, v33\n\tv_bfi_b32 v203, s46, v33, v32\n\tv_mfma_f32_16x16x32_bf16 v[184:187], a[128:131], v[88:91], v[184:187]\n\tds_write_b128 v2, v[200:203] offset:15552\n\tv_mfma_f32_16x16x32_bf16 v[184:187], a[132:135], v[84:87], v[184:187]\n\t" \
    "v_alignbit_b32 v196, v23, v23, 16\n\tv_alignbit_b32 v197, v22, v22, 16\n\tv_mfma_f32_16x16x32_bf16 v[184:187], a[136:139], v[80:83], v[184:187]\n\tv_alignbit_b32 v198, v21, v21, 16\n\tv_alignbit_b32 v199, v20, v20, 16\n\tv_mfma_f32_16x16x32_bf16 v[184:187], a[140:143], v[76:79], v[184:187]\n\tds_write_b128 v2, v[196:199] offset:2048\n\tv_mfma_f32_16x16x32_bf16 v[184:187], a[144:147], v[72:75], v[184:187]\n\t" \
    "v_bfi_b32 v200, s46, v23, v22\n\tv_bfi_b32 v201, s46, v22, v21\n\tv_mfma_f32_16x16x32_bf16 v[184:187], a[148:151], v[68:71], v[184:187]\n\tv_bfi_b32 v202, s46, v21, v20\n\tv_bfi_b32 v203, s46, v20, v35\n\tv_mfma_f32_16x16x32_bf16 v[188:191], a[128:131], v[92:95], v[188:191]\n\tds_write_b128 v2, v[200:203] offset:6208\n\tv_mfma_f32_16x16x32_bf16 v[188:191], a[132:135], v[88:91], v[188:191]\n\t" \
    "v_alignbit_b32 v196, v22, v22, 16\n\tv_alignbit_b32 v197, v21, v21, 16\n\tv_mfma_f32_16x16x32_bf16 v[188:191], a[136:139], v[84:87], v[188:191]\n\tv_alignbit_b32 v198, v20, v20, 16\n\tv_alignbit_b32 v199, v35, v35, 16\n\tv_mfma_f32_16x16x32_bf16 v[188:191], a[140:143], v[80:83], v[188:191]\n\tds_write_b128 v2, v[196:199] offset:10368\n\tv_mfma_f32_16x16x32_bf16 v[188:191], a[144:147], v[76:79], v[188:191]\n\t" \
    "v_bfi_b32 v200, s46, v22, v21\n\tv_bfi_b32 v201, s46, v21, v20\n\tv_mfma_f32_16x16x32_bf16 v[188:191], a[148:151], v[72:75], v[188:191]\n\tv_bfi_b32 v202, s46, v20, v35\n\tv_bfi_b32 v203, s46, v35, v34\n\tv_mfma_f32_16x16x32_bf16 v[188:191], a[152:155], v[68:71], v[188:191]\n\tds_write_b128 v2, v[200:203] offset:14528\n\tv_mfma_f32_16x16x32_bf16 v[192:195], a[128:131], v[96:99], v[192:195]\n\t" \
    "v_alignbit_b32 v196, v27, v27, 16\n\tv_alignbit_b32 v197, v26, v26, 16\n\tv_mfma_f32_16x16x32_bf16 v[192:195], a[132:135], v[92:95], v[192:195]\n\tv_alignbit_b32 v198, v25, v25, 16\n\tv_alignbit_b32 v199, v24, v24, 16\n\tv_mfma_f32_16x16x32_bf16 v[192:195], a[136:139], v[88:91], v[192:195]\n\tds_write_b128 v2, v[196:199] offset:1024\n\tv_mfma_f32_16x16x32_bf16 v[192:195], a[140:143], v[84:87], v[192:195]\n\t" \
    "v_bfi_b32 v200, s46, v27, v26\n\tv_bfi_b32 v201, s46, v26, v25\n\tv_mfma_f32_16x16x32_bf16 v[192:195], a[144:147], v[80:83], v[192:195]\n\tv_bfi_b32 v202, s46, v25, v24\n\tv_bfi_b32 v203, s46, v24, v37\n\tv_mfma_f32_16x16x32_bf16 v[192:195], a[148:151], v[76:79], v[192:195]\n\tds_write_b128 v2, v[200:203] offset:5184\n\tv_mfma_f32_16x16x32_bf16 v[192:195], a[152:155], v[72:75], v[192:195]\n\t" \
    "v_alignbit_b32 v196, v26, v26, 16\n\tv_alignbit_b32 v197, v25, v25, 16\n\tv_mfma_f32_16x16x32_bf16 v[192:195], a[156:159], v[68:71], v[192:195]\n\tv_alignbit_b32 v198, v24, v24, 16\n\tv_alignbit_b32 v199, v37, v37, 16\n\ts_waitcnt lgkmcnt(9)\n\tv_mfma_f32_16x16x32_bf16 v[192:195], a[160:163], v[128:131], v[192:195]\n\tds_write_b128 v2, v[196:199] offset:9344\n\t" \
    "v_mfma_f32_16x16x32_bf16 v[192:195], a[164:167], v[124:127], v[192:195]\n\tds_read_b128 v[68:71], v1 offset:18560\n\tv_bfi_b32 v200, s46, v26, v25\n\tv_mfma_f32_16x16x32_bf16 v[192:195], a[168:171], v[120:123], v[192:195]\n\tds_read_b128 v[72:75], v1 offset:19072\n\tv_bfi_b32 v201, s46, v25, v24\n\tv_mfma_f32_16x16x32_bf16 v[192:195], a[172:175], v[116:119], v[192:195]\n\tds_read_b128 v[76:79], v1 offset:19584\n\t" \
    "v_bfi_b32 v202, s46, v24, v37\n\tv_mfma_f32_16x16x32_bf16 v[192:195], a[176:179], v[112:115], v[192:195]\n\tds_read_b128 v[80:83], v1 offset:20096\n\tv_bfi_b32 v203, s46, v37, v36\n\tv_mfma_f32_16x16x32_bf16 v[192:195], a[180:183], v[108:111], v[192:195]\n\tds_write_b128 v2, v[200:203] offset:13504\n\tv_mfma_f32_16x16x32_bf16 v[192:195], a[184:187], v[104:107], v[192:195]\n\tds_read_b128 v[84:87], v1 offset:20608\n\t" \
    "v_alignbit_b32 v196, v31, v31, 16\n\tv_mfma_f32_16x16x32_bf16 v[192:195], a[188:191], v[100:103], v[192:195]\n\tds_read_b128 v[88:91], v1 offset:21120\n\tv_alignbit_b32 v197, v30, v30, 16\n\tv_mfma_f32_16x16x32_bf16 v[188:191], a[160:163], v[124:127], v[188:191]\n\tds_read_b128 v[92:95], v1 offset:21632\n\tv_alignbit_b32 v198, v29, v29, 16\n\tv_mfma_f32_16x16x32_bf16 v[188:191], a[164:167], v[120:123], v[188:191]\n\t" \
    "ds_read_b128 v[96:99], v1 offset:22144\n\tv_alignbit_b32 v199, v28, v28, 16\n\tv_mfma_f32_16x16x32_bf16 v[188:191], a[168:171], v[116:119], v[188:191]\n\tds_write_b128 v2, v[196:199] offset:0\n\tv_mfma_f32_16x16x32_bf16 v[188:191], a[172:175], v[112:115], v[188:191]\n\tv_bfi_b32 v200, s46, v31, v30\n\tv_bfi_b32 v201, s46, v30, v29\n\tv_mfma_f32_16x16x32_bf16 v[188:191], a[176:179], v[108:111], v[188:191]\n\t" \
    "v_bfi_b32 v202, s46, v29, v28\n\tv_bfi_b32 v203, s46, v28, v39\n\tv_mfma_f32_16x16x32_bf16 v[188:191], a[180:183], v[104:107], v[188:191]\n\tds_write_b128 v2, v[200:203] offset:4160\n\tv_mfma_f32_16x16x32_bf16 v[188:191], a[184:187], v[100:103], v[188:191]\n\tv_alignbit_b32 v196, v30, v30, 16\n\tv_alignbit_b32 v197, v29, v29, 16\n\tv_mfma_f32_16x16x32_bf16 v[184:187], a[160:163], v[120:123], v[184:187]\n\t" \
    "v_alignbit_b32 v198, v28, v28, 16\n\tv_alignbit_b32 v199, v39, v39, 16\n\tv_mfma_f32_16x16x32_bf16 v[184:187], a[164:167], v[116:119], v[184:187]\n\tds_write_b128 v2, v[196:199] offset:8320\n\tv_mfma_f32_16x16x32_bf16 v[184:187], a[168:171], v[112:115], v[184:187]\n\tv_bfi_b32 v200, s46, v30, v29\n\tv_bfi_b32 v201, s46, v29, v28\n\tv_mfma_f32_16x16x32_bf16 v[184:187], a[172:175], v[108:111], v[184:187]\n\t" \
    "v_bfi_b32 v202, s46, v28, v39\n\tv_bfi_b32 v203, s46, v39, v38\n\tv_mfma_f32_16x16x32_bf16 v[184:187], a[176:179], v[104:107], v[184:187]\n\tds_write_b128 v2, v[200:203] offset:12480\n\tv_mfma_f32_16x16x32_bf16 v[184:187], a[180:183], v[100:103], v[184:187]\n\tv_lshl_or_b32 v40, v40, 16, v40\n\tv_and_b32 v228, v40, v232\n\tv_mfma_f32_16x16x32_bf16 v[180:183], a[160:163], v[116:119], v[180:183]\n\t" \
    "v_and_b32 v229, v40, v233\n\tv_and_b32 v230, v40, v234\n\tv_mfma_f32_16x16x32_bf16 v[180:183], a[164:167], v[112:115], v[180:183]\n\tv_and_b32 v231, v40, v235\n\tv_mfma_f32_16x16x32_bf16 v[180:183], a[168:171], v[108:111], v[180:183]\n\tv_mfma_f32_16x16x32_bf16 v[180:183], a[172:175], v[104:107], v[180:183]\n\tv_mfma_f32_16x16x32_bf16 v[180:183], a[176:179], v[100:103], v[180:183]\n\tv_mfma_f32_16x16x32_bf16 v[176:179], a[160:163], v[112:115], v[176:179]\n\t" \
    "v_mfma_f32_16x16x32_bf16 v[176:179], a[164:167], v[108:111], v[176:179]\n\tv_mfma_f32_16x16x32_bf16 v[176:179], a[168:171], v[104:107], v[176:179]\n\tv_mfma_f32_16x16x32_bf16 v[176:179], a[172:175], v[100:103], v[176:179]\n\tv_mfma_f32_16x16x32_bf16 v[172:175], a[160:163], v[108:111], v[172:175]\n\tv_mfma_f32_16x16x32_bf16 v[172:175], a[164:167], v[104:107], v[172:175]\n\tv_mfma_f32_16x16x32_bf16 v[172:175], a[168:171], v[100:103], v[172:175]\n\tv_mfma_f32_16x16x32_bf16 v[168:171], a[160:163], v[104:107], v[168:171]\n\tv_mfma_f32_16x16x32_bf16 v[168:171], a[164:167], v[100:103], v[168:171]\n\t" \
    "v_mfma_f32_16x16x32_bf16 v[164:167], a[160:163], v[100:103], v[164:167]\n\ts_waitcnt lgkmcnt(4)\n\tv_mfma_f32_16x16x32_bf16 v[164:167], a[192:195], v[68:71], v[164:167]\n\ts_add_u32 s68, s44, s38\n\tds_read_b128 v[100:103], v1 offset:18496\n\tv_mfma_f32_16x16x32_bf16 v[168:171], a[192:195], v[72:75], v[168:171]\n\ts_add_u32 s68, s68, 12\n\tds_read_b128 v[104:107], v1 offset:19008\n\t" \
    "v_mfma_f32_16x16x32_bf16 v[168:171], a[196:199], v[68:71], v[168:171]\n\ts_lshl_b32 s66, s68, 1\n\tds_read_b128 v[108:111], v1 offset:19520\n\tv_mfma_f32_16x16x32_bf16 v[172:175], a[192:195], v[76:79], v[172:175]\n\ts_lshl_b32 s68, s68, 12\n\tds_read_b128 v[112:115], v1 offset:20032\n\tv_mfma_f32_16x16x32_bf16 v[172:175], a[196:199], v[72:75], v[172:175]\n\ts_or_b32 s68, s68, s45\n\t" \
    "ds_read_b128 v[116:119], v1 offset:20544\n\tv_mfma_f32_16x16x32_bf16 v[172:175], a[200:203], v[68:71], v[172:175]\n\ts_or_b32 s66, s66, s45\n\tbuffer_load_dwordx4 v[42:45], v8, s[76:79], s68 offen offset:0\n\tds_read_b128 v[120:123], v1 offset:21056\n\tv_mfma_f32_16x16x32_bf16 v[176:179], a[192:195], v[80:83], v[176:179]\n\tds_read_b128 v[124:127], v1 offset:21568\n\tv_mfma_f32_16x16x32_bf16 v[176:179], a[196:199], v[76:79], v[176:179]\n\t" \
    "ds_read_b128 v[128:131], v1 offset:22080\n\tv_mfma_f32_16x16x32_bf16 v[176:179], a[200:203], v[72:75], v[176:179]\n\tv_mfma_f32_16x16x32_bf16 v[176:179], a[204:207], v[68:71], v[176:179]\n\tv_mfma_f32_16x16x32_bf16 v[180:183], a[192:195], v[84:87], v[180:183]\n\tv_mfma_f32_16x16x32_bf16 v[180:183], a[196:199], v[80:83], v[180:183]\n\tbuffer_load_dwordx4 v[46:49], v8, s[76:79], s68 offen offset:1024\n\tv_mfma_f32_16x16x32_bf16 v[180:183], a[200:203], v[76:79], v[180:183]\n\tv_mfma_f32_16x16x32_bf16 v[180:183], a[204:207], v[72:75], v[180:183]\n\t" \
    "v_mfma_f32_16x16x32_bf16 v[180:183], a[208:211], v[68:71], v[180:183]\n\tv_mfma_f32_16x16x32_bf16 v[184:187], a[192:195], v[88:91], v[184:187]\n\tv_mfma_f32_16x16x32_bf16 v[184:187], a[196:199], v[84:87], v[184:187]\n\tv_mfma_f32_16x16x32_bf16 v[184:187], a[200:203], v[80:83], v[184:187]\n\tbuffer_load_dwordx4 v[50:53], v8, s[76:79], s68 offen offset:2048\n\tv_mfma_f32_16x16x32_bf16 v[184:187], a[204:207], v[76:79], v[184:187]\n\tv_mfma_f32_16x16x32_bf16 v[184:187], a[208:211], v[72:75], v[184:187]\n\tv_mfma_f32_16x16x32_bf16 v[184:187], a[212:215], v[68:71], v[184:187]\n\t" \
    "v_mfma_f32_16x16x32_bf16 v[188:191], a[192:195], v[92:95], v[188:191]\n\tv_mfma_f32_16x16x32_bf16 v[188:191], a[196:199], v[88:91], v[188:191]\n\tv_mfma_f32_16x16x32_bf16 v[188:191], a[200:203], v[84:87], v[188:191]\n\tbuffer_load_dwordx4 v[54:57], v8, s[76:79], s68 offen offset:3072\n\tv_mfma_f32_16x16x32_bf16 v[188:191], a[204:207], v[80:83], v[188:191]\n\tv_mfma_f32_16x16x32_bf16 v[188:191], a[208:211], v[76:79], v[188:191]\n\tv_mfma_f32_16x16x32_bf16 v[188:191], a[212:215], v[72:75], v[188:191]\n\tv_mfma_f32_16x16x32_bf16 v[188:191], a[216:219], v[68:71], v[188:191]\n\t" \
    "v_mfma_f32_16x16x32_bf16 v[192:195], a[192:195], v[96:99], v[192:195]\n\tv_mfma_f32_16x16x32_bf16 v[192:195], a[196:199], v[92:95], v[192:195]\n\tbuffer_load_dwordx2 v[58:59], v13, s[76:79], s68 offen\n\tv_mfma_f32_16x16x32_bf16 v[192:195], a[200:203], v[88:91], v[192:195]\n\tv_mfma_f32_16x16x32_bf16 v[192:195], a[204:207], v[84:87], v[192:195]\n\tv_mfma_f32_16x16x32_bf16 v[192:195], a[208:211], v[80:83], v[192:195]\n\tv_mfma_f32_16x16x32_bf16 v[192:195], a[212:215], v[76:79], v[192:195]\n\tv_mfma_f32_16x16x32_bf16 v[192:195], a[216:219], v[72:75], v[192:195]\n\t" \
    "v_mfma_f32_16x16x32_bf16 v[192:195], a[220:223], v[68:71], v[192:195]\n\tbuffer_load_dwordx2 v[60:61], v8, s[76:79], s68 offen offset:1016\n\ts_waitcnt lgkmcnt(0)\n\tv_mfma_f32_16x16x32_bf16 v[192:195], a[224:227], v[128:131], v[192:195]\n\tds_read_b64 a[0:1], v0 offset:4032\n\tv_mfma_f32_16x16x32_bf16 v[192:195], a[228:231], v[124:127], v[192:195]\n\tds_read_b64 a[2:3], v0 offset:4040\n\tv_mfma_f32_16x16x32_bf16 v[192:195], a[232:235], v[120:123], v[192:195]\n\t" \
    "ds_read_b64 a[4:5], v0 offset:3520\n\tv_mfma_f32_16x16x32_bf16 v[192:195], a[236:239], v[116:119], v[192:195]\n\tds_read_b64 a[6:7], v0 offset:3528\n\tv_mfma_f32_16x16x32_bf16 v[192:195], a[240:243], v[112:115], v[192:195]\n\tds_read_b64 a[8:9], v0 offset:3008\n\tv_mfma_f32_16x16x32_bf16 v[192:195], a[244:247], v[108:111], v[192:195]\n\tbuffer_load_dwordx2 v[62:63], v8, s[76:79], s68 offen offset:2040\n\tds_read_b64 a[10:11], v0 offset:3016\n\t" \
    "v_mfma_f32_16x16x32_bf16 v[192:195], a[248:251], v[104:107], v[192:195]\n\tds_read_b64 a[12:13], v0 offset:2496\n\tv_mfma_f32_16x16x32_bf16 v[192:195], a[252:255], v[100:103], v[192:195]\n\tds_read_b64 a[14:15], v0 offset:2504\n\tv_mfma_f32_16x16x32_bf16 v[188:191], a[224:227], v[124:127], v[188:191]\n\tds_read_b64 a[16:17], v0 offset:1984\n\tv_mfma_f32_16x16x32_bf16 v[188:191], a[228:231], v[120:123], v[188:191]\n\tds_read_b64 a[18:19], v0 offset:1992\n\t" \
    "v_mfma_f32_16x16x32_bf16 v[188:191], a[232:235], v[116:119], v[188:191]\n\tds_read_b64 a[20:21], v0 offset:1472\n\tv_mfma_f32_16x16x32_bf16 v[188:191], a[236:239], v[112:115], v[188:191]\n\tbuffer_load_dwordx2 v[64:65], v8, s[76:79], s68 offen offset:3064\n\tds_read_b64 a[22:23], v0 offset:1480\n\tv_mfma_f32_16x16x32_bf16 v[188:191], a[240:243], v[108:111], v[188:191]\n\tds_read_b64 a[24:25], v0 offset:960\n\tv_mfma_f32_16x16x32_bf16 v[188:191], a[244:247], v[104:107], v[188:191]\n\t" \
    "ds_read_b64 a[26:27], v0 offset:968\n\tv_mfma_f32_16x16x32_bf16 v[188:191], a[248:251], v[100:103], v[188:191]\n\tds_read_b64 a[28:29], v0 offset:448\n\tv_mfma_f32_16x16x32_bf16 v[184:187], a[224:227], v[120:123], v[184:187]\n\tds_read_b64 a[30:31], v0 offset:456\n\tv_mfma_f32_16x16x32_bf16 v[184:187], a[228:231], v[116:119], v[184:187]\n\tds_read_b128 v[68:71], v1 offset:37440\n\tv_mfma_f32_16x16x32_bf16 v[184:187], a[232:235], v[112:115], v[184:187]\n\t" \
    "buffer_load_ushort v66, off, s[80:83], s66\n\tds_read_b128 v[72:75], v1 offset:37952\n\tv_mfma_f32_16x16x32_bf16 v[184:187], a[236:239], v[108:111], v[184:187]\n\tds_read_b128 v[76:79], v1 offset:38464\n\tv_mfma_f32_16x16x32_bf16 v[184:187], a[240:243], v[104:107], v[184:187]\n\tds_read_b128 v[80:83], v1 offset:38976\n\tv_mfma_f32_16x16x32_bf16 v[184:187], a[244:247], v[100:103], v[184:187]\n\tds_read_b128 v[84:87], v1 offset:39488\n\t" \
    "v_mfma_f32_16x16x32_bf16 v[180:183], a[224:227], v[116:119], v[180:183]\n\tds_read_b128 v[88:91], v1 offset:40000\n\tv_mfma_f32_16x16x32_bf16 v[180:183], a[228:231], v[112:115], v[180:183]\n\tds_read_b128 v[92:95], v1 offset:40512\n\tv_mfma_f32_16x16x32_bf16 v[180:183], a[232:235], v[108:111], v[180:183]\n\ts_lshl_b32 s69, s67, 13\n\tbuffer_load_dword v15, v14, s[4:7], s69 offen\n\tds_read_b128 v[96:99], v1 offset:41024\n\t" \
    "v_mfma_f32_16x16x32_bf16 v[180:183], a[236:239], v[104:107], v[180:183]\n\ts_waitcnt vmcnt(11)\n\tbuffer_store_dwordx2 v[248:249], off, s[8:11], s51 sc1\n\tv_mfma_f32_16x16x32_bf16 v[180:183], a[240:243], v[100:103], v[180:183]\n\tv_mfma_f32_16x16x32_bf16 v[176:179], a[224:227], v[112:115], v[176:179]\n\tv_mfma_f32_16x16x32_bf16 v[176:179], a[228:231], v[108:111], v[176:179]\n\tv_mfma_f32_16x16x32_bf16 v[176:179], a[232:235], v[104:107], v[176:179]\n\tv_mfma_f32_16x16x32_bf16 v[176:179], a[236:239], v[100:103], v[176:179]\n\t" \
    "v_mfma_f32_16x16x32_bf16 v[172:175], a[224:227], v[108:111], v[172:175]\n\tv_mfma_f32_16x16x32_bf16 v[172:175], a[228:231], v[104:107], v[172:175]\n\tv_mfma_f32_16x16x32_bf16 v[172:175], a[232:235], v[100:103], v[172:175]\n\tv_mfma_f32_16x16x32_bf16 v[168:171], a[224:227], v[104:107], v[168:171]\n\tv_mfma_f32_16x16x32_bf16 v[168:171], a[228:231], v[100:103], v[168:171]\n\tv_mfma_f32_16x16x32_bf16 v[164:167], a[224:227], v[100:103], v[164:167]\n\ts_waitcnt lgkmcnt(0)\n\tv_mfma_f32_16x16x32_bf16 v[132:135], v[228:231], v[68:71], 0\n\t" \
    "ds_read_b64 a[32:33], v0 offset:3968\n\tds_read_b64 a[34:35], v0 offset:3976\n\tv_mfma_f32_16x16x32_bf16 v[132:135], a[0:3], v[68:71], v[132:135]\n\tds_read_b64 a[36:37], v0 offset:3456\n\tds_read_b64 a[38:39], v0 offset:3464\n\tv_mfma_f32_16x16x32_bf16 v[136:139], v[228:231], v[72:75], 0\n\tds_read_b64 a[40:41], v0 offset:2944\n\tds_read_b64 a[42:43], v0 offset:2952\n\t" \
    "v_mfma_f32_16x16x32_bf16 v[136:139], a[0:3], v[72:75], v[136:139]\n\tds_read_b64 a[44:45], v0 offset:2432\n\tds_read_b64 a[46:47], v0 offset:2440\n\tv_mfma_f32_16x16x32_bf16 v[136:139], a[4:7], v[68:71], v[136:139]\n\tds_read_b64 a[48:49], v0 offset:1920\n\tds_read_b64 a[50:51], v0 offset:1928\n\tv_mfma_f32_16x16x32_bf16 v[140:143], v[228:231], v[76:79], 0\n\tds_read_b64 a[52:53], v0 offset:1408\n\t" \
    "v_cvt_pk_bf16_f32 v164, v164, v165\n\tv_mfma_f32_16x16x32_bf16 v[140:143], a[0:3], v[76:79], v[140:143]\n\tds_read_b64 a[54:55], v0 offset:1416\n\tv_cvt_pk_bf16_f32 v165, v166, v167\n\tv_mfma_f32_16x16x32_bf16 v[140:143], a[4:7], v[72:75], v[140:143]\n\tds_read_b64 a[56:57], v0 offset:896\n\tds_write_b64 v3, v[164:165] offset:18496\n\tv_mfma_f32_16x16x32_bf16 v[140:143], a[8:11], v[68:71], v[140:143]\n\t" \
    "ds_read_b64 a[58:59], v0 offset:904\n\tv_cvt_pk_bf16_f32 v168, v168, v169\n\tv_mfma_f32_16x16x32_bf16 v[144:147], v[228:231], v[80:83], 0\n\tds_read_b64 a[60:61], v0 offset:384\n\tv_cvt_pk_bf16_f32 v169, v170, v171\n\tv_mfma_f32_16x16x32_bf16 v[144:147], a[0:3], v[80:83], v[144:147]\n\tds_read_b64 a[62:63], v0 offset:392\n\tds_write_b64 v3, v[168:169] offset:19008\n\t" \
    "v_mfma_f32_16x16x32_bf16 v[144:147], a[4:7], v[76:79], v[144:147]\n\tds_read_b128 v[100:103], v1 offset:37376\n\tv_cvt_pk_bf16_f32 v172, v172, v173\n\tv_mfma_f32_16x16x32_bf16 v[144:147], a[8:11], v[72:75], v[144:147]\n\tds_read_b128 v[104:107], v1 offset:37888\n\tv_cvt_pk_bf16_f32 v173, v174, v175\n\tv_mfma_f32_16x16x32_bf16 v[144:147], a[12:15], v[68:71], v[144:147]\n\tds_read_b128 v[108:111], v1 offset:38400\n\t" \
    "ds_write_b64 v3, v[172:173] offset:19520\n\tv_mfma_f32_16x16x32_bf16 v[148:151], v[228:231], v[84:87], 0\n\tds_read_b128 v[112:115], v1 offset:38912\n\tv_cvt_pk_bf16_f32 v176, v176, v177\n\tv_mfma_f32_16x16x32_bf16 v[148:151], a[0:3], v[84:87], v[148:151]\n\tds_read_b128 v[116:119], v1 offset:39424\n\tv_cvt_pk_bf16_f32 v177, v178, v179\n\tv_mfma_f32_16x16x32_bf16 v[148:151], a[4:7], v[80:83], v[148:151]\n\t" \
    "ds_read_b128 v[120:123], v1 offset:39936\n\tds_write_b64 v3, v[176:177] offset:20032\n\tv_mfma_f32_16x16x32_bf16 v[148:151], a[8:11], v[76:79], v[148:151]\n\tds_read_b128 v[124:127], v1 offset:40448\n\tv_cvt_pk_bf16_f32 v180, v180, v181\n\tv_mfma_f32_16x16x32_bf16 v[148:151], a[12:15], v[72:75], v[148:151]\n\tds_read_b128 v[128:131], v1 offset:40960\n\tv_cvt_pk_bf16_f32 v181, v182, v183\n\t" \
    "v_mfma_f32_16x16x32_bf16 v[148:151], a[16:19], v[68:71], v[148:151]\n\tds_read_b64 a[64:65], v0 offset:3904\n\tds_write_b64 v3, v[180:181] offset:20544\n\tv_mfma_f32_16x16x32_bf16 v[152:155], v[228:231], v[88:91], 0\n\tds_read_b64 a[66:67], v0 offset:3912\n\tv_cvt_pk_bf16_f32 v184, v184, v185\n\tv_mfma_f32_16x16x32_bf16 v[152:155], a[0:3], v[88:91], v[152:155]\n\tds_read_b64 a[68:69], v0 offset:3392\n\t" \
    "v_cvt_pk_bf16_f32 v185, v186, v187\n\tv_mfma_f32_16x16x32_bf16 v[152:155], a[4:7], v[84:87], v[152:155]\n\tbuffer_load_dword v15, v7, s[16:19], s42 offen\n\tds_read_b64 a[70:71], v0 offset:3400\n\tv_mfma_f32_16x16x32_bf16 v[152:155], a[8:11], v[80:83], v[152:155]\n\ts_add_u32 s43, s43, 1\n\tds_read_b64 a[72:73], v0 offset:2880\n\tds_write_b64 v3, v[184:185] offset:21056\n\t" \
    "v_mfma_f32_16x16x32_bf16 v[152:155], a[12:15], v[76:79], v[152:155]\n\tv_readlane_b32 s42, v237, s43\n\ts_nop 4\n\tds_read_b64 a[74:75], v0 offset:2888\n\tv_cvt_pk_bf16_f32 v188, v188, v189\n\tv_mfma_f32_16x16x32_bf16 v[152:155], a[16:19], v[72:75], v[152:155]\n\tds_read_b64 a[76:77], v0 offset:2368\n\tv_cvt_pk_bf16_f32 v189, v190, v191\n\t" \
    "v_mfma_f32_16x16x32_bf16 v[152:155], a[20:23], v[68:71], v[152:155]\n\tds_read_b64 a[78:79], v0 offset:2376\n\tds_write_b64 v3, v[188:189] offset:21568\n\tv_mfma_f32_16x16x32_bf16 v[156:159], v[228:231], v[92:95], 0\n\tds_read_b64 a[80:81], v0 offset:1856\n\tv_cvt_pk_bf16_f32 v192, v192, v193\n\tv_mfma_f32_16x16x32_bf16 v[156:159], a[0:3], v[92:95], v[156:159]\n\tds_read_b64 a[82:83], v0 offset:1864\n\t" \
    "v_cvt_pk_bf16_f32 v193, v194, v195\n\tv_mfma_f32_16x16x32_bf16 v[156:159], a[4:7], v[88:91], v[156:159]\n\tds_read_b64 a[84:85], v0 offset:1344\n\tds_write_b64 v3, v[192:193] offset:22080\n\tv_mfma_f32_16x16x32_bf16 v[156:159], a[8:11], v[84:87], v[156:159]\n\tds_read_b64 a[86:87], v0 offset:1352\n\tv_mfma_f32_16x16x32_bf16 v[156:159], a[12:15], v[80:83], v[156:159]\n\tds_read_b64 a[88:89], v0 offset:832\n\t" \
    "v_mfma_f32_16x16x32_bf16 v[156:159], a[16:19], v[76:79], v[156:159]\n\tds_read_b64 a[90:91], v0 offset:840\n\tv_mfma_f32_16x16x32_bf16 v[156:159], a[20:23], v[72:75], v[156:159]\n\tds_read_b64 a[92:93], v0 offset:320\n\tv_mfma_f32_16x16x32_bf16 v[156:159], a[24:27], v[68:71], v[156:159]\n\tds_read_b64 a[94:95], v0 offset:328\n\tv_mfma_f32_16x16x32_bf16 v[160:163], v[228:231], v[96:99], 0\n\tds_read_b64 a[96:97], v0 offset:3840\n\t" \
    "v_mfma_f32_16x16x32_bf16 v[160:163], a[0:3], v[96:99], v[160:163]\n\tds_read_b64 a[98:99], v0 offset:3848\n\tv_mfma_f32_16x16x32_bf16 v[160:163], a[4:7], v[92:95], v[160:163]\n\tds_read_b64 a[100:101], v0 offset:3328\n\tv_mfma_f32_16x16x32_bf16 v[160:163], a[8:11], v[88:91], v[160:163]\n\tds_read_b64 a[102:103], v0 offset:3336\n\tv_mfma_f32_16x16x32_bf16 v[160:163], a[12:15], v[84:87], v[160:163]\n\tv_mfma_f32_16x16x32_bf16 v[160:163], a[16:19], v[80:83], v[160:163]\n\t" \
    "v_mfma_f32_16x16x32_bf16 v[160:163], a[20:23], v[76:79], v[160:163]\n\tv_mfma_f32_16x16x32_bf16 v[160:163], a[24:27], v[72:75], v[160:163]\n\tv_mfma_f32_16x16x32_bf16 v[160:163], a[28:31], v[68:71], v[160:163]\n\ts_waitcnt lgkmcnt(9)\n\ts_barrier\n\tv_mfma_f32_16x16x32_bf16 v[160:163], a[32:35], v[128:131], v[160:163]\n\tds_read_b64 a[104:105], v0 offset:2816\n\tds_read_b64 a[106:107], v0 offset:2824\n\t" \
    "v_mfma_f32_16x16x32_bf16 v[160:163], a[36:39], v[124:127], v[160:163]\n\tds_read_b64 a[108:109], v0 offset:2304\n\tds_read_b64 a[110:111], v0 offset:2312\n\tv_mfma_f32_16x16x32_bf16 v[160:163], a[40:43], v[120:123], v[160:163]\n\tds_read_b64 a[112:113], v0 offset:1792\n\tds_read_b64 a[114:115], v0 offset:1800\n\tv_mfma_f32_16x16x32_bf16 v[160:163], a[44:47], v[116:119], v[160:163]\n\tds_read_b64 a[116:117], v0 offset:1280\n\t" \
    "ds_read_b64 a[118:119], v0 offset:1288\n\tv_mfma_f32_16x16x32_bf16 v[160:163], a[48:51], v[112:115], v[160:163]\n\tds_read_b64 a[120:121], v0 offset:768\n\tds_read_b64 a[122:123], v0 offset:776\n\tv_mfma_f32_16x16x32_bf16 v[160:163], a[52:55], v[108:111], v[160:163]\n\tds_read_b64 a[124:125], v0 offset:256\n\ts_add_u32 s51, s44, 4\n\tv_mfma_f32_16x16x32_bf16 v[160:163], a[56:59], v[104:107], v[160:163]\n\t" \
    "ds_read_b64 a[126:127], v0 offset:264\n\ts_mul_i32 s65, s51, 1366\n\tv_mfma_f32_16x16x32_bf16 v[160:163], a[60:63], v[100:103], v[160:163]\n\tds_read_b64 a[128:129], v0 offset:3776\n\ts_lshr_b32 s65, s65, 16\n\tv_mfma_f32_16x16x32_bf16 v[156:159], a[32:35], v[124:127], v[156:159]\n\tds_read_b64 a[130:131], v0 offset:3784\n\ts_mul_i32 s56, s65, 48\n\t" \
    "v_mfma_f32_16x16x32_bf16 v[156:159], a[36:39], v[120:123], v[156:159]\n\tds_read_b64 a[132:133], v0 offset:3264\n\ts_sub_u32 s56, s51, s56\n\tv_mfma_f32_16x16x32_bf16 v[156:159], a[40:43], v[116:119], v[156:159]\n\tds_read_b64 a[134:135], v0 offset:3272\n\ts_lshl_b32 s56, s56, 6\n\tv_mfma_f32_16x16x32_bf16 v[156:159], a[44:47], v[112:115], v[156:159]\n\tds_read_b128 v[68:71], v1 offset:37312\n\t" \
    "s_lshr_b32 s70, s65, 2\n\tv_mfma_f32_16x16x32_bf16 v[156:159], a[48:51], v[108:111], v[156:159]\n\tds_read_b128 v[72:75], v1 offset:37824\n\ts_mul_i32 s70, s70, 252\n\tv_mfma_f32_16x16x32_bf16 v[156:159], a[52:55], v[104:107], v[156:159]\n\tds_read_b128 v[76:79], v1 offset:38336\n\ts_add_u32 s65, s65, s70\n\tv_mfma_f32_16x16x32_bf16 v[156:159], a[56:59], v[100:103], v[156:159]\n\t" \
    "ds_read_b128 v[80:83], v1 offset:38848\n\ts_mul_i32 s65, s65, 3072\n\tv_mfma_f32_16x16x32_bf16 v[152:155], a[32:35], v[120:123], v[152:155]\n\tds_read_b128 v[84:87], v1 offset:39360\n\ts_add_u32 s56, s56, s65\n\tv_mfma_f32_16x16x32_bf16 v[152:155], a[36:39], v[116:119], v[152:155]\n\tds_read_b128 v[88:91], v1 offset:39872\n\ts_add_u32 s56, s56, 4849664\n\t" \
    "v_mfma_f32_16x16x32_bf16 v[152:155], a[40:43], v[112:115], v[152:155]\n\tds_read_b128 v[92:95], v1 offset:40384\n\ts_add_u32 s51, s51, s38\n\tv_mfma_f32_16x16x32_bf16 v[152:155], a[44:47], v[108:111], v[152:155]\n\tds_read_b128 v[96:99], v1 offset:40896\n\ts_lshl_b32 s51, s51, 3\n\tv_mfma_f32_16x16x32_bf16 v[152:155], a[48:51], v[104:107], v[152:155]\n\tds_read_b64 a[136:137], v0 offset:2752\n\t" \
    "s_add_u32 s51, s51, 50176\n\tds_read_b64_tr_b16 v[204:205], v4 offset:18496\n\tv_mfma_f32_16x16x32_bf16 v[152:155], a[52:55], v[100:103], v[152:155]\n\tds_read_b64 a[138:139], v0 offset:2760\n\tds_read_b64_tr_b16 v[206:207], v4 offset:18504\n\tv_mfma_f32_16x16x32_bf16 v[148:151], a[32:35], v[116:119], v[148:151]\n\tds_read_b64 a[140:141], v0 offset:2240\n\tds_read_b64_tr_b16 v[208:209], v4 offset:18560\n\t" \
    "v_mfma_f32_16x16x32_bf16 v[148:151], a[36:39], v[112:115], v[148:151]\n\tds_read_b64 a[142:143], v0 offset:2248\n\tds_read_b64_tr_b16 v[210:211], v4 offset:18568\n\tv_mfma_f32_16x16x32_bf16 v[148:151], a[40:43], v[108:111], v[148:151]\n\tds_read_b64 a[144:145], v0 offset:1728\n\tds_read_b64_tr_b16 v[212:213], v4 offset:19008\n\tv_mfma_f32_16x16x32_bf16 v[148:151], a[44:47], v[104:107], v[148:151]\n\tds_read_b64 a[146:147], v0 offset:1736\n\t" \
    "ds_read_b64_tr_b16 v[214:215], v4 offset:19016\n\tv_mfma_f32_16x16x32_bf16 v[148:151], a[48:51], v[100:103], v[148:151]\n\tds_read_b64 a[148:149], v0 offset:1216\n\tds_read_b64_tr_b16 v[216:217], v4 offset:19072\n\tv_mfma_f32_16x16x32_bf16 v[144:147], a[32:35], v[112:115], v[144:147]\n\tds_read_b64 a[150:151], v0 offset:1224\n\tds_read_b64_tr_b16 v[218:219], v4 offset:19080\n\tv_mfma_f32_16x16x32_bf16 v[144:147], a[36:39], v[108:111], v[144:147]\n\t" \
    "ds_read_b64 a[152:153], v0 offset:704\n\tv_mfma_f32_16x16x32_bf16 v[144:147], a[40:43], v[104:107], v[144:147]\n\tds_read_b64 a[154:155], v0 offset:712\n\tv_mfma_f32_16x16x32_bf16 v[144:147], a[44:47], v[100:103], v[144:147]\n\tds_read_b64 a[156:157], v0 offset:192\n\tv_mfma_f32_16x16x32_bf16 v[140:143], a[32:35], v[108:111], v[140:143]\n\tds_read_b64 a[158:159], v0 offset:200\n\tv_mfma_f32_16x16x32_bf16 v[140:143], a[36:39], v[104:107], v[140:143]\n\t" \
    "v_mfma_f32_16x16x32_bf16 v[140:143], a[40:43], v[100:103], v[140:143]\n\ts_waitcnt lgkmcnt(15)\n\tbuffer_store_dwordx4 v[204:207], v5, s[8:11], s56 offen sc1\n\ts_add_u32 s56, s56, 49152\n\tv_mfma_f32_16x16x32_bf16 v[136:139], a[32:35], v[104:107], v[136:139]\n\tv_mfma_f32_16x16x32_bf16 v[136:139], a[36:39], v[100:103], v[136:139]\n\tv_mfma_f32_16x16x32_bf16 v[132:135], a[32:35], v[100:103], v[132:135]\n\tv_mfma_f32_16x16x32_bf16 v[132:135], a[64:67], v[68:71], v[132:135]\n\t" \
    "ds_read_b64 a[160:161], v0 offset:3712\n\tds_read_b64 a[162:163], v0 offset:3720\n\tv_mfma_f32_16x16x32_bf16 v[136:139], a[64:67], v[72:75], v[136:139]\n\tds_read_b64 a[164:165], v0 offset:3200\n\tds_read_b64 a[166:167], v0 offset:3208\n\tv_mfma_f32_16x16x32_bf16 v[136:139], a[68:71], v[68:71], v[136:139]\n\tds_read_b64 a[168:169], v0 offset:2688\n\tds_read_b64 a[170:171], v0 offset:2696\n\t" \
    "v_mfma_f32_16x16x32_bf16 v[140:143], a[64:67], v[76:79], v[140:143]\n\tds_read_b64 a[172:173], v0 offset:2176\n\tds_read_b64 a[174:175], v0 offset:2184\n\tv_mfma_f32_16x16x32_bf16 v[140:143], a[68:71], v[72:75], v[140:143]\n\tds_read_b64 a[176:177], v0 offset:1664\n\tds_read_b64 a[178:179], v0 offset:1672\n\tv_mfma_f32_16x16x32_bf16 v[140:143], a[72:75], v[68:71], v[140:143]\n\tds_read_b64 a[180:181], v0 offset:1152\n\t" \
    "v_mfma_f32_16x16x32_bf16 v[144:147], a[64:67], v[80:83], v[144:147]\n\tds_read_b64 a[182:183], v0 offset:1160\n\tv_mfma_f32_16x16x32_bf16 v[144:147], a[68:71], v[76:79], v[144:147]\n\tds_read_b64 a[184:185], v0 offset:640\n\tv_mfma_f32_16x16x32_bf16 v[144:147], a[72:75], v[72:75], v[144:147]\n\tds_read_b64 a[186:187], v0 offset:648\n\tv_mfma_f32_16x16x32_bf16 v[144:147], a[76:79], v[68:71], v[144:147]\n\tds_read_b64 a[188:189], v0 offset:128\n\t" \
    "v_mfma_f32_16x16x32_bf16 v[148:151], a[64:67], v[84:87], v[148:151]\n\tds_read_b64 a[190:191], v0 offset:136\n\tv_mfma_f32_16x16x32_bf16 v[148:151], a[68:71], v[80:83], v[148:151]\n\tds_read_b128 v[100:103], v1 offset:37248\n\tv_mfma_f32_16x16x32_bf16 v[148:151], a[72:75], v[76:79], v[148:151]\n\tds_read_b128 v[104:107], v1 offset:37760\n\tv_mfma_f32_16x16x32_bf16 v[148:151], a[76:79], v[72:75], v[148:151]\n\tds_read_b128 v[108:111], v1 offset:38272\n\t" \
    "v_mfma_f32_16x16x32_bf16 v[148:151], a[80:83], v[68:71], v[148:151]\n\tds_read_b128 v[112:115], v1 offset:38784\n\ts_waitcnt lgkmcnt(15)\n\tbuffer_store_dwordx4 v[208:211], v5, s[8:11], s56 offen sc1\n\tv_mfma_f32_16x16x32_bf16 v[152:155], a[64:67], v[88:91], v[152:155]\n\tds_read_b128 v[116:119], v1 offset:39296\n\ts_add_u32 s56, s56, 49152\n\tv_mfma_f32_16x16x32_bf16 v[152:155], a[68:71], v[84:87], v[152:155]\n\t" \
    "buffer_load_dword v15, v7, s[16:19], s42 offen\n\tds_read_b128 v[120:123], v1 offset:39808\n\tv_mfma_f32_16x16x32_bf16 v[152:155], a[72:75], v[80:83], v[152:155]\n\ts_add_u32 s43, s43, 1\n\tds_read_b128 v[124:127], v1 offset:40320\n\tv_mfma_f32_16x16x32_bf16 v[152:155], a[76:79], v[76:79], v[152:155]\n\tv_readlane_b32 s42, v237, s43\n\ts_nop 4\n\t" \
    "ds_read_b128 v[128:131], v1 offset:40832\n\tv_mfma_f32_16x16x32_bf16 v[152:155], a[80:83], v[72:75], v[152:155]\n\tds_read_b64 a[192:193], v0 offset:3648\n\tv_mfma_f32_16x16x32_bf16 v[152:155], a[84:87], v[68:71], v[152:155]\n\tds_read_b64 a[194:195], v0 offset:3656\n\tv_mfma_f32_16x16x32_bf16 v[156:159], a[64:67], v[92:95], v[156:159]\n\tds_read_b64 a[196:197], v0 offset:3136\n\tv_mfma_f32_16x16x32_bf16 v[156:159], a[68:71], v[88:91], v[156:159]\n\t" \
    "ds_read_b64 a[198:199], v0 offset:3144\n\tv_mfma_f32_16x16x32_bf16 v[156:159], a[72:75], v[84:87], v[156:159]\n\tds_read_b64 a[200:201], v0 offset:2624\n\tv_mfma_f32_16x16x32_bf16 v[156:159], a[76:79], v[80:83], v[156:159]\n\tds_read_b64 a[202:203], v0 offset:2632\n\tv_mfma_f32_16x16x32_bf16 v[156:159], a[80:83], v[76:79], v[156:159]\n\tds_read_b64 a[204:205], v0 offset:2112\n\tv_mfma_f32_16x16x32_bf16 v[156:159], a[84:87], v[72:75], v[156:159]\n\t" \
    "ds_read_b64 a[206:207], v0 offset:2120\n\tv_mfma_f32_16x16x32_bf16 v[156:159], a[88:91], v[68:71], v[156:159]\n\tds_read_b64 a[208:209], v0 offset:1600\n\tv_mfma_f32_16x16x32_bf16 v[160:163], a[64:67], v[96:99], v[160:163]\n\tds_read_b64 a[210:211], v0 offset:1608\n\tv_mfma_f32_16x16x32_bf16 v[160:163], a[68:71], v[92:95], v[160:163]\n\tds_read_b64 a[212:213], v0 offset:1088\n\tv_mfma_f32_16x16x32_bf16 v[160:163], a[72:75], v[88:91], v[160:163]\n\t" \
    "ds_read_b64 a[214:215], v0 offset:1096\n\tv_mfma_f32_16x16x32_bf16 v[160:163], a[76:79], v[84:87], v[160:163]\n\tbuffer_store_dwordx4 v[212:215], v5, s[8:11], s56 offen sc1\n\tv_mfma_f32_16x16x32_bf16 v[160:163], a[80:83], v[80:83], v[160:163]\n\ts_add_u32 s56, s56, 49152\n\tv_mfma_f32_16x16x32_bf16 v[160:163], a[84:87], v[76:79], v[160:163]\n\tv_mfma_f32_16x16x32_bf16 v[160:163], a[88:91], v[72:75], v[160:163]\n\tv_mfma_f32_16x16x32_bf16 v[160:163], a[92:95], v[68:71], v[160:163]\n\t" \
    "s_waitcnt lgkmcnt(12)\n\tv_mfma_f32_16x16x32_bf16 v[160:163], a[96:99], v[128:131], v[160:163]\n\tds_read_b64 a[216:217], v0 offset:576\n\tv_mfma_f32_16x16x32_bf16 v[160:163], a[100:103], v[124:127], v[160:163]\n\tds_read_b64 a[218:219], v0 offset:584\n\tv_mfma_f32_16x16x32_bf16 v[160:163], a[104:107], v[120:123], v[160:163]\n\tds_read_b64 a[220:221], v0 offset:64\n\tv_mfma_f32_16x16x32_bf16 v[160:163], a[108:111], v[116:119], v[160:163]\n\t" \
    "ds_read_b64 a[222:223], v0 offset:72\n\tv_mfma_f32_16x16x32_bf16 v[160:163], a[112:115], v[112:115], v[160:163]\n\tds_read_b64 a[224:225], v0 offset:3584\n\tv_mfma_f32_16x16x32_bf16 v[160:163], a[116:119], v[108:111], v[160:163]\n\tds_read_b64 a[226:227], v0 offset:3592\n\tv_mfma_f32_16x16x32_bf16 v[160:163], a[120:123], v[104:107], v[160:163]\n\tds_read_b64 a[228:229], v0 offset:3072\n\tv_mfma_f32_16x16x32_bf16 v[160:163], a[124:127], v[100:103], v[160:163]\n\t" \
    "ds_read_b64 a[230:231], v0 offset:3080\n\tv_mfma_f32_16x16x32_bf16 v[156:159], a[96:99], v[124:127], v[156:159]\n\tds_read_b64 a[232:233], v0 offset:2560\n\tbuffer_store_dwordx4 v[216:219], v5, s[8:11], s56 offen sc1\n\tv_mfma_f32_16x16x32_bf16 v[156:159], a[100:103], v[120:123], v[156:159]\n\tds_read_b64 a[234:235], v0 offset:2568\n\ts_add_u32 s56, s56, 49152\n\tv_mfma_f32_16x16x32_bf16 v[156:159], a[104:107], v[116:119], v[156:159]\n\t" \
    "ds_read_b64 a[236:237], v0 offset:2048\n\tv_mfma_f32_16x16x32_bf16 v[156:159], a[108:111], v[112:115], v[156:159]\n\tds_read_b64 a[238:239], v0 offset:2056\n\tv_mfma_f32_16x16x32_bf16 v[156:159], a[112:115], v[108:111], v[156:159]\n\tds_read_b64 a[240:241], v0 offset:1536\n\tv_mfma_f32_16x16x32_bf16 v[156:159], a[116:119], v[104:107], v[156:159]\n\tds_read_b64 a[242:243], v0 offset:1544\n\tv_mfma_f32_16x16x32_bf16 v[156:159], a[120:123], v[100:103], v[156:159]\n\t" \
    "ds_read_b64 a[244:245], v0 offset:1024\n\tv_mfma_f32_16x16x32_bf16 v[152:155], a[96:99], v[120:123], v[152:155]\n\tds_read_b64 a[246:247], v0 offset:1032\n\tv_mfma_f32_16x16x32_bf16 v[152:155], a[100:103], v[116:119], v[152:155]\n\tds_read_b128 v[68:71], v1 offset:37184\n\tv_mfma_f32_16x16x32_bf16 v[152:155], a[104:107], v[112:115], v[152:155]\n\tds_read_b128 v[72:75], v1 offset:37696\n\tv_mfma_f32_16x16x32_bf16 v[152:155], a[108:111], v[108:111], v[152:155]\n\t" \
    "ds_read_b128 v[76:79], v1 offset:38208\n\tv_mfma_f32_16x16x32_bf16 v[152:155], a[112:115], v[104:107], v[152:155]\n\tds_read_b128 v[80:83], v1 offset:38720\n\tv_mfma_f32_16x16x32_bf16 v[152:155], a[116:119], v[100:103], v[152:155]\n\tds_read_b128 v[84:87], v1 offset:39232\n\tv_mfma_f32_16x16x32_bf16 v[148:151], a[96:99], v[116:119], v[148:151]\n\tds_read_b128 v[88:91], v1 offset:39744\n\tv_mfma_f32_16x16x32_bf16 v[148:151], a[100:103], v[112:115], v[148:151]\n\t" \
    "ds_read_b128 v[92:95], v1 offset:40256\n\tv_mfma_f32_16x16x32_bf16 v[148:151], a[104:107], v[108:111], v[148:151]\n\tds_read_b128 v[96:99], v1 offset:40768\n\tv_mfma_f32_16x16x32_bf16 v[148:151], a[108:111], v[104:107], v[148:151]\n\tds_read_b64 a[248:249], v0 offset:512\n\tv_mfma_f32_16x16x32_bf16 v[148:151], a[112:115], v[100:103], v[148:151]\n\tds_read_b64 a[250:251], v0 offset:520\n\tv_mfma_f32_16x16x32_bf16 v[144:147], a[96:99], v[112:115], v[144:147]\n\t" \
    "ds_read_b64 a[252:253], v0 offset:0\n\tv_mfma_f32_16x16x32_bf16 v[144:147], a[100:103], v[108:111], v[144:147]\n\tds_read_b64 a[254:255], v0 offset:8\n\tv_mfma_f32_16x16x32_bf16 v[144:147], a[104:107], v[104:107], v[144:147]\n\tv_mfma_f32_16x16x32_bf16 v[144:147], a[108:111], v[100:103], v[144:147]\n\tv_mfma_f32_16x16x32_bf16 v[140:143], a[96:99], v[108:111], v[140:143]\n\tv_mfma_f32_16x16x32_bf16 v[140:143], a[100:103], v[104:107], v[140:143]\n\tv_mfma_f32_16x16x32_bf16 v[140:143], a[104:107], v[100:103], v[140:143]\n\t" \
    "v_mfma_f32_16x16x32_bf16 v[136:139], a[96:99], v[104:107], v[136:139]\n\tv_mfma_f32_16x16x32_bf16 v[136:139], a[100:103], v[100:103], v[136:139]\n\tv_mfma_f32_16x16x32_bf16 v[132:135], a[96:99], v[100:103], v[132:135]\n\ts_waitcnt lgkmcnt(4)\n\tv_mfma_f32_16x16x32_bf16 v[132:135], a[128:131], v[68:71], v[132:135]\n\tds_read_b128 v[100:103], v1 offset:37120\n\ts_waitcnt vmcnt(8)\n\tv_alignbit_b32 v196, v45, v45, 16\n\t" \
    "v_mfma_f32_16x16x32_bf16 v[136:139], a[128:131], v[72:75], v[136:139]\n\tds_read_b128 v[104:107], v1 offset:37632\n\tv_alignbit_b32 v197, v44, v44, 16\n\tv_mfma_f32_16x16x32_bf16 v[136:139], a[132:135], v[68:71], v[136:139]\n\tds_read_b128 v[108:111], v1 offset:38144\n\tv_alignbit_b32 v198, v43, v43, 16\n\tv_mfma_f32_16x16x32_bf16 v[140:143], a[128:131], v[76:79], v[140:143]\n\tds_read_b128 v[112:115], v1 offset:38656\n\t" \
    "v_alignbit_b32 v199, v42, v42, 16\n\tv_mfma_f32_16x16x32_bf16 v[140:143], a[132:135], v[72:75], v[140:143]\n\tds_write_b128 v2, v[196:199] offset:3072\n\tv_mfma_f32_16x16x32_bf16 v[140:143], a[136:139], v[68:71], v[140:143]\n\tds_read_b128 v[116:119], v1 offset:39168\n\tv_bfi_b32 v200, s46, v45, v44\n\tv_mfma_f32_16x16x32_bf16 v[144:147], a[128:131], v[80:83], v[144:147]\n\tds_read_b128 v[120:123], v1 offset:39680\n\t" \
    "v_bfi_b32 v201, s46, v44, v43\n\tv_mfma_f32_16x16x32_bf16 v[144:147], a[132:135], v[76:79], v[144:147]\n\tds_read_b128 v[124:127], v1 offset:40192\n\tv_bfi_b32 v202, s46, v43, v42\n\tv_mfma_f32_16x16x32_bf16 v[144:147], a[136:139], v[72:75], v[144:147]\n\tds_read_b128 v[128:131], v1 offset:40704\n\tv_bfi_b32 v203, s46, v42, v59\n\tv_mfma_f32_16x16x32_bf16 v[144:147], a[140:143], v[68:71], v[144:147]\n\t" \
    "ds_write_b128 v2, v[200:203] offset:7232\n\tv_mfma_f32_16x16x32_bf16 v[148:151], a[128:131], v[84:87], v[148:151]\n\tv_alignbit_b32 v196, v44, v44, 16\n\tv_alignbit_b32 v197, v43, v43, 16\n\tv_mfma_f32_16x16x32_bf16 v[148:151], a[132:135], v[80:83], v[148:151]\n\tv_alignbit_b32 v198, v42, v42, 16\n\tv_alignbit_b32 v199, v59, v59, 16\n\tv_mfma_f32_16x16x32_bf16 v[148:151], a[136:139], v[76:79], v[148:151]\n\t" \
    "ds_write_b128 v2, v[196:199] offset:11392\n\tv_mfma_f32_16x16x32_bf16 v[148:151], a[140:143], v[72:75], v[148:151]\n\tv_bfi_b32 v200, s46, v44, v43\n\tv_bfi_b32 v201, s46, v43, v42\n\tv_mfma_f32_16x16x32_bf16 v[148:151], a[144:147], v[68:71], v[148:151]\n\tv_bfi_b32 v202, s46, v42, v59\n\tv_bfi_b32 v203, s46, v59, v58\n\tv_mfma_f32_16x16x32_bf16 v[152:155], a[128:131], v[88:91], v[152:155]\n\t" \
    "ds_write_b128 v2, v[200:203] offset:15552\n\tv_mfma_f32_16x16x32_bf16 v[152:155], a[132:135], v[84:87], v[152:155]\n\tv_alignbit_b32 v196, v49, v49, 16\n\tv_alignbit_b32 v197, v48, v48, 16\n\tv_mfma_f32_16x16x32_bf16 v[152:155], a[136:139], v[80:83], v[152:155]\n\tv_alignbit_b32 v198, v47, v47, 16\n\tv_alignbit_b32 v199, v46, v46, 16\n\tv_mfma_f32_16x16x32_bf16 v[152:155], a[140:143], v[76:79], v[152:155]\n\t" \
    "buffer_load_dword v15, v7, s[16:19], s42 offen\n\tv_mfma_f32_16x16x32_bf16 v[152:155], a[144:147], v[72:75], v[152:155]\n\ts_add_u32 s43, s43, 1\n\tds_write_b128 v2, v[196:199] offset:2048\n\tv_mfma_f32_16x16x32_bf16 v[152:155], a[148:151], v[68:71], v[152:155]\n\tv_readlane_b32 s42, v237, s43\n\ts_nop 4\n\tv_bfi_b32 v200, s46, v49, v48\n\t" \
    "v_bfi_b32 v201, s46, v48, v47\n\tv_mfma_f32_16x16x32_bf16 v[156:159], a[128:131], v[92:95], v[156:159]\n\tv_bfi_b32 v202, s46, v47, v46\n\tv_bfi_b32 v203, s46, v46, v61\n\tv_mfma_f32_16x16x32_bf16 v[156:159], a[132:135], v[88:91], v[156:159]\n\tds_write_b128 v2, v[200:203] offset:6208\n\tv_mfma_f32_16x16x32_bf16 v[156:159], a[136:139], v[84:87], v[156:159]\n\tv_alignbit_b32 v196, v48, v48, 16\n\t" \
    "v_alignbit_b32 v197, v47, v47, 16\n\tv_mfma_f32_16x16x32_bf16 v[156:159], a[140:143], v[80:83], v[156:159]\n\tv_alignbit_b32 v198, v46, v46, 16\n\tv_alignbit_b32 v199, v61, v61, 16\n\tv_mfma_f32_16x16x32_bf16 v[156:159], a[144:147], v[76:79], v[156:159]\n\tds_write_b128 v2, v[196:199] offset:10368\n\tv_mfma_f32_16x16x32_bf16 v[156:159], a[148:151], v[72:75], v[156:159]\n\tv_bfi_b32 v200, s46, v48, v47\n\t" \
    "v_bfi_b32 v201, s46, v47, v46\n\tv_mfma_f32_16x16x32_bf16 v[156:159], a[152:155], v[68:71], v[156:159]\n\tv_bfi_b32 v202, s46, v46, v61\n\tv_bfi_b32 v203, s46, v61, v60\n\tv_mfma_f32_16x16x32_bf16 v[160:163], a[128:131], v[96:99], v[160:163]\n\tds_write_b128 v2, v[200:203] offset:14528\n\tv_mfma_f32_16x16x32_bf16 v[160:163], a[132:135], v[92:95], v[160:163]\n\tv_alignbit_b32 v196, v53, v53, 16\n\t" \
    "v_alignbit_b32 v197, v52, v52, 16\n\tv_mfma_f32_16x16x32_bf16 v[160:163], a[136:139], v[88:91], v[160:163]\n\tv_alignbit_b32 v198, v51, v51, 16\n\tv_alignbit_b32 v199, v50, v50, 16\n\tv_mfma_f32_16x16x32_bf16 v[160:163], a[140:143], v[84:87], v[160:163]\n\tds_write_b128 v2, v[196:199] offset:1024\n\tv_mfma_f32_16x16x32_bf16 v[160:163], a[144:147], v[80:83], v[160:163]\n\tv_bfi_b32 v200, s46, v53, v52\n\t" \
    "v_bfi_b32 v201, s46, v52, v51\n\tv_mfma_f32_16x16x32_bf16 v[160:163], a[148:151], v[76:79], v[160:163]\n\tv_bfi_b32 v202, s46, v51, v50\n\tv_bfi_b32 v203, s46, v50, v63\n\tv_mfma_f32_16x16x32_bf16 v[160:163], a[152:155], v[72:75], v[160:163]\n\tds_write_b128 v2, v[200:203] offset:5184\n\tv_mfma_f32_16x16x32_bf16 v[160:163], a[156:159], v[68:71], v[160:163]\n\tv_alignbit_b32 v196, v52, v52, 16\n\t" \
    "v_alignbit_b32 v197, v51, v51, 16\n\ts_waitcnt lgkmcnt(9)\n\tv_mfma_f32_16x16x32_bf16 v[160:163], a[160:163], v[128:131], v[160:163]\n\tds_read_b128 v[68:71], v1 offset:37056\n\tv_alignbit_b32 v198, v50, v50, 16\n\tv_mfma_f32_16x16x32_bf16 v[160:163], a[164:167], v[124:127], v[160:163]\n\tds_read_b128 v[72:75], v1 offset:37568\n\tv_alignbit_b32 v199, v63, v63, 16\n\t" \
    "v_mfma_f32_16x16x32_bf16 v[160:163], a[168:171], v[120:123], v[160:163]\n\tds_write_b128 v2, v[196:199] offset:9344\n\tv_mfma_f32_16x16x32_bf16 v[160:163], a[172:175], v[116:119], v[160:163]\n\tds_read_b128 v[76:79], v1 offset:38080\n\tv_bfi_b32 v200, s46, v52, v51\n\tv_mfma_f32_16x16x32_bf16 v[160:163], a[176:179], v[112:115], v[160:163]\n\tds_read_b128 v[80:83], v1 offset:38592\n\tv_bfi_b32 v201, s46, v51, v50\n\t" \
    "v_mfma_f32_16x16x32_bf16 v[160:163], a[180:183], v[108:111], v[160:163]\n\tds_read_b128 v[84:87], v1 offset:39104\n\tv_bfi_b32 v202, s46, v50, v63\n\tv_mfma_f32_16x16x32_bf16 v[160:163], a[184:187], v[104:107], v[160:163]\n\tds_read_b128 v[88:91], v1 offset:39616\n\tv_bfi_b32 v203, s46, v63, v62\n\tv_mfma_f32_16x16x32_bf16 v[160:163], a[188:191], v[100:103], v[160:163]\n\tds_write_b128 v2, v[200:203] offset:13504\n\t" \
    "v_mfma_f32_16x16x32_bf16 v[156:159], a[160:163], v[124:127], v[156:159]\n\tds_read_b128 v[92:95], v1 offset:40128\n\tv_alignbit_b32 v196, v57, v57, 16\n\tv_mfma_f32_16x16x32_bf16 v[156:159], a[164:167], v[120:123], v[156:159]\n\tds_read_b128 v[96:99], v1 offset:40640\n\tv_alignbit_b32 v197, v56, v56, 16\n\tv_mfma_f32_16x16x32_bf16 v[156:159], a[168:171], v[116:119], v[156:159]\n\tv_alignbit_b32 v198, v55, v55, 16\n\t" \
    "v_alignbit_b32 v199, v54, v54, 16\n\tv_mfma_f32_16x16x32_bf16 v[156:159], a[172:175], v[112:115], v[156:159]\n\tds_write_b128 v2, v[196:199] offset:0\n\tv_mfma_f32_16x16x32_bf16 v[156:159], a[176:179], v[108:111], v[156:159]\n\tv_bfi_b32 v200, s46, v57, v56\n\tv_bfi_b32 v201, s46, v56, v55\n\tv_mfma_f32_16x16x32_bf16 v[156:159], a[180:183], v[104:107], v[156:159]\n\tv_bfi_b32 v202, s46, v55, v54\n\t" \
    "v_bfi_b32 v203, s46, v54, v65\n\tv_mfma_f32_16x16x32_bf16 v[156:159], a[184:187], v[100:103], v[156:159]\n\tds_write_b128 v2, v[200:203] offset:4160\n\tv_mfma_f32_16x16x32_bf16 v[152:155], a[160:163], v[120:123], v[152:155]\n\tv_alignbit_b32 v196, v56, v56, 16\n\tv_alignbit_b32 v197, v55, v55, 16\n\tv_mfma_f32_16x16x32_bf16 v[152:155], a[164:167], v[116:119], v[152:155]\n\tv_alignbit_b32 v198, v54, v54, 16\n\t" \
    "v_alignbit_b32 v199, v65, v65, 16\n\tv_mfma_f32_16x16x32_bf16 v[152:155], a[168:171], v[112:115], v[152:155]\n\tds_write_b128 v2, v[196:199] offset:8320\n\tv_mfma_f32_16x16x32_bf16 v[152:155], a[172:175], v[108:111], v[152:155]\n\tv_bfi_b32 v200, s46, v56, v55\n\tv_bfi_b32 v201, s46, v55, v54\n\tv_mfma_f32_16x16x32_bf16 v[152:155], a[176:179], v[104:107], v[152:155]\n\tv_bfi_b32 v202, s46, v54, v65\n\t" \
    "v_bfi_b32 v203, s46, v65, v64\n\tv_mfma_f32_16x16x32_bf16 v[152:155], a[180:183], v[100:103], v[152:155]\n\tds_write_b128 v2, v[200:203] offset:12480\n\tv_mfma_f32_16x16x32_bf16 v[148:151], a[160:163], v[116:119], v[148:151]\n\tv_lshl_or_b32 v66, v66, 16, v66\n\tv_and_b32 v228, v66, v232\n\tv_mfma_f32_16x16x32_bf16 v[148:151], a[164:167], v[112:115], v[148:151]\n\tv_and_b32 v229, v66, v233\n\t" \
    "v_and_b32 v230, v66, v234\n\tv_mfma_f32_16x16x32_bf16 v[148:151], a[168:171], v[108:111], v[148:151]\n\tv_and_b32 v231, v66, v235\n\tv_mfma_f32_16x16x32_bf16 v[148:151], a[172:175], v[104:107], v[148:151]\n\tv_mfma_f32_16x16x32_bf16 v[148:151], a[176:179], v[100:103], v[148:151]\n\tv_mfma_f32_16x16x32_bf16 v[144:147], a[160:163], v[112:115], v[144:147]\n\tv_mfma_f32_16x16x32_bf16 v[144:147], a[164:167], v[108:111], v[144:147]\n\tv_mfma_f32_16x16x32_bf16 v[144:147], a[168:171], v[104:107], v[144:147]\n\t" \
    "v_mfma_f32_16x16x32_bf16 v[144:147], a[172:175], v[100:103], v[144:147]\n\tv_mfma_f32_16x16x32_bf16 v[140:143], a[160:163], v[108:111], v[140:143]\n\tv_mfma_f32_16x16x32_bf16 v[140:143], a[164:167], v[104:107], v[140:143]\n\tv_mfma_f32_16x16x32_bf16 v[140:143], a[168:171], v[100:103], v[140:143]\n\tv_mfma_f32_16x16x32_bf16 v[136:139], a[160:163], v[104:107], v[136:139]\n\tv_mfma_f32_16x16x32_bf16 v[136:139], a[164:167], v[100:103], v[136:139]\n\tv_mfma_f32_16x16x32_bf16 v[132:135], a[160:163], v[100:103], v[132:135]\n\ts_waitcnt lgkmcnt(4)\n\t" \
    "v_mfma_f32_16x16x32_bf16 v[132:135], a[192:195], v[68:71], v[132:135]\n\tds_read_b128 v[100:103], v1 offset:36992\n\tv_mfma_f32_16x16x32_bf16 v[136:139], a[192:195], v[72:75], v[136:139]\n\tds_read_b128 v[104:107], v1 offset:37504\n\tv_mfma_f32_16x16x32_bf16 v[136:139], a[196:199], v[68:71], v[136:139]\n\tds_read_b128 v[108:111], v1 offset:38016\n\tv_mfma_f32_16x16x32_bf16 v[140:143], a[192:195], v[76:79], v[140:143]\n\tds_read_b128 v[112:115], v1 offset:38528\n\t" \
    "v_mfma_f32_16x16x32_bf16 v[140:143], a[196:199], v[72:75], v[140:143]\n\tds_read_b128 v[116:119], v1 offset:39040\n\tv_mfma_f32_16x16x32_bf16 v[140:143], a[200:203], v[68:71], v[140:143]\n\tds_read_b128 v[120:123], v1 offset:39552\n\tv_mfma_f32_16x16x32_bf16 v[144:147], a[192:195], v[80:83], v[144:147]\n\tds_read_b128 v[124:127], v1 offset:40064\n\tv_mfma_f32_16x16x32_bf16 v[144:147], a[196:199], v[76:79], v[144:147]\n\tds_read_b128 v[128:131], v1 offset:40576\n\t" \
    "v_mfma_f32_16x16x32_bf16 v[144:147], a[200:203], v[72:75], v[144:147]\n\tv_mfma_f32_16x16x32_bf16 v[144:147], a[204:207], v[68:71], v[144:147]\n\tv_mfma_f32_16x16x32_bf16 v[148:151], a[192:195], v[84:87], v[148:151]\n\tv_mfma_f32_16x16x32_bf16 v[148:151], a[196:199], v[80:83], v[148:151]\n\tv_mfma_f32_16x16x32_bf16 v[148:151], a[200:203], v[76:79], v[148:151]\n\tv_mfma_f32_16x16x32_bf16 v[148:151], a[204:207], v[72:75], v[148:151]\n\tv_mfma_f32_16x16x32_bf16 v[148:151], a[208:211], v[68:71], v[148:151]\n\tv_mfma_f32_16x16x32_bf16 v[152:155], a[192:195], v[88:91], v[152:155]\n\t" \
    "v_mfma_f32_16x16x32_bf16 v[152:155], a[196:199], v[84:87], v[152:155]\n\tv_mfma_f32_16x16x32_bf16 v[152:155], a[200:203], v[80:83], v[152:155]\n\tv_mfma_f32_16x16x32_bf16 v[152:155], a[204:207], v[76:79], v[152:155]\n\tv_mfma_f32_16x16x32_bf16 v[152:155], a[208:211], v[72:75], v[152:155]\n\tv_mfma_f32_16x16x32_bf16 v[152:155], a[212:215], v[68:71], v[152:155]\n\tbuffer_load_dword v15, v7, s[16:19], s42 offen\n\tv_mfma_f32_16x16x32_bf16 v[156:159], a[192:195], v[92:95], v[156:159]\n\ts_add_u32 s43, s43, 1\n\t" \
    "v_mfma_f32_16x16x32_bf16 v[156:159], a[196:199], v[88:91], v[156:159]\n\tv_readlane_b32 s42, v237, s43\n\ts_nop 4\n\tv_mfma_f32_16x16x32_bf16 v[156:159], a[200:203], v[84:87], v[156:159]\n\tv_mfma_f32_16x16x32_bf16 v[156:159], a[204:207], v[80:83], v[156:159]\n\tv_mfma_f32_16x16x32_bf16 v[156:159], a[208:211], v[76:79], v[156:159]\n\tv_mfma_f32_16x16x32_bf16 v[156:159], a[212:215], v[72:75], v[156:159]\n\tv_mfma_f32_16x16x32_bf16 v[156:159], a[216:219], v[68:71], v[156:159]\n\t" \
    "v_mfma_f32_16x16x32_bf16 v[160:163], a[192:195], v[96:99], v[160:163]\n\tv_mfma_f32_16x16x32_bf16 v[160:163], a[196:199], v[92:95], v[160:163]\n\tv_mfma_f32_16x16x32_bf16 v[160:163], a[200:203], v[88:91], v[160:163]\n\tv_mfma_f32_16x16x32_bf16 v[160:163], a[204:207], v[84:87], v[160:163]\n\tv_mfma_f32_16x16x32_bf16 v[160:163], a[208:211], v[80:83], v[160:163]\n\tv_mfma_f32_16x16x32_bf16 v[160:163], a[212:215], v[76:79], v[160:163]\n\tv_mfma_f32_16x16x32_bf16 v[160:163], a[216:219], v[72:75], v[160:163]\n\tv_mfma_f32_16x16x32_bf16 v[160:163], a[220:223], v[68:71], v[160:163]\n\t" \
    "s_waitcnt lgkmcnt(0)\n\tv_mfma_f32_16x16x32_bf16 v[160:163], a[224:227], v[128:131], v[160:163]\n\ts_waitcnt vmcnt(2)\n\tbuffer_store_dwordx2 v[248:249], off, s[8:11], s51 sc1\n\tds_read_b64 a[0:1], v0 offset:4032\n\tv_mfma_f32_16x16x32_bf16 v[160:163], a[228:231], v[124:127], v[160:163]\n\tds_read_b64 a[2:3], v0 offset:4040\n\tv_mfma_f32_16x16x32_bf16 v[160:163], a[232:235], v[120:123], v[160:163]\n\t" \
    "ds_read_b64 a[4:5], v0 offset:3520\n\tv_mfma_f32_16x16x32_bf16 v[160:163], a[236:239], v[116:119], v[160:163]\n\tds_read_b64 a[6:7], v0 offset:3528\n\tv_mfma_f32_16x16x32_bf16 v[160:163], a[240:243], v[112:115], v[160:163]\n\tds_read_b64 a[8:9], v0 offset:3008\n\tv_mfma_f32_16x16x32_bf16 v[160:163], a[244:247], v[108:111], v[160:163]\n\tds_read_b64 a[10:11], v0 offset:3016\n\tv_mfma_f32_16x16x32_bf16 v[160:163], a[248:251], v[104:107], v[160:163]\n\t" \
    "ds_read_b64 a[12:13], v0 offset:2496\n\tv_mfma_f32_16x16x32_bf16 v[160:163], a[252:255], v[100:103], v[160:163]\n\tds_read_b64 a[14:15], v0 offset:2504\n\tv_mfma_f32_16x16x32_bf16 v[156:159], a[224:227], v[124:127], v[156:159]\n\tds_read_b64 a[16:17], v0 offset:1984\n\tv_mfma_f32_16x16x32_bf16 v[156:159], a[228:231], v[120:123], v[156:159]\n\tds_read_b64 a[18:19], v0 offset:1992\n\tv_mfma_f32_16x16x32_bf16 v[156:159], a[232:235], v[116:119], v[156:159]\n\t" \
    "ds_read_b64 a[20:21], v0 offset:1472\n\tv_mfma_f32_16x16x32_bf16 v[156:159], a[236:239], v[112:115], v[156:159]\n\tds_read_b64 a[22:23], v0 offset:1480\n\tv_mfma_f32_16x16x32_bf16 v[156:159], a[240:243], v[108:111], v[156:159]\n\tds_read_b64 a[24:25], v0 offset:960\n\tv_mfma_f32_16x16x32_bf16 v[156:159], a[244:247], v[104:107], v[156:159]\n\tds_read_b64 a[26:27], v0 offset:968\n\tv_mfma_f32_16x16x32_bf16 v[156:159], a[248:251], v[100:103], v[156:159]\n\t" \
    "ds_read_b64 a[28:29], v0 offset:448\n\tv_mfma_f32_16x16x32_bf16 v[152:155], a[224:227], v[120:123], v[152:155]\n\tds_read_b64 a[30:31], v0 offset:456\n\tv_mfma_f32_16x16x32_bf16 v[152:155], a[228:231], v[116:119], v[152:155]\n\tds_read_b128 v[68:71], v1 offset:55936\n\tv_mfma_f32_16x16x32_bf16 v[152:155], a[232:235], v[112:115], v[152:155]\n\tds_read_b128 v[72:75], v1 offset:56448\n\tv_mfma_f32_16x16x32_bf16 v[152:155], a[236:239], v[108:111], v[152:155]\n\t" \
    "ds_read_b128 v[76:79], v1 offset:56960\n\tv_mfma_f32_16x16x32_bf16 v[152:155], a[240:243], v[104:107], v[152:155]\n\tds_read_b128 v[80:83], v1 offset:57472\n\tv_mfma_f32_16x16x32_bf16 v[152:155], a[244:247], v[100:103], v[152:155]\n\tds_read_b128 v[84:87], v1 offset:57984\n\tv_mfma_f32_16x16x32_bf16 v[148:151], a[224:227], v[116:119], v[148:151]\n\tds_read_b128 v[88:91], v1 offset:58496\n\tv_mfma_f32_16x16x32_bf16 v[148:151], a[228:231], v[112:115], v[148:151]\n\t" \
    "ds_read_b128 v[92:95], v1 offset:59008\n\tv_mfma_f32_16x16x32_bf16 v[148:151], a[232:235], v[108:111], v[148:151]\n\tds_read_b128 v[96:99], v1 offset:59520\n\tv_mfma_f32_16x16x32_bf16 v[148:151], a[236:239], v[104:107], v[148:151]\n\tv_mfma_f32_16x16x32_bf16 v[148:151], a[240:243], v[100:103], v[148:151]\n\tv_mfma_f32_16x16x32_bf16 v[144:147], a[224:227], v[112:115], v[144:147]\n\tv_mfma_f32_16x16x32_bf16 v[144:147], a[228:231], v[108:111], v[144:147]\n\tv_mfma_f32_16x16x32_bf16 v[144:147], a[232:235], v[104:107], v[144:147]\n\t" \
    "v_mfma_f32_16x16x32_bf16 v[144:147], a[236:239], v[100:103], v[144:147]\n\tv_mfma_f32_16x16x32_bf16 v[140:143], a[224:227], v[108:111], v[140:143]\n\tv_mfma_f32_16x16x32_bf16 v[140:143], a[228:231], v[104:107], v[140:143]\n\tv_mfma_f32_16x16x32_bf16 v[140:143], a[232:235], v[100:103], v[140:143]\n\tv_mfma_f32_16x16x32_bf16 v[136:139], a[224:227], v[104:107], v[136:139]\n\tv_mfma_f32_16x16x32_bf16 v[136:139], a[228:231], v[100:103], v[136:139]\n\tv_mfma_f32_16x16x32_bf16 v[132:135], a[224:227], v[100:103], v[132:135]\n\ts_waitcnt lgkmcnt(0)\n\t" \
    "v_mfma_f32_16x16x32_bf16 v[164:167], v[228:231], v[68:71], 0\n\tds_read_b64 a[32:33], v0 offset:3968\n\tds_read_b64 a[34:35], v0 offset:3976\n\tv_mfma_f32_16x16x32_bf16 v[164:167], a[0:3], v[68:71], v[164:167]\n\tds_read_b64 a[36:37], v0 offset:3456\n\tds_read_b64 a[38:39], v0 offset:3464\n\tv_mfma_f32_16x16x32_bf16 v[168:171], v[228:231], v[72:75], 0\n\tds_read_b64 a[40:41], v0 offset:2944\n\t" \
    "ds_read_b64 a[42:43], v0 offset:2952\n\tv_mfma_f32_16x16x32_bf16 v[168:171], a[0:3], v[72:75], v[168:171]\n\tds_read_b64 a[44:45], v0 offset:2432\n\tds_read_b64 a[46:47], v0 offset:2440\n\tv_mfma_f32_16x16x32_bf16 v[168:171], a[4:7], v[68:71], v[168:171]\n\tds_read_b64 a[48:49], v0 offset:1920\n\tds_read_b64 a[50:51], v0 offset:1928\n\tv_mfma_f32_16x16x32_bf16 v[172:175], v[228:231], v[76:79], 0\n\t" \
    "ds_read_b64 a[52:53], v0 offset:1408\n\tv_cvt_pk_bf16_f32 v132, v132, v133\n\tv_mfma_f32_16x16x32_bf16 v[172:175], a[0:3], v[76:79], v[172:175]\n\tds_read_b64 a[54:55], v0 offset:1416\n\tv_cvt_pk_bf16_f32 v133, v134, v135\n\tv_mfma_f32_16x16x32_bf16 v[172:175], a[4:7], v[72:75], v[172:175]\n\tds_read_b64 a[56:57], v0 offset:896\n\tds_write_b64 v3, v[132:133] offset:36992\n\t" \
    "v_mfma_f32_16x16x32_bf16 v[172:175], a[8:11], v[68:71], v[172:175]\n\tds_read_b64 a[58:59], v0 offset:904\n\tv_cvt_pk_bf16_f32 v136, v136, v137\n\tv_mfma_f32_16x16x32_bf16 v[176:179], v[228:231], v[80:83], 0\n\tds_read_b64 a[60:61], v0 offset:384\n\tv_cvt_pk_bf16_f32 v137, v138, v139\n\tv_mfma_f32_16x16x32_bf16 v[176:179], a[0:3], v[80:83], v[176:179]\n\tds_read_b64 a[62:63], v0 offset:392\n\t" \
    "ds_write_b64 v3, v[136:137] offset:37504\n\tv_mfma_f32_16x16x32_bf16 v[176:179], a[4:7], v[76:79], v[176:179]\n\tds_read_b128 v[100:103], v1 offset:55872\n\tv_cvt_pk_bf16_f32 v140, v140, v141\n\tv_mfma_f32_16x16x32_bf16 v[176:179], a[8:11], v[72:75], v[176:179]\n\tds_read_b128 v[104:107], v1 offset:56384\n\tv_cvt_pk_bf16_f32 v141, v142, v143\n\tv_mfma_f32_16x16x32_bf16 v[176:179], a[12:15], v[68:71], v[176:179]\n\t" \
    "ds_read_b128 v[108:111], v1 offset:56896\n\tds_write_b64 v3, v[140:141] offset:38016\n\tv_mfma_f32_16x16x32_bf16 v[180:183], v[228:231], v[84:87], 0\n\tds_read_b128 v[112:115], v1 offset:57408\n\tv_cvt_pk_bf16_f32 v144, v144, v145\n\tv_mfma_f32_16x16x32_bf16 v[180:183], a[0:3], v[84:87], v[180:183]\n\tds_read_b128 v[116:119], v1 offset:57920\n\tv_cvt_pk_bf16_f32 v145, v146, v147\n\t" \
    "v_mfma_f32_16x16x32_bf16 v[180:183], a[4:7], v[80:83], v[180:183]\n\tds_read_b128 v[120:123], v1 offset:58432\n\tds_write_b64 v3, v[144:145] offset:38528\n\tv_mfma_f32_16x16x32_bf16 v[180:183], a[8:11], v[76:79], v[180:183]\n\tds_read_b128 v[124:127], v1 offset:58944\n\tv_cvt_pk_bf16_f32 v148, v148, v149\n\tv_mfma_f32_16x16x32_bf16 v[180:183], a[12:15], v[72:75], v[180:183]\n\tds_read_b128 v[128:131], v1 offset:59456\n\t" \
    "v_cvt_pk_bf16_f32 v149, v150, v151\n\tv_mfma_f32_16x16x32_bf16 v[180:183], a[16:19], v[68:71], v[180:183]\n\tds_read_b64 a[64:65], v0 offset:3904\n\tds_write_b64 v3, v[148:149] offset:39040\n\tv_mfma_f32_16x16x32_bf16 v[184:187], v[228:231], v[88:91], 0\n\tds_read_b64 a[66:67], v0 offset:3912\n\tv_cvt_pk_bf16_f32 v152, v152, v153\n\tv_mfma_f32_16x16x32_bf16 v[184:187], a[0:3], v[88:91], v[184:187]\n\t" \
    "ds_read_b64 a[68:69], v0 offset:3392\n\tv_cvt_pk_bf16_f32 v153, v154, v155\n\tv_mfma_f32_16x16x32_bf16 v[184:187], a[4:7], v[84:87], v[184:187]\n\tds_read_b64 a[70:71], v0 offset:3400\n\tds_write_b64 v3, v[152:153] offset:39552\n\tv_mfma_f32_16x16x32_bf16 v[184:187], a[8:11], v[80:83], v[184:187]\n\tds_read_b64 a[72:73], v0 offset:2880\n\tv_cvt_pk_bf16_f32 v156, v156, v157\n\t" \
    "v_mfma_f32_16x16x32_bf16 v[184:187], a[12:15], v[76:79], v[184:187]\n\tds_read_b64 a[74:75], v0 offset:2888\n\tv_cvt_pk_bf16_f32 v157, v158, v159\n\tv_mfma_f32_16x16x32_bf16 v[184:187], a[16:19], v[72:75], v[184:187]\n\tds_read_b64 a[76:77], v0 offset:2368\n\tds_write_b64 v3, v[156:157] offset:40064\n\tv_mfma_f32_16x16x32_bf16 v[184:187], a[20:23], v[68:71], v[184:187]\n\tds_read_b64 a[78:79], v0 offset:2376\n\t" \
    "v_cvt_pk_bf16_f32 v160, v160, v161\n\tv_mfma_f32_16x16x32_bf16 v[188:191], v[228:231], v[92:95], 0\n\tds_read_b64 a[80:81], v0 offset:1856\n\tv_cvt_pk_bf16_f32 v161, v162, v163\n\tv_mfma_f32_16x16x32_bf16 v[188:191], a[0:3], v[92:95], v[188:191]\n\tds_read_b64 a[82:83], v0 offset:1864\n\tds_write_b64 v3, v[160:161] offset:40576\n\tv_mfma_f32_16x16x32_bf16 v[188:191], a[4:7], v[88:91], v[188:191]\n\t" \
    "ds_read_b64 a[84:85], v0 offset:1344\n\tv_mfma_f32_16x16x32_bf16 v[188:191], a[8:11], v[84:87], v[188:191]\n\tds_read_b64 a[86:87], v0 offset:1352\n\tv_mfma_f32_16x16x32_bf16 v[188:191], a[12:15], v[80:83], v[188:191]\n\tds_read_b64 a[88:89], v0 offset:832\n\tv_mfma_f32_16x16x32_bf16 v[188:191], a[16:19], v[76:79], v[188:191]\n\tds_read_b64 a[90:91], v0 offset:840\n\tv_mfma_f32_16x16x32_bf16 v[188:191], a[20:23], v[72:75], v[188:191]\n\t" \
    "ds_read_b64 a[92:93], v0 offset:320\n\tv_mfma_f32_16x16x32_bf16 v[188:191], a[24:27], v[68:71], v[188:191]\n\tds_read_b64 a[94:95], v0 offset:328\n\tv_mfma_f32_16x16x32_bf16 v[192:195], v[228:231], v[96:99], 0\n\tds_read_b64 a[96:97], v0 offset:3840\n\tv_mfma_f32_16x16x32_bf16 v[192:195], a[0:3], v[96:99], v[192:195]\n\tds_read_b64 a[98:99], v0 offset:3848\n\tv_mfma_f32_16x16x32_bf16 v[192:195], a[4:7], v[92:95], v[192:195]\n\t" \
    "ds_read_b64 a[100:101], v0 offset:3328\n\tv_mfma_f32_16x16x32_bf16 v[192:195], a[8:11], v[88:91], v[192:195]\n\tds_read_b64 a[102:103], v0 offset:3336\n\tv_mfma_f32_16x16x32_bf16 v[192:195], a[12:15], v[84:87], v[192:195]\n\tv_mfma_f32_16x16x32_bf16 v[192:195], a[16:19], v[80:83], v[192:195]\n\tv_mfma_f32_16x16x32_bf16 v[192:195], a[20:23], v[76:79], v[192:195]\n\tv_mfma_f32_16x16x32_bf16 v[192:195], a[24:27], v[72:75], v[192:195]\n\tv_mfma_f32_16x16x32_bf16 v[192:195], a[28:31], v[68:71], v[192:195]\n\t" \
    "s_waitcnt lgkmcnt(10)\n\ts_barrier\n\tv_mfma_f32_16x16x32_bf16 v[192:195], a[32:35], v[128:131], v[192:195]\n\tds_read_b64 a[104:105], v0 offset:2816\n\tds_read_b64 a[106:107], v0 offset:2824\n\tv_mfma_f32_16x16x32_bf16 v[192:195], a[36:39], v[124:127], v[192:195]\n\tds_read_b64 a[108:109], v0 offset:2304\n\tds_read_b64 a[110:111], v0 offset:2312\n\t" \
    "v_mfma_f32_16x16x32_bf16 v[192:195], a[40:43], v[120:123], v[192:195]\n\tds_read_b64 a[112:113], v0 offset:1792\n\tds_read_b64 a[114:115], v0 offset:1800\n\tv_mfma_f32_16x16x32_bf16 v[192:195], a[44:47], v[116:119], v[192:195]\n\tds_read_b64 a[116:117], v0 offset:1280\n\tds_read_b64 a[118:119], v0 offset:1288\n\tv_mfma_f32_16x16x32_bf16 v[192:195], a[48:51], v[112:115], v[192:195]\n\tds_read_b64 a[120:121], v0 offset:768\n\t" \
    "ds_read_b64 a[122:123], v0 offset:776\n\tv_mfma_f32_16x16x32_bf16 v[192:195], a[52:55], v[108:111], v[192:195]\n\tds_read_b64 a[124:125], v0 offset:256\n\ts_add_u32 s51, s44, 8\n\tv_mfma_f32_16x16x32_bf16 v[192:195], a[56:59], v[104:107], v[192:195]\n\tds_read_b64 a[126:127], v0 offset:264\n\ts_mul_i32 s65, s51, 1366\n\tv_mfma_f32_16x16x32_bf16 v[192:195], a[60:63], v[100:103], v[192:195]\n\t" \
    "ds_read_b64 a[128:129], v0 offset:3776\n\ts_lshr_b32 s65, s65, 16\n\tv_mfma_f32_16x16x32_bf16 v[188:191], a[32:35], v[124:127], v[188:191]\n\tds_read_b64 a[130:131], v0 offset:3784\n\ts_mul_i32 s56, s65, 48\n\tv_mfma_f32_16x16x32_bf16 v[188:191], a[36:39], v[120:123], v[188:191]\n\tds_read_b64 a[132:133], v0 offset:3264\n\ts_sub_u32 s56, s51, s56\n\t" \
    "v_mfma_f32_16x16x32_bf16 v[188:191], a[40:43], v[116:119], v[188:191]\n\tds_read_b64 a[134:135], v0 offset:3272\n\ts_lshl_b32 s56, s56, 6\n\tv_mfma_f32_16x16x32_bf16 v[188:191], a[44:47], v[112:115], v[188:191]\n\tds_read_b128 v[68:71], v1 offset:55808\n\ts_lshr_b32 s70, s65, 2\n\tv_mfma_f32_16x16x32_bf16 v[188:191], a[48:51], v[108:111], v[188:191]\n\tds_read_b128 v[72:75], v1 offset:56320\n\t" \
    "s_mul_i32 s70, s70, 252\n\tv_mfma_f32_16x16x32_bf16 v[188:191], a[52:55], v[104:107], v[188:191]\n\tds_read_b128 v[76:79], v1 offset:56832\n\ts_add_u32 s65, s65, s70\n\tv_mfma_f32_16x16x32_bf16 v[188:191], a[56:59], v[100:103], v[188:191]\n\tds_read_b128 v[80:83], v1 offset:57344\n\ts_mul_i32 s65, s65, 3072\n\tv_mfma_f32_16x16x32_bf16 v[184:187], a[32:35], v[120:123], v[184:187]\n\t" \
    "ds_read_b128 v[84:87], v1 offset:57856\n\ts_add_u32 s56, s56, s65\n\tv_mfma_f32_16x16x32_bf16 v[184:187], a[36:39], v[116:119], v[184:187]\n\tds_read_b128 v[88:91], v1 offset:58368\n\ts_add_u32 s56, s56, 4849664\n\tv_mfma_f32_16x16x32_bf16 v[184:187], a[40:43], v[112:115], v[184:187]\n\tds_read_b128 v[92:95], v1 offset:58880\n\ts_add_u32 s51, s51, s38\n\t" \
    "v_mfma_f32_16x16x32_bf16 v[184:187], a[44:47], v[108:111], v[184:187]\n\tds_read_b128 v[96:99], v1 offset:59392\n\ts_lshl_b32 s51, s51, 3\n\tv_mfma_f32_16x16x32_bf16 v[184:187], a[48:51], v[104:107], v[184:187]\n\tds_read_b64 a[136:137], v0 offset:2752\n\ts_add_u32 s51, s51, 50176\n\tds_read_b64_tr_b16 v[204:205], v4 offset:36992\n\tv_mfma_f32_16x16x32_bf16 v[184:187], a[52:55], v[100:103], v[184:187]\n\t" \
    "ds_read_b64 a[138:139], v0 offset:2760\n\tds_read_b64_tr_b16 v[206:207], v4 offset:37000\n\tv_mfma_f32_16x16x32_bf16 v[180:183], a[32:35], v[116:119], v[180:183]\n\tds_read_b64 a[140:141], v0 offset:2240\n\tds_read_b64_tr_b16 v[208:209], v4 offset:37056\n\tv_mfma_f32_16x16x32_bf16 v[180:183], a[36:39], v[112:115], v[180:183]\n\tds_read_b64 a[142:143], v0 offset:2248\n\tds_read_b64_tr_b16 v[210:211], v4 offset:37064\n\t" \
    "v_mfma_f32_16x16x32_bf16 v[180:183], a[40:43], v[108:111], v[180:183]\n\tds_read_b64 a[144:145], v0 offset:1728\n\tds_read_b64_tr_b16 v[212:213], v4 offset:37504\n\tv_mfma_f32_16x16x32_bf16 v[180:183], a[44:47], v[104:107], v[180:183]\n\tds_read_b64 a[146:147], v0 offset:1736\n\tds_read_b64_tr_b16 v[214:215], v4 offset:37512\n\tv_mfma_f32_16x16x32_bf16 v[180:183], a[48:51], v[100:103], v[180:183]\n\tds_read_b64 a[148:149], v0 offset:1216\n\t" \
    "ds_read_b64_tr_b16 v[216:217], v4 offset:37568\n\tv_mfma_f32_16x16x32_bf16 v[176:179], a[32:35], v[112:115], v[176:179]\n\tds_read_b64 a[150:151], v0 offset:1224\n\tds_read_b64_tr_b16 v[218:219], v4 offset:37576\n\tv_mfma_f32_16x16x32_bf16 v[176:179], a[36:39], v[108:111], v[176:179]\n\tds_read_b64 a[152:153], v0 offset:704\n\tv_mfma_f32_16x16x32_bf16 v[176:179], a[40:43], v[104:107], v[176:179]\n\tds_read_b64 a[154:155], v0 offset:712\n\t" \
    "v_mfma_f32_16x16x32_bf16 v[176:179], a[44:47], v[100:103], v[176:179]\n\tds_read_b64 a[156:157], v0 offset:192\n\tv_mfma_f32_16x16x32_bf16 v[172:175], a[32:35], v[108:111], v[172:175]\n\tds_read_b64 a[158:159], v0 offset:200\n\tv_mfma_f32_16x16x32_bf16 v[172:175], a[36:39], v[104:107], v[172:175]\n\tv_mfma_f32_16x16x32_bf16 v[172:175], a[40:43], v[100:103], v[172:175]\n\ts_waitcnt lgkmcnt(15)\n\tbuffer_store_dwordx4 v[204:207], v5, s[8:11], s56 offen sc1\n\t" \
    "s_add_u32 s56, s56, 49152\n\tv_mfma_f32_16x16x32_bf16 v[168:171], a[32:35], v[104:107], v[168:171]\n\tv_mfma_f32_16x16x32_bf16 v[168:171], a[36:39], v[100:103], v[168:171]\n\tv_mfma_f32_16x16x32_bf16 v[164:167], a[32:35], v[100:103], v[164:167]\n\tv_mfma_f32_16x16x32_bf16 v[164:167], a[64:67], v[68:71], v[164:167]\n\tds_read_b64 a[160:161], v0 offset:3712\n\tds_read_b64 a[162:163], v0 offset:3720\n\tv_mfma_f32_16x16x32_bf16 v[168:171], a[64:67], v[72:75], v[168:171]\n\t" \
    "ds_read_b64 a[164:165], v0 offset:3200\n\tds_read_b64 a[166:167], v0 offset:3208\n\tv_mfma_f32_16x16x32_bf16 v[168:171], a[68:71], v[68:71], v[168:171]\n\tds_read_b64 a[168:169], v0 offset:2688\n\tds_read_b64 a[170:171], v0 offset:2696\n\tv_mfma_f32_16x16x32_bf16 v[172:175], a[64:67], v[76:79], v[172:175]\n\tds_read_b64 a[172:173], v0 offset:2176\n\tds_read_b64 a[174:175], v0 offset:2184\n\t" \
    "v_mfma_f32_16x16x32_bf16 v[172:175], a[68:71], v[72:75], v[172:175]\n\tds_read_b64 a[176:177], v0 offset:1664\n\tds_read_b64 a[178:179], v0 offset:1672\n\tv_mfma_f32_16x16x32_bf16 v[172:175], a[72:75], v[68:71], v[172:175]\n\tds_read_b64 a[180:181], v0 offset:1152\n\tv_mfma_f32_16x16x32_bf16 v[176:179], a[64:67], v[80:83], v[176:179]\n\tds_read_b64 a[182:183], v0 offset:1160\n\tv_mfma_f32_16x16x32_bf16 v[176:179], a[68:71], v[76:79], v[176:179]\n\t" \
    "ds_read_b64 a[184:185], v0 offset:640\n\tv_mfma_f32_16x16x32_bf16 v[176:179], a[72:75], v[72:75], v[176:179]\n\tbuffer_load_dword v15, v7, s[16:19], s42 offen\n\tds_read_b64 a[186:187], v0 offset:648\n\tv_mfma_f32_16x16x32_bf16 v[176:179], a[76:79], v[68:71], v[176:179]\n\ts_add_u32 s43, s43, 1\n\tds_read_b64 a[188:189], v0 offset:128\n\tv_mfma_f32_16x16x32_bf16 v[180:183], a[64:67], v[84:87], v[180:183]\n\t" \
    "v_readlane_b32 s42, v237, s43\n\ts_nop 4\n\tds_read_b64 a[190:191], v0 offset:136\n\tv_mfma_f32_16x16x32_bf16 v[180:183], a[68:71], v[80:83], v[180:183]\n\tds_read_b128 v[100:103], v1 offset:55744\n\tv_mfma_f32_16x16x32_bf16 v[180:183], a[72:75], v[76:79], v[180:183]\n\tds_read_b128 v[104:107], v1 offset:56256\n\tv_mfma_f32_16x16x32_bf16 v[180:183], a[76:79], v[72:75], v[180:183]\n\t" \
    "ds_read_b128 v[108:111], v1 offset:56768\n\tv_mfma_f32_16x16x32_bf16 v[180:183], a[80:83], v[68:71], v[180:183]\n\tds_read_b128 v[112:115], v1 offset:57280\n\tv_mfma_f32_16x16x32_bf16 v[184:187], a[64:67], v[88:91], v[184:187]\n\tds_read_b128 v[116:119], v1 offset:57792\n\ts_waitcnt lgkmcnt(15)\n\tbuffer_store_dwordx4 v[208:211], v5, s[8:11], s56 offen sc1\n\tv_mfma_f32_16x16x32_bf16 v[184:187], a[68:71], v[84:87], v[184:187]\n\t" \
    "ds_read_b128 v[120:123], v1 offset:58304\n\ts_add_u32 s56, s56, 49152\n\tv_mfma_f32_16x16x32_bf16 v[184:187], a[72:75], v[80:83], v[184:187]\n\tds_read_b128 v[124:127], v1 offset:58816\n\tv_mfma_f32_16x16x32_bf16 v[184:187], a[76:79], v[76:79], v[184:187]\n\tds_read_b128 v[128:131], v1 offset:59328\n\tv_mfma_f32_16x16x32_bf16 v[184:187], a[80:83], v[72:75], v[184:187]\n\tds_read_b64 a[192:193], v0 offset:3648\n\t" \
    "v_mfma_f32_16x16x32_bf16 v[184:187], a[84:87], v[68:71], v[184:187]\n\tds_read_b64 a[194:195], v0 offset:3656\n\tv_mfma_f32_16x16x32_bf16 v[188:191], a[64:67], v[92:95], v[188:191]\n\tds_read_b64 a[196:197], v0 offset:3136\n\tv_mfma_f32_16x16x32_bf16 v[188:191], a[68:71], v[88:91], v[188:191]\n\tds_read_b64 a[198:199], v0 offset:3144\n\tv_mfma_f32_16x16x32_bf16 v[188:191], a[72:75], v[84:87], v[188:191]\n\tds_read_b64 a[200:201], v0 offset:2624\n\t" \
    "v_mfma_f32_16x16x32_bf16 v[188:191], a[76:79], v[80:83], v[188:191]\n\tds_read_b64 a[202:203], v0 offset:2632\n\tv_mfma_f32_16x16x32_bf16 v[188:191], a[80:83], v[76:79], v[188:191]\n\tds_read_b64 a[204:205], v0 offset:2112\n\tv_mfma_f32_16x16x32_bf16 v[188:191], a[84:87], v[72:75], v[188:191]\n\tds_read_b64 a[206:207], v0 offset:2120\n\tv_mfma_f32_16x16x32_bf16 v[188:191], a[88:91], v[68:71], v[188:191]\n\tds_read_b64 a[208:209], v0 offset:1600\n\t" \
    "v_mfma_f32_16x16x32_bf16 v[192:195], a[64:67], v[96:99], v[192:195]\n\tds_read_b64 a[210:211], v0 offset:1608\n\tv_mfma_f32_16x16x32_bf16 v[192:195], a[68:71], v[92:95], v[192:195]\n\tds_read_b64 a[212:213], v0 offset:1088\n\tv_mfma_f32_16x16x32_bf16 v[192:195], a[72:75], v[88:91], v[192:195]\n\tds_read_b64 a[214:215], v0 offset:1096\n\tv_mfma_f32_16x16x32_bf16 v[192:195], a[76:79], v[84:87], v[192:195]\n\tbuffer_store_dwordx4 v[212:215], v5, s[8:11], s56 offen sc1\n\t" \
    "v_mfma_f32_16x16x32_bf16 v[192:195], a[80:83], v[80:83], v[192:195]\n\ts_add_u32 s56, s56, 49152\n\tv_mfma_f32_16x16x32_bf16 v[192:195], a[84:87], v[76:79], v[192:195]\n\tv_mfma_f32_16x16x32_bf16 v[192:195], a[88:91], v[72:75], v[192:195]\n\tv_mfma_f32_16x16x32_bf16 v[192:195], a[92:95], v[68:71], v[192:195]\n\ts_waitcnt lgkmcnt(12)\n\tv_mfma_f32_16x16x32_bf16 v[192:195], a[96:99], v[128:131], v[192:195]\n\tds_read_b64 a[216:217], v0 offset:576\n\t" \
    "v_mfma_f32_16x16x32_bf16 v[192:195], a[100:103], v[124:127], v[192:195]\n\tds_read_b64 a[218:219], v0 offset:584\n\tv_mfma_f32_16x16x32_bf16 v[192:195], a[104:107], v[120:123], v[192:195]\n\tds_read_b64 a[220:221], v0 offset:64\n\tv_mfma_f32_16x16x32_bf16 v[192:195], a[108:111], v[116:119], v[192:195]\n\tds_read_b64 a[222:223], v0 offset:72\n\tv_mfma_f32_16x16x32_bf16 v[192:195], a[112:115], v[112:115], v[192:195]\n\tds_read_b64 a[224:225], v0 offset:3584\n\t" \
    "v_mfma_f32_16x16x32_bf16 v[192:195], a[116:119], v[108:111], v[192:195]\n\tds_read_b64 a[226:227], v0 offset:3592\n\tv_mfma_f32_16x16x32_bf16 v[192:195], a[120:123], v[104:107], v[192:195]\n\tds_read_b64 a[228:229], v0 offset:3072\n\tv_mfma_f32_16x16x32_bf16 v[192:195], a[124:127], v[100:103], v[192:195]\n\tds_read_b64 a[230:231], v0 offset:3080\n\tv_mfma_f32_16x16x32_bf16 v[188:191], a[96:99], v[124:127], v[188:191]\n\tds_read_b64 a[232:233], v0 offset:2560\n\t" \
    "buffer_store_dwordx4 v[216:219], v5, s[8:11], s56 offen sc1\n\tv_mfma_f32_16x16x32_bf16 v[188:191], a[100:103], v[120:123], v[188:191]\n\tds_read_b64 a[234:235], v0 offset:2568\n\ts_add_u32 s56, s56, 49152\n\tv_mfma_f32_16x16x32_bf16 v[188:191], a[104:107], v[116:119], v[188:191]\n\tds_read_b64 a[236:237], v0 offset:2048\n\tv_mfma_f32_16x16x32_bf16 v[188:191], a[108:111], v[112:115], v[188:191]\n\tds_read_b64 a[238:239], v0 offset:2056\n\t" \
    "v_mfma_f32_16x16x32_bf16 v[188:191], a[112:115], v[108:111], v[188:191]\n\tds_read_b64 a[240:241], v0 offset:1536\n\tv_mfma_f32_16x16x32_bf16 v[188:191], a[116:119], v[104:107], v[188:191]\n\tds_read_b64 a[242:243], v0 offset:1544\n\tv_mfma_f32_16x16x32_bf16 v[188:191], a[120:123], v[100:103], v[188:191]\n\tds_read_b64 a[244:245], v0 offset:1024\n\tv_mfma_f32_16x16x32_bf16 v[184:187], a[96:99], v[120:123], v[184:187]\n\tds_read_b64 a[246:247], v0 offset:1032\n\t" \
    "v_mfma_f32_16x16x32_bf16 v[184:187], a[100:103], v[116:119], v[184:187]\n\tds_read_b128 v[68:71], v1 offset:55680\n\tv_mfma_f32_16x16x32_bf16 v[184:187], a[104:107], v[112:115], v[184:187]\n\tds_read_b128 v[72:75], v1 offset:56192\n\tv_mfma_f32_16x16x32_bf16 v[184:187], a[108:111], v[108:111], v[184:187]\n\tds_read_b128 v[76:79], v1 offset:56704\n\tv_mfma_f32_16x16x32_bf16 v[184:187], a[112:115], v[104:107], v[184:187]\n\tds_read_b128 v[80:83], v1 offset:57216\n\t" \
    "v_mfma_f32_16x16x32_bf16 v[184:187], a[116:119], v[100:103], v[184:187]\n\tds_read_b128 v[84:87], v1 offset:57728\n\tv_mfma_f32_16x16x32_bf16 v[180:183], a[96:99], v[116:119], v[180:183]\n\tds_read_b128 v[88:91], v1 offset:58240\n\tv_mfma_f32_16x16x32_bf16 v[180:183], a[100:103], v[112:115], v[180:183]\n\tds_read_b128 v[92:95], v1 offset:58752\n\tv_mfma_f32_16x16x32_bf16 v[180:183], a[104:107], v[108:111], v[180:183]\n\tds_read_b128 v[96:99], v1 offset:59264\n\t" \
    "v_mfma_f32_16x16x32_bf16 v[180:183], a[108:111], v[104:107], v[180:183]\n\tds_read_b64 a[248:249], v0 offset:512\n\tv_mfma_f32_16x16x32_bf16 v[180:183], a[112:115], v[100:103], v[180:183]\n\tds_read_b64 a[250:251], v0 offset:520\n\tv_mfma_f32_16x16x32_bf16 v[176:179], a[96:99], v[112:115], v[176:179]\n\tds_read_b64 a[252:253], v0 offset:0\n\tv_mfma_f32_16x16x32_bf16 v[176:179], a[100:103], v[108:111], v[176:179]\n\tds_read_b64 a[254:255], v0 offset:8\n\t" \
    "v_mfma_f32_16x16x32_bf16 v[176:179], a[104:107], v[104:107], v[176:179]\n\tv_mfma_f32_16x16x32_bf16 v[176:179], a[108:111], v[100:103], v[176:179]\n\tv_mfma_f32_16x16x32_bf16 v[172:175], a[96:99], v[108:111], v[172:175]\n\tv_mfma_f32_16x16x32_bf16 v[172:175], a[100:103], v[104:107], v[172:175]\n\tv_mfma_f32_16x16x32_bf16 v[172:175], a[104:107], v[100:103], v[172:175]\n\tv_mfma_f32_16x16x32_bf16 v[168:171], a[96:99], v[104:107], v[168:171]\n\tv_mfma_f32_16x16x32_bf16 v[168:171], a[100:103], v[100:103], v[168:171]\n\tv_mfma_f32_16x16x32_bf16 v[164:167], a[96:99], v[100:103], v[164:167]\n\t" \
    "s_waitcnt lgkmcnt(4)\n\tv_mfma_f32_16x16x32_bf16 v[164:167], a[128:131], v[68:71], v[164:167]\n\tds_read_b128 v[100:103], v1 offset:55616\n\tv_mfma_f32_16x16x32_bf16 v[168:171], a[128:131], v[72:75], v[168:171]\n\tds_read_b128 v[104:107], v1 offset:56128\n\tv_mfma_f32_16x16x32_bf16 v[168:171], a[132:135], v[68:71], v[168:171]\n\tds_read_b128 v[108:111], v1 offset:56640\n\tv_mfma_f32_16x16x32_bf16 v[172:175], a[128:131], v[76:79], v[172:175]\n\t" \
    "ds_read_b128 v[112:115], v1 offset:57152\n\tv_mfma_f32_16x16x32_bf16 v[172:175], a[132:135], v[72:75], v[172:175]\n\tds_read_b128 v[116:119], v1 offset:57664\n\tv_mfma_f32_16x16x32_bf16 v[172:175], a[136:139], v[68:71], v[172:175]\n\tds_read_b128 v[120:123], v1 offset:58176\n\tv_mfma_f32_16x16x32_bf16 v[176:179], a[128:131], v[80:83], v[176:179]\n\tds_read_b128 v[124:127], v1 offset:58688\n\tv_mfma_f32_16x16x32_bf16 v[176:179], a[132:135], v[76:79], v[176:179]\n\t" \
    "ds_read_b128 v[128:131], v1 offset:59200\n\tv_mfma_f32_16x16x32_bf16 v[176:179], a[136:139], v[72:75], v[176:179]\n\tv_mfma_f32_16x16x32_bf16 v[176:179], a[140:143], v[68:71], v[176:179]\n\tv_mfma_f32_16x16x32_bf16 v[180:183], a[128:131], v[84:87], v[180:183]\n\tv_mfma_f32_16x16x32_bf16 v[180:183], a[132:135], v[80:83], v[180:183]\n\tv_mfma_f32_16x16x32_bf16 v[180:183], a[136:139], v[76:79], v[180:183]\n\tv_mfma_f32_16x16x32_bf16 v[180:183], a[140:143], v[72:75], v[180:183]\n\tv_mfma_f32_16x16x32_bf16 v[180:183], a[144:147], v[68:71], v[180:183]\n\t" \
    "v_mfma_f32_16x16x32_bf16 v[184:187], a[128:131], v[88:91], v[184:187]\n\tv_mfma_f32_16x16x32_bf16 v[184:187], a[132:135], v[84:87], v[184:187]\n\tv_mfma_f32_16x16x32_bf16 v[184:187], a[136:139], v[80:83], v[184:187]\n\tv_mfma_f32_16x16x32_bf16 v[184:187], a[140:143], v[76:79], v[184:187]\n\tv_mfma_f32_16x16x32_bf16 v[184:187], a[144:147], v[72:75], v[184:187]\n\tv_mfma_f32_16x16x32_bf16 v[184:187], a[148:151], v[68:71], v[184:187]\n\tv_mfma_f32_16x16x32_bf16 v[188:191], a[128:131], v[92:95], v[188:191]\n\tv_mfma_f32_16x16x32_bf16 v[188:191], a[132:135], v[88:91], v[188:191]\n\t" \
    "v_mfma_f32_16x16x32_bf16 v[188:191], a[136:139], v[84:87], v[188:191]\n\tv_mfma_f32_16x16x32_bf16 v[188:191], a[140:143], v[80:83], v[188:191]\n\tv_mfma_f32_16x16x32_bf16 v[188:191], a[144:147], v[76:79], v[188:191]\n\tv_mfma_f32_16x16x32_bf16 v[188:191], a[148:151], v[72:75], v[188:191]\n\tv_mfma_f32_16x16x32_bf16 v[188:191], a[152:155], v[68:71], v[188:191]\n\tv_mfma_f32_16x16x32_bf16 v[192:195], a[128:131], v[96:99], v[192:195]\n\tv_mfma_f32_16x16x32_bf16 v[192:195], a[132:135], v[92:95], v[192:195]\n\tv_mfma_f32_16x16x32_bf16 v[192:195], a[136:139], v[88:91], v[192:195]\n\t" \
    "v_mfma_f32_16x16x32_bf16 v[192:195], a[140:143], v[84:87], v[192:195]\n\tv_mfma_f32_16x16x32_bf16 v[192:195], a[144:147], v[80:83], v[192:195]\n\tv_mfma_f32_16x16x32_bf16 v[192:195], a[148:151], v[76:79], v[192:195]\n\tv_mfma_f32_16x16x32_bf16 v[192:195], a[152:155], v[72:75], v[192:195]\n\tv_mfma_f32_16x16x32_bf16 v[192:195], a[156:159], v[68:71], v[192:195]\n\ts_waitcnt lgkmcnt(0)\n\tv_mfma_f32_16x16x32_bf16 v[192:195], a[160:163], v[128:131], v[192:195]\n\tds_read_b128 v[68:71], v1 offset:55552\n\t" \
    "v_mfma_f32_16x16x32_bf16 v[192:195], a[164:167], v[124:127], v[192:195]\n\tds_read_b128 v[72:75], v1 offset:56064\n\tv_mfma_f32_16x16x32_bf16 v[192:195], a[168:171], v[120:123], v[192:195]\n\tds_read_b128 v[76:79], v1 offset:56576\n\tv_mfma_f32_16x16x32_bf16 v[192:195], a[172:175], v[116:119], v[192:195]\n\tds_read_b128 v[80:83], v1 offset:57088\n\tv_mfma_f32_16x16x32_bf16 v[192:195], a[176:179], v[112:115], v[192:195]\n\tds_read_b128 v[84:87], v1 offset:57600\n\t" \
    "v_mfma_f32_16x16x32_bf16 v[192:195], a[180:183], v[108:111], v[192:195]\n\tds_read_b128 v[88:91], v1 offset:58112\n\tv_mfma_f32_16x16x32_bf16 v[192:195], a[184:187], v[104:107], v[192:195]\n\tds_read_b128 v[92:95], v1 offset:58624\n\tv_mfma_f32_16x16x32_bf16 v[192:195], a[188:191], v[100:103], v[192:195]\n\tds_read_b128 v[96:99], v1 offset:59136\n\tv_mfma_f32_16x16x32_bf16 v[188:191], a[160:163], v[124:127], v[188:191]\n\tv_mfma_f32_16x16x32_bf16 v[188:191], a[164:167], v[120:123], v[188:191]\n\t" \
    "v_mfma_f32_16x16x32_bf16 v[188:191], a[168:171], v[116:119], v[188:191]\n\tv_mfma_f32_16x16x32_bf16 v[188:191], a[172:175], v[112:115], v[188:191]\n\tv_mfma_f32_16x16x32_bf16 v[188:191], a[176:179], v[108:111], v[188:191]\n\tv_mfma_f32_16x16x32_bf16 v[188:191], a[180:183], v[104:107], v[188:191]\n\tv_mfma_f32_16x16x32_bf16 v[188:191], a[184:187], v[100:103], v[188:191]\n\tv_mfma_f32_16x16x32_bf16 v[184:187], a[160:163], v[120:123], v[184:187]\n\tv_mfma_f32_16x16x32_bf16 v[184:187], a[164:167], v[116:119], v[184:187]\n\tv_mfma_f32_16x16x32_bf16 v[184:187], a[168:171], v[112:115], v[184:187]\n\t" \
    "v_mfma_f32_16x16x32_bf16 v[184:187], a[172:175], v[108:111], v[184:187]\n\tv_mfma_f32_16x16x32_bf16 v[184:187], a[176:179], v[104:107], v[184:187]\n\tv_mfma_f32_16x16x32_bf16 v[184:187], a[180:183], v[100:103], v[184:187]\n\tv_mfma_f32_16x16x32_bf16 v[180:183], a[160:163], v[116:119], v[180:183]\n\tv_mfma_f32_16x16x32_bf16 v[180:183], a[164:167], v[112:115], v[180:183]\n\tv_mfma_f32_16x16x32_bf16 v[180:183], a[168:171], v[108:111], v[180:183]\n\tv_mfma_f32_16x16x32_bf16 v[180:183], a[172:175], v[104:107], v[180:183]\n\tv_mfma_f32_16x16x32_bf16 v[180:183], a[176:179], v[100:103], v[180:183]\n\t" \
    "v_mfma_f32_16x16x32_bf16 v[176:179], a[160:163], v[112:115], v[176:179]\n\tv_mfma_f32_16x16x32_bf16 v[176:179], a[164:167], v[108:111], v[176:179]\n\tv_mfma_f32_16x16x32_bf16 v[176:179], a[168:171], v[104:107], v[176:179]\n\tv_mfma_f32_16x16x32_bf16 v[176:179], a[172:175], v[100:103], v[176:179]\n\tv_mfma_f32_16x16x32_bf16 v[172:175], a[160:163], v[108:111], v[172:175]\n\tv_mfma_f32_16x16x32_bf16 v[172:175], a[164:167], v[104:107], v[172:175]\n\tv_mfma_f32_16x16x32_bf16 v[172:175], a[168:171], v[100:103], v[172:175]\n\tv_mfma_f32_16x16x32_bf16 v[168:171], a[160:163], v[104:107], v[168:171]\n\t" \
    "v_mfma_f32_16x16x32_bf16 v[168:171], a[164:167], v[100:103], v[168:171]\n\tv_mfma_f32_16x16x32_bf16 v[164:167], a[160:163], v[100:103], v[164:167]\n\ts_waitcnt lgkmcnt(0)\n\tv_mfma_f32_16x16x32_bf16 v[164:167], a[192:195], v[68:71], v[164:167]\n\tds_read_b128 v[100:103], v1 offset:55488\n\tv_mfma_f32_16x16x32_bf16 v[168:171], a[192:195], v[72:75], v[168:171]\n\tds_read_b128 v[104:107], v1 offset:56000\n\tv_mfma_f32_16x16x32_bf16 v[168:171], a[196:199], v[68:71], v[168:171]\n\t" \
    "ds_read_b128 v[108:111], v1 offset:56512\n\tv_mfma_f32_16x16x32_bf16 v[172:175], a[192:195], v[76:79], v[172:175]\n\tds_read_b128 v[112:115], v1 offset:57024\n\tv_mfma_f32_16x16x32_bf16 v[172:175], a[196:199], v[72:75], v[172:175]\n\tds_read_b128 v[116:119], v1 offset:57536\n\tv_mfma_f32_16x16x32_bf16 v[172:175], a[200:203], v[68:71], v[172:175]\n\tds_read_b128 v[120:123], v1 offset:58048\n\tv_mfma_f32_16x16x32_bf16 v[176:179], a[192:195], v[80:83], v[176:179]\n\t" \
    "ds_read_b128 v[124:127], v1 offset:58560\n\tv_mfma_f32_16x16x32_bf16 v[176:179], a[196:199], v[76:79], v[176:179]\n\tds_read_b128 v[128:131], v1 offset:59072\n\tv_mfma_f32_16x16x32_bf16 v[176:179], a[200:203], v[72:75], v[176:179]\n\tv_mfma_f32_16x16x32_bf16 v[176:179], a[204:207], v[68:71], v[176:179]\n\tv_mfma_f32_16x16x32_bf16 v[180:183], a[192:195], v[84:87], v[180:183]\n\tv_mfma_f32_16x16x32_bf16 v[180:183], a[196:199], v[80:83], v[180:183]\n\tv_mfma_f32_16x16x32_bf16 v[180:183], a[200:203], v[76:79], v[180:183]\n\t" \
    "v_mfma_f32_16x16x32_bf16 v[180:183], a[204:207], v[72:75], v[180:183]\n\tv_mfma_f32_16x16x32_bf16 v[180:183], a[208:211], v[68:71], v[180:183]\n\tv_mfma_f32_16x16x32_bf16 v[184:187], a[192:195], v[88:91], v[184:187]\n\tv_mfma_f32_16x16x32_bf16 v[184:187], a[196:199], v[84:87], v[184:187]\n\tv_mfma_f32_16x16x32_bf16 v[184:187], a[200:203], v[80:83], v[184:187]\n\tv_mfma_f32_16x16x32_bf16 v[184:187], a[204:207], v[76:79], v[184:187]\n\tv_mfma_f32_16x16x32_bf16 v[184:187], a[208:211], v[72:75], v[184:187]\n\tv_mfma_f32_16x16x32_bf16 v[184:187], a[212:215], v[68:71], v[184:187]\n\t" \
    "v_mfma_f32_16x16x32_bf16 v[188:191], a[192:195], v[92:95], v[188:191]\n\tv_mfma_f32_16x16x32_bf16 v[188:191], a[196:199], v[88:91], v[188:191]\n\tv_mfma_f32_16x16x32_bf16 v[188:191], a[200:203], v[84:87], v[188:191]\n\tv_mfma_f32_16x16x32_bf16 v[188:191], a[204:207], v[80:83], v[188:191]\n\tv_mfma_f32_16x16x32_bf16 v[188:191], a[208:211], v[76:79], v[188:191]\n\tv_mfma_f32_16x16x32_bf16 v[188:191], a[212:215], v[72:75], v[188:191]\n\tv_mfma_f32_16x16x32_bf16 v[188:191], a[216:219], v[68:71], v[188:191]\n\tv_mfma_f32_16x16x32_bf16 v[192:195], a[192:195], v[96:99], v[192:195]\n\t" \
    "v_mfma_f32_16x16x32_bf16 v[192:195], a[196:199], v[92:95], v[192:195]\n\tv_mfma_f32_16x16x32_bf16 v[192:195], a[200:203], v[88:91], v[192:195]\n\tv_mfma_f32_16x16x32_bf16 v[192:195], a[204:207], v[84:87], v[192:195]\n\tv_mfma_f32_16x16x32_bf16 v[192:195], a[208:211], v[80:83], v[192:195]\n\tv_mfma_f32_16x16x32_bf16 v[192:195], a[212:215], v[76:79], v[192:195]\n\tv_mfma_f32_16x16x32_bf16 v[192:195], a[216:219], v[72:75], v[192:195]\n\tv_mfma_f32_16x16x32_bf16 v[192:195], a[220:223], v[68:71], v[192:195]\n\ts_waitcnt lgkmcnt(0)\n\t" \
    "v_mfma_f32_16x16x32_bf16 v[192:195], a[224:227], v[128:131], v[192:195]\n\ts_waitcnt vmcnt(0)\n\tbuffer_store_dwordx2 v[248:249], off, s[8:11], s51 sc1\n\tv_mfma_f32_16x16x32_bf16 v[192:195], a[228:231], v[124:127], v[192:195]\n\tv_mfma_f32_16x16x32_bf16 v[192:195], a[232:235], v[120:123], v[192:195]\n\tv_mfma_f32_16x16x32_bf16 v[192:195], a[236:239], v[116:119], v[192:195]\n\tv_mfma_f32_16x16x32_bf16 v[192:195], a[240:243], v[112:115], v[192:195]\n\tv_mfma_f32_16x16x32_bf16 v[192:195], a[244:247], v[108:111], v[192:195]\n\t" \
    "v_mfma_f32_16x16x32_bf16 v[192:195], a[248:251], v[104:107], v[192:195]\n\tv_mfma_f32_16x16x32_bf16 v[192:195], a[252:255], v[100:103], v[192:195]\n\tv_mfma_f32_16x16x32_bf16 v[188:191], a[224:227], v[124:127], v[188:191]\n\tv_mfma_f32_16x16x32_bf16 v[188:191], a[228:231], v[120:123], v[188:191]\n\tv_mfma_f32_16x16x32_bf16 v[188:191], a[232:235], v[116:119], v[188:191]\n\tv_mfma_f32_16x16x32_bf16 v[188:191], a[236:239], v[112:115], v[188:191]\n\tv_mfma_f32_16x16x32_bf16 v[188:191], a[240:243], v[108:111], v[188:191]\n\tv_mfma_f32_16x16x32_bf16 v[188:191], a[244:247], v[104:107], v[188:191]\n\t" \
    "v_mfma_f32_16x16x32_bf16 v[188:191], a[248:251], v[100:103], v[188:191]\n\tv_mfma_f32_16x16x32_bf16 v[184:187], a[224:227], v[120:123], v[184:187]\n\tv_mfma_f32_16x16x32_bf16 v[184:187], a[228:231], v[116:119], v[184:187]\n\tv_mfma_f32_16x16x32_bf16 v[184:187], a[232:235], v[112:115], v[184:187]\n\tv_mfma_f32_16x16x32_bf16 v[184:187], a[236:239], v[108:111], v[184:187]\n\tv_mfma_f32_16x16x32_bf16 v[184:187], a[240:243], v[104:107], v[184:187]\n\tv_mfma_f32_16x16x32_bf16 v[184:187], a[244:247], v[100:103], v[184:187]\n\tv_mfma_f32_16x16x32_bf16 v[180:183], a[224:227], v[116:119], v[180:183]\n\t" \
    "v_mfma_f32_16x16x32_bf16 v[180:183], a[228:231], v[112:115], v[180:183]\n\tv_mfma_f32_16x16x32_bf16 v[180:183], a[232:235], v[108:111], v[180:183]\n\tv_mfma_f32_16x16x32_bf16 v[180:183], a[236:239], v[104:107], v[180:183]\n\tv_mfma_f32_16x16x32_bf16 v[180:183], a[240:243], v[100:103], v[180:183]\n\tv_mfma_f32_16x16x32_bf16 v[176:179], a[224:227], v[112:115], v[176:179]\n\tv_mfma_f32_16x16x32_bf16 v[176:179], a[228:231], v[108:111], v[176:179]\n\tv_mfma_f32_16x16x32_bf16 v[176:179], a[232:235], v[104:107], v[176:179]\n\tv_mfma_f32_16x16x32_bf16 v[176:179], a[236:239], v[100:103], v[176:179]\n\t" \
    "v_mfma_f32_16x16x32_bf16 v[172:175], a[224:227], v[108:111], v[172:175]\n\tv_mfma_f32_16x16x32_bf16 v[172:175], a[228:231], v[104:107], v[172:175]\n\tv_mfma_f32_16x16x32_bf16 v[172:175], a[232:235], v[100:103], v[172:175]\n\tv_mfma_f32_16x16x32_bf16 v[168:171], a[224:227], v[104:107], v[168:171]\n\tv_mfma_f32_16x16x32_bf16 v[168:171], a[228:231], v[100:103], v[168:171]\n\tv_mfma_f32_16x16x32_bf16 v[164:167], a[224:227], v[100:103], v[164:167]\n\ts_nop 7\n\tv_cvt_pk_bf16_f32 v164, v164, v165\n\t" \
    "v_cvt_pk_bf16_f32 v165, v166, v167\n\tds_write_b64 v3, v[164:165] offset:55488\n\tv_cvt_pk_bf16_f32 v168, v168, v169\n\tv_cvt_pk_bf16_f32 v169, v170, v171\n\tds_write_b64 v3, v[168:169] offset:56000\n\tv_cvt_pk_bf16_f32 v172, v172, v173\n\tv_cvt_pk_bf16_f32 v173, v174, v175\n\tds_write_b64 v3, v[172:173] offset:56512\n\t" \
    "v_cvt_pk_bf16_f32 v176, v176, v177\n\tv_cvt_pk_bf16_f32 v177, v178, v179\n\tds_write_b64 v3, v[176:177] offset:57024\n\tv_cvt_pk_bf16_f32 v180, v180, v181\n\tv_cvt_pk_bf16_f32 v181, v182, v183\n\tds_write_b64 v3, v[180:181] offset:57536\n\tv_cvt_pk_bf16_f32 v184, v184, v185\n\tv_cvt_pk_bf16_f32 v185, v186, v187\n\t" \
    "ds_write_b64 v3, v[184:185] offset:58048\n\tv_cvt_pk_bf16_f32 v188, v188, v189\n\tv_cvt_pk_bf16_f32 v189, v190, v191\n\tds_write_b64 v3, v[188:189] offset:58560\n\tv_cvt_pk_bf16_f32 v192, v192, v193\n\tv_cvt_pk_bf16_f32 v193, v194, v195\n\tds_write_b64 v3, v[192:193] offset:59072\n\ts_waitcnt lgkmcnt(0)\n\t" \
    "s_barrier\n\ts_add_u32 s51, s44, 12\n\ts_mul_i32 s65, s51, 1366\n\ts_lshr_b32 s65, s65, 16\n\ts_mul_i32 s56, s65, 48\n\ts_sub_u32 s56, s51, s56\n\ts_lshl_b32 s56, s56, 6\n\ts_lshr_b32 s70, s65, 2\n\t" \
    "s_mul_i32 s70, s70, 252\n\ts_add_u32 s65, s65, s70\n\ts_mul_i32 s65, s65, 3072\n\ts_add_u32 s56, s56, s65\n\ts_add_u32 s56, s56, 4849664\n\ts_add_u32 s51, s51, s38\n\ts_lshl_b32 s51, s51, 3\n\ts_add_u32 s51, s51, 50176\n\t" \
    "s_or_b32 s56, s56, s45\n\ts_or_b32 s51, s51, s45\n\tds_read_b64_tr_b16 v[204:205], v4 offset:55488\n\tds_read_b64_tr_b16 v[206:207], v4 offset:55496\n\tds_read_b64_tr_b16 v[208:209], v4 offset:55552\n\tds_read_b64_tr_b16 v[210:211], v4 offset:55560\n\tds_read_b64_tr_b16 v[212:213], v4 offset:56000\n\tds_read_b64_tr_b16 v[214:215], v4 offset:56008\n\t" \
    "ds_read_b64_tr_b16 v[216:217], v4 offset:56064\n\tds_read_b64_tr_b16 v[218:219], v4 offset:56072\n\ts_waitcnt lgkmcnt(6)\n\tbuffer_store_dwordx4 v[204:207], v5, s[8:11], s56 offen sc1\n\ts_add_u32 s56, s56, 49152\n\ts_waitcnt lgkmcnt(4)\n\tbuffer_store_dwordx4 v[208:211], v5, s[8:11], s56 offen sc1\n\ts_add_u32 s56, s56, 49152\n\t" \
    "s_waitcnt lgkmcnt(2)\n\tbuffer_store_dwordx4 v[212:215], v5, s[8:11], s56 offen sc1\n\ts_add_u32 s56, s56, 49152\n\ts_waitcnt lgkmcnt(0)\n\tbuffer_store_dwordx4 v[216:219], v5, s[8:11], s56 offen sc1\n\ts_add_u32 s56, s56, 49152\n\ts_waitcnt vmcnt(0)\n\tbuffer_store_dwordx2 v[248:249], off, s[8:11], s51 sc1\n\t" \
    "s_waitcnt vmcnt(0)\n\t103:\n\t98:\n\ts_nop 1\n\t" \
    ""
#define MAIN_CLOB "s2", "s3", "s4", "s5", "s6", "s7", "s8", "s9", "s10", "s11", "s12", "s13", "s14", "s15", "s16", "s17", "s18", "s19", "s20", "s21", "s22", "s23", "s24", "s25", "s26", "s27", "s28", "s29", "s30", "s31", "s33", "s34", "s35", "s36", "s37", "s39", "s41", "s42", "s43", "s44", "s45", "s46", "s47", "s48", "s49", "s50", "s51", "s52", "s53", "s54", "s55", "s56", "s57", "s58", "s59", "s60", "s61", "s62", "s63", "s64", "s65", "s66", "s67", "s68", "s69", "s70", "s71", "s72", "s73", "s74", "s75", "s76", "s77", "s78", "s79", "s80", "s81", "s82", "s83", "s84", "s85", "s86", "s87", "v0", "v1", "v2", "v3", "v4", "v5", "v6", "v7", "v8", "v9", "v10", "v11", "v12", "v13", "v14", "v15", "v16", "v17", "v18", "v19", "v20", "v21", "v22", "v23", "v24", "v25", "v26", "v27", "v28", "v29", "v30", "v31", "v32", "v33", "v34", "v35", "v36", "v37", "v38", "v39", "v40", "v41", "v42", "v43", "v44", "v45", "v46", "v47", "v48", "v49", "v50", "v51", "v52", "v53", "v54", "v55", "v56", "v57", "v58", "v59", "v60", "v61", "v62", "v63", "v64", "v65", "v66", "v67", "v68", "v69", "v70", "v71", "v72", "v73", "v74", "v75", "v76", "v77", "v78", "v79", "v80", "v81", "v82", "v83", "v84", "v85", "v86", "v87", "v88", "v89", "v90", "v91", "v92", "v93", "v94", "v95", "v96", "v97", "v98", "v99", "v100", "v101", "v102", "v103", "v104", "v105", "v106", "v107", "v108", "v109", "v110", "v111", "v112", "v113", "v114", "v115", "v116", "v117", "v118", "v119", "v120", "v121", "v122", "v123", "v124", "v125", "v126", "v127", "v128", "v129", "v130", "v131", "v132", "v133", "v134", "v135", "v136", "v137", "v138", "v139", "v140", "v141", "v142", "v143", "v144", "v145", "v146", "v147", "v148", "v149", "v150", "v151", "v152", "v153", "v154", "v155", "v156", "v157", "v158", "v159", "v160", "v161", "v162", "v163", "v164", "v165", "v166", "v167", "v168", "v169", "v170", "v171", "v172", "v173", "v174", "v175", "v176", "v177", "v178", "v179", "v180", "v181", "v182", "v183", "v184", "v185", "v186", "v187", "v188", "v189", "v190", "v191", "v192", "v193", "v194", "v195", "v196", "v197", "v198", "v199", "v200", "v201", "v202", "v203", "v204", "v205", "v206", "v207", "v208", "v209", "v210", "v211", "v212", "v213", "v214", "v215", "v216", "v217", "v218", "v219", "v220", "v221", "v222", "v223", "v224", "v225", "v226", "v227", "v228", "v229", "v230", "v231", "v232", "v233", "v234", "v235", "v236", "v237", "v238", "v239", "v241", "v242", "v243", "v244", "v245", "v246", "v247", "v248", "v249", "v250", "v251", "v252", "v253", "a0", "a1", "a2", "a3", "a4", "a5", "a6", "a7", "a8", "a9", "a10", "a11", "a12", "a13", "a14", "a15", "a16", "a17", "a18", "a19", "a20", "a21", "a22", "a23", "a24", "a25", "a26", "a27", "a28", "a29", "a30", "a31", "a32", "a33", "a34", "a35", "a36", "a37", "a38", "a39", "a40", "a41", "a42", "a43", "a44", "a45", "a46", "a47", "a48", "a49", "a50", "a51", "a52", "a53", "a54", "a55", "a56", "a57", "a58", "a59", "a60", "a61", "a62", "a63", "a64", "a65", "a66", "a67", "a68", "a69", "a70", "a71", "a72", "a73", "a74", "a75", "a76", "a77", "a78", "a79", "a80", "a81", "a82", "a83", "a84", "a85", "a86", "a87", "a88", "a89", "a90", "a91", "a92", "a93", "a94", "a95", "a96", "a97", "a98", "a99", "a100", "a101", "a102", "a103", "a104", "a105", "a106", "a107", "a108", "a109", "a110", "a111", "a112", "a113", "a114", "a115", "a116", "a117", "a118", "a119", "a120", "a121", "a122", "a123", "a124", "a125", "a126", "a127", "a128", "a129", "a130", "a131", "a132", "a133", "a134", "a135", "a136", "a137", "a138", "a139", "a140", "a141", "a142", "a143", "a144", "a145", "a146", "a147", "a148", "a149", "a150", "a151", "a152", "a153", "a154", "a155", "a156", "a157", "a158", "a159", "a160", "a161", "a162", "a163", "a164", "a165", "a166", "a167", "a168", "a169", "a170", "a171", "a172", "a173", "a174", "a175", "a176", "a177", "a178", "a179", "a180", "a181", "a182", "a183", "a184", "a185", "a186", "a187", "a188", "a189", "a190", "a191", "a192", "a193", "a194", "a195", "a196", "a197", "a198", "a199", "a200", "a201", "a202", "a203", "a204", "a205", "a206", "a207", "a208", "a209", "a210", "a211", "a212", "a213", "a214", "a215", "a216", "a217", "a218", "a219", "a220", "a221", "a222", "a223", "a224", "a225", "a226", "a227", "a228", "a229", "a230", "a231", "a232", "a233", "a234", "a235", "a236", "a237", "a238", "a239", "a240", "a241", "a242", "a243", "a244", "a245", "a246", "a247", "a248", "a249", "a250", "a251", "a252", "a253", "a254", "a255"

#define ARG(i) const void* p##i, unsigned n##i, unsigned f##i
__global__ __launch_bounds__(256, 1) void fused_kernel(ARG(0), ARG(1), ARG(2), ARG(3), ARG(4), ARG(5), ARG(6), ARG(7), ARG(8), ARG(9), ARG(10),
                                                       ARG(11)) {
#define MAIN_OUT
    const int b = blockIdx.x;
    unsigned tid = threadIdx.x;
    u64 ka = (u64)__builtin_amdgcn_kernarg_segment_ptr();
    int w;
    asm volatile(MAIN_ASM
                 : "=&{s38}"(w), "+{s[0:1]}"(ka), "+{v240}"(tid) MAIN_OUT
                 : "{s40}"(b)
                 : "memory", "scc", "vcc", MAIN_CLOB);
}

extern "C" void kernel_launch(void* const* d_in, const int* in_sizes, int n_in, void* d_out, int out_size,
                              void* d_ws, size_t ws_size, hipStream_t stream) {
    (void)hipFuncSetAttribute((const void*)fused_kernel, hipFuncAttributeMaxDynamicSharedMemorySize, LDS_BYTES);
    const unsigned F = 0x20000u, NX = S_LEN * D_DIM * 2, NW = S_LEN * F_DIM * 2;
    fused_kernel<<<256, 256, LDS_BYTES, stream>>>(d_ws, WS_END, F, d_in[7], NW, F, d_in[9], NW, F, d_in[0], NX, F, d_out, NX, F, d_in[8], F_DIM * 2, F,
                                                  d_in[10], S_LEN * 2, F, d_in[1], NX, F, d_in[2], NX, F, d_in[3], D_DIM * 8, F, d_in[4], NX, F,
                                                  d_in[5], D_DIM * 2, F);
}
```

```cpp
#include <hip/hip_runtime.h>

typedef unsigned long long u64;

#define S_LEN 2048
#define D_DIM 576
#define F_DIM 4608
#define LDS_BYTES 163840

#define WS_END 29163520u
#define MAIN_ASM \
    "s_load_dwordx16 s[8:23], s[0:1], 0\n\ts_getpc_b64 s[2:3]\n\t99:\n\tv_lshrrev_b32 v20, 5, v240\n\tv_mul_u32_u24 v20, 1152, v20\n\tv_and_b32 v21, 31, v240\n\tv_lshl_add_u32 v22, v21, 2, v20\n\tv_lshl_add_u32 v21, v21, 4, v20\n\t" \
    "s_mul_i32 s68, s40, 9216\n\tv_lshrrev_b32 v245, 6, v240\n\tv_and_b32 v240, 63, v240\n\ts_and_b32 s2, s2, 0xffffff80\n\ts_mov_b32 s4, s2\n\ts_mov_b32 s5, s3\n\ts_mov_b32 s6, 98f-99b\n\ts_mov_b32 s7, 0x20000\n\t" \
    "v_lshl_add_u32 v16, v245, 16, v240\n\tv_lshlrev_b32 v16, 7, v16\n\ts_lshr_b32 s65, s40, 3\n\ts_lshl_b32 s73, s65, 13\n\tbuffer_load_dword v57, v16, s[4:7], s73 offen\n\ts_waitcnt lgkmcnt(0)\n\tbuffer_load_dwordx4 v[48:51], v21, s[20:23], s68 offen\n\tbuffer_load_dwordx4 v[52:55], v21, s[20:23], s68 offen offset:512\n\t" \
    "buffer_load_dword v56, v22, s[20:23], s68 offen offset:1024\n\tbuffer_load_dwordx2 v[58:59], off, s[8:11], 0 sc1\n\ts_load_dwordx8 s[76:83], s[0:1], 112\n\ts_load_dwordx4 s[84:87], s[0:1], 144\n\ts_load_dwordx8 s[24:31], s[0:1], 64\n\ts_load_dwordx4 s[32:35], s[0:1], 96\n\tv_readfirstlane_b32 s38, v245\n\tv_lshlrev_b32 v8, 4, v240\n\t" \
    "v_and_b32 v244, 0xf0, v8\n\tv_lshrrev_b32 v245, 4, v240\n\ts_and_b32 s57, s40, 1\n\ts_and_b32 s58, s38, 1\n\ts_cmp_lt_u32 s65, 27\n\ts_cselect_b32 s39, 1, 0\n\ts_cmp_lt_u32 s65, 24\n\ts_cselect_b32 s39, 2, s39\n\t" \
    "s_min_u32 s65, s65, 26\n\ts_mul_i32 s42, s65, 43\n\ts_lshr_b32 s42, s42, 7\n\ts_mul_i32 s66, s42, 3\n\ts_sub_u32 s41, s65, s66\n\ts_lshl_b32 s69, s42, 8\n\ts_bfe_u32 s66, s40, 0x20001\n\ts_mul_i32 s66, s66, 9\n\t" \
    "s_add_u32 s42, s42, s66\n\ts_mul_i32 s43, s41, 903\n\ts_lshr_b32 s43, s43, 7\n\ts_sub_u32 s70, s43, 21\n\ts_mul_i32 s67, s57, 9437184\n\ts_lshl_b32 s66, s66, 8\n\ts_add_u32 s67, s67, s66\n\tv_lshrrev_b32 v16, 3, v240\n\t" \
    "s_lshl_b32 s73, s38, 3\n\tv_add_u32 v16, s73, v16\n\tv_mul_u32_u24 v16, 9216, v16\n\tv_and_b32 v17, 0x70, v8\n\tv_and_b32 v18, 16, v240\n\tv_lshlrev_b32 v18, 1, v18\n\tv_xor_b32 v17, v17, v18\n\ts_lshl_b32 s73, s57, 1\n\t" \
    "s_add_u32 s73, s73, s58\n\ts_lshl_b32 s73, s73, 6\n\tv_xor_b32 v17, s73, v17\n\tv_add3_u32 v11, v16, v17, s69\n\tv_xor_b32 v10, 128, v11\n\ts_lshl_b32 s54, s38, 10\n\tv_add_u32 v9, 4096, v8\n\tv_lshlrev_b32 v239, 3, v240\n\t" \
    "s_mul_i32 s73, s40, 9216\n\ts_add_u32 s73, s73, s54\n\tv_add_u32 v23, s73, v8\n\tv_subrev_u32 v19, 3456, v23\n\ts_add_u32 s12, s12, s67\n\ts_addc_u32 s13, s13, 0\n\ts_sub_u32 s14, s14, s67\n\ts_cmp_eq_u32 s39, 0\n\t" \
    "s_cselect_b32 s14, 0, s14\n\tv_and_b32 v88, 31, v240\n\tv_lshrrev_b32 v89, 5, v240\n\ts_lshl_b32 s73, s38, 1\n\tv_add_u32 v89, s73, v89\n\tv_mul_u32_u24 v90, 1168, v89\n\tv_add_u32 v90, 153600, v90\n\tv_lshl_add_u32 v91, v88, 2, v90\n\t" \
    "v_lshl_add_u32 v90, v88, 4, v90\n\tv_cmp_eq_u32 vcc, 0, v88\n\tv_lshlrev_b32 v89, 3, v89\n\tv_mov_b32 v92, 0x7ff00000\n\tv_cndmask_b32 v89, v92, v89, vcc\n\tv_bfe_u32 v88, v240, 2, 2\n\tv_and_b32 v92, 3, v240\n\tv_lshlrev_b32 v92, 3, v92\n\t" \
    "v_lshl_add_u32 v92, v245, 5, v92\n\ts_lshl_b32 s73, s38, 7\n\tv_add_u32 v93, s73, v92\n\tv_mul_u32_u24 v94, 1168, v88\n\tv_add_u32 v94, 153600, v94\n\tv_add_u32 v95, v94, v92\n\tv_add_u32 v94, v94, v93\n\tv_mul_u32_u24 v88, 1152, v88\n\t" \
    "v_add_u32 v88, 131072, v88\n\tv_add_u32 v92, v88, v92\n\tv_add_u32 v93, v88, v93\n\tv_lshl_add_u32 v88, v245, 10, v244\n\ts_waitcnt lgkmcnt(0)\n\ts_lshr_b32 s61, s40, 2\n\ts_mul_i32 s61, s61, 36864\n\ts_and_b32 s65, s40, 3\n\t" \
    "s_lshl_b32 s65, s65, 8\n\ts_add_u32 s61, s61, s65\n\ts_add_u32 s61, s61, 131072\n\ts_mul_i32 s62, s40, 9216\n\ts_add_u32 s62, s62, 2490368\n\ts_lshl_b32 s63, s40, 3\n\ts_lshl_b32 s48, s38, 11\n\ts_add_u32 s48, s48, s63\n\t" \
    "s_add_u32 s48, s48, 17408\n\ts_add_u32 s63, s63, 1024\n\ts_cmp_eq_u32 s38, 0\n\ts_cselect_b32 s63, s63, 2146435072\n\ts_lshl_b32 s64, s40, 6\n\ts_add_u32 s64, s64, 58368\n\ts_add_u32 m0, s54, 131072\n\ts_nop 0\n\t" \
    "buffer_load_dwordx4 v19, s[76:79], 0 offen lds\n\ts_and_b32 s4, s40, 7\n\ts_lshl_b32 s47, s4, 11\n\ts_lshl_b32 s65, s57, 10\n\ts_add_u32 s47, s47, s65\n\tv_add_u32 v19, 4096, v19\n\ts_add_u32 m0, s54, 135168\n\ts_nop 0\n\t" \
    "buffer_load_dwordx4 v19, s[76:79], 0 offen lds\n\ts_add_u32 s47, s47, 1024\n\ts_mul_i32 s44, s57, 1179648\n\ts_mul_i32 s65, s41, 12288\n\ts_add_u32 s44, s44, s65\n\tv_add_u32 v19, 4096, v19\n\ts_add_u32 m0, s54, 139264\n\ts_nop 0\n\t" \
    "buffer_load_dwordx4 v19, s[76:79], 0 offen lds\n\ts_mul_i32 s65, s38, 3072\n\ts_add_u32 s44, s44, s65\n\ts_add_u32 s44, s44, 131072\n\ts_lshr_b32 s5, s4, 1\n\tv_add_u32 v19, 4096, v19\n\ts_add_u32 m0, s54, 144384\n\ts_nop 0\n\t" \
    "buffer_load_dwordx4 v23, s[80:83], 0 offen lds\n\tv_bfe_u32 v16, v240, 2, 2\n\tv_and_b32 v17, 3, v240\n\tv_lshlrev_b32 v17, 3, v17\n\tv_lshl_add_u32 v16, v16, 7, v17\n\tv_add_u32 v23, 4096, v23\n\ts_add_u32 m0, s54, 148480\n\ts_nop 0\n\t" \
    "buffer_load_dwordx4 v23, s[80:83], 0 offen lds\n\tv_bfe_u32 v17, v240, 3, 2\n\tv_lshl_add_u32 v16, v17, 5, v16\n\tv_lshl_add_u32 v0, v245, 10, v16\n\ts_and_b32 s65, s5, 1\n\tv_add_u32 v23, 4096, v23\n\tv_subrev_u32 v19, s54, v19\n\tv_subrev_u32 v23, s54, v23\n\t" \
    "s_cmp_eq_u32 s58, 1\n\ts_cselect_b64 vcc, -1, 0\n\ts_cselect_b64 s[76:77], s[80:81], s[76:77]\n\ts_mov_b32 m0, 143360\n\ts_cselect_b32 m0, 152576, m0\n\tv_cndmask_b32 v19, v19, v23, vcc\n\tbuffer_load_dwordx4 v19, s[76:79], 0 offen lds\n\ts_xor_b32 s65, s65, 1\n\t" \
    "s_lshl_b32 s65, s65, 6\n\tv_xor_b32 v17, s65, v16\n\tv_lshl_add_u32 v4, v245, 9, v17\n\ts_lshr_b32 s71, s54, 1\n\tbuffer_load_dwordx2 v[60:61], v239, s[84:87], s71 offen\n\tv_xor_b32 v1, 32, v0\n\tv_xor_b32 v5, 32, v4\n\tv_xor_b32 v2, 64, v0\n\t" \
    "v_xor_b32 v6, 64, v4\n\tbuffer_load_dwordx2 v[62:63], v239, s[84:87], s71 offen offset:2048\n\tv_xor_b32 v3, 96, v0\n\tv_xor_b32 v7, 96, v4\n\tv_add_u32 v224, 0x10000, v4\n\tv_add_u32 v14, 0x10000, v0\n\ts_mov_b32 s71, 4096\n\tbuffer_load_dwordx2 v[64:65], v239, s[84:87], s71 offen\n\t" \
    "v_add_u32 v220, 0x20000, v4\n\tv_add_u32 v225, 0x10000, v5\n\tv_add_u32 v15, 0x10000, v1\n\tv_add_u32 v221, 0x20000, v5\n\ts_cmp_ge_u32 s43, 21\n\ts_cselect_b32 s69, s70, s43\n\ts_mul_i32 s69, s69, 294912\n\ts_add_u32 s69, s69, 0\n\t" \
    "s_add_u32 m0, s54, 0\n\ts_nop 0\n\tbuffer_load_dwordx4 v10, s[12:15], s69 offen lds\n\tv_add_u32 v226, 0x10000, v6\n\tv_add_u32 v237, 0x10000, v2\n\tv_add_u32 v222, 0x20000, v6\n\tv_add_u32 v227, 0x10000, v7\n\ts_cmp_ge_u32 s43, 20\n\t" \
    "s_cselect_b32 s69, s70, s43\n\ts_mul_i32 s69, s69, 294912\n\ts_add_u32 s69, s69, 294912\n\ts_add_u32 m0, s54, 4096\n\ts_nop 0\n\tbuffer_load_dwordx4 v10, s[12:15], s69 offen lds\n\tv_add_u32 v238, 0x10000, v3\n\tv_add_u32 v223, 0x20000, v7\n\t" \
    "s_lshl_b32 s72, s40, 2\n\ts_add_u32 s72, s72, s38\n\ts_waitcnt vmcnt(12)\n\ts_load_dwordx8 s[76:83], s[0:1], 160\n\tv_lshlrev_b32 v24, 16, v48\n\tv_and_b32 v25, 0xffff0000, v48\n\tv_lshlrev_b32 v26, 16, v49\n\tv_and_b32 v27, 0xffff0000, v49\n\t" \
    "v_lshlrev_b32 v28, 16, v50\n\tv_and_b32 v29, 0xffff0000, v50\n\tv_lshlrev_b32 v30, 16, v51\n\tv_and_b32 v31, 0xffff0000, v51\n\tv_lshlrev_b32 v32, 16, v52\n\tv_and_b32 v33, 0xffff0000, v52\n\tv_lshlrev_b32 v34, 16, v53\n\tv_and_b32 v35, 0xffff0000, v53\n\t" \
    "v_lshlrev_b32 v36, 16, v54\n\tv_and_b32 v37, 0xffff0000, v54\n\tv_lshlrev_b32 v38, 16, v55\n\tv_and_b32 v39, 0xffff0000, v55\n\tv_lshlrev_b32 v40, 16, v56\n\tv_and_b32 v41, 0xffff0000, v56\n\tv_pk_add_f32 v[70:71], v[24:25], v[26:27]\n\tv_pk_add_f32 v[72:73], v[28:29], v[30:31]\n\t" \
    "v_pk_add_f32 v[74:75], v[32:33], v[34:35]\n\tv_pk_add_f32 v[76:77], v[36:37], v[38:39]\n\tv_pk_add_f32 v[70:71], v[70:71], v[72:73]\n\tv_pk_add_f32 v[74:75], v[74:75], v[76:77]\n\tv_pk_add_f32 v[70:71], v[70:71], v[40:41]\n\tv_pk_add_f32 v[70:71], v[70:71], v[74:75]\n\tv_add_f32 v66, v70, v71\n\ts_nop 1\n\t" \
    "v_add_f32_dpp v66, v66, v66 row_ror:8 row_mask:0xf bank_mask:0xf\n\ts_nop 1\n\tv_add_f32_dpp v66, v66, v66 row_ror:4 row_mask:0xf bank_mask:0xf\n\ts_nop 1\n\tv_add_f32_dpp v66, v66, v66 row_ror:2 row_mask:0xf bank_mask:0xf\n\ts_nop 1\n\tv_add_f32_dpp v66, v66, v66 row_ror:1 row_mask:0xf bank_mask:0xf\n\tv_mov_b32 v67, v66\n\t" \
    "s_nop 1\n\tv_permlane16_swap_b32 v66, v67\n\tv_add_f32 v66, v66, v67\n\tv_mul_f32 v66, 0x3ae38e39, v66\n\tv_pk_add_f32 v[70:71], v[24:25], v[66:67] op_sel_hi:[1,0] neg_lo:[0,1] neg_hi:[0,1]\n\tv_pk_add_f32 v[72:73], v[26:27], v[66:67] op_sel_hi:[1,0] neg_lo:[0,1] neg_hi:[0,1]\n\tv_pk_add_f32 v[74:75], v[28:29], v[66:67] op_sel_hi:[1,0] neg_lo:[0,1] neg_hi:[0,1]\n\tv_pk_add_f32 v[76:77], v[30:31], v[66:67] op_sel_hi:[1,0] neg_lo:[0,1] neg_hi:[0,1]\n\t" \
    "v_pk_add_f32 v[78:79], v[32:33], v[66:67] op_sel_hi:[1,0] neg_lo:[0,1] neg_hi:[0,1]\n\tv_pk_add_f32 v[80:81], v[34:35], v[66:67] op_sel_hi:[1,0] neg_lo:[0,1] neg_hi:[0,1]\n\tv_pk_add_f32 v[82:83], v[36:37], v[66:67] op_sel_hi:[1,0] neg_lo:[0,1] neg_hi:[0,1]\n\tv_pk_add_f32 v[84:85], v[38:39], v[66:67] op_sel_hi:[1,0] neg_lo:[0,1] neg_hi:[0,1]\n\tv_pk_add_f32 v[86:87], v[40:41], v[66:67] op_sel_hi:[1,0] neg_lo:[0,1] neg_hi:[0,1]\n\tv_pk_mul_f32 v[44:45], v[70:71], v[70:71]\n\tv_pk_mul_f32 v[46:47], v[72:73], v[72:73]\n\tv_pk_fma_f32 v[44:45], v[74:75], v[74:75], v[44:45]\n\t" \
    "v_pk_fma_f32 v[46:47], v[76:77], v[76:77], v[46:47]\n\tv_pk_fma_f32 v[44:45], v[78:79], v[78:79], v[44:45]\n\tv_pk_fma_f32 v[46:47], v[80:81], v[80:81], v[46:47]\n\tv_pk_fma_f32 v[44:45], v[82:83], v[82:83], v[44:45]\n\tv_pk_fma_f32 v[46:47], v[84:85], v[84:85], v[46:47]\n\tv_pk_fma_f32 v[44:45], v[86:87], v[86:87], v[44:45]\n\tv_pk_add_f32 v[44:45], v[44:45], v[46:47]\n\tv_add_f32 v68, v44, v45\n\t" \
    "s_nop 1\n\tv_add_f32_dpp v68, v68, v68 row_ror:8 row_mask:0xf bank_mask:0xf\n\ts_nop 1\n\tv_add_f32_dpp v68, v68, v68 row_ror:4 row_mask:0xf bank_mask:0xf\n\ts_nop 1\n\tv_add_f32_dpp v68, v68, v68 row_ror:2 row_mask:0xf bank_mask:0xf\n\ts_nop 1\n\tv_add_f32_dpp v68, v68, v68 row_ror:1 row_mask:0xf bank_mask:0xf\n\t" \
    "v_mov_b32 v69, v68\n\ts_nop 1\n\tv_permlane16_swap_b32 v68, v69\n\tv_add_f32 v68, v68, v69\n\tv_mul_f32 v68, 0x3ae38e39, v68\n\tv_cvt_pk_bf16_f32 v68, v68, v68\n\tv_lshlrev_b32 v68, 16, v68\n\tv_add_f32 v68, 0x3727c5ac, v68\n\t" \
    "v_cvt_pk_bf16_f32 v68, v68, v68\n\tv_lshlrev_b32 v68, 16, v68\n\tv_sqrt_f32 v68, v68\n\tv_cvt_pk_bf16_f32 v66, v66, v66\n\tv_lshlrev_b32 v66, 16, v66\n\tv_cvt_pk_bf16_f32 v68, v68, v68\n\tv_lshlrev_b32 v68, 16, v68\n\tv_rcp_f32 v68, v68\n\t" \
    "s_nop 0\n\tv_mul_f32 v69, v66, v68\n\tv_xor_b32 v69, 0x80000000, v69\n\tv_pk_fma_f32 v[24:25], v[24:25], v[68:69], v[68:69] op_sel:[0,0,1] op_sel_hi:[1,0,1]\n\tv_pk_fma_f32 v[26:27], v[26:27], v[68:69], v[68:69] op_sel:[0,0,1] op_sel_hi:[1,0,1]\n\tv_pk_fma_f32 v[28:29], v[28:29], v[68:69], v[68:69] op_sel:[0,0,1] op_sel_hi:[1,0,1]\n\tv_pk_fma_f32 v[30:31], v[30:31], v[68:69], v[68:69] op_sel:[0,0,1] op_sel_hi:[1,0,1]\n\tv_pk_fma_f32 v[32:33], v[32:33], v[68:69], v[68:69] op_sel:[0,0,1] op_sel_hi:[1,0,1]\n\t" \
    "v_pk_fma_f32 v[34:35], v[34:35], v[68:69], v[68:69] op_sel:[0,0,1] op_sel_hi:[1,0,1]\n\tv_pk_fma_f32 v[36:37], v[36:37], v[68:69], v[68:69] op_sel:[0,0,1] op_sel_hi:[1,0,1]\n\tv_pk_fma_f32 v[38:39], v[38:39], v[68:69], v[68:69] op_sel:[0,0,1] op_sel_hi:[1,0,1]\n\tv_pk_fma_f32 v[40:41], v[40:41], v[68:69], v[68:69] op_sel:[0,0,1] op_sel_hi:[1,0,1]\n\tv_cvt_pk_bf16_f32 v48, v24, v25\n\tv_cvt_pk_bf16_f32 v49, v26, v27\n\tv_cvt_pk_bf16_f32 v50, v28, v29\n\tv_cvt_pk_bf16_f32 v51, v30, v31\n\t" \
    "v_cvt_pk_bf16_f32 v52, v32, v33\n\tv_cvt_pk_bf16_f32 v53, v34, v35\n\tv_cvt_pk_bf16_f32 v54, v36, v37\n\tv_cvt_pk_bf16_f32 v55, v38, v39\n\tv_cvt_pk_bf16_f32 v56, v40, v41\n\tv_lshlrev_b32 v24, 16, v48\n\tv_and_b32 v25, 0xffff0000, v48\n\tv_lshlrev_b32 v26, 16, v49\n\t" \
    "v_and_b32 v27, 0xffff0000, v49\n\tv_lshlrev_b32 v28, 16, v50\n\tv_and_b32 v29, 0xffff0000, v50\n\tv_lshlrev_b32 v30, 16, v51\n\tv_and_b32 v31, 0xffff0000, v51\n\tv_lshlrev_b32 v32, 16, v52\n\tv_and_b32 v33, 0xffff0000, v52\n\tv_lshlrev_b32 v34, 16, v53\n\t" \
    "v_and_b32 v35, 0xffff0000, v53\n\tv_lshlrev_b32 v36, 16, v54\n\tv_and_b32 v37, 0xffff0000, v54\n\tv_lshlrev_b32 v38, 16, v55\n\tv_and_b32 v39, 0xffff0000, v55\n\tv_lshlrev_b32 v40, 16, v56\n\tv_and_b32 v41, 0xffff0000, v56\n\tv_pk_add_f32 v[70:71], v[24:25], v[26:27]\n\t" \
    "v_pk_add_f32 v[72:73], v[28:29], v[30:31]\n\tv_pk_add_f32 v[74:75], v[32:33], v[34:35]\n\tv_pk_add_f32 v[76:77], v[36:37], v[38:39]\n\tv_pk_mul_f32 v[44:45], v[24:25], v[24:25]\n\tv_pk_mul_f32 v[46:47], v[26:27], v[26:27]\n\tv_pk_add_f32 v[70:71], v[70:71], v[72:73]\n\tv_pk_add_f32 v[74:75], v[74:75], v[76:77]\n\tv_pk_fma_f32 v[44:45], v[28:29], v[28:29], v[44:45]\n\t" \
    "v_pk_fma_f32 v[46:47], v[30:31], v[30:31], v[46:47]\n\tv_pk_add_f32 v[70:71], v[70:71], v[40:41]\n\tv_pk_fma_f32 v[44:45], v[32:33], v[32:33], v[44:45]\n\tv_pk_fma_f32 v[46:47], v[34:35], v[34:35], v[46:47]\n\tv_pk_add_f32 v[70:71], v[70:71], v[74:75]\n\tv_pk_fma_f32 v[44:45], v[36:37], v[36:37], v[44:45]\n\tv_pk_fma_f32 v[46:47], v[38:39], v[38:39], v[46:47]\n\tv_pk_fma_f32 v[44:45], v[40:41], v[40:41], v[44:45]\n\t" \
    "v_pk_add_f32 v[44:45], v[44:45], v[46:47]\n\tv_add_f32 v66, v70, v71\n\tv_add_f32 v42, v44, v45\n\ts_nop 0\n\tv_add_f32_dpp v66, v66, v66 row_ror:8 row_mask:0xf bank_mask:0xf\n\tv_add_f32_dpp v42, v42, v42 row_ror:8 row_mask:0xf bank_mask:0xf\n\ts_nop 0\n\tv_add_f32_dpp v66, v66, v66 row_ror:4 row_mask:0xf bank_mask:0xf\n\t" \
    "v_add_f32_dpp v42, v42, v42 row_ror:4 row_mask:0xf bank_mask:0xf\n\ts_nop 0\n\tv_add_f32_dpp v66, v66, v66 row_ror:2 row_mask:0xf bank_mask:0xf\n\tv_add_f32_dpp v42, v42, v42 row_ror:2 row_mask:0xf bank_mask:0xf\n\ts_nop 0\n\tv_add_f32_dpp v66, v66, v66 row_ror:1 row_mask:0xf bank_mask:0xf\n\tv_add_f32_dpp v42, v42, v42 row_ror:1 row_mask:0xf bank_mask:0xf\n\tv_mov_b32 v67, v66\n\t" \
    "v_mov_b32 v43, v42\n\ts_nop 0\n\tv_permlane16_swap_b32 v66, v67\n\tv_permlane16_swap_b32 v42, v43\n\tv_add_f32 v66, v66, v67\n\tv_add_f32 v42, v42, v43\n\tv_mul_f32 v66, 0x3ae38e39, v66\n\tv_mul_f32 v42, 0x3ae38e39, v42\n\t" \
    "v_fma_f32 v42, -v66, v66, v42\n\tv_max_f32 v42, 0, v42\n\tv_cvt_pk_bf16_f32 v42, v42, v42\n\tv_lshlrev_b32 v42, 16, v42\n\tv_add_f32 v42, 0x3727c5ac, v42\n\tv_cvt_pk_bf16_f32 v42, v42, v42\n\tv_lshlrev_b32 v42, 16, v42\n\tv_sqrt_f32 v42, v42\n\t" \
    "v_cvt_pk_bf16_f32 v66, v66, v66\n\tv_lshlrev_b32 v66, 16, v66\n\tv_cvt_pk_bf16_f32 v42, v42, v42\n\tv_lshlrev_b32 v42, 16, v42\n\tv_rcp_f32 v42, v42\n\ts_nop 0\n\tv_mul_f32 v43, v66, v42\n\tv_xor_b32 v43, 0x80000000, v43\n\t" \
    "v_pk_fma_f32 v[24:25], v[24:25], v[42:43], v[42:43] op_sel:[0,0,1] op_sel_hi:[1,0,1]\n\tv_pk_fma_f32 v[26:27], v[26:27], v[42:43], v[42:43] op_sel:[0,0,1] op_sel_hi:[1,0,1]\n\tv_pk_fma_f32 v[28:29], v[28:29], v[42:43], v[42:43] op_sel:[0,0,1] op_sel_hi:[1,0,1]\n\tv_pk_fma_f32 v[30:31], v[30:31], v[42:43], v[42:43] op_sel:[0,0,1] op_sel_hi:[1,0,1]\n\tv_pk_fma_f32 v[32:33], v[32:33], v[42:43], v[42:43] op_sel:[0,0,1] op_sel_hi:[1,0,1]\n\tv_pk_fma_f32 v[34:35], v[34:35], v[42:43], v[42:43] op_sel:[0,0,1] op_sel_hi:[1,0,1]\n\tv_pk_fma_f32 v[36:37], v[36:37], v[42:43], v[42:43] op_sel:[0,0,1] op_sel_hi:[1,0,1]\n\tv_pk_fma_f32 v[38:39], v[38:39], v[42:43], v[42:43] op_sel:[0,0,1] op_sel_hi:[1,0,1]\n\t" \
    "v_pk_fma_f32 v[40:41], v[40:41], v[42:43], v[42:43] op_sel:[0,0,1] op_sel_hi:[1,0,1]\n\tv_cvt_pk_bf16_f32 v48, v24, v25\n\tv_cvt_pk_bf16_f32 v49, v26, v27\n\tv_cvt_pk_bf16_f32 v50, v28, v29\n\tv_cvt_pk_bf16_f32 v51, v30, v31\n\tv_cvt_pk_bf16_f32 v52, v32, v33\n\tv_cvt_pk_bf16_f32 v53, v34, v35\n\tv_cvt_pk_bf16_f32 v54, v36, v37\n\t" \
    "v_cvt_pk_bf16_f32 v55, v38, v39\n\tv_cvt_pk_bf16_f32 v56, v40, v41\n\tds_write_b128 v90, v[48:51]\n\tds_write_b128 v90, v[52:55] offset:512\n\tds_write_b32 v91, v56 offset:1024\n\ts_waitcnt vmcnt(11) lgkmcnt(0)\n\ts_barrier\n\tds_read_b64_tr_b16 v[24:25], v94 offset:0\n\t" \
    "ds_read_b64_tr_b16 v[26:27], v94 offset:4672\n\tds_read_b64_tr_b16 v[28:29], v94 offset:512\n\tds_read_b64_tr_b16 v[30:31], v94 offset:5184\n\tds_read_b64_tr_b16 v[32:33], v95 offset:1024\n\tds_read_b64_tr_b16 v[34:35], v95 offset:5696\n\tbuffer_store_dwordx2 v[68:69], v89, s[8:11], s64 offen sc1\n\tv_readfirstlane_b32 s36, v58\n\tv_readfirstlane_b32 s37, v59\n\t" \
    "s_lshl_b32 s73, s38, 12\n\ts_add_u32 s68, s61, s73\n\ts_add_u32 s36, s36, 0x7f4a7c15\n\ts_addc_u32 s37, s37, 0x9e3779b9\n\ts_waitcnt lgkmcnt(4)\n\tbuffer_store_dwordx4 v[24:27], v88, s[8:11], s68 offen sc1\n\ts_add_u32 s68, s68, 16384\n\ts_waitcnt lgkmcnt(2)\n\t" \
    "buffer_store_dwordx4 v[28:31], v88, s[8:11], s68 offen sc1\n\ts_add_u32 s68, s61, 32768\n\ts_cmp_eq_u32 s38, 0\n\ts_cselect_b32 s68, s68, 0x7ff00000\n\ts_waitcnt lgkmcnt(0)\n\tbuffer_store_dwordx4 v[32:35], v88, s[8:11], s68 offen sc1\n\tv_mov_b64 v[248:249], s[36:37]\n\tv_and_b32 v89, 7, v240\n\t" \
    "v_lshlrev_b32 v89, 11, v89\n\ts_cmp_ge_u32 s43, 21\n\ts_cselect_b32 s69, s70, s43\n\ts_mul_i32 s69, s69, 294912\n\ts_add_u32 s69, s69, 0\n\ts_add_u32 m0, s54, 8192\n\ts_nop 0\n\tbuffer_load_dwordx4 v11, s[12:15], s69 offen lds\n\t" \
    "s_xor_b32 s52, s72, 4\n\ts_mul_i32 s72, s72, 6656\n\ts_add_u32 s72, s72, 22347776\n\ts_mul_i32 s52, s52, 6656\n\ts_add_u32 s52, s52, 22347776\n\tv_mov_b32 v246, 0xbdd2d3e8\n\tv_mov_b32 v247, 0xc0135761\n\ts_lshr_b32 s65, s40, 3\n\t" \
    "s_mul_i32 s66, s65, 43\n\ts_lshr_b32 s66, s66, 7\n\ts_mul_i32 s6, s66, 3\n\ts_sub_u32 s6, s65, s6\n\ts_and_b32 s66, s66, 7\n\ts_and_b32 s7, s4, 1\n\ts_lshl_b32 s7, s7, 3\n\ts_add_u32 s7, s7, s66\n\t" \
    "s_mul_i32 s65, s5, 4718592\n\ts_lshl_b32 s67, s7, 8\n\ts_add_u32 s65, s65, s67\n\ts_add_u32 s16, s16, s65\n\ts_addc_u32 s17, s17, 0\n\ts_sub_u32 s18, s18, s65\n\ts_cmp_eq_u32 s39, 2\n\ts_cselect_b32 s18, s18, 0\n\t" \
    "v_lshrrev_b32 v16, 3, v240\n\ts_lshl_b32 s65, s38, 3\n\tv_add_u32 v16, s65, v16\n\tv_and_b32 v17, 0x70, v8\n\tv_lshlrev_b32 v18, 5, v245\n\tv_xor_b32 v17, v17, v18\n\tv_lshl_add_u32 v16, v16, 12, v17\n\ts_lshr_b32 s65, s5, 1\n\t" \
    "s_cmp_ge_u32 s43, 20\n\ts_cselect_b32 s69, s70, s43\n\ts_mul_i32 s69, s69, 294912\n\ts_add_u32 s69, s69, 294912\n\ts_add_u32 m0, s54, 12288\n\ts_nop 0\n\tbuffer_load_dwordx4 v11, s[12:15], s69 offen lds\n\ts_lshl_b32 s65, s65, 7\n\t" \
    "v_add_u32 v13, s65, v16\n\tv_xor_b32 v12, 128, v13\n\ts_lshl_b32 s65, s7, 7\n\ts_lshl_b32 s66, s5, 5\n\ts_add_u32 s65, s65, s66\n\ts_lshl_b32 s59, s65, 3\n\ts_add_u32 s59, s59, 58368\n\ts_lshl_b32 s66, s65, 1\n\t" \
    "s_add_u32 s32, s32, s66\n\ts_addc_u32 s33, s33, 0\n\ts_sub_u32 s34, s34, s66\n\ts_lshl_b32 s66, s6, 2\n\ts_add_u32 s66, s66, s38\n\ts_mul_i32 s67, s66, 96\n\ts_mul_i32 s65, s65, 1152\n\ts_add_u32 s65, s65, s67\n\t" \
    "s_add_u32 s24, s24, s65\n\ts_addc_u32 s25, s25, 0\n\ts_sub_u32 s26, s26, s65\n\ts_add_u32 s20, s20, s65\n\ts_addc_u32 s21, s21, 0\n\ts_sub_u32 s22, s22, s65\n\ts_lshl_b32 s51, s67, 2\n\ts_add_u32 s51, s51, 50176\n\t" \
    "s_lshl_b32 s66, s66, 2\n\ts_add_u32 s66, s66, s5\n\ts_mul_i32 s46, s66, 110592\n\ts_add_u32 s46, s46, 16646144\n\ts_lshl_b32 s50, s66, 13\n\ts_add_u32 s50, s50, 21954560\n\ts_mul_i32 s65, s5, 9\n\ts_sub_u32 s65, s42, s65\n\t" \
    "s_cmp_ge_u32 s43, 19\n\ts_cselect_b32 s69, s70, s43\n\ts_mul_i32 s69, s69, 294912\n\ts_add_u32 s69, s69, 589824\n\ts_add_u32 m0, s54, 16384\n\ts_nop 0\n\tbuffer_load_dwordx4 v10, s[12:15], s69 offen lds\n\ts_lshl_b32 s65, s65, 1\n\t" \
    "s_add_u32 s65, s65, s57\n\ts_mov_b32 s4, s65\n\ts_lshl_b32 s49, s65, 7\n\ts_add_u32 s49, s49, s50\n\ts_mul_i32 s45, s65, 3072\n\ts_add_u32 s45, s45, s46\n\ts_cmp_eq_u32 s39, 2\n\ts_cselect_b32 s46, s46, 2146435072\n\t" \
    "v_and_b32 v16, 31, v240\n\tv_mul_u32_u24 v243, 1152, v16\n\tv_lshrrev_b32 v16, 5, v240\n\tv_lshl_add_u32 v243, v16, 4, v243\n\tv_lshrrev_b32 v242, 1, v244\n\tv_lshlrev_b32 v236, 2, v240\n\tv_min_u32 v241, 17, v240\n\tv_lshlrev_b32 v241, 7, v241\n\t" \
    "s_lshl_b32 s65, s6, 4\n\ts_add_u32 s65, s65, s7\n\ts_lshl_b32 s65, s65, 2\n\ts_add_u32 s65, s65, s5\n\ts_lshl_b32 s65, s65, 2\n\ts_add_u32 s53, s65, s38\n\ts_mul_i32 s55, s53, 9984\n\ts_add_u32 s55, s55, 7208960\n\t" \
    "s_cmp_eq_u32 s39, 2\n\ts_cselect_b32 s55, s55, 2146435072\n\ts_mul_i32 s56, s53, 3072\n\ts_add_u32 s56, s56, 4849664\n\ts_or_b32 s65, s40, s38\n\ts_cselect_b32 s60, 2146435072, 0\n\ts_cmp_ge_u32 s43, 18\n\ts_cselect_b32 s69, s70, s43\n\t" \
    "s_mul_i32 s69, s69, 294912\n\ts_add_u32 s69, s69, 884736\n\ts_add_u32 m0, s54, 20480\n\ts_nop 0\n\tbuffer_load_dwordx4 v10, s[12:15], s69 offen lds\n\ts_cmp_ge_u32 s43, 19\n\ts_cselect_b32 s69, s70, s43\n\ts_mul_i32 s69, s69, 294912\n\t" \
    "s_add_u32 s69, s69, 589824\n\ts_add_u32 m0, s54, 24576\n\ts_nop 0\n\tbuffer_load_dwordx4 v11, s[12:15], s69 offen lds\n\ts_cmp_ge_u32 s43, 18\n\ts_cselect_b32 s69, s70, s43\n\ts_mul_i32 s69, s69, 294912\n\ts_add_u32 s69, s69, 884736\n\t" \
    "s_add_u32 m0, s54, 28672\n\ts_nop 0\n\tbuffer_load_dwordx4 v11, s[12:15], s69 offen lds\n\ts_cmp_ge_u32 s43, 17\n\ts_cselect_b32 s69, s70, s43\n\ts_mul_i32 s69, s69, 294912\n\ts_add_u32 s69, s69, 1179648\n\ts_add_u32 m0, s54, 32768\n\t" \
    "s_nop 0\n\tbuffer_load_dwordx4 v10, s[12:15], s69 offen lds\n\ts_cmp_ge_u32 s43, 16\n\ts_cselect_b32 s69, s70, s43\n\ts_mul_i32 s69, s69, 294912\n\ts_add_u32 s69, s69, 1474560\n\ts_add_u32 m0, s54, 36864\n\ts_nop 0\n\t" \
    "buffer_load_dwordx4 v10, s[12:15], s69 offen lds\n\ts_waitcnt vmcnt(14)\n\ts_barrier\n\tds_read_b64_tr_b16 v[36:37], v93 offset:0\n\tds_read_b64_tr_b16 v[38:39], v93 offset:4608\n\tds_read_b64_tr_b16 v[40:41], v93 offset:9216\n\tds_read_b64_tr_b16 v[42:43], v93 offset:13312\n\tds_read_b64_tr_b16 v[44:45], v93 offset:17920\n\t" \
    "ds_read_b64_tr_b16 v[46:47], v93 offset:512\n\tds_read_b64_tr_b16 v[48:49], v93 offset:5120\n\tds_read_b64_tr_b16 v[50:51], v93 offset:9728\n\tds_read_b64_tr_b16 v[52:53], v93 offset:13824\n\tds_read_b64_tr_b16 v[54:55], v93 offset:18432\n\tds_read_b64_tr_b16 v[120:121], v92 offset:1024\n\tds_read_b64_tr_b16 v[122:123], v92 offset:5632\n\tds_read_b64_tr_b16 v[124:125], v92 offset:10240\n\t" \
    "ds_read_b64_tr_b16 v[126:127], v92 offset:14336\n\tds_read_b64_tr_b16 v[128:129], v92 offset:18944\n\tv_lshlrev_b32 v78, 16, v60\n\tv_and_b32 v79, 0xffff0000, v60\n\tv_lshlrev_b32 v80, 16, v61\n\tv_and_b32 v81, 0xffff0000, v61\n\ts_waitcnt lgkmcnt(10)\n\tv_lshlrev_b32 v66, 16, v36\n\t" \
    "v_and_b32 v67, 0xffff0000, v36\n\tv_lshlrev_b32 v68, 16, v37\n\tv_and_b32 v69, 0xffff0000, v37\n\tv_lshlrev_b32 v70, 16, v38\n\tv_and_b32 v71, 0xffff0000, v38\n\tv_lshlrev_b32 v72, 16, v39\n\tv_and_b32 v73, 0xffff0000, v39\n\tv_lshlrev_b32 v74, 16, v40\n\t" \
    "v_and_b32 v75, 0xffff0000, v40\n\tv_lshlrev_b32 v76, 16, v41\n\tv_mul_f32 v16, v66, v81\n\tv_mul_f32 v17, v67, v81\n\tv_mul_f32 v18, v68, v81\n\tv_mul_f32 v19, v69, v81\n\tv_mul_f32 v20, v70, v81\n\tv_mul_f32 v21, v71, v81\n\t" \
    "v_mul_f32 v22, v72, v81\n\tv_mul_f32 v23, v73, v81\n\tv_fmac_f32 v16, v67, v80\n\tv_fmac_f32 v17, v68, v80\n\tv_fmac_f32 v18, v69, v80\n\tv_fmac_f32 v19, v70, v80\n\tv_fmac_f32 v20, v71, v80\n\tv_fmac_f32 v21, v72, v80\n\t" \
    "v_fmac_f32 v22, v73, v80\n\tv_fmac_f32 v23, v74, v80\n\tv_fmac_f32 v16, v68, v79\n\tv_fmac_f32 v17, v69, v79\n\tv_fmac_f32 v18, v70, v79\n\tv_fmac_f32 v19, v71, v79\n\tv_fmac_f32 v20, v72, v79\n\tv_fmac_f32 v21, v73, v79\n\t" \
    "v_fmac_f32 v22, v74, v79\n\tv_fmac_f32 v23, v75, v79\n\tv_fmac_f32 v16, v69, v78\n\tv_fmac_f32 v17, v70, v78\n\tv_fmac_f32 v18, v71, v78\n\tv_fmac_f32 v19, v72, v78\n\tv_fmac_f32 v20, v73, v78\n\tv_fmac_f32 v21, v74, v78\n\t" \
    "v_fmac_f32 v22, v75, v78\n\tv_fmac_f32 v23, v76, v78\n\tv_cvt_pk_bf16_f32 v66, v16, v17\n\tv_cvt_pk_bf16_f32 v67, v18, v19\n\tv_cvt_pk_bf16_f32 v68, v20, v21\n\tv_cvt_pk_bf16_f32 v69, v22, v23\n\tv_lshlrev_b32 v16, 16, v66\n\tv_and_b32 v17, 0xffff0000, v66\n\t" \
    "v_lshlrev_b32 v18, 16, v67\n\tv_and_b32 v19, 0xffff0000, v67\n\tv_lshlrev_b32 v20, 16, v68\n\tv_and_b32 v21, 0xffff0000, v68\n\tv_lshlrev_b32 v22, 16, v69\n\tv_and_b32 v23, 0xffff0000, v69\n\tv_lshlrev_b32 v66, 16, v42\n\tv_and_b32 v67, 0xffff0000, v42\n\t" \
    "v_lshlrev_b32 v68, 16, v43\n\tv_and_b32 v69, 0xffff0000, v43\n\tv_lshlrev_b32 v70, 16, v44\n\tv_and_b32 v71, 0xffff0000, v44\n\tv_lshlrev_b32 v72, 16, v45\n\tv_and_b32 v73, 0xffff0000, v45\n\tv_pk_mul_f32 v[16:17], v[16:17], v[66:67]\n\tv_pk_mul_f32 v[18:19], v[18:19], v[68:69]\n\t" \
    "v_pk_mul_f32 v[20:21], v[20:21], v[70:71]\n\tv_pk_mul_f32 v[22:23], v[22:23], v[72:73]\n\tv_cvt_pk_bf16_f32 v24, v16, v17\n\tv_cvt_pk_bf16_f32 v25, v18, v19\n\tv_cvt_pk_bf16_f32 v26, v20, v21\n\tv_cvt_pk_bf16_f32 v27, v22, v23\n\ts_add_u32 s68, s62, s54\n\tbuffer_store_dwordx4 v[24:27], v8, s[8:11], s68 offen sc1\n\t" \
    "s_waitcnt vmcnt(9) lgkmcnt(0)\n\ts_barrier\n\tbuffer_store_dwordx2 v[248:249], v89, s[8:11], s63 offen sc1\n\ts_cmp_lg_u32 s39, 0\n\ts_cbranch_scc1 104f\n\ts_sub_u32 s73, s40, 216\n\ts_lshl_b32 s71, s73, 4\n\ts_mul_i32 s68, s73, 12\n\t" \
    "s_add_u32 s68, s68, 96\n\ts_cmp_lt_u32 s73, 24\n\ts_cselect_b32 s71, s71, s68\n\ts_cselect_b32 s73, 0, 2146435072\n\tv_cmp_eq_u32 vcc, 0, v240\n\tv_subrev_u32 v190, 8, v8\n\tv_mov_b32 v191, 2146435072\n\tv_cndmask_b32 v190, v190, v191, vcc\n\t" \
    "s_add_u32 s68, s71, s38\n\ts_add_u32 s68, s68, 0\n\ts_lshl_b32 s73, s68, 1\n\ts_lshl_b32 s68, s68, 12\n\tbuffer_load_dwordx4 v[164:167], v8, s[76:79], s68 offen offset:0\n\tbuffer_load_dwordx4 v[168:171], v8, s[76:79], s68 offen offset:1024\n\tbuffer_load_dwordx4 v[172:175], v8, s[76:79], s68 offen offset:2048\n\tbuffer_load_dwordx4 v[176:179], v8, s[76:79], s68 offen offset:3072\n\t" \
    "buffer_load_dwordx2 v[180:181], v190, s[76:79], s68 offen\n\tbuffer_load_dwordx2 v[182:183], v8, s[76:79], s68 offen offset:1016\n\tbuffer_load_dwordx2 v[184:185], v8, s[76:79], s68 offen offset:2040\n\tbuffer_load_dwordx2 v[186:187], v8, s[76:79], s68 offen offset:3064\n\tbuffer_load_ushort v188, off, s[80:83], s73\n\t104:\n\tv_lshlrev_b32 v78, 16, v62\n\ts_cmp_ge_u32 s43, 17\n\t" \
    "s_cselect_b32 s69, s70, s43\n\ts_mul_i32 s69, s69, 294912\n\ts_add_u32 s69, s69, 1179648\n\ts_add_u32 m0, s54, 40960\n\tv_and_b32 v79, 0xffff0000, v62\n\tv_lshlrev_b32 v80, 16, v63\n\tv_and_b32 v81, 0xffff0000, v63\n\tv_lshlrev_b32 v66, 16, v46\n\t" \
    "v_and_b32 v67, 0xffff0000, v46\n\tv_lshlrev_b32 v68, 16, v47\n\tv_and_b32 v69, 0xffff0000, v47\n\tv_lshlrev_b32 v70, 16, v48\n\tv_and_b32 v71, 0xffff0000, v48\n\tv_lshlrev_b32 v72, 16, v49\n\tv_and_b32 v73, 0xffff0000, v49\n\tv_lshlrev_b32 v74, 16, v50\n\t" \
    "v_and_b32 v75, 0xffff0000, v50\n\tv_lshlrev_b32 v76, 16, v51\n\tv_mul_f32 v16, v66, v81\n\tv_mul_f32 v17, v67, v81\n\tv_mul_f32 v18, v68, v81\n\tv_mul_f32 v19, v69, v81\n\tv_mul_f32 v20, v70, v81\n\tv_mul_f32 v21, v71, v81\n\t" \
    "v_mul_f32 v22, v72, v81\n\tv_mul_f32 v23, v73, v81\n\tv_fmac_f32 v16, v67, v80\n\tv_fmac_f32 v17, v68, v80\n\tv_fmac_f32 v18, v69, v80\n\tv_fmac_f32 v19, v70, v80\n\tv_fmac_f32 v20, v71, v80\n\tv_fmac_f32 v21, v72, v80\n\t" \
    "v_fmac_f32 v22, v73, v80\n\tv_fmac_f32 v23, v74, v80\n\tv_fmac_f32 v16, v68, v79\n\tv_fmac_f32 v17, v69, v79\n\tv_fmac_f32 v18, v70, v79\n\tv_fmac_f32 v19, v71, v79\n\tv_fmac_f32 v20, v72, v79\n\tv_fmac_f32 v21, v73, v79\n\t" \
    "v_fmac_f32 v22, v74, v79\n\tv_fmac_f32 v23, v75, v79\n\tv_fmac_f32 v16, v69, v78\n\tv_fmac_f32 v17, v70, v78\n\tv_fmac_f32 v18, v71, v78\n\tv_fmac_f32 v19, v72, v78\n\tv_fmac_f32 v20, v73, v78\n\tv_fmac_f32 v21, v74, v78\n\t" \
    "v_fmac_f32 v22, v75, v78\n\tv_fmac_f32 v23, v76, v78\n\tv_cvt_pk_bf16_f32 v66, v16, v17\n\tv_cvt_pk_bf16_f32 v67, v18, v19\n\tv_cvt_pk_bf16_f32 v68, v20, v21\n\tv_cvt_pk_bf16_f32 v69, v22, v23\n\tv_lshlrev_b32 v16, 16, v66\n\tv_and_b32 v17, 0xffff0000, v66\n\t" \
    "v_lshlrev_b32 v18, 16, v67\n\tv_and_b32 v19, 0xffff0000, v67\n\tv_lshlrev_b32 v20, 16, v68\n\tv_and_b32 v21, 0xffff0000, v68\n\tv_lshlrev_b32 v22, 16, v69\n\tv_and_b32 v23, 0xffff0000, v69\n\tv_lshlrev_b32 v66, 16, v52\n\tv_and_b32 v67, 0xffff0000, v52\n\t" \
    "v_lshlrev_b32 v68, 16, v53\n\tv_and_b32 v69, 0xffff0000, v53\n\tv_lshlrev_b32 v70, 16, v54\n\tv_and_b32 v71, 0xffff0000, v54\n\tv_lshlrev_b32 v72, 16, v55\n\tv_and_b32 v73, 0xffff0000, v55\n\tv_pk_mul_f32 v[16:17], v[16:17], v[66:67]\n\tv_pk_mul_f32 v[18:19], v[18:19], v[68:69]\n\t" \
    "v_pk_mul_f32 v[20:21], v[20:21], v[70:71]\n\tv_pk_mul_f32 v[22:23], v[22:23], v[72:73]\n\tv_cvt_pk_bf16_f32 v28, v16, v17\n\tv_cvt_pk_bf16_f32 v29, v18, v19\n\tv_cvt_pk_bf16_f32 v30, v20, v21\n\tv_cvt_pk_bf16_f32 v31, v22, v23\n\ts_add_u32 s68, s62, s54\n\tbuffer_store_dwordx4 v[28:31], v9, s[8:11], s68 offen sc1\n\t" \
    "v_lshlrev_b32 v78, 16, v64\n\tv_and_b32 v79, 0xffff0000, v64\n\tv_lshlrev_b32 v80, 16, v65\n\tv_and_b32 v81, 0xffff0000, v65\n\tv_lshlrev_b32 v66, 16, v120\n\tv_and_b32 v67, 0xffff0000, v120\n\tv_lshlrev_b32 v68, 16, v121\n\tv_and_b32 v69, 0xffff0000, v121\n\t" \
    "v_lshlrev_b32 v70, 16, v122\n\tv_and_b32 v71, 0xffff0000, v122\n\tv_lshlrev_b32 v72, 16, v123\n\tv_and_b32 v73, 0xffff0000, v123\n\tv_lshlrev_b32 v74, 16, v124\n\tv_and_b32 v75, 0xffff0000, v124\n\tv_lshlrev_b32 v76, 16, v125\n\tv_mul_f32 v16, v66, v81\n\t" \
    "v_mul_f32 v17, v67, v81\n\tv_mul_f32 v18, v68, v81\n\tv_mul_f32 v19, v69, v81\n\tv_mul_f32 v20, v70, v81\n\tv_mul_f32 v21, v71, v81\n\tv_mul_f32 v22, v72, v81\n\tv_mul_f32 v23, v73, v81\n\tv_fmac_f32 v16, v67, v80\n\t" \
    "v_fmac_f32 v17, v68, v80\n\tv_fmac_f32 v18, v69, v80\n\tv_fmac_f32 v19, v70, v80\n\tv_fmac_f32 v20, v71, v80\n\tv_fmac_f32 v21, v72, v80\n\tv_fmac_f32 v22, v73, v80\n\tv_fmac_f32 v23, v74, v80\n\tv_fmac_f32 v16, v68, v79\n\t" \
    "v_fmac_f32 v17, v69, v79\n\tv_fmac_f32 v18, v70, v79\n\tv_fmac_f32 v19, v71, v79\n\tv_fmac_f32 v20, v72, v79\n\tv_fmac_f32 v21, v73, v79\n\tv_fmac_f32 v22, v74, v79\n\tv_fmac_f32 v23, v75, v79\n\tv_fmac_f32 v16, v69, v78\n\t" \
    "v_fmac_f32 v17, v70, v78\n\tv_fmac_f32 v18, v71, v78\n\tv_fmac_f32 v19, v72, v78\n\tv_fmac_f32 v20, v73, v78\n\tv_fmac_f32 v21, v74, v78\n\tv_fmac_f32 v22, v75, v78\n\tv_fmac_f32 v23, v76, v78\n\tv_cvt_pk_bf16_f32 v66, v16, v17\n\t" \
    "v_cvt_pk_bf16_f32 v67, v18, v19\n\tv_cvt_pk_bf16_f32 v68, v20, v21\n\tv_cvt_pk_bf16_f32 v69, v22, v23\n\tv_lshlrev_b32 v16, 16, v66\n\tv_and_b32 v17, 0xffff0000, v66\n\tv_lshlrev_b32 v18, 16, v67\n\tv_and_b32 v19, 0xffff0000, v67\n\tv_lshlrev_b32 v20, 16, v68\n\t" \
    "v_and_b32 v21, 0xffff0000, v68\n\tv_lshlrev_b32 v22, 16, v69\n\tv_and_b32 v23, 0xffff0000, v69\n\tv_lshlrev_b32 v66, 16, v126\n\tv_and_b32 v67, 0xffff0000, v126\n\tv_lshlrev_b32 v68, 16, v127\n\tv_and_b32 v69, 0xffff0000, v127\n\tv_lshlrev_b32 v70, 16, v128\n\t" \
    "v_and_b32 v71, 0xffff0000, v128\n\tv_lshlrev_b32 v72, 16, v129\n\tv_and_b32 v73, 0xffff0000, v129\n\tv_pk_mul_f32 v[16:17], v[16:17], v[66:67]\n\tv_pk_mul_f32 v[18:19], v[18:19], v[68:69]\n\tv_pk_mul_f32 v[20:21], v[20:21], v[70:71]\n\tv_pk_mul_f32 v[22:23], v[22:23], v[72:73]\n\tv_cvt_pk_bf16_f32 v32, v16, v17\n\t" \
    "v_cvt_pk_bf16_f32 v33, v18, v19\n\tv_cvt_pk_bf16_f32 v34, v20, v21\n\tv_cvt_pk_bf16_f32 v35, v22, v23\n\ts_add_u32 s68, s62, 8192\n\ts_cmp_eq_u32 s38, 1\n\ts_cselect_b32 s68, s68, 0x7ff00000\n\tbuffer_store_dwordx4 v[32:35], v8, s[8:11], s68 offen sc1\n\ts_cmp_eq_u32 s39, 0\n\t" \
    "s_cbranch_scc1 101f\n\tbuffer_load_dwordx4 v11, s[12:15], s69 offen lds\n\ts_cmp_ge_u32 s43, 16\n\ts_cselect_b32 s69, s70, s43\n\ts_mul_i32 s69, s69, 294912\n\ts_add_u32 s69, s69, 1474560\n\ts_add_u32 m0, s54, 45056\n\ts_nop 0\n\t" \
    "buffer_load_dwordx4 v11, s[12:15], s69 offen lds\n\ts_cmp_ge_u32 s43, 15\n\ts_cselect_b32 s69, s70, s43\n\ts_mul_i32 s69, s69, 294912\n\ts_add_u32 s69, s69, 1769472\n\ts_add_u32 m0, s54, 49152\n\ts_nop 0\n\tbuffer_load_dwordx4 v10, s[12:15], s69 offen lds\n\t" \
    "s_waitcnt vmcnt(3)\n\tbuffer_store_dwordx2 v[248:249], off, s[8:11], s48 sc1\n\ts_cmp_ge_u32 s43, 14\n\ts_cselect_b32 s69, s70, s43\n\ts_mul_i32 s69, s69, 294912\n\ts_add_u32 s69, s69, 2064384\n\ts_add_u32 m0, s54, 53248\n\ts_nop 0\n\t" \
    "buffer_load_dwordx4 v10, s[12:15], s69 offen lds\n\ts_cmp_ge_u32 s43, 15\n\ts_cselect_b32 s69, s70, s43\n\ts_mul_i32 s69, s69, 294912\n\ts_add_u32 s69, s69, 1769472\n\ts_add_u32 m0, s54, 57344\n\ts_nop 0\n\tbuffer_load_dwordx4 v11, s[12:15], s69 offen lds\n\t" \
    "s_cmp_ge_u32 s43, 14\n\ts_cselect_b32 s69, s70, s43\n\ts_mul_i32 s69, s69, 294912\n\ts_add_u32 s69, s69, 2064384\n\ts_add_u32 m0, s54, 61440\n\ts_nop 0\n\tbuffer_load_dwordx4 v11, s[12:15], s69 offen lds\n\ts_cmp_ge_u32 s43, 13\n\t" \
    "s_cselect_b32 s69, s70, s43\n\ts_mul_i32 s69, s69, 294912\n\ts_add_u32 s69, s69, 2359296\n\ts_add_u32 m0, s54, 65536\n\ts_nop 0\n\tbuffer_load_dwordx4 v10, s[12:15], s69 offen lds\n\ts_cmp_ge_u32 s43, 12\n\ts_cselect_b32 s69, s70, s43\n\t" \
    "s_mul_i32 s69, s69, 294912\n\ts_add_u32 s69, s69, 2654208\n\ts_add_u32 m0, s54, 69632\n\ts_nop 0\n\tbuffer_load_dwordx4 v10, s[12:15], s69 offen lds\n\ts_cmp_ge_u32 s43, 13\n\ts_cselect_b32 s69, s70, s43\n\ts_mul_i32 s69, s69, 294912\n\t" \
    "s_add_u32 s69, s69, 2359296\n\ts_add_u32 m0, s54, 73728\n\ts_nop 0\n\tbuffer_load_dwordx4 v11, s[12:15], s69 offen lds\n\ts_cmp_ge_u32 s43, 12\n\ts_cselect_b32 s69, s70, s43\n\ts_mul_i32 s69, s69, 294912\n\ts_add_u32 s69, s69, 2654208\n\t" \
    "s_add_u32 m0, s54, 77824\n\ts_nop 0\n\tbuffer_load_dwordx4 v11, s[12:15], s69 offen lds\n\ts_cmp_ge_u32 s43, 11\n\ts_cselect_b32 s69, s70, s43\n\ts_mul_i32 s69, s69, 294912\n\ts_add_u32 s69, s69, 2949120\n\ts_add_u32 m0, s54, 81920\n\t" \
    "s_nop 0\n\tbuffer_load_dwordx4 v10, s[12:15], s69 offen lds\n\ts_lshl_b32 s71, s57, 7\n\ts_lshl_b32 s69, s42, 8\n\ts_add_u32 s71, s71, s69\n\tv_lshlrev_b32 v96, 3, v245\n\tbuffer_load_dwordx2 v[112:113], v96, s[28:31], s71 offen offset:0\n\tbuffer_load_dwordx2 v[114:115], v96, s[28:31], s71 offen offset:32\n\t" \
    "buffer_load_dwordx2 v[116:117], v96, s[28:31], s71 offen offset:64\n\tbuffer_load_dwordx2 v[118:119], v96, s[28:31], s71 offen offset:96\n\tv_lshrrev_b32 v229, 3, v244\n\tbuffer_load_ushort v228, v229, s[32:35], 0 offen offset:0\n\tbuffer_load_ushort v232, v229, s[32:35], 0 offen offset:32\n\ts_cmp_ge_u32 s43, 10\n\ts_cselect_b32 s69, s70, s43\n\ts_mul_i32 s69, s69, 294912\n\t" \
    "s_add_u32 s69, s69, 3244032\n\ts_add_u32 m0, s54, 86016\n\ts_nop 0\n\tbuffer_load_dwordx4 v10, s[12:15], s69 offen lds\n\tbuffer_load_dwordx4 v[250:253], v8, s[8:11], s47 offen sc1\n\ts_cmp_ge_u32 s43, 11\n\ts_cselect_b32 s69, s70, s43\n\ts_mul_i32 s69, s69, 294912\n\t" \
    "s_add_u32 s69, s69, 2949120\n\ts_add_u32 m0, s54, 90112\n\ts_nop 0\n\tbuffer_load_dwordx4 v11, s[12:15], s69 offen lds\n\ts_cmp_ge_u32 s43, 10\n\ts_cselect_b32 s69, s70, s43\n\ts_mul_i32 s69, s69, 294912\n\ts_add_u32 s69, s69, 3244032\n\t" \
    "s_add_u32 m0, s54, 94208\n\ts_nop 0\n\tbuffer_load_dwordx4 v11, s[12:15], s69 offen lds\n\ts_cmp_ge_u32 s43, 9\n\ts_cselect_b32 s69, s70, s43\n\ts_mul_i32 s69, s69, 294912\n\ts_add_u32 s69, s69, 3538944\n\ts_add_u32 m0, s54, 98304\n\t" \
    "s_nop 0\n\tbuffer_load_dwordx4 v10, s[12:15], s69 offen lds\n\ts_cmp_ge_u32 s43, 8\n\ts_cselect_b32 s69, s70, s43\n\ts_mul_i32 s69, s69, 294912\n\ts_add_u32 s69, s69, 3833856\n\ts_add_u32 m0, s54, 102400\n\ts_nop 0\n\t" \
    "buffer_load_dwordx4 v10, s[12:15], s69 offen lds\n\ts_cmp_ge_u32 s43, 7\n\ts_cselect_b32 s69, s70, s43\n\ts_mul_i32 s69, s69, 294912\n\ts_add_u32 s69, s69, 4128768\n\ts_add_u32 m0, s54, 106496\n\ts_nop 0\n\tbuffer_load_dwordx4 v10, s[12:15], s69 offen lds\n\t" \
    "s_cmp_ge_u32 s43, 6\n\ts_cselect_b32 s69, s70, s43\n\ts_mul_i32 s69, s69, 294912\n\ts_add_u32 s69, s69, 4423680\n\ts_add_u32 m0, s54, 110592\n\ts_nop 0\n\tbuffer_load_dwordx4 v10, s[12:15], s69 offen lds\n\ts_cmp_ge_u32 s43, 5\n\t" \
    "s_cselect_b32 s69, s70, s43\n\ts_mul_i32 s69, s69, 294912\n\ts_add_u32 s69, s69, 4718592\n\ts_add_u32 m0, s54, 114688\n\ts_nop 0\n\tbuffer_load_dwordx4 v10, s[12:15], s69 offen lds\n\ts_cmp_ge_u32 s43, 4\n\ts_cselect_b32 s69, s70, s43\n\t" \
    "s_mul_i32 s69, s69, 294912\n\ts_add_u32 s69, s69, 5013504\n\ts_add_u32 m0, s54, 118784\n\ts_nop 0\n\tbuffer_load_dwordx4 v10, s[12:15], s69 offen lds\n\ts_cmp_ge_u32 s43, 3\n\ts_cselect_b32 s69, s70, s43\n\ts_mul_i32 s69, s69, 294912\n\t" \
    "s_add_u32 s69, s69, 5308416\n\ts_add_u32 m0, s54, 122880\n\ts_nop 0\n\tbuffer_load_dwordx4 v10, s[12:15], s69 offen lds\n\ts_cmp_ge_u32 s43, 2\n\ts_cselect_b32 s69, s70, s43\n\ts_mul_i32 s69, s69, 294912\n\ts_add_u32 s69, s69, 5603328\n\t" \
    "s_add_u32 m0, s54, 126976\n\ts_nop 0\n\tbuffer_load_dwordx4 v10, s[12:15], s69 offen lds\n\ts_waitcnt vmcnt(10)\n\tv_cmp_ne_u64 vcc, s[36:37], v[250:251]\n\tv_cmp_ne_u64 s[74:75], s[36:37], v[252:253]\n\ts_or_b64 s[74:75], s[74:75], vcc\n\ts_cbranch_scc0 106f\n\t" \
    "s_mov_b32 s71, 1048576\n\t105:\n\ts_sleep 1\n\tbuffer_load_dwordx4 v[250:253], v8, s[8:11], s47 offen sc1\n\ts_waitcnt vmcnt(0)\n\tv_cmp_ne_u64 vcc, s[36:37], v[250:251]\n\tv_cmp_ne_u64 s[74:75], s[36:37], v[252:253]\n\ts_or_b64 s[74:75], s[74:75], vcc\n\t" \
    "s_cbranch_scc0 106f\n\ts_sub_u32 s71, s71, 1\n\ts_cbranch_scc0 105b\n\t106:\n\ts_cmp_ge_u32 s43, 21\n\ts_cselect_b32 s68, s70, s43\n\ts_mul_i32 s68, s68, 36864\n\ts_add_u32 s68, s68, s44\n\t" \
    "s_add_u32 s68, s68, 0\n\tbuffer_load_dwordx4 v[192:195], v8, s[8:11], s68 offen offset:0 sc1\n\tbuffer_load_dwordx4 v[196:199], v8, s[8:11], s68 offen offset:1024 sc1\n\tbuffer_load_dwordx4 v[200:203], v8, s[8:11], s68 offen offset:2048 sc1\n\ts_cmp_ge_u32 s43, 20\n\ts_cselect_b32 s68, s70, s43\n\ts_mul_i32 s68, s68, 36864\n\ts_add_u32 s68, s68, s44\n\t" \
    "s_add_u32 s68, s68, 36864\n\tbuffer_load_dwordx4 v[168:171], v8, s[8:11], s68 offen offset:0 sc1\n\tbuffer_load_dwordx4 v[172:175], v8, s[8:11], s68 offen offset:1024 sc1\n\tbuffer_load_dwordx4 v[176:179], v8, s[8:11], s68 offen offset:2048 sc1\n\ts_cmp_ge_u32 s43, 19\n\ts_cselect_b32 s68, s70, s43\n\ts_mul_i32 s68, s68, 36864\n\ts_add_u32 s68, s68, s44\n\t" \
    "s_add_u32 s68, s68, 73728\n\tbuffer_load_dwordx4 v[180:183], v8, s[8:11], s68 offen offset:0 sc1\n\tbuffer_load_dwordx4 v[184:187], v8, s[8:11], s68 offen offset:1024 sc1\n\tbuffer_load_dwordx4 v[188:191], v8, s[8:11], s68 offen offset:2048 sc1\n\ts_cmp_ge_u32 s43, 18\n\ts_cselect_b32 s68, s70, s43\n\ts_mul_i32 s68, s68, 36864\n\ts_add_u32 s68, s68, s44\n\t" \
    "s_add_u32 s68, s68, 110592\n\tbuffer_load_dwordx4 v[204:207], v8, s[8:11], s68 offen offset:0 sc1\n\tbuffer_load_dwordx4 v[208:211], v8, s[8:11], s68 offen offset:1024 sc1\n\tbuffer_load_dwordx4 v[212:215], v8, s[8:11], s68 offen offset:2048 sc1\n\tv_lshlrev_b32 v96, 16, v112\n\tv_and_b32 v97, 0xffff0000, v112\n\tv_lshlrev_b32 v98, 16, v113\n\tv_and_b32 v99, 0xffff0000, v113\n\t" \
    "v_lshlrev_b32 v100, 16, v114\n\tv_and_b32 v101, 0xffff0000, v114\n\tv_lshlrev_b32 v102, 16, v115\n\tv_and_b32 v103, 0xffff0000, v115\n\tv_lshlrev_b32 v104, 16, v116\n\tv_and_b32 v105, 0xffff0000, v116\n\tv_lshlrev_b32 v106, 16, v117\n\tv_and_b32 v107, 0xffff0000, v117\n\t" \
    "v_lshlrev_b32 v108, 16, v118\n\tv_and_b32 v109, 0xffff0000, v118\n\tv_lshlrev_b32 v110, 16, v119\n\tv_and_b32 v111, 0xffff0000, v119\n\tv_lshlrev_b32 v228, 16, v228\n\tv_mov_b32 v229, v228\n\tv_mov_b32 v230, v228\n\tv_mov_b32 v231, v228\n\t" \
    "v_lshlrev_b32 v232, 16, v232\n\tv_mov_b32 v233, v232\n\tv_mov_b32 v234, v232\n\tv_mov_b32 v235, v232\n\ts_barrier\n\tds_read_b64_tr_b16 v[16:17], v0 offset:0\n\tds_read_b64_tr_b16 v[18:19], v0 offset:512\n\tds_read_b64_tr_b16 v[20:21], v0 offset:4096\n\t" \
    "ds_read_b64_tr_b16 v[22:23], v0 offset:4608\n\tds_read_b64_tr_b16 v[24:25], v1 offset:0\n\tds_read_b64_tr_b16 v[26:27], v1 offset:512\n\tds_read_b64_tr_b16 v[28:29], v1 offset:4096\n\tds_read_b64_tr_b16 v[30:31], v1 offset:4608\n\tds_read_b64_tr_b16 v[32:33], v2 offset:0\n\tds_read_b64_tr_b16 v[34:35], v2 offset:512\n\tds_read_b64_tr_b16 v[36:37], v2 offset:4096\n\t" \
    "ds_read_b64_tr_b16 v[38:39], v2 offset:4608\n\ts_waitcnt vmcnt(9)\n\ts_cmp_ge_u32 s43, 17\n\ts_cselect_b32 s68, s70, s43\n\ts_mul_i32 s68, s68, 36864\n\ts_add_u32 s68, s68, s44\n\ts_add_u32 s68, s68, 147456\n\tbuffer_load_dwordx4 v[216:219], v8, s[8:11], s68 offen offset:0 sc1\n\t" \
    "buffer_load_dwordx4 a[0:3], v8, s[8:11], s68 offen offset:1024 sc1\n\tbuffer_load_dwordx4 a[4:7], v8, s[8:11], s68 offen offset:2048 sc1\n\ts_cmp_ge_u32 s43, 16\n\ts_cselect_b32 s68, s70, s43\n\ts_mul_i32 s68, s68, 36864\n\ts_add_u32 s68, s68, s44\n\ts_add_u32 s68, s68, 184320\n\tbuffer_load_dwordx4 a[8:11], v8, s[8:11], s68 offen offset:0 sc1\n\t" \
    "buffer_load_dwordx4 a[12:15], v8, s[8:11], s68 offen offset:1024 sc1\n\ts_waitcnt vmcnt(11) lgkmcnt(8)\n\tv_mfma_f32_16x16x32_bf16 v[120:123], v[16:19], v[192:195], 0\n\tds_read_b64_tr_b16 v[40:41], v3 offset:0\n\tds_read_b64_tr_b16 v[42:43], v3 offset:512\n\tv_mfma_f32_16x16x32_bf16 v[120:123], v[20:23], v[168:171], v[120:123]\n\tbuffer_load_dwordx4 a[16:19], v8, s[8:11], s68 offen offset:2048 sc1\n\ts_cmp_ge_u32 s43, 15\n\t" \
    "s_cselect_b32 s68, s70, s43\n\ts_mul_i32 s68, s68, 36864\n\tv_mfma_f32_16x16x32_bf16 v[128:131], v[16:19], v[196:199], 0\n\ts_add_u32 s68, s68, s44\n\ts_add_u32 s68, s68, 221184\n\tv_mfma_f32_16x16x32_bf16 v[128:131], v[20:23], v[172:175], v[128:131]\n\tds_read_b64_tr_b16 v[44:45], v3 offset:4096\n\tds_read_b64_tr_b16 v[46:47], v3 offset:4608\n\t" \
    "v_mfma_f32_16x16x32_bf16 v[136:139], v[16:19], v[200:203], 0\n\tv_mfma_f32_16x16x32_bf16 v[136:139], v[20:23], v[176:179], v[136:139]\n\ts_waitcnt lgkmcnt(8)\n\tv_mfma_f32_16x16x32_bf16 v[124:127], v[24:27], v[192:195], 0\n\tds_read_b64_tr_b16 v[16:17], v0 offset:8192\n\tds_read_b64_tr_b16 v[18:19], v0 offset:8704\n\tv_mfma_f32_16x16x32_bf16 v[124:127], v[28:31], v[168:171], v[124:127]\n\tbuffer_load_dwordx4 a[20:23], v8, s[8:11], s68 offen offset:0 sc1\n\t" \
    "v_mfma_f32_16x16x32_bf16 v[132:135], v[24:27], v[196:199], 0\n\tv_mfma_f32_16x16x32_bf16 v[132:135], v[28:31], v[172:175], v[132:135]\n\tds_read_b64_tr_b16 v[20:21], v0 offset:12288\n\tds_read_b64_tr_b16 v[22:23], v0 offset:12800\n\tv_mfma_f32_16x16x32_bf16 v[140:143], v[24:27], v[200:203], 0\n\tbuffer_load_dwordx4 a[24:27], v8, s[8:11], s68 offen offset:1024 sc1\n\tv_mfma_f32_16x16x32_bf16 v[140:143], v[28:31], v[176:179], v[140:143]\n\ts_waitcnt lgkmcnt(8)\n\t" \
    "v_mfma_f32_16x16x32_bf16 v[144:147], v[32:35], v[192:195], 0\n\tds_read_b64_tr_b16 v[24:25], v1 offset:8192\n\tds_read_b64_tr_b16 v[26:27], v1 offset:8704\n\tv_mfma_f32_16x16x32_bf16 v[144:147], v[36:39], v[168:171], v[144:147]\n\tbuffer_load_dwordx4 a[28:31], v8, s[8:11], s68 offen offset:2048 sc1\n\ts_cmp_ge_u32 s43, 14\n\ts_cselect_b32 s68, s70, s43\n\ts_mul_i32 s68, s68, 36864\n\t" \
    "v_mfma_f32_16x16x32_bf16 v[152:155], v[32:35], v[196:199], 0\n\ts_add_u32 s68, s68, s44\n\ts_add_u32 s68, s68, 258048\n\tv_mfma_f32_16x16x32_bf16 v[152:155], v[36:39], v[172:175], v[152:155]\n\tds_read_b64_tr_b16 v[28:29], v1 offset:12288\n\tds_read_b64_tr_b16 v[30:31], v1 offset:12800\n\tv_mfma_f32_16x16x32_bf16 v[160:163], v[32:35], v[200:203], 0\n\tv_mfma_f32_16x16x32_bf16 v[160:163], v[36:39], v[176:179], v[160:163]\n\t" \
    "s_waitcnt lgkmcnt(8)\n\tv_mfma_f32_16x16x32_bf16 v[148:151], v[40:43], v[192:195], 0\n\tds_read_b64_tr_b16 v[32:33], v2 offset:8192\n\tds_read_b64_tr_b16 v[34:35], v2 offset:8704\n\tv_mfma_f32_16x16x32_bf16 v[148:151], v[44:47], v[168:171], v[148:151]\n\tbuffer_load_dwordx4 a[32:35], v8, s[8:11], s68 offen offset:0 sc1\n\tv_mfma_f32_16x16x32_bf16 v[156:159], v[40:43], v[196:199], 0\n\tv_mfma_f32_16x16x32_bf16 v[156:159], v[44:47], v[172:175], v[156:159]\n\t" \
    "ds_read_b64_tr_b16 v[36:37], v2 offset:12288\n\tds_read_b64_tr_b16 v[38:39], v2 offset:12800\n\tv_mfma_f32_16x16x32_bf16 v[164:167], v[40:43], v[200:203], 0\n\tbuffer_load_dwordx4 a[36:39], v8, s[8:11], s68 offen offset:1024 sc1\n\tv_mfma_f32_16x16x32_bf16 v[164:167], v[44:47], v[176:179], v[164:167]\n\ts_waitcnt lgkmcnt(8)\n\tv_mfma_f32_16x16x32_bf16 v[48:51], v[16:19], v[192:195], v[96:99]\n\tds_read_b64_tr_b16 v[40:41], v3 offset:8192\n\t" \
    "ds_read_b64_tr_b16 v[42:43], v3 offset:8704\n\tv_mfma_f32_16x16x32_bf16 v[48:51], v[20:23], v[168:171], v[48:51]\n\tbuffer_load_dwordx4 a[40:43], v8, s[8:11], s68 offen offset:2048 sc1\n\ts_cmp_ge_u32 s43, 13\n\ts_cselect_b32 s68, s70, s43\n\ts_mul_i32 s68, s68, 36864\n\tv_mfma_f32_16x16x32_bf16 v[56:59], v[16:19], v[196:199], v[96:99]\n\ts_add_u32 s68, s68, s44\n\t" \
    "s_add_u32 s68, s68, 294912\n\tv_mfma_f32_16x16x32_bf16 v[56:59], v[20:23], v[172:175], v[56:59]\n\tds_read_b64_tr_b16 v[44:45], v3 offset:12288\n\tds_read_b64_tr_b16 v[46:47], v3 offset:12800\n\tv_mfma_f32_16x16x32_bf16 v[64:67], v[16:19], v[200:203], v[96:99]\n\tv_mfma_f32_16x16x32_bf16 v[64:67], v[20:23], v[176:179], v[64:67]\n\ts_waitcnt lgkmcnt(8)\n\tv_mfma_f32_16x16x32_bf16 v[52:55], v[24:27], v[192:195], v[100:103]\n\t" \
    "ds_read_b64_tr_b16 v[16:17], v0 offset:16384\n\tds_read_b64_tr_b16 v[18:19], v0 offset:16896\n\tv_mfma_f32_16x16x32_bf16 v[52:55], v[28:31], v[168:171], v[52:55]\n\tbuffer_load_dwordx4 a[44:47], v8, s[8:11], s68 offen offset:0 sc1\n\tv_mfma_f32_16x16x32_bf16 v[60:63], v[24:27], v[196:199], v[100:103]\n\tv_mfma_f32_16x16x32_bf16 v[60:63], v[28:31], v[172:175], v[60:63]\n\tds_read_b64_tr_b16 v[20:21], v0 offset:20480\n\tds_read_b64_tr_b16 v[22:23], v0 offset:20992\n\t" \
    "v_mfma_f32_16x16x32_bf16 v[68:71], v[24:27], v[200:203], v[100:103]\n\tbuffer_load_dwordx4 a[48:51], v8, s[8:11], s68 offen offset:1024 sc1\n\tv_mfma_f32_16x16x32_bf16 v[68:71], v[28:31], v[176:179], v[68:71]\n\ts_waitcnt lgkmcnt(8)\n\tv_mfma_f32_16x16x32_bf16 v[72:75], v[32:35], v[192:195], v[104:107]\n\tds_read_b64_tr_b16 v[24:25], v1 offset:16384\n\tds_read_b64_tr_b16 v[26:27], v1 offset:16896\n\tv_mfma_f32_16x16x32_bf16 v[72:75], v[36:39], v[168:171], v[72:75]\n\t" \
    "v_mfma_f32_16x16x32_bf16 v[80:83], v[32:35], v[196:199], v[104:107]\n\tv_mfma_f32_16x16x32_bf16 v[80:83], v[36:39], v[172:175], v[80:83]\n\tds_read_b64_tr_b16 v[28:29], v1 offset:20480\n\tds_read_b64_tr_b16 v[30:31], v1 offset:20992\n\tv_mfma_f32_16x16x32_bf16 v[88:91], v[32:35], v[200:203], v[104:107]\n\tbuffer_load_dwordx4 a[52:55], v8, s[8:11], s68 offen offset:2048 sc1\n\ts_cmp_ge_u32 s43, 12\n\ts_cselect_b32 s68, s70, s43\n\t" \
    "s_mul_i32 s68, s68, 36864\n\tv_mfma_f32_16x16x32_bf16 v[88:91], v[36:39], v[176:179], v[88:91]\n\ts_add_u32 s68, s68, s44\n\ts_add_u32 s68, s68, 331776\n\ts_waitcnt lgkmcnt(8)\n\tv_mfma_f32_16x16x32_bf16 v[76:79], v[40:43], v[192:195], v[108:111]\n\tds_read_b64_tr_b16 v[32:33], v2 offset:16384\n\tds_read_b64_tr_b16 v[34:35], v2 offset:16896\n\t" \
    "v_mfma_f32_16x16x32_bf16 v[76:79], v[44:47], v[168:171], v[76:79]\n\tbuffer_load_dwordx4 a[56:59], v8, s[8:11], s68 offen offset:0 sc1\n\tv_mfma_f32_16x16x32_bf16 v[84:87], v[40:43], v[196:199], v[108:111]\n\tv_mfma_f32_16x16x32_bf16 v[84:87], v[44:47], v[172:175], v[84:87]\n\tds_read_b64_tr_b16 v[36:37], v2 offset:20480\n\tds_read_b64_tr_b16 v[38:39], v2 offset:20992\n\tv_mfma_f32_16x16x32_bf16 v[92:95], v[40:43], v[200:203], v[108:111]\n\tv_mfma_f32_16x16x32_bf16 v[92:95], v[44:47], v[176:179], v[92:95]\n\t" \
    "s_barrier\n\ts_waitcnt vmcnt(16) lgkmcnt(8)\n\tv_mfma_f32_16x16x32_bf16 v[120:123], v[16:19], v[180:183], v[120:123]\n\tds_read_b64_tr_b16 v[40:41], v3 offset:16384\n\tds_read_b64_tr_b16 v[42:43], v3 offset:16896\n\tv_mfma_f32_16x16x32_bf16 v[120:123], v[20:23], v[204:207], v[120:123]\n\tbuffer_load_dwordx4 a[60:63], v8, s[8:11], s68 offen offset:1024 sc1\n\tv_mfma_f32_16x16x32_bf16 v[128:131], v[20:23], v[208:211], v[128:131]\n\t" \
    "v_mfma_f32_16x16x32_bf16 v[128:131], v[16:19], v[184:187], v[128:131]\n\tds_read_b64_tr_b16 v[44:45], v3 offset:20480\n\tds_read_b64_tr_b16 v[46:47], v3 offset:20992\n\tv_mfma_f32_16x16x32_bf16 v[136:139], v[16:19], v[188:191], v[136:139]\n\tbuffer_load_dwordx4 a[64:67], v8, s[8:11], s68 offen offset:2048 sc1\n\ts_cmp_ge_u32 s43, 11\n\ts_cselect_b32 s68, s70, s43\n\ts_mul_i32 s68, s68, 36864\n\t" \
    "v_mfma_f32_16x16x32_bf16 v[136:139], v[20:23], v[212:215], v[136:139]\n\ts_add_u32 s68, s68, s44\n\ts_add_u32 s68, s68, 368640\n\ts_waitcnt lgkmcnt(8)\n\tv_mfma_f32_16x16x32_bf16 v[124:127], v[24:27], v[180:183], v[124:127]\n\tds_read_b64_tr_b16 v[16:17], v0 offset:24576\n\tds_read_b64_tr_b16 v[18:19], v0 offset:25088\n\tv_mfma_f32_16x16x32_bf16 v[124:127], v[28:31], v[204:207], v[124:127]\n\t" \
    "buffer_load_dwordx4 v[192:195], v8, s[8:11], s68 offen offset:0 sc1\n\tv_mfma_f32_16x16x32_bf16 v[132:135], v[28:31], v[208:211], v[132:135]\n\tv_mfma_f32_16x16x32_bf16 v[132:135], v[24:27], v[184:187], v[132:135]\n\tds_read_b64_tr_b16 v[20:21], v0 offset:28672\n\tds_read_b64_tr_b16 v[22:23], v0 offset:29184\n\tv_mfma_f32_16x16x32_bf16 v[140:143], v[24:27], v[188:191], v[140:143]\n\tbuffer_load_dwordx4 v[196:199], v8, s[8:11], s68 offen offset:1024 sc1\n\tv_mfma_f32_16x16x32_bf16 v[140:143], v[28:31], v[212:215], v[140:143]\n\t" \
    "s_waitcnt lgkmcnt(8)\n\tv_mfma_f32_16x16x32_bf16 v[144:147], v[32:35], v[180:183], v[144:147]\n\tds_read_b64_tr_b16 v[24:25], v1 offset:24576\n\tds_read_b64_tr_b16 v[26:27], v1 offset:25088\n\tv_mfma_f32_16x16x32_bf16 v[144:147], v[36:39], v[204:207], v[144:147]\n\tv_mfma_f32_16x16x32_bf16 v[152:155], v[36:39], v[208:211], v[152:155]\n\tv_mfma_f32_16x16x32_bf16 v[152:155], v[32:35], v[184:187], v[152:155]\n\tds_read_b64_tr_b16 v[28:29], v1 offset:28672\n\t" \
    "ds_read_b64_tr_b16 v[30:31], v1 offset:29184\n\tv_mfma_f32_16x16x32_bf16 v[160:163], v[32:35], v[188:191], v[160:163]\n\tbuffer_load_dwordx4 v[200:203], v8, s[8:11], s68 offen offset:2048 sc1\n\ts_cmp_ge_u32 s43, 10\n\ts_cselect_b32 s68, s70, s43\n\ts_mul_i32 s68, s68, 36864\n\tv_mfma_f32_16x16x32_bf16 v[160:163], v[36:39], v[212:215], v[160:163]\n\ts_add_u32 s68, s68, s44\n\t" \
    "s_add_u32 s68, s68, 405504\n\ts_waitcnt lgkmcnt(8)\n\tv_mfma_f32_16x16x32_bf16 v[148:151], v[40:43], v[180:183], v[148:151]\n\tds_read_b64_tr_b16 v[32:33], v2 offset:24576\n\tds_read_b64_tr_b16 v[34:35], v2 offset:25088\n\tv_mfma_f32_16x16x32_bf16 v[148:151], v[44:47], v[204:207], v[148:151]\n\tbuffer_load_dwordx4 v[168:171], v8, s[8:11], s68 offen offset:0 sc1\n\tv_mfma_f32_16x16x32_bf16 v[156:159], v[44:47], v[208:211], v[156:159]\n\t" \
    "v_mfma_f32_16x16x32_bf16 v[156:159], v[40:43], v[184:187], v[156:159]\n\tds_read_b64_tr_b16 v[36:37], v2 offset:28672\n\tds_read_b64_tr_b16 v[38:39], v2 offset:29184\n\tv_mfma_f32_16x16x32_bf16 v[164:167], v[40:43], v[188:191], v[164:167]\n\tbuffer_load_dwordx4 v[172:175], v8, s[8:11], s68 offen offset:1024 sc1\n\tv_mfma_f32_16x16x32_bf16 v[164:167], v[44:47], v[212:215], v[164:167]\n\ts_waitcnt lgkmcnt(8)\n\tv_mfma_f32_16x16x32_bf16 v[48:51], v[16:19], v[180:183], v[48:51]\n\t" \
    "ds_read_b64_tr_b16 v[40:41], v3 offset:24576\n\tds_read_b64_tr_b16 v[42:43], v3 offset:25088\n\tv_mfma_f32_16x16x32_bf16 v[48:51], v[20:23], v[204:207], v[48:51]\n\tv_mfma_f32_16x16x32_bf16 v[56:59], v[20:23], v[208:211], v[56:59]\n\tv_mfma_f32_16x16x32_bf16 v[56:59], v[16:19], v[184:187], v[56:59]\n\tds_read_b64_tr_b16 v[44:45], v3 offset:28672\n\tds_read_b64_tr_b16 v[46:47], v3 offset:29184\n\tv_mfma_f32_16x16x32_bf16 v[64:67], v[16:19], v[188:191], v[64:67]\n\t" \
    "buffer_load_dwordx4 v[176:179], v8, s[8:11], s68 offen offset:2048 sc1\n\ts_cmp_ge_u32 s43, 9\n\ts_cselect_b32 s68, s70, s43\n\ts_mul_i32 s68, s68, 36864\n\tv_mfma_f32_16x16x32_bf16 v[64:67], v[20:23], v[212:215], v[64:67]\n\ts_add_u32 s68, s68, s44\n\ts_add_u32 s68, s68, 442368\n\ts_waitcnt lgkmcnt(8)\n\t" \
    "v_mfma_f32_16x16x32_bf16 v[52:55], v[24:27], v[180:183], v[52:55]\n\tds_read_b64_tr_b16 v[16:17], v0 offset:32768\n\tds_read_b64_tr_b16 v[18:19], v0 offset:33280\n\tv_mfma_f32_16x16x32_bf16 v[52:55], v[28:31], v[204:207], v[52:55]\n\tbuffer_load_dwordx4 a[68:71], v8, s[8:11], s68 offen offset:0 sc1\n\tv_mfma_f32_16x16x32_bf16 v[60:63], v[28:31], v[208:211], v[60:63]\n\tv_mfma_f32_16x16x32_bf16 v[60:63], v[24:27], v[184:187], v[60:63]\n\tds_read_b64_tr_b16 v[20:21], v0 offset:36864\n\t" \
    "ds_read_b64_tr_b16 v[22:23], v0 offset:37376\n\tv_mfma_f32_16x16x32_bf16 v[68:71], v[24:27], v[188:191], v[68:71]\n\tv_mfma_f32_16x16x32_bf16 v[68:71], v[28:31], v[212:215], v[68:71]\n\ts_waitcnt lgkmcnt(8)\n\tv_mfma_f32_16x16x32_bf16 v[72:75], v[32:35], v[180:183], v[72:75]\n\tds_read_b64_tr_b16 v[24:25], v1 offset:32768\n\tds_read_b64_tr_b16 v[26:27], v1 offset:33280\n\tv_mfma_f32_16x16x32_bf16 v[72:75], v[36:39], v[204:207], v[72:75]\n\t" \
    "buffer_load_dwordx4 a[72:75], v8, s[8:11], s68 offen offset:1024 sc1\n\tv_mfma_f32_16x16x32_bf16 v[80:83], v[36:39], v[208:211], v[80:83]\n\tv_mfma_f32_16x16x32_bf16 v[80:83], v[32:35], v[184:187], v[80:83]\n\tds_read_b64_tr_b16 v[28:29], v1 offset:36864\n\tds_read_b64_tr_b16 v[30:31], v1 offset:37376\n\tv_mfma_f32_16x16x32_bf16 v[88:91], v[32:35], v[188:191], v[88:91]\n\tbuffer_load_dwordx4 a[76:79], v8, s[8:11], s68 offen offset:2048 sc1\n\ts_cmp_ge_u32 s43, 8\n\t" \
    "s_cselect_b32 s68, s70, s43\n\ts_mul_i32 s68, s68, 36864\n\tv_mfma_f32_16x16x32_bf16 v[88:91], v[36:39], v[212:215], v[88:91]\n\ts_add_u32 s68, s68, s44\n\ts_add_u32 s68, s68, 479232\n\ts_waitcnt lgkmcnt(8)\n\tv_mfma_f32_16x16x32_bf16 v[76:79], v[40:43], v[180:183], v[76:79]\n\tds_read_b64_tr_b16 v[32:33], v2 offset:32768\n\t" \
    "ds_read_b64_tr_b16 v[34:35], v2 offset:33280\n\tv_mfma_f32_16x16x32_bf16 v[76:79], v[44:47], v[204:207], v[76:79]\n\tv_mfma_f32_16x16x32_bf16 v[84:87], v[44:47], v[208:211], v[84:87]\n\tv_mfma_f32_16x16x32_bf16 v[84:87], v[40:43], v[184:187], v[84:87]\n\tds_read_b64_tr_b16 v[36:37], v2 offset:36864\n\tds_read_b64_tr_b16 v[38:39], v2 offset:37376\n\tv_mfma_f32_16x16x32_bf16 v[92:95], v[40:43], v[188:191], v[92:95]\n\tbuffer_load_dwordx4 a[80:83], v8, s[8:11], s68 offen offset:0 sc1\n\t" \
    "v_mfma_f32_16x16x32_bf16 v[92:95], v[44:47], v[212:215], v[92:95]\n\ts_barrier\n\ts_waitcnt vmcnt(22) lgkmcnt(8)\n\tv_mfma_f32_16x16x32_bf16 v[120:123], v[16:19], v[216:219], v[120:123]\n\tds_read_b64_tr_b16 v[40:41], v3 offset:32768\n\tds_read_b64_tr_b16 v[42:43], v3 offset:33280\n\tv_mfma_f32_16x16x32_bf16 v[120:123], v[20:23], a[8:11], v[120:123]\n\tbuffer_load_dwordx4 a[84:87], v8, s[8:11], s68 offen offset:1024 sc1\n\t" \
    "v_mfma_f32_16x16x32_bf16 v[128:131], v[20:23], a[12:15], v[128:131]\n\tv_mfma_f32_16x16x32_bf16 v[128:131], v[16:19], a[0:3], v[128:131]\n\tds_read_b64_tr_b16 v[44:45], v3 offset:36864\n\tds_read_b64_tr_b16 v[46:47], v3 offset:37376\n\tv_mfma_f32_16x16x32_bf16 v[136:139], v[16:19], a[4:7], v[136:139]\n\tbuffer_load_dwordx4 a[88:91], v8, s[8:11], s68 offen offset:2048 sc1\n\ts_cmp_ge_u32 s43, 7\n\ts_cselect_b32 s68, s70, s43\n\t" \
    "s_mul_i32 s68, s68, 36864\n\tv_mfma_f32_16x16x32_bf16 v[136:139], v[20:23], a[16:19], v[136:139]\n\ts_add_u32 s68, s68, s44\n\ts_add_u32 s68, s68, 516096\n\ts_waitcnt lgkmcnt(8)\n\tv_mfma_f32_16x16x32_bf16 v[124:127], v[24:27], v[216:219], v[124:127]\n\tds_read_b64_tr_b16 v[16:17], v0 offset:40960\n\tds_read_b64_tr_b16 v[18:19], v0 offset:41472\n\t" \
    "v_mfma_f32_16x16x32_bf16 v[124:127], v[28:31], a[8:11], v[124:127]\n\tbuffer_load_dwordx4 v[180:183], v8, s[8:11], s68 offen offset:0 sc1\n\tv_mfma_f32_16x16x32_bf16 v[132:135], v[28:31], a[12:15], v[132:135]\n\tv_mfma_f32_16x16x32_bf16 v[132:135], v[24:27], a[0:3], v[132:135]\n\tds_read_b64_tr_b16 v[20:21], v0 offset:45056\n\tds_read_b64_tr_b16 v[22:23], v0 offset:45568\n\tv_mfma_f32_16x16x32_bf16 v[140:143], v[24:27], a[4:7], v[140:143]\n\tbuffer_load_dwordx4 v[184:187], v8, s[8:11], s68 offen offset:1024 sc1\n\t" \
    "v_mfma_f32_16x16x32_bf16 v[140:143], v[28:31], a[16:19], v[140:143]\n\ts_waitcnt lgkmcnt(8)\n\tv_mfma_f32_16x16x32_bf16 v[144:147], v[32:35], v[216:219], v[144:147]\n\tds_read_b64_tr_b16 v[24:25], v1 offset:40960\n\tds_read_b64_tr_b16 v[26:27], v1 offset:41472\n\tv_mfma_f32_16x16x32_bf16 v[144:147], v[36:39], a[8:11], v[144:147]\n\tv_mfma_f32_16x16x32_bf16 v[152:155], v[36:39], a[12:15], v[152:155]\n\tv_mfma_f32_16x16x32_bf16 v[152:155], v[32:35], a[0:3], v[152:155]\n\t" \
    "ds_read_b64_tr_b16 v[28:29], v1 offset:45056\n\tds_read_b64_tr_b16 v[30:31], v1 offset:45568\n\tv_mfma_f32_16x16x32_bf16 v[160:163], v[32:35], a[4:7], v[160:163]\n\tbuffer_load_dwordx4 v[188:191], v8, s[8:11], s68 offen offset:2048 sc1\n\ts_cmp_ge_u32 s43, 6\n\ts_cselect_b32 s68, s70, s43\n\ts_mul_i32 s68, s68, 36864\n\tv_mfma_f32_16x16x32_bf16 v[160:163], v[36:39], a[16:19], v[160:163]\n\t" \
    "s_add_u32 s68, s68, s44\n\ts_add_u32 s68, s68, 552960\n\ts_waitcnt lgkmcnt(8)\n\tv_mfma_f32_16x16x32_bf16 v[148:151], v[40:43], v[216:219], v[148:151]\n\tds_read_b64_tr_b16 v[32:33], v2 offset:40960\n\tds_read_b64_tr_b16 v[34:35], v2 offset:41472\n\tv_mfma_f32_16x16x32_bf16 v[148:151], v[44:47], a[8:11], v[148:151]\n\tbuffer_load_dwordx4 v[204:207], v8, s[8:11], s68 offen offset:0 sc1\n\t" \
    "v_mfma_f32_16x16x32_bf16 v[156:159], v[44:47], a[12:15], v[156:159]\n\tv_mfma_f32_16x16x32_bf16 v[156:159], v[40:43], a[0:3], v[156:159]\n\tds_read_b64_tr_b16 v[36:37], v2 offset:45056\n\tds_read_b64_tr_b16 v[38:39], v2 offset:45568\n\tv_mfma_f32_16x16x32_bf16 v[164:167], v[40:43], a[4:7], v[164:167]\n\tv_mfma_f32_16x16x32_bf16 v[164:167], v[44:47], a[16:19], v[164:167]\n\ts_waitcnt lgkmcnt(8)\n\tv_mfma_f32_16x16x32_bf16 v[48:51], v[16:19], v[216:219], v[48:51]\n\t" \
    "ds_read_b64_tr_b16 v[40:41], v3 offset:40960\n\tds_read_b64_tr_b16 v[42:43], v3 offset:41472\n\tv_mfma_f32_16x16x32_bf16 v[48:51], v[20:23], a[8:11], v[48:51]\n\tbuffer_load_dwordx4 v[208:211], v8, s[8:11], s68 offen offset:1024 sc1\n\tv_mfma_f32_16x16x32_bf16 v[56:59], v[20:23], a[12:15], v[56:59]\n\tv_mfma_f32_16x16x32_bf16 v[56:59], v[16:19], a[0:3], v[56:59]\n\tds_read_b64_tr_b16 v[44:45], v3 offset:45056\n\tds_read_b64_tr_b16 v[46:47], v3 offset:45568\n\t" \
    "v_mfma_f32_16x16x32_bf16 v[64:67], v[16:19], a[4:7], v[64:67]\n\tbuffer_load_dwordx4 v[212:215], v8, s[8:11], s68 offen offset:2048 sc1\n\ts_cmp_ge_u32 s43, 1\n\ts_cselect_b32 s69, s70, s43\n\ts_mul_i32 s69, s69, 294912\n\tv_mfma_f32_16x16x32_bf16 v[64:67], v[20:23], a[16:19], v[64:67]\n\ts_add_u32 s69, s69, 5898240\n\ts_add_u32 m0, s54, 0\n\t" \
    "s_waitcnt lgkmcnt(8)\n\tv_mfma_f32_16x16x32_bf16 v[52:55], v[24:27], v[216:219], v[52:55]\n\tds_read_b64_tr_b16 v[16:17], v0 offset:49152\n\tds_read_b64_tr_b16 v[18:19], v0 offset:49664\n\tv_mfma_f32_16x16x32_bf16 v[52:55], v[28:31], a[8:11], v[52:55]\n\tv_mfma_f32_16x16x32_bf16 v[60:63], v[28:31], a[12:15], v[60:63]\n\tv_mfma_f32_16x16x32_bf16 v[60:63], v[24:27], a[0:3], v[60:63]\n\tds_read_b64_tr_b16 v[20:21], v0 offset:53248\n\t" \
    "ds_read_b64_tr_b16 v[22:23], v0 offset:53760\n\tv_mfma_f32_16x16x32_bf16 v[68:71], v[24:27], a[4:7], v[68:71]\n\tbuffer_load_dwordx4 v10, s[12:15], s69 offen lds\n\ts_mov_b32 s69, 6193152\n\ts_add_u32 m0, s54, 4096\n\tv_mfma_f32_16x16x32_bf16 v[68:71], v[28:31], a[16:19], v[68:71]\n\ts_waitcnt lgkmcnt(8)\n\tv_mfma_f32_16x16x32_bf16 v[72:75], v[32:35], v[216:219], v[72:75]\n\t" \
    "ds_read_b64_tr_b16 v[24:25], v1 offset:49152\n\tds_read_b64_tr_b16 v[26:27], v1 offset:49664\n\tv_mfma_f32_16x16x32_bf16 v[72:75], v[36:39], a[8:11], v[72:75]\n\tbuffer_load_dwordx4 v10, s[12:15], s69 offen lds\n\ts_cmp_ge_u32 s43, 5\n\ts_cselect_b32 s68, s70, s43\n\ts_mul_i32 s68, s68, 36864\n\tv_mfma_f32_16x16x32_bf16 v[80:83], v[36:39], a[12:15], v[80:83]\n\t" \
    "s_add_u32 s68, s68, s44\n\ts_add_u32 s68, s68, 589824\n\tv_mfma_f32_16x16x32_bf16 v[80:83], v[32:35], a[0:3], v[80:83]\n\tds_read_b64_tr_b16 v[28:29], v1 offset:53248\n\tds_read_b64_tr_b16 v[30:31], v1 offset:53760\n\tv_mfma_f32_16x16x32_bf16 v[88:91], v[32:35], a[4:7], v[88:91]\n\tbuffer_load_dwordx4 a[92:95], v8, s[8:11], s68 offen offset:0 sc1\n\tv_mfma_f32_16x16x32_bf16 v[88:91], v[36:39], a[16:19], v[88:91]\n\t" \
    "s_waitcnt lgkmcnt(8)\n\tv_mfma_f32_16x16x32_bf16 v[76:79], v[40:43], v[216:219], v[76:79]\n\tds_read_b64_tr_b16 v[32:33], v2 offset:49152\n\tds_read_b64_tr_b16 v[34:35], v2 offset:49664\n\tv_mfma_f32_16x16x32_bf16 v[76:79], v[44:47], a[8:11], v[76:79]\n\tv_mfma_f32_16x16x32_bf16 v[84:87], v[44:47], a[12:15], v[84:87]\n\tv_mfma_f32_16x16x32_bf16 v[84:87], v[40:43], a[0:3], v[84:87]\n\tds_read_b64_tr_b16 v[36:37], v2 offset:53248\n\t" \
    "ds_read_b64_tr_b16 v[38:39], v2 offset:53760\n\tv_mfma_f32_16x16x32_bf16 v[92:95], v[40:43], a[4:7], v[92:95]\n\tbuffer_load_dwordx4 a[96:99], v8, s[8:11], s68 offen offset:1024 sc1\n\tv_mfma_f32_16x16x32_bf16 v[92:95], v[44:47], a[16:19], v[92:95]\n\ts_barrier\n\ts_waitcnt vmcnt(28) lgkmcnt(8)\n\tv_mfma_f32_16x16x32_bf16 v[120:123], v[16:19], a[20:23], v[120:123]\n\tds_read_b64_tr_b16 v[40:41], v3 offset:49152\n\t" \
    "ds_read_b64_tr_b16 v[42:43], v3 offset:49664\n\tv_mfma_f32_16x16x32_bf16 v[120:123], v[20:23], a[32:35], v[120:123]\n\tbuffer_load_dwordx4 a[100:103], v8, s[8:11], s68 offen offset:2048 sc1\n\ts_cmp_ge_u32 s43, 4\n\ts_cselect_b32 s68, s70, s43\n\ts_mul_i32 s68, s68, 36864\n\tv_mfma_f32_16x16x32_bf16 v[128:131], v[20:23], a[36:39], v[128:131]\n\ts_add_u32 s68, s68, s44\n\t" \
    "s_add_u32 s68, s68, 626688\n\tv_mfma_f32_16x16x32_bf16 v[128:131], v[16:19], a[24:27], v[128:131]\n\tds_read_b64_tr_b16 v[44:45], v3 offset:53248\n\tds_read_b64_tr_b16 v[46:47], v3 offset:53760\n\tv_mfma_f32_16x16x32_bf16 v[136:139], v[16:19], a[28:31], v[136:139]\n\tbuffer_load_dwordx4 v[216:219], v8, s[8:11], s68 offen offset:0 sc1\n\tv_mfma_f32_16x16x32_bf16 v[136:139], v[20:23], a[40:43], v[136:139]\n\ts_waitcnt lgkmcnt(8)\n\t" \
    "v_mfma_f32_16x16x32_bf16 v[124:127], v[24:27], a[20:23], v[124:127]\n\tds_read_b64_tr_b16 v[16:17], v0 offset:57344\n\tds_read_b64_tr_b16 v[18:19], v0 offset:57856\n\tv_mfma_f32_16x16x32_bf16 v[124:127], v[28:31], a[32:35], v[124:127]\n\tbuffer_load_dwordx4 a[0:3], v8, s[8:11], s68 offen offset:1024 sc1\n\tv_mfma_f32_16x16x32_bf16 v[132:135], v[28:31], a[36:39], v[132:135]\n\tv_mfma_f32_16x16x32_bf16 v[132:135], v[24:27], a[24:27], v[132:135]\n\tds_read_b64_tr_b16 v[20:21], v0 offset:61440\n\t" \
    "ds_read_b64_tr_b16 v[22:23], v0 offset:61952\n\tv_mfma_f32_16x16x32_bf16 v[140:143], v[24:27], a[28:31], v[140:143]\n\tv_mfma_f32_16x16x32_bf16 v[140:143], v[28:31], a[40:43], v[140:143]\n\ts_waitcnt lgkmcnt(8)\n\tv_mfma_f32_16x16x32_bf16 v[144:147], v[32:35], a[20:23], v[144:147]\n\tds_read_b64_tr_b16 v[24:25], v1 offset:57344\n\tds_read_b64_tr_b16 v[26:27], v1 offset:57856\n\tv_mfma_f32_16x16x32_bf16 v[144:147], v[36:39], a[32:35], v[144:147]\n\t" \
    "buffer_load_dwordx4 a[4:7], v8, s[8:11], s68 offen offset:2048 sc1\n\ts_cmp_ge_u32 s43, 3\n\ts_cselect_b32 s68, s70, s43\n\ts_mul_i32 s68, s68, 36864\n\tv_mfma_f32_16x16x32_bf16 v[152:155], v[36:39], a[36:39], v[152:155]\n\ts_add_u32 s68, s68, s44\n\ts_add_u32 s68, s68, 663552\n\tv_mfma_f32_16x16x32_bf16 v[152:155], v[32:35], a[24:27], v[152:155]\n\t" \
    "ds_read_b64_tr_b16 v[28:29], v1 offset:61440\n\tds_read_b64_tr_b16 v[30:31], v1 offset:61952\n\tv_mfma_f32_16x16x32_bf16 v[160:163], v[32:35], a[28:31], v[160:163]\n\tbuffer_load_dwordx4 a[8:11], v8, s[8:11], s68 offen offset:0 sc1\n\tv_mfma_f32_16x16x32_bf16 v[160:163], v[36:39], a[40:43], v[160:163]\n\ts_waitcnt lgkmcnt(8)\n\tv_mfma_f32_16x16x32_bf16 v[148:151], v[40:43], a[20:23], v[148:151]\n\tds_read_b64_tr_b16 v[32:33], v2 offset:57344\n\t" \
    "ds_read_b64_tr_b16 v[34:35], v2 offset:57856\n\tv_mfma_f32_16x16x32_bf16 v[148:151], v[44:47], a[32:35], v[148:151]\n\tv_mfma_f32_16x16x32_bf16 v[156:159], v[44:47], a[36:39], v[156:159]\n\tv_mfma_f32_16x16x32_bf16 v[156:159], v[40:43], a[24:27], v[156:159]\n\tds_read_b64_tr_b16 v[36:37], v2 offset:61440\n\tds_read_b64_tr_b16 v[38:39], v2 offset:61952\n\tv_mfma_f32_16x16x32_bf16 v[164:167], v[40:43], a[28:31], v[164:167]\n\tbuffer_load_dwordx4 a[12:15], v8, s[8:11], s68 offen offset:1024 sc1\n\t" \
    "v_mfma_f32_16x16x32_bf16 v[164:167], v[44:47], a[40:43], v[164:167]\n\ts_waitcnt lgkmcnt(8)\n\tv_mfma_f32_16x16x32_bf16 v[48:51], v[16:19], a[20:23], v[48:51]\n\tds_read_b64_tr_b16 v[40:41], v3 offset:57344\n\tds_read_b64_tr_b16 v[42:43], v3 offset:57856\n\tv_mfma_f32_16x16x32_bf16 v[48:51], v[20:23], a[32:35], v[48:51]\n\tbuffer_load_dwordx4 a[16:19], v8, s[8:11], s68 offen offset:2048 sc1\n\ts_cmp_ge_u32 s43, 2\n\t" \
    "s_cselect_b32 s68, s70, s43\n\ts_mul_i32 s68, s68, 36864\n\tv_mfma_f32_16x16x32_bf16 v[56:59], v[20:23], a[36:39], v[56:59]\n\ts_add_u32 s68, s68, s44\n\ts_add_u32 s68, s68, 700416\n\tv_mfma_f32_16x16x32_bf16 v[56:59], v[16:19], a[24:27], v[56:59]\n\tds_read_b64_tr_b16 v[44:45], v3 offset:61440\n\tds_read_b64_tr_b16 v[46:47], v3 offset:61952\n\t" \
    "v_mfma_f32_16x16x32_bf16 v[64:67], v[16:19], a[28:31], v[64:67]\n\tbuffer_load_dwordx4 a[104:107], v8, s[8:11], s68 offen offset:0 sc1\n\tv_mfma_f32_16x16x32_bf16 v[64:67], v[20:23], a[40:43], v[64:67]\n\ts_waitcnt lgkmcnt(8)\n\tv_mfma_f32_16x16x32_bf16 v[52:55], v[24:27], a[20:23], v[52:55]\n\tds_read_b64_tr_b16 v[16:17], v14 offset:0\n\tds_read_b64_tr_b16 v[18:19], v14 offset:512\n\tv_mfma_f32_16x16x32_bf16 v[52:55], v[28:31], a[32:35], v[52:55]\n\t" \
    "v_mfma_f32_16x16x32_bf16 v[60:63], v[28:31], a[36:39], v[60:63]\n\tv_mfma_f32_16x16x32_bf16 v[60:63], v[24:27], a[24:27], v[60:63]\n\tds_read_b64_tr_b16 v[20:21], v14 offset:4096\n\tds_read_b64_tr_b16 v[22:23], v14 offset:4608\n\tv_mfma_f32_16x16x32_bf16 v[68:71], v[24:27], a[28:31], v[68:71]\n\tbuffer_load_dwordx4 a[108:111], v8, s[8:11], s68 offen offset:1024 sc1\n\tv_mfma_f32_16x16x32_bf16 v[68:71], v[28:31], a[40:43], v[68:71]\n\ts_waitcnt lgkmcnt(8)\n\t" \
    "v_mfma_f32_16x16x32_bf16 v[72:75], v[32:35], a[20:23], v[72:75]\n\tds_read_b64_tr_b16 v[24:25], v15 offset:0\n\tds_read_b64_tr_b16 v[26:27], v15 offset:512\n\tv_mfma_f32_16x16x32_bf16 v[72:75], v[36:39], a[32:35], v[72:75]\n\tbuffer_load_dwordx4 a[112:115], v8, s[8:11], s68 offen offset:2048 sc1\n\ts_mov_b32 s69, 6488064\n\ts_add_u32 m0, s54, 8192\n\tv_mfma_f32_16x16x32_bf16 v[80:83], v[36:39], a[36:39], v[80:83]\n\t" \
    "v_mfma_f32_16x16x32_bf16 v[80:83], v[32:35], a[24:27], v[80:83]\n\tds_read_b64_tr_b16 v[28:29], v15 offset:4096\n\tds_read_b64_tr_b16 v[30:31], v15 offset:4608\n\tv_mfma_f32_16x16x32_bf16 v[88:91], v[32:35], a[28:31], v[88:91]\n\tv_mfma_f32_16x16x32_bf16 v[88:91], v[36:39], a[40:43], v[88:91]\n\ts_waitcnt lgkmcnt(8)\n\tv_mfma_f32_16x16x32_bf16 v[76:79], v[40:43], a[20:23], v[76:79]\n\tds_read_b64_tr_b16 v[32:33], v237 offset:0\n\t" \
    "ds_read_b64_tr_b16 v[34:35], v237 offset:512\n\tv_mfma_f32_16x16x32_bf16 v[76:79], v[44:47], a[32:35], v[76:79]\n\tbuffer_load_dwordx4 v10, s[12:15], s69 offen lds\n\ts_mov_b32 s69, 6782976\n\ts_add_u32 m0, s54, 12288\n\tv_mfma_f32_16x16x32_bf16 v[84:87], v[44:47], a[36:39], v[84:87]\n\tv_mfma_f32_16x16x32_bf16 v[84:87], v[40:43], a[24:27], v[84:87]\n\tds_read_b64_tr_b16 v[36:37], v237 offset:4096\n\t" \
    "ds_read_b64_tr_b16 v[38:39], v237 offset:4608\n\tv_mfma_f32_16x16x32_bf16 v[92:95], v[40:43], a[28:31], v[92:95]\n\tbuffer_load_dwordx4 v10, s[12:15], s69 offen lds\n\ts_mov_b32 s69, 7077888\n\ts_add_u32 m0, s54, 16384\n\tv_mfma_f32_16x16x32_bf16 v[92:95], v[44:47], a[40:43], v[92:95]\n\ts_barrier\n\ts_waitcnt vmcnt(34) lgkmcnt(8)\n\t" \
    "v_mfma_f32_16x16x32_bf16 v[120:123], v[16:19], a[44:47], v[120:123]\n\tds_read_b64_tr_b16 v[40:41], v238 offset:0\n\tds_read_b64_tr_b16 v[42:43], v238 offset:512\n\tv_mfma_f32_16x16x32_bf16 v[120:123], v[20:23], a[56:59], v[120:123]\n\tbuffer_load_dwordx4 v10, s[12:15], s69 offen lds\n\ts_mov_b32 s69, 7372800\n\ts_add_u32 m0, s54, 20480\n\tv_mfma_f32_16x16x32_bf16 v[128:131], v[20:23], a[60:63], v[128:131]\n\t" \
    "v_mfma_f32_16x16x32_bf16 v[128:131], v[16:19], a[48:51], v[128:131]\n\tds_read_b64_tr_b16 v[44:45], v238 offset:4096\n\tds_read_b64_tr_b16 v[46:47], v238 offset:4608\n\tv_mfma_f32_16x16x32_bf16 v[136:139], v[16:19], a[52:55], v[136:139]\n\tbuffer_load_dwordx4 v10, s[12:15], s69 offen lds\n\ts_mov_b32 s69, 7667712\n\ts_add_u32 m0, s54, 24576\n\tv_mfma_f32_16x16x32_bf16 v[136:139], v[20:23], a[64:67], v[136:139]\n\t" \
    "s_waitcnt lgkmcnt(8)\n\tv_mfma_f32_16x16x32_bf16 v[124:127], v[24:27], a[44:47], v[124:127]\n\tds_read_b64_tr_b16 v[16:17], v14 offset:8192\n\tds_read_b64_tr_b16 v[18:19], v14 offset:8704\n\tv_mfma_f32_16x16x32_bf16 v[124:127], v[28:31], a[56:59], v[124:127]\n\tv_mfma_f32_16x16x32_bf16 v[132:135], v[28:31], a[60:63], v[132:135]\n\tv_mfma_f32_16x16x32_bf16 v[132:135], v[24:27], a[48:51], v[132:135]\n\tds_read_b64_tr_b16 v[20:21], v14 offset:12288\n\t" \
    "ds_read_b64_tr_b16 v[22:23], v14 offset:12800\n\tv_mfma_f32_16x16x32_bf16 v[140:143], v[24:27], a[52:55], v[140:143]\n\tbuffer_load_dwordx4 v10, s[12:15], s69 offen lds\n\ts_mov_b32 s69, 7962624\n\ts_add_u32 m0, s54, 28672\n\tv_mfma_f32_16x16x32_bf16 v[140:143], v[28:31], a[64:67], v[140:143]\n\ts_waitcnt lgkmcnt(8)\n\tv_mfma_f32_16x16x32_bf16 v[144:147], v[32:35], a[44:47], v[144:147]\n\t" \
    "ds_read_b64_tr_b16 v[24:25], v15 offset:8192\n\tds_read_b64_tr_b16 v[26:27], v15 offset:8704\n\tv_mfma_f32_16x16x32_bf16 v[144:147], v[36:39], a[56:59], v[144:147]\n\tbuffer_load_dwordx4 v10, s[12:15], s69 offen lds\n\ts_mov_b32 s69, 8257536\n\ts_add_u32 m0, s54, 32768\n\tv_mfma_f32_16x16x32_bf16 v[152:155], v[36:39], a[60:63], v[152:155]\n\tv_mfma_f32_16x16x32_bf16 v[152:155], v[32:35], a[48:51], v[152:155]\n\t" \
    "ds_read_b64_tr_b16 v[28:29], v15 offset:12288\n\tds_read_b64_tr_b16 v[30:31], v15 offset:12800\n\tv_mfma_f32_16x16x32_bf16 v[160:163], v[32:35], a[52:55], v[160:163]\n\tv_mfma_f32_16x16x32_bf16 v[160:163], v[36:39], a[64:67], v[160:163]\n\ts_waitcnt lgkmcnt(8)\n\tv_mfma_f32_16x16x32_bf16 v[148:151], v[40:43], a[44:47], v[148:151]\n\tds_read_b64_tr_b16 v[32:33], v237 offset:8192\n\tds_read_b64_tr_b16 v[34:35], v237 offset:8704\n\t" \
    "v_mfma_f32_16x16x32_bf16 v[148:151], v[44:47], a[56:59], v[148:151]\n\tbuffer_load_dwordx4 v10, s[12:15], s69 offen lds\n\ts_mov_b32 s69, 8552448\n\ts_add_u32 m0, s54, 36864\n\tv_mfma_f32_16x16x32_bf16 v[156:159], v[44:47], a[60:63], v[156:159]\n\tv_mfma_f32_16x16x32_bf16 v[156:159], v[40:43], a[48:51], v[156:159]\n\tds_read_b64_tr_b16 v[36:37], v237 offset:12288\n\tds_read_b64_tr_b16 v[38:39], v237 offset:12800\n\t" \
    "v_mfma_f32_16x16x32_bf16 v[164:167], v[40:43], a[52:55], v[164:167]\n\tbuffer_load_dwordx4 v10, s[12:15], s69 offen lds\n\ts_cmp_ge_u32 s43, 1\n\ts_cselect_b32 s68, s70, s43\n\ts_mul_i32 s68, s68, 36864\n\tv_mfma_f32_16x16x32_bf16 v[164:167], v[44:47], a[64:67], v[164:167]\n\ts_add_u32 s68, s68, s44\n\ts_add_u32 s68, s68, 737280\n\t" \
    "s_waitcnt lgkmcnt(8)\n\tv_mfma_f32_16x16x32_bf16 v[48:51], v[16:19], a[44:47], v[48:51]\n\tds_read_b64_tr_b16 v[40:41], v238 offset:8192\n\tds_read_b64_tr_b16 v[42:43], v238 offset:8704\n\tv_mfma_f32_16x16x32_bf16 v[48:51], v[20:23], a[56:59], v[48:51]\n\tv_mfma_f32_16x16x32_bf16 v[56:59], v[20:23], a[60:63], v[56:59]\n\tv_mfma_f32_16x16x32_bf16 v[56:59], v[16:19], a[48:51], v[56:59]\n\tds_read_b64_tr_b16 v[44:45], v238 offset:12288\n\t" \
    "ds_read_b64_tr_b16 v[46:47], v238 offset:12800\n\tv_mfma_f32_16x16x32_bf16 v[64:67], v[16:19], a[52:55], v[64:67]\n\tbuffer_load_dwordx4 a[20:23], v8, s[8:11], s68 offen offset:0 sc1\n\tv_mfma_f32_16x16x32_bf16 v[64:67], v[20:23], a[64:67], v[64:67]\n\ts_waitcnt lgkmcnt(8)\n\tv_mfma_f32_16x16x32_bf16 v[52:55], v[24:27], a[44:47], v[52:55]\n\tds_read_b64_tr_b16 v[16:17], v14 offset:16384\n\tds_read_b64_tr_b16 v[18:19], v14 offset:16896\n\t" \
    "v_mfma_f32_16x16x32_bf16 v[52:55], v[28:31], a[56:59], v[52:55]\n\tbuffer_load_dwordx4 a[24:27], v8, s[8:11], s68 offen offset:1024 sc1\n\tv_mfma_f32_16x16x32_bf16 v[60:63], v[28:31], a[60:63], v[60:63]\n\tv_mfma_f32_16x16x32_bf16 v[60:63], v[24:27], a[48:51], v[60:63]\n\tds_read_b64_tr_b16 v[20:21], v14 offset:20480\n\tds_read_b64_tr_b16 v[22:23], v14 offset:20992\n\tv_mfma_f32_16x16x32_bf16 v[68:71], v[24:27], a[52:55], v[68:71]\n\tv_mfma_f32_16x16x32_bf16 v[68:71], v[28:31], a[64:67], v[68:71]\n\t" \
    "s_waitcnt lgkmcnt(8)\n\tv_mfma_f32_16x16x32_bf16 v[72:75], v[32:35], a[44:47], v[72:75]\n\tds_read_b64_tr_b16 v[24:25], v15 offset:16384\n\tds_read_b64_tr_b16 v[26:27], v15 offset:16896\n\tv_mfma_f32_16x16x32_bf16 v[72:75], v[36:39], a[56:59], v[72:75]\n\tbuffer_load_dwordx4 a[28:31], v8, s[8:11], s68 offen offset:2048 sc1\n\ts_add_u32 s68, s44, 774144\n\tv_mfma_f32_16x16x32_bf16 v[80:83], v[36:39], a[60:63], v[80:83]\n\t" \
    "v_mfma_f32_16x16x32_bf16 v[80:83], v[32:35], a[48:51], v[80:83]\n\tds_read_b64_tr_b16 v[28:29], v15 offset:20480\n\tds_read_b64_tr_b16 v[30:31], v15 offset:20992\n\tv_mfma_f32_16x16x32_bf16 v[88:91], v[32:35], a[52:55], v[88:91]\n\tbuffer_load_dwordx4 a[32:35], v8, s[8:11], s68 offen offset:0 sc1\n\tv_mfma_f32_16x16x32_bf16 v[88:91], v[36:39], a[64:67], v[88:91]\n\ts_waitcnt lgkmcnt(8)\n\tv_mfma_f32_16x16x32_bf16 v[76:79], v[40:43], a[44:47], v[76:79]\n\t" \
    "ds_read_b64_tr_b16 v[32:33], v237 offset:16384\n\tds_read_b64_tr_b16 v[34:35], v237 offset:16896\n\tv_mfma_f32_16x16x32_bf16 v[76:79], v[44:47], a[56:59], v[76:79]\n\tbuffer_load_dwordx4 a[36:39], v8, s[8:11], s68 offen offset:1024 sc1\n\tv_mfma_f32_16x16x32_bf16 v[84:87], v[44:47], a[60:63], v[84:87]\n\tv_mfma_f32_16x16x32_bf16 v[84:87], v[40:43], a[48:51], v[84:87]\n\tds_read_b64_tr_b16 v[36:37], v237 offset:20480\n\tds_read_b64_tr_b16 v[38:39], v237 offset:20992\n\t" \
    "v_mfma_f32_16x16x32_bf16 v[92:95], v[40:43], a[52:55], v[92:95]\n\tv_mfma_f32_16x16x32_bf16 v[92:95], v[44:47], a[64:67], v[92:95]\n\ts_barrier\n\ts_waitcnt vmcnt(39) lgkmcnt(8)\n\tv_mfma_f32_16x16x32_bf16 v[120:123], v[16:19], v[192:195], v[120:123]\n\tds_read_b64_tr_b16 v[40:41], v238 offset:16384\n\tds_read_b64_tr_b16 v[42:43], v238 offset:16896\n\tv_mfma_f32_16x16x32_bf16 v[120:123], v[20:23], v[168:171], v[120:123]\n\t" \
    "buffer_load_dwordx4 a[40:43], v8, s[8:11], s68 offen offset:2048 sc1\n\ts_add_u32 s68, s44, 811008\n\tv_mfma_f32_16x16x32_bf16 v[128:131], v[20:23], v[172:175], v[128:131]\n\tv_mfma_f32_16x16x32_bf16 v[128:131], v[16:19], v[196:199], v[128:131]\n\tds_read_b64_tr_b16 v[44:45], v238 offset:20480\n\tds_read_b64_tr_b16 v[46:47], v238 offset:20992\n\tv_mfma_f32_16x16x32_bf16 v[136:139], v[16:19], v[200:203], v[136:139]\n\tbuffer_load_dwordx4 a[44:47], v8, s[8:11], s68 offen offset:0 sc1\n\t" \
    "v_mfma_f32_16x16x32_bf16 v[136:139], v[20:23], v[176:179], v[136:139]\n\ts_waitcnt lgkmcnt(8)\n\tv_mfma_f32_16x16x32_bf16 v[124:127], v[24:27], v[192:195], v[124:127]\n\tds_read_b64_tr_b16 v[16:17], v14 offset:24576\n\tds_read_b64_tr_b16 v[18:19], v14 offset:25088\n\tv_mfma_f32_16x16x32_bf16 v[124:127], v[28:31], v[168:171], v[124:127]\n\tbuffer_load_dwordx4 a[48:51], v8, s[8:11], s68 offen offset:1024 sc1\n\tv_mfma_f32_16x16x32_bf16 v[132:135], v[28:31], v[172:175], v[132:135]\n\t" \
    "v_mfma_f32_16x16x32_bf16 v[132:135], v[24:27], v[196:199], v[132:135]\n\tds_read_b64_tr_b16 v[20:21], v14 offset:28672\n\tds_read_b64_tr_b16 v[22:23], v14 offset:29184\n\tv_mfma_f32_16x16x32_bf16 v[140:143], v[24:27], v[200:203], v[140:143]\n\tbuffer_load_dwordx4 a[52:55], v8, s[8:11], s68 offen offset:2048 sc1\n\ts_add_u32 s68, s44, 847872\n\tv_mfma_f32_16x16x32_bf16 v[140:143], v[28:31], v[176:179], v[140:143]\n\ts_waitcnt lgkmcnt(8)\n\t" \
    "v_mfma_f32_16x16x32_bf16 v[144:147], v[32:35], v[192:195], v[144:147]\n\tds_read_b64_tr_b16 v[24:25], v15 offset:24576\n\tds_read_b64_tr_b16 v[26:27], v15 offset:25088\n\tv_mfma_f32_16x16x32_bf16 v[144:147], v[36:39], v[168:171], v[144:147]\n\tv_mfma_f32_16x16x32_bf16 v[152:155], v[36:39], v[172:175], v[152:155]\n\tv_mfma_f32_16x16x32_bf16 v[152:155], v[32:35], v[196:199], v[152:155]\n\tds_read_b64_tr_b16 v[28:29], v15 offset:28672\n\tds_read_b64_tr_b16 v[30:31], v15 offset:29184\n\t" \
    "v_mfma_f32_16x16x32_bf16 v[160:163], v[32:35], v[200:203], v[160:163]\n\tbuffer_load_dwordx4 a[56:59], v8, s[8:11], s68 offen offset:0 sc1\n\tv_mfma_f32_16x16x32_bf16 v[160:163], v[36:39], v[176:179], v[160:163]\n\ts_waitcnt lgkmcnt(8)\n\tv_mfma_f32_16x16x32_bf16 v[148:151], v[40:43], v[192:195], v[148:151]\n\tds_read_b64_tr_b16 v[32:33], v237 offset:24576\n\tds_read_b64_tr_b16 v[34:35], v237 offset:25088\n\tv_mfma_f32_16x16x32_bf16 v[148:151], v[44:47], v[168:171], v[148:151]\n\t" \
    "buffer_load_dwordx4 a[60:63], v8, s[8:11], s68 offen offset:1024 sc1\n\tv_mfma_f32_16x16x32_bf16 v[156:159], v[44:47], v[172:175], v[156:159]\n\tv_mfma_f32_16x16x32_bf16 v[156:159], v[40:43], v[196:199], v[156:159]\n\tds_read_b64_tr_b16 v[36:37], v237 offset:28672\n\tds_read_b64_tr_b16 v[38:39], v237 offset:29184\n\tv_mfma_f32_16x16x32_bf16 v[164:167], v[40:43], v[200:203], v[164:167]\n\tv_mfma_f32_16x16x32_bf16 v[164:167], v[44:47], v[176:179], v[164:167]\n\ts_waitcnt lgkmcnt(8)\n\t" \
    "v_mfma_f32_16x16x32_bf16 v[48:51], v[16:19], v[192:195], v[48:51]\n\tds_read_b64_tr_b16 v[40:41], v238 offset:24576\n\tds_read_b64_tr_b16 v[42:43], v238 offset:25088\n\tv_mfma_f32_16x16x32_bf16 v[48:51], v[20:23], v[168:171], v[48:51]\n\tbuffer_load_dwordx4 a[64:67], v8, s[8:11], s68 offen offset:2048 sc1\n\ts_add_u32 s68, s44, 884736\n\tv_mfma_f32_16x16x32_bf16 v[56:59], v[20:23], v[172:175], v[56:59]\n\tv_mfma_f32_16x16x32_bf16 v[56:59], v[16:19], v[196:199], v[56:59]\n\t" \
    "ds_read_b64_tr_b16 v[44:45], v238 offset:28672\n\tds_read_b64_tr_b16 v[46:47], v238 offset:29184\n\tv_mfma_f32_16x16x32_bf16 v[64:67], v[16:19], v[200:203], v[64:67]\n\tbuffer_load_dwordx4 a[116:119], v8, s[8:11], s68 offen offset:0 sc1\n\tv_mfma_f32_16x16x32_bf16 v[64:67], v[20:23], v[176:179], v[64:67]\n\ts_waitcnt lgkmcnt(8)\n\tv_mfma_f32_16x16x32_bf16 v[52:55], v[24:27], v[192:195], v[52:55]\n\tds_read_b64_tr_b16 v[16:17], v14 offset:32768\n\t" \
    "ds_read_b64_tr_b16 v[18:19], v14 offset:33280\n\tv_mfma_f32_16x16x32_bf16 v[52:55], v[28:31], v[168:171], v[52:55]\n\tv_mfma_f32_16x16x32_bf16 v[60:63], v[28:31], v[172:175], v[60:63]\n\tv_mfma_f32_16x16x32_bf16 v[60:63], v[24:27], v[196:199], v[60:63]\n\tds_read_b64_tr_b16 v[20:21], v14 offset:36864\n\tds_read_b64_tr_b16 v[22:23], v14 offset:37376\n\tv_mfma_f32_16x16x32_bf16 v[68:71], v[24:27], v[200:203], v[68:71]\n\tbuffer_load_dwordx4 a[120:123], v8, s[8:11], s68 offen offset:1024 sc1\n\t" \
    "v_mfma_f32_16x16x32_bf16 v[68:71], v[28:31], v[176:179], v[68:71]\n\ts_waitcnt lgkmcnt(8)\n\tv_mfma_f32_16x16x32_bf16 v[72:75], v[32:35], v[192:195], v[72:75]\n\tds_read_b64_tr_b16 v[24:25], v15 offset:32768\n\tds_read_b64_tr_b16 v[26:27], v15 offset:33280\n\tv_mfma_f32_16x16x32_bf16 v[72:75], v[36:39], v[168:171], v[72:75]\n\tbuffer_load_dwordx4 a[124:127], v8, s[8:11], s68 offen offset:2048 sc1\n\ts_add_u32 s68, s44, 921600\n\t" \
    "v_mfma_f32_16x16x32_bf16 v[80:83], v[36:39], v[172:175], v[80:83]\n\tv_mfma_f32_16x16x32_bf16 v[80:83], v[32:35], v[196:199], v[80:83]\n\tds_read_b64_tr_b16 v[28:29], v15 offset:36864\n\tds_read_b64_tr_b16 v[30:31], v15 offset:37376\n\tv_mfma_f32_16x16x32_bf16 v[88:91], v[32:35], v[200:203], v[88:91]\n\tv_mfma_f32_16x16x32_bf16 v[88:91], v[36:39], v[176:179], v[88:91]\n\ts_waitcnt lgkmcnt(8)\n\tv_mfma_f32_16x16x32_bf16 v[76:79], v[40:43], v[192:195], v[76:79]\n\t" \
    "ds_read_b64_tr_b16 v[32:33], v237 offset:32768\n\tds_read_b64_tr_b16 v[34:35], v237 offset:33280\n\tv_mfma_f32_16x16x32_bf16 v[76:79], v[44:47], v[168:171], v[76:79]\n\tbuffer_load_dwordx4 a[128:131], v8, s[8:11], s68 offen offset:0 sc1\n\tv_mfma_f32_16x16x32_bf16 v[84:87], v[44:47], v[172:175], v[84:87]\n\tv_mfma_f32_16x16x32_bf16 v[84:87], v[40:43], v[196:199], v[84:87]\n\tds_read_b64_tr_b16 v[36:37], v237 offset:36864\n\tds_read_b64_tr_b16 v[38:39], v237 offset:37376\n\t" \
    "v_mfma_f32_16x16x32_bf16 v[92:95], v[40:43], v[200:203], v[92:95]\n\tbuffer_load_dwordx4 a[132:135], v8, s[8:11], s68 offen offset:1024 sc1\n\tv_mfma_f32_16x16x32_bf16 v[92:95], v[44:47], v[176:179], v[92:95]\n\ts_barrier\n\ts_waitcnt vmcnt(45) lgkmcnt(8)\n\tv_mfma_f32_16x16x32_bf16 v[120:123], v[16:19], a[68:71], v[120:123]\n\tds_read_b64_tr_b16 v[40:41], v238 offset:32768\n\tds_read_b64_tr_b16 v[42:43], v238 offset:33280\n\t" \
    "v_mfma_f32_16x16x32_bf16 v[120:123], v[20:23], a[80:83], v[120:123]\n\tbuffer_load_dwordx4 a[136:139], v8, s[8:11], s68 offen offset:2048 sc1\n\ts_mov_b32 s69, 8847360\n\ts_add_u32 m0, s54, 40960\n\tv_mfma_f32_16x16x32_bf16 v[128:131], v[20:23], a[84:87], v[128:131]\n\tv_mfma_f32_16x16x32_bf16 v[128:131], v[16:19], a[72:75], v[128:131]\n\tds_read_b64_tr_b16 v[44:45], v238 offset:36864\n\tds_read_b64_tr_b16 v[46:47], v238 offset:37376\n\t" \
    "v_mfma_f32_16x16x32_bf16 v[136:139], v[16:19], a[76:79], v[136:139]\n\tbuffer_load_dwordx4 v10, s[12:15], s69 offen lds\n\ts_mov_b32 s69, 9142272\n\ts_add_u32 m0, s54, 45056\n\tv_mfma_f32_16x16x32_bf16 v[136:139], v[20:23], a[88:91], v[136:139]\n\ts_waitcnt lgkmcnt(8)\n\tv_mfma_f32_16x16x32_bf16 v[124:127], v[24:27], a[68:71], v[124:127]\n\tds_read_b64_tr_b16 v[16:17], v14 offset:40960\n\t" \
    "ds_read_b64_tr_b16 v[18:19], v14 offset:41472\n\tv_mfma_f32_16x16x32_bf16 v[124:127], v[28:31], a[80:83], v[124:127]\n\tv_mfma_f32_16x16x32_bf16 v[132:135], v[28:31], a[84:87], v[132:135]\n\tv_mfma_f32_16x16x32_bf16 v[132:135], v[24:27], a[72:75], v[132:135]\n\tds_read_b64_tr_b16 v[20:21], v14 offset:45056\n\tds_read_b64_tr_b16 v[22:23], v14 offset:45568\n\tv_mfma_f32_16x16x32_bf16 v[140:143], v[24:27], a[76:79], v[140:143]\n\tbuffer_load_dwordx4 v10, s[12:15], s69 offen lds\n\t" \
    "s_cmp_ge_u32 s43, 9\n\ts_cselect_b32 s69, s70, s43\n\ts_mul_i32 s69, s69, 294912\n\tv_mfma_f32_16x16x32_bf16 v[140:143], v[28:31], a[88:91], v[140:143]\n\ts_add_u32 s69, s69, 3538944\n\ts_add_u32 m0, s54, 49152\n\ts_waitcnt lgkmcnt(8)\n\tv_mfma_f32_16x16x32_bf16 v[144:147], v[32:35], a[68:71], v[144:147]\n\t" \
    "ds_read_b64_tr_b16 v[24:25], v15 offset:40960\n\tds_read_b64_tr_b16 v[26:27], v15 offset:41472\n\tv_mfma_f32_16x16x32_bf16 v[144:147], v[36:39], a[80:83], v[144:147]\n\tbuffer_load_dwordx4 v11, s[12:15], s69 offen lds\n\ts_cmp_ge_u32 s43, 8\n\ts_cselect_b32 s69, s70, s43\n\ts_mul_i32 s69, s69, 294912\n\tv_mfma_f32_16x16x32_bf16 v[152:155], v[36:39], a[84:87], v[152:155]\n\t" \
    "s_add_u32 s69, s69, 3833856\n\ts_add_u32 m0, s54, 53248\n\tv_mfma_f32_16x16x32_bf16 v[152:155], v[32:35], a[72:75], v[152:155]\n\tds_read_b64_tr_b16 v[28:29], v15 offset:45056\n\tds_read_b64_tr_b16 v[30:31], v15 offset:45568\n\tv_mfma_f32_16x16x32_bf16 v[160:163], v[32:35], a[76:79], v[160:163]\n\tbuffer_load_dwordx4 v11, s[12:15], s69 offen lds\n\ts_cmp_ge_u32 s43, 7\n\t" \
    "s_cselect_b32 s69, s70, s43\n\ts_mul_i32 s69, s69, 294912\n\tv_mfma_f32_16x16x32_bf16 v[160:163], v[36:39], a[88:91], v[160:163]\n\ts_add_u32 s69, s69, 4128768\n\ts_add_u32 m0, s54, 57344\n\ts_waitcnt lgkmcnt(8)\n\tv_mfma_f32_16x16x32_bf16 v[148:151], v[40:43], a[68:71], v[148:151]\n\tds_read_b64_tr_b16 v[32:33], v237 offset:40960\n\t" \
    "ds_read_b64_tr_b16 v[34:35], v237 offset:41472\n\tv_mfma_f32_16x16x32_bf16 v[148:151], v[44:47], a[80:83], v[148:151]\n\tv_mfma_f32_16x16x32_bf16 v[156:159], v[44:47], a[84:87], v[156:159]\n\tv_mfma_f32_16x16x32_bf16 v[156:159], v[40:43], a[72:75], v[156:159]\n\tds_read_b64_tr_b16 v[36:37], v237 offset:45056\n\tds_read_b64_tr_b16 v[38:39], v237 offset:45568\n\tv_mfma_f32_16x16x32_bf16 v[164:167], v[40:43], a[76:79], v[164:167]\n\tbuffer_load_dwordx4 v11, s[12:15], s69 offen lds\n\t" \
    "s_cmp_ge_u32 s43, 6\n\ts_cselect_b32 s69, s70, s43\n\ts_mul_i32 s69, s69, 294912\n\tv_mfma_f32_16x16x32_bf16 v[164:167], v[44:47], a[88:91], v[164:167]\n\ts_add_u32 s69, s69, 4423680\n\ts_add_u32 m0, s54, 61440\n\ts_waitcnt vmcnt(45) lgkmcnt(8)\n\tv_mfma_f32_16x16x32_bf16 v[120:123], v[16:19], v[180:183], v[120:123]\n\t" \
    "ds_read_b64_tr_b16 v[40:41], v238 offset:40960\n\tds_read_b64_tr_b16 v[42:43], v238 offset:41472\n\tv_mfma_f32_16x16x32_bf16 v[120:123], v[20:23], v[204:207], v[120:123]\n\tbuffer_load_dwordx4 v11, s[12:15], s69 offen lds\n\ts_cmp_ge_u32 s43, 5\n\ts_cselect_b32 s69, s70, s43\n\ts_mul_i32 s69, s69, 294912\n\tv_mfma_f32_16x16x32_bf16 v[128:131], v[20:23], v[208:211], v[128:131]\n\t" \
    "s_add_u32 s69, s69, 4718592\n\ts_add_u32 m0, s54, 65536\n\tv_mfma_f32_16x16x32_bf16 v[128:131], v[16:19], v[184:187], v[128:131]\n\tds_read_b64_tr_b16 v[44:45], v238 offset:45056\n\tds_read_b64_tr_b16 v[46:47], v238 offset:45568\n\tv_mfma_f32_16x16x32_bf16 v[136:139], v[16:19], v[188:191], v[136:139]\n\tv_mfma_f32_16x16x32_bf16 v[136:139], v[20:23], v[212:215], v[136:139]\n\ts_waitcnt lgkmcnt(8)\n\t" \
    "v_mfma_f32_16x16x32_bf16 v[124:127], v[24:27], v[180:183], v[124:127]\n\tds_read_b64_tr_b16 v[16:17], v14 offset:49152\n\tds_read_b64_tr_b16 v[18:19], v14 offset:49664\n\tv_mfma_f32_16x16x32_bf16 v[124:127], v[28:31], v[204:207], v[124:127]\n\tbuffer_load_dwordx4 v11, s[12:15], s69 offen lds\n\ts_cmp_ge_u32 s43, 4\n\ts_cselect_b32 s69, s70, s43\n\ts_mul_i32 s69, s69, 294912\n\t" \
    "v_mfma_f32_16x16x32_bf16 v[132:135], v[28:31], v[208:211], v[132:135]\n\ts_add_u32 s69, s69, 5013504\n\ts_add_u32 m0, s54, 69632\n\tv_mfma_f32_16x16x32_bf16 v[132:135], v[24:27], v[184:187], v[132:135]\n\tds_read_b64_tr_b16 v[20:21], v14 offset:53248\n\tds_read_b64_tr_b16 v[22:23], v14 offset:53760\n\tv_mfma_f32_16x16x32_bf16 v[140:143], v[24:27], v[188:191], v[140:143]\n\tbuffer_load_dwordx4 v11, s[12:15], s69 offen lds\n\t" \
    "s_add_u32 s68, s44, 958464\n\tv_mfma_f32_16x16x32_bf16 v[140:143], v[28:31], v[212:215], v[140:143]\n\ts_waitcnt lgkmcnt(8)\n\tv_mfma_f32_16x16x32_bf16 v[144:147], v[32:35], v[180:183], v[144:147]\n\tds_read_b64_tr_b16 v[24:25], v15 offset:49152\n\tds_read_b64_tr_b16 v[26:27], v15 offset:49664\n\tv_mfma_f32_16x16x32_bf16 v[144:147], v[36:39], v[204:207], v[144:147]\n\tbuffer_load_dwordx4 v[168:171], v8, s[8:11], s68 offen offset:0 sc1\n\t" \
    "v_mfma_f32_16x16x32_bf16 v[152:155], v[36:39], v[208:211], v[152:155]\n\tv_mfma_f32_16x16x32_bf16 v[152:155], v[32:35], v[184:187], v[152:155]\n\tds_read_b64_tr_b16 v[28:29], v15 offset:53248\n\tds_read_b64_tr_b16 v[30:31], v15 offset:53760\n\tv_mfma_f32_16x16x32_bf16 v[160:163], v[32:35], v[188:191], v[160:163]\n\tv_mfma_f32_16x16x32_bf16 v[160:163], v[36:39], v[212:215], v[160:163]\n\ts_waitcnt lgkmcnt(8)\n\tv_mfma_f32_16x16x32_bf16 v[148:151], v[40:43], v[180:183], v[148:151]\n\t" \
    "ds_read_b64_tr_b16 v[32:33], v237 offset:49152\n\tds_read_b64_tr_b16 v[34:35], v237 offset:49664\n\tv_mfma_f32_16x16x32_bf16 v[148:151], v[44:47], v[204:207], v[148:151]\n\tbuffer_load_dwordx4 v[172:175], v8, s[8:11], s68 offen offset:1024 sc1\n\tv_mfma_f32_16x16x32_bf16 v[156:159], v[44:47], v[208:211], v[156:159]\n\tv_mfma_f32_16x16x32_bf16 v[156:159], v[40:43], v[184:187], v[156:159]\n\tds_read_b64_tr_b16 v[36:37], v237 offset:53248\n\tds_read_b64_tr_b16 v[38:39], v237 offset:53760\n\t" \
    "v_mfma_f32_16x16x32_bf16 v[164:167], v[40:43], v[188:191], v[164:167]\n\tbuffer_load_dwordx4 v[176:179], v8, s[8:11], s68 offen offset:2048 sc1\n\ts_add_u32 s68, s44, 995328\n\tv_mfma_f32_16x16x32_bf16 v[164:167], v[44:47], v[212:215], v[164:167]\n\ts_waitcnt vmcnt(49)\n\ts_barrier\n\ts_waitcnt vmcnt(43) lgkmcnt(8)\n\tv_mfma_f32_16x16x32_bf16 v[120:123], v[16:19], a[92:95], v[120:123]\n\t" \
    "ds_read_b64_tr_b16 v[40:41], v238 offset:49152\n\tds_read_b64_tr_b16 v[42:43], v238 offset:49664\n\tv_mfma_f32_16x16x32_bf16 v[120:123], v[20:23], v[216:219], v[120:123]\n\tbuffer_load_dwordx4 a[140:143], v8, s[8:11], s68 offen offset:0 sc1\n\tv_mfma_f32_16x16x32_bf16 v[128:131], v[20:23], a[0:3], v[128:131]\n\tv_mfma_f32_16x16x32_bf16 v[128:131], v[16:19], a[96:99], v[128:131]\n\tds_read_b64_tr_b16 v[44:45], v238 offset:53248\n\tds_read_b64_tr_b16 v[46:47], v238 offset:53760\n\t" \
    "v_mfma_f32_16x16x32_bf16 v[136:139], v[16:19], a[100:103], v[136:139]\n\tbuffer_load_dwordx4 a[144:147], v8, s[8:11], s68 offen offset:1024 sc1\n\tv_mfma_f32_16x16x32_bf16 v[136:139], v[20:23], a[4:7], v[136:139]\n\ts_waitcnt lgkmcnt(8)\n\tv_mfma_f32_16x16x32_bf16 v[124:127], v[24:27], a[92:95], v[124:127]\n\tds_read_b64_tr_b16 v[16:17], v14 offset:57344\n\tds_read_b64_tr_b16 v[18:19], v14 offset:57856\n\tv_mfma_f32_16x16x32_bf16 v[124:127], v[28:31], v[216:219], v[124:127]\n\t" \
    "v_mfma_f32_16x16x32_bf16 v[132:135], v[28:31], a[0:3], v[132:135]\n\tv_mfma_f32_16x16x32_bf16 v[132:135], v[24:27], a[96:99], v[132:135]\n\tds_read_b64_tr_b16 v[20:21], v14 offset:61440\n\tds_read_b64_tr_b16 v[22:23], v14 offset:61952\n\tv_mfma_f32_16x16x32_bf16 v[140:143], v[24:27], a[100:103], v[140:143]\n\tbuffer_load_dwordx4 a[148:151], v8, s[8:11], s68 offen offset:2048 sc1\n\ts_add_u32 s68, s44, 1032192\n\tv_mfma_f32_16x16x32_bf16 v[140:143], v[28:31], a[4:7], v[140:143]\n\t" \
    "s_waitcnt lgkmcnt(8)\n\tv_mfma_f32_16x16x32_bf16 v[144:147], v[32:35], a[92:95], v[144:147]\n\tds_read_b64_tr_b16 v[24:25], v15 offset:57344\n\tds_read_b64_tr_b16 v[26:27], v15 offset:57856\n\tv_mfma_f32_16x16x32_bf16 v[144:147], v[36:39], v[216:219], v[144:147]\n\tbuffer_load_dwordx4 a[152:155], v8, s[8:11], s68 offen offset:0 sc1\n\tv_mfma_f32_16x16x32_bf16 v[152:155], v[36:39], a[0:3], v[152:155]\n\tv_mfma_f32_16x16x32_bf16 v[152:155], v[32:35], a[96:99], v[152:155]\n\t" \
    "ds_read_b64_tr_b16 v[28:29], v15 offset:61440\n\tds_read_b64_tr_b16 v[30:31], v15 offset:61952\n\tv_mfma_f32_16x16x32_bf16 v[160:163], v[32:35], a[100:103], v[160:163]\n\tv_mfma_f32_16x16x32_bf16 v[160:163], v[36:39], a[4:7], v[160:163]\n\ts_waitcnt lgkmcnt(8)\n\tv_mfma_f32_16x16x32_bf16 v[148:151], v[40:43], a[92:95], v[148:151]\n\tds_read_b64_tr_b16 v[32:33], v237 offset:57344\n\tds_read_b64_tr_b16 v[34:35], v237 offset:57856\n\t" \
    "v_mfma_f32_16x16x32_bf16 v[148:151], v[44:47], v[216:219], v[148:151]\n\tbuffer_load_dwordx4 a[156:159], v8, s[8:11], s68 offen offset:1024 sc1\n\tv_mfma_f32_16x16x32_bf16 v[156:159], v[44:47], a[0:3], v[156:159]\n\tv_mfma_f32_16x16x32_bf16 v[156:159], v[40:43], a[96:99], v[156:159]\n\tds_read_b64_tr_b16 v[36:37], v237 offset:61440\n\tds_read_b64_tr_b16 v[38:39], v237 offset:61952\n\tv_mfma_f32_16x16x32_bf16 v[164:167], v[40:43], a[100:103], v[164:167]\n\tbuffer_load_dwordx4 a[160:163], v8, s[8:11], s68 offen offset:2048 sc1\n\t" \
    "s_add_u32 s68, s44, 1069056\n\tv_mfma_f32_16x16x32_bf16 v[164:167], v[44:47], a[4:7], v[164:167]\n\ts_waitcnt vmcnt(43) lgkmcnt(8)\n\tv_mfma_f32_16x16x32_bf16 v[120:123], v[16:19], a[8:11], v[120:123]\n\tds_read_b64_tr_b16 v[40:41], v238 offset:57344\n\tds_read_b64_tr_b16 v[42:43], v238 offset:57856\n\tv_mfma_f32_16x16x32_bf16 v[120:123], v[20:23], a[104:107], v[120:123]\n\tv_mfma_f32_16x16x32_bf16 v[128:131], v[20:23], a[108:111], v[128:131]\n\t" \
    "v_mfma_f32_16x16x32_bf16 v[128:131], v[16:19], a[12:15], v[128:131]\n\tds_read_b64_tr_b16 v[44:45], v238 offset:61440\n\tds_read_b64_tr_b16 v[46:47], v238 offset:61952\n\tv_mfma_f32_16x16x32_bf16 v[136:139], v[16:19], a[16:19], v[136:139]\n\tbuffer_load_dwordx4 a[164:167], v8, s[8:11], s68 offen offset:0 sc1\n\tv_mfma_f32_16x16x32_bf16 v[136:139], v[20:23], a[112:115], v[136:139]\n\ts_waitcnt lgkmcnt(8)\n\tv_mfma_f32_16x16x32_bf16 v[124:127], v[24:27], a[8:11], v[124:127]\n\t" \
    "ds_read_b64_tr_b16 v[16:17], v0 offset:0\n\tds_read_b64_tr_b16 v[18:19], v0 offset:512\n\tv_mfma_f32_16x16x32_bf16 v[124:127], v[28:31], a[104:107], v[124:127]\n\tbuffer_load_dwordx4 a[168:171], v8, s[8:11], s68 offen offset:1024 sc1\n\tv_mfma_f32_16x16x32_bf16 v[132:135], v[28:31], a[108:111], v[132:135]\n\tv_mfma_f32_16x16x32_bf16 v[132:135], v[24:27], a[12:15], v[132:135]\n\tds_read_b64_tr_b16 v[20:21], v0 offset:4096\n\tds_read_b64_tr_b16 v[22:23], v0 offset:4608\n\t" \
    "v_mfma_f32_16x16x32_bf16 v[140:143], v[24:27], a[16:19], v[140:143]\n\tv_mfma_f32_16x16x32_bf16 v[140:143], v[28:31], a[112:115], v[140:143]\n\ts_waitcnt lgkmcnt(8)\n\tv_mfma_f32_16x16x32_bf16 v[144:147], v[32:35], a[8:11], v[144:147]\n\tds_read_b64_tr_b16 v[24:25], v1 offset:0\n\tds_read_b64_tr_b16 v[26:27], v1 offset:512\n\tv_mfma_f32_16x16x32_bf16 v[144:147], v[36:39], a[104:107], v[144:147]\n\tbuffer_load_dwordx4 a[172:175], v8, s[8:11], s68 offen offset:2048 sc1\n\t" \
    "s_add_u32 s68, s44, 1105920\n\tv_mfma_f32_16x16x32_bf16 v[152:155], v[36:39], a[108:111], v[152:155]\n\tv_mfma_f32_16x16x32_bf16 v[152:155], v[32:35], a[12:15], v[152:155]\n\tds_read_b64_tr_b16 v[28:29], v1 offset:4096\n\tds_read_b64_tr_b16 v[30:31], v1 offset:4608\n\tv_mfma_f32_16x16x32_bf16 v[160:163], v[32:35], a[16:19], v[160:163]\n\tbuffer_load_dwordx4 a[176:179], v8, s[8:11], s68 offen offset:0 sc1\n\tv_mfma_f32_16x16x32_bf16 v[160:163], v[36:39], a[112:115], v[160:163]\n\t" \
    "s_waitcnt lgkmcnt(8)\n\tv_mfma_f32_16x16x32_bf16 v[148:151], v[40:43], a[8:11], v[148:151]\n\tds_read_b64_tr_b16 v[32:33], v2 offset:0\n\tds_read_b64_tr_b16 v[34:35], v2 offset:512\n\tv_mfma_f32_16x16x32_bf16 v[148:151], v[44:47], a[104:107], v[148:151]\n\tbuffer_load_dwordx4 a[180:183], v8, s[8:11], s68 offen offset:1024 sc1\n\tv_mfma_f32_16x16x32_bf16 v[156:159], v[44:47], a[108:111], v[156:159]\n\tv_mfma_f32_16x16x32_bf16 v[156:159], v[40:43], a[12:15], v[156:159]\n\t" \
    "ds_read_b64_tr_b16 v[36:37], v2 offset:4096\n\tds_read_b64_tr_b16 v[38:39], v2 offset:4608\n\tv_mfma_f32_16x16x32_bf16 v[164:167], v[40:43], a[16:19], v[164:167]\n\tv_mfma_f32_16x16x32_bf16 v[164:167], v[44:47], a[112:115], v[164:167]\n\ts_waitcnt vmcnt(40)\n\ts_barrier\n\ts_waitcnt vmcnt(34) lgkmcnt(8)\n\tv_mfma_f32_16x16x32_bf16 v[120:123], v[16:19], a[20:23], v[120:123]\n\t" \
    "ds_read_b64_tr_b16 v[40:41], v3 offset:0\n\tds_read_b64_tr_b16 v[42:43], v3 offset:512\n\tv_mfma_f32_16x16x32_bf16 v[120:123], v[20:23], a[32:35], v[120:123]\n\tbuffer_load_dwordx4 a[184:187], v8, s[8:11], s68 offen offset:2048 sc1\n\ts_add_u32 s68, s44, 1142784\n\tv_mfma_f32_16x16x32_bf16 v[128:131], v[20:23], a[36:39], v[128:131]\n\tv_mfma_f32_16x16x32_bf16 v[128:131], v[16:19], a[24:27], v[128:131]\n\tds_read_b64_tr_b16 v[44:45], v3 offset:4096\n\t" \
    "ds_read_b64_tr_b16 v[46:47], v3 offset:4608\n\tv_mfma_f32_16x16x32_bf16 v[136:139], v[16:19], a[28:31], v[136:139]\n\tbuffer_load_dwordx4 a[188:191], v8, s[8:11], s68 offen offset:0 sc1\n\tv_mfma_f32_16x16x32_bf16 v[136:139], v[20:23], a[40:43], v[136:139]\n\ts_waitcnt lgkmcnt(8)\n\tv_mfma_f32_16x16x32_bf16 v[124:127], v[24:27], a[20:23], v[124:127]\n\tds_read_b64_tr_b16 v[16:17], v0 offset:8192\n\tds_read_b64_tr_b16 v[18:19], v0 offset:8704\n\t" \
    "v_mfma_f32_16x16x32_bf16 v[124:127], v[28:31], a[32:35], v[124:127]\n\tbuffer_load_dwordx4 a[192:195], v8, s[8:11], s68 offen offset:1024 sc1\n\tv_mfma_f32_16x16x32_bf16 v[132:135], v[28:31], a[36:39], v[132:135]\n\tv_mfma_f32_16x16x32_bf16 v[132:135], v[24:27], a[24:27], v[132:135]\n\tds_read_b64_tr_b16 v[20:21], v0 offset:12288\n\tds_read_b64_tr_b16 v[22:23], v0 offset:12800\n\tv_mfma_f32_16x16x32_bf16 v[140:143], v[24:27], a[28:31], v[140:143]\n\tbuffer_load_dwordx4 a[196:199], v8, s[8:11], s68 offen offset:2048 sc1\n\t" \
    "s_cmp_ge_u32 s43, 3\n\ts_cselect_b32 s69, s70, s43\n\ts_mul_i32 s69, s69, 294912\n\tv_mfma_f32_16x16x32_bf16 v[140:143], v[28:31], a[40:43], v[140:143]\n\ts_add_u32 s69, s69, 5308416\n\ts_add_u32 m0, s54, 73728\n\ts_waitcnt lgkmcnt(8)\n\tv_mfma_f32_16x16x32_bf16 v[144:147], v[32:35], a[20:23], v[144:147]\n\t" \
    "ds_read_b64_tr_b16 v[24:25], v1 offset:8192\n\tds_read_b64_tr_b16 v[26:27], v1 offset:8704\n\tv_mfma_f32_16x16x32_bf16 v[144:147], v[36:39], a[32:35], v[144:147]\n\tv_mfma_f32_16x16x32_bf16 v[152:155], v[36:39], a[36:39], v[152:155]\n\tv_mfma_f32_16x16x32_bf16 v[152:155], v[32:35], a[24:27], v[152:155]\n\tds_read_b64_tr_b16 v[28:29], v1 offset:12288\n\tds_read_b64_tr_b16 v[30:31], v1 offset:12800\n\tv_mfma_f32_16x16x32_bf16 v[160:163], v[32:35], a[28:31], v[160:163]\n\t" \
    "buffer_load_dwordx4 v11, s[12:15], s69 offen lds\n\ts_cmp_ge_u32 s43, 2\n\ts_cselect_b32 s69, s70, s43\n\ts_mul_i32 s69, s69, 294912\n\tv_mfma_f32_16x16x32_bf16 v[160:163], v[36:39], a[40:43], v[160:163]\n\ts_add_u32 s69, s69, 5603328\n\ts_add_u32 m0, s54, 77824\n\ts_waitcnt lgkmcnt(8)\n\t" \
    "v_mfma_f32_16x16x32_bf16 v[148:151], v[40:43], a[20:23], v[148:151]\n\tds_read_b64_tr_b16 v[32:33], v0 offset:16384\n\tds_read_b64_tr_b16 v[34:35], v0 offset:16896\n\tv_mfma_f32_16x16x32_bf16 v[148:151], v[44:47], a[32:35], v[148:151]\n\tbuffer_load_dwordx4 v11, s[12:15], s69 offen lds\n\ts_cmp_ge_u32 s43, 1\n\ts_cselect_b32 s69, s70, s43\n\ts_mul_i32 s69, s69, 294912\n\t" \
    "v_mfma_f32_16x16x32_bf16 v[156:159], v[44:47], a[36:39], v[156:159]\n\ts_add_u32 s69, s69, 5898240\n\ts_add_u32 m0, s54, 81920\n\tv_mfma_f32_16x16x32_bf16 v[156:159], v[40:43], a[24:27], v[156:159]\n\tds_read_b64_tr_b16 v[36:37], v0 offset:20480\n\tds_read_b64_tr_b16 v[38:39], v0 offset:20992\n\tv_mfma_f32_16x16x32_bf16 v[164:167], v[40:43], a[28:31], v[164:167]\n\tv_mfma_f32_16x16x32_bf16 v[164:167], v[44:47], a[40:43], v[164:167]\n\t" \
    "s_waitcnt vmcnt(34) lgkmcnt(8)\n\tv_mfma_f32_16x16x32_bf16 v[120:123], v[16:19], a[44:47], v[120:123]\n\tds_read_b64_tr_b16 v[40:41], v1 offset:16384\n\tds_read_b64_tr_b16 v[42:43], v1 offset:16896\n\tv_mfma_f32_16x16x32_bf16 v[120:123], v[20:23], a[56:59], v[120:123]\n\tbuffer_load_dwordx4 v11, s[12:15], s69 offen lds\n\ts_mov_b32 s69, 6193152\n\ts_add_u32 m0, s54, 86016\n\t" \
    "v_mfma_f32_16x16x32_bf16 v[128:131], v[20:23], a[60:63], v[128:131]\n\tv_mfma_f32_16x16x32_bf16 v[128:131], v[16:19], a[48:51], v[128:131]\n\tds_read_b64_tr_b16 v[44:45], v1 offset:20480\n\tds_read_b64_tr_b16 v[46:47], v1 offset:20992\n\tv_mfma_f32_16x16x32_bf16 v[136:139], v[16:19], a[52:55], v[136:139]\n\tbuffer_load_dwordx4 v11, s[12:15], s69 offen lds\n\ts_mov_b32 s69, 6488064\n\ts_add_u32 m0, s54, 90112\n\t" \
    "v_mfma_f32_16x16x32_bf16 v[136:139], v[20:23], a[64:67], v[136:139]\n\ts_waitcnt lgkmcnt(8)\n\tv_mfma_f32_16x16x32_bf16 v[124:127], v[24:27], a[44:47], v[124:127]\n\tds_read_b64_tr_b16 v[16:17], v0 offset:24576\n\tds_read_b64_tr_b16 v[18:19], v0 offset:25088\n\tv_mfma_f32_16x16x32_bf16 v[124:127], v[28:31], a[56:59], v[124:127]\n\tv_mfma_f32_16x16x32_bf16 v[132:135], v[28:31], a[60:63], v[132:135]\n\tv_mfma_f32_16x16x32_bf16 v[132:135], v[24:27], a[48:51], v[132:135]\n\t" \
    "ds_read_b64_tr_b16 v[20:21], v0 offset:28672\n\tds_read_b64_tr_b16 v[22:23], v0 offset:29184\n\tv_mfma_f32_16x16x32_bf16 v[140:143], v[24:27], a[52:55], v[140:143]\n\tbuffer_load_dwordx4 v11, s[12:15], s69 offen lds\n\ts_mov_b32 s69, 6782976\n\ts_add_u32 m0, s54, 94208\n\tv_mfma_f32_16x16x32_bf16 v[140:143], v[28:31], a[64:67], v[140:143]\n\ts_waitcnt vmcnt(31) lgkmcnt(8)\n\t" \
    "v_mfma_f32_16x16x32_bf16 v[120:123], v[32:35], a[116:119], v[120:123]\n\tds_read_b64_tr_b16 v[24:25], v1 offset:24576\n\tds_read_b64_tr_b16 v[26:27], v1 offset:25088\n\tv_mfma_f32_16x16x32_bf16 v[120:123], v[36:39], a[128:131], v[120:123]\n\tbuffer_load_dwordx4 v11, s[12:15], s69 offen lds\n\ts_mov_b32 s69, 7077888\n\ts_add_u32 m0, s54, 98304\n\tv_mfma_f32_16x16x32_bf16 v[128:131], v[36:39], a[132:135], v[128:131]\n\t" \
    "v_mfma_f32_16x16x32_bf16 v[128:131], v[32:35], a[120:123], v[128:131]\n\tds_read_b64_tr_b16 v[28:29], v1 offset:28672\n\tds_read_b64_tr_b16 v[30:31], v1 offset:29184\n\tv_mfma_f32_16x16x32_bf16 v[136:139], v[32:35], a[124:127], v[136:139]\n\tbuffer_load_dwordx4 v11, s[12:15], s69 offen lds\n\ts_mov_b32 s69, 7372800\n\ts_add_u32 m0, s54, 102400\n\tv_mfma_f32_16x16x32_bf16 v[136:139], v[36:39], a[136:139], v[136:139]\n\t" \
    "s_waitcnt lgkmcnt(8)\n\tv_mfma_f32_16x16x32_bf16 v[124:127], v[40:43], a[116:119], v[124:127]\n\tds_read_b64_tr_b16 v[32:33], v0 offset:32768\n\tds_read_b64_tr_b16 v[34:35], v0 offset:33280\n\tv_mfma_f32_16x16x32_bf16 v[124:127], v[44:47], a[128:131], v[124:127]\n\tv_mfma_f32_16x16x32_bf16 v[132:135], v[44:47], a[132:135], v[132:135]\n\tv_mfma_f32_16x16x32_bf16 v[132:135], v[40:43], a[120:123], v[132:135]\n\tds_read_b64_tr_b16 v[36:37], v0 offset:36864\n\t" \
    "ds_read_b64_tr_b16 v[38:39], v0 offset:37376\n\tv_mfma_f32_16x16x32_bf16 v[140:143], v[40:43], a[124:127], v[140:143]\n\tbuffer_load_dwordx4 v11, s[12:15], s69 offen lds\n\ts_mov_b32 s69, 7667712\n\ts_add_u32 m0, s54, 106496\n\tv_mfma_f32_16x16x32_bf16 v[140:143], v[44:47], a[136:139], v[140:143]\n\ts_waitcnt vmcnt(26)\n\ts_barrier\n\t" \
    "s_waitcnt vmcnt(20) lgkmcnt(8)\n\tv_mfma_f32_16x16x32_bf16 v[120:123], v[16:19], v[168:171], v[120:123]\n\tds_read_b64_tr_b16 v[40:41], v1 offset:32768\n\tds_read_b64_tr_b16 v[42:43], v1 offset:33280\n\tv_mfma_f32_16x16x32_bf16 v[120:123], v[20:23], a[140:143], v[120:123]\n\tbuffer_load_dwordx4 v11, s[12:15], s69 offen lds\n\ts_mov_b32 s69, 7962624\n\ts_add_u32 m0, s54, 110592\n\t" \
    "v_mfma_f32_16x16x32_bf16 v[128:131], v[20:23], a[144:147], v[128:131]\n\tv_mfma_f32_16x16x32_bf16 v[128:131], v[16:19], v[172:175], v[128:131]\n\tds_read_b64_tr_b16 v[44:45], v1 offset:36864\n\tds_read_b64_tr_b16 v[46:47], v1 offset:37376\n\tv_mfma_f32_16x16x32_bf16 v[136:139], v[16:19], v[176:179], v[136:139]\n\tbuffer_load_dwordx4 v11, s[12:15], s69 offen lds\n\ts_mov_b32 s69, 8257536\n\ts_add_u32 m0, s54, 114688\n\t" \
    "v_mfma_f32_16x16x32_bf16 v[136:139], v[20:23], a[148:151], v[136:139]\n\ts_waitcnt lgkmcnt(8)\n\tv_mfma_f32_16x16x32_bf16 v[124:127], v[24:27], v[168:171], v[124:127]\n\tds_read_b64_tr_b16 v[16:17], v0 offset:40960\n\tds_read_b64_tr_b16 v[18:19], v0 offset:41472\n\tv_mfma_f32_16x16x32_bf16 v[124:127], v[28:31], a[140:143], v[124:127]\n\tbuffer_load_dwordx4 v11, s[12:15], s69 offen lds\n\ts_mov_b32 s69, 8552448\n\t" \
    "s_add_u32 m0, s54, 118784\n\tv_mfma_f32_16x16x32_bf16 v[132:135], v[28:31], a[144:147], v[132:135]\n\tv_mfma_f32_16x16x32_bf16 v[132:135], v[24:27], v[172:175], v[132:135]\n\tds_read_b64_tr_b16 v[20:21], v0 offset:45056\n\tds_read_b64_tr_b16 v[22:23], v0 offset:45568\n\tv_mfma_f32_16x16x32_bf16 v[140:143], v[24:27], v[176:179], v[140:143]\n\tv_mfma_f32_16x16x32_bf16 v[140:143], v[28:31], a[148:151], v[140:143]\n\ts_waitcnt vmcnt(17) lgkmcnt(8)\n\t" \
    "v_mfma_f32_16x16x32_bf16 v[120:123], v[32:35], a[152:155], v[120:123]\n\tds_read_b64_tr_b16 v[24:25], v1 offset:40960\n\tds_read_b64_tr_b16 v[26:27], v1 offset:41472\n\tv_mfma_f32_16x16x32_bf16 v[120:123], v[36:39], a[164:167], v[120:123]\n\tbuffer_load_dwordx4 v11, s[12:15], s69 offen lds\n\ts_mov_b32 s69, 8847360\n\ts_add_u32 m0, s54, 122880\n\tv_mfma_f32_16x16x32_bf16 v[128:131], v[36:39], a[168:171], v[128:131]\n\t" \
    "v_mfma_f32_16x16x32_bf16 v[128:131], v[32:35], a[156:159], v[128:131]\n\tds_read_b64_tr_b16 v[28:29], v1 offset:45056\n\tds_read_b64_tr_b16 v[30:31], v1 offset:45568\n\tv_mfma_f32_16x16x32_bf16 v[136:139], v[32:35], a[160:163], v[136:139]\n\tbuffer_load_dwordx4 v11, s[12:15], s69 offen lds\n\ts_mov_b32 s69, 9142272\n\ts_add_u32 m0, s54, 126976\n\tv_mfma_f32_16x16x32_bf16 v[136:139], v[36:39], a[172:175], v[136:139]\n\t" \
    "s_waitcnt lgkmcnt(8)\n\tv_mfma_f32_16x16x32_bf16 v[124:127], v[40:43], a[152:155], v[124:127]\n\tds_read_b64_tr_b16 v[32:33], v0 offset:49152\n\tds_read_b64_tr_b16 v[34:35], v0 offset:49664\n\tv_mfma_f32_16x16x32_bf16 v[124:127], v[44:47], a[164:167], v[124:127]\n\tv_mfma_f32_16x16x32_bf16 v[132:135], v[44:47], a[168:171], v[132:135]\n\tv_mfma_f32_16x16x32_bf16 v[132:135], v[40:43], a[156:159], v[132:135]\n\tds_read_b64_tr_b16 v[36:37], v0 offset:53248\n\t" \
    "ds_read_b64_tr_b16 v[38:39], v0 offset:53760\n\tv_mfma_f32_16x16x32_bf16 v[140:143], v[40:43], a[160:163], v[140:143]\n\tbuffer_load_dwordx4 v11, s[12:15], s69 offen lds\n\tv_mfma_f32_16x16x32_bf16 v[140:143], v[44:47], a[172:175], v[140:143]\n\ts_waitcnt vmcnt(14) lgkmcnt(8)\n\tv_mfma_f32_16x16x32_bf16 v[120:123], v[16:19], a[176:179], v[120:123]\n\tds_read_b64_tr_b16 v[40:41], v1 offset:49152\n\tds_read_b64_tr_b16 v[42:43], v1 offset:49664\n\t" \
    "v_mfma_f32_16x16x32_bf16 v[120:123], v[20:23], a[188:191], v[120:123]\n\tv_mfma_f32_16x16x32_bf16 v[128:131], v[20:23], a[192:195], v[128:131]\n\tv_mfma_f32_16x16x32_bf16 v[128:131], v[16:19], a[180:183], v[128:131]\n\tds_read_b64_tr_b16 v[44:45], v1 offset:53248\n\tds_read_b64_tr_b16 v[46:47], v1 offset:53760\n\tv_mfma_f32_16x16x32_bf16 v[136:139], v[16:19], a[184:187], v[136:139]\n\tv_mfma_f32_16x16x32_bf16 v[136:139], v[20:23], a[196:199], v[136:139]\n\ts_waitcnt lgkmcnt(8)\n\t" \
    "v_mfma_f32_16x16x32_bf16 v[124:127], v[24:27], a[176:179], v[124:127]\n\tds_read_b64_tr_b16 v[16:17], v0 offset:57344\n\tds_read_b64_tr_b16 v[18:19], v0 offset:57856\n\tv_mfma_f32_16x16x32_bf16 v[124:127], v[28:31], a[188:191], v[124:127]\n\tv_mfma_f32_16x16x32_bf16 v[132:135], v[28:31], a[192:195], v[132:135]\n\tv_mfma_f32_16x16x32_bf16 v[132:135], v[24:27], a[180:183], v[132:135]\n\tds_read_b64_tr_b16 v[20:21], v0 offset:61440\n\tds_read_b64_tr_b16 v[22:23], v0 offset:61952\n\t" \
    "v_mfma_f32_16x16x32_bf16 v[140:143], v[24:27], a[184:187], v[140:143]\n\tv_mfma_f32_16x16x32_bf16 v[140:143], v[28:31], a[196:199], v[140:143]\n\ts_add_u32 s68, s52, 0\n\tv_cvt_pk_bf16_f32 v120, v120, v121\n\tv_cvt_pk_bf16_f32 v121, v122, v123\n\tv_cvt_pk_bf16_f32 v122, v124, v125\n\tv_cvt_pk_bf16_f32 v123, v126, v127\n\tbuffer_store_dwordx4 v[120:123], v8, s[8:11], s68 offen offset:0 sc1\n\t" \
    "v_cvt_pk_bf16_f32 v124, v128, v129\n\tv_cvt_pk_bf16_f32 v125, v130, v131\n\tv_cvt_pk_bf16_f32 v126, v132, v133\n\tv_cvt_pk_bf16_f32 v127, v134, v135\n\tbuffer_store_dwordx4 v[124:127], v8, s[8:11], s68 offen offset:1024 sc1\n\tv_cvt_pk_bf16_f32 v128, v136, v137\n\tv_cvt_pk_bf16_f32 v129, v138, v139\n\tv_cvt_pk_bf16_f32 v130, v140, v141\n\t" \
    "v_cvt_pk_bf16_f32 v131, v142, v143\n\tbuffer_store_dwordx4 v[128:131], v8, s[8:11], s68 offen offset:2048 sc1\n\tv_add3_u32 v132, s36, v120, v121\n\tv_add3_u32 v133, v122, v123, v124\n\tv_add3_u32 v132, v132, v125, v126\n\tv_add3_u32 v133, v133, v127, v128\n\tv_add3_u32 v132, v132, v129, v130\n\tv_add3_u32 v132, v132, v133, v131\n\t" \
    "buffer_store_dword v132, v236, s[8:11], s68 offen offset:3072 sc1\n\ts_waitcnt lgkmcnt(8)\n\tv_mfma_f32_16x16x32_bf16 v[48:51], v[32:35], a[68:71], v[48:51]\n\tds_read_b64_tr_b16 v[24:25], v1 offset:57344\n\tds_read_b64_tr_b16 v[26:27], v1 offset:57856\n\tv_mfma_f32_16x16x32_bf16 v[48:51], v[36:39], a[80:83], v[48:51]\n\tv_mfma_f32_16x16x32_bf16 v[56:59], v[36:39], a[84:87], v[56:59]\n\tv_mfma_f32_16x16x32_bf16 v[56:59], v[32:35], a[72:75], v[56:59]\n\t" \
    "ds_read_b64_tr_b16 v[28:29], v1 offset:61440\n\tds_read_b64_tr_b16 v[30:31], v1 offset:61952\n\tv_mfma_f32_16x16x32_bf16 v[64:67], v[32:35], a[76:79], v[64:67]\n\tv_mfma_f32_16x16x32_bf16 v[64:67], v[36:39], a[88:91], v[64:67]\n\ts_waitcnt lgkmcnt(8)\n\tv_mfma_f32_16x16x32_bf16 v[52:55], v[40:43], a[68:71], v[52:55]\n\tds_read_b64_tr_b16 v[32:33], v14 offset:0\n\tds_read_b64_tr_b16 v[34:35], v14 offset:512\n\t" \
    "v_mfma_f32_16x16x32_bf16 v[52:55], v[44:47], a[80:83], v[52:55]\n\tv_mfma_f32_16x16x32_bf16 v[60:63], v[44:47], a[84:87], v[60:63]\n\tv_mfma_f32_16x16x32_bf16 v[60:63], v[40:43], a[72:75], v[60:63]\n\tds_read_b64_tr_b16 v[36:37], v14 offset:4096\n\tds_read_b64_tr_b16 v[38:39], v14 offset:4608\n\tv_mfma_f32_16x16x32_bf16 v[68:71], v[40:43], a[76:79], v[68:71]\n\tv_mfma_f32_16x16x32_bf16 v[68:71], v[44:47], a[88:91], v[68:71]\n\ts_waitcnt vmcnt(10)\n\t" \
    "s_barrier\n\ts_waitcnt lgkmcnt(8)\n\tv_mfma_f32_16x16x32_bf16 v[48:51], v[16:19], v[180:183], v[48:51]\n\tds_read_b64_tr_b16 v[40:41], v15 offset:0\n\tds_read_b64_tr_b16 v[42:43], v15 offset:512\n\ts_lshl_b32 s69, s4, 18\n\tv_mfma_f32_16x16x32_bf16 v[48:51], v[20:23], v[204:207], v[48:51]\n\ts_add_u32 s69, s69, 0\n\t" \
    "s_add_u32 m0, s54, 131072\n\tv_mfma_f32_16x16x32_bf16 v[56:59], v[20:23], v[208:211], v[56:59]\n\tv_mfma_f32_16x16x32_bf16 v[56:59], v[16:19], v[184:187], v[56:59]\n\tds_read_b64_tr_b16 v[44:45], v15 offset:4096\n\tds_read_b64_tr_b16 v[46:47], v15 offset:4608\n\tv_mfma_f32_16x16x32_bf16 v[64:67], v[16:19], v[188:191], v[64:67]\n\tbuffer_load_dwordx4 v12, s[16:19], s69 offen lds\n\ts_lshl_b32 s69, s4, 18\n\t" \
    "s_add_u32 s69, s69, 0\n\tv_mfma_f32_16x16x32_bf16 v[64:67], v[20:23], v[212:215], v[64:67]\n\ts_add_u32 m0, s54, 135168\n\ts_waitcnt lgkmcnt(8)\n\tv_mfma_f32_16x16x32_bf16 v[52:55], v[24:27], v[180:183], v[52:55]\n\tds_read_b64_tr_b16 v[16:17], v14 offset:8192\n\tds_read_b64_tr_b16 v[18:19], v14 offset:8704\n\tv_mfma_f32_16x16x32_bf16 v[52:55], v[28:31], v[204:207], v[52:55]\n\t" \
    "buffer_load_dwordx4 v13, s[16:19], s69 offen lds\n\ts_lshl_b32 s69, s4, 18\n\ts_add_u32 s69, s69, 131072\n\tv_mfma_f32_16x16x32_bf16 v[60:63], v[28:31], v[208:211], v[60:63]\n\ts_add_u32 m0, s54, 139264\n\tv_mfma_f32_16x16x32_bf16 v[60:63], v[24:27], v[184:187], v[60:63]\n\tds_read_b64_tr_b16 v[20:21], v14 offset:12288\n\tds_read_b64_tr_b16 v[22:23], v14 offset:12800\n\t" \
    "v_mfma_f32_16x16x32_bf16 v[68:71], v[24:27], v[188:191], v[68:71]\n\tbuffer_load_dwordx4 v12, s[16:19], s69 offen lds\n\ts_lshl_b32 s69, s4, 18\n\ts_add_u32 s69, s69, 131072\n\tv_mfma_f32_16x16x32_bf16 v[68:71], v[28:31], v[212:215], v[68:71]\n\ts_add_u32 m0, s54, 143360\n\ts_waitcnt lgkmcnt(8)\n\tv_mfma_f32_16x16x32_bf16 v[48:51], v[32:35], a[92:95], v[48:51]\n\t" \
    "ds_read_b64_tr_b16 v[24:25], v15 offset:8192\n\tds_read_b64_tr_b16 v[26:27], v15 offset:8704\n\tv_mfma_f32_16x16x32_bf16 v[48:51], v[36:39], v[216:219], v[48:51]\n\tbuffer_load_dwordx4 v13, s[16:19], s69 offen lds\n\tv_mfma_f32_16x16x32_bf16 v[56:59], v[36:39], a[0:3], v[56:59]\n\tv_mfma_f32_16x16x32_bf16 v[56:59], v[32:35], a[96:99], v[56:59]\n\tds_read_b64_tr_b16 v[28:29], v15 offset:12288\n\tds_read_b64_tr_b16 v[30:31], v15 offset:12800\n\t" \
    "v_mfma_f32_16x16x32_bf16 v[64:67], v[32:35], a[100:103], v[64:67]\n\tv_mfma_f32_16x16x32_bf16 v[64:67], v[36:39], a[4:7], v[64:67]\n\ts_waitcnt lgkmcnt(8)\n\tv_mfma_f32_16x16x32_bf16 v[52:55], v[40:43], a[92:95], v[52:55]\n\tds_read_b64_tr_b16 v[32:33], v14 offset:16384\n\tds_read_b64_tr_b16 v[34:35], v14 offset:16896\n\tv_mfma_f32_16x16x32_bf16 v[52:55], v[44:47], v[216:219], v[52:55]\n\tv_mfma_f32_16x16x32_bf16 v[60:63], v[44:47], a[0:3], v[60:63]\n\t" \
    "v_mfma_f32_16x16x32_bf16 v[60:63], v[40:43], a[96:99], v[60:63]\n\tds_read_b64_tr_b16 v[36:37], v14 offset:20480\n\tds_read_b64_tr_b16 v[38:39], v14 offset:20992\n\tv_mfma_f32_16x16x32_bf16 v[68:71], v[40:43], a[100:103], v[68:71]\n\tv_mfma_f32_16x16x32_bf16 v[68:71], v[44:47], a[4:7], v[68:71]\n\ts_waitcnt lgkmcnt(8)\n\tv_mfma_f32_16x16x32_bf16 v[48:51], v[16:19], a[8:11], v[48:51]\n\tds_read_b64_tr_b16 v[40:41], v15 offset:16384\n\t" \
    "ds_read_b64_tr_b16 v[42:43], v15 offset:16896\n\tv_mfma_f32_16x16x32_bf16 v[48:51], v[20:23], a[104:107], v[48:51]\n\tv_mfma_f32_16x16x32_bf16 v[56:59], v[20:23], a[108:111], v[56:59]\n\tv_mfma_f32_16x16x32_bf16 v[56:59], v[16:19], a[12:15], v[56:59]\n\tds_read_b64_tr_b16 v[44:45], v15 offset:20480\n\tds_read_b64_tr_b16 v[46:47], v15 offset:20992\n\tv_mfma_f32_16x16x32_bf16 v[64:67], v[16:19], a[16:19], v[64:67]\n\tv_mfma_f32_16x16x32_bf16 v[64:67], v[20:23], a[112:115], v[64:67]\n\t" \
    "s_waitcnt lgkmcnt(8)\n\tv_mfma_f32_16x16x32_bf16 v[52:55], v[24:27], a[8:11], v[52:55]\n\tds_read_b64_tr_b16 v[16:17], v14 offset:24576\n\tds_read_b64_tr_b16 v[18:19], v14 offset:25088\n\tv_mfma_f32_16x16x32_bf16 v[52:55], v[28:31], a[104:107], v[52:55]\n\tv_mfma_f32_16x16x32_bf16 v[60:63], v[28:31], a[108:111], v[60:63]\n\tv_mfma_f32_16x16x32_bf16 v[60:63], v[24:27], a[12:15], v[60:63]\n\tds_read_b64_tr_b16 v[20:21], v14 offset:28672\n\t" \
    "ds_read_b64_tr_b16 v[22:23], v14 offset:29184\n\tv_mfma_f32_16x16x32_bf16 v[68:71], v[24:27], a[16:19], v[68:71]\n\tv_mfma_f32_16x16x32_bf16 v[68:71], v[28:31], a[112:115], v[68:71]\n\ts_waitcnt lgkmcnt(8)\n\tv_mfma_f32_16x16x32_bf16 v[48:51], v[32:35], a[20:23], v[48:51]\n\tds_read_b64_tr_b16 v[24:25], v15 offset:24576\n\tds_read_b64_tr_b16 v[26:27], v15 offset:25088\n\tv_mfma_f32_16x16x32_bf16 v[48:51], v[36:39], a[32:35], v[48:51]\n\t" \
    "v_mfma_f32_16x16x32_bf16 v[56:59], v[36:39], a[36:39], v[56:59]\n\tv_mfma_f32_16x16x32_bf16 v[56:59], v[32:35], a[24:27], v[56:59]\n\tds_read_b64_tr_b16 v[28:29], v15 offset:28672\n\tds_read_b64_tr_b16 v[30:31], v15 offset:29184\n\tv_mfma_f32_16x16x32_bf16 v[64:67], v[32:35], a[28:31], v[64:67]\n\tv_mfma_f32_16x16x32_bf16 v[64:67], v[36:39], a[40:43], v[64:67]\n\ts_waitcnt lgkmcnt(8)\n\tv_mfma_f32_16x16x32_bf16 v[52:55], v[40:43], a[20:23], v[52:55]\n\t" \
    "ds_read_b64_tr_b16 v[32:33], v14 offset:32768\n\tds_read_b64_tr_b16 v[34:35], v14 offset:33280\n\ts_add_u32 s68, s72, 0\n\tv_mfma_f32_16x16x32_bf16 v[52:55], v[44:47], a[32:35], v[52:55]\n\tbuffer_load_dwordx4 v[120:123], v8, s[8:11], s68 offen offset:0 sc1\n\tv_mfma_f32_16x16x32_bf16 v[60:63], v[44:47], a[36:39], v[60:63]\n\tv_mfma_f32_16x16x32_bf16 v[60:63], v[40:43], a[24:27], v[60:63]\n\tds_read_b64_tr_b16 v[36:37], v14 offset:36864\n\t" \
    "ds_read_b64_tr_b16 v[38:39], v14 offset:37376\n\tv_mfma_f32_16x16x32_bf16 v[68:71], v[40:43], a[28:31], v[68:71]\n\tbuffer_load_dwordx4 v[124:127], v8, s[8:11], s68 offen offset:1024 sc1\n\tv_mfma_f32_16x16x32_bf16 v[68:71], v[44:47], a[40:43], v[68:71]\n\ts_waitcnt vmcnt(10)\n\ts_barrier\n\ts_waitcnt lgkmcnt(8)\n\tv_mfma_f32_16x16x32_bf16 v[48:51], v[16:19], a[44:47], v[48:51]\n\t" \
    "ds_read_b64_tr_b16 v[40:41], v15 offset:32768\n\tds_read_b64_tr_b16 v[42:43], v15 offset:33280\n\tv_mfma_f32_16x16x32_bf16 v[48:51], v[20:23], a[56:59], v[48:51]\n\tbuffer_load_dwordx4 v[128:131], v8, s[8:11], s68 offen offset:2048 sc1\n\tv_mfma_f32_16x16x32_bf16 v[56:59], v[20:23], a[60:63], v[56:59]\n\tv_mfma_f32_16x16x32_bf16 v[56:59], v[16:19], a[48:51], v[56:59]\n\tds_read_b64_tr_b16 v[44:45], v15 offset:36864\n\tds_read_b64_tr_b16 v[46:47], v15 offset:37376\n\t" \
    "v_mfma_f32_16x16x32_bf16 v[64:67], v[16:19], a[52:55], v[64:67]\n\tbuffer_load_dword v132, v236, s[8:11], s68 offen offset:3072 sc1\n\ts_lshl_b32 s65, s41, 2\n\ts_add_u32 s65, s65, s38\n\tv_mfma_f32_16x16x32_bf16 v[64:67], v[20:23], a[64:67], v[64:67]\n\ts_lshr_b32 s65, s65, 1\n\ts_add_u32 s65, s65, 5\n\ts_waitcnt lgkmcnt(8)\n\t" \
    "v_mfma_f32_16x16x32_bf16 v[52:55], v[24:27], a[44:47], v[52:55]\n\tds_read_b64_tr_b16 v[16:17], v14 offset:40960\n\tds_read_b64_tr_b16 v[18:19], v14 offset:41472\n\ts_cmp_ge_u32 s65, 18\n\ts_cselect_b32 s66, 1120, 0\n\tv_mfma_f32_16x16x32_bf16 v[52:55], v[28:31], a[56:59], v[52:55]\n\ts_lshl_b32 s65, s65, 6\n\ts_sub_u32 s65, s65, s66\n\t" \
    "s_lshl_b32 s66, s58, 4\n\tv_mfma_f32_16x16x32_bf16 v[60:63], v[28:31], a[60:63], v[60:63]\n\ts_add_u32 s65, s65, s66\n\ts_lshl_b32 s65, s65, 12\n\tv_bfe_u32 v252, v240, 1, 4\n\tv_mfma_f32_16x16x32_bf16 v[60:63], v[24:27], a[48:51], v[60:63]\n\tds_read_b64_tr_b16 v[20:21], v14 offset:45056\n\tds_read_b64_tr_b16 v[22:23], v14 offset:45568\n\t" \
    "v_and_b32 v251, 1, v240\n\tv_mfma_f32_16x16x32_bf16 v[68:71], v[24:27], a[52:55], v[68:71]\n\tv_lshlrev_b32 v252, 12, v252\n\tv_lshl_add_u32 v252, v251, 7, v252\n\tv_mfma_f32_16x16x32_bf16 v[68:71], v[28:31], a[64:67], v[68:71]\n\ts_waitcnt lgkmcnt(8)\n\tv_mfma_f32_16x16x32_bf16 v[48:51], v[32:35], a[116:119], v[48:51]\n\tds_read_b64_tr_b16 v[24:25], v15 offset:40960\n\t" \
    "ds_read_b64_tr_b16 v[26:27], v15 offset:41472\n\tv_mfma_f32_16x16x32_bf16 v[48:51], v[36:39], a[128:131], v[48:51]\n\tbuffer_load_dword v253, v252, s[16:19], s65 offen\n\tv_mfma_f32_16x16x32_bf16 v[56:59], v[36:39], a[132:135], v[56:59]\n\tv_mfma_f32_16x16x32_bf16 v[56:59], v[32:35], a[120:123], v[56:59]\n\tds_read_b64_tr_b16 v[28:29], v15 offset:45056\n\tds_read_b64_tr_b16 v[30:31], v15 offset:45568\n\tv_mfma_f32_16x16x32_bf16 v[64:67], v[32:35], a[124:127], v[64:67]\n\t" \
    "v_mfma_f32_16x16x32_bf16 v[64:67], v[36:39], a[136:139], v[64:67]\n\ts_waitcnt lgkmcnt(8)\n\tv_mfma_f32_16x16x32_bf16 v[52:55], v[40:43], a[116:119], v[52:55]\n\tds_read_b64_tr_b16 v[32:33], v14 offset:49152\n\tds_read_b64_tr_b16 v[34:35], v14 offset:49664\n\tv_mfma_f32_16x16x32_bf16 v[52:55], v[44:47], a[128:131], v[52:55]\n\tv_mfma_f32_16x16x32_bf16 v[60:63], v[44:47], a[132:135], v[60:63]\n\tv_mfma_f32_16x16x32_bf16 v[60:63], v[40:43], a[120:123], v[60:63]\n\t" \
    "ds_read_b64_tr_b16 v[36:37], v14 offset:53248\n\tds_read_b64_tr_b16 v[38:39], v14 offset:53760\n\tv_mfma_f32_16x16x32_bf16 v[68:71], v[40:43], a[124:127], v[68:71]\n\tv_mfma_f32_16x16x32_bf16 v[68:71], v[44:47], a[136:139], v[68:71]\n\ts_waitcnt lgkmcnt(8)\n\tv_mfma_f32_16x16x32_bf16 v[48:51], v[16:19], v[168:171], v[48:51]\n\tds_read_b64_tr_b16 v[40:41], v15 offset:49152\n\tds_read_b64_tr_b16 v[42:43], v15 offset:49664\n\t" \
    "v_mfma_f32_16x16x32_bf16 v[48:51], v[20:23], a[140:143], v[48:51]\n\tv_mfma_f32_16x16x32_bf16 v[56:59], v[20:23], a[144:147], v[56:59]\n\tv_mfma_f32_16x16x32_bf16 v[56:59], v[16:19], v[172:175], v[56:59]\n\tds_read_b64_tr_b16 v[44:45], v15 offset:53248\n\tds_read_b64_tr_b16 v[46:47], v15 offset:53760\n\tv_mfma_f32_16x16x32_bf16 v[64:67], v[16:19], v[176:179], v[64:67]\n\tv_mfma_f32_16x16x32_bf16 v[64:67], v[20:23], a[148:151], v[64:67]\n\ts_waitcnt lgkmcnt(8)\n\t" \
    "v_mfma_f32_16x16x32_bf16 v[52:55], v[24:27], v[168:171], v[52:55]\n\tds_read_b64_tr_b16 v[16:17], v14 offset:57344\n\tds_read_b64_tr_b16 v[18:19], v14 offset:57856\n\tv_mfma_f32_16x16x32_bf16 v[52:55], v[28:31], a[140:143], v[52:55]\n\tv_mfma_f32_16x16x32_bf16 v[60:63], v[28:31], a[144:147], v[60:63]\n\tv_mfma_f32_16x16x32_bf16 v[60:63], v[24:27], v[172:175], v[60:63]\n\tds_read_b64_tr_b16 v[20:21], v14 offset:61440\n\tds_read_b64_tr_b16 v[22:23], v14 offset:61952\n\t" \
    "v_mfma_f32_16x16x32_bf16 v[68:71], v[24:27], v[176:179], v[68:71]\n\tv_mfma_f32_16x16x32_bf16 v[68:71], v[28:31], a[148:151], v[68:71]\n\ts_waitcnt lgkmcnt(8)\n\tv_mfma_f32_16x16x32_bf16 v[48:51], v[32:35], a[152:155], v[48:51]\n\tds_read_b64_tr_b16 v[24:25], v15 offset:57344\n\tds_read_b64_tr_b16 v[26:27], v15 offset:57856\n\tv_mfma_f32_16x16x32_bf16 v[48:51], v[36:39], a[164:167], v[48:51]\n\tv_mfma_f32_16x16x32_bf16 v[56:59], v[36:39], a[168:171], v[56:59]\n\t" \
    "v_mfma_f32_16x16x32_bf16 v[56:59], v[32:35], a[156:159], v[56:59]\n\tds_read_b64_tr_b16 v[28:29], v15 offset:61440\n\tds_read_b64_tr_b16 v[30:31], v15 offset:61952\n\tv_mfma_f32_16x16x32_bf16 v[64:67], v[32:35], a[160:163], v[64:67]\n\tv_mfma_f32_16x16x32_bf16 v[64:67], v[36:39], a[172:175], v[64:67]\n\ts_waitcnt lgkmcnt(8)\n\tv_mfma_f32_16x16x32_bf16 v[52:55], v[40:43], a[152:155], v[52:55]\n\tds_read_b64_tr_b16 v[32:33], v2 offset:8192\n\t" \
    "ds_read_b64_tr_b16 v[34:35], v2 offset:8704\n\tv_mfma_f32_16x16x32_bf16 v[52:55], v[44:47], a[164:167], v[52:55]\n\tv_mfma_f32_16x16x32_bf16 v[60:63], v[44:47], a[168:171], v[60:63]\n\tv_mfma_f32_16x16x32_bf16 v[60:63], v[40:43], a[156:159], v[60:63]\n\tds_read_b64_tr_b16 v[36:37], v2 offset:12288\n\tds_read_b64_tr_b16 v[38:39], v2 offset:12800\n\tv_mfma_f32_16x16x32_bf16 v[68:71], v[40:43], a[160:163], v[68:71]\n\tv_mfma_f32_16x16x32_bf16 v[68:71], v[44:47], a[172:175], v[68:71]\n\t" \
    "s_barrier\n\ts_waitcnt lgkmcnt(8)\n\tv_mfma_f32_16x16x32_bf16 v[48:51], v[16:19], a[176:179], v[48:51]\n\tds_read_b64_tr_b16 v[40:41], v3 offset:8192\n\tds_read_b64_tr_b16 v[42:43], v3 offset:8704\n\tv_mfma_f32_16x16x32_bf16 v[48:51], v[20:23], a[188:191], v[48:51]\n\tv_mfma_f32_16x16x32_bf16 v[56:59], v[20:23], a[192:195], v[56:59]\n\tv_mfma_f32_16x16x32_bf16 v[56:59], v[16:19], a[180:183], v[56:59]\n\t" \
    "ds_read_b64_tr_b16 v[44:45], v3 offset:12288\n\tds_read_b64_tr_b16 v[46:47], v3 offset:12800\n\tv_mfma_f32_16x16x32_bf16 v[64:67], v[16:19], a[184:187], v[64:67]\n\tv_mfma_f32_16x16x32_bf16 v[64:67], v[20:23], a[196:199], v[64:67]\n\ts_waitcnt lgkmcnt(8)\n\tv_mfma_f32_16x16x32_bf16 v[52:55], v[24:27], a[176:179], v[52:55]\n\tds_read_b64_tr_b16 v[16:17], v2 offset:16384\n\tds_read_b64_tr_b16 v[18:19], v2 offset:16896\n\t" \
    "v_mfma_f32_16x16x32_bf16 v[52:55], v[28:31], a[188:191], v[52:55]\n\tv_mfma_f32_16x16x32_bf16 v[60:63], v[28:31], a[192:195], v[60:63]\n\tv_mfma_f32_16x16x32_bf16 v[60:63], v[24:27], a[180:183], v[60:63]\n\tds_read_b64_tr_b16 v[20:21], v2 offset:20480\n\tds_read_b64_tr_b16 v[22:23], v2 offset:20992\n\tv_mfma_f32_16x16x32_bf16 v[68:71], v[24:27], a[184:187], v[68:71]\n\tv_mfma_f32_16x16x32_bf16 v[68:71], v[28:31], a[196:199], v[68:71]\n\ts_mov_b32 s71, 1048576\n\t" \
    "s_waitcnt vmcnt(1)\n\t107:\n\tv_add3_u32 v134, s36, v120, v121\n\tv_add3_u32 v135, v122, v123, v124\n\tv_add3_u32 v134, v134, v125, v126\n\tv_add3_u32 v135, v135, v127, v128\n\tv_add3_u32 v134, v134, v129, v130\n\tv_add3_u32 v134, v134, v135, v131\n\t" \
    "v_cmp_ne_u32 vcc, v134, v132\n\ts_cbranch_vccz 108f\n\ts_sub_u32 s71, s71, 1\n\ts_cbranch_scc1 108f\n\ts_sleep 6\n\ts_add_u32 s73, s72, 0\n\tbuffer_load_dwordx4 v[120:123], v8, s[8:11], s73 offen offset:0 sc1\n\tbuffer_load_dwordx4 v[124:127], v8, s[8:11], s73 offen offset:1024 sc1\n\t" \
    "buffer_load_dwordx4 v[128:131], v8, s[8:11], s73 offen offset:2048 sc1\n\tbuffer_load_dword v132, v236, s[8:11], s73 offen offset:3072 sc1\n\ts_waitcnt vmcnt(0)\n\ts_branch 107b\n\t108:\n\tv_lshlrev_b32 v96, 16, v120\n\tv_and_b32 v97, 0xffff0000, v120\n\tv_lshlrev_b32 v98, 16, v121\n\t" \
    "v_and_b32 v99, 0xffff0000, v121\n\tv_lshlrev_b32 v100, 16, v122\n\tv_and_b32 v101, 0xffff0000, v122\n\tv_lshlrev_b32 v102, 16, v123\n\tv_and_b32 v103, 0xffff0000, v123\n\tv_pk_add_f32 v[48:49], v[48:49], v[96:97]\n\tv_pk_add_f32 v[50:51], v[50:51], v[98:99]\n\tv_pk_add_f32 v[52:53], v[52:53], v[100:101]\n\t" \
    "v_pk_add_f32 v[54:55], v[54:55], v[102:103]\n\tv_pk_mul_f32 v[96:97], v[48:49], v[48:49]\n\tv_pk_mul_f32 v[98:99], v[50:51], v[50:51]\n\tv_pk_mul_f32 v[100:101], v[52:53], v[52:53]\n\tv_pk_mul_f32 v[102:103], v[54:55], v[54:55]\n\tv_pk_fma_f32 v[96:97], v[96:97], v[246:247], v[246:247] op_sel:[0,0,1] op_sel_hi:[1,0,1]\n\tv_pk_fma_f32 v[98:99], v[98:99], v[246:247], v[246:247] op_sel:[0,0,1] op_sel_hi:[1,0,1]\n\tv_pk_fma_f32 v[100:101], v[100:101], v[246:247], v[246:247] op_sel:[0,0,1] op_sel_hi:[1,0,1]\n\t" \
    "v_pk_fma_f32 v[102:103], v[102:103], v[246:247], v[246:247] op_sel:[0,0,1] op_sel_hi:[1,0,1]\n\tv_pk_mul_f32 v[96:97], v[96:97], v[48:49]\n\tv_pk_mul_f32 v[98:99], v[98:99], v[50:51]\n\tv_pk_mul_f32 v[100:101], v[100:101], v[52:53]\n\tv_pk_mul_f32 v[102:103], v[102:103], v[54:55]\n\tv_lshlrev_b32 v104, 16, v124\n\tv_exp_f32 v96, v96\n\tv_and_b32 v105, 0xffff0000, v124\n\t" \
    "v_exp_f32 v97, v97\n\tv_lshlrev_b32 v106, 16, v125\n\tv_exp_f32 v98, v98\n\tv_and_b32 v107, 0xffff0000, v125\n\tv_exp_f32 v99, v99\n\tv_lshlrev_b32 v108, 16, v126\n\tv_exp_f32 v100, v100\n\tv_and_b32 v109, 0xffff0000, v126\n\t" \
    "v_exp_f32 v101, v101\n\tv_lshlrev_b32 v110, 16, v127\n\tv_exp_f32 v102, v102\n\tv_and_b32 v111, 0xffff0000, v127\n\tv_exp_f32 v103, v103\n\tv_pk_add_f32 v[56:57], v[56:57], v[104:105]\n\tv_pk_add_f32 v[96:97], v[96:97], 1.0 op_sel_hi:[1,0]\n\tv_pk_add_f32 v[98:99], v[98:99], 1.0 op_sel_hi:[1,0]\n\t" \
    "v_pk_add_f32 v[100:101], v[100:101], 1.0 op_sel_hi:[1,0]\n\tv_pk_add_f32 v[102:103], v[102:103], 1.0 op_sel_hi:[1,0]\n\tv_pk_add_f32 v[58:59], v[58:59], v[106:107]\n\tv_rcp_f32 v96, v96\n\tv_pk_add_f32 v[60:61], v[60:61], v[108:109]\n\tv_rcp_f32 v97, v97\n\tv_pk_add_f32 v[62:63], v[62:63], v[110:111]\n\tv_rcp_f32 v98, v98\n\t" \
    "v_pk_mul_f32 v[104:105], v[56:57], v[56:57]\n\tv_rcp_f32 v99, v99\n\tv_pk_mul_f32 v[106:107], v[58:59], v[58:59]\n\tv_rcp_f32 v100, v100\n\tv_pk_mul_f32 v[108:109], v[60:61], v[60:61]\n\tv_rcp_f32 v101, v101\n\tv_pk_mul_f32 v[110:111], v[62:63], v[62:63]\n\tv_rcp_f32 v102, v102\n\t" \
    "v_pk_fma_f32 v[104:105], v[104:105], v[246:247], v[246:247] op_sel:[0,0,1] op_sel_hi:[1,0,1]\n\tv_rcp_f32 v103, v103\n\tv_pk_fma_f32 v[106:107], v[106:107], v[246:247], v[246:247] op_sel:[0,0,1] op_sel_hi:[1,0,1]\n\tv_pk_mul_f32 v[96:97], v[96:97], v[48:49]\n\tv_pk_mul_f32 v[98:99], v[98:99], v[50:51]\n\tv_pk_mul_f32 v[100:101], v[100:101], v[52:53]\n\tv_pk_mul_f32 v[102:103], v[102:103], v[54:55]\n\tv_cvt_pk_bf16_f32 v192, v96, v97\n\t" \
    "v_cvt_pk_bf16_f32 v193, v98, v99\n\tv_cvt_pk_bf16_f32 v194, v100, v101\n\tv_cvt_pk_bf16_f32 v195, v102, v103\n\tbuffer_store_dwordx4 v[192:195], v8, s[8:11], s45 offen offset:0 sc1\n\tv_pk_fma_f32 v[108:109], v[108:109], v[246:247], v[246:247] op_sel:[0,0,1] op_sel_hi:[1,0,1]\n\tv_pk_fma_f32 v[110:111], v[110:111], v[246:247], v[246:247] op_sel:[0,0,1] op_sel_hi:[1,0,1]\n\tv_pk_mul_f32 v[104:105], v[104:105], v[56:57]\n\tv_pk_mul_f32 v[106:107], v[106:107], v[58:59]\n\t" \
    "v_pk_mul_f32 v[108:109], v[108:109], v[60:61]\n\tv_pk_mul_f32 v[110:111], v[110:111], v[62:63]\n\tv_lshlrev_b32 v112, 16, v128\n\tv_exp_f32 v104, v104\n\tv_and_b32 v113, 0xffff0000, v128\n\tv_exp_f32 v105, v105\n\tv_lshlrev_b32 v114, 16, v129\n\tv_exp_f32 v106, v106\n\t" \
    "v_and_b32 v115, 0xffff0000, v129\n\tv_exp_f32 v107, v107\n\tv_lshlrev_b32 v116, 16, v130\n\tv_exp_f32 v108, v108\n\tv_and_b32 v117, 0xffff0000, v130\n\tv_exp_f32 v109, v109\n\tv_lshlrev_b32 v118, 16, v131\n\tv_exp_f32 v110, v110\n\t" \
    "v_and_b32 v119, 0xffff0000, v131\n\tv_exp_f32 v111, v111\n\tv_pk_add_f32 v[64:65], v[64:65], v[112:113]\n\tv_pk_add_f32 v[104:105], v[104:105], 1.0 op_sel_hi:[1,0]\n\tv_pk_add_f32 v[106:107], v[106:107], 1.0 op_sel_hi:[1,0]\n\tv_pk_add_f32 v[108:109], v[108:109], 1.0 op_sel_hi:[1,0]\n\tv_pk_add_f32 v[110:111], v[110:111], 1.0 op_sel_hi:[1,0]\n\tv_pk_add_f32 v[66:67], v[66:67], v[114:115]\n\t" \
    "v_rcp_f32 v104, v104\n\tv_pk_add_f32 v[68:69], v[68:69], v[116:117]\n\tv_rcp_f32 v105, v105\n\tv_pk_add_f32 v[70:71], v[70:71], v[118:119]\n\tv_rcp_f32 v106, v106\n\tv_pk_mul_f32 v[112:113], v[64:65], v[64:65]\n\tv_rcp_f32 v107, v107\n\tv_pk_mul_f32 v[114:115], v[66:67], v[66:67]\n\t" \
    "v_rcp_f32 v108, v108\n\tv_pk_mul_f32 v[116:117], v[68:69], v[68:69]\n\tv_rcp_f32 v109, v109\n\tv_pk_mul_f32 v[118:119], v[70:71], v[70:71]\n\tv_rcp_f32 v110, v110\n\tv_pk_fma_f32 v[112:113], v[112:113], v[246:247], v[246:247] op_sel:[0,0,1] op_sel_hi:[1,0,1]\n\tv_rcp_f32 v111, v111\n\tv_pk_fma_f32 v[114:115], v[114:115], v[246:247], v[246:247] op_sel:[0,0,1] op_sel_hi:[1,0,1]\n\t" \
    "v_pk_mul_f32 v[104:105], v[104:105], v[56:57]\n\tv_pk_mul_f32 v[106:107], v[106:107], v[58:59]\n\tv_pk_mul_f32 v[108:109], v[108:109], v[60:61]\n\tv_pk_mul_f32 v[110:111], v[110:111], v[62:63]\n\tv_cvt_pk_bf16_f32 v196, v104, v105\n\tv_cvt_pk_bf16_f32 v197, v106, v107\n\tv_cvt_pk_bf16_f32 v198, v108, v109\n\tv_cvt_pk_bf16_f32 v199, v110, v111\n\t" \
    "buffer_store_dwordx4 v[196:199], v8, s[8:11], s45 offen offset:1024 sc1\n\tv_pk_fma_f32 v[116:117], v[116:117], v[246:247], v[246:247] op_sel:[0,0,1] op_sel_hi:[1,0,1]\n\tv_pk_fma_f32 v[118:119], v[118:119], v[246:247], v[246:247] op_sel:[0,0,1] op_sel_hi:[1,0,1]\n\tv_pk_mul_f32 v[112:113], v[112:113], v[64:65]\n\tv_pk_mul_f32 v[114:115], v[114:115], v[66:67]\n\tv_pk_mul_f32 v[116:117], v[116:117], v[68:69]\n\tv_pk_mul_f32 v[118:119], v[118:119], v[70:71]\n\tv_exp_f32 v112, v112\n\t" \
    "v_exp_f32 v113, v113\n\tv_exp_f32 v114, v114\n\tv_exp_f32 v115, v115\n\tv_exp_f32 v116, v116\n\tv_exp_f32 v117, v117\n\tv_exp_f32 v118, v118\n\tv_exp_f32 v119, v119\n\tv_pk_add_f32 v[112:113], v[112:113], 1.0 op_sel_hi:[1,0]\n\t" \
    "v_pk_add_f32 v[114:115], v[114:115], 1.0 op_sel_hi:[1,0]\n\tv_pk_add_f32 v[116:117], v[116:117], 1.0 op_sel_hi:[1,0]\n\tv_pk_add_f32 v[118:119], v[118:119], 1.0 op_sel_hi:[1,0]\n\tv_rcp_f32 v112, v112\n\tv_rcp_f32 v113, v113\n\tv_rcp_f32 v114, v114\n\tv_rcp_f32 v115, v115\n\tv_rcp_f32 v116, v116\n\t" \
    "v_rcp_f32 v117, v117\n\tv_rcp_f32 v118, v118\n\tv_rcp_f32 v119, v119\n\tv_pk_mul_f32 v[112:113], v[112:113], v[64:65]\n\tv_pk_mul_f32 v[114:115], v[114:115], v[66:67]\n\tv_pk_mul_f32 v[116:117], v[116:117], v[68:69]\n\tv_pk_mul_f32 v[118:119], v[118:119], v[70:71]\n\tv_cvt_pk_bf16_f32 v200, v112, v113\n\t" \
    "v_cvt_pk_bf16_f32 v201, v114, v115\n\tv_cvt_pk_bf16_f32 v202, v116, v117\n\tv_cvt_pk_bf16_f32 v203, v118, v119\n\tbuffer_store_dwordx4 v[200:203], v8, s[8:11], s45 offen offset:2048 sc1\n\ts_waitcnt lgkmcnt(8)\n\tv_mfma_f32_16x16x32_bf16 v[144:147], v[32:35], a[44:47], v[144:147]\n\tds_read_b64_tr_b16 v[24:25], v3 offset:16384\n\tds_read_b64_tr_b16 v[26:27], v3 offset:16896\n\t" \
    "v_mfma_f32_16x16x32_bf16 v[144:147], v[36:39], a[56:59], v[144:147]\n\tbuffer_store_dwordx2 v[248:249], off, s[8:11], s60 sc1\n\tv_mfma_f32_16x16x32_bf16 v[152:155], v[36:39], a[60:63], v[152:155]\n\tv_mfma_f32_16x16x32_bf16 v[152:155], v[32:35], a[48:51], v[152:155]\n\tds_read_b64_tr_b16 v[28:29], v3 offset:20480\n\tds_read_b64_tr_b16 v[30:31], v3 offset:20992\n\tv_mfma_f32_16x16x32_bf16 v[160:163], v[32:35], a[52:55], v[160:163]\n\tv_mfma_f32_16x16x32_bf16 v[160:163], v[36:39], a[64:67], v[160:163]\n\t" \
    "s_waitcnt lgkmcnt(8)\n\tv_mfma_f32_16x16x32_bf16 v[148:151], v[40:43], a[44:47], v[148:151]\n\tds_read_b64_tr_b16 v[32:33], v2 offset:24576\n\tds_read_b64_tr_b16 v[34:35], v2 offset:25088\n\tv_mfma_f32_16x16x32_bf16 v[148:151], v[44:47], a[56:59], v[148:151]\n\tv_mfma_f32_16x16x32_bf16 v[156:159], v[44:47], a[60:63], v[156:159]\n\tv_mfma_f32_16x16x32_bf16 v[156:159], v[40:43], a[48:51], v[156:159]\n\tds_read_b64_tr_b16 v[36:37], v2 offset:28672\n\t" \
    "ds_read_b64_tr_b16 v[38:39], v2 offset:29184\n\tv_mfma_f32_16x16x32_bf16 v[164:167], v[40:43], a[52:55], v[164:167]\n\tv_mfma_f32_16x16x32_bf16 v[164:167], v[44:47], a[64:67], v[164:167]\n\ts_waitcnt lgkmcnt(8)\n\tv_mfma_f32_16x16x32_bf16 v[144:147], v[16:19], a[116:119], v[144:147]\n\tds_read_b64_tr_b16 v[40:41], v3 offset:24576\n\tds_read_b64_tr_b16 v[42:43], v3 offset:25088\n\tv_mfma_f32_16x16x32_bf16 v[144:147], v[20:23], a[128:131], v[144:147]\n\t" \
    "v_mfma_f32_16x16x32_bf16 v[152:155], v[20:23], a[132:135], v[152:155]\n\tv_mfma_f32_16x16x32_bf16 v[152:155], v[16:19], a[120:123], v[152:155]\n\tds_read_b64_tr_b16 v[44:45], v3 offset:28672\n\tds_read_b64_tr_b16 v[46:47], v3 offset:29184\n\tv_mfma_f32_16x16x32_bf16 v[160:163], v[16:19], a[124:127], v[160:163]\n\tv_mfma_f32_16x16x32_bf16 v[160:163], v[20:23], a[136:139], v[160:163]\n\ts_waitcnt lgkmcnt(8)\n\tv_mfma_f32_16x16x32_bf16 v[148:151], v[24:27], a[116:119], v[148:151]\n\t" \
    "ds_read_b64_tr_b16 v[16:17], v2 offset:32768\n\tds_read_b64_tr_b16 v[18:19], v2 offset:33280\n\tv_mfma_f32_16x16x32_bf16 v[148:151], v[28:31], a[128:131], v[148:151]\n\tv_mfma_f32_16x16x32_bf16 v[156:159], v[28:31], a[132:135], v[156:159]\n\tv_mfma_f32_16x16x32_bf16 v[156:159], v[24:27], a[120:123], v[156:159]\n\tds_read_b64_tr_b16 v[20:21], v2 offset:36864\n\tds_read_b64_tr_b16 v[22:23], v2 offset:37376\n\tv_mfma_f32_16x16x32_bf16 v[164:167], v[24:27], a[124:127], v[164:167]\n\t" \
    "v_mfma_f32_16x16x32_bf16 v[164:167], v[28:31], a[136:139], v[164:167]\n\ts_waitcnt lgkmcnt(8)\n\tv_mfma_f32_16x16x32_bf16 v[144:147], v[32:35], v[168:171], v[144:147]\n\tds_read_b64_tr_b16 v[24:25], v3 offset:32768\n\tds_read_b64_tr_b16 v[26:27], v3 offset:33280\n\tv_mfma_f32_16x16x32_bf16 v[144:147], v[36:39], a[140:143], v[144:147]\n\tv_mfma_f32_16x16x32_bf16 v[152:155], v[36:39], a[144:147], v[152:155]\n\tv_mfma_f32_16x16x32_bf16 v[152:155], v[32:35], v[172:175], v[152:155]\n\t" \
    "ds_read_b64_tr_b16 v[28:29], v3 offset:36864\n\tds_read_b64_tr_b16 v[30:31], v3 offset:37376\n\tv_mfma_f32_16x16x32_bf16 v[160:163], v[32:35], v[176:179], v[160:163]\n\tv_mfma_f32_16x16x32_bf16 v[160:163], v[36:39], a[148:151], v[160:163]\n\ts_waitcnt lgkmcnt(8)\n\tv_mfma_f32_16x16x32_bf16 v[148:151], v[40:43], v[168:171], v[148:151]\n\tds_read_b64_tr_b16 v[32:33], v2 offset:40960\n\tds_read_b64_tr_b16 v[34:35], v2 offset:41472\n\t" \
    "v_mfma_f32_16x16x32_bf16 v[148:151], v[44:47], a[140:143], v[148:151]\n\tv_mfma_f32_16x16x32_bf16 v[156:159], v[44:47], a[144:147], v[156:159]\n\tv_mfma_f32_16x16x32_bf16 v[156:159], v[40:43], v[172:175], v[156:159]\n\tds_read_b64_tr_b16 v[36:37], v2 offset:45056\n\tds_read_b64_tr_b16 v[38:39], v2 offset:45568\n\tv_mfma_f32_16x16x32_bf16 v[164:167], v[40:43], v[176:179], v[164:167]\n\tv_mfma_f32_16x16x32_bf16 v[164:167], v[44:47], a[148:151], v[164:167]\n\ts_barrier\n\t" \
    "s_waitcnt lgkmcnt(8)\n\tv_mfma_f32_16x16x32_bf16 v[144:147], v[16:19], a[152:155], v[144:147]\n\tds_read_b64_tr_b16 v[40:41], v3 offset:40960\n\tds_read_b64_tr_b16 v[42:43], v3 offset:41472\n\tv_mfma_f32_16x16x32_bf16 v[144:147], v[20:23], a[164:167], v[144:147]\n\tv_mfma_f32_16x16x32_bf16 v[152:155], v[20:23], a[168:171], v[152:155]\n\tv_mfma_f32_16x16x32_bf16 v[152:155], v[16:19], a[156:159], v[152:155]\n\tds_read_b64_tr_b16 v[44:45], v3 offset:45056\n\t" \
    "ds_read_b64_tr_b16 v[46:47], v3 offset:45568\n\tv_mfma_f32_16x16x32_bf16 v[160:163], v[16:19], a[160:163], v[160:163]\n\tv_mfma_f32_16x16x32_bf16 v[160:163], v[20:23], a[172:175], v[160:163]\n\ts_waitcnt lgkmcnt(8)\n\tv_mfma_f32_16x16x32_bf16 v[148:151], v[24:27], a[152:155], v[148:151]\n\tds_read_b64_tr_b16 v[16:17], v2 offset:49152\n\tds_read_b64_tr_b16 v[18:19], v2 offset:49664\n\tv_mfma_f32_16x16x32_bf16 v[148:151], v[28:31], a[164:167], v[148:151]\n\t" \
    "v_mfma_f32_16x16x32_bf16 v[156:159], v[28:31], a[168:171], v[156:159]\n\tv_mfma_f32_16x16x32_bf16 v[156:159], v[24:27], a[156:159], v[156:159]\n\tds_read_b64_tr_b16 v[20:21], v2 offset:53248\n\tds_read_b64_tr_b16 v[22:23], v2 offset:53760\n\tv_mfma_f32_16x16x32_bf16 v[164:167], v[24:27], a[160:163], v[164:167]\n\tv_mfma_f32_16x16x32_bf16 v[164:167], v[28:31], a[172:175], v[164:167]\n\ts_waitcnt lgkmcnt(8)\n\tv_mfma_f32_16x16x32_bf16 v[144:147], v[32:35], a[176:179], v[144:147]\n\t" \
    "ds_read_b64_tr_b16 v[24:25], v3 offset:49152\n\tds_read_b64_tr_b16 v[26:27], v3 offset:49664\n\tv_mfma_f32_16x16x32_bf16 v[144:147], v[36:39], a[188:191], v[144:147]\n\tv_mfma_f32_16x16x32_bf16 v[152:155], v[36:39], a[192:195], v[152:155]\n\tv_mfma_f32_16x16x32_bf16 v[152:155], v[32:35], a[180:183], v[152:155]\n\tds_read_b64_tr_b16 v[28:29], v3 offset:53248\n\tds_read_b64_tr_b16 v[30:31], v3 offset:53760\n\tv_mfma_f32_16x16x32_bf16 v[160:163], v[32:35], a[184:187], v[160:163]\n\t" \
    "v_mfma_f32_16x16x32_bf16 v[160:163], v[36:39], a[196:199], v[160:163]\n\ts_waitcnt vmcnt(1)\n\tbuffer_store_dwordx2 v[248:249], off, s[8:11], s49 sc1\n\ts_waitcnt lgkmcnt(8)\n\tv_mfma_f32_16x16x32_bf16 v[148:151], v[40:43], a[176:179], v[148:151]\n\tds_read_b64_tr_b16 v[32:33], v2 offset:57344\n\tds_read_b64_tr_b16 v[34:35], v2 offset:57856\n\tv_mfma_f32_16x16x32_bf16 v[148:151], v[44:47], a[188:191], v[148:151]\n\t" \
    "v_mfma_f32_16x16x32_bf16 v[156:159], v[44:47], a[192:195], v[156:159]\n\tv_mfma_f32_16x16x32_bf16 v[156:159], v[40:43], a[180:183], v[156:159]\n\tds_read_b64_tr_b16 v[36:37], v2 offset:61440\n\tds_read_b64_tr_b16 v[38:39], v2 offset:61952\n\tv_mfma_f32_16x16x32_bf16 v[164:167], v[40:43], a[184:187], v[164:167]\n\tv_mfma_f32_16x16x32_bf16 v[164:167], v[44:47], a[196:199], v[164:167]\n\ts_waitcnt lgkmcnt(8)\n\tv_mfma_f32_16x16x32_bf16 v[72:75], v[16:19], a[68:71], v[72:75]\n\t" \
    "ds_read_b64_tr_b16 v[40:41], v3 offset:57344\n\tds_read_b64_tr_b16 v[42:43], v3 offset:57856\n\ts_add_u32 s68, s52, 3328\n\tv_mfma_f32_16x16x32_bf16 v[72:75], v[20:23], a[80:83], v[72:75]\n\tv_cvt_pk_bf16_f32 v144, v144, v145\n\tv_cvt_pk_bf16_f32 v145, v146, v147\n\tv_cvt_pk_bf16_f32 v146, v148, v149\n\tv_mfma_f32_16x16x32_bf16 v[80:83], v[20:23], a[84:87], v[80:83]\n\t" \
    "v_cvt_pk_bf16_f32 v147, v150, v151\n\tv_mfma_f32_16x16x32_bf16 v[80:83], v[16:19], a[72:75], v[80:83]\n\tds_read_b64_tr_b16 v[44:45], v3 offset:61440\n\tds_read_b64_tr_b16 v[46:47], v3 offset:61952\n\tv_mfma_f32_16x16x32_bf16 v[88:91], v[16:19], a[76:79], v[88:91]\n\tbuffer_store_dwordx4 v[144:147], v8, s[8:11], s68 offen offset:0 sc1\n\tv_cvt_pk_bf16_f32 v148, v152, v153\n\tv_cvt_pk_bf16_f32 v149, v154, v155\n\t" \
    "v_mfma_f32_16x16x32_bf16 v[88:91], v[20:23], a[88:91], v[88:91]\n\tv_cvt_pk_bf16_f32 v150, v156, v157\n\tv_cvt_pk_bf16_f32 v151, v158, v159\n\ts_waitcnt lgkmcnt(8)\n\tv_mfma_f32_16x16x32_bf16 v[76:79], v[24:27], a[68:71], v[76:79]\n\tds_read_b64_tr_b16 v[16:17], v237 offset:0\n\tds_read_b64_tr_b16 v[18:19], v237 offset:512\n\tv_mfma_f32_16x16x32_bf16 v[76:79], v[28:31], a[80:83], v[76:79]\n\t" \
    "buffer_store_dwordx4 v[148:151], v8, s[8:11], s68 offen offset:1024 sc1\n\tv_cvt_pk_bf16_f32 v152, v160, v161\n\tv_cvt_pk_bf16_f32 v153, v162, v163\n\tv_mfma_f32_16x16x32_bf16 v[84:87], v[28:31], a[84:87], v[84:87]\n\tv_cvt_pk_bf16_f32 v154, v164, v165\n\tv_cvt_pk_bf16_f32 v155, v166, v167\n\tv_mfma_f32_16x16x32_bf16 v[84:87], v[24:27], a[72:75], v[84:87]\n\tds_read_b64_tr_b16 v[20:21], v237 offset:4096\n\t" \
    "ds_read_b64_tr_b16 v[22:23], v237 offset:4608\n\tv_mfma_f32_16x16x32_bf16 v[92:95], v[24:27], a[76:79], v[92:95]\n\tbuffer_store_dwordx4 v[152:155], v8, s[8:11], s68 offen offset:2048 sc1\n\tv_add3_u32 v156, s36, v144, v145\n\tv_add3_u32 v157, v146, v147, v148\n\tv_mfma_f32_16x16x32_bf16 v[92:95], v[28:31], a[88:91], v[92:95]\n\tv_add3_u32 v156, v156, v149, v150\n\tv_add3_u32 v157, v157, v151, v152\n\t" \
    "s_waitcnt lgkmcnt(8)\n\tv_mfma_f32_16x16x32_bf16 v[72:75], v[32:35], v[180:183], v[72:75]\n\tds_read_b64_tr_b16 v[24:25], v238 offset:0\n\tds_read_b64_tr_b16 v[26:27], v238 offset:512\n\tv_add3_u32 v156, v156, v153, v154\n\tv_mfma_f32_16x16x32_bf16 v[72:75], v[36:39], v[204:207], v[72:75]\n\tv_add3_u32 v156, v156, v157, v155\n\tv_mfma_f32_16x16x32_bf16 v[80:83], v[36:39], v[208:211], v[80:83]\n\t" \
    "v_mfma_f32_16x16x32_bf16 v[80:83], v[32:35], v[184:187], v[80:83]\n\tds_read_b64_tr_b16 v[28:29], v238 offset:4096\n\tds_read_b64_tr_b16 v[30:31], v238 offset:4608\n\tv_mfma_f32_16x16x32_bf16 v[88:91], v[32:35], v[188:191], v[88:91]\n\tbuffer_store_dword v156, v236, s[8:11], s68 offen offset:3072 sc1\n\ts_cmp_le_u32 s4, 0\n\ts_cselect_b32 s69, 262144, 0\n\ts_add_u32 s69, s69, 0\n\t" \
    "v_mfma_f32_16x16x32_bf16 v[88:91], v[36:39], v[212:215], v[88:91]\n\ts_add_u32 m0, s54, 147456\n\ts_waitcnt lgkmcnt(8)\n\tv_mfma_f32_16x16x32_bf16 v[76:79], v[40:43], v[180:183], v[76:79]\n\tds_read_b64_tr_b16 v[32:33], v237 offset:8192\n\tds_read_b64_tr_b16 v[34:35], v237 offset:8704\n\tv_mfma_f32_16x16x32_bf16 v[76:79], v[44:47], v[204:207], v[76:79]\n\tbuffer_load_dwordx4 v12, s[16:19], s69 offen lds\n\t" \
    "s_cmp_le_u32 s4, 1\n\ts_cselect_b32 s69, 262144, 0\n\ts_add_u32 s69, s69, 262144\n\tv_mfma_f32_16x16x32_bf16 v[84:87], v[44:47], v[208:211], v[84:87]\n\ts_add_u32 m0, s54, 151552\n\tv_mfma_f32_16x16x32_bf16 v[84:87], v[40:43], v[184:187], v[84:87]\n\tds_read_b64_tr_b16 v[36:37], v237 offset:12288\n\tds_read_b64_tr_b16 v[38:39], v237 offset:12800\n\t" \
    "v_mfma_f32_16x16x32_bf16 v[92:95], v[40:43], v[188:191], v[92:95]\n\tbuffer_load_dwordx4 v12, s[16:19], s69 offen lds\n\ts_cmp_le_u32 s4, 0\n\ts_cselect_b32 s69, 262144, 0\n\ts_add_u32 s69, s69, 0\n\tv_mfma_f32_16x16x32_bf16 v[92:95], v[44:47], v[212:215], v[92:95]\n\ts_add_u32 m0, s54, 155648\n\ts_barrier\n\t" \
    "s_waitcnt lgkmcnt(8)\n\tv_mfma_f32_16x16x32_bf16 v[72:75], v[16:19], a[92:95], v[72:75]\n\tds_read_b64_tr_b16 v[40:41], v238 offset:8192\n\tds_read_b64_tr_b16 v[42:43], v238 offset:8704\n\tv_mfma_f32_16x16x32_bf16 v[72:75], v[20:23], v[216:219], v[72:75]\n\tbuffer_load_dwordx4 v13, s[16:19], s69 offen lds\n\ts_cmp_le_u32 s4, 1\n\ts_cselect_b32 s69, 262144, 0\n\t" \
    "s_add_u32 s69, s69, 262144\n\tv_mfma_f32_16x16x32_bf16 v[80:83], v[20:23], a[0:3], v[80:83]\n\ts_add_u32 m0, s54, 159744\n\tv_mfma_f32_16x16x32_bf16 v[80:83], v[16:19], a[96:99], v[80:83]\n\tds_read_b64_tr_b16 v[44:45], v238 offset:12288\n\tds_read_b64_tr_b16 v[46:47], v238 offset:12800\n\tv_mfma_f32_16x16x32_bf16 v[88:91], v[16:19], a[100:103], v[88:91]\n\tbuffer_load_dwordx4 v13, s[16:19], s69 offen lds\n\t" \
    "s_cmp_le_u32 s4, 2\n\ts_cselect_b32 s69, 262144, 0\n\ts_add_u32 s69, s69, 524288\n\tv_mfma_f32_16x16x32_bf16 v[88:91], v[20:23], a[4:7], v[88:91]\n\ts_add_u32 m0, s54, 0\n\ts_waitcnt lgkmcnt(8)\n\tv_mfma_f32_16x16x32_bf16 v[76:79], v[24:27], a[92:95], v[76:79]\n\tds_read_b64_tr_b16 v[16:17], v237 offset:16384\n\t" \
    "ds_read_b64_tr_b16 v[18:19], v237 offset:16896\n\tv_mfma_f32_16x16x32_bf16 v[76:79], v[28:31], v[216:219], v[76:79]\n\tbuffer_load_dwordx4 v12, s[16:19], s69 offen lds\n\ts_cmp_le_u32 s4, 3\n\ts_cselect_b32 s69, 262144, 0\n\ts_add_u32 s69, s69, 786432\n\tv_mfma_f32_16x16x32_bf16 v[84:87], v[28:31], a[0:3], v[84:87]\n\ts_add_u32 m0, s54, 4096\n\t" \
    "v_mfma_f32_16x16x32_bf16 v[84:87], v[24:27], a[96:99], v[84:87]\n\tds_read_b64_tr_b16 v[20:21], v237 offset:20480\n\tds_read_b64_tr_b16 v[22:23], v237 offset:20992\n\tv_mfma_f32_16x16x32_bf16 v[92:95], v[24:27], a[100:103], v[92:95]\n\tbuffer_load_dwordx4 v12, s[16:19], s69 offen lds\n\ts_cmp_le_u32 s4, 2\n\ts_cselect_b32 s69, 262144, 0\n\ts_add_u32 s69, s69, 524288\n\t" \
    "v_mfma_f32_16x16x32_bf16 v[92:95], v[28:31], a[4:7], v[92:95]\n\ts_add_u32 m0, s54, 8192\n\ts_waitcnt lgkmcnt(8)\n\tv_mfma_f32_16x16x32_bf16 v[72:75], v[32:35], a[8:11], v[72:75]\n\tds_read_b64_tr_b16 v[24:25], v238 offset:16384\n\tds_read_b64_tr_b16 v[26:27], v238 offset:16896\n\tv_mfma_f32_16x16x32_bf16 v[72:75], v[36:39], a[104:107], v[72:75]\n\tbuffer_load_dwordx4 v13, s[16:19], s69 offen lds\n\t" \
    "s_cmp_le_u32 s4, 3\n\ts_cselect_b32 s69, 262144, 0\n\ts_add_u32 s69, s69, 786432\n\tv_mfma_f32_16x16x32_bf16 v[80:83], v[36:39], a[108:111], v[80:83]\n\ts_add_u32 m0, s54, 12288\n\tv_mfma_f32_16x16x32_bf16 v[80:83], v[32:35], a[12:15], v[80:83]\n\tds_read_b64_tr_b16 v[28:29], v238 offset:20480\n\tds_read_b64_tr_b16 v[30:31], v238 offset:20992\n\t" \
    "v_mfma_f32_16x16x32_bf16 v[88:91], v[32:35], a[16:19], v[88:91]\n\tbuffer_load_dwordx4 v13, s[16:19], s69 offen lds\n\ts_cmp_le_u32 s4, 4\n\ts_cselect_b32 s69, 262144, 0\n\ts_add_u32 s69, s69, 1048576\n\tv_mfma_f32_16x16x32_bf16 v[88:91], v[36:39], a[112:115], v[88:91]\n\ts_add_u32 m0, s54, 16384\n\ts_waitcnt lgkmcnt(8)\n\t" \
    "v_mfma_f32_16x16x32_bf16 v[76:79], v[40:43], a[8:11], v[76:79]\n\tds_read_b64_tr_b16 v[32:33], v237 offset:24576\n\tds_read_b64_tr_b16 v[34:35], v237 offset:25088\n\tv_mfma_f32_16x16x32_bf16 v[76:79], v[44:47], a[104:107], v[76:79]\n\tbuffer_load_dwordx4 v12, s[16:19], s69 offen lds\n\ts_cmp_le_u32 s4, 5\n\ts_cselect_b32 s69, 262144, 0\n\ts_add_u32 s69, s69, 1310720\n\t" \
    "v_mfma_f32_16x16x32_bf16 v[84:87], v[44:47], a[108:111], v[84:87]\n\ts_add_u32 m0, s54, 20480\n\tv_mfma_f32_16x16x32_bf16 v[84:87], v[40:43], a[12:15], v[84:87]\n\tds_read_b64_tr_b16 v[36:37], v237 offset:28672\n\tds_read_b64_tr_b16 v[38:39], v237 offset:29184\n\tv_mfma_f32_16x16x32_bf16 v[92:95], v[40:43], a[16:19], v[92:95]\n\tv_mfma_f32_16x16x32_bf16 v[92:95], v[44:47], a[112:115], v[92:95]\n\ts_waitcnt lgkmcnt(8)\n\t" \
    "v_mfma_f32_16x16x32_bf16 v[72:75], v[16:19], a[20:23], v[72:75]\n\tds_read_b64_tr_b16 v[40:41], v238 offset:24576\n\tds_read_b64_tr_b16 v[42:43], v238 offset:25088\n\tv_mfma_f32_16x16x32_bf16 v[72:75], v[20:23], a[32:35], v[72:75]\n\tbuffer_load_dwordx4 v12, s[16:19], s69 offen lds\n\ts_cmp_le_u32 s4, 4\n\ts_cselect_b32 s69, 262144, 0\n\ts_add_u32 s69, s69, 1048576\n\t" \
    "v_mfma_f32_16x16x32_bf16 v[80:83], v[20:23], a[36:39], v[80:83]\n\ts_add_u32 m0, s54, 24576\n\tv_mfma_f32_16x16x32_bf16 v[80:83], v[16:19], a[24:27], v[80:83]\n\tds_read_b64_tr_b16 v[44:45], v238 offset:28672\n\tds_read_b64_tr_b16 v[46:47], v238 offset:29184\n\tv_mfma_f32_16x16x32_bf16 v[88:91], v[16:19], a[28:31], v[88:91]\n\tbuffer_load_dwordx4 v13, s[16:19], s69 offen lds\n\ts_cmp_le_u32 s4, 5\n\t" \
    "s_cselect_b32 s69, 262144, 0\n\ts_add_u32 s69, s69, 1310720\n\tv_mfma_f32_16x16x32_bf16 v[88:91], v[20:23], a[40:43], v[88:91]\n\ts_add_u32 m0, s54, 28672\n\ts_waitcnt lgkmcnt(8)\n\tv_mfma_f32_16x16x32_bf16 v[76:79], v[24:27], a[20:23], v[76:79]\n\tds_read_b64_tr_b16 v[16:17], v237 offset:32768\n\tds_read_b64_tr_b16 v[18:19], v237 offset:33280\n\t" \
    "v_mfma_f32_16x16x32_bf16 v[76:79], v[28:31], a[32:35], v[76:79]\n\tbuffer_load_dwordx4 v13, s[16:19], s69 offen lds\n\ts_cmp_le_u32 s4, 6\n\ts_cselect_b32 s69, 262144, 0\n\ts_add_u32 s69, s69, 1572864\n\tv_mfma_f32_16x16x32_bf16 v[84:87], v[28:31], a[36:39], v[84:87]\n\ts_add_u32 m0, s54, 32768\n\tv_mfma_f32_16x16x32_bf16 v[84:87], v[24:27], a[24:27], v[84:87]\n\t" \
    "ds_read_b64_tr_b16 v[20:21], v237 offset:36864\n\tds_read_b64_tr_b16 v[22:23], v237 offset:37376\n\tv_mfma_f32_16x16x32_bf16 v[92:95], v[24:27], a[28:31], v[92:95]\n\tv_mfma_f32_16x16x32_bf16 v[92:95], v[28:31], a[40:43], v[92:95]\n\ts_waitcnt lgkmcnt(8)\n\tv_mfma_f32_16x16x32_bf16 v[72:75], v[32:35], a[44:47], v[72:75]\n\tds_read_b64_tr_b16 v[24:25], v238 offset:32768\n\tds_read_b64_tr_b16 v[26:27], v238 offset:33280\n\t" \
    "v_mfma_f32_16x16x32_bf16 v[72:75], v[36:39], a[56:59], v[72:75]\n\tbuffer_load_dwordx4 v12, s[16:19], s69 offen lds\n\ts_cmp_le_u32 s4, 7\n\ts_cselect_b32 s69, 262144, 0\n\ts_add_u32 s69, s69, 1835008\n\tv_mfma_f32_16x16x32_bf16 v[80:83], v[36:39], a[60:63], v[80:83]\n\ts_add_u32 m0, s54, 36864\n\tv_mfma_f32_16x16x32_bf16 v[80:83], v[32:35], a[48:51], v[80:83]\n\t" \
    "ds_read_b64_tr_b16 v[28:29], v238 offset:36864\n\tds_read_b64_tr_b16 v[30:31], v238 offset:37376\n\tv_mfma_f32_16x16x32_bf16 v[88:91], v[32:35], a[52:55], v[88:91]\n\tbuffer_load_dwordx4 v12, s[16:19], s69 offen lds\n\ts_cmp_le_u32 s4, 6\n\ts_cselect_b32 s69, 262144, 0\n\ts_add_u32 s69, s69, 1572864\n\tv_mfma_f32_16x16x32_bf16 v[88:91], v[36:39], a[64:67], v[88:91]\n\t" \
    "s_add_u32 m0, s54, 40960\n\ts_waitcnt lgkmcnt(8)\n\tv_mfma_f32_16x16x32_bf16 v[76:79], v[40:43], a[44:47], v[76:79]\n\tds_read_b64_tr_b16 v[32:33], v237 offset:40960\n\tds_read_b64_tr_b16 v[34:35], v237 offset:41472\n\tv_mfma_f32_16x16x32_bf16 v[76:79], v[44:47], a[56:59], v[76:79]\n\tbuffer_load_dwordx4 v13, s[16:19], s69 offen lds\n\ts_add_u32 s68, s72, 3328\n\t" \
    "v_mfma_f32_16x16x32_bf16 v[84:87], v[44:47], a[60:63], v[84:87]\n\tv_mfma_f32_16x16x32_bf16 v[84:87], v[40:43], a[48:51], v[84:87]\n\tds_read_b64_tr_b16 v[36:37], v237 offset:45056\n\tds_read_b64_tr_b16 v[38:39], v237 offset:45568\n\tv_mfma_f32_16x16x32_bf16 v[92:95], v[40:43], a[52:55], v[92:95]\n\tbuffer_load_dwordx4 v[144:147], v8, s[8:11], s68 offen offset:0 sc1\n\tv_mfma_f32_16x16x32_bf16 v[92:95], v[44:47], a[64:67], v[92:95]\n\ts_barrier\n\t" \
    "s_waitcnt lgkmcnt(8)\n\tv_mfma_f32_16x16x32_bf16 v[72:75], v[16:19], a[116:119], v[72:75]\n\tds_read_b64_tr_b16 v[40:41], v238 offset:40960\n\tds_read_b64_tr_b16 v[42:43], v238 offset:41472\n\tv_mfma_f32_16x16x32_bf16 v[72:75], v[20:23], a[128:131], v[72:75]\n\tbuffer_load_dwordx4 v[148:151], v8, s[8:11], s68 offen offset:1024 sc1\n\tv_mfma_f32_16x16x32_bf16 v[80:83], v[20:23], a[132:135], v[80:83]\n\tv_mfma_f32_16x16x32_bf16 v[80:83], v[16:19], a[120:123], v[80:83]\n\t" \
    "ds_read_b64_tr_b16 v[44:45], v238 offset:45056\n\tds_read_b64_tr_b16 v[46:47], v238 offset:45568\n\tv_mfma_f32_16x16x32_bf16 v[88:91], v[16:19], a[124:127], v[88:91]\n\tbuffer_load_dwordx4 v[152:155], v8, s[8:11], s68 offen offset:2048 sc1\n\tv_mfma_f32_16x16x32_bf16 v[88:91], v[20:23], a[136:139], v[88:91]\n\ts_waitcnt lgkmcnt(8)\n\tv_mfma_f32_16x16x32_bf16 v[76:79], v[24:27], a[116:119], v[76:79]\n\tds_read_b64_tr_b16 v[16:17], v237 offset:49152\n\t" \
    "ds_read_b64_tr_b16 v[18:19], v237 offset:49664\n\tv_mfma_f32_16x16x32_bf16 v[76:79], v[28:31], a[128:131], v[76:79]\n\tv_mfma_f32_16x16x32_bf16 v[84:87], v[28:31], a[132:135], v[84:87]\n\tv_mfma_f32_16x16x32_bf16 v[84:87], v[24:27], a[120:123], v[84:87]\n\tds_read_b64_tr_b16 v[20:21], v237 offset:53248\n\tds_read_b64_tr_b16 v[22:23], v237 offset:53760\n\tv_mfma_f32_16x16x32_bf16 v[92:95], v[24:27], a[124:127], v[92:95]\n\tbuffer_load_dword v156, v236, s[8:11], s68 offen offset:3072 sc1\n\t" \
    "s_cmp_le_u32 s4, 7\n\ts_cselect_b32 s69, 262144, 0\n\ts_add_u32 s69, s69, 1835008\n\tv_mfma_f32_16x16x32_bf16 v[92:95], v[28:31], a[136:139], v[92:95]\n\ts_add_u32 m0, s54, 45056\n\ts_waitcnt lgkmcnt(8)\n\tv_mfma_f32_16x16x32_bf16 v[72:75], v[32:35], v[168:171], v[72:75]\n\tds_read_b64_tr_b16 v[24:25], v238 offset:49152\n\t" \
    "ds_read_b64_tr_b16 v[26:27], v238 offset:49664\n\tv_mfma_f32_16x16x32_bf16 v[72:75], v[36:39], a[140:143], v[72:75]\n\tv_mfma_f32_16x16x32_bf16 v[80:83], v[36:39], a[144:147], v[80:83]\n\tv_mfma_f32_16x16x32_bf16 v[80:83], v[32:35], v[172:175], v[80:83]\n\tds_read_b64_tr_b16 v[28:29], v238 offset:53248\n\tds_read_b64_tr_b16 v[30:31], v238 offset:53760\n\tv_mfma_f32_16x16x32_bf16 v[88:91], v[32:35], v[176:179], v[88:91]\n\tbuffer_load_dwordx4 v13, s[16:19], s69 offen lds\n\t" \
    "s_cmp_le_u32 s4, 8\n\ts_cselect_b32 s69, 262144, 0\n\ts_add_u32 s69, s69, 2097152\n\tv_mfma_f32_16x16x32_bf16 v[88:91], v[36:39], a[148:151], v[88:91]\n\ts_add_u32 m0, s54, 49152\n\ts_waitcnt lgkmcnt(8)\n\tv_mfma_f32_16x16x32_bf16 v[76:79], v[40:43], v[168:171], v[76:79]\n\tds_read_b64_tr_b16 v[32:33], v237 offset:57344\n\t" \
    "ds_read_b64_tr_b16 v[34:35], v237 offset:57856\n\tv_mfma_f32_16x16x32_bf16 v[76:79], v[44:47], a[140:143], v[76:79]\n\tbuffer_load_dwordx4 v12, s[16:19], s69 offen lds\n\tv_mfma_f32_16x16x32_bf16 v[84:87], v[44:47], a[144:147], v[84:87]\n\tv_mfma_f32_16x16x32_bf16 v[84:87], v[40:43], v[172:175], v[84:87]\n\tds_read_b64_tr_b16 v[36:37], v237 offset:61440\n\tds_read_b64_tr_b16 v[38:39], v237 offset:61952\n\tv_mfma_f32_16x16x32_bf16 v[92:95], v[40:43], v[176:179], v[92:95]\n\t" \
    "v_mfma_f32_16x16x32_bf16 v[92:95], v[44:47], a[148:151], v[92:95]\n\ts_waitcnt lgkmcnt(8)\n\tv_mfma_f32_16x16x32_bf16 v[72:75], v[16:19], a[152:155], v[72:75]\n\tds_read_b64_tr_b16 v[40:41], v238 offset:57344\n\tds_read_b64_tr_b16 v[42:43], v238 offset:57856\n\tv_mfma_f32_16x16x32_bf16 v[72:75], v[20:23], a[164:167], v[72:75]\n\tv_mfma_f32_16x16x32_bf16 v[80:83], v[20:23], a[168:171], v[80:83]\n\tv_mfma_f32_16x16x32_bf16 v[80:83], v[16:19], a[156:159], v[80:83]\n\t" \
    "ds_read_b64_tr_b16 v[44:45], v238 offset:61440\n\tds_read_b64_tr_b16 v[46:47], v238 offset:61952\n\tv_mfma_f32_16x16x32_bf16 v[88:91], v[16:19], a[160:163], v[88:91]\n\tv_mfma_f32_16x16x32_bf16 v[88:91], v[20:23], a[172:175], v[88:91]\n\tbuffer_load_dwordx2 v[250:251], v241, s[8:11], s50 offen sc1\n\ts_waitcnt lgkmcnt(8)\n\tv_mfma_f32_16x16x32_bf16 v[76:79], v[24:27], a[152:155], v[76:79]\n\tds_read_b64_tr_b16 v[16:17], v220 offset:0\n\t" \
    "ds_read_b64_tr_b16 v[18:19], v220 offset:2048\n\tv_mfma_f32_16x16x32_bf16 v[76:79], v[28:31], a[164:167], v[76:79]\n\tv_mfma_f32_16x16x32_bf16 v[84:87], v[28:31], a[168:171], v[84:87]\n\tv_mfma_f32_16x16x32_bf16 v[84:87], v[24:27], a[156:159], v[84:87]\n\tds_read_b64_tr_b16 v[20:21], v220 offset:8192\n\tds_read_b64_tr_b16 v[22:23], v220 offset:10240\n\tv_mfma_f32_16x16x32_bf16 v[92:95], v[24:27], a[160:163], v[92:95]\n\tv_mfma_f32_16x16x32_bf16 v[92:95], v[28:31], a[172:175], v[92:95]\n\t" \
    "s_waitcnt lgkmcnt(8)\n\tv_mfma_f32_16x16x32_bf16 v[72:75], v[32:35], a[176:179], v[72:75]\n\tds_read_b64_tr_b16 v[24:25], v221 offset:0\n\tds_read_b64_tr_b16 v[26:27], v221 offset:2048\n\tv_mfma_f32_16x16x32_bf16 v[72:75], v[36:39], a[188:191], v[72:75]\n\tv_mfma_f32_16x16x32_bf16 v[80:83], v[36:39], a[192:195], v[80:83]\n\tv_mfma_f32_16x16x32_bf16 v[80:83], v[32:35], a[180:183], v[80:83]\n\tds_read_b64_tr_b16 v[28:29], v221 offset:8192\n\t" \
    "ds_read_b64_tr_b16 v[30:31], v221 offset:10240\n\tv_mfma_f32_16x16x32_bf16 v[88:91], v[32:35], a[184:187], v[88:91]\n\tv_mfma_f32_16x16x32_bf16 v[88:91], v[36:39], a[196:199], v[88:91]\n\ts_waitcnt lgkmcnt(8)\n\tv_mfma_f32_16x16x32_bf16 v[76:79], v[40:43], a[176:179], v[76:79]\n\tds_read_b64_tr_b16 v[32:33], v222 offset:0\n\tds_read_b64_tr_b16 v[34:35], v222 offset:2048\n\tv_mfma_f32_16x16x32_bf16 v[76:79], v[44:47], a[188:191], v[76:79]\n\t" \
    "v_mfma_f32_16x16x32_bf16 v[84:87], v[44:47], a[192:195], v[84:87]\n\tv_mfma_f32_16x16x32_bf16 v[84:87], v[40:43], a[180:183], v[84:87]\n\tds_read_b64_tr_b16 v[36:37], v222 offset:8192\n\tds_read_b64_tr_b16 v[38:39], v222 offset:10240\n\tv_mfma_f32_16x16x32_bf16 v[92:95], v[40:43], a[184:187], v[92:95]\n\tv_mfma_f32_16x16x32_bf16 v[92:95], v[44:47], a[196:199], v[92:95]\n\ts_mov_b32 s71, 1048576\n\ts_waitcnt vmcnt(3)\n\t" \
    "109:\n\tv_add3_u32 v158, s36, v144, v145\n\tv_add3_u32 v159, v146, v147, v148\n\tv_add3_u32 v158, v158, v149, v150\n\tv_add3_u32 v159, v159, v151, v152\n\tv_add3_u32 v158, v158, v153, v154\n\tv_add3_u32 v158, v158, v159, v155\n\tv_cmp_ne_u32 vcc, v158, v156\n\t" \
    "s_cbranch_vccz 110f\n\ts_sub_u32 s71, s71, 1\n\ts_cbranch_scc1 110f\n\ts_sleep 6\n\ts_add_u32 s73, s72, 3328\n\tbuffer_load_dwordx4 v[144:147], v8, s[8:11], s73 offen offset:0 sc1\n\tbuffer_load_dwordx4 v[148:151], v8, s[8:11], s73 offen offset:1024 sc1\n\tbuffer_load_dwordx4 v[152:155], v8, s[8:11], s73 offen offset:2048 sc1\n\t" \
    "buffer_load_dword v156, v236, s[8:11], s73 offen offset:3072 sc1\n\ts_waitcnt vmcnt(0)\n\ts_branch 109b\n\t110:\n\ts_add_u32 s45, s45, 55296\n\tv_lshlrev_b32 v96, 16, v144\n\tv_and_b32 v97, 0xffff0000, v144\n\tv_lshlrev_b32 v98, 16, v145\n\t" \
    "v_and_b32 v99, 0xffff0000, v145\n\tv_lshlrev_b32 v100, 16, v146\n\tv_and_b32 v101, 0xffff0000, v146\n\tv_lshlrev_b32 v102, 16, v147\n\tv_and_b32 v103, 0xffff0000, v147\n\tv_pk_add_f32 v[72:73], v[72:73], v[96:97]\n\tv_pk_add_f32 v[74:75], v[74:75], v[98:99]\n\tv_pk_add_f32 v[76:77], v[76:77], v[100:101]\n\t" \
    "v_pk_add_f32 v[78:79], v[78:79], v[102:103]\n\tv_pk_mul_f32 v[96:97], v[72:73], v[72:73]\n\tv_pk_mul_f32 v[98:99], v[74:75], v[74:75]\n\tv_pk_mul_f32 v[100:101], v[76:77], v[76:77]\n\tv_pk_mul_f32 v[102:103], v[78:79], v[78:79]\n\tv_pk_fma_f32 v[96:97], v[96:97], v[246:247], v[246:247] op_sel:[0,0,1] op_sel_hi:[1,0,1]\n\tv_pk_fma_f32 v[98:99], v[98:99], v[246:247], v[246:247] op_sel:[0,0,1] op_sel_hi:[1,0,1]\n\tv_pk_fma_f32 v[100:101], v[100:101], v[246:247], v[246:247] op_sel:[0,0,1] op_sel_hi:[1,0,1]\n\t" \
    "v_pk_fma_f32 v[102:103], v[102:103], v[246:247], v[246:247] op_sel:[0,0,1] op_sel_hi:[1,0,1]\n\tv_pk_mul_f32 v[96:97], v[96:97], v[72:73]\n\tv_pk_mul_f32 v[98:99], v[98:99], v[74:75]\n\tv_pk_mul_f32 v[100:101], v[100:101], v[76:77]\n\tv_pk_mul_f32 v[102:103], v[102:103], v[78:79]\n\tv_lshlrev_b32 v104, 16, v148\n\tv_exp_f32 v96, v96\n\tv_and_b32 v105, 0xffff0000, v148\n\t" \
    "v_exp_f32 v97, v97\n\tv_lshlrev_b32 v106, 16, v149\n\tv_exp_f32 v98, v98\n\tv_and_b32 v107, 0xffff0000, v149\n\tv_exp_f32 v99, v99\n\tv_lshlrev_b32 v108, 16, v150\n\tv_exp_f32 v100, v100\n\tv_and_b32 v109, 0xffff0000, v150\n\t" \
    "v_exp_f32 v101, v101\n\tv_lshlrev_b32 v110, 16, v151\n\tv_exp_f32 v102, v102\n\tv_and_b32 v111, 0xffff0000, v151\n\tv_exp_f32 v103, v103\n\tv_pk_add_f32 v[80:81], v[80:81], v[104:105]\n\tv_pk_add_f32 v[96:97], v[96:97], 1.0 op_sel_hi:[1,0]\n\tv_pk_add_f32 v[98:99], v[98:99], 1.0 op_sel_hi:[1,0]\n\t" \
    "v_pk_add_f32 v[100:101], v[100:101], 1.0 op_sel_hi:[1,0]\n\tv_pk_add_f32 v[102:103], v[102:103], 1.0 op_sel_hi:[1,0]\n\tv_pk_add_f32 v[82:83], v[82:83], v[106:107]\n\tv_rcp_f32 v96, v96\n\tv_pk_add_f32 v[84:85], v[84:85], v[108:109]\n\tv_rcp_f32 v97, v97\n\tv_pk_add_f32 v[86:87], v[86:87], v[110:111]\n\tv_rcp_f32 v98, v98\n\t" \
    "v_pk_mul_f32 v[104:105], v[80:81], v[80:81]\n\tv_rcp_f32 v99, v99\n\tv_pk_mul_f32 v[106:107], v[82:83], v[82:83]\n\tv_rcp_f32 v100, v100\n\tv_pk_mul_f32 v[108:109], v[84:85], v[84:85]\n\tv_rcp_f32 v101, v101\n\tv_pk_mul_f32 v[110:111], v[86:87], v[86:87]\n\tv_rcp_f32 v102, v102\n\t" \
    "v_pk_fma_f32 v[104:105], v[104:105], v[246:247], v[246:247] op_sel:[0,0,1] op_sel_hi:[1,0,1]\n\tv_rcp_f32 v103, v103\n\tv_pk_fma_f32 v[106:107], v[106:107], v[246:247], v[246:247] op_sel:[0,0,1] op_sel_hi:[1,0,1]\n\tv_pk_mul_f32 v[96:97], v[96:97], v[72:73]\n\tv_pk_mul_f32 v[98:99], v[98:99], v[74:75]\n\tv_pk_mul_f32 v[100:101], v[100:101], v[76:77]\n\tv_pk_mul_f32 v[102:103], v[102:103], v[78:79]\n\tv_cvt_pk_bf16_f32 v72, v96, v97\n\t" \
    "v_cvt_pk_bf16_f32 v73, v98, v99\n\tv_cvt_pk_bf16_f32 v74, v100, v101\n\tv_cvt_pk_bf16_f32 v75, v102, v103\n\ts_cmp_lg_u32 s39, 2\n\ts_cbranch_scc1 111f\n\ts_waitcnt vmcnt(0)\n\tv_cmp_ne_u64 vcc, s[36:37], v[250:251]\n\ts_cbranch_vccz 113f\n\t" \
    "s_mov_b32 s71, 1048576\n\t112:\n\ts_sleep 4\n\tbuffer_load_dwordx2 v[250:251], v241, s[8:11], s50 offen sc1\n\ts_waitcnt vmcnt(0)\n\tv_cmp_ne_u64 vcc, s[36:37], v[250:251]\n\ts_cbranch_vccz 113f\n\ts_sub_u32 s71, s71, 1\n\t" \
    "s_cbranch_scc0 112b\n\t113:\n\t111:\n\ts_cmp_le_u32 s4, 0\n\ts_cselect_b32 s68, 3072, 0\n\ts_add_u32 s68, s68, s46\n\ts_add_u32 s68, s68, 0\n\tbuffer_load_dwordx4 v[204:207], v8, s[8:11], s68 offen offset:0 sc1\n\t" \
    "buffer_load_dwordx4 v[208:211], v8, s[8:11], s68 offen offset:1024 sc1\n\tbuffer_load_dwordx4 v[212:215], v8, s[8:11], s68 offen offset:2048 sc1\n\tv_pk_fma_f32 v[108:109], v[108:109], v[246:247], v[246:247] op_sel:[0,0,1] op_sel_hi:[1,0,1]\n\ts_cmp_le_u32 s4, 1\n\ts_cselect_b32 s68, 3072, 0\n\tv_pk_fma_f32 v[110:111], v[110:111], v[246:247], v[246:247] op_sel:[0,0,1] op_sel_hi:[1,0,1]\n\ts_add_u32 s68, s68, s46\n\tv_pk_mul_f32 v[104:105], v[104:105], v[80:81]\n\t" \
    "s_add_u32 s68, s68, 3072\n\tv_pk_mul_f32 v[106:107], v[106:107], v[82:83]\n\tv_pk_mul_f32 v[108:109], v[108:109], v[84:85]\n\tv_pk_mul_f32 v[110:111], v[110:111], v[86:87]\n\tv_lshlrev_b32 v112, 16, v152\n\tv_exp_f32 v104, v104\n\tv_and_b32 v113, 0xffff0000, v152\n\tv_exp_f32 v105, v105\n\t" \
    "v_lshlrev_b32 v114, 16, v153\n\tv_exp_f32 v106, v106\n\tv_and_b32 v115, 0xffff0000, v153\n\tv_exp_f32 v107, v107\n\tv_lshlrev_b32 v116, 16, v154\n\tbuffer_load_dwordx4 v[216:219], v8, s[8:11], s68 offen offset:0 sc1\n\tv_exp_f32 v108, v108\n\tv_and_b32 v117, 0xffff0000, v154\n\t" \
    "v_exp_f32 v109, v109\n\tv_lshlrev_b32 v118, 16, v155\n\tv_exp_f32 v110, v110\n\tv_and_b32 v119, 0xffff0000, v155\n\tv_exp_f32 v111, v111\n\tv_pk_add_f32 v[88:89], v[88:89], v[112:113]\n\tv_pk_add_f32 v[104:105], v[104:105], 1.0 op_sel_hi:[1,0]\n\tv_pk_add_f32 v[106:107], v[106:107], 1.0 op_sel_hi:[1,0]\n\t" \
    "v_pk_add_f32 v[108:109], v[108:109], 1.0 op_sel_hi:[1,0]\n\tbuffer_load_dwordx4 a[0:3], v8, s[8:11], s68 offen offset:1024 sc1\n\tv_pk_add_f32 v[110:111], v[110:111], 1.0 op_sel_hi:[1,0]\n\tv_pk_add_f32 v[90:91], v[90:91], v[114:115]\n\tv_rcp_f32 v104, v104\n\tv_pk_add_f32 v[92:93], v[92:93], v[116:117]\n\tv_rcp_f32 v105, v105\n\tv_pk_add_f32 v[94:95], v[94:95], v[118:119]\n\t" \
    "v_rcp_f32 v106, v106\n\tv_pk_mul_f32 v[112:113], v[88:89], v[88:89]\n\tv_rcp_f32 v107, v107\n\tv_pk_mul_f32 v[114:115], v[90:91], v[90:91]\n\tv_rcp_f32 v108, v108\n\tbuffer_load_dwordx4 a[4:7], v8, s[8:11], s68 offen offset:2048 sc1\n\tv_pk_mul_f32 v[116:117], v[92:93], v[92:93]\n\ts_cmp_le_u32 s4, 2\n\t" \
    "s_cselect_b32 s68, 3072, 0\n\tv_rcp_f32 v109, v109\n\ts_add_u32 s68, s68, s46\n\tv_pk_mul_f32 v[118:119], v[94:95], v[94:95]\n\ts_add_u32 s68, s68, 6144\n\tv_rcp_f32 v110, v110\n\tv_pk_fma_f32 v[112:113], v[112:113], v[246:247], v[246:247] op_sel:[0,0,1] op_sel_hi:[1,0,1]\n\tv_rcp_f32 v111, v111\n\t" \
    "v_pk_fma_f32 v[114:115], v[114:115], v[246:247], v[246:247] op_sel:[0,0,1] op_sel_hi:[1,0,1]\n\tv_pk_mul_f32 v[104:105], v[104:105], v[80:81]\n\tbuffer_load_dwordx4 a[8:11], v8, s[8:11], s68 offen offset:0 sc1\n\tv_pk_mul_f32 v[106:107], v[106:107], v[82:83]\n\tv_pk_mul_f32 v[108:109], v[108:109], v[84:85]\n\tv_pk_mul_f32 v[110:111], v[110:111], v[86:87]\n\tv_cvt_pk_bf16_f32 v80, v104, v105\n\tv_cvt_pk_bf16_f32 v81, v106, v107\n\t" \
    "v_cvt_pk_bf16_f32 v82, v108, v109\n\tv_cvt_pk_bf16_f32 v83, v110, v111\n\tv_pk_fma_f32 v[116:117], v[116:117], v[246:247], v[246:247] op_sel:[0,0,1] op_sel_hi:[1,0,1]\n\tv_pk_fma_f32 v[118:119], v[118:119], v[246:247], v[246:247] op_sel:[0,0,1] op_sel_hi:[1,0,1]\n\tv_pk_mul_f32 v[112:113], v[112:113], v[88:89]\n\tv_pk_mul_f32 v[114:115], v[114:115], v[90:91]\n\tv_pk_mul_f32 v[116:117], v[116:117], v[92:93]\n\tv_pk_mul_f32 v[118:119], v[118:119], v[94:95]\n\t" \
    "v_exp_f32 v112, v112\n\tv_exp_f32 v113, v113\n\tbuffer_load_dwordx4 a[12:15], v8, s[8:11], s68 offen offset:1024 sc1\n\tv_exp_f32 v114, v114\n\tv_exp_f32 v115, v115\n\tv_exp_f32 v116, v116\n\tv_exp_f32 v117, v117\n\tv_exp_f32 v118, v118\n\t" \
    "v_exp_f32 v119, v119\n\tv_pk_add_f32 v[112:113], v[112:113], 1.0 op_sel_hi:[1,0]\n\tv_pk_add_f32 v[114:115], v[114:115], 1.0 op_sel_hi:[1,0]\n\tbuffer_load_dwordx4 a[16:19], v8, s[8:11], s68 offen offset:2048 sc1\n\tv_pk_add_f32 v[116:117], v[116:117], 1.0 op_sel_hi:[1,0]\n\ts_cmp_le_u32 s4, 3\n\ts_cselect_b32 s68, 3072, 0\n\tv_pk_add_f32 v[118:119], v[118:119], 1.0 op_sel_hi:[1,0]\n\t" \
    "s_add_u32 s68, s68, s46\n\tv_rcp_f32 v112, v112\n\ts_add_u32 s68, s68, 9216\n\tv_rcp_f32 v113, v113\n\tv_rcp_f32 v114, v114\n\tv_rcp_f32 v115, v115\n\tv_rcp_f32 v116, v116\n\tbuffer_load_dwordx4 a[20:23], v8, s[8:11], s68 offen offset:0 sc1\n\t" \
    "v_rcp_f32 v117, v117\n\tv_rcp_f32 v118, v118\n\tv_rcp_f32 v119, v119\n\tv_pk_mul_f32 v[112:113], v[112:113], v[88:89]\n\tv_pk_mul_f32 v[114:115], v[114:115], v[90:91]\n\tv_pk_mul_f32 v[116:117], v[116:117], v[92:93]\n\tv_pk_mul_f32 v[118:119], v[118:119], v[94:95]\n\tv_cvt_pk_bf16_f32 v88, v112, v113\n\t" \
    "v_cvt_pk_bf16_f32 v89, v114, v115\n\tv_cvt_pk_bf16_f32 v90, v116, v117\n\tv_cvt_pk_bf16_f32 v91, v118, v119\n\ts_cmp_lg_u32 s39, 2\n\ts_cbranch_scc1 102f\n\ts_barrier\n\ts_waitcnt lgkmcnt(8)\n\tv_mfma_f32_16x16x32_bf16 v[120:123], v[192:195], v[16:19], 0\n\t" \
    "ds_read_b64_tr_b16 v[40:41], v223 offset:0\n\tds_read_b64_tr_b16 v[42:43], v223 offset:2048\n\tv_mfma_f32_16x16x32_bf16 v[120:123], v[72:75], v[20:23], v[120:123]\n\tbuffer_load_dwordx4 a[24:27], v8, s[8:11], s68 offen offset:1024 sc1\n\tv_mfma_f32_16x16x32_bf16 v[128:131], v[196:199], v[16:19], 0\n\tv_mfma_f32_16x16x32_bf16 v[128:131], v[80:83], v[20:23], v[128:131]\n\tds_read_b64_tr_b16 v[44:45], v223 offset:8192\n\tds_read_b64_tr_b16 v[46:47], v223 offset:10240\n\t" \
    "v_mfma_f32_16x16x32_bf16 v[136:139], v[200:203], v[16:19], 0\n\tbuffer_load_dwordx4 a[28:31], v8, s[8:11], s68 offen offset:2048 sc1\n\ts_cmp_le_u32 s4, 4\n\ts_cselect_b32 s68, 3072, 0\n\ts_add_u32 s68, s68, s46\n\tv_mfma_f32_16x16x32_bf16 v[136:139], v[88:91], v[20:23], v[136:139]\n\ts_add_u32 s68, s68, 12288\n\ts_waitcnt lgkmcnt(8)\n\t" \
    "v_mfma_f32_16x16x32_bf16 v[124:127], v[192:195], v[24:27], 0\n\tds_read_b64_tr_b16 v[16:17], v220 offset:4096\n\tds_read_b64_tr_b16 v[18:19], v220 offset:6144\n\tv_mfma_f32_16x16x32_bf16 v[124:127], v[72:75], v[28:31], v[124:127]\n\tbuffer_load_dwordx4 a[32:35], v8, s[8:11], s68 offen offset:0 sc1\n\tv_mfma_f32_16x16x32_bf16 v[132:135], v[196:199], v[24:27], 0\n\tv_mfma_f32_16x16x32_bf16 v[132:135], v[80:83], v[28:31], v[132:135]\n\tds_read_b64_tr_b16 v[20:21], v220 offset:12288\n\t" \
    "ds_read_b64_tr_b16 v[22:23], v220 offset:14336\n\tv_mfma_f32_16x16x32_bf16 v[140:143], v[200:203], v[24:27], 0\n\tbuffer_load_dwordx4 a[36:39], v8, s[8:11], s68 offen offset:1024 sc1\n\tv_mfma_f32_16x16x32_bf16 v[140:143], v[88:91], v[28:31], v[140:143]\n\ts_waitcnt lgkmcnt(8)\n\tv_mfma_f32_16x16x32_bf16 v[144:147], v[192:195], v[32:35], 0\n\tds_read_b64_tr_b16 v[24:25], v221 offset:4096\n\tds_read_b64_tr_b16 v[26:27], v221 offset:6144\n\t" \
    "v_mfma_f32_16x16x32_bf16 v[144:147], v[72:75], v[36:39], v[144:147]\n\tv_mfma_f32_16x16x32_bf16 v[152:155], v[196:199], v[32:35], 0\n\tv_mfma_f32_16x16x32_bf16 v[152:155], v[80:83], v[36:39], v[152:155]\n\tds_read_b64_tr_b16 v[28:29], v221 offset:12288\n\tds_read_b64_tr_b16 v[30:31], v221 offset:14336\n\tv_mfma_f32_16x16x32_bf16 v[160:163], v[200:203], v[32:35], 0\n\tbuffer_load_dwordx4 a[40:43], v8, s[8:11], s68 offen offset:2048 sc1\n\tv_mfma_f32_16x16x32_bf16 v[160:163], v[88:91], v[36:39], v[160:163]\n\t" \
    "s_waitcnt lgkmcnt(8)\n\tv_mfma_f32_16x16x32_bf16 v[148:151], v[192:195], v[40:43], 0\n\tds_read_b64_tr_b16 v[32:33], v222 offset:4096\n\tds_read_b64_tr_b16 v[34:35], v222 offset:6144\n\tv_mfma_f32_16x16x32_bf16 v[148:151], v[72:75], v[44:47], v[148:151]\n\tbuffer_store_dwordx4 v[72:75], v8, s[8:11], s45 offen offset:0 sc1\n\tv_mfma_f32_16x16x32_bf16 v[156:159], v[196:199], v[40:43], 0\n\tv_mfma_f32_16x16x32_bf16 v[156:159], v[80:83], v[44:47], v[156:159]\n\t" \
    "ds_read_b64_tr_b16 v[36:37], v222 offset:12288\n\tds_read_b64_tr_b16 v[38:39], v222 offset:14336\n\tv_mfma_f32_16x16x32_bf16 v[164:167], v[200:203], v[40:43], 0\n\tbuffer_store_dwordx4 v[80:83], v8, s[8:11], s45 offen offset:1024 sc1\n\tv_mfma_f32_16x16x32_bf16 v[164:167], v[88:91], v[44:47], v[164:167]\n\ts_waitcnt lgkmcnt(8)\n\tv_mfma_f32_16x16x32_bf16 v[168:171], v[192:195], v[16:19], 0\n\tds_read_b64_tr_b16 v[40:41], v223 offset:4096\n\t" \
    "ds_read_b64_tr_b16 v[42:43], v223 offset:6144\n\tv_mfma_f32_16x16x32_bf16 v[168:171], v[72:75], v[20:23], v[168:171]\n\tbuffer_store_dwordx4 v[88:91], v8, s[8:11], s45 offen offset:2048 sc1\n\ts_cmp_le_u32 s4, 5\n\ts_cselect_b32 s68, 3072, 0\n\ts_add_u32 s68, s68, s46\n\tv_mfma_f32_16x16x32_bf16 v[176:179], v[196:199], v[16:19], 0\n\ts_add_u32 s68, s68, 15360\n\t" \
    "v_mfma_f32_16x16x32_bf16 v[176:179], v[80:83], v[20:23], v[176:179]\n\tds_read_b64_tr_b16 v[44:45], v223 offset:12288\n\tds_read_b64_tr_b16 v[46:47], v223 offset:14336\n\tv_mfma_f32_16x16x32_bf16 v[184:187], v[200:203], v[16:19], 0\n\tv_mfma_f32_16x16x32_bf16 v[184:187], v[88:91], v[20:23], v[184:187]\n\ts_waitcnt lgkmcnt(8)\n\tv_mfma_f32_16x16x32_bf16 v[172:175], v[192:195], v[24:27], 0\n\tds_read_b64_tr_b16 v[16:17], v220 offset:16384\n\t" \
    "ds_read_b64_tr_b16 v[18:19], v220 offset:18432\n\tv_mfma_f32_16x16x32_bf16 v[172:175], v[72:75], v[28:31], v[172:175]\n\tbuffer_load_dwordx4 a[44:47], v8, s[8:11], s68 offen offset:0 sc1\n\tv_mfma_f32_16x16x32_bf16 v[180:183], v[196:199], v[24:27], 0\n\tv_mfma_f32_16x16x32_bf16 v[180:183], v[80:83], v[28:31], v[180:183]\n\tds_read_b64_tr_b16 v[20:21], v220 offset:20480\n\tds_read_b64_tr_b16 v[22:23], v220 offset:22528\n\tv_mfma_f32_16x16x32_bf16 v[188:191], v[200:203], v[24:27], 0\n\t" \
    "v_mfma_f32_16x16x32_bf16 v[188:191], v[88:91], v[28:31], v[188:191]\n\ts_waitcnt lgkmcnt(8)\n\tv_mfma_f32_16x16x32_bf16 v[48:51], v[192:195], v[32:35], v[228:231]\n\tds_read_b64_tr_b16 v[24:25], v221 offset:16384\n\tds_read_b64_tr_b16 v[26:27], v221 offset:18432\n\tv_mfma_f32_16x16x32_bf16 v[48:51], v[72:75], v[36:39], v[48:51]\n\tbuffer_load_dwordx4 a[48:51], v8, s[8:11], s68 offen offset:1024 sc1\n\tv_mfma_f32_16x16x32_bf16 v[56:59], v[196:199], v[32:35], v[228:231]\n\t" \
    "v_mfma_f32_16x16x32_bf16 v[56:59], v[80:83], v[36:39], v[56:59]\n\tds_read_b64_tr_b16 v[28:29], v221 offset:20480\n\tds_read_b64_tr_b16 v[30:31], v221 offset:22528\n\tv_mfma_f32_16x16x32_bf16 v[64:67], v[200:203], v[32:35], v[228:231]\n\tbuffer_load_dwordx4 a[52:55], v8, s[8:11], s68 offen offset:2048 sc1\n\ts_cmp_le_u32 s4, 9\n\ts_cselect_b32 s69, 262144, 0\n\ts_add_u32 s69, s69, 2359296\n\t" \
    "v_mfma_f32_16x16x32_bf16 v[64:67], v[88:91], v[36:39], v[64:67]\n\ts_add_u32 m0, s54, 53248\n\ts_waitcnt lgkmcnt(8)\n\tv_mfma_f32_16x16x32_bf16 v[52:55], v[192:195], v[40:43], v[232:235]\n\tds_read_b64_tr_b16 v[32:33], v222 offset:16384\n\tds_read_b64_tr_b16 v[34:35], v222 offset:18432\n\tv_mfma_f32_16x16x32_bf16 v[52:55], v[72:75], v[44:47], v[52:55]\n\tv_mfma_f32_16x16x32_bf16 v[60:63], v[196:199], v[40:43], v[232:235]\n\t" \
    "v_mfma_f32_16x16x32_bf16 v[60:63], v[80:83], v[44:47], v[60:63]\n\tds_read_b64_tr_b16 v[36:37], v222 offset:20480\n\tds_read_b64_tr_b16 v[38:39], v222 offset:22528\n\tv_mfma_f32_16x16x32_bf16 v[68:71], v[200:203], v[40:43], v[232:235]\n\tbuffer_load_dwordx4 v12, s[16:19], s69 offen lds\n\ts_cmp_le_u32 s4, 6\n\ts_cselect_b32 s68, 3072, 0\n\ts_add_u32 s68, s68, s46\n\t" \
    "v_mfma_f32_16x16x32_bf16 v[68:71], v[88:91], v[44:47], v[68:71]\n\ts_add_u32 s68, s68, 18432\n\ts_barrier\n\ts_waitcnt vmcnt(16) lgkmcnt(8)\n\tv_mfma_f32_16x16x32_bf16 v[120:123], v[204:207], v[16:19], v[120:123]\n\tds_read_b64_tr_b16 v[40:41], v223 offset:16384\n\tds_read_b64_tr_b16 v[42:43], v223 offset:18432\n\tv_mfma_f32_16x16x32_bf16 v[120:123], v[216:219], v[20:23], v[120:123]\n\t" \
    "buffer_load_dwordx4 a[56:59], v8, s[8:11], s68 offen offset:0 sc1\n\tv_mfma_f32_16x16x32_bf16 v[128:131], a[0:3], v[20:23], v[128:131]\n\tv_mfma_f32_16x16x32_bf16 v[128:131], v[208:211], v[16:19], v[128:131]\n\tds_read_b64_tr_b16 v[44:45], v223 offset:20480\n\tds_read_b64_tr_b16 v[46:47], v223 offset:22528\n\tv_mfma_f32_16x16x32_bf16 v[136:139], v[212:215], v[16:19], v[136:139]\n\tbuffer_load_dwordx4 a[60:63], v8, s[8:11], s68 offen offset:1024 sc1\n\tv_mfma_f32_16x16x32_bf16 v[136:139], a[4:7], v[20:23], v[136:139]\n\t" \
    "s_waitcnt lgkmcnt(8)\n\tv_mfma_f32_16x16x32_bf16 v[124:127], v[204:207], v[24:27], v[124:127]\n\tds_read_b64_tr_b16 v[16:17], v220 offset:24576\n\tds_read_b64_tr_b16 v[18:19], v220 offset:26624\n\tv_mfma_f32_16x16x32_bf16 v[124:127], v[216:219], v[28:31], v[124:127]\n\tbuffer_load_dwordx4 a[64:67], v8, s[8:11], s68 offen offset:2048 sc1\n\ts_cmp_le_u32 s4, 7\n\ts_cselect_b32 s68, 3072, 0\n\t" \
    "s_add_u32 s68, s68, s46\n\tv_mfma_f32_16x16x32_bf16 v[132:135], a[0:3], v[28:31], v[132:135]\n\ts_add_u32 s68, s68, 21504\n\tv_mfma_f32_16x16x32_bf16 v[132:135], v[208:211], v[24:27], v[132:135]\n\tds_read_b64_tr_b16 v[20:21], v220 offset:28672\n\tds_read_b64_tr_b16 v[22:23], v220 offset:30720\n\tv_mfma_f32_16x16x32_bf16 v[140:143], v[212:215], v[24:27], v[140:143]\n\tv_mfma_f32_16x16x32_bf16 v[140:143], a[4:7], v[28:31], v[140:143]\n\t" \
    "s_waitcnt lgkmcnt(8)\n\tv_mfma_f32_16x16x32_bf16 v[144:147], v[204:207], v[32:35], v[144:147]\n\tds_read_b64_tr_b16 v[24:25], v221 offset:24576\n\tds_read_b64_tr_b16 v[26:27], v221 offset:26624\n\tv_mfma_f32_16x16x32_bf16 v[144:147], v[216:219], v[36:39], v[144:147]\n\tbuffer_load_dwordx4 a[68:71], v8, s[8:11], s68 offen offset:0 sc1\n\tv_mfma_f32_16x16x32_bf16 v[152:155], a[0:3], v[36:39], v[152:155]\n\tv_mfma_f32_16x16x32_bf16 v[152:155], v[208:211], v[32:35], v[152:155]\n\t" \
    "ds_read_b64_tr_b16 v[28:29], v221 offset:28672\n\tds_read_b64_tr_b16 v[30:31], v221 offset:30720\n\tv_mfma_f32_16x16x32_bf16 v[160:163], v[212:215], v[32:35], v[160:163]\n\tbuffer_load_dwordx4 a[72:75], v8, s[8:11], s68 offen offset:1024 sc1\n\tv_mfma_f32_16x16x32_bf16 v[160:163], a[4:7], v[36:39], v[160:163]\n\ts_waitcnt lgkmcnt(8)\n\tv_mfma_f32_16x16x32_bf16 v[148:151], v[204:207], v[40:43], v[148:151]\n\tds_read_b64_tr_b16 v[32:33], v222 offset:24576\n\t" \
    "ds_read_b64_tr_b16 v[34:35], v222 offset:26624\n\tv_mfma_f32_16x16x32_bf16 v[148:151], v[216:219], v[44:47], v[148:151]\n\tv_mfma_f32_16x16x32_bf16 v[156:159], a[0:3], v[44:47], v[156:159]\n\tv_mfma_f32_16x16x32_bf16 v[156:159], v[208:211], v[40:43], v[156:159]\n\tds_read_b64_tr_b16 v[36:37], v222 offset:28672\n\tds_read_b64_tr_b16 v[38:39], v222 offset:30720\n\tv_mfma_f32_16x16x32_bf16 v[164:167], v[212:215], v[40:43], v[164:167]\n\tbuffer_load_dwordx4 a[76:79], v8, s[8:11], s68 offen offset:2048 sc1\n\t" \
    "s_cmp_le_u32 s4, 8\n\ts_cselect_b32 s69, 262144, 0\n\ts_add_u32 s69, s69, 2097152\n\tv_mfma_f32_16x16x32_bf16 v[164:167], a[4:7], v[44:47], v[164:167]\n\ts_add_u32 m0, s54, 131072\n\ts_waitcnt lgkmcnt(8)\n\tv_mfma_f32_16x16x32_bf16 v[168:171], v[204:207], v[16:19], v[168:171]\n\tds_read_b64_tr_b16 v[40:41], v223 offset:24576\n\t" \
    "ds_read_b64_tr_b16 v[42:43], v223 offset:26624\n\tv_mfma_f32_16x16x32_bf16 v[168:171], v[216:219], v[20:23], v[168:171]\n\tbuffer_load_dwordx4 v13, s[16:19], s69 offen lds\n\ts_cmp_le_u32 s4, 9\n\ts_cselect_b32 s69, 262144, 0\n\ts_add_u32 s69, s69, 2359296\n\tv_mfma_f32_16x16x32_bf16 v[176:179], a[0:3], v[20:23], v[176:179]\n\ts_add_u32 m0, s54, 135168\n\t" \
    "v_mfma_f32_16x16x32_bf16 v[176:179], v[208:211], v[16:19], v[176:179]\n\tds_read_b64_tr_b16 v[44:45], v223 offset:28672\n\tds_read_b64_tr_b16 v[46:47], v223 offset:30720\n\tv_mfma_f32_16x16x32_bf16 v[184:187], v[212:215], v[16:19], v[184:187]\n\tbuffer_load_dwordx4 v13, s[16:19], s69 offen lds\n\ts_cmp_le_u32 s4, 10\n\ts_cselect_b32 s69, 262144, 0\n\ts_add_u32 s69, s69, 2621440\n\t" \
    "v_mfma_f32_16x16x32_bf16 v[184:187], a[4:7], v[20:23], v[184:187]\n\ts_add_u32 m0, s54, 139264\n\ts_waitcnt lgkmcnt(8)\n\tv_mfma_f32_16x16x32_bf16 v[172:175], v[204:207], v[24:27], v[172:175]\n\tds_read_b64_tr_b16 v[16:17], v4 offset:0\n\tds_read_b64_tr_b16 v[18:19], v4 offset:2048\n\tv_mfma_f32_16x16x32_bf16 v[172:175], v[216:219], v[28:31], v[172:175]\n\tv_mfma_f32_16x16x32_bf16 v[180:183], a[0:3], v[28:31], v[180:183]\n\t" \
    "v_mfma_f32_16x16x32_bf16 v[180:183], v[208:211], v[24:27], v[180:183]\n\tds_read_b64_tr_b16 v[20:21], v4 offset:4096\n\tds_read_b64_tr_b16 v[22:23], v4 offset:6144\n\tv_mfma_f32_16x16x32_bf16 v[188:191], v[212:215], v[24:27], v[188:191]\n\tbuffer_load_dwordx4 v12, s[16:19], s69 offen lds\n\ts_cmp_le_u32 s4, 11\n\ts_cselect_b32 s69, 262144, 0\n\ts_add_u32 s69, s69, 2883584\n\t" \
    "v_mfma_f32_16x16x32_bf16 v[188:191], a[4:7], v[28:31], v[188:191]\n\ts_add_u32 m0, s54, 143360\n\ts_waitcnt lgkmcnt(8)\n\tv_mfma_f32_16x16x32_bf16 v[48:51], v[204:207], v[32:35], v[48:51]\n\tds_read_b64_tr_b16 v[24:25], v5 offset:0\n\tds_read_b64_tr_b16 v[26:27], v5 offset:2048\n\tv_mfma_f32_16x16x32_bf16 v[48:51], v[216:219], v[36:39], v[48:51]\n\tbuffer_load_dwordx4 v12, s[16:19], s69 offen lds\n\t" \
    "s_cmp_le_u32 s4, 8\n\ts_cselect_b32 s68, 3072, 0\n\ts_add_u32 s68, s68, s46\n\tv_mfma_f32_16x16x32_bf16 v[56:59], a[0:3], v[36:39], v[56:59]\n\ts_add_u32 s68, s68, 24576\n\tv_mfma_f32_16x16x32_bf16 v[56:59], v[208:211], v[32:35], v[56:59]\n\tds_read_b64_tr_b16 v[28:29], v5 offset:4096\n\tds_read_b64_tr_b16 v[30:31], v5 offset:6144\n\t" \
    "v_mfma_f32_16x16x32_bf16 v[64:67], v[212:215], v[32:35], v[64:67]\n\tv_mfma_f32_16x16x32_bf16 v[64:67], a[4:7], v[36:39], v[64:67]\n\ts_waitcnt lgkmcnt(8)\n\tv_mfma_f32_16x16x32_bf16 v[52:55], v[204:207], v[40:43], v[52:55]\n\tds_read_b64_tr_b16 v[32:33], v6 offset:0\n\tds_read_b64_tr_b16 v[34:35], v6 offset:2048\n\tv_mfma_f32_16x16x32_bf16 v[52:55], v[216:219], v[44:47], v[52:55]\n\tbuffer_load_dwordx4 a[80:83], v8, s[8:11], s68 offen offset:0 sc1\n\t" \
    "v_mfma_f32_16x16x32_bf16 v[60:63], a[0:3], v[44:47], v[60:63]\n\tv_mfma_f32_16x16x32_bf16 v[60:63], v[208:211], v[40:43], v[60:63]\n\tds_read_b64_tr_b16 v[36:37], v6 offset:4096\n\tds_read_b64_tr_b16 v[38:39], v6 offset:6144\n\tv_mfma_f32_16x16x32_bf16 v[68:71], v[212:215], v[40:43], v[68:71]\n\tbuffer_load_dwordx4 a[84:87], v8, s[8:11], s68 offen offset:1024 sc1\n\tv_mfma_f32_16x16x32_bf16 v[68:71], a[4:7], v[44:47], v[68:71]\n\ts_waitcnt vmcnt(16)\n\t" \
    "s_add_u32 s73, s49, 4096\n\tbuffer_store_dwordx2 v[248:249], off, s[8:11], s73 sc1\n\ts_barrier\n\ts_waitcnt lgkmcnt(8)\n\tv_mfma_f32_16x16x32_bf16 v[120:123], a[8:11], v[16:19], v[120:123]\n\tds_read_b64_tr_b16 v[40:41], v7 offset:0\n\tds_read_b64_tr_b16 v[42:43], v7 offset:2048\n\tv_mfma_f32_16x16x32_bf16 v[120:123], a[20:23], v[20:23], v[120:123]\n\t" \
    "buffer_load_dwordx4 a[88:91], v8, s[8:11], s68 offen offset:2048 sc1\n\ts_cmp_le_u32 s4, 9\n\ts_cselect_b32 s68, 3072, 0\n\ts_add_u32 s68, s68, s46\n\tv_mfma_f32_16x16x32_bf16 v[128:131], a[24:27], v[20:23], v[128:131]\n\ts_add_u32 s68, s68, 27648\n\tv_mfma_f32_16x16x32_bf16 v[128:131], a[12:15], v[16:19], v[128:131]\n\tds_read_b64_tr_b16 v[44:45], v7 offset:4096\n\t" \
    "ds_read_b64_tr_b16 v[46:47], v7 offset:6144\n\tv_mfma_f32_16x16x32_bf16 v[136:139], a[16:19], v[16:19], v[136:139]\n\tbuffer_load_dwordx4 v[192:195], v8, s[8:11], s68 offen offset:0 sc1\n\tv_mfma_f32_16x16x32_bf16 v[136:139], a[28:31], v[20:23], v[136:139]\n\ts_waitcnt lgkmcnt(8)\n\tv_mfma_f32_16x16x32_bf16 v[124:127], a[8:11], v[24:27], v[124:127]\n\tds_read_b64_tr_b16 v[16:17], v4 offset:8192\n\tds_read_b64_tr_b16 v[18:19], v4 offset:10240\n\t" \
    "v_mfma_f32_16x16x32_bf16 v[124:127], a[20:23], v[28:31], v[124:127]\n\tbuffer_load_dwordx4 v[196:199], v8, s[8:11], s68 offen offset:1024 sc1\n\tv_mfma_f32_16x16x32_bf16 v[132:135], a[24:27], v[28:31], v[132:135]\n\tv_mfma_f32_16x16x32_bf16 v[132:135], a[12:15], v[24:27], v[132:135]\n\tds_read_b64_tr_b16 v[20:21], v4 offset:12288\n\tds_read_b64_tr_b16 v[22:23], v4 offset:14336\n\tv_mfma_f32_16x16x32_bf16 v[140:143], a[16:19], v[24:27], v[140:143]\n\tbuffer_load_dwordx4 v[200:203], v8, s[8:11], s68 offen offset:2048 sc1\n\t" \
    "s_cmp_le_u32 s4, 10\n\ts_cselect_b32 s69, 262144, 0\n\ts_add_u32 s69, s69, 2621440\n\tv_mfma_f32_16x16x32_bf16 v[140:143], a[28:31], v[28:31], v[140:143]\n\ts_add_u32 m0, s54, 147456\n\ts_waitcnt lgkmcnt(8)\n\tv_mfma_f32_16x16x32_bf16 v[144:147], a[8:11], v[32:35], v[144:147]\n\tds_read_b64_tr_b16 v[24:25], v5 offset:8192\n\t" \
    "ds_read_b64_tr_b16 v[26:27], v5 offset:10240\n\tv_mfma_f32_16x16x32_bf16 v[144:147], a[20:23], v[36:39], v[144:147]\n\tbuffer_load_dwordx4 v13, s[16:19], s69 offen lds\n\ts_cmp_le_u32 s4, 11\n\ts_cselect_b32 s69, 262144, 0\n\ts_add_u32 s69, s69, 2883584\n\tv_mfma_f32_16x16x32_bf16 v[152:155], a[24:27], v[36:39], v[152:155]\n\ts_add_u32 m0, s54, 151552\n\t" \
    "v_mfma_f32_16x16x32_bf16 v[152:155], a[12:15], v[32:35], v[152:155]\n\tds_read_b64_tr_b16 v[28:29], v5 offset:12288\n\tds_read_b64_tr_b16 v[30:31], v5 offset:14336\n\tv_mfma_f32_16x16x32_bf16 v[160:163], a[16:19], v[32:35], v[160:163]\n\tbuffer_load_dwordx4 v13, s[16:19], s69 offen lds\n\ts_cmp_le_u32 s4, 12\n\ts_cselect_b32 s69, 262144, 0\n\ts_add_u32 s69, s69, 3145728\n\t" \
    "v_mfma_f32_16x16x32_bf16 v[160:163], a[28:31], v[36:39], v[160:163]\n\ts_add_u32 m0, s54, 155648\n\ts_waitcnt lgkmcnt(8)\n\tv_mfma_f32_16x16x32_bf16 v[148:151], a[8:11], v[40:43], v[148:151]\n\tds_read_b64_tr_b16 v[32:33], v6 offset:8192\n\tds_read_b64_tr_b16 v[34:35], v6 offset:10240\n\tv_mfma_f32_16x16x32_bf16 v[148:151], a[20:23], v[44:47], v[148:151]\n\tbuffer_load_dwordx4 v12, s[16:19], s69 offen lds\n\t" \
    "s_cmp_le_u32 s4, 13\n\ts_cselect_b32 s69, 262144, 0\n\ts_add_u32 s69, s69, 3407872\n\tv_mfma_f32_16x16x32_bf16 v[156:159], a[24:27], v[44:47], v[156:159]\n\ts_add_u32 m0, s54, 159744\n\tv_mfma_f32_16x16x32_bf16 v[156:159], a[12:15], v[40:43], v[156:159]\n\tds_read_b64_tr_b16 v[36:37], v6 offset:12288\n\tds_read_b64_tr_b16 v[38:39], v6 offset:14336\n\t" \
    "v_mfma_f32_16x16x32_bf16 v[164:167], a[16:19], v[40:43], v[164:167]\n\tv_mfma_f32_16x16x32_bf16 v[164:167], a[28:31], v[44:47], v[164:167]\n\ts_waitcnt lgkmcnt(8)\n\tv_mfma_f32_16x16x32_bf16 v[168:171], a[8:11], v[16:19], v[168:171]\n\tds_read_b64_tr_b16 v[40:41], v7 offset:8192\n\tds_read_b64_tr_b16 v[42:43], v7 offset:10240\n\tv_mfma_f32_16x16x32_bf16 v[168:171], a[20:23], v[20:23], v[168:171]\n\tbuffer_load_dwordx4 v12, s[16:19], s69 offen lds\n\t" \
    "s_cmp_le_u32 s4, 10\n\ts_cselect_b32 s68, 3072, 0\n\ts_add_u32 s68, s68, s46\n\tv_mfma_f32_16x16x32_bf16 v[176:179], a[24:27], v[20:23], v[176:179]\n\ts_add_u32 s68, s68, 30720\n\tv_mfma_f32_16x16x32_bf16 v[176:179], a[12:15], v[16:19], v[176:179]\n\tds_read_b64_tr_b16 v[44:45], v7 offset:12288\n\tds_read_b64_tr_b16 v[46:47], v7 offset:14336\n\t" \
    "v_mfma_f32_16x16x32_bf16 v[184:187], a[16:19], v[16:19], v[184:187]\n\tbuffer_load_dwordx4 v[216:219], v8, s[8:11], s68 offen offset:0 sc1\n\tv_mfma_f32_16x16x32_bf16 v[184:187], a[28:31], v[20:23], v[184:187]\n\ts_waitcnt lgkmcnt(8)\n\tv_mfma_f32_16x16x32_bf16 v[172:175], a[8:11], v[24:27], v[172:175]\n\tds_read_b64_tr_b16 v[16:17], v4 offset:16384\n\tds_read_b64_tr_b16 v[18:19], v4 offset:18432\n\tv_mfma_f32_16x16x32_bf16 v[172:175], a[20:23], v[28:31], v[172:175]\n\t" \
    "buffer_load_dwordx4 v[204:207], v8, s[8:11], s68 offen offset:1024 sc1\n\tv_mfma_f32_16x16x32_bf16 v[180:183], a[24:27], v[28:31], v[180:183]\n\tv_mfma_f32_16x16x32_bf16 v[180:183], a[12:15], v[24:27], v[180:183]\n\tds_read_b64_tr_b16 v[20:21], v4 offset:20480\n\tds_read_b64_tr_b16 v[22:23], v4 offset:22528\n\tv_mfma_f32_16x16x32_bf16 v[188:191], a[16:19], v[24:27], v[188:191]\n\tv_mfma_f32_16x16x32_bf16 v[188:191], a[28:31], v[28:31], v[188:191]\n\ts_waitcnt lgkmcnt(8)\n\t" \
    "v_mfma_f32_16x16x32_bf16 v[48:51], a[8:11], v[32:35], v[48:51]\n\tds_read_b64_tr_b16 v[24:25], v5 offset:16384\n\tds_read_b64_tr_b16 v[26:27], v5 offset:18432\n\tv_mfma_f32_16x16x32_bf16 v[48:51], a[20:23], v[36:39], v[48:51]\n\tbuffer_load_dwordx4 v[208:211], v8, s[8:11], s68 offen offset:2048 sc1\n\ts_cmp_le_u32 s4, 11\n\ts_cselect_b32 s68, 3072, 0\n\ts_add_u32 s68, s68, s46\n\t" \
    "v_mfma_f32_16x16x32_bf16 v[56:59], a[24:27], v[36:39], v[56:59]\n\ts_add_u32 s68, s68, 33792\n\tv_mfma_f32_16x16x32_bf16 v[56:59], a[12:15], v[32:35], v[56:59]\n\tds_read_b64_tr_b16 v[28:29], v5 offset:20480\n\tds_read_b64_tr_b16 v[30:31], v5 offset:22528\n\tv_mfma_f32_16x16x32_bf16 v[64:67], a[16:19], v[32:35], v[64:67]\n\tbuffer_load_dwordx4 v[212:215], v8, s[8:11], s68 offen offset:0 sc1\n\tv_mfma_f32_16x16x32_bf16 v[64:67], a[28:31], v[36:39], v[64:67]\n\t" \
    "s_waitcnt lgkmcnt(8)\n\tv_mfma_f32_16x16x32_bf16 v[52:55], a[8:11], v[40:43], v[52:55]\n\tds_read_b64_tr_b16 v[32:33], v6 offset:16384\n\tds_read_b64_tr_b16 v[34:35], v6 offset:18432\n\tv_mfma_f32_16x16x32_bf16 v[52:55], a[20:23], v[44:47], v[52:55]\n\tbuffer_load_dwordx4 v[72:75], v8, s[8:11], s68 offen offset:1024 sc1\n\tv_mfma_f32_16x16x32_bf16 v[60:63], a[24:27], v[44:47], v[60:63]\n\tv_mfma_f32_16x16x32_bf16 v[60:63], a[12:15], v[40:43], v[60:63]\n\t" \
    "ds_read_b64_tr_b16 v[36:37], v6 offset:20480\n\tds_read_b64_tr_b16 v[38:39], v6 offset:22528\n\tv_mfma_f32_16x16x32_bf16 v[68:71], a[16:19], v[40:43], v[68:71]\n\tv_mfma_f32_16x16x32_bf16 v[68:71], a[28:31], v[44:47], v[68:71]\n\ts_barrier\n\ts_waitcnt vmcnt(27) lgkmcnt(8)\n\tv_mfma_f32_16x16x32_bf16 v[120:123], a[32:35], v[16:19], v[120:123]\n\tds_read_b64_tr_b16 v[40:41], v7 offset:16384\n\t" \
    "ds_read_b64_tr_b16 v[42:43], v7 offset:18432\n\tv_mfma_f32_16x16x32_bf16 v[120:123], a[44:47], v[20:23], v[120:123]\n\tbuffer_load_dwordx4 v[76:79], v8, s[8:11], s68 offen offset:2048 sc1\n\ts_cmp_le_u32 s4, 12\n\ts_cselect_b32 s69, 262144, 0\n\ts_add_u32 s69, s69, 3145728\n\tv_mfma_f32_16x16x32_bf16 v[128:131], a[48:51], v[20:23], v[128:131]\n\ts_add_u32 m0, s54, 57344\n\t" \
    "v_mfma_f32_16x16x32_bf16 v[128:131], a[36:39], v[16:19], v[128:131]\n\tds_read_b64_tr_b16 v[44:45], v7 offset:20480\n\tds_read_b64_tr_b16 v[46:47], v7 offset:22528\n\tv_mfma_f32_16x16x32_bf16 v[136:139], a[40:43], v[16:19], v[136:139]\n\tbuffer_load_dwordx4 v13, s[16:19], s69 offen lds\n\ts_cmp_le_u32 s4, 13\n\ts_cselect_b32 s69, 262144, 0\n\ts_add_u32 s69, s69, 3407872\n\t" \
    "v_mfma_f32_16x16x32_bf16 v[136:139], a[52:55], v[20:23], v[136:139]\n\ts_add_u32 m0, s54, 61440\n\ts_waitcnt lgkmcnt(8)\n\tv_mfma_f32_16x16x32_bf16 v[124:127], a[32:35], v[24:27], v[124:127]\n\tds_read_b64_tr_b16 v[16:17], v4 offset:24576\n\tds_read_b64_tr_b16 v[18:19], v4 offset:26624\n\tv_mfma_f32_16x16x32_bf16 v[124:127], a[44:47], v[28:31], v[124:127]\n\tbuffer_load_dwordx4 v13, s[16:19], s69 offen lds\n\t" \
    "s_cmp_le_u32 s4, 14\n\ts_cselect_b32 s69, 262144, 0\n\ts_add_u32 s69, s69, 3670016\n\tv_mfma_f32_16x16x32_bf16 v[132:135], a[48:51], v[28:31], v[132:135]\n\ts_add_u32 m0, s54, 65536\n\tv_mfma_f32_16x16x32_bf16 v[132:135], a[36:39], v[24:27], v[132:135]\n\tds_read_b64_tr_b16 v[20:21], v4 offset:28672\n\tds_read_b64_tr_b16 v[22:23], v4 offset:30720\n\t" \
    "v_mfma_f32_16x16x32_bf16 v[140:143], a[40:43], v[24:27], v[140:143]\n\tbuffer_load_dwordx4 v12, s[16:19], s69 offen lds\n\ts_cmp_le_u32 s4, 15\n\ts_cselect_b32 s69, 262144, 0\n\ts_add_u32 s69, s69, 3932160\n\tv_mfma_f32_16x16x32_bf16 v[140:143], a[52:55], v[28:31], v[140:143]\n\ts_add_u32 m0, s54, 69632\n\ts_waitcnt lgkmcnt(8)\n\t" \
    "v_mfma_f32_16x16x32_bf16 v[144:147], a[32:35], v[32:35], v[144:147]\n\tds_read_b64_tr_b16 v[24:25], v5 offset:24576\n\tds_read_b64_tr_b16 v[26:27], v5 offset:26624\n\tv_mfma_f32_16x16x32_bf16 v[144:147], a[44:47], v[36:39], v[144:147]\n\tv_mfma_f32_16x16x32_bf16 v[152:155], a[48:51], v[36:39], v[152:155]\n\tv_mfma_f32_16x16x32_bf16 v[152:155], a[36:39], v[32:35], v[152:155]\n\tds_read_b64_tr_b16 v[28:29], v5 offset:28672\n\tds_read_b64_tr_b16 v[30:31], v5 offset:30720\n\t" \
    "v_mfma_f32_16x16x32_bf16 v[160:163], a[40:43], v[32:35], v[160:163]\n\tbuffer_load_dwordx4 v12, s[16:19], s69 offen lds\n\ts_cmp_le_u32 s4, 12\n\ts_cselect_b32 s68, 3072, 0\n\ts_add_u32 s68, s68, s46\n\tv_mfma_f32_16x16x32_bf16 v[160:163], a[52:55], v[36:39], v[160:163]\n\ts_add_u32 s68, s68, 36864\n\ts_waitcnt lgkmcnt(8)\n\t" \
    "v_mfma_f32_16x16x32_bf16 v[148:151], a[32:35], v[40:43], v[148:151]\n\tds_read_b64_tr_b16 v[32:33], v6 offset:24576\n\tds_read_b64_tr_b16 v[34:35], v6 offset:26624\n\tv_mfma_f32_16x16x32_bf16 v[148:151], a[44:47], v[44:47], v[148:151]\n\tbuffer_load_dwordx4 v[80:83], v8, s[8:11], s68 offen offset:0 sc1\n\tv_mfma_f32_16x16x32_bf16 v[156:159], a[48:51], v[44:47], v[156:159]\n\tv_mfma_f32_16x16x32_bf16 v[156:159], a[36:39], v[40:43], v[156:159]\n\tds_read_b64_tr_b16 v[36:37], v6 offset:28672\n\t" \
    "ds_read_b64_tr_b16 v[38:39], v6 offset:30720\n\tv_mfma_f32_16x16x32_bf16 v[164:167], a[40:43], v[40:43], v[164:167]\n\tv_mfma_f32_16x16x32_bf16 v[164:167], a[52:55], v[44:47], v[164:167]\n\ts_waitcnt lgkmcnt(8)\n\tv_mfma_f32_16x16x32_bf16 v[168:171], a[32:35], v[16:19], v[168:171]\n\tds_read_b64_tr_b16 v[40:41], v7 offset:24576\n\tds_read_b64_tr_b16 v[42:43], v7 offset:26624\n\tv_mfma_f32_16x16x32_bf16 v[168:171], a[44:47], v[20:23], v[168:171]\n\t" \
    "buffer_load_dwordx4 v[84:87], v8, s[8:11], s68 offen offset:1024 sc1\n\tv_mfma_f32_16x16x32_bf16 v[176:179], a[48:51], v[20:23], v[176:179]\n\tv_mfma_f32_16x16x32_bf16 v[176:179], a[36:39], v[16:19], v[176:179]\n\tds_read_b64_tr_b16 v[44:45], v7 offset:28672\n\tds_read_b64_tr_b16 v[46:47], v7 offset:30720\n\tv_mfma_f32_16x16x32_bf16 v[184:187], a[40:43], v[16:19], v[184:187]\n\tbuffer_load_dwordx4 v[88:91], v8, s[8:11], s68 offen offset:2048 sc1\n\ts_cmp_le_u32 s4, 13\n\t" \
    "s_cselect_b32 s68, 3072, 0\n\ts_add_u32 s68, s68, s46\n\tv_mfma_f32_16x16x32_bf16 v[184:187], a[52:55], v[20:23], v[184:187]\n\ts_add_u32 s68, s68, 39936\n\ts_waitcnt lgkmcnt(8)\n\tv_mfma_f32_16x16x32_bf16 v[172:175], a[32:35], v[24:27], v[172:175]\n\tds_read_b64_tr_b16 v[16:17], v4 offset:32768\n\tds_read_b64_tr_b16 v[18:19], v4 offset:34816\n\t" \
    "v_mfma_f32_16x16x32_bf16 v[172:175], a[44:47], v[28:31], v[172:175]\n\tbuffer_load_dwordx4 v[92:95], v8, s[8:11], s68 offen offset:0 sc1\n\tv_mfma_f32_16x16x32_bf16 v[180:183], a[48:51], v[28:31], v[180:183]\n\tv_mfma_f32_16x16x32_bf16 v[180:183], a[36:39], v[24:27], v[180:183]\n\tds_read_b64_tr_b16 v[20:21], v4 offset:36864\n\tds_read_b64_tr_b16 v[22:23], v4 offset:38912\n\tv_mfma_f32_16x16x32_bf16 v[188:191], a[40:43], v[24:27], v[188:191]\n\tv_mfma_f32_16x16x32_bf16 v[188:191], a[52:55], v[28:31], v[188:191]\n\t" \
    "s_waitcnt lgkmcnt(8)\n\tv_mfma_f32_16x16x32_bf16 v[48:51], a[32:35], v[32:35], v[48:51]\n\tds_read_b64_tr_b16 v[24:25], v5 offset:32768\n\tds_read_b64_tr_b16 v[26:27], v5 offset:34816\n\tv_mfma_f32_16x16x32_bf16 v[48:51], a[44:47], v[36:39], v[48:51]\n\tbuffer_load_dwordx4 v[96:99], v8, s[8:11], s68 offen offset:1024 sc1\n\tv_mfma_f32_16x16x32_bf16 v[56:59], a[48:51], v[36:39], v[56:59]\n\tv_mfma_f32_16x16x32_bf16 v[56:59], a[36:39], v[32:35], v[56:59]\n\t" \
    "ds_read_b64_tr_b16 v[28:29], v5 offset:36864\n\tds_read_b64_tr_b16 v[30:31], v5 offset:38912\n\tv_mfma_f32_16x16x32_bf16 v[64:67], a[40:43], v[32:35], v[64:67]\n\tbuffer_load_dwordx4 v[100:103], v8, s[8:11], s68 offen offset:2048 sc1\n\ts_cmp_le_u32 s4, 14\n\ts_cselect_b32 s69, 262144, 0\n\ts_add_u32 s69, s69, 3670016\n\tv_mfma_f32_16x16x32_bf16 v[64:67], a[52:55], v[36:39], v[64:67]\n\t" \
    "s_add_u32 m0, s54, 73728\n\ts_waitcnt lgkmcnt(8)\n\tv_mfma_f32_16x16x32_bf16 v[52:55], a[32:35], v[40:43], v[52:55]\n\tds_read_b64_tr_b16 v[32:33], v6 offset:32768\n\tds_read_b64_tr_b16 v[34:35], v6 offset:34816\n\tv_mfma_f32_16x16x32_bf16 v[52:55], a[44:47], v[44:47], v[52:55]\n\tv_mfma_f32_16x16x32_bf16 v[60:63], a[48:51], v[44:47], v[60:63]\n\tv_mfma_f32_16x16x32_bf16 v[60:63], a[36:39], v[40:43], v[60:63]\n\t" \
    "ds_read_b64_tr_b16 v[36:37], v6 offset:36864\n\tds_read_b64_tr_b16 v[38:39], v6 offset:38912\n\tv_mfma_f32_16x16x32_bf16 v[68:71], a[40:43], v[40:43], v[68:71]\n\tbuffer_load_dwordx4 v13, s[16:19], s69 offen lds\n\ts_cmp_le_u32 s4, 15\n\ts_cselect_b32 s69, 262144, 0\n\ts_add_u32 s69, s69, 3932160\n\tv_mfma_f32_16x16x32_bf16 v[68:71], a[52:55], v[44:47], v[68:71]\n\t" \
    "s_add_u32 m0, s54, 77824\n\ts_waitcnt vmcnt(38)\n\ts_barrier\n\ts_waitcnt vmcnt(32) lgkmcnt(8)\n\tv_mfma_f32_16x16x32_bf16 v[120:123], a[56:59], v[16:19], v[120:123]\n\tds_read_b64_tr_b16 v[40:41], v7 offset:32768\n\tds_read_b64_tr_b16 v[42:43], v7 offset:34816\n\tv_mfma_f32_16x16x32_bf16 v[120:123], a[68:71], v[20:23], v[120:123]\n\t" \
    "buffer_load_dwordx4 v13, s[16:19], s69 offen lds\n\ts_cmp_le_u32 s4, 16\n\ts_cselect_b32 s69, 262144, 0\n\ts_add_u32 s69, s69, 4194304\n\tv_mfma_f32_16x16x32_bf16 v[128:131], a[72:75], v[20:23], v[128:131]\n\ts_add_u32 m0, s54, 81920\n\tv_mfma_f32_16x16x32_bf16 v[128:131], a[60:63], v[16:19], v[128:131]\n\tds_read_b64_tr_b16 v[44:45], v7 offset:36864\n\t" \
    "ds_read_b64_tr_b16 v[46:47], v7 offset:38912\n\tv_mfma_f32_16x16x32_bf16 v[136:139], a[64:67], v[16:19], v[136:139]\n\tbuffer_load_dwordx4 v12, s[16:19], s69 offen lds\n\ts_cmp_le_u32 s4, 0\n\ts_cselect_b32 s69, 262144, 0\n\ts_add_u32 s69, s69, 131072\n\tv_mfma_f32_16x16x32_bf16 v[136:139], a[76:79], v[20:23], v[136:139]\n\ts_add_u32 m0, s54, 86016\n\t" \
    "s_waitcnt lgkmcnt(8)\n\tv_mfma_f32_16x16x32_bf16 v[124:127], a[56:59], v[24:27], v[124:127]\n\tds_read_b64_tr_b16 v[16:17], v4 offset:40960\n\tds_read_b64_tr_b16 v[18:19], v4 offset:43008\n\tv_mfma_f32_16x16x32_bf16 v[124:127], a[68:71], v[28:31], v[124:127]\n\tbuffer_load_dwordx4 v12, s[16:19], s69 offen lds\n\ts_cmp_le_u32 s4, 16\n\ts_cselect_b32 s69, 262144, 0\n\t" \
    "s_add_u32 s69, s69, 4194304\n\tv_mfma_f32_16x16x32_bf16 v[132:135], a[72:75], v[28:31], v[132:135]\n\ts_add_u32 m0, s54, 90112\n\tv_mfma_f32_16x16x32_bf16 v[132:135], a[60:63], v[24:27], v[132:135]\n\tds_read_b64_tr_b16 v[20:21], v4 offset:45056\n\tds_read_b64_tr_b16 v[22:23], v4 offset:47104\n\tv_mfma_f32_16x16x32_bf16 v[140:143], a[64:67], v[24:27], v[140:143]\n\tbuffer_load_dwordx4 v13, s[16:19], s69 offen lds\n\t" \
    "s_cmp_le_u32 s4, 0\n\ts_cselect_b32 s69, 262144, 0\n\ts_add_u32 s69, s69, 131072\n\tv_mfma_f32_16x16x32_bf16 v[140:143], a[76:79], v[28:31], v[140:143]\n\ts_add_u32 m0, s54, 94208\n\ts_waitcnt lgkmcnt(8)\n\tv_mfma_f32_16x16x32_bf16 v[144:147], a[56:59], v[32:35], v[144:147]\n\tds_read_b64_tr_b16 v[24:25], v5 offset:40960\n\t" \
    "ds_read_b64_tr_b16 v[26:27], v5 offset:43008\n\tv_mfma_f32_16x16x32_bf16 v[144:147], a[68:71], v[36:39], v[144:147]\n\tv_mfma_f32_16x16x32_bf16 v[152:155], a[72:75], v[36:39], v[152:155]\n\tv_mfma_f32_16x16x32_bf16 v[152:155], a[60:63], v[32:35], v[152:155]\n\tds_read_b64_tr_b16 v[28:29], v5 offset:45056\n\tds_read_b64_tr_b16 v[30:31], v5 offset:47104\n\tv_mfma_f32_16x16x32_bf16 v[160:163], a[64:67], v[32:35], v[160:163]\n\tbuffer_load_dwordx4 v13, s[16:19], s69 offen lds\n\t" \
    "s_cmp_le_u32 s4, 14\n\ts_cselect_b32 s68, 3072, 0\n\ts_add_u32 s68, s68, s46\n\tv_mfma_f32_16x16x32_bf16 v[160:163], a[76:79], v[36:39], v[160:163]\n\ts_add_u32 s68, s68, 43008\n\ts_waitcnt lgkmcnt(8)\n\tv_mfma_f32_16x16x32_bf16 v[148:151], a[56:59], v[40:43], v[148:151]\n\tds_read_b64_tr_b16 v[32:33], v6 offset:40960\n\t" \
    "ds_read_b64_tr_b16 v[34:35], v6 offset:43008\n\tv_mfma_f32_16x16x32_bf16 v[148:151], a[68:71], v[44:47], v[148:151]\n\tbuffer_load_dwordx4 v[104:107], v8, s[8:11], s68 offen offset:0 sc1\n\tv_mfma_f32_16x16x32_bf16 v[156:159], a[72:75], v[44:47], v[156:159]\n\tv_mfma_f32_16x16x32_bf16 v[156:159], a[60:63], v[40:43], v[156:159]\n\tds_read_b64_tr_b16 v[36:37], v6 offset:45056\n\tds_read_b64_tr_b16 v[38:39], v6 offset:47104\n\tv_mfma_f32_16x16x32_bf16 v[164:167], a[64:67], v[40:43], v[164:167]\n\t" \
    "v_mfma_f32_16x16x32_bf16 v[164:167], a[76:79], v[44:47], v[164:167]\n\ts_waitcnt lgkmcnt(8)\n\tv_mfma_f32_16x16x32_bf16 v[168:171], a[56:59], v[16:19], v[168:171]\n\tds_read_b64_tr_b16 v[40:41], v7 offset:40960\n\tds_read_b64_tr_b16 v[42:43], v7 offset:43008\n\tv_mfma_f32_16x16x32_bf16 v[168:171], a[68:71], v[20:23], v[168:171]\n\tbuffer_load_dwordx4 v[108:111], v8, s[8:11], s68 offen offset:1024 sc1\n\tv_mfma_f32_16x16x32_bf16 v[176:179], a[72:75], v[20:23], v[176:179]\n\t" \
    "v_mfma_f32_16x16x32_bf16 v[176:179], a[60:63], v[16:19], v[176:179]\n\tds_read_b64_tr_b16 v[44:45], v7 offset:45056\n\tds_read_b64_tr_b16 v[46:47], v7 offset:47104\n\tv_mfma_f32_16x16x32_bf16 v[184:187], a[64:67], v[16:19], v[184:187]\n\tbuffer_load_dwordx4 v[112:115], v8, s[8:11], s68 offen offset:2048 sc1\n\ts_cmp_le_u32 s4, 15\n\ts_cselect_b32 s68, 3072, 0\n\ts_add_u32 s68, s68, s46\n\t" \
    "v_mfma_f32_16x16x32_bf16 v[184:187], a[76:79], v[20:23], v[184:187]\n\ts_add_u32 s68, s68, 46080\n\ts_waitcnt lgkmcnt(8)\n\tv_mfma_f32_16x16x32_bf16 v[172:175], a[56:59], v[24:27], v[172:175]\n\tds_read_b64_tr_b16 v[16:17], v4 offset:49152\n\tds_read_b64_tr_b16 v[18:19], v4 offset:51200\n\tv_mfma_f32_16x16x32_bf16 v[172:175], a[68:71], v[28:31], v[172:175]\n\tv_mfma_f32_16x16x32_bf16 v[180:183], a[72:75], v[28:31], v[180:183]\n\t" \
    "v_mfma_f32_16x16x32_bf16 v[180:183], a[60:63], v[24:27], v[180:183]\n\tds_read_b64_tr_b16 v[20:21], v4 offset:53248\n\tds_read_b64_tr_b16 v[22:23], v4 offset:55296\n\tv_mfma_f32_16x16x32_bf16 v[188:191], a[64:67], v[24:27], v[188:191]\n\tbuffer_load_dwordx4 v[116:119], v8, s[8:11], s68 offen offset:0 sc1\n\tv_mfma_f32_16x16x32_bf16 v[188:191], a[76:79], v[28:31], v[188:191]\n\ts_waitcnt lgkmcnt(8)\n\tv_mfma_f32_16x16x32_bf16 v[48:51], a[56:59], v[32:35], v[48:51]\n\t" \
    "ds_read_b64_tr_b16 v[24:25], v5 offset:49152\n\tds_read_b64_tr_b16 v[26:27], v5 offset:51200\n\tv_mfma_f32_16x16x32_bf16 v[48:51], a[68:71], v[36:39], v[48:51]\n\tbuffer_load_dwordx4 a[0:3], v8, s[8:11], s68 offen offset:1024 sc1\n\tv_mfma_f32_16x16x32_bf16 v[56:59], a[72:75], v[36:39], v[56:59]\n\tv_mfma_f32_16x16x32_bf16 v[56:59], a[60:63], v[32:35], v[56:59]\n\tds_read_b64_tr_b16 v[28:29], v5 offset:53248\n\tds_read_b64_tr_b16 v[30:31], v5 offset:55296\n\t" \
    "v_mfma_f32_16x16x32_bf16 v[64:67], a[64:67], v[32:35], v[64:67]\n\tv_mfma_f32_16x16x32_bf16 v[64:67], a[76:79], v[36:39], v[64:67]\n\ts_add_u32 s50, s50, 4096\n\tbuffer_load_dwordx2 v[250:251], v241, s[8:11], s50 offen sc1\n\ts_waitcnt lgkmcnt(8)\n\tv_mfma_f32_16x16x32_bf16 v[52:55], a[56:59], v[40:43], v[52:55]\n\tds_read_b64_tr_b16 v[32:33], v6 offset:49152\n\tds_read_b64_tr_b16 v[34:35], v6 offset:51200\n\t" \
    "v_mfma_f32_16x16x32_bf16 v[52:55], a[68:71], v[44:47], v[52:55]\n\tv_mfma_f32_16x16x32_bf16 v[60:63], a[72:75], v[44:47], v[60:63]\n\tv_mfma_f32_16x16x32_bf16 v[60:63], a[60:63], v[40:43], v[60:63]\n\tds_read_b64_tr_b16 v[36:37], v6 offset:53248\n\tds_read_b64_tr_b16 v[38:39], v6 offset:55296\n\tv_mfma_f32_16x16x32_bf16 v[68:71], a[64:67], v[40:43], v[68:71]\n\tbuffer_load_dwordx4 a[4:7], v8, s[8:11], s68 offen offset:2048 sc1\n\ts_cmp_le_u32 s4, 16\n\t" \
    "s_cselect_b32 s68, 3072, 0\n\ts_add_u32 s68, s68, s46\n\tv_mfma_f32_16x16x32_bf16 v[68:71], a[76:79], v[44:47], v[68:71]\n\ts_add_u32 s68, s68, 49152\n\ts_waitcnt vmcnt(40)\n\ts_barrier\n\ts_waitcnt vmcnt(33) lgkmcnt(8)\n\tv_mfma_f32_16x16x32_bf16 v[120:123], a[80:83], v[16:19], v[120:123]\n\t" \
    "ds_read_b64_tr_b16 v[40:41], v7 offset:49152\n\tds_read_b64_tr_b16 v[42:43], v7 offset:51200\n\tv_mfma_f32_16x16x32_bf16 v[120:123], v[192:195], v[20:23], v[120:123]\n\tbuffer_load_dwordx4 a[8:11], v8, s[8:11], s68 offen offset:0 sc1\n\tv_mfma_f32_16x16x32_bf16 v[128:131], v[196:199], v[20:23], v[128:131]\n\tv_mfma_f32_16x16x32_bf16 v[128:131], a[84:87], v[16:19], v[128:131]\n\tds_read_b64_tr_b16 v[44:45], v7 offset:53248\n\tds_read_b64_tr_b16 v[46:47], v7 offset:55296\n\t" \
    "v_mfma_f32_16x16x32_bf16 v[136:139], a[88:91], v[16:19], v[136:139]\n\tbuffer_load_dwordx4 a[12:15], v8, s[8:11], s68 offen offset:1024 sc1\n\tv_mfma_f32_16x16x32_bf16 v[136:139], v[200:203], v[20:23], v[136:139]\n\ts_waitcnt lgkmcnt(8)\n\tv_mfma_f32_16x16x32_bf16 v[124:127], a[80:83], v[24:27], v[124:127]\n\tds_read_b64_tr_b16 v[16:17], v220 offset:0\n\tds_read_b64_tr_b16 v[18:19], v220 offset:2048\n\tv_mfma_f32_16x16x32_bf16 v[124:127], v[192:195], v[28:31], v[124:127]\n\t" \
    "v_mfma_f32_16x16x32_bf16 v[132:135], v[196:199], v[28:31], v[132:135]\n\tv_mfma_f32_16x16x32_bf16 v[132:135], a[84:87], v[24:27], v[132:135]\n\tds_read_b64_tr_b16 v[20:21], v220 offset:4096\n\tds_read_b64_tr_b16 v[22:23], v220 offset:6144\n\tv_mfma_f32_16x16x32_bf16 v[140:143], a[88:91], v[24:27], v[140:143]\n\tbuffer_load_dwordx4 a[16:19], v8, s[8:11], s68 offen offset:2048 sc1\n\ts_cmp_le_u32 s4, 1\n\ts_cselect_b32 s69, 262144, 0\n\t" \
    "s_add_u32 s69, s69, 393216\n\tv_mfma_f32_16x16x32_bf16 v[140:143], v[200:203], v[28:31], v[140:143]\n\ts_add_u32 m0, s54, 98304\n\ts_waitcnt lgkmcnt(8)\n\tv_mfma_f32_16x16x32_bf16 v[144:147], a[80:83], v[32:35], v[144:147]\n\tds_read_b64_tr_b16 v[24:25], v221 offset:0\n\tds_read_b64_tr_b16 v[26:27], v221 offset:2048\n\tv_mfma_f32_16x16x32_bf16 v[144:147], v[192:195], v[36:39], v[144:147]\n\t" \
    "buffer_load_dwordx4 v12, s[16:19], s69 offen lds\n\ts_cmp_le_u32 s4, 2\n\ts_cselect_b32 s69, 262144, 0\n\ts_add_u32 s69, s69, 655360\n\tv_mfma_f32_16x16x32_bf16 v[152:155], v[196:199], v[36:39], v[152:155]\n\ts_add_u32 m0, s54, 102400\n\tv_mfma_f32_16x16x32_bf16 v[152:155], a[84:87], v[32:35], v[152:155]\n\tds_read_b64_tr_b16 v[28:29], v221 offset:4096\n\t" \
    "ds_read_b64_tr_b16 v[30:31], v221 offset:6144\n\tv_mfma_f32_16x16x32_bf16 v[160:163], a[88:91], v[32:35], v[160:163]\n\tbuffer_load_dwordx4 v12, s[16:19], s69 offen lds\n\ts_cmp_le_u32 s4, 1\n\ts_cselect_b32 s69, 262144, 0\n\ts_add_u32 s69, s69, 393216\n\tv_mfma_f32_16x16x32_bf16 v[160:163], v[200:203], v[36:39], v[160:163]\n\ts_add_u32 m0, s54, 106496\n\t" \
    "s_waitcnt lgkmcnt(8)\n\tv_mfma_f32_16x16x32_bf16 v[148:151], a[80:83], v[40:43], v[148:151]\n\tds_read_b64_tr_b16 v[32:33], v222 offset:0\n\tds_read_b64_tr_b16 v[34:35], v222 offset:2048\n\tv_mfma_f32_16x16x32_bf16 v[148:151], v[192:195], v[44:47], v[148:151]\n\tv_mfma_f32_16x16x32_bf16 v[156:159], v[196:199], v[44:47], v[156:159]\n\tv_mfma_f32_16x16x32_bf16 v[156:159], a[84:87], v[40:43], v[156:159]\n\tds_read_b64_tr_b16 v[36:37], v222 offset:4096\n\t" \
    "ds_read_b64_tr_b16 v[38:39], v222 offset:6144\n\tv_mfma_f32_16x16x32_bf16 v[164:167], a[88:91], v[40:43], v[164:167]\n\tbuffer_load_dwordx4 v13, s[16:19], s69 offen lds\n\ts_cmp_le_u32 s4, 2\n\ts_cselect_b32 s69, 262144, 0\n\ts_add_u32 s69, s69, 655360\n\tv_mfma_f32_16x16x32_bf16 v[164:167], v[200:203], v[44:47], v[164:167]\n\ts_add_u32 m0, s54, 110592\n\t" \
    "s_waitcnt lgkmcnt(8)\n\tv_mfma_f32_16x16x32_bf16 v[168:171], a[80:83], v[16:19], v[168:171]\n\tds_read_b64_tr_b16 v[40:41], v223 offset:0\n\tds_read_b64_tr_b16 v[42:43], v223 offset:2048\n\tv_mfma_f32_16x16x32_bf16 v[168:171], v[192:195], v[20:23], v[168:171]\n\tbuffer_load_dwordx4 v13, s[16:19], s69 offen lds\n\ts_cmp_le_u32 s4, 3\n\ts_cselect_b32 s69, 262144, 0\n\t" \
    "s_add_u32 s69, s69, 917504\n\tv_mfma_f32_16x16x32_bf16 v[176:179], v[196:199], v[20:23], v[176:179]\n\ts_add_u32 m0, s54, 114688\n\tv_mfma_f32_16x16x32_bf16 v[176:179], a[84:87], v[16:19], v[176:179]\n\tds_read_b64_tr_b16 v[44:45], v223 offset:4096\n\tds_read_b64_tr_b16 v[46:47], v223 offset:6144\n\tv_mfma_f32_16x16x32_bf16 v[184:187], a[88:91], v[16:19], v[184:187]\n\tbuffer_load_dwordx4 v12, s[16:19], s69 offen lds\n\t" \
    "s_cmp_le_u32 s4, 4\n\ts_cselect_b32 s69, 262144, 0\n\ts_add_u32 s69, s69, 1179648\n\tv_mfma_f32_16x16x32_bf16 v[184:187], v[200:203], v[20:23], v[184:187]\n\ts_add_u32 m0, s54, 118784\n\ts_waitcnt lgkmcnt(8)\n\tv_mfma_f32_16x16x32_bf16 v[172:175], a[80:83], v[24:27], v[172:175]\n\tds_read_b64_tr_b16 v[16:17], v220 offset:8192\n\t" \
    "ds_read_b64_tr_b16 v[18:19], v220 offset:10240\n\tv_mfma_f32_16x16x32_bf16 v[172:175], v[192:195], v[28:31], v[172:175]\n\tv_mfma_f32_16x16x32_bf16 v[180:183], v[196:199], v[28:31], v[180:183]\n\tv_mfma_f32_16x16x32_bf16 v[180:183], a[84:87], v[24:27], v[180:183]\n\tds_read_b64_tr_b16 v[20:21], v220 offset:12288\n\tds_read_b64_tr_b16 v[22:23], v220 offset:14336\n\tv_mfma_f32_16x16x32_bf16 v[188:191], a[88:91], v[24:27], v[188:191]\n\tbuffer_load_dwordx4 v12, s[16:19], s69 offen lds\n\t" \
    "s_cmp_le_u32 s4, 3\n\ts_cselect_b32 s69, 262144, 0\n\ts_add_u32 s69, s69, 917504\n\tv_mfma_f32_16x16x32_bf16 v[188:191], v[200:203], v[28:31], v[188:191]\n\ts_add_u32 m0, s54, 122880\n\ts_waitcnt lgkmcnt(8)\n\tv_mfma_f32_16x16x32_bf16 v[48:51], a[80:83], v[32:35], v[48:51]\n\tds_read_b64_tr_b16 v[24:25], v221 offset:8192\n\t" \
    "ds_read_b64_tr_b16 v[26:27], v221 offset:10240\n\tv_mfma_f32_16x16x32_bf16 v[48:51], v[192:195], v[36:39], v[48:51]\n\tbuffer_load_dwordx4 v13, s[16:19], s69 offen lds\n\ts_cmp_le_u32 s4, 4\n\ts_cselect_b32 s69, 262144, 0\n\ts_add_u32 s69, s69, 1179648\n\tv_mfma_f32_16x16x32_bf16 v[56:59], v[196:199], v[36:39], v[56:59]\n\ts_add_u32 m0, s54, 126976\n\t" \
    "v_mfma_f32_16x16x32_bf16 v[56:59], a[84:87], v[32:35], v[56:59]\n\tds_read_b64_tr_b16 v[28:29], v221 offset:12288\n\tds_read_b64_tr_b16 v[30:31], v221 offset:14336\n\tv_mfma_f32_16x16x32_bf16 v[64:67], a[88:91], v[32:35], v[64:67]\n\tbuffer_load_dwordx4 v13, s[16:19], s69 offen lds\n\ts_cmp_le_u32 s4, 5\n\ts_cselect_b32 s69, 262144, 0\n\ts_add_u32 s69, s69, 1441792\n\t" \
    "v_mfma_f32_16x16x32_bf16 v[64:67], v[200:203], v[36:39], v[64:67]\n\ts_add_u32 m0, s54, 0\n\ts_waitcnt lgkmcnt(8)\n\tv_mfma_f32_16x16x32_bf16 v[52:55], a[80:83], v[40:43], v[52:55]\n\tds_read_b64_tr_b16 v[32:33], v222 offset:8192\n\tds_read_b64_tr_b16 v[34:35], v222 offset:10240\n\tv_mfma_f32_16x16x32_bf16 v[52:55], v[192:195], v[44:47], v[52:55]\n\tbuffer_load_dwordx4 v12, s[16:19], s69 offen lds\n\t" \
    "s_cmp_le_u32 s4, 6\n\ts_cselect_b32 s69, 262144, 0\n\ts_add_u32 s69, s69, 1703936\n\tv_mfma_f32_16x16x32_bf16 v[60:63], v[196:199], v[44:47], v[60:63]\n\ts_add_u32 m0, s54, 4096\n\tv_mfma_f32_16x16x32_bf16 v[60:63], a[84:87], v[40:43], v[60:63]\n\tds_read_b64_tr_b16 v[36:37], v222 offset:12288\n\tds_read_b64_tr_b16 v[38:39], v222 offset:14336\n\t" \
    "v_mfma_f32_16x16x32_bf16 v[68:71], a[88:91], v[40:43], v[68:71]\n\tbuffer_load_dwordx4 v12, s[16:19], s69 offen lds\n\ts_cmp_le_u32 s4, 5\n\ts_cselect_b32 s69, 262144, 0\n\ts_add_u32 s69, s69, 1441792\n\tv_mfma_f32_16x16x32_bf16 v[68:71], v[200:203], v[44:47], v[68:71]\n\ts_add_u32 m0, s54, 8192\n\ts_waitcnt vmcnt(42)\n\t" \
    "s_barrier\n\ts_waitcnt vmcnt(36) lgkmcnt(8)\n\tv_mfma_f32_16x16x32_bf16 v[120:123], v[216:219], v[16:19], v[120:123]\n\tds_read_b64_tr_b16 v[40:41], v223 offset:8192\n\tds_read_b64_tr_b16 v[42:43], v223 offset:10240\n\tv_mfma_f32_16x16x32_bf16 v[120:123], v[212:215], v[20:23], v[120:123]\n\tbuffer_load_dwordx4 v13, s[16:19], s69 offen lds\n\ts_cmp_le_u32 s4, 6\n\t" \
    "s_cselect_b32 s69, 262144, 0\n\ts_add_u32 s69, s69, 1703936\n\tv_mfma_f32_16x16x32_bf16 v[128:131], v[72:75], v[20:23], v[128:131]\n\ts_add_u32 m0, s54, 131072\n\tv_mfma_f32_16x16x32_bf16 v[128:131], v[204:207], v[16:19], v[128:131]\n\tds_read_b64_tr_b16 v[44:45], v223 offset:12288\n\tds_read_b64_tr_b16 v[46:47], v223 offset:14336\n\tv_mfma_f32_16x16x32_bf16 v[136:139], v[208:211], v[16:19], v[136:139]\n\t" \
    "buffer_load_dwordx4 v13, s[16:19], s69 offen lds\n\ts_cmp_le_u32 s4, 7\n\ts_cselect_b32 s69, 262144, 0\n\ts_add_u32 s69, s69, 1966080\n\tv_mfma_f32_16x16x32_bf16 v[136:139], v[76:79], v[20:23], v[136:139]\n\ts_add_u32 m0, s54, 135168\n\ts_waitcnt lgkmcnt(8)\n\tv_mfma_f32_16x16x32_bf16 v[124:127], v[216:219], v[24:27], v[124:127]\n\t" \
    "ds_read_b64_tr_b16 v[16:17], v220 offset:16384\n\tds_read_b64_tr_b16 v[18:19], v220 offset:18432\n\tv_mfma_f32_16x16x32_bf16 v[124:127], v[212:215], v[28:31], v[124:127]\n\tv_mfma_f32_16x16x32_bf16 v[132:135], v[72:75], v[28:31], v[132:135]\n\tv_mfma_f32_16x16x32_bf16 v[132:135], v[204:207], v[24:27], v[132:135]\n\tds_read_b64_tr_b16 v[20:21], v220 offset:20480\n\tds_read_b64_tr_b16 v[22:23], v220 offset:22528\n\tv_mfma_f32_16x16x32_bf16 v[140:143], v[208:211], v[24:27], v[140:143]\n\t" \
    "buffer_load_dwordx4 v12, s[16:19], s69 offen lds\n\ts_cmp_le_u32 s4, 8\n\ts_cselect_b32 s69, 262144, 0\n\ts_add_u32 s69, s69, 2228224\n\tv_mfma_f32_16x16x32_bf16 v[140:143], v[76:79], v[28:31], v[140:143]\n\ts_add_u32 m0, s54, 12288\n\ts_waitcnt vmcnt(17)\n\tv_cmp_ne_u64 vcc, s[36:37], v[250:251]\n\t" \
    "s_cbranch_vccz 115f\n\ts_mov_b32 s71, 1048576\n\t114:\n\ts_sleep 1\n\tbuffer_load_dwordx2 v[250:251], v241, s[8:11], s50 offen sc1\n\ts_waitcnt vmcnt(0)\n\tv_cmp_ne_u64 vcc, s[36:37], v[250:251]\n\ts_cbranch_vccz 115f\n\t" \
    "s_sub_u32 s71, s71, 1\n\ts_cbranch_scc0 114b\n\t115:\n\ts_waitcnt lgkmcnt(8)\n\tv_mfma_f32_16x16x32_bf16 v[144:147], v[216:219], v[32:35], v[144:147]\n\tds_read_b64_tr_b16 v[24:25], v221 offset:16384\n\tds_read_b64_tr_b16 v[26:27], v221 offset:18432\n\tv_mfma_f32_16x16x32_bf16 v[144:147], v[212:215], v[36:39], v[144:147]\n\t" \
    "v_mfma_f32_16x16x32_bf16 v[152:155], v[72:75], v[36:39], v[152:155]\n\tv_mfma_f32_16x16x32_bf16 v[152:155], v[204:207], v[32:35], v[152:155]\n\tds_read_b64_tr_b16 v[28:29], v221 offset:20480\n\tds_read_b64_tr_b16 v[30:31], v221 offset:22528\n\tv_mfma_f32_16x16x32_bf16 v[160:163], v[208:211], v[32:35], v[160:163]\n\tbuffer_load_dwordx4 v12, s[16:19], s69 offen lds\n\ts_cmp_le_u32 s4, 0\n\ts_cselect_b32 s68, 3072, 0\n\t" \
    "s_add_u32 s68, s68, s46\n\tv_mfma_f32_16x16x32_bf16 v[160:163], v[76:79], v[36:39], v[160:163]\n\ts_add_u32 s68, s68, 55296\n\ts_waitcnt lgkmcnt(8)\n\tv_mfma_f32_16x16x32_bf16 v[148:151], v[216:219], v[40:43], v[148:151]\n\tds_read_b64_tr_b16 v[32:33], v222 offset:16384\n\tds_read_b64_tr_b16 v[34:35], v222 offset:18432\n\tv_mfma_f32_16x16x32_bf16 v[148:151], v[212:215], v[44:47], v[148:151]\n\t" \
    "buffer_load_dwordx4 a[20:23], v8, s[8:11], s68 offen offset:0 sc1\n\tv_mfma_f32_16x16x32_bf16 v[156:159], v[72:75], v[44:47], v[156:159]\n\tv_mfma_f32_16x16x32_bf16 v[156:159], v[204:207], v[40:43], v[156:159]\n\tds_read_b64_tr_b16 v[36:37], v222 offset:20480\n\tds_read_b64_tr_b16 v[38:39], v222 offset:22528\n\tv_mfma_f32_16x16x32_bf16 v[164:167], v[208:211], v[40:43], v[164:167]\n\tbuffer_load_dwordx4 a[24:27], v8, s[8:11], s68 offen offset:1024 sc1\n\tv_mfma_f32_16x16x32_bf16 v[164:167], v[76:79], v[44:47], v[164:167]\n\t" \
    "s_waitcnt lgkmcnt(8)\n\tv_mfma_f32_16x16x32_bf16 v[168:171], v[216:219], v[16:19], v[168:171]\n\tds_read_b64_tr_b16 v[40:41], v223 offset:16384\n\tds_read_b64_tr_b16 v[42:43], v223 offset:18432\n\tv_mfma_f32_16x16x32_bf16 v[168:171], v[212:215], v[20:23], v[168:171]\n\tbuffer_load_dwordx4 a[28:31], v8, s[8:11], s68 offen offset:2048 sc1\n\ts_cmp_le_u32 s4, 1\n\ts_cselect_b32 s68, 3072, 0\n\t" \
    "s_add_u32 s68, s68, s46\n\tv_mfma_f32_16x16x32_bf16 v[176:179], v[72:75], v[20:23], v[176:179]\n\ts_add_u32 s68, s68, 58368\n\tv_mfma_f32_16x16x32_bf16 v[176:179], v[204:207], v[16:19], v[176:179]\n\tds_read_b64_tr_b16 v[44:45], v223 offset:20480\n\tds_read_b64_tr_b16 v[46:47], v223 offset:22528\n\tv_mfma_f32_16x16x32_bf16 v[184:187], v[208:211], v[16:19], v[184:187]\n\tv_mfma_f32_16x16x32_bf16 v[184:187], v[76:79], v[20:23], v[184:187]\n\t" \
    "s_waitcnt lgkmcnt(8)\n\tv_mfma_f32_16x16x32_bf16 v[172:175], v[216:219], v[24:27], v[172:175]\n\tds_read_b64_tr_b16 v[16:17], v220 offset:24576\n\tds_read_b64_tr_b16 v[18:19], v220 offset:26624\n\tv_mfma_f32_16x16x32_bf16 v[172:175], v[212:215], v[28:31], v[172:175]\n\tbuffer_load_dwordx4 a[32:35], v8, s[8:11], s68 offen offset:0 sc1\n\tv_mfma_f32_16x16x32_bf16 v[180:183], v[72:75], v[28:31], v[180:183]\n\tv_mfma_f32_16x16x32_bf16 v[180:183], v[204:207], v[24:27], v[180:183]\n\t" \
    "ds_read_b64_tr_b16 v[20:21], v220 offset:28672\n\tds_read_b64_tr_b16 v[22:23], v220 offset:30720\n\tv_mfma_f32_16x16x32_bf16 v[188:191], v[208:211], v[24:27], v[188:191]\n\tbuffer_load_dwordx4 a[36:39], v8, s[8:11], s68 offen offset:1024 sc1\n\tv_mfma_f32_16x16x32_bf16 v[188:191], v[76:79], v[28:31], v[188:191]\n\ts_waitcnt lgkmcnt(8)\n\tv_mfma_f32_16x16x32_bf16 v[48:51], v[216:219], v[32:35], v[48:51]\n\tds_read_b64_tr_b16 v[24:25], v221 offset:24576\n\t" \
    "ds_read_b64_tr_b16 v[26:27], v221 offset:26624\n\tv_mfma_f32_16x16x32_bf16 v[48:51], v[212:215], v[36:39], v[48:51]\n\tv_mfma_f32_16x16x32_bf16 v[56:59], v[72:75], v[36:39], v[56:59]\n\tv_mfma_f32_16x16x32_bf16 v[56:59], v[204:207], v[32:35], v[56:59]\n\tds_read_b64_tr_b16 v[28:29], v221 offset:28672\n\tds_read_b64_tr_b16 v[30:31], v221 offset:30720\n\tv_mfma_f32_16x16x32_bf16 v[64:67], v[208:211], v[32:35], v[64:67]\n\tbuffer_load_dwordx4 a[40:43], v8, s[8:11], s68 offen offset:2048 sc1\n\t" \
    "s_cmp_le_u32 s4, 2\n\ts_cselect_b32 s68, 3072, 0\n\ts_add_u32 s68, s68, s46\n\tv_mfma_f32_16x16x32_bf16 v[64:67], v[76:79], v[36:39], v[64:67]\n\ts_add_u32 s68, s68, 61440\n\ts_waitcnt lgkmcnt(8)\n\tv_mfma_f32_16x16x32_bf16 v[52:55], v[216:219], v[40:43], v[52:55]\n\tds_read_b64_tr_b16 v[32:33], v222 offset:24576\n\t" \
    "ds_read_b64_tr_b16 v[34:35], v222 offset:26624\n\tv_mfma_f32_16x16x32_bf16 v[52:55], v[212:215], v[44:47], v[52:55]\n\tbuffer_load_dwordx4 a[44:47], v8, s[8:11], s68 offen offset:0 sc1\n\tv_mfma_f32_16x16x32_bf16 v[60:63], v[72:75], v[44:47], v[60:63]\n\tv_mfma_f32_16x16x32_bf16 v[60:63], v[204:207], v[40:43], v[60:63]\n\tds_read_b64_tr_b16 v[36:37], v222 offset:28672\n\tds_read_b64_tr_b16 v[38:39], v222 offset:30720\n\tv_mfma_f32_16x16x32_bf16 v[68:71], v[208:211], v[40:43], v[68:71]\n\t" \
    "buffer_load_dwordx4 a[48:51], v8, s[8:11], s68 offen offset:1024 sc1\n\tv_mfma_f32_16x16x32_bf16 v[68:71], v[76:79], v[44:47], v[68:71]\n\ts_barrier\n\ts_waitcnt lgkmcnt(8)\n\tv_mfma_f32_16x16x32_bf16 v[120:123], v[80:83], v[16:19], v[120:123]\n\tds_read_b64_tr_b16 v[40:41], v223 offset:24576\n\tds_read_b64_tr_b16 v[42:43], v223 offset:26624\n\tv_mfma_f32_16x16x32_bf16 v[120:123], v[92:95], v[20:23], v[120:123]\n\t" \
    "buffer_load_dwordx4 a[52:55], v8, s[8:11], s68 offen offset:2048 sc1\n\ts_cmp_le_u32 s4, 3\n\ts_cselect_b32 s68, 3072, 0\n\ts_add_u32 s68, s68, s46\n\tv_mfma_f32_16x16x32_bf16 v[128:131], v[96:99], v[20:23], v[128:131]\n\ts_add_u32 s68, s68, 64512\n\tv_mfma_f32_16x16x32_bf16 v[128:131], v[84:87], v[16:19], v[128:131]\n\tds_read_b64_tr_b16 v[44:45], v223 offset:28672\n\t" \
    "ds_read_b64_tr_b16 v[46:47], v223 offset:30720\n\tv_mfma_f32_16x16x32_bf16 v[136:139], v[88:91], v[16:19], v[136:139]\n\tbuffer_load_dwordx4 a[56:59], v8, s[8:11], s68 offen offset:0 sc1\n\tv_mfma_f32_16x16x32_bf16 v[136:139], v[100:103], v[20:23], v[136:139]\n\ts_waitcnt lgkmcnt(8)\n\tv_mfma_f32_16x16x32_bf16 v[124:127], v[80:83], v[24:27], v[124:127]\n\tds_read_b64_tr_b16 v[16:17], v4 offset:57344\n\tds_read_b64_tr_b16 v[18:19], v4 offset:59392\n\t" \
    "v_mfma_f32_16x16x32_bf16 v[124:127], v[92:95], v[28:31], v[124:127]\n\tv_mfma_f32_16x16x32_bf16 v[132:135], v[96:99], v[28:31], v[132:135]\n\tv_mfma_f32_16x16x32_bf16 v[132:135], v[84:87], v[24:27], v[132:135]\n\tds_read_b64_tr_b16 v[20:21], v4 offset:61440\n\tds_read_b64_tr_b16 v[22:23], v4 offset:63488\n\tv_mfma_f32_16x16x32_bf16 v[140:143], v[88:91], v[24:27], v[140:143]\n\tbuffer_load_dwordx4 a[60:63], v8, s[8:11], s68 offen offset:1024 sc1\n\tv_mfma_f32_16x16x32_bf16 v[140:143], v[100:103], v[28:31], v[140:143]\n\t" \
    "s_waitcnt lgkmcnt(8)\n\tv_mfma_f32_16x16x32_bf16 v[144:147], v[80:83], v[32:35], v[144:147]\n\tds_read_b64_tr_b16 v[24:25], v5 offset:57344\n\tds_read_b64_tr_b16 v[26:27], v5 offset:59392\n\tv_mfma_f32_16x16x32_bf16 v[144:147], v[92:95], v[36:39], v[144:147]\n\tbuffer_load_dwordx4 a[64:67], v8, s[8:11], s68 offen offset:2048 sc1\n\ts_cmp_le_u32 s4, 4\n\ts_cselect_b32 s68, 3072, 0\n\t" \
    "s_add_u32 s68, s68, s46\n\tv_mfma_f32_16x16x32_bf16 v[152:155], v[96:99], v[36:39], v[152:155]\n\ts_add_u32 s68, s68, 67584\n\tv_mfma_f32_16x16x32_bf16 v[152:155], v[84:87], v[32:35], v[152:155]\n\tds_read_b64_tr_b16 v[28:29], v5 offset:61440\n\tds_read_b64_tr_b16 v[30:31], v5 offset:63488\n\tv_mfma_f32_16x16x32_bf16 v[160:163], v[88:91], v[32:35], v[160:163]\n\tv_mfma_f32_16x16x32_bf16 v[160:163], v[100:103], v[36:39], v[160:163]\n\t" \
    "s_waitcnt lgkmcnt(8)\n\tv_mfma_f32_16x16x32_bf16 v[148:151], v[80:83], v[40:43], v[148:151]\n\tds_read_b64_tr_b16 v[32:33], v6 offset:57344\n\tds_read_b64_tr_b16 v[34:35], v6 offset:59392\n\tv_mfma_f32_16x16x32_bf16 v[148:151], v[92:95], v[44:47], v[148:151]\n\tbuffer_load_dwordx4 a[68:71], v8, s[8:11], s68 offen offset:0 sc1\n\tv_mfma_f32_16x16x32_bf16 v[156:159], v[96:99], v[44:47], v[156:159]\n\tv_mfma_f32_16x16x32_bf16 v[156:159], v[84:87], v[40:43], v[156:159]\n\t" \
    "ds_read_b64_tr_b16 v[36:37], v6 offset:61440\n\tds_read_b64_tr_b16 v[38:39], v6 offset:63488\n\tv_mfma_f32_16x16x32_bf16 v[164:167], v[88:91], v[40:43], v[164:167]\n\tbuffer_load_dwordx4 a[72:75], v8, s[8:11], s68 offen offset:1024 sc1\n\tv_mfma_f32_16x16x32_bf16 v[164:167], v[100:103], v[44:47], v[164:167]\n\ts_waitcnt lgkmcnt(8)\n\tv_mfma_f32_16x16x32_bf16 v[168:171], v[80:83], v[16:19], v[168:171]\n\tds_read_b64_tr_b16 v[40:41], v7 offset:57344\n\t" \
    "ds_read_b64_tr_b16 v[42:43], v7 offset:59392\n\tv_mfma_f32_16x16x32_bf16 v[168:171], v[92:95], v[20:23], v[168:171]\n\tv_mfma_f32_16x16x32_bf16 v[176:179], v[96:99], v[20:23], v[176:179]\n\tv_mfma_f32_16x16x32_bf16 v[176:179], v[84:87], v[16:19], v[176:179]\n\tds_read_b64_tr_b16 v[44:45], v7 offset:61440\n\tds_read_b64_tr_b16 v[46:47], v7 offset:63488\n\tv_mfma_f32_16x16x32_bf16 v[184:187], v[88:91], v[16:19], v[184:187]\n\tbuffer_load_dwordx4 a[76:79], v8, s[8:11], s68 offen offset:2048 sc1\n\t" \
    "s_cmp_le_u32 s4, 9\n\ts_cselect_b32 s69, 262144, 0\n\ts_add_u32 s69, s69, 2490368\n\tv_mfma_f32_16x16x32_bf16 v[184:187], v[100:103], v[20:23], v[184:187]\n\ts_add_u32 m0, s54, 139264\n\ts_waitcnt lgkmcnt(8)\n\tv_mfma_f32_16x16x32_bf16 v[172:175], v[80:83], v[24:27], v[172:175]\n\tds_read_b64_tr_b16 v[16:17], v224 offset:0\n\t" \
    "ds_read_b64_tr_b16 v[18:19], v224 offset:2048\n\tv_mfma_f32_16x16x32_bf16 v[172:175], v[92:95], v[28:31], v[172:175]\n\tbuffer_load_dwordx4 v12, s[16:19], s69 offen lds\n\ts_cmp_le_u32 s4, 10\n\ts_cselect_b32 s69, 262144, 0\n\ts_add_u32 s69, s69, 2752512\n\tv_mfma_f32_16x16x32_bf16 v[180:183], v[96:99], v[28:31], v[180:183]\n\ts_add_u32 m0, s54, 143360\n\t" \
    "v_mfma_f32_16x16x32_bf16 v[180:183], v[84:87], v[24:27], v[180:183]\n\tds_read_b64_tr_b16 v[20:21], v224 offset:4096\n\tds_read_b64_tr_b16 v[22:23], v224 offset:6144\n\tv_mfma_f32_16x16x32_bf16 v[188:191], v[88:91], v[24:27], v[188:191]\n\tv_mfma_f32_16x16x32_bf16 v[188:191], v[100:103], v[28:31], v[188:191]\n\ts_waitcnt lgkmcnt(8)\n\tv_mfma_f32_16x16x32_bf16 v[48:51], v[80:83], v[32:35], v[48:51]\n\tds_read_b64_tr_b16 v[24:25], v225 offset:0\n\t" \
    "ds_read_b64_tr_b16 v[26:27], v225 offset:2048\n\tv_mfma_f32_16x16x32_bf16 v[48:51], v[92:95], v[36:39], v[48:51]\n\tbuffer_load_dwordx4 v12, s[16:19], s69 offen lds\n\ts_cmp_le_u32 s4, 11\n\ts_cselect_b32 s69, 262144, 0\n\ts_add_u32 s69, s69, 3014656\n\tv_mfma_f32_16x16x32_bf16 v[56:59], v[96:99], v[36:39], v[56:59]\n\ts_add_u32 m0, s54, 147456\n\t" \
    "v_mfma_f32_16x16x32_bf16 v[56:59], v[84:87], v[32:35], v[56:59]\n\tds_read_b64_tr_b16 v[28:29], v225 offset:4096\n\tds_read_b64_tr_b16 v[30:31], v225 offset:6144\n\tv_mfma_f32_16x16x32_bf16 v[64:67], v[88:91], v[32:35], v[64:67]\n\tbuffer_load_dwordx4 v12, s[16:19], s69 offen lds\n\ts_cmp_le_u32 s4, 12\n\ts_cselect_b32 s69, 262144, 0\n\ts_add_u32 s69, s69, 3276800\n\t" \
    "v_mfma_f32_16x16x32_bf16 v[64:67], v[100:103], v[36:39], v[64:67]\n\ts_add_u32 m0, s54, 151552\n\ts_waitcnt lgkmcnt(8)\n\tv_mfma_f32_16x16x32_bf16 v[52:55], v[80:83], v[40:43], v[52:55]\n\tds_read_b64_tr_b16 v[32:33], v226 offset:0\n\tds_read_b64_tr_b16 v[34:35], v226 offset:2048\n\tv_mfma_f32_16x16x32_bf16 v[52:55], v[92:95], v[44:47], v[52:55]\n\tbuffer_load_dwordx4 v12, s[16:19], s69 offen lds\n\t" \
    "s_cmp_le_u32 s4, 5\n\ts_cselect_b32 s68, 3072, 0\n\ts_add_u32 s68, s68, s46\n\tv_mfma_f32_16x16x32_bf16 v[60:63], v[96:99], v[44:47], v[60:63]\n\ts_add_u32 s68, s68, 70656\n\tv_mfma_f32_16x16x32_bf16 v[60:63], v[84:87], v[40:43], v[60:63]\n\tds_read_b64_tr_b16 v[36:37], v226 offset:4096\n\tds_read_b64_tr_b16 v[38:39], v226 offset:6144\n\t" \
    "v_mfma_f32_16x16x32_bf16 v[68:71], v[88:91], v[40:43], v[68:71]\n\tv_mfma_f32_16x16x32_bf16 v[68:71], v[100:103], v[44:47], v[68:71]\n\ts_barrier\n\ts_waitcnt vmcnt(36) lgkmcnt(8)\n\tv_mfma_f32_16x16x32_bf16 v[120:123], v[104:107], v[16:19], v[120:123]\n\tds_read_b64_tr_b16 v[40:41], v227 offset:0\n\tds_read_b64_tr_b16 v[42:43], v227 offset:2048\n\tv_mfma_f32_16x16x32_bf16 v[120:123], v[116:119], v[20:23], v[120:123]\n\t" \
    "buffer_load_dwordx4 a[80:83], v8, s[8:11], s68 offen offset:0 sc1\n\tv_mfma_f32_16x16x32_bf16 v[128:131], a[0:3], v[20:23], v[128:131]\n\tv_mfma_f32_16x16x32_bf16 v[128:131], v[108:111], v[16:19], v[128:131]\n\tds_read_b64_tr_b16 v[44:45], v227 offset:4096\n\tds_read_b64_tr_b16 v[46:47], v227 offset:6144\n\tv_mfma_f32_16x16x32_bf16 v[136:139], v[112:115], v[16:19], v[136:139]\n\tbuffer_load_dwordx4 a[84:87], v8, s[8:11], s68 offen offset:1024 sc1\n\tv_mfma_f32_16x16x32_bf16 v[136:139], a[4:7], v[20:23], v[136:139]\n\t" \
    "s_waitcnt lgkmcnt(8)\n\tv_mfma_f32_16x16x32_bf16 v[124:127], v[104:107], v[24:27], v[124:127]\n\tds_read_b64_tr_b16 v[16:17], v224 offset:8192\n\tds_read_b64_tr_b16 v[18:19], v224 offset:10240\n\tv_mfma_f32_16x16x32_bf16 v[124:127], v[116:119], v[28:31], v[124:127]\n\tbuffer_load_dwordx4 a[88:91], v8, s[8:11], s68 offen offset:2048 sc1\n\ts_cmp_le_u32 s4, 6\n\ts_cselect_b32 s68, 3072, 0\n\t" \
    "s_add_u32 s68, s68, s46\n\tv_mfma_f32_16x16x32_bf16 v[132:135], a[0:3], v[28:31], v[132:135]\n\ts_add_u32 s68, s68, 73728\n\tv_mfma_f32_16x16x32_bf16 v[132:135], v[108:111], v[24:27], v[132:135]\n\tds_read_b64_tr_b16 v[20:21], v224 offset:12288\n\tds_read_b64_tr_b16 v[22:23], v224 offset:14336\n\tv_mfma_f32_16x16x32_bf16 v[140:143], v[112:115], v[24:27], v[140:143]\n\tbuffer_load_dwordx4 a[92:95], v8, s[8:11], s68 offen offset:0 sc1\n\t" \
    "v_mfma_f32_16x16x32_bf16 v[140:143], a[4:7], v[28:31], v[140:143]\n\ts_waitcnt lgkmcnt(8)\n\tv_mfma_f32_16x16x32_bf16 v[144:147], v[104:107], v[32:35], v[144:147]\n\tds_read_b64_tr_b16 v[24:25], v225 offset:8192\n\tds_read_b64_tr_b16 v[26:27], v225 offset:10240\n\tv_mfma_f32_16x16x32_bf16 v[144:147], v[116:119], v[36:39], v[144:147]\n\tv_mfma_f32_16x16x32_bf16 v[152:155], a[0:3], v[36:39], v[152:155]\n\tv_mfma_f32_16x16x32_bf16 v[152:155], v[108:111], v[32:35], v[152:155]\n\t" \
    "ds_read_b64_tr_b16 v[28:29], v225 offset:12288\n\tds_read_b64_tr_b16 v[30:31], v225 offset:14336\n\tv_mfma_f32_16x16x32_bf16 v[160:163], v[112:115], v[32:35], v[160:163]\n\tbuffer_load_dwordx4 a[96:99], v8, s[8:11], s68 offen offset:1024 sc1\n\tv_mfma_f32_16x16x32_bf16 v[160:163], a[4:7], v[36:39], v[160:163]\n\ts_waitcnt lgkmcnt(8)\n\tv_mfma_f32_16x16x32_bf16 v[148:151], v[104:107], v[40:43], v[148:151]\n\tds_read_b64_tr_b16 v[32:33], v224 offset:16384\n\t" \
    "ds_read_b64_tr_b16 v[34:35], v224 offset:18432\n\tv_mfma_f32_16x16x32_bf16 v[148:151], v[116:119], v[44:47], v[148:151]\n\tbuffer_load_dwordx4 a[100:103], v8, s[8:11], s68 offen offset:2048 sc1\n\ts_cmp_le_u32 s4, 7\n\ts_cselect_b32 s68, 3072, 0\n\ts_add_u32 s68, s68, s46\n\tv_mfma_f32_16x16x32_bf16 v[156:159], a[0:3], v[44:47], v[156:159]\n\ts_add_u32 s68, s68, 76800\n\t" \
    "v_mfma_f32_16x16x32_bf16 v[156:159], v[108:111], v[40:43], v[156:159]\n\tds_read_b64_tr_b16 v[36:37], v224 offset:20480\n\tds_read_b64_tr_b16 v[38:39], v224 offset:22528\n\tv_mfma_f32_16x16x32_bf16 v[164:167], v[112:115], v[40:43], v[164:167]\n\tv_mfma_f32_16x16x32_bf16 v[164:167], a[4:7], v[44:47], v[164:167]\n\ts_waitcnt lgkmcnt(8)\n\tv_mfma_f32_16x16x32_bf16 v[168:171], v[104:107], v[16:19], v[168:171]\n\tds_read_b64_tr_b16 v[40:41], v225 offset:16384\n\t" \
    "ds_read_b64_tr_b16 v[42:43], v225 offset:18432\n\tv_mfma_f32_16x16x32_bf16 v[168:171], v[116:119], v[20:23], v[168:171]\n\tbuffer_load_dwordx4 a[104:107], v8, s[8:11], s68 offen offset:0 sc1\n\tv_mfma_f32_16x16x32_bf16 v[176:179], a[0:3], v[20:23], v[176:179]\n\tv_mfma_f32_16x16x32_bf16 v[176:179], v[108:111], v[16:19], v[176:179]\n\tds_read_b64_tr_b16 v[44:45], v225 offset:20480\n\tds_read_b64_tr_b16 v[46:47], v225 offset:22528\n\tv_mfma_f32_16x16x32_bf16 v[184:187], v[112:115], v[16:19], v[184:187]\n\t" \
    "buffer_load_dwordx4 a[108:111], v8, s[8:11], s68 offen offset:1024 sc1\n\tv_mfma_f32_16x16x32_bf16 v[184:187], a[4:7], v[20:23], v[184:187]\n\ts_waitcnt lgkmcnt(8)\n\tv_mfma_f32_16x16x32_bf16 v[172:175], v[104:107], v[24:27], v[172:175]\n\tds_read_b64_tr_b16 v[16:17], v226 offset:16384\n\tds_read_b64_tr_b16 v[18:19], v226 offset:18432\n\tv_mfma_f32_16x16x32_bf16 v[172:175], v[116:119], v[28:31], v[172:175]\n\tv_mfma_f32_16x16x32_bf16 v[180:183], a[0:3], v[28:31], v[180:183]\n\t" \
    "v_mfma_f32_16x16x32_bf16 v[180:183], v[108:111], v[24:27], v[180:183]\n\tds_read_b64_tr_b16 v[20:21], v226 offset:20480\n\tds_read_b64_tr_b16 v[22:23], v226 offset:22528\n\tv_mfma_f32_16x16x32_bf16 v[188:191], v[112:115], v[24:27], v[188:191]\n\tbuffer_load_dwordx4 a[112:115], v8, s[8:11], s68 offen offset:2048 sc1\n\ts_cmp_le_u32 s4, 8\n\ts_cselect_b32 s68, 3072, 0\n\ts_add_u32 s68, s68, s46\n\t" \
    "v_mfma_f32_16x16x32_bf16 v[188:191], a[4:7], v[28:31], v[188:191]\n\ts_add_u32 s68, s68, 79872\n\ts_waitcnt vmcnt(25) lgkmcnt(8)\n\tv_mfma_f32_16x16x32_bf16 v[120:123], a[8:11], v[32:35], v[120:123]\n\tds_read_b64_tr_b16 v[24:25], v227 offset:16384\n\tds_read_b64_tr_b16 v[26:27], v227 offset:18432\n\tv_mfma_f32_16x16x32_bf16 v[120:123], a[20:23], v[36:39], v[120:123]\n\tbuffer_load_dwordx4 a[116:119], v8, s[8:11], s68 offen offset:0 sc1\n\t" \
    "v_mfma_f32_16x16x32_bf16 v[128:131], a[24:27], v[36:39], v[128:131]\n\tv_mfma_f32_16x16x32_bf16 v[128:131], a[12:15], v[32:35], v[128:131]\n\tds_read_b64_tr_b16 v[28:29], v227 offset:20480\n\tds_read_b64_tr_b16 v[30:31], v227 offset:22528\n\tv_mfma_f32_16x16x32_bf16 v[136:139], a[16:19], v[32:35], v[136:139]\n\tbuffer_load_dwordx4 a[120:123], v8, s[8:11], s68 offen offset:1024 sc1\n\tv_mfma_f32_16x16x32_bf16 v[136:139], a[28:31], v[36:39], v[136:139]\n\ts_waitcnt lgkmcnt(8)\n\t" \
    "v_mfma_f32_16x16x32_bf16 v[124:127], a[8:11], v[40:43], v[124:127]\n\tds_read_b64_tr_b16 v[32:33], v224 offset:24576\n\tds_read_b64_tr_b16 v[34:35], v224 offset:26624\n\tv_mfma_f32_16x16x32_bf16 v[124:127], a[20:23], v[44:47], v[124:127]\n\tv_mfma_f32_16x16x32_bf16 v[132:135], a[24:27], v[44:47], v[132:135]\n\tv_mfma_f32_16x16x32_bf16 v[132:135], a[12:15], v[40:43], v[132:135]\n\tds_read_b64_tr_b16 v[36:37], v224 offset:28672\n\tds_read_b64_tr_b16 v[38:39], v224 offset:30720\n\t" \
    "v_mfma_f32_16x16x32_bf16 v[140:143], a[16:19], v[40:43], v[140:143]\n\tbuffer_load_dwordx4 a[124:127], v8, s[8:11], s68 offen offset:2048 sc1\n\ts_cmp_le_u32 s4, 13\n\ts_cselect_b32 s69, 262144, 0\n\ts_add_u32 s69, s69, 3538944\n\tv_mfma_f32_16x16x32_bf16 v[140:143], a[28:31], v[44:47], v[140:143]\n\ts_add_u32 m0, s54, 155648\n\ts_barrier\n\t" \
    "s_waitcnt lgkmcnt(8)\n\tv_mfma_f32_16x16x32_bf16 v[144:147], a[8:11], v[16:19], v[144:147]\n\tds_read_b64_tr_b16 v[40:41], v225 offset:24576\n\tds_read_b64_tr_b16 v[42:43], v225 offset:26624\n\tv_mfma_f32_16x16x32_bf16 v[144:147], a[20:23], v[20:23], v[144:147]\n\tbuffer_load_dwordx4 v12, s[16:19], s69 offen lds\n\ts_cmp_le_u32 s4, 14\n\ts_cselect_b32 s69, 262144, 0\n\t" \
    "s_add_u32 s69, s69, 3801088\n\tv_mfma_f32_16x16x32_bf16 v[152:155], a[24:27], v[20:23], v[152:155]\n\ts_add_u32 m0, s54, 159744\n\tv_mfma_f32_16x16x32_bf16 v[152:155], a[12:15], v[16:19], v[152:155]\n\tds_read_b64_tr_b16 v[44:45], v225 offset:28672\n\tds_read_b64_tr_b16 v[46:47], v225 offset:30720\n\tv_mfma_f32_16x16x32_bf16 v[160:163], a[16:19], v[16:19], v[160:163]\n\tbuffer_load_dwordx4 v12, s[16:19], s69 offen lds\n\t" \
    "s_cmp_le_u32 s4, 15\n\ts_cselect_b32 s69, 262144, 0\n\ts_add_u32 s69, s69, 4063232\n\tv_mfma_f32_16x16x32_bf16 v[160:163], a[28:31], v[20:23], v[160:163]\n\ts_add_u32 m0, s54, 16384\n\ts_waitcnt lgkmcnt(8)\n\tv_mfma_f32_16x16x32_bf16 v[148:151], a[8:11], v[24:27], v[148:151]\n\tds_read_b64_tr_b16 v[16:17], v224 offset:32768\n\t" \
    "ds_read_b64_tr_b16 v[18:19], v224 offset:34816\n\tv_mfma_f32_16x16x32_bf16 v[148:151], a[20:23], v[28:31], v[148:151]\n\tbuffer_load_dwordx4 v12, s[16:19], s69 offen lds\n\ts_cmp_le_u32 s4, 16\n\ts_cselect_b32 s69, 262144, 0\n\ts_add_u32 s69, s69, 4325376\n\tv_mfma_f32_16x16x32_bf16 v[156:159], a[24:27], v[28:31], v[156:159]\n\ts_add_u32 m0, s54, 20480\n\t" \
    "v_mfma_f32_16x16x32_bf16 v[156:159], a[12:15], v[24:27], v[156:159]\n\tds_read_b64_tr_b16 v[20:21], v224 offset:36864\n\tds_read_b64_tr_b16 v[22:23], v224 offset:38912\n\tv_mfma_f32_16x16x32_bf16 v[164:167], a[16:19], v[24:27], v[164:167]\n\tv_mfma_f32_16x16x32_bf16 v[164:167], a[28:31], v[28:31], v[164:167]\n\ts_waitcnt lgkmcnt(8)\n\tv_mfma_f32_16x16x32_bf16 v[168:171], a[8:11], v[32:35], v[168:171]\n\tds_read_b64_tr_b16 v[24:25], v225 offset:32768\n\t" \
    "ds_read_b64_tr_b16 v[26:27], v225 offset:34816\n\tv_mfma_f32_16x16x32_bf16 v[168:171], a[20:23], v[36:39], v[168:171]\n\tbuffer_load_dwordx4 v12, s[16:19], s69 offen lds\n\ts_cmp_le_u32 s4, 9\n\ts_cselect_b32 s68, 3072, 0\n\ts_add_u32 s68, s68, s46\n\tv_mfma_f32_16x16x32_bf16 v[176:179], a[24:27], v[36:39], v[176:179]\n\ts_add_u32 s68, s68, 82944\n\t" \
    "v_mfma_f32_16x16x32_bf16 v[176:179], a[12:15], v[32:35], v[176:179]\n\tds_read_b64_tr_b16 v[28:29], v225 offset:36864\n\tds_read_b64_tr_b16 v[30:31], v225 offset:38912\n\tv_mfma_f32_16x16x32_bf16 v[184:187], a[16:19], v[32:35], v[184:187]\n\tbuffer_load_dwordx4 a[128:131], v8, s[8:11], s68 offen offset:0 sc1\n\tv_mfma_f32_16x16x32_bf16 v[184:187], a[28:31], v[36:39], v[184:187]\n\ts_waitcnt lgkmcnt(8)\n\tv_mfma_f32_16x16x32_bf16 v[172:175], a[8:11], v[40:43], v[172:175]\n\t" \
    "ds_read_b64_tr_b16 v[32:33], v226 offset:32768\n\tds_read_b64_tr_b16 v[34:35], v226 offset:34816\n\tv_mfma_f32_16x16x32_bf16 v[172:175], a[20:23], v[44:47], v[172:175]\n\tv_mfma_f32_16x16x32_bf16 v[180:183], a[24:27], v[44:47], v[180:183]\n\tv_mfma_f32_16x16x32_bf16 v[180:183], a[12:15], v[40:43], v[180:183]\n\tds_read_b64_tr_b16 v[36:37], v226 offset:36864\n\tds_read_b64_tr_b16 v[38:39], v226 offset:38912\n\tv_mfma_f32_16x16x32_bf16 v[188:191], a[16:19], v[40:43], v[188:191]\n\t" \
    "buffer_load_dwordx4 a[132:135], v8, s[8:11], s68 offen offset:1024 sc1\n\tv_mfma_f32_16x16x32_bf16 v[188:191], a[28:31], v[44:47], v[188:191]\n\ts_waitcnt vmcnt(28) lgkmcnt(8)\n\tv_mfma_f32_16x16x32_bf16 v[120:123], a[32:35], v[16:19], v[120:123]\n\tds_read_b64_tr_b16 v[40:41], v227 offset:32768\n\tds_read_b64_tr_b16 v[42:43], v227 offset:34816\n\tv_mfma_f32_16x16x32_bf16 v[120:123], a[44:47], v[20:23], v[120:123]\n\tbuffer_load_dwordx4 a[136:139], v8, s[8:11], s68 offen offset:2048 sc1\n\t" \
    "s_cmp_le_u32 s4, 10\n\ts_cselect_b32 s68, 3072, 0\n\ts_add_u32 s68, s68, s46\n\tv_mfma_f32_16x16x32_bf16 v[128:131], a[48:51], v[20:23], v[128:131]\n\ts_add_u32 s68, s68, 86016\n\tv_mfma_f32_16x16x32_bf16 v[128:131], a[36:39], v[16:19], v[128:131]\n\tds_read_b64_tr_b16 v[44:45], v227 offset:36864\n\tds_read_b64_tr_b16 v[46:47], v227 offset:38912\n\t" \
    "v_mfma_f32_16x16x32_bf16 v[136:139], a[40:43], v[16:19], v[136:139]\n\tbuffer_load_dwordx4 a[140:143], v8, s[8:11], s68 offen offset:0 sc1\n\tv_mfma_f32_16x16x32_bf16 v[136:139], a[52:55], v[20:23], v[136:139]\n\ts_waitcnt lgkmcnt(8)\n\tv_mfma_f32_16x16x32_bf16 v[124:127], a[32:35], v[24:27], v[124:127]\n\tds_read_b64_tr_b16 v[16:17], v224 offset:40960\n\tds_read_b64_tr_b16 v[18:19], v224 offset:43008\n\tv_mfma_f32_16x16x32_bf16 v[124:127], a[44:47], v[28:31], v[124:127]\n\t" \
    "v_mfma_f32_16x16x32_bf16 v[132:135], a[48:51], v[28:31], v[132:135]\n\tv_mfma_f32_16x16x32_bf16 v[132:135], a[36:39], v[24:27], v[132:135]\n\tds_read_b64_tr_b16 v[20:21], v224 offset:45056\n\tds_read_b64_tr_b16 v[22:23], v224 offset:47104\n\tv_mfma_f32_16x16x32_bf16 v[140:143], a[40:43], v[24:27], v[140:143]\n\tbuffer_load_dwordx4 a[144:147], v8, s[8:11], s68 offen offset:1024 sc1\n\tv_mfma_f32_16x16x32_bf16 v[140:143], a[52:55], v[28:31], v[140:143]\n\ts_waitcnt lgkmcnt(8)\n\t" \
    "v_mfma_f32_16x16x32_bf16 v[144:147], a[32:35], v[32:35], v[144:147]\n\tds_read_b64_tr_b16 v[24:25], v225 offset:40960\n\tds_read_b64_tr_b16 v[26:27], v225 offset:43008\n\tv_mfma_f32_16x16x32_bf16 v[144:147], a[44:47], v[36:39], v[144:147]\n\tbuffer_load_dwordx4 a[148:151], v8, s[8:11], s68 offen offset:2048 sc1\n\ts_cmp_le_u32 s4, 11\n\ts_cselect_b32 s68, 3072, 0\n\ts_add_u32 s68, s68, s46\n\t" \
    "v_mfma_f32_16x16x32_bf16 v[152:155], a[48:51], v[36:39], v[152:155]\n\ts_add_u32 s68, s68, 89088\n\tv_mfma_f32_16x16x32_bf16 v[152:155], a[36:39], v[32:35], v[152:155]\n\tds_read_b64_tr_b16 v[28:29], v225 offset:45056\n\tds_read_b64_tr_b16 v[30:31], v225 offset:47104\n\tv_mfma_f32_16x16x32_bf16 v[160:163], a[40:43], v[32:35], v[160:163]\n\tv_mfma_f32_16x16x32_bf16 v[160:163], a[52:55], v[36:39], v[160:163]\n\ts_waitcnt lgkmcnt(8)\n\t" \
    "v_mfma_f32_16x16x32_bf16 v[148:151], a[32:35], v[40:43], v[148:151]\n\tds_read_b64_tr_b16 v[32:33], v224 offset:49152\n\tds_read_b64_tr_b16 v[34:35], v224 offset:51200\n\tv_mfma_f32_16x16x32_bf16 v[148:151], a[44:47], v[44:47], v[148:151]\n\tbuffer_load_dwordx4 a[152:155], v8, s[8:11], s68 offen offset:0 sc1\n\tv_mfma_f32_16x16x32_bf16 v[156:159], a[48:51], v[44:47], v[156:159]\n\tv_mfma_f32_16x16x32_bf16 v[156:159], a[36:39], v[40:43], v[156:159]\n\tds_read_b64_tr_b16 v[36:37], v224 offset:53248\n\t" \
    "ds_read_b64_tr_b16 v[38:39], v224 offset:55296\n\tv_mfma_f32_16x16x32_bf16 v[164:167], a[40:43], v[40:43], v[164:167]\n\tbuffer_load_dwordx4 a[156:159], v8, s[8:11], s68 offen offset:1024 sc1\n\tv_mfma_f32_16x16x32_bf16 v[164:167], a[52:55], v[44:47], v[164:167]\n\ts_barrier\n\ts_waitcnt lgkmcnt(8)\n\tv_mfma_f32_16x16x32_bf16 v[168:171], a[32:35], v[16:19], v[168:171]\n\tds_read_b64_tr_b16 v[40:41], v225 offset:49152\n\t" \
    "ds_read_b64_tr_b16 v[42:43], v225 offset:51200\n\tv_mfma_f32_16x16x32_bf16 v[168:171], a[44:47], v[20:23], v[168:171]\n\tbuffer_load_dwordx4 a[160:163], v8, s[8:11], s68 offen offset:2048 sc1\n\ts_cmp_le_u32 s4, 12\n\ts_cselect_b32 s68, 3072, 0\n\ts_add_u32 s68, s68, s46\n\tv_mfma_f32_16x16x32_bf16 v[176:179], a[48:51], v[20:23], v[176:179]\n\ts_add_u32 s68, s68, 92160\n\t" \
    "v_mfma_f32_16x16x32_bf16 v[176:179], a[36:39], v[16:19], v[176:179]\n\tds_read_b64_tr_b16 v[44:45], v225 offset:53248\n\tds_read_b64_tr_b16 v[46:47], v225 offset:55296\n\tv_mfma_f32_16x16x32_bf16 v[184:187], a[40:43], v[16:19], v[184:187]\n\tbuffer_load_dwordx4 a[164:167], v8, s[8:11], s68 offen offset:0 sc1\n\tv_mfma_f32_16x16x32_bf16 v[184:187], a[52:55], v[20:23], v[184:187]\n\ts_waitcnt lgkmcnt(8)\n\tv_mfma_f32_16x16x32_bf16 v[172:175], a[32:35], v[24:27], v[172:175]\n\t" \
    "ds_read_b64_tr_b16 v[16:17], v226 offset:49152\n\tds_read_b64_tr_b16 v[18:19], v226 offset:51200\n\tv_mfma_f32_16x16x32_bf16 v[172:175], a[44:47], v[28:31], v[172:175]\n\tv_mfma_f32_16x16x32_bf16 v[180:183], a[48:51], v[28:31], v[180:183]\n\tv_mfma_f32_16x16x32_bf16 v[180:183], a[36:39], v[24:27], v[180:183]\n\tds_read_b64_tr_b16 v[20:21], v226 offset:53248\n\tds_read_b64_tr_b16 v[22:23], v226 offset:55296\n\tv_mfma_f32_16x16x32_bf16 v[188:191], a[40:43], v[24:27], v[188:191]\n\t" \
    "buffer_load_dwordx4 a[168:171], v8, s[8:11], s68 offen offset:1024 sc1\n\tv_mfma_f32_16x16x32_bf16 v[188:191], a[52:55], v[28:31], v[188:191]\n\ts_waitcnt vmcnt(31) lgkmcnt(8)\n\tv_mfma_f32_16x16x32_bf16 v[120:123], a[56:59], v[32:35], v[120:123]\n\tds_read_b64_tr_b16 v[24:25], v227 offset:49152\n\tds_read_b64_tr_b16 v[26:27], v227 offset:51200\n\tv_mfma_f32_16x16x32_bf16 v[120:123], a[68:71], v[36:39], v[120:123]\n\tbuffer_load_dwordx4 a[172:175], v8, s[8:11], s68 offen offset:2048 sc1\n\t" \
    "s_cmp_le_u32 s4, 13\n\ts_cselect_b32 s68, 3072, 0\n\ts_add_u32 s68, s68, s46\n\tv_mfma_f32_16x16x32_bf16 v[128:131], a[72:75], v[36:39], v[128:131]\n\ts_add_u32 s68, s68, 95232\n\tv_mfma_f32_16x16x32_bf16 v[128:131], a[60:63], v[32:35], v[128:131]\n\tds_read_b64_tr_b16 v[28:29], v227 offset:53248\n\tds_read_b64_tr_b16 v[30:31], v227 offset:55296\n\t" \
    "v_mfma_f32_16x16x32_bf16 v[136:139], a[64:67], v[32:35], v[136:139]\n\tv_mfma_f32_16x16x32_bf16 v[136:139], a[76:79], v[36:39], v[136:139]\n\ts_waitcnt lgkmcnt(8)\n\tv_mfma_f32_16x16x32_bf16 v[124:127], a[56:59], v[40:43], v[124:127]\n\tds_read_b64_tr_b16 v[32:33], v224 offset:57344\n\tds_read_b64_tr_b16 v[34:35], v224 offset:59392\n\tv_mfma_f32_16x16x32_bf16 v[124:127], a[68:71], v[44:47], v[124:127]\n\tbuffer_load_dwordx4 a[176:179], v8, s[8:11], s68 offen offset:0 sc1\n\t" \
    "v_mfma_f32_16x16x32_bf16 v[132:135], a[72:75], v[44:47], v[132:135]\n\tv_mfma_f32_16x16x32_bf16 v[132:135], a[60:63], v[40:43], v[132:135]\n\tds_read_b64_tr_b16 v[36:37], v224 offset:61440\n\tds_read_b64_tr_b16 v[38:39], v224 offset:63488\n\tv_mfma_f32_16x16x32_bf16 v[140:143], a[64:67], v[40:43], v[140:143]\n\tbuffer_load_dwordx4 a[180:183], v8, s[8:11], s68 offen offset:1024 sc1\n\tv_mfma_f32_16x16x32_bf16 v[140:143], a[76:79], v[44:47], v[140:143]\n\ts_waitcnt lgkmcnt(8)\n\t" \
    "v_mfma_f32_16x16x32_bf16 v[144:147], a[56:59], v[16:19], v[144:147]\n\tds_read_b64_tr_b16 v[40:41], v225 offset:57344\n\tds_read_b64_tr_b16 v[42:43], v225 offset:59392\n\tv_mfma_f32_16x16x32_bf16 v[144:147], a[68:71], v[20:23], v[144:147]\n\tv_mfma_f32_16x16x32_bf16 v[152:155], a[72:75], v[20:23], v[152:155]\n\tv_mfma_f32_16x16x32_bf16 v[152:155], a[60:63], v[16:19], v[152:155]\n\tds_read_b64_tr_b16 v[44:45], v225 offset:61440\n\tds_read_b64_tr_b16 v[46:47], v225 offset:63488\n\t" \
    "v_mfma_f32_16x16x32_bf16 v[160:163], a[64:67], v[16:19], v[160:163]\n\tbuffer_load_dwordx4 a[184:187], v8, s[8:11], s68 offen offset:2048 sc1\n\ts_cmp_le_u32 s4, 14\n\ts_cselect_b32 s68, 3072, 0\n\ts_add_u32 s68, s68, s46\n\tv_mfma_f32_16x16x32_bf16 v[160:163], a[76:79], v[20:23], v[160:163]\n\ts_add_u32 s68, s68, 98304\n\ts_waitcnt lgkmcnt(8)\n\t" \
    "v_mfma_f32_16x16x32_bf16 v[148:151], a[56:59], v[24:27], v[148:151]\n\tds_read_b64_tr_b16 v[16:17], v4 offset:0\n\tds_read_b64_tr_b16 v[18:19], v4 offset:2048\n\tv_mfma_f32_16x16x32_bf16 v[148:151], a[68:71], v[28:31], v[148:151]\n\tbuffer_load_dwordx4 a[188:191], v8, s[8:11], s68 offen offset:0 sc1\n\tv_mfma_f32_16x16x32_bf16 v[156:159], a[72:75], v[28:31], v[156:159]\n\tv_mfma_f32_16x16x32_bf16 v[156:159], a[60:63], v[24:27], v[156:159]\n\tds_read_b64_tr_b16 v[20:21], v4 offset:4096\n\t" \
    "ds_read_b64_tr_b16 v[22:23], v4 offset:6144\n\tv_mfma_f32_16x16x32_bf16 v[164:167], a[64:67], v[24:27], v[164:167]\n\tbuffer_load_dwordx4 a[192:195], v8, s[8:11], s68 offen offset:1024 sc1\n\tv_mfma_f32_16x16x32_bf16 v[164:167], a[76:79], v[28:31], v[164:167]\n\ts_waitcnt lgkmcnt(8)\n\tv_mfma_f32_16x16x32_bf16 v[168:171], a[56:59], v[32:35], v[168:171]\n\tds_read_b64_tr_b16 v[24:25], v5 offset:0\n\tds_read_b64_tr_b16 v[26:27], v5 offset:2048\n\t" \
    "v_mfma_f32_16x16x32_bf16 v[168:171], a[68:71], v[36:39], v[168:171]\n\tv_mfma_f32_16x16x32_bf16 v[176:179], a[72:75], v[36:39], v[176:179]\n\tv_mfma_f32_16x16x32_bf16 v[176:179], a[60:63], v[32:35], v[176:179]\n\tds_read_b64_tr_b16 v[28:29], v5 offset:4096\n\tds_read_b64_tr_b16 v[30:31], v5 offset:6144\n\tv_mfma_f32_16x16x32_bf16 v[184:187], a[64:67], v[32:35], v[184:187]\n\tbuffer_load_dwordx4 a[196:199], v8, s[8:11], s68 offen offset:2048 sc1\n\ts_cmp_le_u32 s4, 15\n\t" \
    "s_cselect_b32 s68, 3072, 0\n\ts_add_u32 s68, s68, s46\n\tv_mfma_f32_16x16x32_bf16 v[184:187], a[76:79], v[36:39], v[184:187]\n\ts_add_u32 s68, s68, 101376\n\ts_waitcnt lgkmcnt(8)\n\tv_mfma_f32_16x16x32_bf16 v[172:175], a[56:59], v[40:43], v[172:175]\n\tds_read_b64_tr_b16 v[32:33], v6 offset:0\n\tds_read_b64_tr_b16 v[34:35], v6 offset:2048\n\t" \
    "v_mfma_f32_16x16x32_bf16 v[172:175], a[68:71], v[44:47], v[172:175]\n\tbuffer_load_dwordx4 a[200:203], v8, s[8:11], s68 offen offset:0 sc1\n\tv_mfma_f32_16x16x32_bf16 v[180:183], a[72:75], v[44:47], v[180:183]\n\tv_mfma_f32_16x16x32_bf16 v[180:183], a[60:63], v[40:43], v[180:183]\n\tds_read_b64_tr_b16 v[36:37], v6 offset:4096\n\tds_read_b64_tr_b16 v[38:39], v6 offset:6144\n\tv_mfma_f32_16x16x32_bf16 v[188:191], a[64:67], v[40:43], v[188:191]\n\tv_mfma_f32_16x16x32_bf16 v[188:191], a[76:79], v[44:47], v[188:191]\n\t" \
    "s_waitcnt vmcnt(35)\n\ts_barrier\n\ts_waitcnt vmcnt(29) lgkmcnt(8)\n\tv_mfma_f32_16x16x32_bf16 v[120:123], a[80:83], v[16:19], v[120:123]\n\tds_read_b64_tr_b16 v[40:41], v7 offset:0\n\tds_read_b64_tr_b16 v[42:43], v7 offset:2048\n\tv_mfma_f32_16x16x32_bf16 v[120:123], a[92:95], v[20:23], v[120:123]\n\tbuffer_load_dwordx4 a[204:207], v8, s[8:11], s68 offen offset:1024 sc1\n\t" \
    "v_mfma_f32_16x16x32_bf16 v[128:131], a[96:99], v[20:23], v[128:131]\n\tv_mfma_f32_16x16x32_bf16 v[128:131], a[84:87], v[16:19], v[128:131]\n\tds_read_b64_tr_b16 v[44:45], v7 offset:4096\n\tds_read_b64_tr_b16 v[46:47], v7 offset:6144\n\tv_mfma_f32_16x16x32_bf16 v[136:139], a[88:91], v[16:19], v[136:139]\n\tbuffer_load_dwordx4 a[208:211], v8, s[8:11], s68 offen offset:2048 sc1\n\ts_cmp_le_u32 s4, 16\n\ts_cselect_b32 s68, 3072, 0\n\t" \
    "s_add_u32 s68, s68, s46\n\tv_mfma_f32_16x16x32_bf16 v[136:139], a[100:103], v[20:23], v[136:139]\n\ts_add_u32 s68, s68, 104448\n\ts_waitcnt lgkmcnt(8)\n\tv_mfma_f32_16x16x32_bf16 v[124:127], a[80:83], v[24:27], v[124:127]\n\tds_read_b64_tr_b16 v[16:17], v4 offset:8192\n\tds_read_b64_tr_b16 v[18:19], v4 offset:10240\n\tv_mfma_f32_16x16x32_bf16 v[124:127], a[92:95], v[28:31], v[124:127]\n\t" \
    "buffer_load_dwordx4 a[212:215], v8, s[8:11], s68 offen offset:0 sc1\n\tv_mfma_f32_16x16x32_bf16 v[132:135], a[96:99], v[28:31], v[132:135]\n\tv_mfma_f32_16x16x32_bf16 v[132:135], a[84:87], v[24:27], v[132:135]\n\tds_read_b64_tr_b16 v[20:21], v220 offset:0\n\tds_read_b64_tr_b16 v[22:23], v220 offset:2048\n\tv_mfma_f32_16x16x32_bf16 v[140:143], a[88:91], v[24:27], v[140:143]\n\tbuffer_load_dwordx4 a[216:219], v8, s[8:11], s68 offen offset:1024 sc1\n\tv_mfma_f32_16x16x32_bf16 v[140:143], a[100:103], v[28:31], v[140:143]\n\t" \
    "s_waitcnt lgkmcnt(8)\n\tv_mfma_f32_16x16x32_bf16 v[144:147], a[80:83], v[32:35], v[144:147]\n\tds_read_b64_tr_b16 v[24:25], v5 offset:8192\n\tds_read_b64_tr_b16 v[26:27], v5 offset:10240\n\tv_mfma_f32_16x16x32_bf16 v[144:147], a[92:95], v[36:39], v[144:147]\n\tv_mfma_f32_16x16x32_bf16 v[152:155], a[96:99], v[36:39], v[152:155]\n\tv_mfma_f32_16x16x32_bf16 v[152:155], a[84:87], v[32:35], v[152:155]\n\tds_read_b64_tr_b16 v[28:29], v221 offset:0\n\t" \
    "ds_read_b64_tr_b16 v[30:31], v221 offset:2048\n\tv_mfma_f32_16x16x32_bf16 v[160:163], a[88:91], v[32:35], v[160:163]\n\tbuffer_load_dwordx4 a[220:223], v8, s[8:11], s68 offen offset:2048 sc1\n\ts_cmp_le_u32 s4, 7\n\ts_cselect_b32 s69, 262144, 0\n\ts_add_u32 s69, s69, 1966080\n\tv_mfma_f32_16x16x32_bf16 v[160:163], a[100:103], v[36:39], v[160:163]\n\ts_add_u32 m0, s54, 24576\n\t" \
    "s_waitcnt lgkmcnt(8)\n\tv_mfma_f32_16x16x32_bf16 v[148:151], a[80:83], v[40:43], v[148:151]\n\tds_read_b64_tr_b16 v[32:33], v220 offset:4096\n\tds_read_b64_tr_b16 v[34:35], v220 offset:6144\n\tv_mfma_f32_16x16x32_bf16 v[148:151], a[92:95], v[44:47], v[148:151]\n\tbuffer_load_dwordx4 v13, s[16:19], s69 offen lds\n\ts_cmp_le_u32 s4, 8\n\ts_cselect_b32 s69, 262144, 0\n\t" \
    "s_add_u32 s69, s69, 2228224\n\tv_mfma_f32_16x16x32_bf16 v[156:159], a[96:99], v[44:47], v[156:159]\n\ts_add_u32 m0, s54, 28672\n\tv_mfma_f32_16x16x32_bf16 v[156:159], a[84:87], v[40:43], v[156:159]\n\tds_read_b64_tr_b16 v[36:37], v4 offset:12288\n\tds_read_b64_tr_b16 v[38:39], v4 offset:14336\n\tv_mfma_f32_16x16x32_bf16 v[164:167], a[88:91], v[40:43], v[164:167]\n\tv_mfma_f32_16x16x32_bf16 v[164:167], a[100:103], v[44:47], v[164:167]\n\t" \
    "s_waitcnt lgkmcnt(8)\n\tv_mfma_f32_16x16x32_bf16 v[168:171], a[80:83], v[16:19], v[168:171]\n\tds_read_b64_tr_b16 v[40:41], v221 offset:4096\n\tds_read_b64_tr_b16 v[42:43], v221 offset:6144\n\tv_mfma_f32_16x16x32_bf16 v[168:171], a[92:95], v[20:23], v[168:171]\n\tbuffer_load_dwordx4 v13, s[16:19], s69 offen lds\n\ts_cmp_le_u32 s4, 9\n\ts_cselect_b32 s69, 262144, 0\n\t" \
    "s_add_u32 s69, s69, 2490368\n\tv_mfma_f32_16x16x32_bf16 v[176:179], a[96:99], v[20:23], v[176:179]\n\ts_add_u32 m0, s54, 32768\n\tv_mfma_f32_16x16x32_bf16 v[176:179], a[84:87], v[16:19], v[176:179]\n\tds_read_b64_tr_b16 v[44:45], v5 offset:12288\n\tds_read_b64_tr_b16 v[46:47], v5 offset:14336\n\tv_mfma_f32_16x16x32_bf16 v[184:187], a[88:91], v[16:19], v[184:187]\n\tbuffer_load_dwordx4 v13, s[16:19], s69 offen lds\n\t" \
    "s_cmp_le_u32 s4, 10\n\ts_cselect_b32 s69, 262144, 0\n\ts_add_u32 s69, s69, 2752512\n\tv_mfma_f32_16x16x32_bf16 v[184:187], a[100:103], v[20:23], v[184:187]\n\ts_add_u32 m0, s54, 36864\n\ts_waitcnt lgkmcnt(8)\n\tv_mfma_f32_16x16x32_bf16 v[172:175], a[80:83], v[24:27], v[172:175]\n\tds_read_b64_tr_b16 v[16:17], v220 offset:8192\n\t" \
    "ds_read_b64_tr_b16 v[18:19], v220 offset:10240\n\tv_mfma_f32_16x16x32_bf16 v[172:175], a[92:95], v[28:31], v[172:175]\n\tbuffer_load_dwordx4 v13, s[16:19], s69 offen lds\n\ts_cmp_le_u32 s4, 11\n\ts_cselect_b32 s69, 262144, 0\n\ts_add_u32 s69, s69, 3014656\n\tv_mfma_f32_16x16x32_bf16 v[180:183], a[96:99], v[28:31], v[180:183]\n\ts_add_u32 m0, s54, 40960\n\t" \
    "v_mfma_f32_16x16x32_bf16 v[180:183], a[84:87], v[24:27], v[180:183]\n\tds_read_b64_tr_b16 v[20:21], v220 offset:12288\n\tds_read_b64_tr_b16 v[22:23], v220 offset:14336\n\tv_mfma_f32_16x16x32_bf16 v[188:191], a[88:91], v[24:27], v[188:191]\n\tv_mfma_f32_16x16x32_bf16 v[188:191], a[100:103], v[28:31], v[188:191]\n\ts_waitcnt vmcnt(32) lgkmcnt(8)\n\tv_mfma_f32_16x16x32_bf16 v[120:123], a[104:107], v[32:35], v[120:123]\n\tds_read_b64_tr_b16 v[24:25], v221 offset:8192\n\t" \
    "ds_read_b64_tr_b16 v[26:27], v221 offset:10240\n\tv_mfma_f32_16x16x32_bf16 v[120:123], a[116:119], v[36:39], v[120:123]\n\tbuffer_load_dwordx4 v13, s[16:19], s69 offen lds\n\ts_cmp_le_u32 s4, 12\n\ts_cselect_b32 s69, 262144, 0\n\ts_add_u32 s69, s69, 3276800\n\tv_mfma_f32_16x16x32_bf16 v[128:131], a[120:123], v[36:39], v[128:131]\n\ts_add_u32 m0, s54, 45056\n\t" \
    "v_mfma_f32_16x16x32_bf16 v[128:131], a[108:111], v[32:35], v[128:131]\n\tds_read_b64_tr_b16 v[28:29], v221 offset:12288\n\tds_read_b64_tr_b16 v[30:31], v221 offset:14336\n\tv_mfma_f32_16x16x32_bf16 v[136:139], a[112:115], v[32:35], v[136:139]\n\tbuffer_load_dwordx4 v13, s[16:19], s69 offen lds\n\ts_cmp_le_u32 s4, 13\n\ts_cselect_b32 s69, 262144, 0\n\ts_add_u32 s69, s69, 3538944\n\t" \
    "v_mfma_f32_16x16x32_bf16 v[136:139], a[124:127], v[36:39], v[136:139]\n\ts_add_u32 m0, s54, 49152\n\ts_waitcnt lgkmcnt(8)\n\tv_mfma_f32_16x16x32_bf16 v[124:127], a[104:107], v[40:43], v[124:127]\n\tds_read_b64_tr_b16 v[32:33], v220 offset:16384\n\tds_read_b64_tr_b16 v[34:35], v220 offset:18432\n\tv_mfma_f32_16x16x32_bf16 v[124:127], a[116:119], v[44:47], v[124:127]\n\tv_mfma_f32_16x16x32_bf16 v[132:135], a[120:123], v[44:47], v[132:135]\n\t" \
    "v_mfma_f32_16x16x32_bf16 v[132:135], a[108:111], v[40:43], v[132:135]\n\tds_read_b64_tr_b16 v[36:37], v220 offset:20480\n\tds_read_b64_tr_b16 v[38:39], v220 offset:22528\n\tv_mfma_f32_16x16x32_bf16 v[140:143], a[112:115], v[40:43], v[140:143]\n\tbuffer_load_dwordx4 v13, s[16:19], s69 offen lds\n\ts_cmp_le_u32 s4, 14\n\ts_cselect_b32 s69, 262144, 0\n\ts_add_u32 s69, s69, 3801088\n\t" \
    "v_mfma_f32_16x16x32_bf16 v[140:143], a[124:127], v[44:47], v[140:143]\n\ts_add_u32 m0, s54, 53248\n\ts_waitcnt vmcnt(31)\n\ts_barrier\n\ts_waitcnt vmcnt(25) lgkmcnt(8)\n\tv_mfma_f32_16x16x32_bf16 v[120:123], a[128:131], v[16:19], v[120:123]\n\tds_read_b64_tr_b16 v[40:41], v221 offset:16384\n\tds_read_b64_tr_b16 v[42:43], v221 offset:18432\n\t" \
    "v_mfma_f32_16x16x32_bf16 v[120:123], a[140:143], v[20:23], v[120:123]\n\tbuffer_load_dwordx4 v13, s[16:19], s69 offen lds\n\ts_cmp_le_u32 s4, 15\n\ts_cselect_b32 s69, 262144, 0\n\ts_add_u32 s69, s69, 4063232\n\tv_mfma_f32_16x16x32_bf16 v[128:131], a[144:147], v[20:23], v[128:131]\n\ts_add_u32 m0, s54, 57344\n\tv_mfma_f32_16x16x32_bf16 v[128:131], a[132:135], v[16:19], v[128:131]\n\t" \
    "ds_read_b64_tr_b16 v[44:45], v221 offset:20480\n\tds_read_b64_tr_b16 v[46:47], v221 offset:22528\n\tv_mfma_f32_16x16x32_bf16 v[136:139], a[136:139], v[16:19], v[136:139]\n\tbuffer_load_dwordx4 v13, s[16:19], s69 offen lds\n\ts_cmp_le_u32 s4, 16\n\ts_cselect_b32 s69, 262144, 0\n\ts_add_u32 s69, s69, 4325376\n\tv_mfma_f32_16x16x32_bf16 v[136:139], a[148:151], v[20:23], v[136:139]\n\t" \
    "s_add_u32 m0, s54, 61440\n\ts_waitcnt lgkmcnt(8)\n\tv_mfma_f32_16x16x32_bf16 v[124:127], a[128:131], v[24:27], v[124:127]\n\tds_read_b64_tr_b16 v[16:17], v220 offset:24576\n\tds_read_b64_tr_b16 v[18:19], v220 offset:26624\n\tv_mfma_f32_16x16x32_bf16 v[124:127], a[140:143], v[28:31], v[124:127]\n\tbuffer_load_dwordx4 v13, s[16:19], s69 offen lds\n\tv_mfma_f32_16x16x32_bf16 v[132:135], a[144:147], v[28:31], v[132:135]\n\t" \
    "v_mfma_f32_16x16x32_bf16 v[132:135], a[132:135], v[24:27], v[132:135]\n\tds_read_b64_tr_b16 v[20:21], v220 offset:28672\n\tds_read_b64_tr_b16 v[22:23], v220 offset:30720\n\tv_mfma_f32_16x16x32_bf16 v[140:143], a[136:139], v[24:27], v[140:143]\n\tbuffer_load_dwordx4 v[192:195], v243, s[20:23], 0 offen offset:0\n\tv_mfma_f32_16x16x32_bf16 v[140:143], a[148:151], v[28:31], v[140:143]\n\ts_waitcnt vmcnt(23) lgkmcnt(8)\n\tv_mfma_f32_16x16x32_bf16 v[120:123], a[152:155], v[32:35], v[120:123]\n\t" \
    "ds_read_b64_tr_b16 v[24:25], v221 offset:24576\n\tds_read_b64_tr_b16 v[26:27], v221 offset:26624\n\tv_mfma_f32_16x16x32_bf16 v[120:123], a[164:167], v[36:39], v[120:123]\n\tv_mfma_f32_16x16x32_bf16 v[128:131], a[168:171], v[36:39], v[128:131]\n\tv_mfma_f32_16x16x32_bf16 v[128:131], a[156:159], v[32:35], v[128:131]\n\tds_read_b64_tr_b16 v[28:29], v221 offset:28672\n\tds_read_b64_tr_b16 v[30:31], v221 offset:30720\n\tv_mfma_f32_16x16x32_bf16 v[136:139], a[160:163], v[32:35], v[136:139]\n\t" \
    "buffer_load_dwordx4 v[196:199], v243, s[20:23], 0 offen offset:32\n\tv_mfma_f32_16x16x32_bf16 v[136:139], a[172:175], v[36:39], v[136:139]\n\ts_waitcnt lgkmcnt(8)\n\tv_mfma_f32_16x16x32_bf16 v[124:127], a[152:155], v[40:43], v[124:127]\n\tds_read_b64_tr_b16 v[32:33], v4 offset:16384\n\tds_read_b64_tr_b16 v[34:35], v4 offset:18432\n\tv_mfma_f32_16x16x32_bf16 v[124:127], a[164:167], v[44:47], v[124:127]\n\tbuffer_load_dwordx4 v[200:203], v243, s[20:23], 0 offen offset:64\n\t" \
    "v_mfma_f32_16x16x32_bf16 v[132:135], a[168:171], v[44:47], v[132:135]\n\tv_mfma_f32_16x16x32_bf16 v[132:135], a[156:159], v[40:43], v[132:135]\n\tds_read_b64_tr_b16 v[36:37], v4 offset:20480\n\tds_read_b64_tr_b16 v[38:39], v4 offset:22528\n\tv_mfma_f32_16x16x32_bf16 v[140:143], a[160:163], v[40:43], v[140:143]\n\tv_mfma_f32_16x16x32_bf16 v[140:143], a[172:175], v[44:47], v[140:143]\n\ts_waitcnt vmcnt(19) lgkmcnt(8)\n\tv_mfma_f32_16x16x32_bf16 v[120:123], a[176:179], v[16:19], v[120:123]\n\t" \
    "ds_read_b64_tr_b16 v[40:41], v5 offset:16384\n\tds_read_b64_tr_b16 v[42:43], v5 offset:18432\n\tv_mfma_f32_16x16x32_bf16 v[120:123], a[188:191], v[20:23], v[120:123]\n\tbuffer_load_dwordx2 v[216:217], v242, s[8:11], s59 offen offset:0 sc1\n\tv_mfma_f32_16x16x32_bf16 v[128:131], a[192:195], v[20:23], v[128:131]\n\tv_mfma_f32_16x16x32_bf16 v[128:131], a[180:183], v[16:19], v[128:131]\n\tds_read_b64_tr_b16 v[44:45], v5 offset:20480\n\tds_read_b64_tr_b16 v[46:47], v5 offset:22528\n\t" \
    "v_mfma_f32_16x16x32_bf16 v[136:139], a[184:187], v[16:19], v[136:139]\n\tbuffer_load_dwordx2 v[218:219], v242, s[8:11], s59 offen offset:128 sc1\n\tv_mfma_f32_16x16x32_bf16 v[136:139], a[196:199], v[20:23], v[136:139]\n\ts_waitcnt lgkmcnt(8)\n\tv_mfma_f32_16x16x32_bf16 v[124:127], a[176:179], v[24:27], v[124:127]\n\tds_read_b64_tr_b16 v[16:17], v222 offset:4096\n\tds_read_b64_tr_b16 v[18:19], v222 offset:6144\n\tv_mfma_f32_16x16x32_bf16 v[124:127], a[188:191], v[28:31], v[124:127]\n\t" \
    "v_mfma_f32_16x16x32_bf16 v[132:135], a[192:195], v[28:31], v[132:135]\n\tv_mfma_f32_16x16x32_bf16 v[132:135], a[180:183], v[24:27], v[132:135]\n\tds_read_b64_tr_b16 v[20:21], v6 offset:12288\n\tds_read_b64_tr_b16 v[22:23], v6 offset:14336\n\tv_mfma_f32_16x16x32_bf16 v[140:143], a[184:187], v[24:27], v[140:143]\n\tv_mfma_f32_16x16x32_bf16 v[140:143], a[196:199], v[28:31], v[140:143]\n\ts_waitcnt vmcnt(15) lgkmcnt(8)\n\tv_mfma_f32_16x16x32_bf16 v[120:123], a[200:203], v[32:35], v[120:123]\n\t" \
    "ds_read_b64_tr_b16 v[24:25], v223 offset:4096\n\tds_read_b64_tr_b16 v[26:27], v223 offset:6144\n\tv_mfma_f32_16x16x32_bf16 v[120:123], a[212:215], v[36:39], v[120:123]\n\tv_mfma_f32_16x16x32_bf16 v[128:131], a[216:219], v[36:39], v[128:131]\n\tv_mfma_f32_16x16x32_bf16 v[128:131], a[204:207], v[32:35], v[128:131]\n\tds_read_b64_tr_b16 v[28:29], v7 offset:12288\n\tds_read_b64_tr_b16 v[30:31], v7 offset:14336\n\tv_mfma_f32_16x16x32_bf16 v[136:139], a[208:211], v[32:35], v[136:139]\n\t" \
    "v_mfma_f32_16x16x32_bf16 v[136:139], a[220:223], v[36:39], v[136:139]\n\ts_waitcnt lgkmcnt(8)\n\tv_mfma_f32_16x16x32_bf16 v[124:127], a[200:203], v[40:43], v[124:127]\n\tds_read_b64_tr_b16 v[32:33], v222 offset:8192\n\tds_read_b64_tr_b16 v[34:35], v222 offset:10240\n\tv_mfma_f32_16x16x32_bf16 v[124:127], a[212:215], v[44:47], v[124:127]\n\tv_mfma_f32_16x16x32_bf16 v[132:135], a[216:219], v[44:47], v[132:135]\n\tv_mfma_f32_16x16x32_bf16 v[132:135], a[204:207], v[40:43], v[132:135]\n\t" \
    "ds_read_b64_tr_b16 v[36:37], v222 offset:12288\n\tds_read_b64_tr_b16 v[38:39], v222 offset:14336\n\tv_mfma_f32_16x16x32_bf16 v[140:143], a[208:211], v[40:43], v[140:143]\n\tv_mfma_f32_16x16x32_bf16 v[140:143], a[220:223], v[44:47], v[140:143]\n\ts_waitcnt vmcnt(8)\n\ts_barrier\n\ts_waitcnt lgkmcnt(8)\n\tv_mfma_f32_16x16x32_bf16 v[144:147], a[104:107], v[16:19], v[144:147]\n\t" \
    "ds_read_b64_tr_b16 v[40:41], v223 offset:8192\n\tds_read_b64_tr_b16 v[42:43], v223 offset:10240\n\ts_xor_b32 s68, s53, 12\n\tv_mfma_f32_16x16x32_bf16 v[144:147], a[116:119], v[20:23], v[144:147]\n\ts_mul_i32 s68, s68, 9984\n\ts_add_u32 s68, s68, 7208960\n\tv_cvt_pk_bf16_f32 v120, v120, v121\n\tv_mfma_f32_16x16x32_bf16 v[152:155], a[120:123], v[20:23], v[152:155]\n\t" \
    "v_cvt_pk_bf16_f32 v121, v122, v123\n\tv_cvt_pk_bf16_f32 v122, v124, v125\n\tv_cvt_pk_bf16_f32 v123, v126, v127\n\tv_mfma_f32_16x16x32_bf16 v[152:155], a[108:111], v[16:19], v[152:155]\n\tds_read_b64_tr_b16 v[44:45], v223 offset:12288\n\tds_read_b64_tr_b16 v[46:47], v223 offset:14336\n\tv_mfma_f32_16x16x32_bf16 v[160:163], a[112:115], v[16:19], v[160:163]\n\tbuffer_store_dwordx4 v[120:123], v8, s[8:11], s68 offen offset:0 sc1\n\t" \
    "v_cvt_pk_bf16_f32 v124, v128, v129\n\tv_cvt_pk_bf16_f32 v125, v130, v131\n\tv_mfma_f32_16x16x32_bf16 v[160:163], a[124:127], v[20:23], v[160:163]\n\tv_cvt_pk_bf16_f32 v126, v132, v133\n\tv_cvt_pk_bf16_f32 v127, v134, v135\n\ts_waitcnt lgkmcnt(8)\n\tv_mfma_f32_16x16x32_bf16 v[148:151], a[104:107], v[24:27], v[148:151]\n\tds_read_b64_tr_b16 v[16:17], v222 offset:16384\n\t" \
    "ds_read_b64_tr_b16 v[18:19], v222 offset:18432\n\tv_mfma_f32_16x16x32_bf16 v[148:151], a[116:119], v[28:31], v[148:151]\n\tbuffer_store_dwordx4 v[124:127], v8, s[8:11], s68 offen offset:1024 sc1\n\tv_cvt_pk_bf16_f32 v128, v136, v137\n\tv_cvt_pk_bf16_f32 v129, v138, v139\n\tv_mfma_f32_16x16x32_bf16 v[156:159], a[120:123], v[28:31], v[156:159]\n\tv_cvt_pk_bf16_f32 v130, v140, v141\n\tv_cvt_pk_bf16_f32 v131, v142, v143\n\t" \
    "v_mfma_f32_16x16x32_bf16 v[156:159], a[108:111], v[24:27], v[156:159]\n\tds_read_b64_tr_b16 v[20:21], v222 offset:20480\n\tds_read_b64_tr_b16 v[22:23], v222 offset:22528\n\tv_mfma_f32_16x16x32_bf16 v[164:167], a[112:115], v[24:27], v[164:167]\n\tbuffer_store_dwordx4 v[128:131], v8, s[8:11], s68 offen offset:2048 sc1\n\tv_add3_u32 v132, s36, v120, v121\n\tv_add3_u32 v133, v122, v123, v124\n\tv_mfma_f32_16x16x32_bf16 v[164:167], a[124:127], v[28:31], v[164:167]\n\t" \
    "v_add3_u32 v132, v132, v125, v126\n\tv_add3_u32 v133, v133, v127, v128\n\ts_waitcnt lgkmcnt(8)\n\tv_mfma_f32_16x16x32_bf16 v[144:147], a[128:131], v[32:35], v[144:147]\n\tds_read_b64_tr_b16 v[24:25], v223 offset:16384\n\tds_read_b64_tr_b16 v[26:27], v223 offset:18432\n\tv_add3_u32 v132, v132, v129, v130\n\tv_mfma_f32_16x16x32_bf16 v[144:147], a[140:143], v[36:39], v[144:147]\n\t" \
    "v_add3_u32 v132, v132, v133, v131\n\tv_mfma_f32_16x16x32_bf16 v[152:155], a[144:147], v[36:39], v[152:155]\n\tv_mfma_f32_16x16x32_bf16 v[152:155], a[132:135], v[32:35], v[152:155]\n\tds_read_b64_tr_b16 v[28:29], v223 offset:20480\n\tds_read_b64_tr_b16 v[30:31], v223 offset:22528\n\tv_mfma_f32_16x16x32_bf16 v[160:163], a[136:139], v[32:35], v[160:163]\n\tbuffer_store_dword v132, v236, s[8:11], s68 offen offset:3072 sc1\n\tv_mfma_f32_16x16x32_bf16 v[160:163], a[148:151], v[36:39], v[160:163]\n\t" \
    "s_waitcnt lgkmcnt(8)\n\tv_mfma_f32_16x16x32_bf16 v[148:151], a[128:131], v[40:43], v[148:151]\n\tds_read_b64_tr_b16 v[32:33], v222 offset:24576\n\tds_read_b64_tr_b16 v[34:35], v222 offset:26624\n\tv_mfma_f32_16x16x32_bf16 v[148:151], a[140:143], v[44:47], v[148:151]\n\tv_mfma_f32_16x16x32_bf16 v[156:159], a[144:147], v[44:47], v[156:159]\n\tv_mfma_f32_16x16x32_bf16 v[156:159], a[132:135], v[40:43], v[156:159]\n\tds_read_b64_tr_b16 v[36:37], v222 offset:28672\n\t" \
    "ds_read_b64_tr_b16 v[38:39], v222 offset:30720\n\tv_mfma_f32_16x16x32_bf16 v[164:167], a[136:139], v[40:43], v[164:167]\n\tv_mfma_f32_16x16x32_bf16 v[164:167], a[148:151], v[44:47], v[164:167]\n\ts_waitcnt lgkmcnt(8)\n\tv_mfma_f32_16x16x32_bf16 v[144:147], a[152:155], v[16:19], v[144:147]\n\tds_read_b64_tr_b16 v[40:41], v223 offset:24576\n\tds_read_b64_tr_b16 v[42:43], v223 offset:26624\n\tv_mfma_f32_16x16x32_bf16 v[144:147], a[164:167], v[20:23], v[144:147]\n\t" \
    "v_mfma_f32_16x16x32_bf16 v[152:155], a[168:171], v[20:23], v[152:155]\n\tv_mfma_f32_16x16x32_bf16 v[152:155], a[156:159], v[16:19], v[152:155]\n\tds_read_b64_tr_b16 v[44:45], v223 offset:28672\n\tds_read_b64_tr_b16 v[46:47], v223 offset:30720\n\tv_mfma_f32_16x16x32_bf16 v[160:163], a[160:163], v[16:19], v[160:163]\n\tv_mfma_f32_16x16x32_bf16 v[160:163], a[172:175], v[20:23], v[160:163]\n\ts_waitcnt lgkmcnt(8)\n\tv_mfma_f32_16x16x32_bf16 v[148:151], a[152:155], v[24:27], v[148:151]\n\t" \
    "ds_read_b64_tr_b16 v[16:17], v6 offset:16384\n\tds_read_b64_tr_b16 v[18:19], v6 offset:18432\n\tv_mfma_f32_16x16x32_bf16 v[148:151], a[164:167], v[28:31], v[148:151]\n\tv_mfma_f32_16x16x32_bf16 v[156:159], a[168:171], v[28:31], v[156:159]\n\tv_mfma_f32_16x16x32_bf16 v[156:159], a[156:159], v[24:27], v[156:159]\n\tds_read_b64_tr_b16 v[20:21], v6 offset:20480\n\tds_read_b64_tr_b16 v[22:23], v6 offset:22528\n\tv_mfma_f32_16x16x32_bf16 v[164:167], a[160:163], v[24:27], v[164:167]\n\t" \
    "v_mfma_f32_16x16x32_bf16 v[164:167], a[172:175], v[28:31], v[164:167]\n\ts_waitcnt lgkmcnt(8)\n\tv_mfma_f32_16x16x32_bf16 v[144:147], a[176:179], v[32:35], v[144:147]\n\tds_read_b64_tr_b16 v[24:25], v7 offset:16384\n\tds_read_b64_tr_b16 v[26:27], v7 offset:18432\n\tv_mfma_f32_16x16x32_bf16 v[144:147], a[188:191], v[36:39], v[144:147]\n\tv_mfma_f32_16x16x32_bf16 v[152:155], a[192:195], v[36:39], v[152:155]\n\tv_mfma_f32_16x16x32_bf16 v[152:155], a[180:183], v[32:35], v[152:155]\n\t" \
    "ds_read_b64_tr_b16 v[28:29], v7 offset:20480\n\tds_read_b64_tr_b16 v[30:31], v7 offset:22528\n\tv_mfma_f32_16x16x32_bf16 v[160:163], a[184:187], v[32:35], v[160:163]\n\tv_mfma_f32_16x16x32_bf16 v[160:163], a[196:199], v[36:39], v[160:163]\n\ts_waitcnt lgkmcnt(8)\n\tv_mfma_f32_16x16x32_bf16 v[148:151], a[176:179], v[40:43], v[148:151]\n\tds_read_b64_tr_b16 v[32:33], v4 offset:24576\n\tds_read_b64_tr_b16 v[34:35], v4 offset:26624\n\t" \
    "v_mfma_f32_16x16x32_bf16 v[148:151], a[188:191], v[44:47], v[148:151]\n\tv_mfma_f32_16x16x32_bf16 v[156:159], a[192:195], v[44:47], v[156:159]\n\tv_mfma_f32_16x16x32_bf16 v[156:159], a[180:183], v[40:43], v[156:159]\n\tds_read_b64_tr_b16 v[36:37], v4 offset:28672\n\tds_read_b64_tr_b16 v[38:39], v4 offset:30720\n\tv_mfma_f32_16x16x32_bf16 v[164:167], a[184:187], v[40:43], v[164:167]\n\tv_mfma_f32_16x16x32_bf16 v[164:167], a[196:199], v[44:47], v[164:167]\n\ts_waitcnt vmcnt(9)\n\t" \
    "s_barrier\n\ts_waitcnt lgkmcnt(8)\n\tv_mfma_f32_16x16x32_bf16 v[144:147], a[200:203], v[16:19], v[144:147]\n\tds_read_b64_tr_b16 v[40:41], v5 offset:24576\n\tds_read_b64_tr_b16 v[42:43], v5 offset:26624\n\tv_mfma_f32_16x16x32_bf16 v[144:147], a[212:215], v[20:23], v[144:147]\n\tv_mfma_f32_16x16x32_bf16 v[152:155], a[216:219], v[20:23], v[152:155]\n\tv_mfma_f32_16x16x32_bf16 v[152:155], a[204:207], v[16:19], v[152:155]\n\t" \
    "ds_read_b64_tr_b16 v[44:45], v5 offset:28672\n\tds_read_b64_tr_b16 v[46:47], v5 offset:30720\n\tv_mfma_f32_16x16x32_bf16 v[160:163], a[208:211], v[16:19], v[160:163]\n\tv_mfma_f32_16x16x32_bf16 v[160:163], a[220:223], v[20:23], v[160:163]\n\tv_min_u32 v241, 376, v239\n\tbuffer_load_dwordx2 v[250:251], v241, s[8:11], s51 offen sc1\n\ts_waitcnt lgkmcnt(8)\n\tv_mfma_f32_16x16x32_bf16 v[148:151], a[200:203], v[24:27], v[148:151]\n\t" \
    "ds_read_b64_tr_b16 v[16:17], v4 offset:32768\n\tds_read_b64_tr_b16 v[18:19], v4 offset:34816\n\tv_mfma_f32_16x16x32_bf16 v[148:151], a[212:215], v[28:31], v[148:151]\n\tv_mfma_f32_16x16x32_bf16 v[156:159], a[216:219], v[28:31], v[156:159]\n\tv_mfma_f32_16x16x32_bf16 v[156:159], a[204:207], v[24:27], v[156:159]\n\tds_read_b64_tr_b16 v[20:21], v4 offset:36864\n\tds_read_b64_tr_b16 v[22:23], v4 offset:38912\n\tv_mfma_f32_16x16x32_bf16 v[164:167], a[208:211], v[24:27], v[164:167]\n\t" \
    "v_mfma_f32_16x16x32_bf16 v[164:167], a[220:223], v[28:31], v[164:167]\n\ts_waitcnt lgkmcnt(8)\n\tv_mfma_f32_16x16x32_bf16 v[168:171], a[104:107], v[32:35], v[168:171]\n\tds_read_b64_tr_b16 v[24:25], v5 offset:32768\n\tds_read_b64_tr_b16 v[26:27], v5 offset:34816\n\ts_xor_b32 s68, s53, 8\n\tv_mfma_f32_16x16x32_bf16 v[168:171], a[116:119], v[36:39], v[168:171]\n\ts_mul_i32 s68, s68, 9984\n\t" \
    "s_add_u32 s68, s68, 7212288\n\tv_cvt_pk_bf16_f32 v144, v144, v145\n\tv_mfma_f32_16x16x32_bf16 v[176:179], a[120:123], v[36:39], v[176:179]\n\tv_cvt_pk_bf16_f32 v145, v146, v147\n\tv_cvt_pk_bf16_f32 v146, v148, v149\n\tv_cvt_pk_bf16_f32 v147, v150, v151\n\tv_mfma_f32_16x16x32_bf16 v[176:179], a[108:111], v[32:35], v[176:179]\n\tds_read_b64_tr_b16 v[28:29], v5 offset:36864\n\t" \
    "ds_read_b64_tr_b16 v[30:31], v5 offset:38912\n\tv_mfma_f32_16x16x32_bf16 v[184:187], a[112:115], v[32:35], v[184:187]\n\tbuffer_store_dwordx4 v[144:147], v8, s[8:11], s68 offen offset:0 sc1\n\tv_cvt_pk_bf16_f32 v148, v152, v153\n\tv_cvt_pk_bf16_f32 v149, v154, v155\n\tv_mfma_f32_16x16x32_bf16 v[184:187], a[124:127], v[36:39], v[184:187]\n\tv_cvt_pk_bf16_f32 v150, v156, v157\n\tv_cvt_pk_bf16_f32 v151, v158, v159\n\t" \
    "s_waitcnt lgkmcnt(8)\n\tv_mfma_f32_16x16x32_bf16 v[172:175], a[104:107], v[40:43], v[172:175]\n\tds_read_b64_tr_b16 v[32:33], v4 offset:40960\n\tds_read_b64_tr_b16 v[34:35], v4 offset:43008\n\tv_mfma_f32_16x16x32_bf16 v[172:175], a[116:119], v[44:47], v[172:175]\n\tbuffer_store_dwordx4 v[148:151], v8, s[8:11], s68 offen offset:1024 sc1\n\tv_cvt_pk_bf16_f32 v152, v160, v161\n\tv_cvt_pk_bf16_f32 v153, v162, v163\n\t" \
    "v_mfma_f32_16x16x32_bf16 v[180:183], a[120:123], v[44:47], v[180:183]\n\tv_cvt_pk_bf16_f32 v154, v164, v165\n\tv_cvt_pk_bf16_f32 v155, v166, v167\n\tv_mfma_f32_16x16x32_bf16 v[180:183], a[108:111], v[40:43], v[180:183]\n\tds_read_b64_tr_b16 v[36:37], v4 offset:45056\n\tds_read_b64_tr_b16 v[38:39], v4 offset:47104\n\tv_mfma_f32_16x16x32_bf16 v[188:191], a[112:115], v[40:43], v[188:191]\n\tbuffer_store_dwordx4 v[152:155], v8, s[8:11], s68 offen offset:2048 sc1\n\t" \
    "v_add3_u32 v156, s36, v144, v145\n\tv_add3_u32 v157, v146, v147, v148\n\tv_mfma_f32_16x16x32_bf16 v[188:191], a[124:127], v[44:47], v[188:191]\n\tv_add3_u32 v156, v156, v149, v150\n\tv_add3_u32 v157, v157, v151, v152\n\ts_waitcnt lgkmcnt(8)\n\tv_mfma_f32_16x16x32_bf16 v[168:171], a[128:131], v[16:19], v[168:171]\n\tds_read_b64_tr_b16 v[40:41], v5 offset:40960\n\t" \
    "ds_read_b64_tr_b16 v[42:43], v5 offset:43008\n\tv_add3_u32 v156, v156, v153, v154\n\tv_mfma_f32_16x16x32_bf16 v[168:171], a[140:143], v[20:23], v[168:171]\n\tv_add3_u32 v156, v156, v157, v155\n\tv_mfma_f32_16x16x32_bf16 v[176:179], a[144:147], v[20:23], v[176:179]\n\tv_mfma_f32_16x16x32_bf16 v[176:179], a[132:135], v[16:19], v[176:179]\n\tds_read_b64_tr_b16 v[44:45], v5 offset:45056\n\tds_read_b64_tr_b16 v[46:47], v5 offset:47104\n\t" \
    "v_mfma_f32_16x16x32_bf16 v[184:187], a[136:139], v[16:19], v[184:187]\n\tbuffer_store_dword v156, v236, s[8:11], s68 offen offset:3072 sc1\n\tv_mfma_f32_16x16x32_bf16 v[184:187], a[148:151], v[20:23], v[184:187]\n\ts_waitcnt lgkmcnt(8)\n\tv_mfma_f32_16x16x32_bf16 v[172:175], a[128:131], v[24:27], v[172:175]\n\tds_read_b64_tr_b16 v[16:17], v4 offset:49152\n\tds_read_b64_tr_b16 v[18:19], v4 offset:51200\n\tv_mfma_f32_16x16x32_bf16 v[172:175], a[140:143], v[28:31], v[172:175]\n\t" \
    "v_mfma_f32_16x16x32_bf16 v[180:183], a[144:147], v[28:31], v[180:183]\n\tv_mfma_f32_16x16x32_bf16 v[180:183], a[132:135], v[24:27], v[180:183]\n\tds_read_b64_tr_b16 v[20:21], v4 offset:53248\n\tds_read_b64_tr_b16 v[22:23], v4 offset:55296\n\tv_mfma_f32_16x16x32_bf16 v[188:191], a[136:139], v[24:27], v[188:191]\n\tv_mfma_f32_16x16x32_bf16 v[188:191], a[148:151], v[28:31], v[188:191]\n\ts_waitcnt lgkmcnt(8)\n\tv_mfma_f32_16x16x32_bf16 v[168:171], a[152:155], v[32:35], v[168:171]\n\t" \
    "ds_read_b64_tr_b16 v[24:25], v5 offset:49152\n\tds_read_b64_tr_b16 v[26:27], v5 offset:51200\n\tv_mfma_f32_16x16x32_bf16 v[168:171], a[164:167], v[36:39], v[168:171]\n\tv_mfma_f32_16x16x32_bf16 v[176:179], a[168:171], v[36:39], v[176:179]\n\tv_mfma_f32_16x16x32_bf16 v[176:179], a[156:159], v[32:35], v[176:179]\n\tds_read_b64_tr_b16 v[28:29], v5 offset:53248\n\tds_read_b64_tr_b16 v[30:31], v5 offset:55296\n\tv_mfma_f32_16x16x32_bf16 v[184:187], a[160:163], v[32:35], v[184:187]\n\t" \
    "v_mfma_f32_16x16x32_bf16 v[184:187], a[172:175], v[36:39], v[184:187]\n\ts_waitcnt lgkmcnt(8)\n\tv_mfma_f32_16x16x32_bf16 v[172:175], a[152:155], v[40:43], v[172:175]\n\tds_read_b64_tr_b16 v[32:33], v4 offset:57344\n\tds_read_b64_tr_b16 v[34:35], v4 offset:59392\n\tv_mfma_f32_16x16x32_bf16 v[172:175], a[164:167], v[44:47], v[172:175]\n\tv_mfma_f32_16x16x32_bf16 v[180:183], a[168:171], v[44:47], v[180:183]\n\tv_mfma_f32_16x16x32_bf16 v[180:183], a[156:159], v[40:43], v[180:183]\n\t" \
    "ds_read_b64_tr_b16 v[36:37], v4 offset:61440\n\tds_read_b64_tr_b16 v[38:39], v4 offset:63488\n\tv_mfma_f32_16x16x32_bf16 v[188:191], a[160:163], v[40:43], v[188:191]\n\tv_mfma_f32_16x16x32_bf16 v[188:191], a[172:175], v[44:47], v[188:191]\n\ts_waitcnt lgkmcnt(8)\n\tv_mfma_f32_16x16x32_bf16 v[168:171], a[176:179], v[16:19], v[168:171]\n\tds_read_b64_tr_b16 v[40:41], v5 offset:57344\n\tds_read_b64_tr_b16 v[42:43], v5 offset:59392\n\t" \
    "v_mfma_f32_16x16x32_bf16 v[168:171], a[188:191], v[20:23], v[168:171]\n\tv_mfma_f32_16x16x32_bf16 v[176:179], a[192:195], v[20:23], v[176:179]\n\tv_mfma_f32_16x16x32_bf16 v[176:179], a[180:183], v[16:19], v[176:179]\n\tds_read_b64_tr_b16 v[44:45], v5 offset:61440\n\tds_read_b64_tr_b16 v[46:47], v5 offset:63488\n\tv_mfma_f32_16x16x32_bf16 v[184:187], a[184:187], v[16:19], v[184:187]\n\tv_mfma_f32_16x16x32_bf16 v[184:187], a[196:199], v[20:23], v[184:187]\n\ts_waitcnt lgkmcnt(8)\n\t" \
    "v_mfma_f32_16x16x32_bf16 v[172:175], a[176:179], v[24:27], v[172:175]\n\tds_read_b64_tr_b16 v[16:17], v226 offset:8192\n\tds_read_b64_tr_b16 v[18:19], v226 offset:10240\n\tv_mfma_f32_16x16x32_bf16 v[172:175], a[188:191], v[28:31], v[172:175]\n\tv_mfma_f32_16x16x32_bf16 v[180:183], a[192:195], v[28:31], v[180:183]\n\tv_mfma_f32_16x16x32_bf16 v[180:183], a[180:183], v[24:27], v[180:183]\n\tds_read_b64_tr_b16 v[20:21], v226 offset:12288\n\tds_read_b64_tr_b16 v[22:23], v226 offset:14336\n\t" \
    "v_mfma_f32_16x16x32_bf16 v[188:191], a[184:187], v[24:27], v[188:191]\n\tv_mfma_f32_16x16x32_bf16 v[188:191], a[196:199], v[28:31], v[188:191]\n\ts_waitcnt lgkmcnt(8)\n\tv_mfma_f32_16x16x32_bf16 v[168:171], a[200:203], v[32:35], v[168:171]\n\tds_read_b64_tr_b16 v[24:25], v227 offset:8192\n\tds_read_b64_tr_b16 v[26:27], v227 offset:10240\n\tv_mfma_f32_16x16x32_bf16 v[168:171], a[212:215], v[36:39], v[168:171]\n\tv_mfma_f32_16x16x32_bf16 v[176:179], a[216:219], v[36:39], v[176:179]\n\t" \
    "v_mfma_f32_16x16x32_bf16 v[176:179], a[204:207], v[32:35], v[176:179]\n\tds_read_b64_tr_b16 v[28:29], v227 offset:12288\n\tds_read_b64_tr_b16 v[30:31], v227 offset:14336\n\tv_mfma_f32_16x16x32_bf16 v[184:187], a[208:211], v[32:35], v[184:187]\n\tv_mfma_f32_16x16x32_bf16 v[184:187], a[220:223], v[36:39], v[184:187]\n\ts_waitcnt vmcnt(4)\n\tv_cmp_ne_u64 vcc, s[36:37], v[250:251]\n\ts_cmp_lg_u64 vcc, 0\n\t" \
    "s_cselect_b32 s70, 1, 0\n\ts_waitcnt lgkmcnt(8)\n\tv_mfma_f32_16x16x32_bf16 v[172:175], a[200:203], v[40:43], v[172:175]\n\tds_read_b64_tr_b16 v[32:33], v226 offset:24576\n\tds_read_b64_tr_b16 v[34:35], v226 offset:26624\n\tv_mfma_f32_16x16x32_bf16 v[172:175], a[212:215], v[44:47], v[172:175]\n\tbuffer_load_dwordx4 v[204:207], v8, s[8:11], s56 offen offset:0 sc1\n\tv_mfma_f32_16x16x32_bf16 v[180:183], a[216:219], v[44:47], v[180:183]\n\t" \
    "v_mfma_f32_16x16x32_bf16 v[180:183], a[204:207], v[40:43], v[180:183]\n\tds_read_b64_tr_b16 v[36:37], v226 offset:28672\n\tds_read_b64_tr_b16 v[38:39], v226 offset:30720\n\tv_mfma_f32_16x16x32_bf16 v[188:191], a[208:211], v[40:43], v[188:191]\n\tbuffer_load_dwordx4 v[208:211], v8, s[8:11], s56 offen offset:1024 sc1\n\tv_mfma_f32_16x16x32_bf16 v[188:191], a[220:223], v[44:47], v[188:191]\n\ts_waitcnt lgkmcnt(8)\n\tv_mfma_f32_16x16x32_bf16 v[48:51], v[104:107], v[16:19], v[48:51]\n\t" \
    "ds_read_b64_tr_b16 v[40:41], v227 offset:24576\n\tds_read_b64_tr_b16 v[42:43], v227 offset:26624\n\tv_mfma_f32_16x16x32_bf16 v[48:51], v[116:119], v[20:23], v[48:51]\n\tbuffer_load_dwordx4 v[212:215], v8, s[8:11], s56 offen offset:2048 sc1\n\ts_xor_b32 s68, s53, 4\n\ts_mul_i32 s68, s68, 9984\n\tv_mfma_f32_16x16x32_bf16 v[56:59], a[0:3], v[20:23], v[56:59]\n\ts_add_u32 s68, s68, 7215616\n\t" \
    "v_cvt_pk_bf16_f32 v168, v168, v169\n\tv_cvt_pk_bf16_f32 v169, v170, v171\n\tv_mfma_f32_16x16x32_bf16 v[56:59], v[108:111], v[16:19], v[56:59]\n\tds_read_b64_tr_b16 v[44:45], v227 offset:28672\n\tds_read_b64_tr_b16 v[46:47], v227 offset:30720\n\tv_cvt_pk_bf16_f32 v170, v172, v173\n\tv_mfma_f32_16x16x32_bf16 v[64:67], v[112:115], v[16:19], v[64:67]\n\tv_cvt_pk_bf16_f32 v171, v174, v175\n\t" \
    "v_mfma_f32_16x16x32_bf16 v[64:67], a[4:7], v[20:23], v[64:67]\n\ts_waitcnt lgkmcnt(8)\n\tv_mfma_f32_16x16x32_bf16 v[52:55], v[104:107], v[24:27], v[52:55]\n\tds_read_b64_tr_b16 v[16:17], v226 offset:40960\n\tds_read_b64_tr_b16 v[18:19], v226 offset:43008\n\tv_mfma_f32_16x16x32_bf16 v[52:55], v[116:119], v[28:31], v[52:55]\n\tbuffer_store_dwordx4 v[168:171], v8, s[8:11], s68 offen offset:0 sc1\n\tv_cvt_pk_bf16_f32 v172, v176, v177\n\t" \
    "v_cvt_pk_bf16_f32 v173, v178, v179\n\tv_mfma_f32_16x16x32_bf16 v[60:63], a[0:3], v[28:31], v[60:63]\n\tv_cvt_pk_bf16_f32 v174, v180, v181\n\tv_cvt_pk_bf16_f32 v175, v182, v183\n\tv_mfma_f32_16x16x32_bf16 v[60:63], v[108:111], v[24:27], v[60:63]\n\tds_read_b64_tr_b16 v[20:21], v226 offset:45056\n\tds_read_b64_tr_b16 v[22:23], v226 offset:47104\n\tv_mfma_f32_16x16x32_bf16 v[68:71], v[112:115], v[24:27], v[68:71]\n\t" \
    "buffer_store_dwordx4 v[172:175], v8, s[8:11], s68 offen offset:1024 sc1\n\tv_cvt_pk_bf16_f32 v176, v184, v185\n\tv_cvt_pk_bf16_f32 v177, v186, v187\n\tv_mfma_f32_16x16x32_bf16 v[68:71], a[4:7], v[28:31], v[68:71]\n\tv_cvt_pk_bf16_f32 v178, v188, v189\n\tv_cvt_pk_bf16_f32 v179, v190, v191\n\ts_waitcnt lgkmcnt(8)\n\tv_mfma_f32_16x16x32_bf16 v[48:51], a[8:11], v[32:35], v[48:51]\n\t" \
    "ds_read_b64_tr_b16 v[24:25], v227 offset:40960\n\tds_read_b64_tr_b16 v[26:27], v227 offset:43008\n\tv_mfma_f32_16x16x32_bf16 v[48:51], a[20:23], v[36:39], v[48:51]\n\tbuffer_store_dwordx4 v[176:179], v8, s[8:11], s68 offen offset:2048 sc1\n\tv_add3_u32 v180, s36, v168, v169\n\tv_add3_u32 v181, v170, v171, v172\n\tv_mfma_f32_16x16x32_bf16 v[56:59], a[24:27], v[36:39], v[56:59]\n\tv_add3_u32 v180, v180, v173, v174\n\t" \
    "v_add3_u32 v181, v181, v175, v176\n\tv_add3_u32 v180, v180, v177, v178\n\tv_mfma_f32_16x16x32_bf16 v[56:59], a[12:15], v[32:35], v[56:59]\n\tds_read_b64_tr_b16 v[28:29], v227 offset:45056\n\tds_read_b64_tr_b16 v[30:31], v227 offset:47104\n\tv_add3_u32 v180, v180, v181, v179\n\tv_mfma_f32_16x16x32_bf16 v[64:67], a[16:19], v[32:35], v[64:67]\n\tbuffer_store_dword v180, v236, s[8:11], s68 offen offset:3072 sc1\n\t" \
    "v_mfma_f32_16x16x32_bf16 v[64:67], a[28:31], v[36:39], v[64:67]\n\ts_waitcnt lgkmcnt(8)\n\tv_mfma_f32_16x16x32_bf16 v[52:55], a[8:11], v[40:43], v[52:55]\n\tds_read_b64_tr_b16 v[32:33], v226 offset:57344\n\tds_read_b64_tr_b16 v[34:35], v226 offset:59392\n\tv_mfma_f32_16x16x32_bf16 v[52:55], a[20:23], v[44:47], v[52:55]\n\tv_permlane16_swap_b32 v192, v194\n\tv_permlane16_swap_b32 v193, v195\n\t" \
    "v_mfma_f32_16x16x32_bf16 v[60:63], a[24:27], v[44:47], v[60:63]\n\tv_permlane16_swap_b32 v196, v198\n\tv_permlane16_swap_b32 v197, v199\n\tv_mfma_f32_16x16x32_bf16 v[60:63], a[12:15], v[40:43], v[60:63]\n\tds_read_b64_tr_b16 v[36:37], v226 offset:61440\n\tds_read_b64_tr_b16 v[38:39], v226 offset:63488\n\tv_mfma_f32_16x16x32_bf16 v[68:71], a[16:19], v[40:43], v[68:71]\n\tv_permlane16_swap_b32 v200, v202\n\t" \
    "v_permlane16_swap_b32 v201, v203\n\tv_mfma_f32_16x16x32_bf16 v[68:71], a[28:31], v[44:47], v[68:71]\n\tv_lshlrev_b32 v72, 16, v192\n\tv_and_b32 v73, 0xffff0000, v192\n\ts_waitcnt lgkmcnt(8)\n\tv_mfma_f32_16x16x32_bf16 v[48:51], a[32:35], v[16:19], v[48:51]\n\tds_read_b64_tr_b16 v[40:41], v227 offset:57344\n\tds_read_b64_tr_b16 v[42:43], v227 offset:59392\n\t" \
    "v_mfma_f32_16x16x32_bf16 v[48:51], a[44:47], v[20:23], v[48:51]\n\tv_lshlrev_b32 v74, 16, v193\n\tv_and_b32 v75, 0xffff0000, v193\n\tv_mfma_f32_16x16x32_bf16 v[56:59], a[48:51], v[20:23], v[56:59]\n\tv_lshlrev_b32 v76, 16, v194\n\tv_and_b32 v77, 0xffff0000, v194\n\tv_mfma_f32_16x16x32_bf16 v[56:59], a[36:39], v[16:19], v[56:59]\n\tds_read_b64_tr_b16 v[44:45], v227 offset:61440\n\t" \
    "ds_read_b64_tr_b16 v[46:47], v227 offset:63488\n\tv_mfma_f32_16x16x32_bf16 v[64:67], a[40:43], v[16:19], v[64:67]\n\tv_lshlrev_b32 v78, 16, v195\n\tv_and_b32 v79, 0xffff0000, v195\n\tv_mfma_f32_16x16x32_bf16 v[64:67], a[52:55], v[20:23], v[64:67]\n\tv_lshlrev_b32 v80, 16, v196\n\tv_and_b32 v81, 0xffff0000, v196\n\ts_waitcnt lgkmcnt(8)\n\t" \
    "v_mfma_f32_16x16x32_bf16 v[52:55], a[32:35], v[24:27], v[52:55]\n\tds_read_b64_tr_b16 v[16:17], v6 offset:8192\n\tds_read_b64_tr_b16 v[18:19], v6 offset:10240\n\tv_mfma_f32_16x16x32_bf16 v[52:55], a[44:47], v[28:31], v[52:55]\n\tv_lshlrev_b32 v82, 16, v197\n\tv_and_b32 v83, 0xffff0000, v197\n\tv_mfma_f32_16x16x32_bf16 v[60:63], a[48:51], v[28:31], v[60:63]\n\tv_lshlrev_b32 v84, 16, v198\n\t" \
    "v_and_b32 v85, 0xffff0000, v198\n\tv_mfma_f32_16x16x32_bf16 v[60:63], a[36:39], v[24:27], v[60:63]\n\tds_read_b64_tr_b16 v[20:21], v222 offset:0\n\tds_read_b64_tr_b16 v[22:23], v222 offset:2048\n\tv_mfma_f32_16x16x32_bf16 v[68:71], a[40:43], v[24:27], v[68:71]\n\tv_lshlrev_b32 v86, 16, v199\n\tv_and_b32 v87, 0xffff0000, v199\n\tv_mfma_f32_16x16x32_bf16 v[68:71], a[52:55], v[28:31], v[68:71]\n\t" \
    "v_lshlrev_b32 v88, 16, v200\n\tv_and_b32 v89, 0xffff0000, v200\n\ts_waitcnt lgkmcnt(8)\n\tv_mfma_f32_16x16x32_bf16 v[48:51], a[56:59], v[32:35], v[48:51]\n\tds_read_b64_tr_b16 v[24:25], v7 offset:8192\n\tds_read_b64_tr_b16 v[26:27], v7 offset:10240\n\tv_mfma_f32_16x16x32_bf16 v[48:51], a[68:71], v[36:39], v[48:51]\n\tv_lshlrev_b32 v90, 16, v201\n\t" \
    "v_and_b32 v91, 0xffff0000, v201\n\tv_mfma_f32_16x16x32_bf16 v[56:59], a[72:75], v[36:39], v[56:59]\n\tv_lshlrev_b32 v92, 16, v202\n\tv_and_b32 v93, 0xffff0000, v202\n\tv_mfma_f32_16x16x32_bf16 v[56:59], a[60:63], v[32:35], v[56:59]\n\tds_read_b64_tr_b16 v[28:29], v223 offset:0\n\tds_read_b64_tr_b16 v[30:31], v223 offset:2048\n\tv_mfma_f32_16x16x32_bf16 v[64:67], a[64:67], v[32:35], v[64:67]\n\t" \
    "v_lshlrev_b32 v94, 16, v203\n\tv_and_b32 v95, 0xffff0000, v203\n\tv_mfma_f32_16x16x32_bf16 v[64:67], a[76:79], v[36:39], v[64:67]\n\tv_fma_f32 v96, v72, v216, v217\n\tv_fma_f32 v97, v73, v216, v217\n\ts_waitcnt lgkmcnt(8)\n\tv_mfma_f32_16x16x32_bf16 v[52:55], a[56:59], v[40:43], v[52:55]\n\tds_read_b64_tr_b16 v[32:33], v6 offset:24576\n\t" \
    "ds_read_b64_tr_b16 v[34:35], v6 offset:26624\n\tv_mfma_f32_16x16x32_bf16 v[52:55], a[68:71], v[44:47], v[52:55]\n\tv_fma_f32 v98, v74, v216, v217\n\tv_fma_f32 v99, v75, v216, v217\n\tv_mfma_f32_16x16x32_bf16 v[60:63], a[72:75], v[44:47], v[60:63]\n\tv_fma_f32 v100, v76, v218, v219\n\tv_fma_f32 v101, v77, v218, v219\n\tv_mfma_f32_16x16x32_bf16 v[60:63], a[60:63], v[40:43], v[60:63]\n\t" \
    "ds_read_b64_tr_b16 v[36:37], v6 offset:28672\n\tds_read_b64_tr_b16 v[38:39], v6 offset:30720\n\tv_mfma_f32_16x16x32_bf16 v[68:71], a[64:67], v[40:43], v[68:71]\n\tv_fma_f32 v102, v78, v218, v219\n\tv_fma_f32 v103, v79, v218, v219\n\tv_mfma_f32_16x16x32_bf16 v[68:71], a[76:79], v[44:47], v[68:71]\n\tv_fma_f32 v104, v80, v216, v217\n\tv_fma_f32 v105, v81, v216, v217\n\t" \
    "s_waitcnt lgkmcnt(8)\n\tv_mfma_f32_16x16x32_bf16 v[48:51], a[80:83], v[16:19], v[48:51]\n\tds_read_b64_tr_b16 v[40:41], v7 offset:24576\n\tds_read_b64_tr_b16 v[42:43], v7 offset:26624\n\tv_mfma_f32_16x16x32_bf16 v[48:51], a[92:95], v[20:23], v[48:51]\n\tv_fma_f32 v106, v82, v216, v217\n\tv_fma_f32 v107, v83, v216, v217\n\tv_mfma_f32_16x16x32_bf16 v[56:59], a[96:99], v[20:23], v[56:59]\n\t" \
    "v_fma_f32 v108, v84, v218, v219\n\tv_fma_f32 v109, v85, v218, v219\n\tv_mfma_f32_16x16x32_bf16 v[56:59], a[84:87], v[16:19], v[56:59]\n\tds_read_b64_tr_b16 v[44:45], v7 offset:28672\n\tds_read_b64_tr_b16 v[46:47], v7 offset:30720\n\tv_mfma_f32_16x16x32_bf16 v[64:67], a[88:91], v[16:19], v[64:67]\n\tv_fma_f32 v110, v86, v218, v219\n\tv_fma_f32 v111, v87, v218, v219\n\t" \
    "v_mfma_f32_16x16x32_bf16 v[64:67], a[100:103], v[20:23], v[64:67]\n\tv_fma_f32 v112, v88, v216, v217\n\tv_fma_f32 v113, v89, v216, v217\n\ts_waitcnt lgkmcnt(8)\n\tv_mfma_f32_16x16x32_bf16 v[52:55], a[80:83], v[24:27], v[52:55]\n\tds_read_b64_tr_b16 v[16:17], v6 offset:32768\n\tds_read_b64_tr_b16 v[18:19], v6 offset:34816\n\tv_mfma_f32_16x16x32_bf16 v[52:55], a[92:95], v[28:31], v[52:55]\n\t" \
    "v_fma_f32 v114, v90, v216, v217\n\tv_fma_f32 v115, v91, v216, v217\n\tv_mfma_f32_16x16x32_bf16 v[60:63], a[96:99], v[28:31], v[60:63]\n\tv_fma_f32 v116, v92, v218, v219\n\tv_fma_f32 v117, v93, v218, v219\n\tv_mfma_f32_16x16x32_bf16 v[60:63], a[84:87], v[24:27], v[60:63]\n\tds_read_b64_tr_b16 v[20:21], v6 offset:36864\n\tds_read_b64_tr_b16 v[22:23], v6 offset:38912\n\t" \
    "v_mfma_f32_16x16x32_bf16 v[68:71], a[88:91], v[24:27], v[68:71]\n\tv_fma_f32 v118, v94, v218, v219\n\tv_fma_f32 v119, v95, v218, v219\n\tv_mfma_f32_16x16x32_bf16 v[68:71], a[100:103], v[28:31], v[68:71]\n\ts_waitcnt lgkmcnt(8)\n\tv_mfma_f32_16x16x32_bf16 v[48:51], a[104:107], v[32:35], v[48:51]\n\tds_read_b64_tr_b16 v[24:25], v7 offset:32768\n\tds_read_b64_tr_b16 v[26:27], v7 offset:34816\n\t" \
    "v_mfma_f32_16x16x32_bf16 v[48:51], a[116:119], v[36:39], v[48:51]\n\tv_mfma_f32_16x16x32_bf16 v[56:59], a[120:123], v[36:39], v[56:59]\n\tv_mfma_f32_16x16x32_bf16 v[56:59], a[108:111], v[32:35], v[56:59]\n\tds_read_b64_tr_b16 v[28:29], v7 offset:36864\n\tds_read_b64_tr_b16 v[30:31], v7 offset:38912\n\tv_mfma_f32_16x16x32_bf16 v[64:67], a[112:115], v[32:35], v[64:67]\n\tv_mfma_f32_16x16x32_bf16 v[64:67], a[124:127], v[36:39], v[64:67]\n\ts_waitcnt lgkmcnt(8)\n\t" \
    "v_mfma_f32_16x16x32_bf16 v[52:55], a[104:107], v[40:43], v[52:55]\n\tds_read_b64_tr_b16 v[32:33], v6 offset:40960\n\tds_read_b64_tr_b16 v[34:35], v6 offset:43008\n\tv_mfma_f32_16x16x32_bf16 v[52:55], a[116:119], v[44:47], v[52:55]\n\tv_mfma_f32_16x16x32_bf16 v[60:63], a[120:123], v[44:47], v[60:63]\n\tv_mfma_f32_16x16x32_bf16 v[60:63], a[108:111], v[40:43], v[60:63]\n\tds_read_b64_tr_b16 v[36:37], v6 offset:45056\n\tds_read_b64_tr_b16 v[38:39], v6 offset:47104\n\t" \
    "v_mfma_f32_16x16x32_bf16 v[68:71], a[112:115], v[40:43], v[68:71]\n\tv_mfma_f32_16x16x32_bf16 v[68:71], a[124:127], v[44:47], v[68:71]\n\ts_waitcnt lgkmcnt(8)\n\tv_mfma_f32_16x16x32_bf16 v[48:51], a[128:131], v[16:19], v[48:51]\n\tds_read_b64_tr_b16 v[40:41], v7 offset:40960\n\tds_read_b64_tr_b16 v[42:43], v7 offset:43008\n\tv_mfma_f32_16x16x32_bf16 v[48:51], a[140:143], v[20:23], v[48:51]\n\tv_mfma_f32_16x16x32_bf16 v[56:59], a[144:147], v[20:23], v[56:59]\n\t" \
    "v_mfma_f32_16x16x32_bf16 v[56:59], a[132:135], v[16:19], v[56:59]\n\tds_read_b64_tr_b16 v[44:45], v7 offset:45056\n\tds_read_b64_tr_b16 v[46:47], v7 offset:47104\n\tv_mfma_f32_16x16x32_bf16 v[64:67], a[136:139], v[16:19], v[64:67]\n\tv_mfma_f32_16x16x32_bf16 v[64:67], a[148:151], v[20:23], v[64:67]\n\ts_waitcnt lgkmcnt(8)\n\tv_mfma_f32_16x16x32_bf16 v[52:55], a[128:131], v[24:27], v[52:55]\n\tds_read_b64_tr_b16 v[16:17], v6 offset:49152\n\t" \
    "ds_read_b64_tr_b16 v[18:19], v6 offset:51200\n\ts_add_u32 s68, s55, 0\n\tv_mfma_f32_16x16x32_bf16 v[52:55], a[140:143], v[28:31], v[52:55]\n\tbuffer_load_dwordx4 v[120:123], v8, s[8:11], s68 offen offset:0 sc1\n\tv_mfma_f32_16x16x32_bf16 v[60:63], a[144:147], v[28:31], v[60:63]\n\tv_mfma_f32_16x16x32_bf16 v[60:63], a[132:135], v[24:27], v[60:63]\n\tds_read_b64_tr_b16 v[20:21], v6 offset:53248\n\tds_read_b64_tr_b16 v[22:23], v6 offset:55296\n\t" \
    "v_mfma_f32_16x16x32_bf16 v[68:71], a[136:139], v[24:27], v[68:71]\n\tbuffer_load_dwordx4 v[124:127], v8, s[8:11], s68 offen offset:1024 sc1\n\tv_mfma_f32_16x16x32_bf16 v[68:71], a[148:151], v[28:31], v[68:71]\n\ts_waitcnt lgkmcnt(8)\n\tv_mfma_f32_16x16x32_bf16 v[48:51], a[152:155], v[32:35], v[48:51]\n\tds_read_b64_tr_b16 v[24:25], v7 offset:49152\n\tds_read_b64_tr_b16 v[26:27], v7 offset:51200\n\tv_mfma_f32_16x16x32_bf16 v[48:51], a[164:167], v[36:39], v[48:51]\n\t" \
    "buffer_load_dwordx4 v[128:131], v8, s[8:11], s68 offen offset:2048 sc1\n\tv_mfma_f32_16x16x32_bf16 v[56:59], a[168:171], v[36:39], v[56:59]\n\tv_mfma_f32_16x16x32_bf16 v[56:59], a[156:159], v[32:35], v[56:59]\n\tds_read_b64_tr_b16 v[28:29], v7 offset:53248\n\tds_read_b64_tr_b16 v[30:31], v7 offset:55296\n\tv_mfma_f32_16x16x32_bf16 v[64:67], a[160:163], v[32:35], v[64:67]\n\tbuffer_load_dword v132, v236, s[8:11], s68 offen offset:3072 sc1\n\tv_mfma_f32_16x16x32_bf16 v[64:67], a[172:175], v[36:39], v[64:67]\n\t" \
    "s_waitcnt lgkmcnt(8)\n\tv_mfma_f32_16x16x32_bf16 v[52:55], a[152:155], v[40:43], v[52:55]\n\tds_read_b64_tr_b16 v[32:33], v6 offset:57344\n\tds_read_b64_tr_b16 v[34:35], v6 offset:59392\n\tv_mfma_f32_16x16x32_bf16 v[52:55], a[164:167], v[44:47], v[52:55]\n\tv_mfma_f32_16x16x32_bf16 v[60:63], a[168:171], v[44:47], v[60:63]\n\tv_mfma_f32_16x16x32_bf16 v[60:63], a[156:159], v[40:43], v[60:63]\n\tds_read_b64_tr_b16 v[36:37], v6 offset:61440\n\t" \
    "ds_read_b64_tr_b16 v[38:39], v6 offset:63488\n\tv_mfma_f32_16x16x32_bf16 v[68:71], a[160:163], v[40:43], v[68:71]\n\tv_mfma_f32_16x16x32_bf16 v[68:71], a[172:175], v[44:47], v[68:71]\n\ts_waitcnt lgkmcnt(8)\n\tv_mfma_f32_16x16x32_bf16 v[48:51], a[176:179], v[16:19], v[48:51]\n\tds_read_b64_tr_b16 v[40:41], v7 offset:57344\n\tds_read_b64_tr_b16 v[42:43], v7 offset:59392\n\ts_add_u32 s68, s55, 3328\n\t" \
    "v_mfma_f32_16x16x32_bf16 v[48:51], a[188:191], v[20:23], v[48:51]\n\tbuffer_load_dwordx4 v[144:147], v8, s[8:11], s68 offen offset:0 sc1\n\tv_mfma_f32_16x16x32_bf16 v[56:59], a[192:195], v[20:23], v[56:59]\n\tv_mfma_f32_16x16x32_bf16 v[56:59], a[180:183], v[16:19], v[56:59]\n\tds_read_b64_tr_b16 v[44:45], v7 offset:61440\n\tds_read_b64_tr_b16 v[46:47], v7 offset:63488\n\tv_mfma_f32_16x16x32_bf16 v[64:67], a[184:187], v[16:19], v[64:67]\n\tbuffer_load_dwordx4 v[148:151], v8, s[8:11], s68 offen offset:1024 sc1\n\t" \
    "v_mfma_f32_16x16x32_bf16 v[64:67], a[196:199], v[20:23], v[64:67]\n\ts_waitcnt lgkmcnt(8)\n\tv_mfma_f32_16x16x32_bf16 v[52:55], a[176:179], v[24:27], v[52:55]\n\tv_mfma_f32_16x16x32_bf16 v[52:55], a[188:191], v[28:31], v[52:55]\n\tbuffer_load_dwordx4 v[152:155], v8, s[8:11], s68 offen offset:2048 sc1\n\tv_mfma_f32_16x16x32_bf16 v[60:63], a[192:195], v[28:31], v[60:63]\n\tv_mfma_f32_16x16x32_bf16 v[60:63], a[180:183], v[24:27], v[60:63]\n\tv_mfma_f32_16x16x32_bf16 v[68:71], a[184:187], v[24:27], v[68:71]\n\t" \
    "buffer_load_dword v156, v236, s[8:11], s68 offen offset:3072 sc1\n\tv_mfma_f32_16x16x32_bf16 v[68:71], a[196:199], v[28:31], v[68:71]\n\ts_waitcnt lgkmcnt(4)\n\tv_mfma_f32_16x16x32_bf16 v[48:51], a[200:203], v[32:35], v[48:51]\n\ts_add_u32 s68, s55, 6656\n\tv_mfma_f32_16x16x32_bf16 v[48:51], a[212:215], v[36:39], v[48:51]\n\tbuffer_load_dwordx4 v[168:171], v8, s[8:11], s68 offen offset:0 sc1\n\tv_mfma_f32_16x16x32_bf16 v[56:59], a[216:219], v[36:39], v[56:59]\n\t" \
    "v_mfma_f32_16x16x32_bf16 v[56:59], a[204:207], v[32:35], v[56:59]\n\tv_mfma_f32_16x16x32_bf16 v[64:67], a[208:211], v[32:35], v[64:67]\n\tbuffer_load_dwordx4 v[172:175], v8, s[8:11], s68 offen offset:1024 sc1\n\tv_mfma_f32_16x16x32_bf16 v[64:67], a[220:223], v[36:39], v[64:67]\n\ts_waitcnt lgkmcnt(0)\n\tv_mfma_f32_16x16x32_bf16 v[52:55], a[200:203], v[40:43], v[52:55]\n\tv_mfma_f32_16x16x32_bf16 v[52:55], a[212:215], v[44:47], v[52:55]\n\tbuffer_load_dwordx4 v[176:179], v8, s[8:11], s68 offen offset:2048 sc1\n\t" \
    "v_mfma_f32_16x16x32_bf16 v[60:63], a[216:219], v[44:47], v[60:63]\n\tv_mfma_f32_16x16x32_bf16 v[60:63], a[204:207], v[40:43], v[60:63]\n\tv_mfma_f32_16x16x32_bf16 v[68:71], a[208:211], v[40:43], v[68:71]\n\tbuffer_load_dword v180, v236, s[8:11], s68 offen offset:3072 sc1\n\tv_mfma_f32_16x16x32_bf16 v[68:71], a[220:223], v[44:47], v[68:71]\n\ts_cmp_eq_u32 s70, 0\n\ts_cbranch_scc1 116f\n\tv_cmp_ne_u64 vcc, s[36:37], v[250:251]\n\t" \
    "s_cbranch_vccz 118f\n\ts_mov_b32 s71, 1048576\n\t117:\n\ts_sleep 1\n\tbuffer_load_dwordx2 v[250:251], v241, s[8:11], s51 offen sc1\n\ts_waitcnt vmcnt(0)\n\tv_cmp_ne_u64 vcc, s[36:37], v[250:251]\n\ts_cbranch_vccz 118f\n\t" \
    "s_sub_u32 s71, s71, 1\n\ts_cbranch_scc0 117b\n\t118:\n\tbuffer_load_dwordx4 v[204:207], v8, s[8:11], s56 offen offset:0 sc1\n\tbuffer_load_dwordx4 v[208:211], v8, s[8:11], s56 offen offset:1024 sc1\n\tbuffer_load_dwordx4 v[212:215], v8, s[8:11], s56 offen offset:2048 sc1\n\ts_waitcnt vmcnt(0)\n\t116:\n\t" \
    "s_waitcnt vmcnt(16)\n\tv_lshlrev_b32 v16, 16, v204\n\tv_and_b32 v17, 0xffff0000, v204\n\tv_lshlrev_b32 v18, 16, v205\n\tv_and_b32 v19, 0xffff0000, v205\n\tv_lshlrev_b32 v20, 16, v206\n\tv_and_b32 v21, 0xffff0000, v206\n\tv_lshlrev_b32 v22, 16, v207\n\t" \
    "v_and_b32 v23, 0xffff0000, v207\n\tv_lshlrev_b32 v24, 16, v208\n\tv_and_b32 v25, 0xffff0000, v208\n\tv_lshlrev_b32 v26, 16, v209\n\tv_and_b32 v27, 0xffff0000, v209\n\tv_lshlrev_b32 v28, 16, v210\n\tv_and_b32 v29, 0xffff0000, v210\n\tv_lshlrev_b32 v30, 16, v211\n\t" \
    "v_and_b32 v31, 0xffff0000, v211\n\tv_lshlrev_b32 v32, 16, v212\n\tv_and_b32 v33, 0xffff0000, v212\n\tv_lshlrev_b32 v34, 16, v213\n\tv_and_b32 v35, 0xffff0000, v213\n\tv_lshlrev_b32 v36, 16, v214\n\tv_and_b32 v37, 0xffff0000, v214\n\tv_lshlrev_b32 v38, 16, v215\n\t" \
    "v_and_b32 v39, 0xffff0000, v215\n\tv_pk_mul_f32 v[96:97], v[96:97], v[16:17]\n\tv_pk_mul_f32 v[98:99], v[98:99], v[18:19]\n\tv_pk_mul_f32 v[100:101], v[100:101], v[20:21]\n\tv_pk_mul_f32 v[102:103], v[102:103], v[22:23]\n\tv_pk_mul_f32 v[104:105], v[104:105], v[24:25]\n\tv_pk_mul_f32 v[106:107], v[106:107], v[26:27]\n\tv_pk_mul_f32 v[108:109], v[108:109], v[28:29]\n\t" \
    "v_pk_mul_f32 v[110:111], v[110:111], v[30:31]\n\tv_pk_mul_f32 v[112:113], v[112:113], v[32:33]\n\tv_pk_mul_f32 v[114:115], v[114:115], v[34:35]\n\tv_pk_mul_f32 v[116:117], v[116:117], v[36:37]\n\tv_pk_mul_f32 v[118:119], v[118:119], v[38:39]\n\ts_mov_b32 s71, 1048576\n\ts_waitcnt vmcnt(8)\n\t119:\n\t" \
    "v_add3_u32 v134, s36, v120, v121\n\tv_add3_u32 v135, v122, v123, v124\n\tv_add3_u32 v134, v134, v125, v126\n\tv_add3_u32 v135, v135, v127, v128\n\tv_add3_u32 v134, v134, v129, v130\n\tv_add3_u32 v134, v134, v135, v131\n\tv_cmp_ne_u32 vcc, v134, v132\n\ts_cbranch_vccz 120f\n\t" \
    "s_sub_u32 s71, s71, 1\n\ts_cbranch_scc1 120f\n\ts_sleep 6\n\ts_add_u32 s73, s55, 0\n\tbuffer_load_dwordx4 v[120:123], v8, s[8:11], s73 offen offset:0 sc1\n\tbuffer_load_dwordx4 v[124:127], v8, s[8:11], s73 offen offset:1024 sc1\n\tbuffer_load_dwordx4 v[128:131], v8, s[8:11], s73 offen offset:2048 sc1\n\tbuffer_load_dword v132, v236, s[8:11], s73 offen offset:3072 sc1\n\t" \
    "s_waitcnt vmcnt(0)\n\ts_branch 119b\n\t120:\n\tv_lshlrev_b32 v16, 16, v120\n\tv_and_b32 v17, 0xffff0000, v120\n\tv_lshlrev_b32 v18, 16, v121\n\tv_and_b32 v19, 0xffff0000, v121\n\tv_lshlrev_b32 v20, 16, v122\n\t" \
    "v_and_b32 v21, 0xffff0000, v122\n\tv_lshlrev_b32 v22, 16, v123\n\tv_and_b32 v23, 0xffff0000, v123\n\tv_lshlrev_b32 v24, 16, v124\n\tv_and_b32 v25, 0xffff0000, v124\n\tv_lshlrev_b32 v26, 16, v125\n\tv_and_b32 v27, 0xffff0000, v125\n\tv_lshlrev_b32 v28, 16, v126\n\t" \
    "v_and_b32 v29, 0xffff0000, v126\n\tv_lshlrev_b32 v30, 16, v127\n\tv_and_b32 v31, 0xffff0000, v127\n\tv_lshlrev_b32 v32, 16, v128\n\tv_and_b32 v33, 0xffff0000, v128\n\tv_lshlrev_b32 v34, 16, v129\n\tv_and_b32 v35, 0xffff0000, v129\n\tv_lshlrev_b32 v36, 16, v130\n\t" \
    "v_and_b32 v37, 0xffff0000, v130\n\tv_lshlrev_b32 v38, 16, v131\n\tv_and_b32 v39, 0xffff0000, v131\n\tv_pk_add_f32 v[48:49], v[48:49], v[16:17]\n\tv_pk_add_f32 v[50:51], v[50:51], v[18:19]\n\tv_pk_add_f32 v[52:53], v[52:53], v[20:21]\n\tv_pk_add_f32 v[54:55], v[54:55], v[22:23]\n\tv_pk_add_f32 v[56:57], v[56:57], v[24:25]\n\t" \
    "v_pk_add_f32 v[58:59], v[58:59], v[26:27]\n\tv_pk_add_f32 v[60:61], v[60:61], v[28:29]\n\tv_pk_add_f32 v[62:63], v[62:63], v[30:31]\n\tv_pk_add_f32 v[64:65], v[64:65], v[32:33]\n\tv_pk_add_f32 v[66:67], v[66:67], v[34:35]\n\tv_pk_add_f32 v[68:69], v[68:69], v[36:37]\n\tv_pk_add_f32 v[70:71], v[70:71], v[38:39]\n\ts_mov_b32 s71, 1048576\n\t" \
    "s_waitcnt vmcnt(4)\n\t121:\n\tv_add3_u32 v158, s36, v144, v145\n\tv_add3_u32 v159, v146, v147, v148\n\tv_add3_u32 v158, v158, v149, v150\n\tv_add3_u32 v159, v159, v151, v152\n\tv_add3_u32 v158, v158, v153, v154\n\tv_add3_u32 v158, v158, v159, v155\n\t" \
    "v_cmp_ne_u32 vcc, v158, v156\n\ts_cbranch_vccz 122f\n\ts_sub_u32 s71, s71, 1\n\ts_cbranch_scc1 122f\n\ts_sleep 6\n\ts_add_u32 s73, s55, 3328\n\tbuffer_load_dwordx4 v[144:147], v8, s[8:11], s73 offen offset:0 sc1\n\tbuffer_load_dwordx4 v[148:151], v8, s[8:11], s73 offen offset:1024 sc1\n\t" \
    "buffer_load_dwordx4 v[152:155], v8, s[8:11], s73 offen offset:2048 sc1\n\tbuffer_load_dword v156, v236, s[8:11], s73 offen offset:3072 sc1\n\ts_waitcnt vmcnt(0)\n\ts_branch 121b\n\t122:\n\tv_lshlrev_b32 v16, 16, v144\n\tv_and_b32 v17, 0xffff0000, v144\n\tv_lshlrev_b32 v18, 16, v145\n\t" \
    "v_and_b32 v19, 0xffff0000, v145\n\tv_lshlrev_b32 v20, 16, v146\n\tv_and_b32 v21, 0xffff0000, v146\n\tv_lshlrev_b32 v22, 16, v147\n\tv_and_b32 v23, 0xffff0000, v147\n\tv_lshlrev_b32 v24, 16, v148\n\tv_and_b32 v25, 0xffff0000, v148\n\tv_lshlrev_b32 v26, 16, v149\n\t" \
    "v_and_b32 v27, 0xffff0000, v149\n\tv_lshlrev_b32 v28, 16, v150\n\tv_and_b32 v29, 0xffff0000, v150\n\tv_lshlrev_b32 v30, 16, v151\n\tv_and_b32 v31, 0xffff0000, v151\n\tv_lshlrev_b32 v32, 16, v152\n\tv_and_b32 v33, 0xffff0000, v152\n\tv_lshlrev_b32 v34, 16, v153\n\t" \
    "v_and_b32 v35, 0xffff0000, v153\n\tv_lshlrev_b32 v36, 16, v154\n\tv_and_b32 v37, 0xffff0000, v154\n\tv_lshlrev_b32 v38, 16, v155\n\tv_and_b32 v39, 0xffff0000, v155\n\tv_pk_add_f32 v[48:49], v[48:49], v[16:17]\n\tv_pk_add_f32 v[50:51], v[50:51], v[18:19]\n\tv_pk_add_f32 v[52:53], v[52:53], v[20:21]\n\t" \
    "v_pk_add_f32 v[54:55], v[54:55], v[22:23]\n\tv_pk_add_f32 v[56:57], v[56:57], v[24:25]\n\tv_pk_add_f32 v[58:59], v[58:59], v[26:27]\n\tv_pk_add_f32 v[60:61], v[60:61], v[28:29]\n\tv_pk_add_f32 v[62:63], v[62:63], v[30:31]\n\tv_pk_add_f32 v[64:65], v[64:65], v[32:33]\n\tv_pk_add_f32 v[66:67], v[66:67], v[34:35]\n\tv_pk_add_f32 v[68:69], v[68:69], v[36:37]\n\t" \
    "v_pk_add_f32 v[70:71], v[70:71], v[38:39]\n\ts_mov_b32 s71, 1048576\n\ts_waitcnt vmcnt(0)\n\t123:\n\tv_add3_u32 v182, s36, v168, v169\n\tv_add3_u32 v183, v170, v171, v172\n\tv_add3_u32 v182, v182, v173, v174\n\tv_add3_u32 v183, v183, v175, v176\n\t" \
    "v_add3_u32 v182, v182, v177, v178\n\tv_add3_u32 v182, v182, v183, v179\n\tv_cmp_ne_u32 vcc, v182, v180\n\ts_cbranch_vccz 124f\n\ts_sub_u32 s71, s71, 1\n\ts_cbranch_scc1 124f\n\ts_sleep 6\n\ts_add_u32 s73, s55, 6656\n\t" \
    "buffer_load_dwordx4 v[168:171], v8, s[8:11], s73 offen offset:0 sc1\n\tbuffer_load_dwordx4 v[172:175], v8, s[8:11], s73 offen offset:1024 sc1\n\tbuffer_load_dwordx4 v[176:179], v8, s[8:11], s73 offen offset:2048 sc1\n\tbuffer_load_dword v180, v236, s[8:11], s73 offen offset:3072 sc1\n\ts_waitcnt vmcnt(0)\n\ts_branch 123b\n\t124:\n\ts_mov_b32 s68, 0xbfb8aa3b\n\t" \
    "s_mov_b32 s69, 0xbfb8aa3b\n\tv_lshlrev_b32 v16, 16, v168\n\tv_and_b32 v17, 0xffff0000, v168\n\tv_lshlrev_b32 v18, 16, v169\n\tv_and_b32 v19, 0xffff0000, v169\n\tv_lshlrev_b32 v20, 16, v170\n\tv_and_b32 v21, 0xffff0000, v170\n\tv_lshlrev_b32 v22, 16, v171\n\t" \
    "v_and_b32 v23, 0xffff0000, v171\n\tv_pk_add_f32 v[48:49], v[48:49], v[16:17]\n\tv_pk_add_f32 v[50:51], v[50:51], v[18:19]\n\tv_pk_add_f32 v[52:53], v[52:53], v[20:21]\n\tv_pk_add_f32 v[54:55], v[54:55], v[22:23]\n\tv_pk_mul_f32 v[16:17], v[48:49], s[68:69]\n\tv_pk_mul_f32 v[18:19], v[50:51], s[68:69]\n\tv_pk_mul_f32 v[20:21], v[52:53], s[68:69]\n\t" \
    "v_pk_mul_f32 v[22:23], v[54:55], s[68:69]\n\tv_lshlrev_b32 v24, 16, v172\n\tv_exp_f32 v16, v16\n\tv_and_b32 v25, 0xffff0000, v172\n\tv_exp_f32 v17, v17\n\tv_lshlrev_b32 v26, 16, v173\n\tv_exp_f32 v18, v18\n\tv_and_b32 v27, 0xffff0000, v173\n\t" \
    "v_exp_f32 v19, v19\n\tv_lshlrev_b32 v28, 16, v174\n\tv_exp_f32 v20, v20\n\tv_and_b32 v29, 0xffff0000, v174\n\tv_exp_f32 v21, v21\n\tv_lshlrev_b32 v30, 16, v175\n\tv_exp_f32 v22, v22\n\tv_and_b32 v31, 0xffff0000, v175\n\t" \
    "v_exp_f32 v23, v23\n\tv_pk_add_f32 v[56:57], v[56:57], v[24:25]\n\tv_pk_add_f32 v[16:17], v[16:17], 1.0 op_sel_hi:[1,0]\n\tv_pk_add_f32 v[18:19], v[18:19], 1.0 op_sel_hi:[1,0]\n\tv_pk_add_f32 v[20:21], v[20:21], 1.0 op_sel_hi:[1,0]\n\tv_pk_add_f32 v[22:23], v[22:23], 1.0 op_sel_hi:[1,0]\n\tv_pk_add_f32 v[58:59], v[58:59], v[26:27]\n\tv_rcp_f32 v16, v16\n\t" \
    "v_pk_add_f32 v[60:61], v[60:61], v[28:29]\n\tv_rcp_f32 v17, v17\n\tv_pk_add_f32 v[62:63], v[62:63], v[30:31]\n\tv_rcp_f32 v18, v18\n\tv_pk_mul_f32 v[24:25], v[56:57], s[68:69]\n\tv_rcp_f32 v19, v19\n\tv_pk_mul_f32 v[26:27], v[58:59], s[68:69]\n\tv_rcp_f32 v20, v20\n\t" \
    "v_pk_mul_f32 v[28:29], v[60:61], s[68:69]\n\tv_rcp_f32 v21, v21\n\tv_pk_mul_f32 v[30:31], v[62:63], s[68:69]\n\tv_rcp_f32 v22, v22\n\tv_lshlrev_b32 v32, 16, v176\n\tv_rcp_f32 v23, v23\n\tv_and_b32 v33, 0xffff0000, v176\n\tv_exp_f32 v24, v24\n\t" \
    "v_pk_mul_f32 v[48:49], v[48:49], v[96:97]\n\tv_exp_f32 v25, v25\n\tv_pk_mul_f32 v[50:51], v[50:51], v[98:99]\n\tv_exp_f32 v26, v26\n\tv_pk_mul_f32 v[52:53], v[52:53], v[100:101]\n\tv_exp_f32 v27, v27\n\tv_pk_mul_f32 v[54:55], v[54:55], v[102:103]\n\tv_exp_f32 v28, v28\n\t" \
    "v_pk_fma_f32 v[16:17], v[16:17], v[48:49], v[72:73]\n\tv_exp_f32 v29, v29\n\tv_pk_fma_f32 v[18:19], v[18:19], v[50:51], v[74:75]\n\tv_exp_f32 v30, v30\n\tv_pk_fma_f32 v[20:21], v[20:21], v[52:53], v[76:77]\n\tv_exp_f32 v31, v31\n\tv_pk_fma_f32 v[22:23], v[22:23], v[54:55], v[78:79]\n\tv_cvt_pk_bf16_f32 v48, v16, v17\n\t" \
    "v_cvt_pk_bf16_f32 v49, v18, v19\n\tv_cvt_pk_bf16_f32 v50, v20, v21\n\tv_cvt_pk_bf16_f32 v51, v22, v23\n\ts_nop 0\n\tv_permlane16_swap_b32 v48, v50\n\tv_permlane16_swap_b32 v49, v51\n\tbuffer_store_dwordx4 v[48:51], v243, s[24:27], 0 offen offset:0\n\tv_pk_add_f32 v[24:25], v[24:25], 1.0 op_sel_hi:[1,0]\n\t" \
    "v_pk_add_f32 v[26:27], v[26:27], 1.0 op_sel_hi:[1,0]\n\tv_pk_add_f32 v[28:29], v[28:29], 1.0 op_sel_hi:[1,0]\n\tv_pk_add_f32 v[30:31], v[30:31], 1.0 op_sel_hi:[1,0]\n\tv_lshlrev_b32 v34, 16, v177\n\tv_rcp_f32 v24, v24\n\tv_and_b32 v35, 0xffff0000, v177\n\tv_rcp_f32 v25, v25\n\tv_lshlrev_b32 v36, 16, v178\n\t" \
    "v_rcp_f32 v26, v26\n\tv_and_b32 v37, 0xffff0000, v178\n\tv_rcp_f32 v27, v27\n\tv_lshlrev_b32 v38, 16, v179\n\tv_rcp_f32 v28, v28\n\tv_and_b32 v39, 0xffff0000, v179\n\tv_rcp_f32 v29, v29\n\tv_pk_add_f32 v[64:65], v[64:65], v[32:33]\n\t" \
    "v_rcp_f32 v30, v30\n\tv_pk_add_f32 v[66:67], v[66:67], v[34:35]\n\tv_rcp_f32 v31, v31\n\tv_pk_add_f32 v[68:69], v[68:69], v[36:37]\n\tv_pk_mul_f32 v[56:57], v[56:57], v[104:105]\n\tv_pk_mul_f32 v[58:59], v[58:59], v[106:107]\n\tv_pk_mul_f32 v[60:61], v[60:61], v[108:109]\n\tv_pk_mul_f32 v[62:63], v[62:63], v[110:111]\n\t" \
    "v_pk_fma_f32 v[24:25], v[24:25], v[56:57], v[80:81]\n\tv_pk_fma_f32 v[26:27], v[26:27], v[58:59], v[82:83]\n\tv_pk_fma_f32 v[28:29], v[28:29], v[60:61], v[84:85]\n\tv_pk_fma_f32 v[30:31], v[30:31], v[62:63], v[86:87]\n\tv_cvt_pk_bf16_f32 v56, v24, v25\n\tv_cvt_pk_bf16_f32 v57, v26, v27\n\tv_cvt_pk_bf16_f32 v58, v28, v29\n\tv_cvt_pk_bf16_f32 v59, v30, v31\n\t" \
    "s_nop 0\n\tv_permlane16_swap_b32 v56, v58\n\tv_permlane16_swap_b32 v57, v59\n\tbuffer_store_dwordx4 v[56:59], v243, s[24:27], 0 offen offset:32\n\tv_pk_add_f32 v[70:71], v[70:71], v[38:39]\n\tv_pk_mul_f32 v[32:33], v[64:65], s[68:69]\n\tv_pk_mul_f32 v[34:35], v[66:67], s[68:69]\n\tv_pk_mul_f32 v[36:37], v[68:69], s[68:69]\n\t" \
    "v_pk_mul_f32 v[38:39], v[70:71], s[68:69]\n\tv_exp_f32 v32, v32\n\tv_exp_f32 v33, v33\n\tv_exp_f32 v34, v34\n\tv_exp_f32 v35, v35\n\tv_exp_f32 v36, v36\n\tv_exp_f32 v37, v37\n\tv_exp_f32 v38, v38\n\t" \
    "v_exp_f32 v39, v39\n\tv_pk_add_f32 v[32:33], v[32:33], 1.0 op_sel_hi:[1,0]\n\tv_pk_add_f32 v[34:35], v[34:35], 1.0 op_sel_hi:[1,0]\n\tv_pk_add_f32 v[36:37], v[36:37], 1.0 op_sel_hi:[1,0]\n\tv_pk_add_f32 v[38:39], v[38:39], 1.0 op_sel_hi:[1,0]\n\tv_rcp_f32 v32, v32\n\tv_rcp_f32 v33, v33\n\tv_rcp_f32 v34, v34\n\t" \
    "v_rcp_f32 v35, v35\n\tv_rcp_f32 v36, v36\n\tv_rcp_f32 v37, v37\n\tv_rcp_f32 v38, v38\n\tv_rcp_f32 v39, v39\n\tv_pk_mul_f32 v[64:65], v[64:65], v[112:113]\n\tv_pk_mul_f32 v[66:67], v[66:67], v[114:115]\n\tv_pk_mul_f32 v[68:69], v[68:69], v[116:117]\n\t" \
    "v_pk_mul_f32 v[70:71], v[70:71], v[118:119]\n\tv_pk_fma_f32 v[32:33], v[32:33], v[64:65], v[88:89]\n\tv_pk_fma_f32 v[34:35], v[34:35], v[66:67], v[90:91]\n\tv_pk_fma_f32 v[36:37], v[36:37], v[68:69], v[92:93]\n\tv_pk_fma_f32 v[38:39], v[38:39], v[70:71], v[94:95]\n\tv_cvt_pk_bf16_f32 v64, v32, v33\n\tv_cvt_pk_bf16_f32 v65, v34, v35\n\tv_cvt_pk_bf16_f32 v66, v36, v37\n\t" \
    "v_cvt_pk_bf16_f32 v67, v38, v39\n\ts_nop 0\n\tv_permlane16_swap_b32 v64, v66\n\tv_permlane16_swap_b32 v65, v67\n\tbuffer_store_dwordx4 v[64:67], v243, s[24:27], 0 offen offset:64\n\ts_branch 103f\n\t102:\n\tbuffer_store_dwordx4 v[72:75], v8, s[8:11], s45 offen offset:0 sc1\n\t" \
    "buffer_store_dwordx4 v[80:83], v8, s[8:11], s45 offen offset:1024 sc1\n\tbuffer_store_dwordx4 v[88:91], v8, s[8:11], s45 offen offset:2048 sc1\n\ts_add_u32 s49, s49, 4096\n\ts_waitcnt vmcnt(0) lgkmcnt(0)\n\tbuffer_store_dwordx2 v[248:249], off, s[8:11], s49 sc1\n\ts_load_dwordx8 s[12:19], s[0:1], 16\n\ts_lshr_b32 s67, s40, 3\n\ts_sub_u32 s67, s67, 24\n\t" \
    "s_lshl_b32 s67, s67, 2\n\ts_add_u32 s67, s67, s38\n\ts_lshl_b32 s65, s67, 6\n\tv_add_u32 v6, s65, v240\n\tv_mul_u32_u24 v7, 7282, v6\n\tv_lshrrev_b32 v7, 16, v7\n\tv_mul_u32_u24 v5, 9, v7\n\tv_sub_u32 v6, v6, v5\n\t" \
    "v_mul_u32_u24 v7, 9216, v7\n\tv_lshl_add_u32 v6, v6, 8, v7\n\tv_lshlrev_b32 v7, 3, v244\n\tv_lshl_add_u32 v7, v245, 12, v7\n\ts_and_b32 s65, s40, 7\n\ts_lshr_b32 s73, s65, 1\n\ts_mul_i32 s66, s73, 2304\n\ts_mul_i32 s69, s57, 9437184\n\t" \
    "s_add_u32 s66, s66, s69\n\ts_mul_i32 s73, s73, 4718592\n\ts_and_b32 s65, s65, 1\n\ts_lshl_b32 s65, s65, 11\n\ts_add_u32 s65, s65, s73\n\ts_waitcnt lgkmcnt(0)\n\ts_add_u32 s12, s12, s66\n\ts_addc_u32 s13, s13, 0\n\t" \
    "s_mov_b32 s14, 9430272\n\ts_add_u32 s16, s16, s65\n\ts_addc_u32 s17, s17, 0\n\ts_sub_u32 s18, s18, s65\n\tv_mov_b32 v238, 2146435072\n\tv_mov_b32 v236, 2146435072\n\ts_lshl_b32 s69, s57, 7\n\tv_mov_b32 v237, v240\n\t" \
    "v_mul_u32_u24 v237, 12, v237\n\ts_add_u32 s73, s67, 196\n\tv_add_u32 v237, s73, v237\n\tv_lshrrev_b32 v5, 3, v237\n\tv_lshlrev_b32 v5, 6, v5\n\tv_cmp_le_u32 vcc, 144, v237\n\tv_mov_b32 v4, 1120\n\tv_and_b32 v3, 7, v237\n\t" \
    "v_cndmask_b32 v4, 0, v4, vcc\n\tv_lshl_add_u32 v5, v3, 2, v5\n\tv_sub_u32 v5, v5, v4\n\tv_subrev_u32 v237, 196, v237\n\tv_cmp_gt_u32 vcc, 92, v237\n\tv_lshlrev_b32 v5, 12, v5\n\ts_mov_b32 s43, 0\n\tv_cndmask_b32 v237, v238, v5, vcc\n\t" \
    "s_nop 0\n\tv_readlane_b32 s41, v236, 0\n\tv_readlane_b32 s42, v237, 0\n\ts_nop 4\n\ts_mov_b32 s4, s2\n\ts_mov_b32 s5, s3\n\ts_mov_b32 s6, 98f-99b\n\ts_mov_b32 s7, 0x20000\n\t" \
    "v_lshlrev_b32 v14, 7, v240\n\ts_lshl_b32 s69, s67, 13\n\tbuffer_load_dword v15, v7, s[16:19], s42 offen\n\ts_add_u32 s43, s43, 1\n\tv_readlane_b32 s42, v237, s43\n\ts_nop 4\n\tbuffer_load_dword v15, v7, s[16:19], s42 offen\n\ts_add_u32 s43, s43, 1\n\t" \
    "v_readlane_b32 s42, v237, s43\n\ts_nop 4\n\tbuffer_load_dword v15, v7, s[16:19], s42 offen\n\ts_add_u32 s43, s43, 1\n\tv_readlane_b32 s42, v237, s43\n\ts_nop 4\n\tbuffer_load_dword v15, v14, s[4:7], s69 offen\n\tbuffer_load_dword v15, v7, s[16:19], s42 offen\n\t" \
    "s_add_u32 s43, s43, 1\n\tv_readlane_b32 s42, v237, s43\n\ts_nop 4\n\tbuffer_load_dword v15, v7, s[16:19], s42 offen\n\ts_add_u32 s43, s43, 1\n\tv_readlane_b32 s42, v237, s43\n\ts_nop 4\n\tbuffer_load_dword v15, v7, s[16:19], s42 offen\n\t" \
    "s_add_u32 s43, s43, 1\n\tv_readlane_b32 s42, v237, s43\n\ts_nop 4\n\tbuffer_load_dword v15, v7, s[16:19], s42 offen\n\ts_add_u32 s43, s43, 1\n\tv_readlane_b32 s42, v237, s43\n\ts_nop 4\n\tbuffer_load_dword v15, v7, s[16:19], s42 offen\n\t" \
    "s_add_u32 s43, s43, 1\n\tv_readlane_b32 s42, v237, s43\n\ts_nop 4\n\ts_waitcnt vmcnt(0)\n\ts_branch 103f\n\t101:\n\ts_load_dwordx8 s[12:19], s[0:1], 16\n\ts_lshr_b32 s67, s40, 3\n\t" \
    "s_sub_u32 s67, s67, 27\n\ts_lshl_b32 s67, s67, 2\n\ts_add_u32 s67, s67, s38\n\ts_lshl_b32 s65, s67, 6\n\tv_add_u32 v6, s65, v240\n\tv_mul_u32_u24 v7, 7282, v6\n\tv_lshrrev_b32 v7, 16, v7\n\tv_mul_u32_u24 v5, 9, v7\n\t" \
    "v_sub_u32 v6, v6, v5\n\tv_mul_u32_u24 v7, 9216, v7\n\tv_lshl_add_u32 v6, v6, 8, v7\n\tv_lshlrev_b32 v7, 3, v244\n\tv_lshl_add_u32 v7, v245, 12, v7\n\ts_and_b32 s65, s40, 7\n\ts_lshr_b32 s73, s65, 1\n\ts_mul_i32 s66, s73, 2304\n\t" \
    "s_mul_i32 s69, s57, 9437184\n\ts_add_u32 s66, s66, s69\n\ts_mul_i32 s73, s73, 4718592\n\ts_and_b32 s65, s65, 1\n\ts_lshl_b32 s65, s65, 11\n\ts_add_u32 s65, s65, s73\n\ts_waitcnt lgkmcnt(0)\n\ts_add_u32 s12, s12, s66\n\t" \
    "s_addc_u32 s13, s13, 0\n\ts_mov_b32 s14, 9430272\n\ts_add_u32 s16, s16, s65\n\ts_addc_u32 s17, s17, 0\n\ts_sub_u32 s18, s18, s65\n\tv_mov_b32 v238, 2146435072\n\tv_mov_b32 v236, 2146435072\n\ts_lshl_b32 s69, s57, 7\n\t" \
    "v_mov_b32 v237, v240\n\ts_xor_b32 s73, s69, 6193280\n\tv_writelane_b32 v236, s73, 0\n\tv_writelane_b32 v237, 0, 0\n\ts_xor_b32 s73, s69, 7501952\n\tv_writelane_b32 v236, s73, 1\n\tv_writelane_b32 v237, 0, 1\n\ts_xor_b32 s73, s69, 8810624\n\t" \
    "v_writelane_b32 v236, s73, 2\n\tv_writelane_b32 v237, 0, 2\n\tv_writelane_b32 v237, 6, 3\n\ts_xor_b32 s73, s69, 6193152\n\tv_writelane_b32 v236, s73, 4\n\tv_writelane_b32 v237, 0, 4\n\ts_xor_b32 s73, s69, 7501824\n\tv_writelane_b32 v236, s73, 5\n\t" \
    "v_writelane_b32 v237, 0, 5\n\ts_xor_b32 s73, s69, 8810496\n\tv_writelane_b32 v236, s73, 6\n\tv_writelane_b32 v237, 0, 6\n\tv_mul_u32_u24 v237, 20, v237\n\ts_add_u32 s73, s67, -120\n\tv_add_u32 v237, s73, v237\n\tv_cmp_le_i32 vcc, 40, v237\n\t" \
    "v_add_u32 v5, 48, v237\n\ts_nop 0\n\tv_cndmask_b32 v237, v237, v5, vcc\n\tv_lshrrev_b32 v5, 3, v237\n\tv_lshlrev_b32 v5, 6, v5\n\tv_cmp_le_u32 vcc, 144, v237\n\tv_mov_b32 v4, 1120\n\tv_and_b32 v3, 7, v237\n\t" \
    "v_cndmask_b32 v4, 0, v4, vcc\n\tv_lshl_add_u32 v5, v3, 2, v5\n\tv_sub_u32 v5, v5, v4\n\tv_subrev_u32 v237, 0, v237\n\tv_cmp_gt_u32 vcc, 196, v237\n\tv_lshlrev_b32 v5, 12, v5\n\ts_mov_b32 s43, 0\n\tv_cndmask_b32 v237, v238, v5, vcc\n\t" \
    "s_nop 0\n\tv_readlane_b32 s41, v236, 0\n\tv_readlane_b32 s42, v237, 0\n\ts_nop 4\n\ts_sub_u32 s65, s40, 216\n\ts_lshl_b32 s44, s65, 4\n\ts_mul_i32 s66, s65, 12\n\ts_add_u32 s66, s66, 96\n\t" \
    "s_cmp_lt_u32 s65, 24\n\ts_cselect_b32 s44, s44, s66\n\ts_cselect_b32 s45, 0, 2146435072\n\tv_cmp_eq_u32 vcc, 0, v240\n\tv_subrev_u32 v13, 8, v8\n\tv_mov_b32 v12, 2146435072\n\tv_cndmask_b32 v13, v13, v12, vcc\n\ts_mov_b32 s4, s2\n\t" \
    "s_mov_b32 s5, s3\n\ts_mov_b32 s6, 98f-99b\n\ts_mov_b32 s7, 0x20000\n\tv_lshlrev_b32 v14, 7, v240\n\ts_mov_b32 s46, 0xffff\n\tv_and_b32 v204, 15, v240\n\tv_lshlrev_b32 v205, 3, v245\n\tv_sub_u32 v206, v205, v204\n\t" \
    "v_add_u32 v206, 31, v206\n\tv_and_b32 v207, 3, v206\n\tv_mul_u32_u24 v207, 4160, v207\n\tv_and_b32 v206, 0xfc, v206\n\tv_lshl_add_u32 v0, v206, 1, v207\n\ts_mul_i32 s65, s38, 16640\n\ts_add_u32 s65, s65, 73984\n\tv_add_u32 v0, s65, v0\n\t" \
    "s_add_u32 s66, s65, 1040\n\tv_sub_u32 v2, s66, v8\n\tv_bfe_u32 v206, v240, 1, 2\n\tv_mul_u32_u24 v206, 4160, v206\n\tv_and_b32 v207, 16, v8\n\tv_add3_u32 v9, v206, v207, s65\n\ts_mul_i32 s66, s38, 4624\n\tv_lshl_add_u32 v3, v204, 5, v205\n\t" \
    "v_add_u32 v3, s66, v3\n\tv_add3_u32 v1, v3, v205, 32\n\tv_add_u32 v3, s66, v239\n\ts_lshl_b32 s73, s38, 4\n\tv_add_u32 v206, s73, v245\n\tv_bfe_u32 v207, v240, 2, 2\n\tv_mul_u32_u24 v4, 4624, v207\n\tv_lshl_add_u32 v4, v245, 4, v4\n\t" \
    "v_and_b32 v207, 3, v240\n\tv_lshl_add_u32 v4, v207, 7, v4\n\tv_add_u32 v4, s54, v4\n\tv_mul_u32_u24 v5, 12288, v206\n\tv_add_u32 v5, v5, v244\n\tv_lshrrev_b32 v206, 5, v240\n\ts_lshl_b32 s73, s38, 2\n\tv_add_u32 v206, s73, v206\n\t" \
    "v_mul_u32_u24 v206, 4624, v206\n\tv_and_b32 v207, 0x1f0, v8\n\tv_add_u32 v12, v206, v207\n\tv_mul_u32_u24 v10, 9216, v245\n\tv_add_u32 v10, v10, v244\n\tv_mul_u32_u24 v11, 4624, v204\n\tv_lshl_add_u32 v11, v245, 4, v11\n\tv_add_u32 v11, s54, v11\n\t" \
    "s_mul_i32 s62, s38, 589824\n\ts_lshl_b32 s73, s44, 4\n\ts_add_u32 s62, s62, s73\n\ts_add_u32 s62, s62, 2490368\n\tv_lshrrev_b32 v206, 3, v204\n\tv_add_u32 v206, 2, v206\n\tv_cmp_eq_u32 vcc, v206, v245\n\tv_and_b32 v207, 1, v204\n\t" \
    "v_lshlrev_b32 v207, 4, v207\n\tv_lshlrev_b32 v207, v207, s46\n\tv_cndmask_b32 v207, 0, v207, vcc\n\tv_bfe_u32 v206, v204, 1, 2\n\tv_cmp_eq_u32 vcc, 0, v206\n\tv_mov_b32 v196, 0\n\ts_nop 0\n\tv_cndmask_b32 v232, 0, v207, vcc\n\t" \
    "v_cmp_eq_u32 vcc, 1, v206\n\tv_mov_b32 v197, 0\n\ts_nop 0\n\tv_cndmask_b32 v233, 0, v207, vcc\n\tv_cmp_eq_u32 vcc, 2, v206\n\tv_mov_b32 v198, 0\n\ts_nop 0\n\tv_cndmask_b32 v234, 0, v207, vcc\n\t" \
    "v_cmp_eq_u32 vcc, 3, v206\n\tv_mov_b32 v199, 0\n\ts_nop 0\n\tv_cndmask_b32 v235, 0, v207, vcc\n\ts_add_u32 s65, s44, 0\n\ts_lshr_b32 s65, s65, 6\n\ts_cmp_eq_u32 s65, 8\n\ts_cselect_b32 s65, 1, s65\n\t" \
    "s_and_b32 s65, s65, 3\n\ts_lshl_b32 s47, s65, 11\n\ts_lshr_b32 s65, s54, 1\n\ts_add_u32 s47, s47, s65\n\ts_add_u32 s47, s47, 17408\n\ts_add_u32 s65, s44, 11\n\ts_lshr_b32 s65, s65, 6\n\ts_cmp_eq_u32 s65, 8\n\t" \
    "s_cselect_b32 s65, 1, s65\n\ts_and_b32 s65, s65, 3\n\ts_lshl_b32 s50, s65, 11\n\ts_lshr_b32 s65, s54, 1\n\ts_add_u32 s50, s50, s65\n\ts_add_u32 s50, s50, 17408\n\tds_write_b128 v9, v[196:199] offset:4128\n\tbuffer_load_dword v15, v6, s[12:15], s41 offen\n\t" \
    "buffer_load_dword v15, v7, s[16:19], s42 offen\n\ts_add_u32 s43, s43, 1\n\tv_readlane_b32 s41, v236, s43\n\tv_readlane_b32 s42, v237, s43\n\ts_nop 4\n\ts_waitcnt vmcnt(2)\n\tbuffer_store_dwordx2 v[248:249], off, s[8:11], s48 sc1\n\tv_alignbit_b32 v196, v167, v167, 16\n\t" \
    "v_alignbit_b32 v197, v166, v166, 16\n\tv_alignbit_b32 v198, v165, v165, 16\n\tv_alignbit_b32 v199, v164, v164, 16\n\tds_write_b128 v2, v[196:199] offset:3072\n\tv_bfi_b32 v200, s46, v167, v166\n\tv_bfi_b32 v201, s46, v166, v165\n\tv_bfi_b32 v202, s46, v165, v164\n\tv_bfi_b32 v203, s46, v164, v181\n\t" \
    "ds_write_b128 v2, v[200:203] offset:7232\n\tv_alignbit_b32 v196, v166, v166, 16\n\tv_alignbit_b32 v197, v165, v165, 16\n\tv_alignbit_b32 v198, v164, v164, 16\n\tv_alignbit_b32 v199, v181, v181, 16\n\tds_write_b128 v2, v[196:199] offset:11392\n\tv_bfi_b32 v200, s46, v166, v165\n\tv_bfi_b32 v201, s46, v165, v164\n\t" \
    "v_bfi_b32 v202, s46, v164, v181\n\tv_bfi_b32 v203, s46, v181, v180\n\tds_write_b128 v2, v[200:203] offset:15552\n\tv_alignbit_b32 v196, v171, v171, 16\n\tv_alignbit_b32 v197, v170, v170, 16\n\tv_alignbit_b32 v198, v169, v169, 16\n\tv_alignbit_b32 v199, v168, v168, 16\n\tds_write_b128 v2, v[196:199] offset:2048\n\t" \
    "v_bfi_b32 v200, s46, v171, v170\n\tv_bfi_b32 v201, s46, v170, v169\n\tv_bfi_b32 v202, s46, v169, v168\n\tv_bfi_b32 v203, s46, v168, v183\n\tds_write_b128 v2, v[200:203] offset:6208\n\tv_alignbit_b32 v196, v170, v170, 16\n\tv_alignbit_b32 v197, v169, v169, 16\n\tv_alignbit_b32 v198, v168, v168, 16\n\t" \
    "v_alignbit_b32 v199, v183, v183, 16\n\tds_write_b128 v2, v[196:199] offset:10368\n\tv_bfi_b32 v200, s46, v170, v169\n\tv_bfi_b32 v201, s46, v169, v168\n\tv_bfi_b32 v202, s46, v168, v183\n\tv_bfi_b32 v203, s46, v183, v182\n\tds_write_b128 v2, v[200:203] offset:14528\n\tv_alignbit_b32 v196, v175, v175, 16\n\t" \
    "v_alignbit_b32 v197, v174, v174, 16\n\tv_alignbit_b32 v198, v173, v173, 16\n\tv_alignbit_b32 v199, v172, v172, 16\n\tds_write_b128 v2, v[196:199] offset:1024\n\tv_bfi_b32 v200, s46, v175, v174\n\tv_bfi_b32 v201, s46, v174, v173\n\tv_bfi_b32 v202, s46, v173, v172\n\tv_bfi_b32 v203, s46, v172, v185\n\t" \
    "ds_write_b128 v2, v[200:203] offset:5184\n\tv_alignbit_b32 v196, v174, v174, 16\n\tv_alignbit_b32 v197, v173, v173, 16\n\tv_alignbit_b32 v198, v172, v172, 16\n\tv_alignbit_b32 v199, v185, v185, 16\n\tds_write_b128 v2, v[196:199] offset:9344\n\tv_bfi_b32 v200, s46, v174, v173\n\tv_bfi_b32 v201, s46, v173, v172\n\t" \
    "v_bfi_b32 v202, s46, v172, v185\n\tv_bfi_b32 v203, s46, v185, v184\n\tds_write_b128 v2, v[200:203] offset:13504\n\tv_alignbit_b32 v196, v179, v179, 16\n\tv_alignbit_b32 v197, v178, v178, 16\n\tv_alignbit_b32 v198, v177, v177, 16\n\tv_alignbit_b32 v199, v176, v176, 16\n\tds_write_b128 v2, v[196:199] offset:0\n\t" \
    "v_bfi_b32 v200, s46, v179, v178\n\tv_bfi_b32 v201, s46, v178, v177\n\tv_bfi_b32 v202, s46, v177, v176\n\tv_bfi_b32 v203, s46, v176, v187\n\tds_write_b128 v2, v[200:203] offset:4160\n\tv_alignbit_b32 v196, v178, v178, 16\n\tv_alignbit_b32 v197, v177, v177, 16\n\tv_alignbit_b32 v198, v176, v176, 16\n\t" \
    "v_alignbit_b32 v199, v187, v187, 16\n\tds_write_b128 v2, v[196:199] offset:8320\n\tv_bfi_b32 v200, s46, v178, v177\n\tv_bfi_b32 v201, s46, v177, v176\n\tv_bfi_b32 v202, s46, v176, v187\n\tv_bfi_b32 v203, s46, v187, v186\n\tds_write_b128 v2, v[200:203] offset:12480\n\tv_lshl_or_b32 v188, v188, 16, v188\n\t" \
    "v_and_b32 v228, v188, v232\n\tv_and_b32 v229, v188, v233\n\tv_and_b32 v230, v188, v234\n\tv_and_b32 v231, v188, v235\n\tds_read_b64 a[0:1], v0 offset:4032\n\tds_read_b64 a[2:3], v0 offset:4040\n\tds_read_b64 a[4:5], v0 offset:3520\n\tds_read_b64 a[6:7], v0 offset:3528\n\t" \
    "ds_read_b64 a[8:9], v0 offset:3008\n\tds_read_b64 a[10:11], v0 offset:3016\n\tds_read_b64 a[12:13], v0 offset:2496\n\tds_read_b64 a[14:15], v0 offset:2504\n\tds_read_b64 a[16:17], v0 offset:1984\n\tds_read_b64 a[18:19], v0 offset:1992\n\tds_read_b64 a[20:21], v0 offset:1472\n\tds_read_b64 a[22:23], v0 offset:1480\n\t" \
    "ds_read_b64 a[24:25], v0 offset:960\n\tds_read_b64 a[26:27], v0 offset:968\n\tds_read_b64 a[28:29], v0 offset:448\n\tds_read_b64 a[30:31], v0 offset:456\n\ts_barrier\n\tv_mov_b32 v196, 0\n\tv_mov_b32 v197, 0\n\tv_mov_b32 v198, 0\n\t" \
    "v_mov_b32 v199, 0\n\tds_write_b128 v12, v[196:199]\n\tds_write_b128 v12, v[196:199] offset:9248\n\ts_sleep 30\n\ts_mov_b32 s71, 1048576\n\t125:\n\tbuffer_load_dwordx2 v[250:251], v239, s[8:11], s47 offen sc1\n\tbuffer_load_dwordx2 v[252:253], v239, s[8:11], s50 offen sc1\n\t" \
    "s_waitcnt vmcnt(0)\n\tv_cmp_ne_u64 vcc, s[36:37], v[250:251]\n\tv_cmp_ne_u64 s[74:75], s[36:37], v[252:253]\n\ts_or_b64 s[74:75], s[74:75], vcc\n\ts_cbranch_scc0 126f\n\ts_sleep 16\n\ts_sub_u32 s71, s71, 1\n\ts_cbranch_scc0 125b\n\t" \
    "126:\n\tbuffer_load_dwordx4 v[68:71], v10, s[8:11], s62 offen sc1\n\ts_add_u32 s62, s62, 36864\n\tbuffer_load_dwordx4 v[72:75], v10, s[8:11], s62 offen sc1\n\ts_add_u32 s62, s62, 36864\n\tbuffer_load_dwordx4 v[76:79], v10, s[8:11], s62 offen sc1\n\ts_add_u32 s62, s62, 36864\n\tbuffer_load_dwordx4 v[80:83], v10, s[8:11], s62 offen sc1\n\t" \
    "s_add_u32 s62, s62, 36864\n\tbuffer_load_dwordx4 v[84:87], v10, s[8:11], s62 offen sc1\n\ts_add_u32 s62, s62, 36864\n\tbuffer_load_dwordx4 v[88:91], v10, s[8:11], s62 offen sc1\n\ts_add_u32 s62, s62, 36864\n\tbuffer_load_dwordx4 v[92:95], v10, s[8:11], s62 offen sc1\n\ts_add_u32 s62, s62, 36864\n\tbuffer_load_dwordx4 v[96:99], v10, s[8:11], s62 offen sc1\n\t" \
    "s_add_u32 s62, s62, 36864\n\tbuffer_load_dwordx4 v[100:103], v10, s[8:11], s62 offen sc1\n\ts_add_u32 s62, s62, 36864\n\tbuffer_load_dwordx4 v[104:107], v10, s[8:11], s62 offen sc1\n\ts_add_u32 s62, s62, 36864\n\tbuffer_load_dwordx4 v[108:111], v10, s[8:11], s62 offen sc1\n\ts_add_u32 s62, s62, 36864\n\tbuffer_load_dwordx4 v[112:115], v10, s[8:11], s62 offen sc1\n\t" \
    "s_add_u32 s62, s62, 36864\n\tbuffer_load_dwordx4 v[116:119], v10, s[8:11], s62 offen sc1\n\ts_add_u32 s62, s62, 36864\n\tbuffer_load_dwordx4 v[120:123], v10, s[8:11], s62 offen sc1\n\ts_add_u32 s62, s62, 36864\n\tbuffer_load_dwordx4 v[124:127], v10, s[8:11], s62 offen sc1\n\ts_add_u32 s62, s62, 36864\n\tbuffer_load_dwordx4 v[128:131], v10, s[8:11], s62 offen sc1\n\t" \
    "s_add_u32 s62, s62, 36864\n\ts_waitcnt vmcnt(15)\n\tds_write_b128 v11, v[68:71] offset:512\n\ts_waitcnt vmcnt(14)\n\tds_write_b128 v11, v[72:75] offset:576\n\ts_waitcnt vmcnt(13)\n\tds_write_b128 v11, v[76:79] offset:640\n\ts_waitcnt vmcnt(12)\n\t" \
    "ds_write_b128 v11, v[80:83] offset:704\n\ts_waitcnt vmcnt(11)\n\tds_write_b128 v11, v[84:87] offset:768\n\ts_waitcnt vmcnt(10)\n\tds_write_b128 v11, v[88:91] offset:832\n\ts_waitcnt vmcnt(9)\n\tds_write_b128 v11, v[92:95] offset:896\n\ts_waitcnt vmcnt(8)\n\t" \
    "ds_write_b128 v11, v[96:99] offset:960\n\ts_waitcnt vmcnt(7)\n\tds_write_b128 v11, v[100:103] offset:1024\n\ts_waitcnt vmcnt(6)\n\tds_write_b128 v11, v[104:107] offset:1088\n\ts_waitcnt vmcnt(5)\n\tds_write_b128 v11, v[108:111] offset:1152\n\ts_waitcnt vmcnt(4)\n\t" \
    "ds_write_b128 v11, v[112:115] offset:1216\n\ts_waitcnt vmcnt(3)\n\tds_write_b128 v11, v[116:119] offset:1280\n\ts_waitcnt vmcnt(2)\n\tds_write_b128 v11, v[120:123] offset:1344\n\ts_waitcnt vmcnt(1)\n\tds_write_b128 v11, v[124:127] offset:1408\n\ts_waitcnt vmcnt(0)\n\t" \
    "ds_write_b128 v11, v[128:131] offset:1472\n\ts_waitcnt lgkmcnt(0)\n\ts_barrier\n\tds_read_b128 v[68:71], v1 offset:448\n\tds_read_b128 v[72:75], v1 offset:960\n\tds_read_b128 v[76:79], v1 offset:1472\n\tds_read_b128 v[80:83], v1 offset:1984\n\tds_read_b128 v[84:87], v1 offset:2496\n\t" \
    "ds_read_b128 v[88:91], v1 offset:3008\n\tds_read_b128 v[92:95], v1 offset:3520\n\tds_read_b128 v[96:99], v1 offset:4032\n\ts_add_u32 s68, s44, s38\n\ts_add_u32 s68, s68, 4\n\ts_lshl_b32 s66, s68, 1\n\ts_lshl_b32 s68, s68, 12\n\tbuffer_load_dwordx4 v[42:45], v8, s[76:79], s68 offen offset:0\n\t" \
    "buffer_load_dwordx4 v[46:49], v8, s[76:79], s68 offen offset:1024\n\tbuffer_load_dwordx4 v[50:53], v8, s[76:79], s68 offen offset:2048\n\tbuffer_load_dwordx4 v[54:57], v8, s[76:79], s68 offen offset:3072\n\tbuffer_load_dwordx2 v[58:59], v13, s[76:79], s68 offen\n\tbuffer_load_dwordx2 v[60:61], v8, s[76:79], s68 offen offset:1016\n\tbuffer_load_dwordx2 v[62:63], v8, s[76:79], s68 offen offset:2040\n\tbuffer_load_dwordx2 v[64:65], v8, s[76:79], s68 offen offset:3064\n\tbuffer_load_ushort v66, off, s[80:83], s66\n\t" \
    "buffer_load_dword v15, v6, s[12:15], s41 offen\n\tbuffer_load_dword v15, v7, s[16:19], s42 offen\n\ts_add_u32 s43, s43, 1\n\tv_readlane_b32 s41, v236, s43\n\tv_readlane_b32 s42, v237, s43\n\ts_nop 4\n\ts_waitcnt lgkmcnt(0)\n\tv_mfma_f32_16x16x32_bf16 v[132:135], v[228:231], v[68:71], 0\n\t" \
    "ds_read_b64 a[32:33], v0 offset:3968\n\tds_read_b64 a[34:35], v0 offset:3976\n\tv_mfma_f32_16x16x32_bf16 v[132:135], a[0:3], v[68:71], v[132:135]\n\tds_read_b64 a[36:37], v0 offset:3456\n\tds_read_b64 a[38:39], v0 offset:3464\n\tv_mfma_f32_16x16x32_bf16 v[136:139], v[228:231], v[72:75], 0\n\tds_read_b64 a[40:41], v0 offset:2944\n\tds_read_b64 a[42:43], v0 offset:2952\n\t" \
    "v_mfma_f32_16x16x32_bf16 v[136:139], a[0:3], v[72:75], v[136:139]\n\tds_read_b64 a[44:45], v0 offset:2432\n\tds_read_b64 a[46:47], v0 offset:2440\n\tv_mfma_f32_16x16x32_bf16 v[136:139], a[4:7], v[68:71], v[136:139]\n\tds_read_b64 a[48:49], v0 offset:1920\n\tds_read_b64 a[50:51], v0 offset:1928\n\tv_mfma_f32_16x16x32_bf16 v[140:143], v[228:231], v[76:79], 0\n\tds_read_b64 a[52:53], v0 offset:1408\n\t" \
    "v_mfma_f32_16x16x32_bf16 v[140:143], a[0:3], v[76:79], v[140:143]\n\tds_read_b64 a[54:55], v0 offset:1416\n\tv_mfma_f32_16x16x32_bf16 v[140:143], a[4:7], v[72:75], v[140:143]\n\tds_read_b64 a[56:57], v0 offset:896\n\tv_mfma_f32_16x16x32_bf16 v[140:143], a[8:11], v[68:71], v[140:143]\n\tds_read_b64 a[58:59], v0 offset:904\n\tv_mfma_f32_16x16x32_bf16 v[144:147], v[228:231], v[80:83], 0\n\tds_read_b64 a[60:61], v0 offset:384\n\t" \
    "v_mfma_f32_16x16x32_bf16 v[144:147], a[0:3], v[80:83], v[144:147]\n\tds_read_b64 a[62:63], v0 offset:392\n\tv_mfma_f32_16x16x32_bf16 v[144:147], a[4:7], v[76:79], v[144:147]\n\tds_read_b128 v[100:103], v1 offset:384\n\tv_mfma_f32_16x16x32_bf16 v[144:147], a[8:11], v[72:75], v[144:147]\n\tds_read_b128 v[104:107], v1 offset:896\n\tv_mfma_f32_16x16x32_bf16 v[144:147], a[12:15], v[68:71], v[144:147]\n\tds_read_b128 v[108:111], v1 offset:1408\n\t" \
    "v_mfma_f32_16x16x32_bf16 v[148:151], v[228:231], v[84:87], 0\n\tds_read_b128 v[112:115], v1 offset:1920\n\tv_mfma_f32_16x16x32_bf16 v[148:151], a[0:3], v[84:87], v[148:151]\n\tds_read_b128 v[116:119], v1 offset:2432\n\tv_mfma_f32_16x16x32_bf16 v[148:151], a[4:7], v[80:83], v[148:151]\n\tds_read_b128 v[120:123], v1 offset:2944\n\tv_mfma_f32_16x16x32_bf16 v[148:151], a[8:11], v[76:79], v[148:151]\n\tbuffer_load_dword v15, v6, s[12:15], s41 offen\n\t" \
    "ds_read_b128 v[124:127], v1 offset:3456\n\tv_mfma_f32_16x16x32_bf16 v[148:151], a[12:15], v[72:75], v[148:151]\n\tbuffer_load_dword v15, v7, s[16:19], s42 offen\n\tds_read_b128 v[128:131], v1 offset:3968\n\tv_mfma_f32_16x16x32_bf16 v[148:151], a[16:19], v[68:71], v[148:151]\n\ts_add_u32 s43, s43, 1\n\tds_read_b64 a[64:65], v0 offset:3904\n\tv_mfma_f32_16x16x32_bf16 v[152:155], v[228:231], v[88:91], 0\n\t" \
    "v_readlane_b32 s41, v236, s43\n\tv_readlane_b32 s42, v237, s43\n\ts_nop 4\n\tds_read_b64 a[66:67], v0 offset:3912\n\tv_mfma_f32_16x16x32_bf16 v[152:155], a[0:3], v[88:91], v[152:155]\n\tds_read_b64 a[68:69], v0 offset:3392\n\tv_mfma_f32_16x16x32_bf16 v[152:155], a[4:7], v[84:87], v[152:155]\n\tds_read_b64 a[70:71], v0 offset:3400\n\t" \
    "v_mfma_f32_16x16x32_bf16 v[152:155], a[8:11], v[80:83], v[152:155]\n\tds_read_b64 a[72:73], v0 offset:2880\n\tv_mfma_f32_16x16x32_bf16 v[152:155], a[12:15], v[76:79], v[152:155]\n\tds_read_b64 a[74:75], v0 offset:2888\n\tv_mfma_f32_16x16x32_bf16 v[152:155], a[16:19], v[72:75], v[152:155]\n\tds_read_b64 a[76:77], v0 offset:2368\n\tv_mfma_f32_16x16x32_bf16 v[152:155], a[20:23], v[68:71], v[152:155]\n\tds_read_b64 a[78:79], v0 offset:2376\n\t" \
    "v_mfma_f32_16x16x32_bf16 v[156:159], v[228:231], v[92:95], 0\n\tds_read_b64 a[80:81], v0 offset:1856\n\tv_mfma_f32_16x16x32_bf16 v[156:159], a[0:3], v[92:95], v[156:159]\n\tds_read_b64 a[82:83], v0 offset:1864\n\tv_mfma_f32_16x16x32_bf16 v[156:159], a[4:7], v[88:91], v[156:159]\n\tds_read_b64 a[84:85], v0 offset:1344\n\tv_mfma_f32_16x16x32_bf16 v[156:159], a[8:11], v[84:87], v[156:159]\n\tds_read_b64 a[86:87], v0 offset:1352\n\t" \
    "v_mfma_f32_16x16x32_bf16 v[156:159], a[12:15], v[80:83], v[156:159]\n\tds_read_b64 a[88:89], v0 offset:832\n\tv_mfma_f32_16x16x32_bf16 v[156:159], a[16:19], v[76:79], v[156:159]\n\tds_read_b64 a[90:91], v0 offset:840\n\tv_mfma_f32_16x16x32_bf16 v[156:159], a[20:23], v[72:75], v[156:159]\n\tds_read_b64 a[92:93], v0 offset:320\n\tv_mfma_f32_16x16x32_bf16 v[156:159], a[24:27], v[68:71], v[156:159]\n\tds_read_b64 a[94:95], v0 offset:328\n\t" \
    "v_mfma_f32_16x16x32_bf16 v[160:163], v[228:231], v[96:99], 0\n\tds_read_b64 a[96:97], v0 offset:3840\n\tv_mfma_f32_16x16x32_bf16 v[160:163], a[0:3], v[96:99], v[160:163]\n\tds_read_b64 a[98:99], v0 offset:3848\n\tv_mfma_f32_16x16x32_bf16 v[160:163], a[4:7], v[92:95], v[160:163]\n\tds_read_b64 a[100:101], v0 offset:3328\n\tv_mfma_f32_16x16x32_bf16 v[160:163], a[8:11], v[88:91], v[160:163]\n\tds_read_b64 a[102:103], v0 offset:3336\n\t" \
    "v_mfma_f32_16x16x32_bf16 v[160:163], a[12:15], v[84:87], v[160:163]\n\tv_mfma_f32_16x16x32_bf16 v[160:163], a[16:19], v[80:83], v[160:163]\n\tv_mfma_f32_16x16x32_bf16 v[160:163], a[20:23], v[76:79], v[160:163]\n\tv_mfma_f32_16x16x32_bf16 v[160:163], a[24:27], v[72:75], v[160:163]\n\tv_mfma_f32_16x16x32_bf16 v[160:163], a[28:31], v[68:71], v[160:163]\n\ts_waitcnt lgkmcnt(15)\n\tv_mfma_f32_16x16x32_bf16 v[160:163], a[32:35], v[128:131], v[160:163]\n\tds_read_b64 a[104:105], v0 offset:2816\n\t" \
    "ds_read_b64 a[106:107], v0 offset:2824\n\tv_mfma_f32_16x16x32_bf16 v[160:163], a[36:39], v[124:127], v[160:163]\n\tds_read_b64 a[108:109], v0 offset:2304\n\tds_read_b64 a[110:111], v0 offset:2312\n\tv_mfma_f32_16x16x32_bf16 v[160:163], a[40:43], v[120:123], v[160:163]\n\tds_read_b64 a[112:113], v0 offset:1792\n\tds_read_b64 a[114:115], v0 offset:1800\n\tv_mfma_f32_16x16x32_bf16 v[160:163], a[44:47], v[116:119], v[160:163]\n\t" \
    "ds_read_b64 a[116:117], v0 offset:1280\n\tds_read_b64 a[118:119], v0 offset:1288\n\tv_mfma_f32_16x16x32_bf16 v[160:163], a[48:51], v[112:115], v[160:163]\n\tds_read_b64 a[120:121], v0 offset:768\n\tds_read_b64 a[122:123], v0 offset:776\n\tv_mfma_f32_16x16x32_bf16 v[160:163], a[52:55], v[108:111], v[160:163]\n\tds_read_b64 a[124:125], v0 offset:256\n\tv_mfma_f32_16x16x32_bf16 v[160:163], a[56:59], v[104:107], v[160:163]\n\t" \
    "ds_read_b64 a[126:127], v0 offset:264\n\tv_mfma_f32_16x16x32_bf16 v[160:163], a[60:63], v[100:103], v[160:163]\n\tds_read_b64 a[128:129], v0 offset:3776\n\tv_mfma_f32_16x16x32_bf16 v[156:159], a[32:35], v[124:127], v[156:159]\n\tds_read_b64 a[130:131], v0 offset:3784\n\tv_mfma_f32_16x16x32_bf16 v[156:159], a[36:39], v[120:123], v[156:159]\n\tds_read_b64 a[132:133], v0 offset:3264\n\tv_mfma_f32_16x16x32_bf16 v[156:159], a[40:43], v[116:119], v[156:159]\n\t" \
    "ds_read_b64 a[134:135], v0 offset:3272\n\tv_mfma_f32_16x16x32_bf16 v[156:159], a[44:47], v[112:115], v[156:159]\n\tds_read_b128 v[68:71], v1 offset:320\n\tv_mfma_f32_16x16x32_bf16 v[156:159], a[48:51], v[108:111], v[156:159]\n\tds_read_b128 v[72:75], v1 offset:832\n\tv_mfma_f32_16x16x32_bf16 v[156:159], a[52:55], v[104:107], v[156:159]\n\tds_read_b128 v[76:79], v1 offset:1344\n\tv_mfma_f32_16x16x32_bf16 v[156:159], a[56:59], v[100:103], v[156:159]\n\t" \
    "ds_read_b128 v[80:83], v1 offset:1856\n\tv_mfma_f32_16x16x32_bf16 v[152:155], a[32:35], v[120:123], v[152:155]\n\tds_read_b128 v[84:87], v1 offset:2368\n\tv_mfma_f32_16x16x32_bf16 v[152:155], a[36:39], v[116:119], v[152:155]\n\tds_read_b128 v[88:91], v1 offset:2880\n\tv_mfma_f32_16x16x32_bf16 v[152:155], a[40:43], v[112:115], v[152:155]\n\tds_read_b128 v[92:95], v1 offset:3392\n\tv_mfma_f32_16x16x32_bf16 v[152:155], a[44:47], v[108:111], v[152:155]\n\t" \
    "ds_read_b128 v[96:99], v1 offset:3904\n\tv_mfma_f32_16x16x32_bf16 v[152:155], a[48:51], v[104:107], v[152:155]\n\tds_read_b64 a[136:137], v0 offset:2752\n\tv_mfma_f32_16x16x32_bf16 v[152:155], a[52:55], v[100:103], v[152:155]\n\tds_read_b64 a[138:139], v0 offset:2760\n\tv_mfma_f32_16x16x32_bf16 v[148:151], a[32:35], v[116:119], v[148:151]\n\tds_read_b64 a[140:141], v0 offset:2240\n\tv_mfma_f32_16x16x32_bf16 v[148:151], a[36:39], v[112:115], v[148:151]\n\t" \
    "ds_read_b64 a[142:143], v0 offset:2248\n\tv_mfma_f32_16x16x32_bf16 v[148:151], a[40:43], v[108:111], v[148:151]\n\tds_read_b64 a[144:145], v0 offset:1728\n\tv_mfma_f32_16x16x32_bf16 v[148:151], a[44:47], v[104:107], v[148:151]\n\tds_read_b64 a[146:147], v0 offset:1736\n\tv_mfma_f32_16x16x32_bf16 v[148:151], a[48:51], v[100:103], v[148:151]\n\tds_read_b64 a[148:149], v0 offset:1216\n\tv_mfma_f32_16x16x32_bf16 v[144:147], a[32:35], v[112:115], v[144:147]\n\t" \
    "ds_read_b64 a[150:151], v0 offset:1224\n\tv_mfma_f32_16x16x32_bf16 v[144:147], a[36:39], v[108:111], v[144:147]\n\tds_read_b64 a[152:153], v0 offset:704\n\tv_mfma_f32_16x16x32_bf16 v[144:147], a[40:43], v[104:107], v[144:147]\n\tds_read_b64 a[154:155], v0 offset:712\n\tv_mfma_f32_16x16x32_bf16 v[144:147], a[44:47], v[100:103], v[144:147]\n\tds_read_b64 a[156:157], v0 offset:192\n\tv_mfma_f32_16x16x32_bf16 v[140:143], a[32:35], v[108:111], v[140:143]\n\t" \
    "ds_read_b64 a[158:159], v0 offset:200\n\tv_mfma_f32_16x16x32_bf16 v[140:143], a[36:39], v[104:107], v[140:143]\n\tv_mfma_f32_16x16x32_bf16 v[140:143], a[40:43], v[100:103], v[140:143]\n\tbuffer_load_dword v15, v6, s[12:15], s41 offen\n\tv_mfma_f32_16x16x32_bf16 v[136:139], a[32:35], v[104:107], v[136:139]\n\tbuffer_load_dword v15, v7, s[16:19], s42 offen\n\tv_mfma_f32_16x16x32_bf16 v[136:139], a[36:39], v[100:103], v[136:139]\n\ts_add_u32 s43, s43, 1\n\t" \
    "v_mfma_f32_16x16x32_bf16 v[132:135], a[32:35], v[100:103], v[132:135]\n\tv_readlane_b32 s41, v236, s43\n\tv_readlane_b32 s42, v237, s43\n\ts_nop 4\n\ts_waitcnt lgkmcnt(12)\n\tv_mfma_f32_16x16x32_bf16 v[132:135], a[64:67], v[68:71], v[132:135]\n\tds_read_b64 a[160:161], v0 offset:3712\n\tds_read_b64 a[162:163], v0 offset:3720\n\t" \
    "v_mfma_f32_16x16x32_bf16 v[136:139], a[64:67], v[72:75], v[136:139]\n\tds_read_b64 a[164:165], v0 offset:3200\n\tds_read_b64 a[166:167], v0 offset:3208\n\tv_mfma_f32_16x16x32_bf16 v[136:139], a[68:71], v[68:71], v[136:139]\n\tds_read_b64 a[168:169], v0 offset:2688\n\tds_read_b64 a[170:171], v0 offset:2696\n\tv_mfma_f32_16x16x32_bf16 v[140:143], a[64:67], v[76:79], v[140:143]\n\tds_read_b64 a[172:173], v0 offset:2176\n\t" \
    "ds_read_b64 a[174:175], v0 offset:2184\n\tv_mfma_f32_16x16x32_bf16 v[140:143], a[68:71], v[72:75], v[140:143]\n\tds_read_b64 a[176:177], v0 offset:1664\n\tds_read_b64 a[178:179], v0 offset:1672\n\tv_mfma_f32_16x16x32_bf16 v[140:143], a[72:75], v[68:71], v[140:143]\n\tds_read_b64 a[180:181], v0 offset:1152\n\tv_mfma_f32_16x16x32_bf16 v[144:147], a[64:67], v[80:83], v[144:147]\n\tds_read_b64 a[182:183], v0 offset:1160\n\t" \
    "v_mfma_f32_16x16x32_bf16 v[144:147], a[68:71], v[76:79], v[144:147]\n\tds_read_b64 a[184:185], v0 offset:640\n\tv_mfma_f32_16x16x32_bf16 v[144:147], a[72:75], v[72:75], v[144:147]\n\tds_read_b64 a[186:187], v0 offset:648\n\tv_mfma_f32_16x16x32_bf16 v[144:147], a[76:79], v[68:71], v[144:147]\n\tds_read_b64 a[188:189], v0 offset:128\n\tv_mfma_f32_16x16x32_bf16 v[148:151], a[64:67], v[84:87], v[148:151]\n\tds_read_b64 a[190:191], v0 offset:136\n\t" \
    "v_mfma_f32_16x16x32_bf16 v[148:151], a[68:71], v[80:83], v[148:151]\n\tds_read_b128 v[100:103], v1 offset:256\n\tv_mfma_f32_16x16x32_bf16 v[148:151], a[72:75], v[76:79], v[148:151]\n\tds_read_b128 v[104:107], v1 offset:768\n\tv_mfma_f32_16x16x32_bf16 v[148:151], a[76:79], v[72:75], v[148:151]\n\tds_read_b128 v[108:111], v1 offset:1280\n\tv_mfma_f32_16x16x32_bf16 v[148:151], a[80:83], v[68:71], v[148:151]\n\tds_read_b128 v[112:115], v1 offset:1792\n\t" \
    "v_mfma_f32_16x16x32_bf16 v[152:155], a[64:67], v[88:91], v[152:155]\n\tds_read_b128 v[116:119], v1 offset:2304\n\tv_mfma_f32_16x16x32_bf16 v[152:155], a[68:71], v[84:87], v[152:155]\n\tds_read_b128 v[120:123], v1 offset:2816\n\tv_mfma_f32_16x16x32_bf16 v[152:155], a[72:75], v[80:83], v[152:155]\n\tds_read_b128 v[124:127], v1 offset:3328\n\tv_mfma_f32_16x16x32_bf16 v[152:155], a[76:79], v[76:79], v[152:155]\n\tds_read_b128 v[128:131], v1 offset:3840\n\t" \
    "v_mfma_f32_16x16x32_bf16 v[152:155], a[80:83], v[72:75], v[152:155]\n\tds_read_b64 a[192:193], v0 offset:3648\n\tv_mfma_f32_16x16x32_bf16 v[152:155], a[84:87], v[68:71], v[152:155]\n\tds_read_b64 a[194:195], v0 offset:3656\n\tv_mfma_f32_16x16x32_bf16 v[156:159], a[64:67], v[92:95], v[156:159]\n\tds_read_b64 a[196:197], v0 offset:3136\n\tv_mfma_f32_16x16x32_bf16 v[156:159], a[68:71], v[88:91], v[156:159]\n\tds_read_b64 a[198:199], v0 offset:3144\n\t" \
    "v_mfma_f32_16x16x32_bf16 v[156:159], a[72:75], v[84:87], v[156:159]\n\tds_read_b64 a[200:201], v0 offset:2624\n\tv_mfma_f32_16x16x32_bf16 v[156:159], a[76:79], v[80:83], v[156:159]\n\tds_read_b64 a[202:203], v0 offset:2632\n\tv_mfma_f32_16x16x32_bf16 v[156:159], a[80:83], v[76:79], v[156:159]\n\tds_read_b64 a[204:205], v0 offset:2112\n\tv_mfma_f32_16x16x32_bf16 v[156:159], a[84:87], v[72:75], v[156:159]\n\tds_read_b64 a[206:207], v0 offset:2120\n\t" \
    "v_mfma_f32_16x16x32_bf16 v[156:159], a[88:91], v[68:71], v[156:159]\n\tds_read_b64 a[208:209], v0 offset:1600\n\tv_mfma_f32_16x16x32_bf16 v[160:163], a[64:67], v[96:99], v[160:163]\n\tds_read_b64 a[210:211], v0 offset:1608\n\tv_mfma_f32_16x16x32_bf16 v[160:163], a[68:71], v[92:95], v[160:163]\n\tds_read_b64 a[212:213], v0 offset:1088\n\tv_mfma_f32_16x16x32_bf16 v[160:163], a[72:75], v[88:91], v[160:163]\n\tds_read_b64 a[214:215], v0 offset:1096\n\t" \
    "v_mfma_f32_16x16x32_bf16 v[160:163], a[76:79], v[84:87], v[160:163]\n\tv_mfma_f32_16x16x32_bf16 v[160:163], a[80:83], v[80:83], v[160:163]\n\tv_mfma_f32_16x16x32_bf16 v[160:163], a[84:87], v[76:79], v[160:163]\n\tv_mfma_f32_16x16x32_bf16 v[160:163], a[88:91], v[72:75], v[160:163]\n\tv_mfma_f32_16x16x32_bf16 v[160:163], a[92:95], v[68:71], v[160:163]\n\ts_waitcnt lgkmcnt(12)\n\tv_mfma_f32_16x16x32_bf16 v[160:163], a[96:99], v[128:131], v[160:163]\n\tds_read_b64 a[216:217], v0 offset:576\n\t" \
    "v_mfma_f32_16x16x32_bf16 v[160:163], a[100:103], v[124:127], v[160:163]\n\tds_read_b64 a[218:219], v0 offset:584\n\tv_mfma_f32_16x16x32_bf16 v[160:163], a[104:107], v[120:123], v[160:163]\n\tds_read_b64 a[220:221], v0 offset:64\n\tv_mfma_f32_16x16x32_bf16 v[160:163], a[108:111], v[116:119], v[160:163]\n\tds_read_b64 a[222:223], v0 offset:72\n\tv_mfma_f32_16x16x32_bf16 v[160:163], a[112:115], v[112:115], v[160:163]\n\tds_read_b64 a[224:225], v0 offset:3584\n\t" \
    "v_mfma_f32_16x16x32_bf16 v[160:163], a[116:119], v[108:111], v[160:163]\n\tds_read_b64 a[226:227], v0 offset:3592\n\tv_mfma_f32_16x16x32_bf16 v[160:163], a[120:123], v[104:107], v[160:163]\n\tds_read_b64 a[228:229], v0 offset:3072\n\tv_mfma_f32_16x16x32_bf16 v[160:163], a[124:127], v[100:103], v[160:163]\n\tds_read_b64 a[230:231], v0 offset:3080\n\tv_mfma_f32_16x16x32_bf16 v[156:159], a[96:99], v[124:127], v[156:159]\n\tds_read_b64 a[232:233], v0 offset:2560\n\t" \
    "v_mfma_f32_16x16x32_bf16 v[156:159], a[100:103], v[120:123], v[156:159]\n\tds_read_b64 a[234:235], v0 offset:2568\n\tv_mfma_f32_16x16x32_bf16 v[156:159], a[104:107], v[116:119], v[156:159]\n\tds_read_b64 a[236:237], v0 offset:2048\n\tv_mfma_f32_16x16x32_bf16 v[156:159], a[108:111], v[112:115], v[156:159]\n\tds_read_b64 a[238:239], v0 offset:2056\n\tv_mfma_f32_16x16x32_bf16 v[156:159], a[112:115], v[108:111], v[156:159]\n\tds_read_b64 a[240:241], v0 offset:1536\n\t" \
    "v_mfma_f32_16x16x32_bf16 v[156:159], a[116:119], v[104:107], v[156:159]\n\tds_read_b64 a[242:243], v0 offset:1544\n\tv_mfma_f32_16x16x32_bf16 v[156:159], a[120:123], v[100:103], v[156:159]\n\tds_read_b64 a[244:245], v0 offset:1024\n\tv_mfma_f32_16x16x32_bf16 v[152:155], a[96:99], v[120:123], v[152:155]\n\tds_read_b64 a[246:247], v0 offset:1032\n\tv_mfma_f32_16x16x32_bf16 v[152:155], a[100:103], v[116:119], v[152:155]\n\tds_read_b128 v[68:71], v1 offset:192\n\t" \
    "v_mfma_f32_16x16x32_bf16 v[152:155], a[104:107], v[112:115], v[152:155]\n\tds_read_b128 v[72:75], v1 offset:704\n\tv_mfma_f32_16x16x32_bf16 v[152:155], a[108:111], v[108:111], v[152:155]\n\tds_read_b128 v[76:79], v1 offset:1216\n\tv_mfma_f32_16x16x32_bf16 v[152:155], a[112:115], v[104:107], v[152:155]\n\tds_read_b128 v[80:83], v1 offset:1728\n\tv_mfma_f32_16x16x32_bf16 v[152:155], a[116:119], v[100:103], v[152:155]\n\tbuffer_load_dword v15, v6, s[12:15], s41 offen\n\t" \
    "ds_read_b128 v[84:87], v1 offset:2240\n\tv_mfma_f32_16x16x32_bf16 v[148:151], a[96:99], v[116:119], v[148:151]\n\tbuffer_load_dword v15, v7, s[16:19], s42 offen\n\tds_read_b128 v[88:91], v1 offset:2752\n\tv_mfma_f32_16x16x32_bf16 v[148:151], a[100:103], v[112:115], v[148:151]\n\ts_add_u32 s43, s43, 1\n\tds_read_b128 v[92:95], v1 offset:3264\n\tv_mfma_f32_16x16x32_bf16 v[148:151], a[104:107], v[108:111], v[148:151]\n\t" \
    "v_readlane_b32 s41, v236, s43\n\tv_readlane_b32 s42, v237, s43\n\ts_nop 4\n\tds_read_b128 v[96:99], v1 offset:3776\n\tv_mfma_f32_16x16x32_bf16 v[148:151], a[108:111], v[104:107], v[148:151]\n\tds_read_b64 a[248:249], v0 offset:512\n\tv_mfma_f32_16x16x32_bf16 v[148:151], a[112:115], v[100:103], v[148:151]\n\tds_read_b64 a[250:251], v0 offset:520\n\t" \
    "v_mfma_f32_16x16x32_bf16 v[144:147], a[96:99], v[112:115], v[144:147]\n\tds_read_b64 a[252:253], v0 offset:0\n\tv_mfma_f32_16x16x32_bf16 v[144:147], a[100:103], v[108:111], v[144:147]\n\tds_read_b64 a[254:255], v0 offset:8\n\tv_mfma_f32_16x16x32_bf16 v[144:147], a[104:107], v[104:107], v[144:147]\n\tv_mfma_f32_16x16x32_bf16 v[144:147], a[108:111], v[100:103], v[144:147]\n\tv_mfma_f32_16x16x32_bf16 v[140:143], a[96:99], v[108:111], v[140:143]\n\tv_mfma_f32_16x16x32_bf16 v[140:143], a[100:103], v[104:107], v[140:143]\n\t" \
    "v_mfma_f32_16x16x32_bf16 v[140:143], a[104:107], v[100:103], v[140:143]\n\tv_mfma_f32_16x16x32_bf16 v[136:139], a[96:99], v[104:107], v[136:139]\n\tv_mfma_f32_16x16x32_bf16 v[136:139], a[100:103], v[100:103], v[136:139]\n\tv_mfma_f32_16x16x32_bf16 v[132:135], a[96:99], v[100:103], v[132:135]\n\ts_waitcnt lgkmcnt(4)\n\tv_mfma_f32_16x16x32_bf16 v[132:135], a[128:131], v[68:71], v[132:135]\n\tds_read_b128 v[100:103], v1 offset:128\n\ts_waitcnt vmcnt(8)\n\t" \
    "v_alignbit_b32 v196, v45, v45, 16\n\tv_mfma_f32_16x16x32_bf16 v[136:139], a[128:131], v[72:75], v[136:139]\n\tds_read_b128 v[104:107], v1 offset:640\n\tv_alignbit_b32 v197, v44, v44, 16\n\tv_mfma_f32_16x16x32_bf16 v[136:139], a[132:135], v[68:71], v[136:139]\n\tds_read_b128 v[108:111], v1 offset:1152\n\tv_alignbit_b32 v198, v43, v43, 16\n\tv_mfma_f32_16x16x32_bf16 v[140:143], a[128:131], v[76:79], v[140:143]\n\t" \
    "ds_read_b128 v[112:115], v1 offset:1664\n\tv_alignbit_b32 v199, v42, v42, 16\n\tv_mfma_f32_16x16x32_bf16 v[140:143], a[132:135], v[72:75], v[140:143]\n\tds_write_b128 v2, v[196:199] offset:3072\n\tv_mfma_f32_16x16x32_bf16 v[140:143], a[136:139], v[68:71], v[140:143]\n\tds_read_b128 v[116:119], v1 offset:2176\n\tv_bfi_b32 v200, s46, v45, v44\n\tv_mfma_f32_16x16x32_bf16 v[144:147], a[128:131], v[80:83], v[144:147]\n\t" \
    "ds_read_b128 v[120:123], v1 offset:2688\n\tv_bfi_b32 v201, s46, v44, v43\n\tv_mfma_f32_16x16x32_bf16 v[144:147], a[132:135], v[76:79], v[144:147]\n\tds_read_b128 v[124:127], v1 offset:3200\n\tv_bfi_b32 v202, s46, v43, v42\n\tv_mfma_f32_16x16x32_bf16 v[144:147], a[136:139], v[72:75], v[144:147]\n\tds_read_b128 v[128:131], v1 offset:3712\n\tv_bfi_b32 v203, s46, v42, v59\n\t" \
    "v_mfma_f32_16x16x32_bf16 v[144:147], a[140:143], v[68:71], v[144:147]\n\tds_write_b128 v2, v[200:203] offset:7232\n\tv_mfma_f32_16x16x32_bf16 v[148:151], a[128:131], v[84:87], v[148:151]\n\tv_alignbit_b32 v196, v44, v44, 16\n\tv_alignbit_b32 v197, v43, v43, 16\n\tv_mfma_f32_16x16x32_bf16 v[148:151], a[132:135], v[80:83], v[148:151]\n\tv_alignbit_b32 v198, v42, v42, 16\n\tv_alignbit_b32 v199, v59, v59, 16\n\t" \
    "v_mfma_f32_16x16x32_bf16 v[148:151], a[136:139], v[76:79], v[148:151]\n\tds_write_b128 v2, v[196:199] offset:11392\n\tv_mfma_f32_16x16x32_bf16 v[148:151], a[140:143], v[72:75], v[148:151]\n\tv_bfi_b32 v200, s46, v44, v43\n\tv_bfi_b32 v201, s46, v43, v42\n\tv_mfma_f32_16x16x32_bf16 v[148:151], a[144:147], v[68:71], v[148:151]\n\tv_bfi_b32 v202, s46, v42, v59\n\tv_bfi_b32 v203, s46, v59, v58\n\t" \
    "v_mfma_f32_16x16x32_bf16 v[152:155], a[128:131], v[88:91], v[152:155]\n\tds_write_b128 v2, v[200:203] offset:15552\n\tv_mfma_f32_16x16x32_bf16 v[152:155], a[132:135], v[84:87], v[152:155]\n\tv_alignbit_b32 v196, v49, v49, 16\n\tv_alignbit_b32 v197, v48, v48, 16\n\tv_mfma_f32_16x16x32_bf16 v[152:155], a[136:139], v[80:83], v[152:155]\n\tv_alignbit_b32 v198, v47, v47, 16\n\tv_alignbit_b32 v199, v46, v46, 16\n\t" \
    "v_mfma_f32_16x16x32_bf16 v[152:155], a[140:143], v[76:79], v[152:155]\n\tds_write_b128 v2, v[196:199] offset:2048\n\tv_mfma_f32_16x16x32_bf16 v[152:155], a[144:147], v[72:75], v[152:155]\n\tv_bfi_b32 v200, s46, v49, v48\n\tv_bfi_b32 v201, s46, v48, v47\n\tv_mfma_f32_16x16x32_bf16 v[152:155], a[148:151], v[68:71], v[152:155]\n\tv_bfi_b32 v202, s46, v47, v46\n\tv_bfi_b32 v203, s46, v46, v61\n\t" \
    "v_mfma_f32_16x16x32_bf16 v[156:159], a[128:131], v[92:95], v[156:159]\n\tds_write_b128 v2, v[200:203] offset:6208\n\tv_mfma_f32_16x16x32_bf16 v[156:159], a[132:135], v[88:91], v[156:159]\n\tv_alignbit_b32 v196, v48, v48, 16\n\tv_alignbit_b32 v197, v47, v47, 16\n\tv_mfma_f32_16x16x32_bf16 v[156:159], a[136:139], v[84:87], v[156:159]\n\tv_alignbit_b32 v198, v46, v46, 16\n\tv_alignbit_b32 v199, v61, v61, 16\n\t" \
    "v_mfma_f32_16x16x32_bf16 v[156:159], a[140:143], v[80:83], v[156:159]\n\tds_write_b128 v2, v[196:199] offset:10368\n\tv_mfma_f32_16x16x32_bf16 v[156:159], a[144:147], v[76:79], v[156:159]\n\tv_bfi_b32 v200, s46, v48, v47\n\tv_bfi_b32 v201, s46, v47, v46\n\tv_mfma_f32_16x16x32_bf16 v[156:159], a[148:151], v[72:75], v[156:159]\n\tv_bfi_b32 v202, s46, v46, v61\n\tv_bfi_b32 v203, s46, v61, v60\n\t" \
    "v_mfma_f32_16x16x32_bf16 v[156:159], a[152:155], v[68:71], v[156:159]\n\tds_write_b128 v2, v[200:203] offset:14528\n\tv_mfma_f32_16x16x32_bf16 v[160:163], a[128:131], v[96:99], v[160:163]\n\tv_alignbit_b32 v196, v53, v53, 16\n\tv_alignbit_b32 v197, v52, v52, 16\n\tv_mfma_f32_16x16x32_bf16 v[160:163], a[132:135], v[92:95], v[160:163]\n\tv_alignbit_b32 v198, v51, v51, 16\n\tv_alignbit_b32 v199, v50, v50, 16\n\t" \
    "v_mfma_f32_16x16x32_bf16 v[160:163], a[136:139], v[88:91], v[160:163]\n\tds_write_b128 v2, v[196:199] offset:1024\n\tv_mfma_f32_16x16x32_bf16 v[160:163], a[140:143], v[84:87], v[160:163]\n\tv_bfi_b32 v200, s46, v53, v52\n\tv_bfi_b32 v201, s46, v52, v51\n\tv_mfma_f32_16x16x32_bf16 v[160:163], a[144:147], v[80:83], v[160:163]\n\tv_bfi_b32 v202, s46, v51, v50\n\tv_bfi_b32 v203, s46, v50, v63\n\t" \
    "v_mfma_f32_16x16x32_bf16 v[160:163], a[148:151], v[76:79], v[160:163]\n\tds_write_b128 v2, v[200:203] offset:5184\n\tv_mfma_f32_16x16x32_bf16 v[160:163], a[152:155], v[72:75], v[160:163]\n\tv_alignbit_b32 v196, v52, v52, 16\n\tv_alignbit_b32 v197, v51, v51, 16\n\tv_mfma_f32_16x16x32_bf16 v[160:163], a[156:159], v[68:71], v[160:163]\n\tv_alignbit_b32 v198, v50, v50, 16\n\tv_alignbit_b32 v199, v63, v63, 16\n\t" \
    "s_waitcnt lgkmcnt(9)\n\tv_mfma_f32_16x16x32_bf16 v[160:163], a[160:163], v[128:131], v[160:163]\n\tds_write_b128 v2, v[196:199] offset:9344\n\tv_mfma_f32_16x16x32_bf16 v[160:163], a[164:167], v[124:127], v[160:163]\n\tds_read_b128 v[68:71], v1 offset:64\n\tv_bfi_b32 v200, s46, v52, v51\n\tv_mfma_f32_16x16x32_bf16 v[160:163], a[168:171], v[120:123], v[160:163]\n\tds_read_b128 v[72:75], v1 offset:576\n\t" \
    "v_bfi_b32 v201, s46, v51, v50\n\tv_mfma_f32_16x16x32_bf16 v[160:163], a[172:175], v[116:119], v[160:163]\n\tds_read_b128 v[76:79], v1 offset:1088\n\tv_bfi_b32 v202, s46, v50, v63\n\tv_mfma_f32_16x16x32_bf16 v[160:163], a[176:179], v[112:115], v[160:163]\n\tds_read_b128 v[80:83], v1 offset:1600\n\tv_bfi_b32 v203, s46, v63, v62\n\tv_mfma_f32_16x16x32_bf16 v[160:163], a[180:183], v[108:111], v[160:163]\n\t" \
    "ds_write_b128 v2, v[200:203] offset:13504\n\tv_mfma_f32_16x16x32_bf16 v[160:163], a[184:187], v[104:107], v[160:163]\n\tds_read_b128 v[84:87], v1 offset:2112\n\tv_alignbit_b32 v196, v57, v57, 16\n\tv_mfma_f32_16x16x32_bf16 v[160:163], a[188:191], v[100:103], v[160:163]\n\tbuffer_load_dword v15, v6, s[12:15], s41 offen\n\tds_read_b128 v[88:91], v1 offset:2624\n\tv_mfma_f32_16x16x32_bf16 v[156:159], a[160:163], v[124:127], v[156:159]\n\t" \
    "buffer_load_dword v15, v7, s[16:19], s42 offen\n\tds_read_b128 v[92:95], v1 offset:3136\n\tv_mfma_f32_16x16x32_bf16 v[156:159], a[164:167], v[120:123], v[156:159]\n\ts_add_u32 s43, s43, 1\n\tds_read_b128 v[96:99], v1 offset:3648\n\tv_alignbit_b32 v197, v56, v56, 16\n\tv_mfma_f32_16x16x32_bf16 v[156:159], a[168:171], v[116:119], v[156:159]\n\tv_readlane_b32 s41, v236, s43\n\t" \
    "v_readlane_b32 s42, v237, s43\n\ts_nop 4\n\tv_alignbit_b32 v198, v55, v55, 16\n\tv_alignbit_b32 v199, v54, v54, 16\n\tv_mfma_f32_16x16x32_bf16 v[156:159], a[172:175], v[112:115], v[156:159]\n\tds_write_b128 v2, v[196:199] offset:0\n\tv_mfma_f32_16x16x32_bf16 v[156:159], a[176:179], v[108:111], v[156:159]\n\tv_bfi_b32 v200, s46, v57, v56\n\t" \
    "v_bfi_b32 v201, s46, v56, v55\n\tv_mfma_f32_16x16x32_bf16 v[156:159], a[180:183], v[104:107], v[156:159]\n\tv_bfi_b32 v202, s46, v55, v54\n\tv_bfi_b32 v203, s46, v54, v65\n\tv_mfma_f32_16x16x32_bf16 v[156:159], a[184:187], v[100:103], v[156:159]\n\tds_write_b128 v2, v[200:203] offset:4160\n\tv_mfma_f32_16x16x32_bf16 v[152:155], a[160:163], v[120:123], v[152:155]\n\tv_alignbit_b32 v196, v56, v56, 16\n\t" \
    "v_alignbit_b32 v197, v55, v55, 16\n\tv_mfma_f32_16x16x32_bf16 v[152:155], a[164:167], v[116:119], v[152:155]\n\tv_alignbit_b32 v198, v54, v54, 16\n\tv_alignbit_b32 v199, v65, v65, 16\n\tv_mfma_f32_16x16x32_bf16 v[152:155], a[168:171], v[112:115], v[152:155]\n\tds_write_b128 v2, v[196:199] offset:8320\n\tv_mfma_f32_16x16x32_bf16 v[152:155], a[172:175], v[108:111], v[152:155]\n\tv_bfi_b32 v200, s46, v56, v55\n\t" \
    "v_bfi_b32 v201, s46, v55, v54\n\tv_mfma_f32_16x16x32_bf16 v[152:155], a[176:179], v[104:107], v[152:155]\n\tv_bfi_b32 v202, s46, v54, v65\n\tv_bfi_b32 v203, s46, v65, v64\n\tv_mfma_f32_16x16x32_bf16 v[152:155], a[180:183], v[100:103], v[152:155]\n\tds_write_b128 v2, v[200:203] offset:12480\n\tv_mfma_f32_16x16x32_bf16 v[148:151], a[160:163], v[116:119], v[148:151]\n\tv_lshl_or_b32 v66, v66, 16, v66\n\t" \
    "v_and_b32 v228, v66, v232\n\tv_mfma_f32_16x16x32_bf16 v[148:151], a[164:167], v[112:115], v[148:151]\n\tv_and_b32 v229, v66, v233\n\tv_and_b32 v230, v66, v234\n\tv_mfma_f32_16x16x32_bf16 v[148:151], a[168:171], v[108:111], v[148:151]\n\tv_and_b32 v231, v66, v235\n\tv_mfma_f32_16x16x32_bf16 v[148:151], a[172:175], v[104:107], v[148:151]\n\tv_mfma_f32_16x16x32_bf16 v[148:151], a[176:179], v[100:103], v[148:151]\n\t" \
    "v_mfma_f32_16x16x32_bf16 v[144:147], a[160:163], v[112:115], v[144:147]\n\tv_mfma_f32_16x16x32_bf16 v[144:147], a[164:167], v[108:111], v[144:147]\n\tv_mfma_f32_16x16x32_bf16 v[144:147], a[168:171], v[104:107], v[144:147]\n\tv_mfma_f32_16x16x32_bf16 v[144:147], a[172:175], v[100:103], v[144:147]\n\tv_mfma_f32_16x16x32_bf16 v[140:143], a[160:163], v[108:111], v[140:143]\n\tv_mfma_f32_16x16x32_bf16 v[140:143], a[164:167], v[104:107], v[140:143]\n\tv_mfma_f32_16x16x32_bf16 v[140:143], a[168:171], v[100:103], v[140:143]\n\tv_mfma_f32_16x16x32_bf16 v[136:139], a[160:163], v[104:107], v[136:139]\n\t" \
    "v_mfma_f32_16x16x32_bf16 v[136:139], a[164:167], v[100:103], v[136:139]\n\tv_mfma_f32_16x16x32_bf16 v[132:135], a[160:163], v[100:103], v[132:135]\n\ts_waitcnt lgkmcnt(4)\n\tv_mfma_f32_16x16x32_bf16 v[132:135], a[192:195], v[68:71], v[132:135]\n\ts_add_u32 s68, s44, s38\n\tds_read_b128 v[100:103], v1 offset:0\n\tv_mfma_f32_16x16x32_bf16 v[136:139], a[192:195], v[72:75], v[136:139]\n\ts_add_u32 s68, s68, 8\n\t" \
    "ds_read_b128 v[104:107], v1 offset:512\n\tv_mfma_f32_16x16x32_bf16 v[136:139], a[196:199], v[68:71], v[136:139]\n\ts_lshl_b32 s66, s68, 1\n\tds_read_b128 v[108:111], v1 offset:1024\n\tv_mfma_f32_16x16x32_bf16 v[140:143], a[192:195], v[76:79], v[140:143]\n\ts_lshl_b32 s68, s68, 12\n\tbuffer_load_dwordx4 v[16:19], v8, s[76:79], s68 offen offset:0\n\tds_read_b128 v[112:115], v1 offset:1536\n\t" \
    "v_mfma_f32_16x16x32_bf16 v[140:143], a[196:199], v[72:75], v[140:143]\n\tds_read_b128 v[116:119], v1 offset:2048\n\tv_mfma_f32_16x16x32_bf16 v[140:143], a[200:203], v[68:71], v[140:143]\n\tds_read_b128 v[120:123], v1 offset:2560\n\tv_mfma_f32_16x16x32_bf16 v[144:147], a[192:195], v[80:83], v[144:147]\n\tds_read_b128 v[124:127], v1 offset:3072\n\tv_mfma_f32_16x16x32_bf16 v[144:147], a[196:199], v[76:79], v[144:147]\n\tds_read_b128 v[128:131], v1 offset:3584\n\t" \
    "v_mfma_f32_16x16x32_bf16 v[144:147], a[200:203], v[72:75], v[144:147]\n\tv_mfma_f32_16x16x32_bf16 v[144:147], a[204:207], v[68:71], v[144:147]\n\tbuffer_load_dwordx4 v[20:23], v8, s[76:79], s68 offen offset:1024\n\tv_mfma_f32_16x16x32_bf16 v[148:151], a[192:195], v[84:87], v[148:151]\n\tv_mfma_f32_16x16x32_bf16 v[148:151], a[196:199], v[80:83], v[148:151]\n\tv_mfma_f32_16x16x32_bf16 v[148:151], a[200:203], v[76:79], v[148:151]\n\tv_mfma_f32_16x16x32_bf16 v[148:151], a[204:207], v[72:75], v[148:151]\n\tv_mfma_f32_16x16x32_bf16 v[148:151], a[208:211], v[68:71], v[148:151]\n\t" \
    "v_mfma_f32_16x16x32_bf16 v[152:155], a[192:195], v[88:91], v[152:155]\n\tbuffer_load_dwordx4 v[24:27], v8, s[76:79], s68 offen offset:2048\n\tv_mfma_f32_16x16x32_bf16 v[152:155], a[196:199], v[84:87], v[152:155]\n\tv_mfma_f32_16x16x32_bf16 v[152:155], a[200:203], v[80:83], v[152:155]\n\tv_mfma_f32_16x16x32_bf16 v[152:155], a[204:207], v[76:79], v[152:155]\n\tv_mfma_f32_16x16x32_bf16 v[152:155], a[208:211], v[72:75], v[152:155]\n\tv_mfma_f32_16x16x32_bf16 v[152:155], a[212:215], v[68:71], v[152:155]\n\tv_mfma_f32_16x16x32_bf16 v[156:159], a[192:195], v[92:95], v[156:159]\n\t" \
    "buffer_load_dwordx4 v[28:31], v8, s[76:79], s68 offen offset:3072\n\tv_mfma_f32_16x16x32_bf16 v[156:159], a[196:199], v[88:91], v[156:159]\n\tv_mfma_f32_16x16x32_bf16 v[156:159], a[200:203], v[84:87], v[156:159]\n\tv_mfma_f32_16x16x32_bf16 v[156:159], a[204:207], v[80:83], v[156:159]\n\tv_mfma_f32_16x16x32_bf16 v[156:159], a[208:211], v[76:79], v[156:159]\n\tv_mfma_f32_16x16x32_bf16 v[156:159], a[212:215], v[72:75], v[156:159]\n\tv_mfma_f32_16x16x32_bf16 v[156:159], a[216:219], v[68:71], v[156:159]\n\tbuffer_load_dwordx2 v[32:33], v13, s[76:79], s68 offen\n\t" \
    "v_mfma_f32_16x16x32_bf16 v[160:163], a[192:195], v[96:99], v[160:163]\n\tv_mfma_f32_16x16x32_bf16 v[160:163], a[196:199], v[92:95], v[160:163]\n\tv_mfma_f32_16x16x32_bf16 v[160:163], a[200:203], v[88:91], v[160:163]\n\tv_mfma_f32_16x16x32_bf16 v[160:163], a[204:207], v[84:87], v[160:163]\n\tv_mfma_f32_16x16x32_bf16 v[160:163], a[208:211], v[80:83], v[160:163]\n\tv_mfma_f32_16x16x32_bf16 v[160:163], a[212:215], v[76:79], v[160:163]\n\tbuffer_load_dwordx2 v[34:35], v8, s[76:79], s68 offen offset:1016\n\tv_mfma_f32_16x16x32_bf16 v[160:163], a[216:219], v[72:75], v[160:163]\n\t" \
    "v_mfma_f32_16x16x32_bf16 v[160:163], a[220:223], v[68:71], v[160:163]\n\ts_waitcnt lgkmcnt(0)\n\tv_mfma_f32_16x16x32_bf16 v[160:163], a[224:227], v[128:131], v[160:163]\n\tds_read_b64 a[0:1], v0 offset:4032\n\tv_mfma_f32_16x16x32_bf16 v[160:163], a[228:231], v[124:127], v[160:163]\n\tds_read_b64 a[2:3], v0 offset:4040\n\tv_mfma_f32_16x16x32_bf16 v[160:163], a[232:235], v[120:123], v[160:163]\n\tds_read_b64 a[4:5], v0 offset:3520\n\t" \
    "v_mfma_f32_16x16x32_bf16 v[160:163], a[236:239], v[116:119], v[160:163]\n\tbuffer_load_dwordx2 v[36:37], v8, s[76:79], s68 offen offset:2040\n\tds_read_b64 a[6:7], v0 offset:3528\n\tv_mfma_f32_16x16x32_bf16 v[160:163], a[240:243], v[112:115], v[160:163]\n\tds_read_b64 a[8:9], v0 offset:3008\n\tv_mfma_f32_16x16x32_bf16 v[160:163], a[244:247], v[108:111], v[160:163]\n\tds_read_b64 a[10:11], v0 offset:3016\n\tv_mfma_f32_16x16x32_bf16 v[160:163], a[248:251], v[104:107], v[160:163]\n\t" \
    "ds_read_b64 a[12:13], v0 offset:2496\n\tv_mfma_f32_16x16x32_bf16 v[160:163], a[252:255], v[100:103], v[160:163]\n\tds_read_b64 a[14:15], v0 offset:2504\n\tv_mfma_f32_16x16x32_bf16 v[156:159], a[224:227], v[124:127], v[156:159]\n\tds_read_b64 a[16:17], v0 offset:1984\n\tv_mfma_f32_16x16x32_bf16 v[156:159], a[228:231], v[120:123], v[156:159]\n\tbuffer_load_dwordx2 v[38:39], v8, s[76:79], s68 offen offset:3064\n\tds_read_b64 a[18:19], v0 offset:1992\n\t" \
    "v_mfma_f32_16x16x32_bf16 v[156:159], a[232:235], v[116:119], v[156:159]\n\tds_read_b64 a[20:21], v0 offset:1472\n\tv_mfma_f32_16x16x32_bf16 v[156:159], a[236:239], v[112:115], v[156:159]\n\tds_read_b64 a[22:23], v0 offset:1480\n\tv_mfma_f32_16x16x32_bf16 v[156:159], a[240:243], v[108:111], v[156:159]\n\tds_read_b64 a[24:25], v0 offset:960\n\tv_mfma_f32_16x16x32_bf16 v[156:159], a[244:247], v[104:107], v[156:159]\n\tds_read_b64 a[26:27], v0 offset:968\n\t" \
    "v_mfma_f32_16x16x32_bf16 v[156:159], a[248:251], v[100:103], v[156:159]\n\tds_read_b64 a[28:29], v0 offset:448\n\tv_mfma_f32_16x16x32_bf16 v[152:155], a[224:227], v[120:123], v[152:155]\n\tbuffer_load_ushort v40, off, s[80:83], s66\n\tds_read_b64 a[30:31], v0 offset:456\n\tv_mfma_f32_16x16x32_bf16 v[152:155], a[228:231], v[116:119], v[152:155]\n\tds_read_b128 v[68:71], v1 offset:18944\n\tv_mfma_f32_16x16x32_bf16 v[152:155], a[232:235], v[112:115], v[152:155]\n\t" \
    "ds_read_b128 v[72:75], v1 offset:19456\n\tv_mfma_f32_16x16x32_bf16 v[152:155], a[236:239], v[108:111], v[152:155]\n\tds_read_b128 v[76:79], v1 offset:19968\n\tv_mfma_f32_16x16x32_bf16 v[152:155], a[240:243], v[104:107], v[152:155]\n\tds_read_b128 v[80:83], v1 offset:20480\n\tv_mfma_f32_16x16x32_bf16 v[152:155], a[244:247], v[100:103], v[152:155]\n\tds_read_b128 v[84:87], v1 offset:20992\n\tv_mfma_f32_16x16x32_bf16 v[148:151], a[224:227], v[116:119], v[148:151]\n\t" \
    "s_lshl_b32 s69, s67, 13\n\tbuffer_load_dword v15, v14, s[4:7], s69 offen\n\tds_read_b128 v[88:91], v1 offset:21504\n\tv_mfma_f32_16x16x32_bf16 v[148:151], a[228:231], v[112:115], v[148:151]\n\tds_read_b128 v[92:95], v1 offset:22016\n\tv_mfma_f32_16x16x32_bf16 v[148:151], a[232:235], v[108:111], v[148:151]\n\tds_read_b128 v[96:99], v1 offset:22528\n\tv_mfma_f32_16x16x32_bf16 v[148:151], a[236:239], v[104:107], v[148:151]\n\t" \
    "v_mfma_f32_16x16x32_bf16 v[148:151], a[240:243], v[100:103], v[148:151]\n\tv_mfma_f32_16x16x32_bf16 v[144:147], a[224:227], v[112:115], v[144:147]\n\tv_mfma_f32_16x16x32_bf16 v[144:147], a[228:231], v[108:111], v[144:147]\n\tv_mfma_f32_16x16x32_bf16 v[144:147], a[232:235], v[104:107], v[144:147]\n\tv_mfma_f32_16x16x32_bf16 v[144:147], a[236:239], v[100:103], v[144:147]\n\tv_mfma_f32_16x16x32_bf16 v[140:143], a[224:227], v[108:111], v[140:143]\n\tv_mfma_f32_16x16x32_bf16 v[140:143], a[228:231], v[104:107], v[140:143]\n\tv_mfma_f32_16x16x32_bf16 v[140:143], a[232:235], v[100:103], v[140:143]\n\t" \
    "v_mfma_f32_16x16x32_bf16 v[136:139], a[224:227], v[104:107], v[136:139]\n\tv_mfma_f32_16x16x32_bf16 v[136:139], a[228:231], v[100:103], v[136:139]\n\tv_mfma_f32_16x16x32_bf16 v[132:135], a[224:227], v[100:103], v[132:135]\n\ts_waitcnt lgkmcnt(0)\n\tv_mfma_f32_16x16x32_bf16 v[164:167], v[228:231], v[68:71], 0\n\tds_read_b64 a[32:33], v0 offset:3968\n\tds_read_b64 a[34:35], v0 offset:3976\n\tv_mfma_f32_16x16x32_bf16 v[164:167], a[0:3], v[68:71], v[164:167]\n\t" \
    "ds_read_b64 a[36:37], v0 offset:3456\n\tds_read_b64 a[38:39], v0 offset:3464\n\tv_mfma_f32_16x16x32_bf16 v[168:171], v[228:231], v[72:75], 0\n\tds_read_b64 a[40:41], v0 offset:2944\n\tds_read_b64 a[42:43], v0 offset:2952\n\tv_mfma_f32_16x16x32_bf16 v[168:171], a[0:3], v[72:75], v[168:171]\n\tds_read_b64 a[44:45], v0 offset:2432\n\tds_read_b64 a[46:47], v0 offset:2440\n\t" \
    "v_mfma_f32_16x16x32_bf16 v[168:171], a[4:7], v[68:71], v[168:171]\n\tds_read_b64 a[48:49], v0 offset:1920\n\tds_read_b64 a[50:51], v0 offset:1928\n\tv_mfma_f32_16x16x32_bf16 v[172:175], v[228:231], v[76:79], 0\n\tds_read_b64 a[52:53], v0 offset:1408\n\tv_cvt_pk_bf16_f32 v132, v132, v133\n\tv_mfma_f32_16x16x32_bf16 v[172:175], a[0:3], v[76:79], v[172:175]\n\tds_read_b64 a[54:55], v0 offset:1416\n\t" \
    "v_cvt_pk_bf16_f32 v133, v134, v135\n\tv_mfma_f32_16x16x32_bf16 v[172:175], a[4:7], v[72:75], v[172:175]\n\tds_read_b64 a[56:57], v0 offset:896\n\tds_write_b64 v3, v[132:133] offset:0\n\tv_mfma_f32_16x16x32_bf16 v[172:175], a[8:11], v[68:71], v[172:175]\n\tds_read_b64 a[58:59], v0 offset:904\n\tv_cvt_pk_bf16_f32 v136, v136, v137\n\tv_mfma_f32_16x16x32_bf16 v[176:179], v[228:231], v[80:83], 0\n\t" \
    "ds_read_b64 a[60:61], v0 offset:384\n\tv_cvt_pk_bf16_f32 v137, v138, v139\n\tv_mfma_f32_16x16x32_bf16 v[176:179], a[0:3], v[80:83], v[176:179]\n\tds_read_b64 a[62:63], v0 offset:392\n\tds_write_b64 v3, v[136:137] offset:512\n\tv_mfma_f32_16x16x32_bf16 v[176:179], a[4:7], v[76:79], v[176:179]\n\tds_read_b128 v[100:103], v1 offset:18880\n\tv_cvt_pk_bf16_f32 v140, v140, v141\n\t" \
    "v_mfma_f32_16x16x32_bf16 v[176:179], a[8:11], v[72:75], v[176:179]\n\tds_read_b128 v[104:107], v1 offset:19392\n\tv_cvt_pk_bf16_f32 v141, v142, v143\n\tv_mfma_f32_16x16x32_bf16 v[176:179], a[12:15], v[68:71], v[176:179]\n\tds_read_b128 v[108:111], v1 offset:19904\n\tds_write_b64 v3, v[140:141] offset:1024\n\tv_mfma_f32_16x16x32_bf16 v[180:183], v[228:231], v[84:87], 0\n\tds_read_b128 v[112:115], v1 offset:20416\n\t" \
    "v_cvt_pk_bf16_f32 v144, v144, v145\n\tv_mfma_f32_16x16x32_bf16 v[180:183], a[0:3], v[84:87], v[180:183]\n\tds_read_b128 v[116:119], v1 offset:20928\n\tv_cvt_pk_bf16_f32 v145, v146, v147\n\tv_mfma_f32_16x16x32_bf16 v[180:183], a[4:7], v[80:83], v[180:183]\n\tds_read_b128 v[120:123], v1 offset:21440\n\tds_write_b64 v3, v[144:145] offset:1536\n\tv_mfma_f32_16x16x32_bf16 v[180:183], a[8:11], v[76:79], v[180:183]\n\t" \
    "ds_read_b128 v[124:127], v1 offset:21952\n\tv_cvt_pk_bf16_f32 v148, v148, v149\n\tv_mfma_f32_16x16x32_bf16 v[180:183], a[12:15], v[72:75], v[180:183]\n\tds_read_b128 v[128:131], v1 offset:22464\n\tv_cvt_pk_bf16_f32 v149, v150, v151\n\tv_mfma_f32_16x16x32_bf16 v[180:183], a[16:19], v[68:71], v[180:183]\n\tds_read_b64 a[64:65], v0 offset:3904\n\tds_write_b64 v3, v[148:149] offset:2048\n\t" \
    "v_mfma_f32_16x16x32_bf16 v[184:187], v[228:231], v[88:91], 0\n\tds_read_b64 a[66:67], v0 offset:3912\n\tv_cvt_pk_bf16_f32 v152, v152, v153\n\tv_mfma_f32_16x16x32_bf16 v[184:187], a[0:3], v[88:91], v[184:187]\n\tds_read_b64 a[68:69], v0 offset:3392\n\tv_cvt_pk_bf16_f32 v153, v154, v155\n\tv_mfma_f32_16x16x32_bf16 v[184:187], a[4:7], v[84:87], v[184:187]\n\tds_read_b64 a[70:71], v0 offset:3400\n\t" \
    "ds_write_b64 v3, v[152:153] offset:2560\n\tv_mfma_f32_16x16x32_bf16 v[184:187], a[8:11], v[80:83], v[184:187]\n\tds_read_b64 a[72:73], v0 offset:2880\n\tv_cvt_pk_bf16_f32 v156, v156, v157\n\tv_mfma_f32_16x16x32_bf16 v[184:187], a[12:15], v[76:79], v[184:187]\n\tds_read_b64 a[74:75], v0 offset:2888\n\tv_cvt_pk_bf16_f32 v157, v158, v159\n\tv_mfma_f32_16x16x32_bf16 v[184:187], a[16:19], v[72:75], v[184:187]\n\t" \
    "ds_read_b64 a[76:77], v0 offset:2368\n\tds_write_b64 v3, v[156:157] offset:3072\n\tv_mfma_f32_16x16x32_bf16 v[184:187], a[20:23], v[68:71], v[184:187]\n\tds_read_b64 a[78:79], v0 offset:2376\n\tv_cvt_pk_bf16_f32 v160, v160, v161\n\tv_mfma_f32_16x16x32_bf16 v[188:191], v[228:231], v[92:95], 0\n\tds_read_b64 a[80:81], v0 offset:1856\n\tv_cvt_pk_bf16_f32 v161, v162, v163\n\t" \
    "v_mfma_f32_16x16x32_bf16 v[188:191], a[0:3], v[92:95], v[188:191]\n\tds_read_b64 a[82:83], v0 offset:1864\n\tds_write_b64 v3, v[160:161] offset:3584\n\tv_mfma_f32_16x16x32_bf16 v[188:191], a[4:7], v[88:91], v[188:191]\n\tbuffer_load_dword v15, v6, s[12:15], s41 offen\n\tds_read_b64 a[84:85], v0 offset:1344\n\tv_mfma_f32_16x16x32_bf16 v[188:191], a[8:11], v[84:87], v[188:191]\n\tbuffer_load_dword v15, v7, s[16:19], s42 offen\n\t" \
    "ds_read_b64 a[86:87], v0 offset:1352\n\tv_mfma_f32_16x16x32_bf16 v[188:191], a[12:15], v[80:83], v[188:191]\n\ts_add_u32 s43, s43, 1\n\tds_read_b64 a[88:89], v0 offset:832\n\tv_mfma_f32_16x16x32_bf16 v[188:191], a[16:19], v[76:79], v[188:191]\n\tv_readlane_b32 s41, v236, s43\n\tv_readlane_b32 s42, v237, s43\n\ts_nop 4\n\t" \
    "ds_read_b64 a[90:91], v0 offset:840\n\tv_mfma_f32_16x16x32_bf16 v[188:191], a[20:23], v[72:75], v[188:191]\n\tds_read_b64 a[92:93], v0 offset:320\n\tv_mfma_f32_16x16x32_bf16 v[188:191], a[24:27], v[68:71], v[188:191]\n\tds_read_b64 a[94:95], v0 offset:328\n\tv_mfma_f32_16x16x32_bf16 v[192:195], v[228:231], v[96:99], 0\n\tds_read_b64 a[96:97], v0 offset:3840\n\tv_mfma_f32_16x16x32_bf16 v[192:195], a[0:3], v[96:99], v[192:195]\n\t" \
    "ds_read_b64 a[98:99], v0 offset:3848\n\tv_mfma_f32_16x16x32_bf16 v[192:195], a[4:7], v[92:95], v[192:195]\n\tds_read_b64 a[100:101], v0 offset:3328\n\tv_mfma_f32_16x16x32_bf16 v[192:195], a[8:11], v[88:91], v[192:195]\n\tds_read_b64 a[102:103], v0 offset:3336\n\tv_mfma_f32_16x16x32_bf16 v[192:195], a[12:15], v[84:87], v[192:195]\n\tv_mfma_f32_16x16x32_bf16 v[192:195], a[16:19], v[80:83], v[192:195]\n\tv_mfma_f32_16x16x32_bf16 v[192:195], a[20:23], v[76:79], v[192:195]\n\t" \
    "v_mfma_f32_16x16x32_bf16 v[192:195], a[24:27], v[72:75], v[192:195]\n\tv_mfma_f32_16x16x32_bf16 v[192:195], a[28:31], v[68:71], v[192:195]\n\ts_waitcnt lgkmcnt(10)\n\ts_barrier\n\tv_mfma_f32_16x16x32_bf16 v[192:195], a[32:35], v[128:131], v[192:195]\n\tds_read_b64 a[104:105], v0 offset:2816\n\tds_read_b64 a[106:107], v0 offset:2824\n\tv_mfma_f32_16x16x32_bf16 v[192:195], a[36:39], v[124:127], v[192:195]\n\t" \
    "ds_read_b64 a[108:109], v0 offset:2304\n\tds_read_b64 a[110:111], v0 offset:2312\n\tv_mfma_f32_16x16x32_bf16 v[192:195], a[40:43], v[120:123], v[192:195]\n\tds_read_b64 a[112:113], v0 offset:1792\n\tds_read_b64 a[114:115], v0 offset:1800\n\tv_mfma_f32_16x16x32_bf16 v[192:195], a[44:47], v[116:119], v[192:195]\n\tds_read_b64 a[116:117], v0 offset:1280\n\tds_read_b64 a[118:119], v0 offset:1288\n\t" \
    "v_mfma_f32_16x16x32_bf16 v[192:195], a[48:51], v[112:115], v[192:195]\n\tds_read_b64 a[120:121], v0 offset:768\n\tds_read_b64 a[122:123], v0 offset:776\n\tv_mfma_f32_16x16x32_bf16 v[192:195], a[52:55], v[108:111], v[192:195]\n\tds_read_b64 a[124:125], v0 offset:256\n\ts_add_u32 s51, s44, 0\n\tv_mfma_f32_16x16x32_bf16 v[192:195], a[56:59], v[104:107], v[192:195]\n\tds_read_b64 a[126:127], v0 offset:264\n\t" \
    "s_mul_i32 s65, s51, 1366\n\tv_mfma_f32_16x16x32_bf16 v[192:195], a[60:63], v[100:103], v[192:195]\n\tds_read_b64 a[128:129], v0 offset:3776\n\ts_lshr_b32 s65, s65, 16\n\tv_mfma_f32_16x16x32_bf16 v[188:191], a[32:35], v[124:127], v[188:191]\n\tds_read_b64 a[130:131], v0 offset:3784\n\ts_mul_i32 s56, s65, 48\n\tv_mfma_f32_16x16x32_bf16 v[188:191], a[36:39], v[120:123], v[188:191]\n\t" \
    "ds_read_b64 a[132:133], v0 offset:3264\n\ts_sub_u32 s56, s51, s56\n\tv_mfma_f32_16x16x32_bf16 v[188:191], a[40:43], v[116:119], v[188:191]\n\tds_read_b64 a[134:135], v0 offset:3272\n\ts_lshl_b32 s56, s56, 6\n\tv_mfma_f32_16x16x32_bf16 v[188:191], a[44:47], v[112:115], v[188:191]\n\tds_read_b128 v[68:71], v1 offset:18816\n\ts_lshr_b32 s70, s65, 2\n\t" \
    "v_mfma_f32_16x16x32_bf16 v[188:191], a[48:51], v[108:111], v[188:191]\n\tds_read_b128 v[72:75], v1 offset:19328\n\ts_mul_i32 s70, s70, 252\n\tv_mfma_f32_16x16x32_bf16 v[188:191], a[52:55], v[104:107], v[188:191]\n\tds_read_b128 v[76:79], v1 offset:19840\n\ts_add_u32 s65, s65, s70\n\tv_mfma_f32_16x16x32_bf16 v[188:191], a[56:59], v[100:103], v[188:191]\n\tds_read_b128 v[80:83], v1 offset:20352\n\t" \
    "s_mul_i32 s65, s65, 3072\n\tv_mfma_f32_16x16x32_bf16 v[184:187], a[32:35], v[120:123], v[184:187]\n\tds_read_b128 v[84:87], v1 offset:20864\n\ts_add_u32 s56, s56, s65\n\tv_mfma_f32_16x16x32_bf16 v[184:187], a[36:39], v[116:119], v[184:187]\n\tds_read_b128 v[88:91], v1 offset:21376\n\ts_add_u32 s56, s56, 4849664\n\tv_mfma_f32_16x16x32_bf16 v[184:187], a[40:43], v[112:115], v[184:187]\n\t" \
    "ds_read_b128 v[92:95], v1 offset:21888\n\ts_add_u32 s51, s51, s38\n\tv_mfma_f32_16x16x32_bf16 v[184:187], a[44:47], v[108:111], v[184:187]\n\tds_read_b128 v[96:99], v1 offset:22400\n\ts_lshl_b32 s51, s51, 3\n\tv_mfma_f32_16x16x32_bf16 v[184:187], a[48:51], v[104:107], v[184:187]\n\tds_read_b64 a[136:137], v0 offset:2752\n\ts_add_u32 s51, s51, 50176\n\t" \
    "ds_read_b64_tr_b16 v[204:205], v4 offset:0\n\tv_mfma_f32_16x16x32_bf16 v[184:187], a[52:55], v[100:103], v[184:187]\n\tds_read_b64 a[138:139], v0 offset:2760\n\tds_read_b64_tr_b16 v[206:207], v4 offset:8\n\tv_mfma_f32_16x16x32_bf16 v[180:183], a[32:35], v[116:119], v[180:183]\n\tds_read_b64 a[140:141], v0 offset:2240\n\tds_read_b64_tr_b16 v[208:209], v4 offset:64\n\tv_mfma_f32_16x16x32_bf16 v[180:183], a[36:39], v[112:115], v[180:183]\n\t" \
    "ds_read_b64 a[142:143], v0 offset:2248\n\tds_read_b64_tr_b16 v[210:211], v4 offset:72\n\tv_mfma_f32_16x16x32_bf16 v[180:183], a[40:43], v[108:111], v[180:183]\n\tds_read_b64 a[144:145], v0 offset:1728\n\tds_read_b64_tr_b16 v[212:213], v4 offset:512\n\tv_mfma_f32_16x16x32_bf16 v[180:183], a[44:47], v[104:107], v[180:183]\n\tds_read_b64 a[146:147], v0 offset:1736\n\tds_read_b64_tr_b16 v[214:215], v4 offset:520\n\t" \
    "v_mfma_f32_16x16x32_bf16 v[180:183], a[48:51], v[100:103], v[180:183]\n\tds_read_b64 a[148:149], v0 offset:1216\n\tds_read_b64_tr_b16 v[216:217], v4 offset:576\n\tv_mfma_f32_16x16x32_bf16 v[176:179], a[32:35], v[112:115], v[176:179]\n\tds_read_b64 a[150:151], v0 offset:1224\n\tds_read_b64_tr_b16 v[218:219], v4 offset:584\n\tv_mfma_f32_16x16x32_bf16 v[176:179], a[36:39], v[108:111], v[176:179]\n\tds_read_b64 a[152:153], v0 offset:704\n\t" \
    "v_mfma_f32_16x16x32_bf16 v[176:179], a[40:43], v[104:107], v[176:179]\n\tds_read_b64 a[154:155], v0 offset:712\n\tv_mfma_f32_16x16x32_bf16 v[176:179], a[44:47], v[100:103], v[176:179]\n\tds_read_b64 a[156:157], v0 offset:192\n\tv_mfma_f32_16x16x32_bf16 v[172:175], a[32:35], v[108:111], v[172:175]\n\tds_read_b64 a[158:159], v0 offset:200\n\tv_mfma_f32_16x16x32_bf16 v[172:175], a[36:39], v[104:107], v[172:175]\n\tv_mfma_f32_16x16x32_bf16 v[172:175], a[40:43], v[100:103], v[172:175]\n\t" \
    "s_waitcnt lgkmcnt(15)\n\tbuffer_store_dwordx4 v[204:207], v5, s[8:11], s56 offen sc1\n\ts_add_u32 s56, s56, 49152\n\tv_mfma_f32_16x16x32_bf16 v[168:171], a[32:35], v[104:107], v[168:171]\n\tv_mfma_f32_16x16x32_bf16 v[168:171], a[36:39], v[100:103], v[168:171]\n\tv_mfma_f32_16x16x32_bf16 v[164:167], a[32:35], v[100:103], v[164:167]\n\tv_mfma_f32_16x16x32_bf16 v[164:167], a[64:67], v[68:71], v[164:167]\n\tds_read_b64 a[160:161], v0 offset:3712\n\t" \
    "ds_read_b64 a[162:163], v0 offset:3720\n\tv_mfma_f32_16x16x32_bf16 v[168:171], a[64:67], v[72:75], v[168:171]\n\tds_read_b64 a[164:165], v0 offset:3200\n\tds_read_b64 a[166:167], v0 offset:3208\n\tv_mfma_f32_16x16x32_bf16 v[168:171], a[68:71], v[68:71], v[168:171]\n\tds_read_b64 a[168:169], v0 offset:2688\n\tds_read_b64 a[170:171], v0 offset:2696\n\tv_mfma_f32_16x16x32_bf16 v[172:175], a[64:67], v[76:79], v[172:175]\n\t" \
    "ds_read_b64 a[172:173], v0 offset:2176\n\tds_read_b64 a[174:175], v0 offset:2184\n\tv_mfma_f32_16x16x32_bf16 v[172:175], a[68:71], v[72:75], v[172:175]\n\tds_read_b64 a[176:177], v0 offset:1664\n\tds_read_b64 a[178:179], v0 offset:1672\n\tv_mfma_f32_16x16x32_bf16 v[172:175], a[72:75], v[68:71], v[172:175]\n\tds_read_b64 a[180:181], v0 offset:1152\n\tv_mfma_f32_16x16x32_bf16 v[176:179], a[64:67], v[80:83], v[176:179]\n\t" \
    "ds_read_b64 a[182:183], v0 offset:1160\n\tv_mfma_f32_16x16x32_bf16 v[176:179], a[68:71], v[76:79], v[176:179]\n\tds_read_b64 a[184:185], v0 offset:640\n\tv_mfma_f32_16x16x32_bf16 v[176:179], a[72:75], v[72:75], v[176:179]\n\tds_read_b64 a[186:187], v0 offset:648\n\tv_mfma_f32_16x16x32_bf16 v[176:179], a[76:79], v[68:71], v[176:179]\n\tds_read_b64 a[188:189], v0 offset:128\n\tv_mfma_f32_16x16x32_bf16 v[180:183], a[64:67], v[84:87], v[180:183]\n\t" \
    "ds_read_b64 a[190:191], v0 offset:136\n\tv_mfma_f32_16x16x32_bf16 v[180:183], a[68:71], v[80:83], v[180:183]\n\tds_read_b128 v[100:103], v1 offset:18752\n\tv_mfma_f32_16x16x32_bf16 v[180:183], a[72:75], v[76:79], v[180:183]\n\tds_read_b128 v[104:107], v1 offset:19264\n\tv_mfma_f32_16x16x32_bf16 v[180:183], a[76:79], v[72:75], v[180:183]\n\tds_read_b128 v[108:111], v1 offset:19776\n\tv_mfma_f32_16x16x32_bf16 v[180:183], a[80:83], v[68:71], v[180:183]\n\t" \
    "ds_read_b128 v[112:115], v1 offset:20288\n\ts_waitcnt lgkmcnt(15)\n\tbuffer_store_dwordx4 v[208:211], v5, s[8:11], s56 offen sc1\n\tv_mfma_f32_16x16x32_bf16 v[184:187], a[64:67], v[88:91], v[184:187]\n\tds_read_b128 v[116:119], v1 offset:20800\n\ts_add_u32 s56, s56, 49152\n\tv_mfma_f32_16x16x32_bf16 v[184:187], a[68:71], v[84:87], v[184:187]\n\tds_read_b128 v[120:123], v1 offset:21312\n\t" \
    "v_mfma_f32_16x16x32_bf16 v[184:187], a[72:75], v[80:83], v[184:187]\n\tds_read_b128 v[124:127], v1 offset:21824\n\tv_mfma_f32_16x16x32_bf16 v[184:187], a[76:79], v[76:79], v[184:187]\n\tds_read_b128 v[128:131], v1 offset:22336\n\tv_mfma_f32_16x16x32_bf16 v[184:187], a[80:83], v[72:75], v[184:187]\n\tds_read_b64 a[192:193], v0 offset:3648\n\tv_mfma_f32_16x16x32_bf16 v[184:187], a[84:87], v[68:71], v[184:187]\n\tds_read_b64 a[194:195], v0 offset:3656\n\t" \
    "v_mfma_f32_16x16x32_bf16 v[188:191], a[64:67], v[92:95], v[188:191]\n\tds_read_b64 a[196:197], v0 offset:3136\n\tv_mfma_f32_16x16x32_bf16 v[188:191], a[68:71], v[88:91], v[188:191]\n\tds_read_b64 a[198:199], v0 offset:3144\n\tv_mfma_f32_16x16x32_bf16 v[188:191], a[72:75], v[84:87], v[188:191]\n\tds_read_b64 a[200:201], v0 offset:2624\n\tv_mfma_f32_16x16x32_bf16 v[188:191], a[76:79], v[80:83], v[188:191]\n\tds_read_b64 a[202:203], v0 offset:2632\n\t" \
    "v_mfma_f32_16x16x32_bf16 v[188:191], a[80:83], v[76:79], v[188:191]\n\tds_read_b64 a[204:205], v0 offset:2112\n\tv_mfma_f32_16x16x32_bf16 v[188:191], a[84:87], v[72:75], v[188:191]\n\tds_read_b64 a[206:207], v0 offset:2120\n\tv_mfma_f32_16x16x32_bf16 v[188:191], a[88:91], v[68:71], v[188:191]\n\tds_read_b64 a[208:209], v0 offset:1600\n\tv_mfma_f32_16x16x32_bf16 v[192:195], a[64:67], v[96:99], v[192:195]\n\tds_read_b64 a[210:211], v0 offset:1608\n\t" \
    "v_mfma_f32_16x16x32_bf16 v[192:195], a[68:71], v[92:95], v[192:195]\n\tds_read_b64 a[212:213], v0 offset:1088\n\tv_mfma_f32_16x16x32_bf16 v[192:195], a[72:75], v[88:91], v[192:195]\n\tds_read_b64 a[214:215], v0 offset:1096\n\tv_mfma_f32_16x16x32_bf16 v[192:195], a[76:79], v[84:87], v[192:195]\n\tbuffer_store_dwordx4 v[212:215], v5, s[8:11], s56 offen sc1\n\ts_add_u32 s56, s56, 49152\n\tv_mfma_f32_16x16x32_bf16 v[192:195], a[80:83], v[80:83], v[192:195]\n\t" \
    "v_mfma_f32_16x16x32_bf16 v[192:195], a[84:87], v[76:79], v[192:195]\n\tv_mfma_f32_16x16x32_bf16 v[192:195], a[88:91], v[72:75], v[192:195]\n\tv_mfma_f32_16x16x32_bf16 v[192:195], a[92:95], v[68:71], v[192:195]\n\ts_waitcnt lgkmcnt(12)\n\tv_mfma_f32_16x16x32_bf16 v[192:195], a[96:99], v[128:131], v[192:195]\n\tds_read_b64 a[216:217], v0 offset:576\n\tv_mfma_f32_16x16x32_bf16 v[192:195], a[100:103], v[124:127], v[192:195]\n\tds_read_b64 a[218:219], v0 offset:584\n\t" \
    "v_mfma_f32_16x16x32_bf16 v[192:195], a[104:107], v[120:123], v[192:195]\n\tds_read_b64 a[220:221], v0 offset:64\n\tv_mfma_f32_16x16x32_bf16 v[192:195], a[108:111], v[116:119], v[192:195]\n\tds_read_b64 a[222:223], v0 offset:72\n\tv_mfma_f32_16x16x32_bf16 v[192:195], a[112:115], v[112:115], v[192:195]\n\tds_read_b64 a[224:225], v0 offset:3584\n\tv_mfma_f32_16x16x32_bf16 v[192:195], a[116:119], v[108:111], v[192:195]\n\tds_read_b64 a[226:227], v0 offset:3592\n\t" \
    "v_mfma_f32_16x16x32_bf16 v[192:195], a[120:123], v[104:107], v[192:195]\n\tds_read_b64 a[228:229], v0 offset:3072\n\tv_mfma_f32_16x16x32_bf16 v[192:195], a[124:127], v[100:103], v[192:195]\n\tds_read_b64 a[230:231], v0 offset:3080\n\tbuffer_store_dwordx4 v[216:219], v5, s[8:11], s56 offen sc1\n\tv_mfma_f32_16x16x32_bf16 v[188:191], a[96:99], v[124:127], v[188:191]\n\tds_read_b64 a[232:233], v0 offset:2560\n\ts_add_u32 s56, s56, 49152\n\t" \
    "v_mfma_f32_16x16x32_bf16 v[188:191], a[100:103], v[120:123], v[188:191]\n\tds_read_b64 a[234:235], v0 offset:2568\n\tv_mfma_f32_16x16x32_bf16 v[188:191], a[104:107], v[116:119], v[188:191]\n\tds_read_b64 a[236:237], v0 offset:2048\n\tv_mfma_f32_16x16x32_bf16 v[188:191], a[108:111], v[112:115], v[188:191]\n\tds_read_b64 a[238:239], v0 offset:2056\n\tv_mfma_f32_16x16x32_bf16 v[188:191], a[112:115], v[108:111], v[188:191]\n\tbuffer_load_dword v15, v7, s[16:19], s42 offen\n\t" \
    "ds_read_b64 a[240:241], v0 offset:1536\n\tv_mfma_f32_16x16x32_bf16 v[188:191], a[116:119], v[104:107], v[188:191]\n\ts_add_u32 s43, s43, 1\n\tds_read_b64 a[242:243], v0 offset:1544\n\tv_mfma_f32_16x16x32_bf16 v[188:191], a[120:123], v[100:103], v[188:191]\n\tv_readlane_b32 s42, v237, s43\n\ts_nop 4\n\tds_read_b64 a[244:245], v0 offset:1024\n\t" \
    "v_mfma_f32_16x16x32_bf16 v[184:187], a[96:99], v[120:123], v[184:187]\n\tds_read_b64 a[246:247], v0 offset:1032\n\tv_mfma_f32_16x16x32_bf16 v[184:187], a[100:103], v[116:119], v[184:187]\n\tds_read_b128 v[68:71], v1 offset:18688\n\tv_mfma_f32_16x16x32_bf16 v[184:187], a[104:107], v[112:115], v[184:187]\n\tds_read_b128 v[72:75], v1 offset:19200\n\tv_mfma_f32_16x16x32_bf16 v[184:187], a[108:111], v[108:111], v[184:187]\n\tds_read_b128 v[76:79], v1 offset:19712\n\t" \
    "v_mfma_f32_16x16x32_bf16 v[184:187], a[112:115], v[104:107], v[184:187]\n\tds_read_b128 v[80:83], v1 offset:20224\n\tv_mfma_f32_16x16x32_bf16 v[184:187], a[116:119], v[100:103], v[184:187]\n\tds_read_b128 v[84:87], v1 offset:20736\n\tv_mfma_f32_16x16x32_bf16 v[180:183], a[96:99], v[116:119], v[180:183]\n\tds_read_b128 v[88:91], v1 offset:21248\n\tv_mfma_f32_16x16x32_bf16 v[180:183], a[100:103], v[112:115], v[180:183]\n\tds_read_b128 v[92:95], v1 offset:21760\n\t" \
    "v_mfma_f32_16x16x32_bf16 v[180:183], a[104:107], v[108:111], v[180:183]\n\tds_read_b128 v[96:99], v1 offset:22272\n\tv_mfma_f32_16x16x32_bf16 v[180:183], a[108:111], v[104:107], v[180:183]\n\tds_read_b64 a[248:249], v0 offset:512\n\tv_mfma_f32_16x16x32_bf16 v[180:183], a[112:115], v[100:103], v[180:183]\n\tds_read_b64 a[250:251], v0 offset:520\n\tv_mfma_f32_16x16x32_bf16 v[176:179], a[96:99], v[112:115], v[176:179]\n\tds_read_b64 a[252:253], v0 offset:0\n\t" \
    "v_mfma_f32_16x16x32_bf16 v[176:179], a[100:103], v[108:111], v[176:179]\n\tds_read_b64 a[254:255], v0 offset:8\n\tv_mfma_f32_16x16x32_bf16 v[176:179], a[104:107], v[104:107], v[176:179]\n\tv_mfma_f32_16x16x32_bf16 v[176:179], a[108:111], v[100:103], v[176:179]\n\tv_mfma_f32_16x16x32_bf16 v[172:175], a[96:99], v[108:111], v[172:175]\n\tv_mfma_f32_16x16x32_bf16 v[172:175], a[100:103], v[104:107], v[172:175]\n\tv_mfma_f32_16x16x32_bf16 v[172:175], a[104:107], v[100:103], v[172:175]\n\tv_mfma_f32_16x16x32_bf16 v[168:171], a[96:99], v[104:107], v[168:171]\n\t" \
    "v_mfma_f32_16x16x32_bf16 v[168:171], a[100:103], v[100:103], v[168:171]\n\tv_mfma_f32_16x16x32_bf16 v[164:167], a[96:99], v[100:103], v[164:167]\n\ts_waitcnt lgkmcnt(4)\n\tv_mfma_f32_16x16x32_bf16 v[164:167], a[128:131], v[68:71], v[164:167]\n\tds_read_b128 v[100:103], v1 offset:18624\n\ts_waitcnt vmcnt(8)\n\tv_alignbit_b32 v196, v19, v19, 16\n\tv_mfma_f32_16x16x32_bf16 v[168:171], a[128:131], v[72:75], v[168:171]\n\t" \
    "ds_read_b128 v[104:107], v1 offset:19136\n\tv_alignbit_b32 v197, v18, v18, 16\n\tv_mfma_f32_16x16x32_bf16 v[168:171], a[132:135], v[68:71], v[168:171]\n\tds_read_b128 v[108:111], v1 offset:19648\n\tv_alignbit_b32 v198, v17, v17, 16\n\tv_mfma_f32_16x16x32_bf16 v[172:175], a[128:131], v[76:79], v[172:175]\n\tds_read_b128 v[112:115], v1 offset:20160\n\tv_alignbit_b32 v199, v16, v16, 16\n\t" \
    "v_mfma_f32_16x16x32_bf16 v[172:175], a[132:135], v[72:75], v[172:175]\n\tds_write_b128 v2, v[196:199] offset:3072\n\tv_mfma_f32_16x16x32_bf16 v[172:175], a[136:139], v[68:71], v[172:175]\n\tds_read_b128 v[116:119], v1 offset:20672\n\tv_bfi_b32 v200, s46, v19, v18\n\tv_mfma_f32_16x16x32_bf16 v[176:179], a[128:131], v[80:83], v[176:179]\n\tds_read_b128 v[120:123], v1 offset:21184\n\tv_bfi_b32 v201, s46, v18, v17\n\t" \
    "v_mfma_f32_16x16x32_bf16 v[176:179], a[132:135], v[76:79], v[176:179]\n\tds_read_b128 v[124:127], v1 offset:21696\n\tv_bfi_b32 v202, s46, v17, v16\n\tv_mfma_f32_16x16x32_bf16 v[176:179], a[136:139], v[72:75], v[176:179]\n\tds_read_b128 v[128:131], v1 offset:22208\n\tv_bfi_b32 v203, s46, v16, v33\n\tv_mfma_f32_16x16x32_bf16 v[176:179], a[140:143], v[68:71], v[176:179]\n\tds_write_b128 v2, v[200:203] offset:7232\n\t" \
    "v_mfma_f32_16x16x32_bf16 v[180:183], a[128:131], v[84:87], v[180:183]\n\tv_alignbit_b32 v196, v18, v18, 16\n\tv_alignbit_b32 v197, v17, v17, 16\n\tv_mfma_f32_16x16x32_bf16 v[180:183], a[132:135], v[80:83], v[180:183]\n\tv_alignbit_b32 v198, v16, v16, 16\n\tv_alignbit_b32 v199, v33, v33, 16\n\tv_mfma_f32_16x16x32_bf16 v[180:183], a[136:139], v[76:79], v[180:183]\n\tds_write_b128 v2, v[196:199] offset:11392\n\t" \
    "v_mfma_f32_16x16x32_bf16 v[180:183], a[140:143], v[72:75], v[180:183]\n\tv_bfi_b32 v200, s46, v18, v17\n\tv_bfi_b32 v201, s46, v17, v16\n\tv_mfma_f32_16x16x32_bf16 v[180:183], a[144:147], v[68:71], v[180:183]\n\tv_bfi_b32 v202, s46, v16, v33\n\tv_bfi_b32 v203, s46, v33, v32\n\tv_mfma_f32_16x16x32_bf16 v[184:187], a[128:131], v[88:91], v[184:187]\n\tds_write_b128 v2, v[200:203] offset:15552\n\t" \
    "v_mfma_f32_16x16x32_bf16 v[184:187], a[132:135], v[84:87], v[184:187]\n\tv_alignbit_b32 v196, v23, v23, 16\n\tv_alignbit_b32 v197, v22, v22, 16\n\tv_mfma_f32_16x16x32_bf16 v[184:187], a[136:139], v[80:83], v[184:187]\n\tv_alignbit_b32 v198, v21, v21, 16\n\tv_alignbit_b32 v199, v20, v20, 16\n\tv_mfma_f32_16x16x32_bf16 v[184:187], a[140:143], v[76:79], v[184:187]\n\tds_write_b128 v2, v[196:199] offset:2048\n\t" \
    "v_mfma_f32_16x16x32_bf16 v[184:187], a[144:147], v[72:75], v[184:187]\n\tv_bfi_b32 v200, s46, v23, v22\n\tv_bfi_b32 v201, s46, v22, v21\n\tv_mfma_f32_16x16x32_bf16 v[184:187], a[148:151], v[68:71], v[184:187]\n\tv_bfi_b32 v202, s46, v21, v20\n\tv_bfi_b32 v203, s46, v20, v35\n\tv_mfma_f32_16x16x32_bf16 v[188:191], a[128:131], v[92:95], v[188:191]\n\tds_write_b128 v2, v[200:203] offset:6208\n\t" \
    "v_mfma_f32_16x16x32_bf16 v[188:191], a[132:135], v[88:91], v[188:191]\n\tv_alignbit_b32 v196, v22, v22, 16\n\tv_alignbit_b32 v197, v21, v21, 16\n\tv_mfma_f32_16x16x32_bf16 v[188:191], a[136:139], v[84:87], v[188:191]\n\tv_alignbit_b32 v198, v20, v20, 16\n\tv_alignbit_b32 v199, v35, v35, 16\n\tv_mfma_f32_16x16x32_bf16 v[188:191], a[140:143], v[80:83], v[188:191]\n\tds_write_b128 v2, v[196:199] offset:10368\n\t" \
    "v_mfma_f32_16x16x32_bf16 v[188:191], a[144:147], v[76:79], v[188:191]\n\tv_bfi_b32 v200, s46, v22, v21\n\tv_bfi_b32 v201, s46, v21, v20\n\tv_mfma_f32_16x16x32_bf16 v[188:191], a[148:151], v[72:75], v[188:191]\n\tv_bfi_b32 v202, s46, v20, v35\n\tv_bfi_b32 v203, s46, v35, v34\n\tv_mfma_f32_16x16x32_bf16 v[188:191], a[152:155], v[68:71], v[188:191]\n\tds_write_b128 v2, v[200:203] offset:14528\n\t" \
    "v_mfma_f32_16x16x32_bf16 v[192:195], a[128:131], v[96:99], v[192:195]\n\tv_alignbit_b32 v196, v27, v27, 16\n\tv_alignbit_b32 v197, v26, v26, 16\n\tv_mfma_f32_16x16x32_bf16 v[192:195], a[132:135], v[92:95], v[192:195]\n\tv_alignbit_b32 v198, v25, v25, 16\n\tv_alignbit_b32 v199, v24, v24, 16\n\tv_mfma_f32_16x16x32_bf16 v[192:195], a[136:139], v[88:91], v[192:195]\n\tds_write_b128 v2, v[196:199] offset:1024\n\t" \
    "v_mfma_f32_16x16x32_bf16 v[192:195], a[140:143], v[84:87], v[192:195]\n\tv_bfi_b32 v200, s46, v27, v26\n\tv_bfi_b32 v201, s46, v26, v25\n\tv_mfma_f32_16x16x32_bf16 v[192:195], a[144:147], v[80:83], v[192:195]\n\tv_bfi_b32 v202, s46, v25, v24\n\tv_bfi_b32 v203, s46, v24, v37\n\tv_mfma_f32_16x16x32_bf16 v[192:195], a[148:151], v[76:79], v[192:195]\n\tds_write_b128 v2, v[200:203] offset:5184\n\t" \
    "v_mfma_f32_16x16x32_bf16 v[192:195], a[152:155], v[72:75], v[192:195]\n\tv_alignbit_b32 v196, v26, v26, 16\n\tv_alignbit_b32 v197, v25, v25, 16\n\tv_mfma_f32_16x16x32_bf16 v[192:195], a[156:159], v[68:71], v[192:195]\n\tv_alignbit_b32 v198, v24, v24, 16\n\tv_alignbit_b32 v199, v37, v37, 16\n\ts_waitcnt lgkmcnt(9)\n\tv_mfma_f32_16x16x32_bf16 v[192:195], a[160:163], v[128:131], v[192:195]\n\t" \
    "ds_write_b128 v2, v[196:199] offset:9344\n\tv_mfma_f32_16x16x32_bf16 v[192:195], a[164:167], v[124:127], v[192:195]\n\tds_read_b128 v[68:71], v1 offset:18560\n\tv_bfi_b32 v200, s46, v26, v25\n\tv_mfma_f32_16x16x32_bf16 v[192:195], a[168:171], v[120:123], v[192:195]\n\tds_read_b128 v[72:75], v1 offset:19072\n\tv_bfi_b32 v201, s46, v25, v24\n\tv_mfma_f32_16x16x32_bf16 v[192:195], a[172:175], v[116:119], v[192:195]\n\t" \
    "ds_read_b128 v[76:79], v1 offset:19584\n\tv_bfi_b32 v202, s46, v24, v37\n\tv_mfma_f32_16x16x32_bf16 v[192:195], a[176:179], v[112:115], v[192:195]\n\tds_read_b128 v[80:83], v1 offset:20096\n\tv_bfi_b32 v203, s46, v37, v36\n\tv_mfma_f32_16x16x32_bf16 v[192:195], a[180:183], v[108:111], v[192:195]\n\tds_write_b128 v2, v[200:203] offset:13504\n\tv_mfma_f32_16x16x32_bf16 v[192:195], a[184:187], v[104:107], v[192:195]\n\t" \
    "ds_read_b128 v[84:87], v1 offset:20608\n\tv_alignbit_b32 v196, v31, v31, 16\n\tv_mfma_f32_16x16x32_bf16 v[192:195], a[188:191], v[100:103], v[192:195]\n\tds_read_b128 v[88:91], v1 offset:21120\n\tv_alignbit_b32 v197, v30, v30, 16\n\tv_mfma_f32_16x16x32_bf16 v[188:191], a[160:163], v[124:127], v[188:191]\n\tds_read_b128 v[92:95], v1 offset:21632\n\tv_alignbit_b32 v198, v29, v29, 16\n\t" \
    "v_mfma_f32_16x16x32_bf16 v[188:191], a[164:167], v[120:123], v[188:191]\n\tds_read_b128 v[96:99], v1 offset:22144\n\tv_alignbit_b32 v199, v28, v28, 16\n\tv_mfma_f32_16x16x32_bf16 v[188:191], a[168:171], v[116:119], v[188:191]\n\tds_write_b128 v2, v[196:199] offset:0\n\tv_mfma_f32_16x16x32_bf16 v[188:191], a[172:175], v[112:115], v[188:191]\n\tv_bfi_b32 v200, s46, v31, v30\n\tv_bfi_b32 v201, s46, v30, v29\n\t" \
    "v_mfma_f32_16x16x32_bf16 v[188:191], a[176:179], v[108:111], v[188:191]\n\tv_bfi_b32 v202, s46, v29, v28\n\tv_bfi_b32 v203, s46, v28, v39\n\tv_mfma_f32_16x16x32_bf16 v[188:191], a[180:183], v[104:107], v[188:191]\n\tds_write_b128 v2, v[200:203] offset:4160\n\tv_mfma_f32_16x16x32_bf16 v[188:191], a[184:187], v[100:103], v[188:191]\n\tv_alignbit_b32 v196, v30, v30, 16\n\tv_alignbit_b32 v197, v29, v29, 16\n\t" \
    "v_mfma_f32_16x16x32_bf16 v[184:187], a[160:163], v[120:123], v[184:187]\n\tv_alignbit_b32 v198, v28, v28, 16\n\tv_alignbit_b32 v199, v39, v39, 16\n\tv_mfma_f32_16x16x32_bf16 v[184:187], a[164:167], v[116:119], v[184:187]\n\tds_write_b128 v2, v[196:199] offset:8320\n\tv_mfma_f32_16x16x32_bf16 v[184:187], a[168:171], v[112:115], v[184:187]\n\tv_bfi_b32 v200, s46, v30, v29\n\tv_bfi_b32 v201, s46, v29, v28\n\t" \
    "v_mfma_f32_16x16x32_bf16 v[184:187], a[172:175], v[108:111], v[184:187]\n\tv_bfi_b32 v202, s46, v28, v39\n\tv_bfi_b32 v203, s46, v39, v38\n\tv_mfma_f32_16x16x32_bf16 v[184:187], a[176:179], v[104:107], v[184:187]\n\tds_write_b128 v2, v[200:203] offset:12480\n\tv_mfma_f32_16x16x32_bf16 v[184:187], a[180:183], v[100:103], v[184:187]\n\tv_lshl_or_b32 v40, v40, 16, v40\n\tv_and_b32 v228, v40, v232\n\t" \
    "v_mfma_f32_16x16x32_bf16 v[180:183], a[160:163], v[116:119], v[180:183]\n\tv_and_b32 v229, v40, v233\n\tv_and_b32 v230, v40, v234\n\tv_mfma_f32_16x16x32_bf16 v[180:183], a[164:167], v[112:115], v[180:183]\n\tv_and_b32 v231, v40, v235\n\tv_mfma_f32_16x16x32_bf16 v[180:183], a[168:171], v[108:111], v[180:183]\n\tv_mfma_f32_16x16x32_bf16 v[180:183], a[172:175], v[104:107], v[180:183]\n\tv_mfma_f32_16x16x32_bf16 v[180:183], a[176:179], v[100:103], v[180:183]\n\t" \
    "v_mfma_f32_16x16x32_bf16 v[176:179], a[160:163], v[112:115], v[176:179]\n\tv_mfma_f32_16x16x32_bf16 v[176:179], a[164:167], v[108:111], v[176:179]\n\tv_mfma_f32_16x16x32_bf16 v[176:179], a[168:171], v[104:107], v[176:179]\n\tv_mfma_f32_16x16x32_bf16 v[176:179], a[172:175], v[100:103], v[176:179]\n\tv_mfma_f32_16x16x32_bf16 v[172:175], a[160:163], v[108:111], v[172:175]\n\tv_mfma_f32_16x16x32_bf16 v[172:175], a[164:167], v[104:107], v[172:175]\n\tv_mfma_f32_16x16x32_bf16 v[172:175], a[168:171], v[100:103], v[172:175]\n\tv_mfma_f32_16x16x32_bf16 v[168:171], a[160:163], v[104:107], v[168:171]\n\t" \
    "v_mfma_f32_16x16x32_bf16 v[168:171], a[164:167], v[100:103], v[168:171]\n\tv_mfma_f32_16x16x32_bf16 v[164:167], a[160:163], v[100:103], v[164:167]\n\ts_waitcnt lgkmcnt(4)\n\tv_mfma_f32_16x16x32_bf16 v[164:167], a[192:195], v[68:71], v[164:167]\n\ts_add_u32 s68, s44, s38\n\tds_read_b128 v[100:103], v1 offset:18496\n\tv_mfma_f32_16x16x32_bf16 v[168:171], a[192:195], v[72:75], v[168:171]\n\ts_add_u32 s68, s68, 12\n\t" \
    "ds_read_b128 v[104:107], v1 offset:19008\n\tv_mfma_f32_16x16x32_bf16 v[168:171], a[196:199], v[68:71], v[168:171]\n\ts_lshl_b32 s66, s68, 1\n\tds_read_b128 v[108:111], v1 offset:19520\n\tv_mfma_f32_16x16x32_bf16 v[172:175], a[192:195], v[76:79], v[172:175]\n\ts_lshl_b32 s68, s68, 12\n\tds_read_b128 v[112:115], v1 offset:20032\n\tv_mfma_f32_16x16x32_bf16 v[172:175], a[196:199], v[72:75], v[172:175]\n\t" \
    "s_or_b32 s68, s68, s45\n\tds_read_b128 v[116:119], v1 offset:20544\n\tv_mfma_f32_16x16x32_bf16 v[172:175], a[200:203], v[68:71], v[172:175]\n\ts_or_b32 s66, s66, s45\n\tbuffer_load_dwordx4 v[42:45], v8, s[76:79], s68 offen offset:0\n\tds_read_b128 v[120:123], v1 offset:21056\n\tv_mfma_f32_16x16x32_bf16 v[176:179], a[192:195], v[80:83], v[176:179]\n\tds_read_b128 v[124:127], v1 offset:21568\n\t" \
    "v_mfma_f32_16x16x32_bf16 v[176:179], a[196:199], v[76:79], v[176:179]\n\tds_read_b128 v[128:131], v1 offset:22080\n\tv_mfma_f32_16x16x32_bf16 v[176:179], a[200:203], v[72:75], v[176:179]\n\tv_mfma_f32_16x16x32_bf16 v[176:179], a[204:207], v[68:71], v[176:179]\n\tv_mfma_f32_16x16x32_bf16 v[180:183], a[192:195], v[84:87], v[180:183]\n\tv_mfma_f32_16x16x32_bf16 v[180:183], a[196:199], v[80:83], v[180:183]\n\tbuffer_load_dwordx4 v[46:49], v8, s[76:79], s68 offen offset:1024\n\tv_mfma_f32_16x16x32_bf16 v[180:183], a[200:203], v[76:79], v[180:183]\n\t" \
    "v_mfma_f32_16x16x32_bf16 v[180:183], a[204:207], v[72:75], v[180:183]\n\tv_mfma_f32_16x16x32_bf16 v[180:183], a[208:211], v[68:71], v[180:183]\n\tv_mfma_f32_16x16x32_bf16 v[184:187], a[192:195], v[88:91], v[184:187]\n\tv_mfma_f32_16x16x32_bf16 v[184:187], a[196:199], v[84:87], v[184:187]\n\tv_mfma_f32_16x16x32_bf16 v[184:187], a[200:203], v[80:83], v[184:187]\n\tbuffer_load_dwordx4 v[50:53], v8, s[76:79], s68 offen offset:2048\n\tv_mfma_f32_16x16x32_bf16 v[184:187], a[204:207], v[76:79], v[184:187]\n\tv_mfma_f32_16x16x32_bf16 v[184:187], a[208:211], v[72:75], v[184:187]\n\t" \
    "v_mfma_f32_16x16x32_bf16 v[184:187], a[212:215], v[68:71], v[184:187]\n\tv_mfma_f32_16x16x32_bf16 v[188:191], a[192:195], v[92:95], v[188:191]\n\tv_mfma_f32_16x16x32_bf16 v[188:191], a[196:199], v[88:91], v[188:191]\n\tv_mfma_f32_16x16x32_bf16 v[188:191], a[200:203], v[84:87], v[188:191]\n\tbuffer_load_dwordx4 v[54:57], v8, s[76:79], s68 offen offset:3072\n\tv_mfma_f32_16x16x32_bf16 v[188:191], a[204:207], v[80:83], v[188:191]\n\tv_mfma_f32_16x16x32_bf16 v[188:191], a[208:211], v[76:79], v[188:191]\n\tv_mfma_f32_16x16x32_bf16 v[188:191], a[212:215], v[72:75], v[188:191]\n\t" \
    "v_mfma_f32_16x16x32_bf16 v[188:191], a[216:219], v[68:71], v[188:191]\n\tv_mfma_f32_16x16x32_bf16 v[192:195], a[192:195], v[96:99], v[192:195]\n\tv_mfma_f32_16x16x32_bf16 v[192:195], a[196:199], v[92:95], v[192:195]\n\tbuffer_load_dwordx2 v[58:59], v13, s[76:79], s68 offen\n\tv_mfma_f32_16x16x32_bf16 v[192:195], a[200:203], v[88:91], v[192:195]\n\tv_mfma_f32_16x16x32_bf16 v[192:195], a[204:207], v[84:87], v[192:195]\n\tv_mfma_f32_16x16x32_bf16 v[192:195], a[208:211], v[80:83], v[192:195]\n\tv_mfma_f32_16x16x32_bf16 v[192:195], a[212:215], v[76:79], v[192:195]\n\t" \
    "v_mfma_f32_16x16x32_bf16 v[192:195], a[216:219], v[72:75], v[192:195]\n\tv_mfma_f32_16x16x32_bf16 v[192:195], a[220:223], v[68:71], v[192:195]\n\tbuffer_load_dwordx2 v[60:61], v8, s[76:79], s68 offen offset:1016\n\ts_waitcnt lgkmcnt(0)\n\tv_mfma_f32_16x16x32_bf16 v[192:195], a[224:227], v[128:131], v[192:195]\n\tds_read_b64 a[0:1], v0 offset:4032\n\tv_mfma_f32_16x16x32_bf16 v[192:195], a[228:231], v[124:127], v[192:195]\n\tds_read_b64 a[2:3], v0 offset:4040\n\t" \
    "v_mfma_f32_16x16x32_bf16 v[192:195], a[232:235], v[120:123], v[192:195]\n\tds_read_b64 a[4:5], v0 offset:3520\n\tv_mfma_f32_16x16x32_bf16 v[192:195], a[236:239], v[116:119], v[192:195]\n\tds_read_b64 a[6:7], v0 offset:3528\n\tv_mfma_f32_16x16x32_bf16 v[192:195], a[240:243], v[112:115], v[192:195]\n\tds_read_b64 a[8:9], v0 offset:3008\n\tv_mfma_f32_16x16x32_bf16 v[192:195], a[244:247], v[108:111], v[192:195]\n\tbuffer_load_dwordx2 v[62:63], v8, s[76:79], s68 offen offset:2040\n\t" \
    "ds_read_b64 a[10:11], v0 offset:3016\n\tv_mfma_f32_16x16x32_bf16 v[192:195], a[248:251], v[104:107], v[192:195]\n\tds_read_b64 a[12:13], v0 offset:2496\n\tv_mfma_f32_16x16x32_bf16 v[192:195], a[252:255], v[100:103], v[192:195]\n\tds_read_b64 a[14:15], v0 offset:2504\n\tv_mfma_f32_16x16x32_bf16 v[188:191], a[224:227], v[124:127], v[188:191]\n\tds_read_b64 a[16:17], v0 offset:1984\n\tv_mfma_f32_16x16x32_bf16 v[188:191], a[228:231], v[120:123], v[188:191]\n\t" \
    "ds_read_b64 a[18:19], v0 offset:1992\n\tv_mfma_f32_16x16x32_bf16 v[188:191], a[232:235], v[116:119], v[188:191]\n\tds_read_b64 a[20:21], v0 offset:1472\n\tv_mfma_f32_16x16x32_bf16 v[188:191], a[236:239], v[112:115], v[188:191]\n\tbuffer_load_dwordx2 v[64:65], v8, s[76:79], s68 offen offset:3064\n\tds_read_b64 a[22:23], v0 offset:1480\n\tv_mfma_f32_16x16x32_bf16 v[188:191], a[240:243], v[108:111], v[188:191]\n\tds_read_b64 a[24:25], v0 offset:960\n\t" \
    "v_mfma_f32_16x16x32_bf16 v[188:191], a[244:247], v[104:107], v[188:191]\n\tds_read_b64 a[26:27], v0 offset:968\n\tv_mfma_f32_16x16x32_bf16 v[188:191], a[248:251], v[100:103], v[188:191]\n\tds_read_b64 a[28:29], v0 offset:448\n\tv_mfma_f32_16x16x32_bf16 v[184:187], a[224:227], v[120:123], v[184:187]\n\tds_read_b64 a[30:31], v0 offset:456\n\tv_mfma_f32_16x16x32_bf16 v[184:187], a[228:231], v[116:119], v[184:187]\n\tds_read_b128 v[68:71], v1 offset:37440\n\t" \
    "v_mfma_f32_16x16x32_bf16 v[184:187], a[232:235], v[112:115], v[184:187]\n\tbuffer_load_ushort v66, off, s[80:83], s66\n\tds_read_b128 v[72:75], v1 offset:37952\n\tv_mfma_f32_16x16x32_bf16 v[184:187], a[236:239], v[108:111], v[184:187]\n\tds_read_b128 v[76:79], v1 offset:38464\n\tv_mfma_f32_16x16x32_bf16 v[184:187], a[240:243], v[104:107], v[184:187]\n\tds_read_b128 v[80:83], v1 offset:38976\n\tv_mfma_f32_16x16x32_bf16 v[184:187], a[244:247], v[100:103], v[184:187]\n\t" \
    "ds_read_b128 v[84:87], v1 offset:39488\n\tv_mfma_f32_16x16x32_bf16 v[180:183], a[224:227], v[116:119], v[180:183]\n\tds_read_b128 v[88:91], v1 offset:40000\n\tv_mfma_f32_16x16x32_bf16 v[180:183], a[228:231], v[112:115], v[180:183]\n\tds_read_b128 v[92:95], v1 offset:40512\n\tv_mfma_f32_16x16x32_bf16 v[180:183], a[232:235], v[108:111], v[180:183]\n\tbuffer_load_dword v15, v7, s[16:19], s42 offen\n\tds_read_b128 v[96:99], v1 offset:41024\n\t" \
    "v_mfma_f32_16x16x32_bf16 v[180:183], a[236:239], v[104:107], v[180:183]\n\ts_add_u32 s43, s43, 1\n\tv_mfma_f32_16x16x32_bf16 v[180:183], a[240:243], v[100:103], v[180:183]\n\tv_readlane_b32 s42, v237, s43\n\ts_nop 4\n\ts_waitcnt vmcnt(11)\n\tbuffer_store_dwordx2 v[248:249], off, s[8:11], s51 sc1\n\tv_mfma_f32_16x16x32_bf16 v[176:179], a[224:227], v[112:115], v[176:179]\n\t" \
    "v_mfma_f32_16x16x32_bf16 v[176:179], a[228:231], v[108:111], v[176:179]\n\tv_mfma_f32_16x16x32_bf16 v[176:179], a[232:235], v[104:107], v[176:179]\n\tv_mfma_f32_16x16x32_bf16 v[176:179], a[236:239], v[100:103], v[176:179]\n\tv_mfma_f32_16x16x32_bf16 v[172:175], a[224:227], v[108:111], v[172:175]\n\tv_mfma_f32_16x16x32_bf16 v[172:175], a[228:231], v[104:107], v[172:175]\n\tv_mfma_f32_16x16x32_bf16 v[172:175], a[232:235], v[100:103], v[172:175]\n\tv_mfma_f32_16x16x32_bf16 v[168:171], a[224:227], v[104:107], v[168:171]\n\tv_mfma_f32_16x16x32_bf16 v[168:171], a[228:231], v[100:103], v[168:171]\n\t" \
    "v_mfma_f32_16x16x32_bf16 v[164:167], a[224:227], v[100:103], v[164:167]\n\ts_waitcnt lgkmcnt(0)\n\tv_mfma_f32_16x16x32_bf16 v[132:135], v[228:231], v[68:71], 0\n\tds_read_b64 a[32:33], v0 offset:3968\n\tds_read_b64 a[34:35], v0 offset:3976\n\tv_mfma_f32_16x16x32_bf16 v[132:135], a[0:3], v[68:71], v[132:135]\n\tds_read_b64 a[36:37], v0 offset:3456\n\tds_read_b64 a[38:39], v0 offset:3464\n\t" \
    "v_mfma_f32_16x16x32_bf16 v[136:139], v[228:231], v[72:75], 0\n\tds_read_b64 a[40:41], v0 offset:2944\n\tds_read_b64 a[42:43], v0 offset:2952\n\tv_mfma_f32_16x16x32_bf16 v[136:139], a[0:3], v[72:75], v[136:139]\n\tds_read_b64 a[44:45], v0 offset:2432\n\tds_read_b64 a[46:47], v0 offset:2440\n\tv_mfma_f32_16x16x32_bf16 v[136:139], a[4:7], v[68:71], v[136:139]\n\tds_read_b64 a[48:49], v0 offset:1920\n\t" \
    "ds_read_b64 a[50:51], v0 offset:1928\n\tv_mfma_f32_16x16x32_bf16 v[140:143], v[228:231], v[76:79], 0\n\tds_read_b64 a[52:53], v0 offset:1408\n\tv_cvt_pk_bf16_f32 v164, v164, v165\n\tv_mfma_f32_16x16x32_bf16 v[140:143], a[0:3], v[76:79], v[140:143]\n\tds_read_b64 a[54:55], v0 offset:1416\n\tv_cvt_pk_bf16_f32 v165, v166, v167\n\tv_mfma_f32_16x16x32_bf16 v[140:143], a[4:7], v[72:75], v[140:143]\n\t" \
    "ds_read_b64 a[56:57], v0 offset:896\n\tds_write_b64 v3, v[164:165] offset:18496\n\tv_mfma_f32_16x16x32_bf16 v[140:143], a[8:11], v[68:71], v[140:143]\n\tds_read_b64 a[58:59], v0 offset:904\n\tv_cvt_pk_bf16_f32 v168, v168, v169\n\tv_mfma_f32_16x16x32_bf16 v[144:147], v[228:231], v[80:83], 0\n\tds_read_b64 a[60:61], v0 offset:384\n\tv_cvt_pk_bf16_f32 v169, v170, v171\n\t" \
    "v_mfma_f32_16x16x32_bf16 v[144:147], a[0:3], v[80:83], v[144:147]\n\tds_read_b64 a[62:63], v0 offset:392\n\tds_write_b64 v3, v[168:169] offset:19008\n\tv_mfma_f32_16x16x32_bf16 v[144:147], a[4:7], v[76:79], v[144:147]\n\tds_read_b128 v[100:103], v1 offset:37376\n\tv_cvt_pk_bf16_f32 v172, v172, v173\n\tv_mfma_f32_16x16x32_bf16 v[144:147], a[8:11], v[72:75], v[144:147]\n\tds_read_b128 v[104:107], v1 offset:37888\n\t" \
    "v_cvt_pk_bf16_f32 v173, v174, v175\n\tv_mfma_f32_16x16x32_bf16 v[144:147], a[12:15], v[68:71], v[144:147]\n\tds_read_b128 v[108:111], v1 offset:38400\n\tds_write_b64 v3, v[172:173] offset:19520\n\tv_mfma_f32_16x16x32_bf16 v[148:151], v[228:231], v[84:87], 0\n\tds_read_b128 v[112:115], v1 offset:38912\n\tv_cvt_pk_bf16_f32 v176, v176, v177\n\tv_mfma_f32_16x16x32_bf16 v[148:151], a[0:3], v[84:87], v[148:151]\n\t" \
    "ds_read_b128 v[116:119], v1 offset:39424\n\tv_cvt_pk_bf16_f32 v177, v178, v179\n\tv_mfma_f32_16x16x32_bf16 v[148:151], a[4:7], v[80:83], v[148:151]\n\tds_read_b128 v[120:123], v1 offset:39936\n\tds_write_b64 v3, v[176:177] offset:20032\n\tv_mfma_f32_16x16x32_bf16 v[148:151], a[8:11], v[76:79], v[148:151]\n\tds_read_b128 v[124:127], v1 offset:40448\n\tv_cvt_pk_bf16_f32 v180, v180, v181\n\t" \
    "v_mfma_f32_16x16x32_bf16 v[148:151], a[12:15], v[72:75], v[148:151]\n\tds_read_b128 v[128:131], v1 offset:40960\n\tv_cvt_pk_bf16_f32 v181, v182, v183\n\tv_mfma_f32_16x16x32_bf16 v[148:151], a[16:19], v[68:71], v[148:151]\n\tds_read_b64 a[64:65], v0 offset:3904\n\tds_write_b64 v3, v[180:181] offset:20544\n\tv_mfma_f32_16x16x32_bf16 v[152:155], v[228:231], v[88:91], 0\n\tds_read_b64 a[66:67], v0 offset:3912\n\t" \
    "v_cvt_pk_bf16_f32 v184, v184, v185\n\tv_mfma_f32_16x16x32_bf16 v[152:155], a[0:3], v[88:91], v[152:155]\n\tds_read_b64 a[68:69], v0 offset:3392\n\tv_cvt_pk_bf16_f32 v185, v186, v187\n\tv_mfma_f32_16x16x32_bf16 v[152:155], a[4:7], v[84:87], v[152:155]\n\tbuffer_load_dword v15, v7, s[16:19], s42 offen\n\tds_read_b64 a[70:71], v0 offset:3400\n\tv_mfma_f32_16x16x32_bf16 v[152:155], a[8:11], v[80:83], v[152:155]\n\t" \
    "s_add_u32 s43, s43, 1\n\tds_read_b64 a[72:73], v0 offset:2880\n\tds_write_b64 v3, v[184:185] offset:21056\n\tv_mfma_f32_16x16x32_bf16 v[152:155], a[12:15], v[76:79], v[152:155]\n\tv_readlane_b32 s42, v237, s43\n\ts_nop 4\n\tds_read_b64 a[74:75], v0 offset:2888\n\tv_cvt_pk_bf16_f32 v188, v188, v189\n\t" \
    "v_mfma_f32_16x16x32_bf16 v[152:155], a[16:19], v[72:75], v[152:155]\n\tds_read_b64 a[76:77], v0 offset:2368\n\tv_cvt_pk_bf16_f32 v189, v190, v191\n\tv_mfma_f32_16x16x32_bf16 v[152:155], a[20:23], v[68:71], v[152:155]\n\tds_read_b64 a[78:79], v0 offset:2376\n\tds_write_b64 v3, v[188:189] offset:21568\n\tv_mfma_f32_16x16x32_bf16 v[156:159], v[228:231], v[92:95], 0\n\tds_read_b64 a[80:81], v0 offset:1856\n\t" \
    "v_cvt_pk_bf16_f32 v192, v192, v193\n\tv_mfma_f32_16x16x32_bf16 v[156:159], a[0:3], v[92:95], v[156:159]\n\tds_read_b64 a[82:83], v0 offset:1864\n\tv_cvt_pk_bf16_f32 v193, v194, v195\n\tv_mfma_f32_16x16x32_bf16 v[156:159], a[4:7], v[88:91], v[156:159]\n\tds_read_b64 a[84:85], v0 offset:1344\n\tds_write_b64 v3, v[192:193] offset:22080\n\tv_mfma_f32_16x16x32_bf16 v[156:159], a[8:11], v[84:87], v[156:159]\n\t" \
    "ds_read_b64 a[86:87], v0 offset:1352\n\tv_mfma_f32_16x16x32_bf16 v[156:159], a[12:15], v[80:83], v[156:159]\n\tds_read_b64 a[88:89], v0 offset:832\n\tv_mfma_f32_16x16x32_bf16 v[156:159], a[16:19], v[76:79], v[156:159]\n\tds_read_b64 a[90:91], v0 offset:840\n\tv_mfma_f32_16x16x32_bf16 v[156:159], a[20:23], v[72:75], v[156:159]\n\tds_read_b64 a[92:93], v0 offset:320\n\tv_mfma_f32_16x16x32_bf16 v[156:159], a[24:27], v[68:71], v[156:159]\n\t" \
    "ds_read_b64 a[94:95], v0 offset:328\n\tv_mfma_f32_16x16x32_bf16 v[160:163], v[228:231], v[96:99], 0\n\tds_read_b64 a[96:97], v0 offset:3840\n\tv_mfma_f32_16x16x32_bf16 v[160:163], a[0:3], v[96:99], v[160:163]\n\tds_read_b64 a[98:99], v0 offset:3848\n\tv_mfma_f32_16x16x32_bf16 v[160:163], a[4:7], v[92:95], v[160:163]\n\tds_read_b64 a[100:101], v0 offset:3328\n\tv_mfma_f32_16x16x32_bf16 v[160:163], a[8:11], v[88:91], v[160:163]\n\t" \
    "ds_read_b64 a[102:103], v0 offset:3336\n\tv_mfma_f32_16x16x32_bf16 v[160:163], a[12:15], v[84:87], v[160:163]\n\tv_mfma_f32_16x16x32_bf16 v[160:163], a[16:19], v[80:83], v[160:163]\n\tv_mfma_f32_16x16x32_bf16 v[160:163], a[20:23], v[76:79], v[160:163]\n\tv_mfma_f32_16x16x32_bf16 v[160:163], a[24:27], v[72:75], v[160:163]\n\tv_mfma_f32_16x16x32_bf16 v[160:163], a[28:31], v[68:71], v[160:163]\n\ts_waitcnt lgkmcnt(9)\n\ts_barrier\n\t" \
    "v_mfma_f32_16x16x32_bf16 v[160:163], a[32:35], v[128:131], v[160:163]\n\tds_read_b64 a[104:105], v0 offset:2816\n\tds_read_b64 a[106:107], v0 offset:2824\n\tv_mfma_f32_16x16x32_bf16 v[160:163], a[36:39], v[124:127], v[160:163]\n\tds_read_b64 a[108:109], v0 offset:2304\n\tds_read_b64 a[110:111], v0 offset:2312\n\tv_mfma_f32_16x16x32_bf16 v[160:163], a[40:43], v[120:123], v[160:163]\n\tds_read_b64 a[112:113], v0 offset:1792\n\t" \
    "ds_read_b64 a[114:115], v0 offset:1800\n\tv_mfma_f32_16x16x32_bf16 v[160:163], a[44:47], v[116:119], v[160:163]\n\tds_read_b64 a[116:117], v0 offset:1280\n\tds_read_b64 a[118:119], v0 offset:1288\n\tv_mfma_f32_16x16x32_bf16 v[160:163], a[48:51], v[112:115], v[160:163]\n\tds_read_b64 a[120:121], v0 offset:768\n\tds_read_b64 a[122:123], v0 offset:776\n\tv_mfma_f32_16x16x32_bf16 v[160:163], a[52:55], v[108:111], v[160:163]\n\t" \
    "ds_read_b64 a[124:125], v0 offset:256\n\ts_add_u32 s51, s44, 4\n\tv_mfma_f32_16x16x32_bf16 v[160:163], a[56:59], v[104:107], v[160:163]\n\tds_read_b64 a[126:127], v0 offset:264\n\ts_mul_i32 s65, s51, 1366\n\tv_mfma_f32_16x16x32_bf16 v[160:163], a[60:63], v[100:103], v[160:163]\n\tds_read_b64 a[128:129], v0 offset:3776\n\ts_lshr_b32 s65, s65, 16\n\t" \
    "v_mfma_f32_16x16x32_bf16 v[156:159], a[32:35], v[124:127], v[156:159]\n\tds_read_b64 a[130:131], v0 offset:3784\n\ts_mul_i32 s56, s65, 48\n\tv_mfma_f32_16x16x32_bf16 v[156:159], a[36:39], v[120:123], v[156:159]\n\tds_read_b64 a[132:133], v0 offset:3264\n\ts_sub_u32 s56, s51, s56\n\tv_mfma_f32_16x16x32_bf16 v[156:159], a[40:43], v[116:119], v[156:159]\n\tds_read_b64 a[134:135], v0 offset:3272\n\t" \
    "s_lshl_b32 s56, s56, 6\n\tv_mfma_f32_16x16x32_bf16 v[156:159], a[44:47], v[112:115], v[156:159]\n\tds_read_b128 v[68:71], v1 offset:37312\n\ts_lshr_b32 s70, s65, 2\n\tv_mfma_f32_16x16x32_bf16 v[156:159], a[48:51], v[108:111], v[156:159]\n\tds_read_b128 v[72:75], v1 offset:37824\n\ts_mul_i32 s70, s70, 252\n\tv_mfma_f32_16x16x32_bf16 v[156:159], a[52:55], v[104:107], v[156:159]\n\t" \
    "ds_read_b128 v[76:79], v1 offset:38336\n\ts_add_u32 s65, s65, s70\n\tv_mfma_f32_16x16x32_bf16 v[156:159], a[56:59], v[100:103], v[156:159]\n\tds_read_b128 v[80:83], v1 offset:38848\n\ts_mul_i32 s65, s65, 3072\n\tv_mfma_f32_16x16x32_bf16 v[152:155], a[32:35], v[120:123], v[152:155]\n\tds_read_b128 v[84:87], v1 offset:39360\n\ts_add_u32 s56, s56, s65\n\t" \
    "v_mfma_f32_16x16x32_bf16 v[152:155], a[36:39], v[116:119], v[152:155]\n\tds_read_b128 v[88:91], v1 offset:39872\n\ts_add_u32 s56, s56, 4849664\n\tv_mfma_f32_16x16x32_bf16 v[152:155], a[40:43], v[112:115], v[152:155]\n\tds_read_b128 v[92:95], v1 offset:40384\n\ts_add_u32 s51, s51, s38\n\tv_mfma_f32_16x16x32_bf16 v[152:155], a[44:47], v[108:111], v[152:155]\n\tds_read_b128 v[96:99], v1 offset:40896\n\t" \
    "s_lshl_b32 s51, s51, 3\n\tv_mfma_f32_16x16x32_bf16 v[152:155], a[48:51], v[104:107], v[152:155]\n\tds_read_b64 a[136:137], v0 offset:2752\n\ts_add_u32 s51, s51, 50176\n\tds_read_b64_tr_b16 v[204:205], v4 offset:18496\n\tv_mfma_f32_16x16x32_bf16 v[152:155], a[52:55], v[100:103], v[152:155]\n\tds_read_b64 a[138:139], v0 offset:2760\n\tds_read_b64_tr_b16 v[206:207], v4 offset:18504\n\t" \
    "v_mfma_f32_16x16x32_bf16 v[148:151], a[32:35], v[116:119], v[148:151]\n\tds_read_b64 a[140:141], v0 offset:2240\n\tds_read_b64_tr_b16 v[208:209], v4 offset:18560\n\tv_mfma_f32_16x16x32_bf16 v[148:151], a[36:39], v[112:115], v[148:151]\n\tds_read_b64 a[142:143], v0 offset:2248\n\tds_read_b64_tr_b16 v[210:211], v4 offset:18568\n\tv_mfma_f32_16x16x32_bf16 v[148:151], a[40:43], v[108:111], v[148:151]\n\tds_read_b64 a[144:145], v0 offset:1728\n\t" \
    "ds_read_b64_tr_b16 v[212:213], v4 offset:19008\n\tv_mfma_f32_16x16x32_bf16 v[148:151], a[44:47], v[104:107], v[148:151]\n\tds_read_b64 a[146:147], v0 offset:1736\n\tds_read_b64_tr_b16 v[214:215], v4 offset:19016\n\tv_mfma_f32_16x16x32_bf16 v[148:151], a[48:51], v[100:103], v[148:151]\n\tds_read_b64 a[148:149], v0 offset:1216\n\tds_read_b64_tr_b16 v[216:217], v4 offset:19072\n\tv_mfma_f32_16x16x32_bf16 v[144:147], a[32:35], v[112:115], v[144:147]\n\t" \
    "ds_read_b64 a[150:151], v0 offset:1224\n\tds_read_b64_tr_b16 v[218:219], v4 offset:19080\n\tv_mfma_f32_16x16x32_bf16 v[144:147], a[36:39], v[108:111], v[144:147]\n\tds_read_b64 a[152:153], v0 offset:704\n\tv_mfma_f32_16x16x32_bf16 v[144:147], a[40:43], v[104:107], v[144:147]\n\tds_read_b64 a[154:155], v0 offset:712\n\tv_mfma_f32_16x16x32_bf16 v[144:147], a[44:47], v[100:103], v[144:147]\n\tds_read_b64 a[156:157], v0 offset:192\n\t" \
    "v_mfma_f32_16x16x32_bf16 v[140:143], a[32:35], v[108:111], v[140:143]\n\tds_read_b64 a[158:159], v0 offset:200\n\tv_mfma_f32_16x16x32_bf16 v[140:143], a[36:39], v[104:107], v[140:143]\n\tv_mfma_f32_16x16x32_bf16 v[140:143], a[40:43], v[100:103], v[140:143]\n\ts_waitcnt lgkmcnt(15)\n\tbuffer_store_dwordx4 v[204:207], v5, s[8:11], s56 offen sc1\n\ts_add_u32 s56, s56, 49152\n\tv_mfma_f32_16x16x32_bf16 v[136:139], a[32:35], v[104:107], v[136:139]\n\t" \
    "v_mfma_f32_16x16x32_bf16 v[136:139], a[36:39], v[100:103], v[136:139]\n\tv_mfma_f32_16x16x32_bf16 v[132:135], a[32:35], v[100:103], v[132:135]\n\tv_mfma_f32_16x16x32_bf16 v[132:135], a[64:67], v[68:71], v[132:135]\n\tds_read_b64 a[160:161], v0 offset:3712\n\tds_read_b64 a[162:163], v0 offset:3720\n\tv_mfma_f32_16x16x32_bf16 v[136:139], a[64:67], v[72:75], v[136:139]\n\tds_read_b64 a[164:165], v0 offset:3200\n\tds_read_b64 a[166:167], v0 offset:3208\n\t" \
    "v_mfma_f32_16x16x32_bf16 v[136:139], a[68:71], v[68:71], v[136:139]\n\tds_read_b64 a[168:169], v0 offset:2688\n\tds_read_b64 a[170:171], v0 offset:2696\n\tv_mfma_f32_16x16x32_bf16 v[140:143], a[64:67], v[76:79], v[140:143]\n\tds_read_b64 a[172:173], v0 offset:2176\n\tds_read_b64 a[174:175], v0 offset:2184\n\tv_mfma_f32_16x16x32_bf16 v[140:143], a[68:71], v[72:75], v[140:143]\n\tds_read_b64 a[176:177], v0 offset:1664\n\t" \
    "ds_read_b64 a[178:179], v0 offset:1672\n\tv_mfma_f32_16x16x32_bf16 v[140:143], a[72:75], v[68:71], v[140:143]\n\tds_read_b64 a[180:181], v0 offset:1152\n\tv_mfma_f32_16x16x32_bf16 v[144:147], a[64:67], v[80:83], v[144:147]\n\tds_read_b64 a[182:183], v0 offset:1160\n\tv_mfma_f32_16x16x32_bf16 v[144:147], a[68:71], v[76:79], v[144:147]\n\tds_read_b64 a[184:185], v0 offset:640\n\tv_mfma_f32_16x16x32_bf16 v[144:147], a[72:75], v[72:75], v[144:147]\n\t" \
    "ds_read_b64 a[186:187], v0 offset:648\n\tv_mfma_f32_16x16x32_bf16 v[144:147], a[76:79], v[68:71], v[144:147]\n\tds_read_b64 a[188:189], v0 offset:128\n\tv_mfma_f32_16x16x32_bf16 v[148:151], a[64:67], v[84:87], v[148:151]\n\tds_read_b64 a[190:191], v0 offset:136\n\tv_mfma_f32_16x16x32_bf16 v[148:151], a[68:71], v[80:83], v[148:151]\n\tds_read_b128 v[100:103], v1 offset:37248\n\tv_mfma_f32_16x16x32_bf16 v[148:151], a[72:75], v[76:79], v[148:151]\n\t" \
    "ds_read_b128 v[104:107], v1 offset:37760\n\tv_mfma_f32_16x16x32_bf16 v[148:151], a[76:79], v[72:75], v[148:151]\n\tds_read_b128 v[108:111], v1 offset:38272\n\tv_mfma_f32_16x16x32_bf16 v[148:151], a[80:83], v[68:71], v[148:151]\n\tds_read_b128 v[112:115], v1 offset:38784\n\ts_waitcnt lgkmcnt(15)\n\tbuffer_store_dwordx4 v[208:211], v5, s[8:11], s56 offen sc1\n\tv_mfma_f32_16x16x32_bf16 v[152:155], a[64:67], v[88:91], v[152:155]\n\t" \
    "ds_read_b128 v[116:119], v1 offset:39296\n\ts_add_u32 s56, s56, 49152\n\tv_mfma_f32_16x16x32_bf16 v[152:155], a[68:71], v[84:87], v[152:155]\n\tbuffer_load_dword v15, v7, s[16:19], s42 offen\n\tds_read_b128 v[120:123], v1 offset:39808\n\tv_mfma_f32_16x16x32_bf16 v[152:155], a[72:75], v[80:83], v[152:155]\n\ts_add_u32 s43, s43, 1\n\tds_read_b128 v[124:127], v1 offset:40320\n\t" \
    "v_mfma_f32_16x16x32_bf16 v[152:155], a[76:79], v[76:79], v[152:155]\n\tv_readlane_b32 s42, v237, s43\n\ts_nop 4\n\tds_read_b128 v[128:131], v1 offset:40832\n\tv_mfma_f32_16x16x32_bf16 v[152:155], a[80:83], v[72:75], v[152:155]\n\tds_read_b64 a[192:193], v0 offset:3648\n\tv_mfma_f32_16x16x32_bf16 v[152:155], a[84:87], v[68:71], v[152:155]\n\tds_read_b64 a[194:195], v0 offset:3656\n\t" \
    "v_mfma_f32_16x16x32_bf16 v[156:159], a[64:67], v[92:95], v[156:159]\n\tds_read_b64 a[196:197], v0 offset:3136\n\tv_mfma_f32_16x16x32_bf16 v[156:159], a[68:71], v[88:91], v[156:159]\n\tds_read_b64 a[198:199], v0 offset:3144\n\tv_mfma_f32_16x16x32_bf16 v[156:159], a[72:75], v[84:87], v[156:159]\n\tds_read_b64 a[200:201], v0 offset:2624\n\tv_mfma_f32_16x16x32_bf16 v[156:159], a[76:79], v[80:83], v[156:159]\n\tds_read_b64 a[202:203], v0 offset:2632\n\t" \
    "v_mfma_f32_16x16x32_bf16 v[156:159], a[80:83], v[76:79], v[156:159]\n\tds_read_b64 a[204:205], v0 offset:2112\n\tv_mfma_f32_16x16x32_bf16 v[156:159], a[84:87], v[72:75], v[156:159]\n\tds_read_b64 a[206:207], v0 offset:2120\n\tv_mfma_f32_16x16x32_bf16 v[156:159], a[88:91], v[68:71], v[156:159]\n\tds_read_b64 a[208:209], v0 offset:1600\n\tv_mfma_f32_16x16x32_bf16 v[160:163], a[64:67], v[96:99], v[160:163]\n\tds_read_b64 a[210:211], v0 offset:1608\n\t" \
    "v_mfma_f32_16x16x32_bf16 v[160:163], a[68:71], v[92:95], v[160:163]\n\tds_read_b64 a[212:213], v0 offset:1088\n\tv_mfma_f32_16x16x32_bf16 v[160:163], a[72:75], v[88:91], v[160:163]\n\tds_read_b64 a[214:215], v0 offset:1096\n\tv_mfma_f32_16x16x32_bf16 v[160:163], a[76:79], v[84:87], v[160:163]\n\tbuffer_store_dwordx4 v[212:215], v5, s[8:11], s56 offen sc1\n\tv_mfma_f32_16x16x32_bf16 v[160:163], a[80:83], v[80:83], v[160:163]\n\ts_add_u32 s56, s56, 49152\n\t" \
    "v_mfma_f32_16x16x32_bf16 v[160:163], a[84:87], v[76:79], v[160:163]\n\tv_mfma_f32_16x16x32_bf16 v[160:163], a[88:91], v[72:75], v[160:163]\n\tv_mfma_f32_16x16x32_bf16 v[160:163], a[92:95], v[68:71], v[160:163]\n\ts_waitcnt lgkmcnt(12)\n\tv_mfma_f32_16x16x32_bf16 v[160:163], a[96:99], v[128:131], v[160:163]\n\tds_read_b64 a[216:217], v0 offset:576\n\tv_mfma_f32_16x16x32_bf16 v[160:163], a[100:103], v[124:127], v[160:163]\n\tds_read_b64 a[218:219], v0 offset:584\n\t" \
    "v_mfma_f32_16x16x32_bf16 v[160:163], a[104:107], v[120:123], v[160:163]\n\tds_read_b64 a[220:221], v0 offset:64\n\tv_mfma_f32_16x16x32_bf16 v[160:163], a[108:111], v[116:119], v[160:163]\n\tds_read_b64 a[222:223], v0 offset:72\n\tv_mfma_f32_16x16x32_bf16 v[160:163], a[112:115], v[112:115], v[160:163]\n\tds_read_b64 a[224:225], v0 offset:3584\n\tv_mfma_f32_16x16x32_bf16 v[160:163], a[116:119], v[108:111], v[160:163]\n\tds_read_b64 a[226:227], v0 offset:3592\n\t" \
    "v_mfma_f32_16x16x32_bf16 v[160:163], a[120:123], v[104:107], v[160:163]\n\tds_read_b64 a[228:229], v0 offset:3072\n\tv_mfma_f32_16x16x32_bf16 v[160:163], a[124:127], v[100:103], v[160:163]\n\tds_read_b64 a[230:231], v0 offset:3080\n\tv_mfma_f32_16x16x32_bf16 v[156:159], a[96:99], v[124:127], v[156:159]\n\tds_read_b64 a[232:233], v0 offset:2560\n\tbuffer_store_dwordx4 v[216:219], v5, s[8:11], s56 offen sc1\n\tv_mfma_f32_16x16x32_bf16 v[156:159], a[100:103], v[120:123], v[156:159]\n\t" \
    "ds_read_b64 a[234:235], v0 offset:2568\n\ts_add_u32 s56, s56, 49152\n\tv_mfma_f32_16x16x32_bf16 v[156:159], a[104:107], v[116:119], v[156:159]\n\tds_read_b64 a[236:237], v0 offset:2048\n\tv_mfma_f32_16x16x32_bf16 v[156:159], a[108:111], v[112:115], v[156:159]\n\tds_read_b64 a[238:239], v0 offset:2056\n\tv_mfma_f32_16x16x32_bf16 v[156:159], a[112:115], v[108:111], v[156:159]\n\tds_read_b64 a[240:241], v0 offset:1536\n\t" \
    "v_mfma_f32_16x16x32_bf16 v[156:159], a[116:119], v[104:107], v[156:159]\n\tds_read_b64 a[242:243], v0 offset:1544\n\tv_mfma_f32_16x16x32_bf16 v[156:159], a[120:123], v[100:103], v[156:159]\n\tds_read_b64 a[244:245], v0 offset:1024\n\tv_mfma_f32_16x16x32_bf16 v[152:155], a[96:99], v[120:123], v[152:155]\n\tds_read_b64 a[246:247], v0 offset:1032\n\tv_mfma_f32_16x16x32_bf16 v[152:155], a[100:103], v[116:119], v[152:155]\n\tds_read_b128 v[68:71], v1 offset:37184\n\t" \
    "v_mfma_f32_16x16x32_bf16 v[152:155], a[104:107], v[112:115], v[152:155]\n\tds_read_b128 v[72:75], v1 offset:37696\n\tv_mfma_f32_16x16x32_bf16 v[152:155], a[108:111], v[108:111], v[152:155]\n\tds_read_b128 v[76:79], v1 offset:38208\n\tv_mfma_f32_16x16x32_bf16 v[152:155], a[112:115], v[104:107], v[152:155]\n\tds_read_b128 v[80:83], v1 offset:38720\n\tv_mfma_f32_16x16x32_bf16 v[152:155], a[116:119], v[100:103], v[152:155]\n\tds_read_b128 v[84:87], v1 offset:39232\n\t" \
    "v_mfma_f32_16x16x32_bf16 v[148:151], a[96:99], v[116:119], v[148:151]\n\tds_read_b128 v[88:91], v1 offset:39744\n\tv_mfma_f32_16x16x32_bf16 v[148:151], a[100:103], v[112:115], v[148:151]\n\tds_read_b128 v[92:95], v1 offset:40256\n\tv_mfma_f32_16x16x32_bf16 v[148:151], a[104:107], v[108:111], v[148:151]\n\tds_read_b128 v[96:99], v1 offset:40768\n\tv_mfma_f32_16x16x32_bf16 v[148:151], a[108:111], v[104:107], v[148:151]\n\tds_read_b64 a[248:249], v0 offset:512\n\t" \
    "v_mfma_f32_16x16x32_bf16 v[148:151], a[112:115], v[100:103], v[148:151]\n\tds_read_b64 a[250:251], v0 offset:520\n\tv_mfma_f32_16x16x32_bf16 v[144:147], a[96:99], v[112:115], v[144:147]\n\tds_read_b64 a[252:253], v0 offset:0\n\tv_mfma_f32_16x16x32_bf16 v[144:147], a[100:103], v[108:111], v[144:147]\n\tds_read_b64 a[254:255], v0 offset:8\n\tv_mfma_f32_16x16x32_bf16 v[144:147], a[104:107], v[104:107], v[144:147]\n\tv_mfma_f32_16x16x32_bf16 v[144:147], a[108:111], v[100:103], v[144:147]\n\t" \
    "v_mfma_f32_16x16x32_bf16 v[140:143], a[96:99], v[108:111], v[140:143]\n\tv_mfma_f32_16x16x32_bf16 v[140:143], a[100:103], v[104:107], v[140:143]\n\tv_mfma_f32_16x16x32_bf16 v[140:143], a[104:107], v[100:103], v[140:143]\n\tv_mfma_f32_16x16x32_bf16 v[136:139], a[96:99], v[104:107], v[136:139]\n\tv_mfma_f32_16x16x32_bf16 v[136:139], a[100:103], v[100:103], v[136:139]\n\tv_mfma_f32_16x16x32_bf16 v[132:135], a[96:99], v[100:103], v[132:135]\n\ts_waitcnt lgkmcnt(4)\n\tv_mfma_f32_16x16x32_bf16 v[132:135], a[128:131], v[68:71], v[132:135]\n\t" \
    "ds_read_b128 v[100:103], v1 offset:37120\n\ts_waitcnt vmcnt(8)\n\tv_alignbit_b32 v196, v45, v45, 16\n\tv_mfma_f32_16x16x32_bf16 v[136:139], a[128:131], v[72:75], v[136:139]\n\tds_read_b128 v[104:107], v1 offset:37632\n\tv_alignbit_b32 v197, v44, v44, 16\n\tv_mfma_f32_16x16x32_bf16 v[136:139], a[132:135], v[68:71], v[136:139]\n\tds_read_b128 v[108:111], v1 offset:38144\n\t" \
    "v_alignbit_b32 v198, v43, v43, 16\n\tv_mfma_f32_16x16x32_bf16 v[140:143], a[128:131], v[76:79], v[140:143]\n\tds_read_b128 v[112:115], v1 offset:38656\n\tv_alignbit_b32 v199, v42, v42, 16\n\tv_mfma_f32_16x16x32_bf16 v[140:143], a[132:135], v[72:75], v[140:143]\n\tds_write_b128 v2, v[196:199] offset:3072\n\tv_mfma_f32_16x16x32_bf16 v[140:143], a[136:139], v[68:71], v[140:143]\n\tds_read_b128 v[116:119], v1 offset:39168\n\t" \
    "v_bfi_b32 v200, s46, v45, v44\n\tv_mfma_f32_16x16x32_bf16 v[144:147], a[128:131], v[80:83], v[144:147]\n\tds_read_b128 v[120:123], v1 offset:39680\n\tv_bfi_b32 v201, s46, v44, v43\n\tv_mfma_f32_16x16x32_bf16 v[144:147], a[132:135], v[76:79], v[144:147]\n\tds_read_b128 v[124:127], v1 offset:40192\n\tv_bfi_b32 v202, s46, v43, v42\n\tv_mfma_f32_16x16x32_bf16 v[144:147], a[136:139], v[72:75], v[144:147]\n\t" \
    "ds_read_b128 v[128:131], v1 offset:40704\n\tv_bfi_b32 v203, s46, v42, v59\n\tv_mfma_f32_16x16x32_bf16 v[144:147], a[140:143], v[68:71], v[144:147]\n\tds_write_b128 v2, v[200:203] offset:7232\n\tv_mfma_f32_16x16x32_bf16 v[148:151], a[128:131], v[84:87], v[148:151]\n\tv_alignbit_b32 v196, v44, v44, 16\n\tv_alignbit_b32 v197, v43, v43, 16\n\tv_mfma_f32_16x16x32_bf16 v[148:151], a[132:135], v[80:83], v[148:151]\n\t" \
    "v_alignbit_b32 v198, v42, v42, 16\n\tv_alignbit_b32 v199, v59, v59, 16\n\tv_mfma_f32_16x16x32_bf16 v[148:151], a[136:139], v[76:79], v[148:151]\n\tds_write_b128 v2, v[196:199] offset:11392\n\tv_mfma_f32_16x16x32_bf16 v[148:151], a[140:143], v[72:75], v[148:151]\n\tv_bfi_b32 v200, s46, v44, v43\n\tv_bfi_b32 v201, s46, v43, v42\n\tv_mfma_f32_16x16x32_bf16 v[148:151], a[144:147], v[68:71], v[148:151]\n\t" \
    "v_bfi_b32 v202, s46, v42, v59\n\tv_bfi_b32 v203, s46, v59, v58\n\tv_mfma_f32_16x16x32_bf16 v[152:155], a[128:131], v[88:91], v[152:155]\n\tds_write_b128 v2, v[200:203] offset:15552\n\tv_mfma_f32_16x16x32_bf16 v[152:155], a[132:135], v[84:87], v[152:155]\n\tv_alignbit_b32 v196, v49, v49, 16\n\tv_alignbit_b32 v197, v48, v48, 16\n\tv_mfma_f32_16x16x32_bf16 v[152:155], a[136:139], v[80:83], v[152:155]\n\t" \
    "v_alignbit_b32 v198, v47, v47, 16\n\tv_alignbit_b32 v199, v46, v46, 16\n\tv_mfma_f32_16x16x32_bf16 v[152:155], a[140:143], v[76:79], v[152:155]\n\tbuffer_load_dword v15, v7, s[16:19], s42 offen\n\tv_mfma_f32_16x16x32_bf16 v[152:155], a[144:147], v[72:75], v[152:155]\n\ts_add_u32 s43, s43, 1\n\tds_write_b128 v2, v[196:199] offset:2048\n\tv_mfma_f32_16x16x32_bf16 v[152:155], a[148:151], v[68:71], v[152:155]\n\t" \
    "v_readlane_b32 s42, v237, s43\n\ts_nop 4\n\tv_bfi_b32 v200, s46, v49, v48\n\tv_bfi_b32 v201, s46, v48, v47\n\tv_mfma_f32_16x16x32_bf16 v[156:159], a[128:131], v[92:95], v[156:159]\n\tv_bfi_b32 v202, s46, v47, v46\n\tv_bfi_b32 v203, s46, v46, v61\n\tv_mfma_f32_16x16x32_bf16 v[156:159], a[132:135], v[88:91], v[156:159]\n\t" \
    "ds_write_b128 v2, v[200:203] offset:6208\n\tv_mfma_f32_16x16x32_bf16 v[156:159], a[136:139], v[84:87], v[156:159]\n\tv_alignbit_b32 v196, v48, v48, 16\n\tv_alignbit_b32 v197, v47, v47, 16\n\tv_mfma_f32_16x16x32_bf16 v[156:159], a[140:143], v[80:83], v[156:159]\n\tv_alignbit_b32 v198, v46, v46, 16\n\tv_alignbit_b32 v199, v61, v61, 16\n\tv_mfma_f32_16x16x32_bf16 v[156:159], a[144:147], v[76:79], v[156:159]\n\t" \
    "ds_write_b128 v2, v[196:199] offset:10368\n\tv_mfma_f32_16x16x32_bf16 v[156:159], a[148:151], v[72:75], v[156:159]\n\tv_bfi_b32 v200, s46, v48, v47\n\tv_bfi_b32 v201, s46, v47, v46\n\tv_mfma_f32_16x16x32_bf16 v[156:159], a[152:155], v[68:71], v[156:159]\n\tv_bfi_b32 v202, s46, v46, v61\n\tv_bfi_b32 v203, s46, v61, v60\n\tv_mfma_f32_16x16x32_bf16 v[160:163], a[128:131], v[96:99], v[160:163]\n\t" \
    "ds_write_b128 v2, v[200:203] offset:14528\n\tv_mfma_f32_16x16x32_bf16 v[160:163], a[132:135], v[92:95], v[160:163]\n\tv_alignbit_b32 v196, v53, v53, 16\n\tv_alignbit_b32 v197, v52, v52, 16\n\tv_mfma_f32_16x16x32_bf16 v[160:163], a[136:139], v[88:91], v[160:163]\n\tv_alignbit_b32 v198, v51, v51, 16\n\tv_alignbit_b32 v199, v50, v50, 16\n\tv_mfma_f32_16x16x32_bf16 v[160:163], a[140:143], v[84:87], v[160:163]\n\t" \
    "ds_write_b128 v2, v[196:199] offset:1024\n\tv_mfma_f32_16x16x32_bf16 v[160:163], a[144:147], v[80:83], v[160:163]\n\tv_bfi_b32 v200, s46, v53, v52\n\tv_bfi_b32 v201, s46, v52, v51\n\tv_mfma_f32_16x16x32_bf16 v[160:163], a[148:151], v[76:79], v[160:163]\n\tv_bfi_b32 v202, s46, v51, v50\n\tv_bfi_b32 v203, s46, v50, v63\n\tv_mfma_f32_16x16x32_bf16 v[160:163], a[152:155], v[72:75], v[160:163]\n\t" \
    "ds_write_b128 v2, v[200:203] offset:5184\n\tv_mfma_f32_16x16x32_bf16 v[160:163], a[156:159], v[68:71], v[160:163]\n\tv_alignbit_b32 v196, v52, v52, 16\n\tv_alignbit_b32 v197, v51, v51, 16\n\ts_waitcnt lgkmcnt(9)\n\tv_mfma_f32_16x16x32_bf16 v[160:163], a[160:163], v[128:131], v[160:163]\n\tds_read_b128 v[68:71], v1 offset:37056\n\tv_alignbit_b32 v198, v50, v50, 16\n\t" \
    "v_mfma_f32_16x16x32_bf16 v[160:163], a[164:167], v[124:127], v[160:163]\n\tds_read_b128 v[72:75], v1 offset:37568\n\tv_alignbit_b32 v199, v63, v63, 16\n\tv_mfma_f32_16x16x32_bf16 v[160:163], a[168:171], v[120:123], v[160:163]\n\tds_write_b128 v2, v[196:199] offset:9344\n\tv_mfma_f32_16x16x32_bf16 v[160:163], a[172:175], v[116:119], v[160:163]\n\tds_read_b128 v[76:79], v1 offset:38080\n\tv_bfi_b32 v200, s46, v52, v51\n\t" \
    "v_mfma_f32_16x16x32_bf16 v[160:163], a[176:179], v[112:115], v[160:163]\n\tds_read_b128 v[80:83], v1 offset:38592\n\tv_bfi_b32 v201, s46, v51, v50\n\tv_mfma_f32_16x16x32_bf16 v[160:163], a[180:183], v[108:111], v[160:163]\n\tds_read_b128 v[84:87], v1 offset:39104\n\tv_bfi_b32 v202, s46, v50, v63\n\tv_mfma_f32_16x16x32_bf16 v[160:163], a[184:187], v[104:107], v[160:163]\n\tds_read_b128 v[88:91], v1 offset:39616\n\t" \
    "v_bfi_b32 v203, s46, v63, v62\n\tv_mfma_f32_16x16x32_bf16 v[160:163], a[188:191], v[100:103], v[160:163]\n\tds_write_b128 v2, v[200:203] offset:13504\n\tv_mfma_f32_16x16x32_bf16 v[156:159], a[160:163], v[124:127], v[156:159]\n\tds_read_b128 v[92:95], v1 offset:40128\n\tv_alignbit_b32 v196, v57, v57, 16\n\tv_mfma_f32_16x16x32_bf16 v[156:159], a[164:167], v[120:123], v[156:159]\n\tds_read_b128 v[96:99], v1 offset:40640\n\t" \
    "v_alignbit_b32 v197, v56, v56, 16\n\tv_mfma_f32_16x16x32_bf16 v[156:159], a[168:171], v[116:119], v[156:159]\n\tv_alignbit_b32 v198, v55, v55, 16\n\tv_alignbit_b32 v199, v54, v54, 16\n\tv_mfma_f32_16x16x32_bf16 v[156:159], a[172:175], v[112:115], v[156:159]\n\tds_write_b128 v2, v[196:199] offset:0\n\tv_mfma_f32_16x16x32_bf16 v[156:159], a[176:179], v[108:111], v[156:159]\n\tv_bfi_b32 v200, s46, v57, v56\n\t" \
    "v_bfi_b32 v201, s46, v56, v55\n\tv_mfma_f32_16x16x32_bf16 v[156:159], a[180:183], v[104:107], v[156:159]\n\tv_bfi_b32 v202, s46, v55, v54\n\tv_bfi_b32 v203, s46, v54, v65\n\tv_mfma_f32_16x16x32_bf16 v[156:159], a[184:187], v[100:103], v[156:159]\n\tds_write_b128 v2, v[200:203] offset:4160\n\tv_mfma_f32_16x16x32_bf16 v[152:155], a[160:163], v[120:123], v[152:155]\n\tv_alignbit_b32 v196, v56, v56, 16\n\t" \
    "v_alignbit_b32 v197, v55, v55, 16\n\tv_mfma_f32_16x16x32_bf16 v[152:155], a[164:167], v[116:119], v[152:155]\n\tv_alignbit_b32 v198, v54, v54, 16\n\tv_alignbit_b32 v199, v65, v65, 16\n\tv_mfma_f32_16x16x32_bf16 v[152:155], a[168:171], v[112:115], v[152:155]\n\tds_write_b128 v2, v[196:199] offset:8320\n\tv_mfma_f32_16x16x32_bf16 v[152:155], a[172:175], v[108:111], v[152:155]\n\tv_bfi_b32 v200, s46, v56, v55\n\t" \
    "v_bfi_b32 v201, s46, v55, v54\n\tv_mfma_f32_16x16x32_bf16 v[152:155], a[176:179], v[104:107], v[152:155]\n\tv_bfi_b32 v202, s46, v54, v65\n\tv_bfi_b32 v203, s46, v65, v64\n\tv_mfma_f32_16x16x32_bf16 v[152:155], a[180:183], v[100:103], v[152:155]\n\tds_write_b128 v2, v[200:203] offset:12480\n\tv_mfma_f32_16x16x32_bf16 v[148:151], a[160:163], v[116:119], v[148:151]\n\tv_lshl_or_b32 v66, v66, 16, v66\n\t" \
    "v_and_b32 v228, v66, v232\n\tv_mfma_f32_16x16x32_bf16 v[148:151], a[164:167], v[112:115], v[148:151]\n\tv_and_b32 v229, v66, v233\n\tv_and_b32 v230, v66, v234\n\tv_mfma_f32_16x16x32_bf16 v[148:151], a[168:171], v[108:111], v[148:151]\n\tv_and_b32 v231, v66, v235\n\tv_mfma_f32_16x16x32_bf16 v[148:151], a[172:175], v[104:107], v[148:151]\n\tv_mfma_f32_16x16x32_bf16 v[148:151], a[176:179], v[100:103], v[148:151]\n\t" \
    "v_mfma_f32_16x16x32_bf16 v[144:147], a[160:163], v[112:115], v[144:147]\n\tv_mfma_f32_16x16x32_bf16 v[144:147], a[164:167], v[108:111], v[144:147]\n\tv_mfma_f32_16x16x32_bf16 v[144:147], a[168:171], v[104:107], v[144:147]\n\tv_mfma_f32_16x16x32_bf16 v[144:147], a[172:175], v[100:103], v[144:147]\n\tv_mfma_f32_16x16x32_bf16 v[140:143], a[160:163], v[108:111], v[140:143]\n\tv_mfma_f32_16x16x32_bf16 v[140:143], a[164:167], v[104:107], v[140:143]\n\tv_mfma_f32_16x16x32_bf16 v[140:143], a[168:171], v[100:103], v[140:143]\n\tv_mfma_f32_16x16x32_bf16 v[136:139], a[160:163], v[104:107], v[136:139]\n\t" \
    "v_mfma_f32_16x16x32_bf16 v[136:139], a[164:167], v[100:103], v[136:139]\n\tv_mfma_f32_16x16x32_bf16 v[132:135], a[160:163], v[100:103], v[132:135]\n\ts_waitcnt lgkmcnt(4)\n\tv_mfma_f32_16x16x32_bf16 v[132:135], a[192:195], v[68:71], v[132:135]\n\tds_read_b128 v[100:103], v1 offset:36992\n\tv_mfma_f32_16x16x32_bf16 v[136:139], a[192:195], v[72:75], v[136:139]\n\tds_read_b128 v[104:107], v1 offset:37504\n\tv_mfma_f32_16x16x32_bf16 v[136:139], a[196:199], v[68:71], v[136:139]\n\t" \
    "ds_read_b128 v[108:111], v1 offset:38016\n\tv_mfma_f32_16x16x32_bf16 v[140:143], a[192:195], v[76:79], v[140:143]\n\tds_read_b128 v[112:115], v1 offset:38528\n\tv_mfma_f32_16x16x32_bf16 v[140:143], a[196:199], v[72:75], v[140:143]\n\tds_read_b128 v[116:119], v1 offset:39040\n\tv_mfma_f32_16x16x32_bf16 v[140:143], a[200:203], v[68:71], v[140:143]\n\tds_read_b128 v[120:123], v1 offset:39552\n\tv_mfma_f32_16x16x32_bf16 v[144:147], a[192:195], v[80:83], v[144:147]\n\t" \
    "ds_read_b128 v[124:127], v1 offset:40064\n\tv_mfma_f32_16x16x32_bf16 v[144:147], a[196:199], v[76:79], v[144:147]\n\tds_read_b128 v[128:131], v1 offset:40576\n\tv_mfma_f32_16x16x32_bf16 v[144:147], a[200:203], v[72:75], v[144:147]\n\tv_mfma_f32_16x16x32_bf16 v[144:147], a[204:207], v[68:71], v[144:147]\n\tv_mfma_f32_16x16x32_bf16 v[148:151], a[192:195], v[84:87], v[148:151]\n\tv_mfma_f32_16x16x32_bf16 v[148:151], a[196:199], v[80:83], v[148:151]\n\tv_mfma_f32_16x16x32_bf16 v[148:151], a[200:203], v[76:79], v[148:151]\n\t" \
    "v_mfma_f32_16x16x32_bf16 v[148:151], a[204:207], v[72:75], v[148:151]\n\tv_mfma_f32_16x16x32_bf16 v[148:151], a[208:211], v[68:71], v[148:151]\n\tv_mfma_f32_16x16x32_bf16 v[152:155], a[192:195], v[88:91], v[152:155]\n\tv_mfma_f32_16x16x32_bf16 v[152:155], a[196:199], v[84:87], v[152:155]\n\tv_mfma_f32_16x16x32_bf16 v[152:155], a[200:203], v[80:83], v[152:155]\n\tv_mfma_f32_16x16x32_bf16 v[152:155], a[204:207], v[76:79], v[152:155]\n\tv_mfma_f32_16x16x32_bf16 v[152:155], a[208:211], v[72:75], v[152:155]\n\tv_mfma_f32_16x16x32_bf16 v[152:155], a[212:215], v[68:71], v[152:155]\n\t" \
    "buffer_load_dword v15, v7, s[16:19], s42 offen\n\tv_mfma_f32_16x16x32_bf16 v[156:159], a[192:195], v[92:95], v[156:159]\n\ts_add_u32 s43, s43, 1\n\tv_mfma_f32_16x16x32_bf16 v[156:159], a[196:199], v[88:91], v[156:159]\n\tv_readlane_b32 s42, v237, s43\n\ts_nop 4\n\tv_mfma_f32_16x16x32_bf16 v[156:159], a[200:203], v[84:87], v[156:159]\n\tv_mfma_f32_16x16x32_bf16 v[156:159], a[204:207], v[80:83], v[156:159]\n\t" \
    "v_mfma_f32_16x16x32_bf16 v[156:159], a[208:211], v[76:79], v[156:159]\n\tv_mfma_f32_16x16x32_bf16 v[156:159], a[212:215], v[72:75], v[156:159]\n\tv_mfma_f32_16x16x32_bf16 v[156:159], a[216:219], v[68:71], v[156:159]\n\tv_mfma_f32_16x16x32_bf16 v[160:163], a[192:195], v[96:99], v[160:163]\n\tv_mfma_f32_16x16x32_bf16 v[160:163], a[196:199], v[92:95], v[160:163]\n\tv_mfma_f32_16x16x32_bf16 v[160:163], a[200:203], v[88:91], v[160:163]\n\tv_mfma_f32_16x16x32_bf16 v[160:163], a[204:207], v[84:87], v[160:163]\n\tv_mfma_f32_16x16x32_bf16 v[160:163], a[208:211], v[80:83], v[160:163]\n\t" \
    "v_mfma_f32_16x16x32_bf16 v[160:163], a[212:215], v[76:79], v[160:163]\n\tv_mfma_f32_16x16x32_bf16 v[160:163], a[216:219], v[72:75], v[160:163]\n\tv_mfma_f32_16x16x32_bf16 v[160:163], a[220:223], v[68:71], v[160:163]\n\ts_waitcnt lgkmcnt(0)\n\tv_mfma_f32_16x16x32_bf16 v[160:163], a[224:227], v[128:131], v[160:163]\n\ts_waitcnt vmcnt(2)\n\tbuffer_store_dwordx2 v[248:249], off, s[8:11], s51 sc1\n\tds_read_b64 a[0:1], v0 offset:4032\n\t" \
    "v_mfma_f32_16x16x32_bf16 v[160:163], a[228:231], v[124:127], v[160:163]\n\tds_read_b64 a[2:3], v0 offset:4040\n\tv_mfma_f32_16x16x32_bf16 v[160:163], a[232:235], v[120:123], v[160:163]\n\tds_read_b64 a[4:5], v0 offset:3520\n\tv_mfma_f32_16x16x32_bf16 v[160:163], a[236:239], v[116:119], v[160:163]\n\tds_read_b64 a[6:7], v0 offset:3528\n\tv_mfma_f32_16x16x32_bf16 v[160:163], a[240:243], v[112:115], v[160:163]\n\tds_read_b64 a[8:9], v0 offset:3008\n\t" \
    "v_mfma_f32_16x16x32_bf16 v[160:163], a[244:247], v[108:111], v[160:163]\n\tds_read_b64 a[10:11], v0 offset:3016\n\tv_mfma_f32_16x16x32_bf16 v[160:163], a[248:251], v[104:107], v[160:163]\n\tds_read_b64 a[12:13], v0 offset:2496\n\tv_mfma_f32_16x16x32_bf16 v[160:163], a[252:255], v[100:103], v[160:163]\n\tds_read_b64 a[14:15], v0 offset:2504\n\tv_mfma_f32_16x16x32_bf16 v[156:159], a[224:227], v[124:127], v[156:159]\n\tds_read_b64 a[16:17], v0 offset:1984\n\t" \
    "v_mfma_f32_16x16x32_bf16 v[156:159], a[228:231], v[120:123], v[156:159]\n\tds_read_b64 a[18:19], v0 offset:1992\n\tv_mfma_f32_16x16x32_bf16 v[156:159], a[232:235], v[116:119], v[156:159]\n\tds_read_b64 a[20:21], v0 offset:1472\n\tv_mfma_f32_16x16x32_bf16 v[156:159], a[236:239], v[112:115], v[156:159]\n\tds_read_b64 a[22:23], v0 offset:1480\n\tv_mfma_f32_16x16x32_bf16 v[156:159], a[240:243], v[108:111], v[156:159]\n\tds_read_b64 a[24:25], v0 offset:960\n\t" \
    "v_mfma_f32_16x16x32_bf16 v[156:159], a[244:247], v[104:107], v[156:159]\n\tds_read_b64 a[26:27], v0 offset:968\n\tv_mfma_f32_16x16x32_bf16 v[156:159], a[248:251], v[100:103], v[156:159]\n\tds_read_b64 a[28:29], v0 offset:448\n\tv_mfma_f32_16x16x32_bf16 v[152:155], a[224:227], v[120:123], v[152:155]\n\tds_read_b64 a[30:31], v0 offset:456\n\tv_mfma_f32_16x16x32_bf16 v[152:155], a[228:231], v[116:119], v[152:155]\n\tds_read_b128 v[68:71], v1 offset:55936\n\t" \
    "v_mfma_f32_16x16x32_bf16 v[152:155], a[232:235], v[112:115], v[152:155]\n\tds_read_b128 v[72:75], v1 offset:56448\n\tv_mfma_f32_16x16x32_bf16 v[152:155], a[236:239], v[108:111], v[152:155]\n\tds_read_b128 v[76:79], v1 offset:56960\n\tv_mfma_f32_16x16x32_bf16 v[152:155], a[240:243], v[104:107], v[152:155]\n\tds_read_b128 v[80:83], v1 offset:57472\n\tv_mfma_f32_16x16x32_bf16 v[152:155], a[244:247], v[100:103], v[152:155]\n\tds_read_b128 v[84:87], v1 offset:57984\n\t" \
    "v_mfma_f32_16x16x32_bf16 v[148:151], a[224:227], v[116:119], v[148:151]\n\tds_read_b128 v[88:91], v1 offset:58496\n\tv_mfma_f32_16x16x32_bf16 v[148:151], a[228:231], v[112:115], v[148:151]\n\tds_read_b128 v[92:95], v1 offset:59008\n\tv_mfma_f32_16x16x32_bf16 v[148:151], a[232:235], v[108:111], v[148:151]\n\tds_read_b128 v[96:99], v1 offset:59520\n\tv_mfma_f32_16x16x32_bf16 v[148:151], a[236:239], v[104:107], v[148:151]\n\tv_mfma_f32_16x16x32_bf16 v[148:151], a[240:243], v[100:103], v[148:151]\n\t" \
    "v_mfma_f32_16x16x32_bf16 v[144:147], a[224:227], v[112:115], v[144:147]\n\tv_mfma_f32_16x16x32_bf16 v[144:147], a[228:231], v[108:111], v[144:147]\n\tv_mfma_f32_16x16x32_bf16 v[144:147], a[232:235], v[104:107], v[144:147]\n\tv_mfma_f32_16x16x32_bf16 v[144:147], a[236:239], v[100:103], v[144:147]\n\tv_mfma_f32_16x16x32_bf16 v[140:143], a[224:227], v[108:111], v[140:143]\n\tv_mfma_f32_16x16x32_bf16 v[140:143], a[228:231], v[104:107], v[140:143]\n\tv_mfma_f32_16x16x32_bf16 v[140:143], a[232:235], v[100:103], v[140:143]\n\tv_mfma_f32_16x16x32_bf16 v[136:139], a[224:227], v[104:107], v[136:139]\n\t" \
    "v_mfma_f32_16x16x32_bf16 v[136:139], a[228:231], v[100:103], v[136:139]\n\tv_mfma_f32_16x16x32_bf16 v[132:135], a[224:227], v[100:103], v[132:135]\n\ts_waitcnt lgkmcnt(0)\n\tv_mfma_f32_16x16x32_bf16 v[164:167], v[228:231], v[68:71], 0\n\tds_read_b64 a[32:33], v0 offset:3968\n\tds_read_b64 a[34:35], v0 offset:3976\n\tv_mfma_f32_16x16x32_bf16 v[164:167], a[0:3], v[68:71], v[164:167]\n\tds_read_b64 a[36:37], v0 offset:3456\n\t" \
    "ds_read_b64 a[38:39], v0 offset:3464\n\tv_mfma_f32_16x16x32_bf16 v[168:171], v[228:231], v[72:75], 0\n\tds_read_b64 a[40:41], v0 offset:2944\n\tds_read_b64 a[42:43], v0 offset:2952\n\tv_mfma_f32_16x16x32_bf16 v[168:171], a[0:3], v[72:75], v[168:171]\n\tds_read_b64 a[44:45], v0 offset:2432\n\tds_read_b64 a[46:47], v0 offset:2440\n\tv_mfma_f32_16x16x32_bf16 v[168:171], a[4:7], v[68:71], v[168:171]\n\t" \
    "ds_read_b64 a[48:49], v0 offset:1920\n\tds_read_b64 a[50:51], v0 offset:1928\n\tv_mfma_f32_16x16x32_bf16 v[172:175], v[228:231], v[76:79], 0\n\tds_read_b64 a[52:53], v0 offset:1408\n\tv_cvt_pk_bf16_f32 v132, v132, v133\n\tv_mfma_f32_16x16x32_bf16 v[172:175], a[0:3], v[76:79], v[172:175]\n\tds_read_b64 a[54:55], v0 offset:1416\n\tv_cvt_pk_bf16_f32 v133, v134, v135\n\t" \
    "v_mfma_f32_16x16x32_bf16 v[172:175], a[4:7], v[72:75], v[172:175]\n\tds_read_b64 a[56:57], v0 offset:896\n\tds_write_b64 v3, v[132:133] offset:36992\n\tv_mfma_f32_16x16x32_bf16 v[172:175], a[8:11], v[68:71], v[172:175]\n\tds_read_b64 a[58:59], v0 offset:904\n\tv_cvt_pk_bf16_f32 v136, v136, v137\n\tv_mfma_f32_16x16x32_bf16 v[176:179], v[228:231], v[80:83], 0\n\tds_read_b64 a[60:61], v0 offset:384\n\t" \
    "v_cvt_pk_bf16_f32 v137, v138, v139\n\tv_mfma_f32_16x16x32_bf16 v[176:179], a[0:3], v[80:83], v[176:179]\n\tds_read_b64 a[62:63], v0 offset:392\n\tds_write_b64 v3, v[136:137] offset:37504\n\tv_mfma_f32_16x16x32_bf16 v[176:179], a[4:7], v[76:79], v[176:179]\n\tds_read_b128 v[100:103], v1 offset:55872\n\tv_cvt_pk_bf16_f32 v140, v140, v141\n\tv_mfma_f32_16x16x32_bf16 v[176:179], a[8:11], v[72:75], v[176:179]\n\t" \
    "ds_read_b128 v[104:107], v1 offset:56384\n\tv_cvt_pk_bf16_f32 v141, v142, v143\n\tv_mfma_f32_16x16x32_bf16 v[176:179], a[12:15], v[68:71], v[176:179]\n\tds_read_b128 v[108:111], v1 offset:56896\n\tds_write_b64 v3, v[140:141] offset:38016\n\tv_mfma_f32_16x16x32_bf16 v[180:183], v[228:231], v[84:87], 0\n\tds_read_b128 v[112:115], v1 offset:57408\n\tv_cvt_pk_bf16_f32 v144, v144, v145\n\t" \
    "v_mfma_f32_16x16x32_bf16 v[180:183], a[0:3], v[84:87], v[180:183]\n\tds_read_b128 v[116:119], v1 offset:57920\n\tv_cvt_pk_bf16_f32 v145, v146, v147\n\tv_mfma_f32_16x16x32_bf16 v[180:183], a[4:7], v[80:83], v[180:183]\n\tds_read_b128 v[120:123], v1 offset:58432\n\tds_write_b64 v3, v[144:145] offset:38528\n\tv_mfma_f32_16x16x32_bf16 v[180:183], a[8:11], v[76:79], v[180:183]\n\tds_read_b128 v[124:127], v1 offset:58944\n\t" \
    "v_cvt_pk_bf16_f32 v148, v148, v149\n\tv_mfma_f32_16x16x32_bf16 v[180:183], a[12:15], v[72:75], v[180:183]\n\tds_read_b128 v[128:131], v1 offset:59456\n\tv_cvt_pk_bf16_f32 v149, v150, v151\n\tv_mfma_f32_16x16x32_bf16 v[180:183], a[16:19], v[68:71], v[180:183]\n\tds_read_b64 a[64:65], v0 offset:3904\n\tds_write_b64 v3, v[148:149] offset:39040\n\tv_mfma_f32_16x16x32_bf16 v[184:187], v[228:231], v[88:91], 0\n\t" \
    "ds_read_b64 a[66:67], v0 offset:3912\n\tv_cvt_pk_bf16_f32 v152, v152, v153\n\tv_mfma_f32_16x16x32_bf16 v[184:187], a[0:3], v[88:91], v[184:187]\n\tds_read_b64 a[68:69], v0 offset:3392\n\tv_cvt_pk_bf16_f32 v153, v154, v155\n\tv_mfma_f32_16x16x32_bf16 v[184:187], a[4:7], v[84:87], v[184:187]\n\tds_read_b64 a[70:71], v0 offset:3400\n\tds_write_b64 v3, v[152:153] offset:39552\n\t" \
    "v_mfma_f32_16x16x32_bf16 v[184:187], a[8:11], v[80:83], v[184:187]\n\tds_read_b64 a[72:73], v0 offset:2880\n\tv_cvt_pk_bf16_f32 v156, v156, v157\n\tv_mfma_f32_16x16x32_bf16 v[184:187], a[12:15], v[76:79], v[184:187]\n\tds_read_b64 a[74:75], v0 offset:2888\n\tv_cvt_pk_bf16_f32 v157, v158, v159\n\tv_mfma_f32_16x16x32_bf16 v[184:187], a[16:19], v[72:75], v[184:187]\n\tds_read_b64 a[76:77], v0 offset:2368\n\t" \
    "ds_write_b64 v3, v[156:157] offset:40064\n\tv_mfma_f32_16x16x32_bf16 v[184:187], a[20:23], v[68:71], v[184:187]\n\tds_read_b64 a[78:79], v0 offset:2376\n\tv_cvt_pk_bf16_f32 v160, v160, v161\n\tv_mfma_f32_16x16x32_bf16 v[188:191], v[228:231], v[92:95], 0\n\tds_read_b64 a[80:81], v0 offset:1856\n\tv_cvt_pk_bf16_f32 v161, v162, v163\n\tv_mfma_f32_16x16x32_bf16 v[188:191], a[0:3], v[92:95], v[188:191]\n\t" \
    "ds_read_b64 a[82:83], v0 offset:1864\n\tds_write_b64 v3, v[160:161] offset:40576\n\tv_mfma_f32_16x16x32_bf16 v[188:191], a[4:7], v[88:91], v[188:191]\n\tds_read_b64 a[84:85], v0 offset:1344\n\tv_mfma_f32_16x16x32_bf16 v[188:191], a[8:11], v[84:87], v[188:191]\n\tds_read_b64 a[86:87], v0 offset:1352\n\tv_mfma_f32_16x16x32_bf16 v[188:191], a[12:15], v[80:83], v[188:191]\n\tds_read_b64 a[88:89], v0 offset:832\n\t" \
    "v_mfma_f32_16x16x32_bf16 v[188:191], a[16:19], v[76:79], v[188:191]\n\tds_read_b64 a[90:91], v0 offset:840\n\tv_mfma_f32_16x16x32_bf16 v[188:191], a[20:23], v[72:75], v[188:191]\n\tds_read_b64 a[92:93], v0 offset:320\n\tv_mfma_f32_16x16x32_bf16 v[188:191], a[24:27], v[68:71], v[188:191]\n\tds_read_b64 a[94:95], v0 offset:328\n\tv_mfma_f32_16x16x32_bf16 v[192:195], v[228:231], v[96:99], 0\n\tds_read_b64 a[96:97], v0 offset:3840\n\t" \
    "v_mfma_f32_16x16x32_bf16 v[192:195], a[0:3], v[96:99], v[192:195]\n\tds_read_b64 a[98:99], v0 offset:3848\n\tv_mfma_f32_16x16x32_bf16 v[192:195], a[4:7], v[92:95], v[192:195]\n\tds_read_b64 a[100:101], v0 offset:3328\n\tv_mfma_f32_16x16x32_bf16 v[192:195], a[8:11], v[88:91], v[192:195]\n\tds_read_b64 a[102:103], v0 offset:3336\n\tv_mfma_f32_16x16x32_bf16 v[192:195], a[12:15], v[84:87], v[192:195]\n\tv_mfma_f32_16x16x32_bf16 v[192:195], a[16:19], v[80:83], v[192:195]\n\t" \
    "v_mfma_f32_16x16x32_bf16 v[192:195], a[20:23], v[76:79], v[192:195]\n\tv_mfma_f32_16x16x32_bf16 v[192:195], a[24:27], v[72:75], v[192:195]\n\tv_mfma_f32_16x16x32_bf16 v[192:195], a[28:31], v[68:71], v[192:195]\n\ts_waitcnt lgkmcnt(10)\n\ts_barrier\n\tv_mfma_f32_16x16x32_bf16 v[192:195], a[32:35], v[128:131], v[192:195]\n\tds_read_b64 a[104:105], v0 offset:2816\n\tds_read_b64 a[106:107], v0 offset:2824\n\t" \
    "v_mfma_f32_16x16x32_bf16 v[192:195], a[36:39], v[124:127], v[192:195]\n\tds_read_b64 a[108:109], v0 offset:2304\n\tds_read_b64 a[110:111], v0 offset:2312\n\tv_mfma_f32_16x16x32_bf16 v[192:195], a[40:43], v[120:123], v[192:195]\n\tds_read_b64 a[112:113], v0 offset:1792\n\tds_read_b64 a[114:115], v0 offset:1800\n\tv_mfma_f32_16x16x32_bf16 v[192:195], a[44:47], v[116:119], v[192:195]\n\tds_read_b64 a[116:117], v0 offset:1280\n\t" \
    "ds_read_b64 a[118:119], v0 offset:1288\n\tv_mfma_f32_16x16x32_bf16 v[192:195], a[48:51], v[112:115], v[192:195]\n\tds_read_b64 a[120:121], v0 offset:768\n\tds_read_b64 a[122:123], v0 offset:776\n\tv_mfma_f32_16x16x32_bf16 v[192:195], a[52:55], v[108:111], v[192:195]\n\tds_read_b64 a[124:125], v0 offset:256\n\ts_add_u32 s51, s44, 8\n\tv_mfma_f32_16x16x32_bf16 v[192:195], a[56:59], v[104:107], v[192:195]\n\t" \
    "ds_read_b64 a[126:127], v0 offset:264\n\ts_mul_i32 s65, s51, 1366\n\tv_mfma_f32_16x16x32_bf16 v[192:195], a[60:63], v[100:103], v[192:195]\n\tds_read_b64 a[128:129], v0 offset:3776\n\ts_lshr_b32 s65, s65, 16\n\tv_mfma_f32_16x16x32_bf16 v[188:191], a[32:35], v[124:127], v[188:191]\n\tds_read_b64 a[130:131], v0 offset:3784\n\ts_mul_i32 s56, s65, 48\n\t" \
    "v_mfma_f32_16x16x32_bf16 v[188:191], a[36:39], v[120:123], v[188:191]\n\tds_read_b64 a[132:133], v0 offset:3264\n\ts_sub_u32 s56, s51, s56\n\tv_mfma_f32_16x16x32_bf16 v[188:191], a[40:43], v[116:119], v[188:191]\n\tds_read_b64 a[134:135], v0 offset:3272\n\ts_lshl_b32 s56, s56, 6\n\tv_mfma_f32_16x16x32_bf16 v[188:191], a[44:47], v[112:115], v[188:191]\n\tds_read_b128 v[68:71], v1 offset:55808\n\t" \
    "s_lshr_b32 s70, s65, 2\n\tv_mfma_f32_16x16x32_bf16 v[188:191], a[48:51], v[108:111], v[188:191]\n\tds_read_b128 v[72:75], v1 offset:56320\n\ts_mul_i32 s70, s70, 252\n\tv_mfma_f32_16x16x32_bf16 v[188:191], a[52:55], v[104:107], v[188:191]\n\tds_read_b128 v[76:79], v1 offset:56832\n\ts_add_u32 s65, s65, s70\n\tv_mfma_f32_16x16x32_bf16 v[188:191], a[56:59], v[100:103], v[188:191]\n\t" \
    "ds_read_b128 v[80:83], v1 offset:57344\n\ts_mul_i32 s65, s65, 3072\n\tv_mfma_f32_16x16x32_bf16 v[184:187], a[32:35], v[120:123], v[184:187]\n\tds_read_b128 v[84:87], v1 offset:57856\n\ts_add_u32 s56, s56, s65\n\tv_mfma_f32_16x16x32_bf16 v[184:187], a[36:39], v[116:119], v[184:187]\n\tds_read_b128 v[88:91], v1 offset:58368\n\ts_add_u32 s56, s56, 4849664\n\t" \
    "v_mfma_f32_16x16x32_bf16 v[184:187], a[40:43], v[112:115], v[184:187]\n\tds_read_b128 v[92:95], v1 offset:58880\n\ts_add_u32 s51, s51, s38\n\tv_mfma_f32_16x16x32_bf16 v[184:187], a[44:47], v[108:111], v[184:187]\n\tds_read_b128 v[96:99], v1 offset:59392\n\ts_lshl_b32 s51, s51, 3\n\tv_mfma_f32_16x16x32_bf16 v[184:187], a[48:51], v[104:107], v[184:187]\n\tds_read_b64 a[136:137], v0 offset:2752\n\t" \
    "s_add_u32 s51, s51, 50176\n\tds_read_b64_tr_b16 v[204:205], v4 offset:36992\n\tv_mfma_f32_16x16x32_bf16 v[184:187], a[52:55], v[100:103], v[184:187]\n\tds_read_b64 a[138:139], v0 offset:2760\n\tds_read_b64_tr_b16 v[206:207], v4 offset:37000\n\tv_mfma_f32_16x16x32_bf16 v[180:183], a[32:35], v[116:119], v[180:183]\n\tds_read_b64 a[140:141], v0 offset:2240\n\tds_read_b64_tr_b16 v[208:209], v4 offset:37056\n\t" \
    "v_mfma_f32_16x16x32_bf16 v[180:183], a[36:39], v[112:115], v[180:183]\n\tds_read_b64 a[142:143], v0 offset:2248\n\tds_read_b64_tr_b16 v[210:211], v4 offset:37064\n\tv_mfma_f32_16x16x32_bf16 v[180:183], a[40:43], v[108:111], v[180:183]\n\tds_read_b64 a[144:145], v0 offset:1728\n\tds_read_b64_tr_b16 v[212:213], v4 offset:37504\n\tv_mfma_f32_16x16x32_bf16 v[180:183], a[44:47], v[104:107], v[180:183]\n\tds_read_b64 a[146:147], v0 offset:1736\n\t" \
    "ds_read_b64_tr_b16 v[214:215], v4 offset:37512\n\tv_mfma_f32_16x16x32_bf16 v[180:183], a[48:51], v[100:103], v[180:183]\n\tds_read_b64 a[148:149], v0 offset:1216\n\tds_read_b64_tr_b16 v[216:217], v4 offset:37568\n\tv_mfma_f32_16x16x32_bf16 v[176:179], a[32:35], v[112:115], v[176:179]\n\tds_read_b64 a[150:151], v0 offset:1224\n\tds_read_b64_tr_b16 v[218:219], v4 offset:37576\n\tv_mfma_f32_16x16x32_bf16 v[176:179], a[36:39], v[108:111], v[176:179]\n\t" \
    "ds_read_b64 a[152:153], v0 offset:704\n\tv_mfma_f32_16x16x32_bf16 v[176:179], a[40:43], v[104:107], v[176:179]\n\tds_read_b64 a[154:155], v0 offset:712\n\tv_mfma_f32_16x16x32_bf16 v[176:179], a[44:47], v[100:103], v[176:179]\n\tds_read_b64 a[156:157], v0 offset:192\n\tv_mfma_f32_16x16x32_bf16 v[172:175], a[32:35], v[108:111], v[172:175]\n\tds_read_b64 a[158:159], v0 offset:200\n\tv_mfma_f32_16x16x32_bf16 v[172:175], a[36:39], v[104:107], v[172:175]\n\t" \
    "v_mfma_f32_16x16x32_bf16 v[172:175], a[40:43], v[100:103], v[172:175]\n\ts_waitcnt lgkmcnt(15)\n\tbuffer_store_dwordx4 v[204:207], v5, s[8:11], s56 offen sc1\n\ts_add_u32 s56, s56, 49152\n\tv_mfma_f32_16x16x32_bf16 v[168:171], a[32:35], v[104:107], v[168:171]\n\tv_mfma_f32_16x16x32_bf16 v[168:171], a[36:39], v[100:103], v[168:171]\n\tv_mfma_f32_16x16x32_bf16 v[164:167], a[32:35], v[100:103], v[164:167]\n\tv_mfma_f32_16x16x32_bf16 v[164:167], a[64:67], v[68:71], v[164:167]\n\t" \
    "ds_read_b64 a[160:161], v0 offset:3712\n\tds_read_b64 a[162:163], v0 offset:3720\n\tv_mfma_f32_16x16x32_bf16 v[168:171], a[64:67], v[72:75], v[168:171]\n\tds_read_b64 a[164:165], v0 offset:3200\n\tds_read_b64 a[166:167], v0 offset:3208\n\tv_mfma_f32_16x16x32_bf16 v[168:171], a[68:71], v[68:71], v[168:171]\n\tds_read_b64 a[168:169], v0 offset:2688\n\tds_read_b64 a[170:171], v0 offset:2696\n\t" \
    "v_mfma_f32_16x16x32_bf16 v[172:175], a[64:67], v[76:79], v[172:175]\n\tds_read_b64 a[172:173], v0 offset:2176\n\tds_read_b64 a[174:175], v0 offset:2184\n\tv_mfma_f32_16x16x32_bf16 v[172:175], a[68:71], v[72:75], v[172:175]\n\tds_read_b64 a[176:177], v0 offset:1664\n\tds_read_b64 a[178:179], v0 offset:1672\n\tv_mfma_f32_16x16x32_bf16 v[172:175], a[72:75], v[68:71], v[172:175]\n\tds_read_b64 a[180:181], v0 offset:1152\n\t" \
    "v_mfma_f32_16x16x32_bf16 v[176:179], a[64:67], v[80:83], v[176:179]\n\tds_read_b64 a[182:183], v0 offset:1160\n\tv_mfma_f32_16x16x32_bf16 v[176:179], a[68:71], v[76:79], v[176:179]\n\tds_read_b64 a[184:185], v0 offset:640\n\tv_mfma_f32_16x16x32_bf16 v[176:179], a[72:75], v[72:75], v[176:179]\n\tbuffer_load_dword v15, v7, s[16:19], s42 offen\n\tds_read_b64 a[186:187], v0 offset:648\n\tv_mfma_f32_16x16x32_bf16 v[176:179], a[76:79], v[68:71], v[176:179]\n\t" \
    "s_add_u32 s43, s43, 1\n\tds_read_b64 a[188:189], v0 offset:128\n\tv_mfma_f32_16x16x32_bf16 v[180:183], a[64:67], v[84:87], v[180:183]\n\tv_readlane_b32 s42, v237, s43\n\ts_nop 4\n\tds_read_b64 a[190:191], v0 offset:136\n\tv_mfma_f32_16x16x32_bf16 v[180:183], a[68:71], v[80:83], v[180:183]\n\tds_read_b128 v[100:103], v1 offset:55744\n\t" \
    "v_mfma_f32_16x16x32_bf16 v[180:183], a[72:75], v[76:79], v[180:183]\n\tds_read_b128 v[104:107], v1 offset:56256\n\tv_mfma_f32_16x16x32_bf16 v[180:183], a[76:79], v[72:75], v[180:183]\n\tds_read_b128 v[108:111], v1 offset:56768\n\tv_mfma_f32_16x16x32_bf16 v[180:183], a[80:83], v[68:71], v[180:183]\n\tds_read_b128 v[112:115], v1 offset:57280\n\tv_mfma_f32_16x16x32_bf16 v[184:187], a[64:67], v[88:91], v[184:187]\n\tds_read_b128 v[116:119], v1 offset:57792\n\t" \
    "s_waitcnt lgkmcnt(15)\n\tbuffer_store_dwordx4 v[208:211], v5, s[8:11], s56 offen sc1\n\tv_mfma_f32_16x16x32_bf16 v[184:187], a[68:71], v[84:87], v[184:187]\n\tds_read_b128 v[120:123], v1 offset:58304\n\ts_add_u32 s56, s56, 49152\n\tv_mfma_f32_16x16x32_bf16 v[184:187], a[72:75], v[80:83], v[184:187]\n\tds_read_b128 v[124:127], v1 offset:58816\n\tv_mfma_f32_16x16x32_bf16 v[184:187], a[76:79], v[76:79], v[184:187]\n\t" \
    "ds_read_b128 v[128:131], v1 offset:59328\n\tv_mfma_f32_16x16x32_bf16 v[184:187], a[80:83], v[72:75], v[184:187]\n\tds_read_b64 a[192:193], v0 offset:3648\n\tv_mfma_f32_16x16x32_bf16 v[184:187], a[84:87], v[68:71], v[184:187]\n\tds_read_b64 a[194:195], v0 offset:3656\n\tv_mfma_f32_16x16x32_bf16 v[188:191], a[64:67], v[92:95], v[188:191]\n\tds_read_b64 a[196:197], v0 offset:3136\n\tv_mfma_f32_16x16x32_bf16 v[188:191], a[68:71], v[88:91], v[188:191]\n\t" \
    "ds_read_b64 a[198:199], v0 offset:3144\n\tv_mfma_f32_16x16x32_bf16 v[188:191], a[72:75], v[84:87], v[188:191]\n\tds_read_b64 a[200:201], v0 offset:2624\n\tv_mfma_f32_16x16x32_bf16 v[188:191], a[76:79], v[80:83], v[188:191]\n\tds_read_b64 a[202:203], v0 offset:2632\n\tv_mfma_f32_16x16x32_bf16 v[188:191], a[80:83], v[76:79], v[188:191]\n\tds_read_b64 a[204:205], v0 offset:2112\n\tv_mfma_f32_16x16x32_bf16 v[188:191], a[84:87], v[72:75], v[188:191]\n\t" \
    "ds_read_b64 a[206:207], v0 offset:2120\n\tv_mfma_f32_16x16x32_bf16 v[188:191], a[88:91], v[68:71], v[188:191]\n\tds_read_b64 a[208:209], v0 offset:1600\n\tv_mfma_f32_16x16x32_bf16 v[192:195], a[64:67], v[96:99], v[192:195]\n\tds_read_b64 a[210:211], v0 offset:1608\n\tv_mfma_f32_16x16x32_bf16 v[192:195], a[68:71], v[92:95], v[192:195]\n\tds_read_b64 a[212:213], v0 offset:1088\n\tv_mfma_f32_16x16x32_bf16 v[192:195], a[72:75], v[88:91], v[192:195]\n\t" \
    "ds_read_b64 a[214:215], v0 offset:1096\n\tv_mfma_f32_16x16x32_bf16 v[192:195], a[76:79], v[84:87], v[192:195]\n\tbuffer_store_dwordx4 v[212:215], v5, s[8:11], s56 offen sc1\n\tv_mfma_f32_16x16x32_bf16 v[192:195], a[80:83], v[80:83], v[192:195]\n\ts_add_u32 s56, s56, 49152\n\tv_mfma_f32_16x16x32_bf16 v[192:195], a[84:87], v[76:79], v[192:195]\n\tv_mfma_f32_16x16x32_bf16 v[192:195], a[88:91], v[72:75], v[192:195]\n\tv_mfma_f32_16x16x32_bf16 v[192:195], a[92:95], v[68:71], v[192:195]\n\t" \
    "s_waitcnt lgkmcnt(12)\n\tv_mfma_f32_16x16x32_bf16 v[192:195], a[96:99], v[128:131], v[192:195]\n\tds_read_b64 a[216:217], v0 offset:576\n\tv_mfma_f32_16x16x32_bf16 v[192:195], a[100:103], v[124:127], v[192:195]\n\tds_read_b64 a[218:219], v0 offset:584\n\tv_mfma_f32_16x16x32_bf16 v[192:195], a[104:107], v[120:123], v[192:195]\n\tds_read_b64 a[220:221], v0 offset:64\n\tv_mfma_f32_16x16x32_bf16 v[192:195], a[108:111], v[116:119], v[192:195]\n\t" \
    "ds_read_b64 a[222:223], v0 offset:72\n\tv_mfma_f32_16x16x32_bf16 v[192:195], a[112:115], v[112:115], v[192:195]\n\tds_read_b64 a[224:225], v0 offset:3584\n\tv_mfma_f32_16x16x32_bf16 v[192:195], a[116:119], v[108:111], v[192:195]\n\tds_read_b64 a[226:227], v0 offset:3592\n\tv_mfma_f32_16x16x32_bf16 v[192:195], a[120:123], v[104:107], v[192:195]\n\tds_read_b64 a[228:229], v0 offset:3072\n\tv_mfma_f32_16x16x32_bf16 v[192:195], a[124:127], v[100:103], v[192:195]\n\t" \
    "ds_read_b64 a[230:231], v0 offset:3080\n\tv_mfma_f32_16x16x32_bf16 v[188:191], a[96:99], v[124:127], v[188:191]\n\tds_read_b64 a[232:233], v0 offset:2560\n\tbuffer_store_dwordx4 v[216:219], v5, s[8:11], s56 offen sc1\n\tv_mfma_f32_16x16x32_bf16 v[188:191], a[100:103], v[120:123], v[188:191]\n\tds_read_b64 a[234:235], v0 offset:2568\n\ts_add_u32 s56, s56, 49152\n\tv_mfma_f32_16x16x32_bf16 v[188:191], a[104:107], v[116:119], v[188:191]\n\t" \
    "ds_read_b64 a[236:237], v0 offset:2048\n\tv_mfma_f32_16x16x32_bf16 v[188:191], a[108:111], v[112:115], v[188:191]\n\tds_read_b64 a[238:239], v0 offset:2056\n\tv_mfma_f32_16x16x32_bf16 v[188:191], a[112:115], v[108:111], v[188:191]\n\tds_read_b64 a[240:241], v0 offset:1536\n\tv_mfma_f32_16x16x32_bf16 v[188:191], a[116:119], v[104:107], v[188:191]\n\tds_read_b64 a[242:243], v0 offset:1544\n\tv_mfma_f32_16x16x32_bf16 v[188:191], a[120:123], v[100:103], v[188:191]\n\t" \
    "ds_read_b64 a[244:245], v0 offset:1024\n\tv_mfma_f32_16x16x32_bf16 v[184:187], a[96:99], v[120:123], v[184:187]\n\tds_read_b64 a[246:247], v0 offset:1032\n\tv_mfma_f32_16x16x32_bf16 v[184:187], a[100:103], v[116:119], v[184:187]\n\tds_read_b128 v[68:71], v1 offset:55680\n\tv_mfma_f32_16x16x32_bf16 v[184:187], a[104:107], v[112:115], v[184:187]\n\tds_read_b128 v[72:75], v1 offset:56192\n\tv_mfma_f32_16x16x32_bf16 v[184:187], a[108:111], v[108:111], v[184:187]\n\t" \
    "ds_read_b128 v[76:79], v1 offset:56704\n\tv_mfma_f32_16x16x32_bf16 v[184:187], a[112:115], v[104:107], v[184:187]\n\tds_read_b128 v[80:83], v1 offset:57216\n\tv_mfma_f32_16x16x32_bf16 v[184:187], a[116:119], v[100:103], v[184:187]\n\tds_read_b128 v[84:87], v1 offset:57728\n\tv_mfma_f32_16x16x32_bf16 v[180:183], a[96:99], v[116:119], v[180:183]\n\tds_read_b128 v[88:91], v1 offset:58240\n\tv_mfma_f32_16x16x32_bf16 v[180:183], a[100:103], v[112:115], v[180:183]\n\t" \
    "ds_read_b128 v[92:95], v1 offset:58752\n\tv_mfma_f32_16x16x32_bf16 v[180:183], a[104:107], v[108:111], v[180:183]\n\tds_read_b128 v[96:99], v1 offset:59264\n\tv_mfma_f32_16x16x32_bf16 v[180:183], a[108:111], v[104:107], v[180:183]\n\tds_read_b64 a[248:249], v0 offset:512\n\tv_mfma_f32_16x16x32_bf16 v[180:183], a[112:115], v[100:103], v[180:183]\n\tds_read_b64 a[250:251], v0 offset:520\n\tv_mfma_f32_16x16x32_bf16 v[176:179], a[96:99], v[112:115], v[176:179]\n\t" \
    "ds_read_b64 a[252:253], v0 offset:0\n\tv_mfma_f32_16x16x32_bf16 v[176:179], a[100:103], v[108:111], v[176:179]\n\tds_read_b64 a[254:255], v0 offset:8\n\tv_mfma_f32_16x16x32_bf16 v[176:179], a[104:107], v[104:107], v[176:179]\n\tv_mfma_f32_16x16x32_bf16 v[176:179], a[108:111], v[100:103], v[176:179]\n\tv_mfma_f32_16x16x32_bf16 v[172:175], a[96:99], v[108:111], v[172:175]\n\tv_mfma_f32_16x16x32_bf16 v[172:175], a[100:103], v[104:107], v[172:175]\n\tv_mfma_f32_16x16x32_bf16 v[172:175], a[104:107], v[100:103], v[172:175]\n\t" \
    "v_mfma_f32_16x16x32_bf16 v[168:171], a[96:99], v[104:107], v[168:171]\n\tv_mfma_f32_16x16x32_bf16 v[168:171], a[100:103], v[100:103], v[168:171]\n\tv_mfma_f32_16x16x32_bf16 v[164:167], a[96:99], v[100:103], v[164:167]\n\ts_waitcnt lgkmcnt(4)\n\tv_mfma_f32_16x16x32_bf16 v[164:167], a[128:131], v[68:71], v[164:167]\n\tds_read_b128 v[100:103], v1 offset:55616\n\tv_mfma_f32_16x16x32_bf16 v[168:171], a[128:131], v[72:75], v[168:171]\n\tds_read_b128 v[104:107], v1 offset:56128\n\t" \
    "v_mfma_f32_16x16x32_bf16 v[168:171], a[132:135], v[68:71], v[168:171]\n\tds_read_b128 v[108:111], v1 offset:56640\n\tv_mfma_f32_16x16x32_bf16 v[172:175], a[128:131], v[76:79], v[172:175]\n\tds_read_b128 v[112:115], v1 offset:57152\n\tv_mfma_f32_16x16x32_bf16 v[172:175], a[132:135], v[72:75], v[172:175]\n\tds_read_b128 v[116:119], v1 offset:57664\n\tv_mfma_f32_16x16x32_bf16 v[172:175], a[136:139], v[68:71], v[172:175]\n\tds_read_b128 v[120:123], v1 offset:58176\n\t" \
    "v_mfma_f32_16x16x32_bf16 v[176:179], a[128:131], v[80:83], v[176:179]\n\tds_read_b128 v[124:127], v1 offset:58688\n\tv_mfma_f32_16x16x32_bf16 v[176:179], a[132:135], v[76:79], v[176:179]\n\tds_read_b128 v[128:131], v1 offset:59200\n\tv_mfma_f32_16x16x32_bf16 v[176:179], a[136:139], v[72:75], v[176:179]\n\tv_mfma_f32_16x16x32_bf16 v[176:179], a[140:143], v[68:71], v[176:179]\n\tv_mfma_f32_16x16x32_bf16 v[180:183], a[128:131], v[84:87], v[180:183]\n\tv_mfma_f32_16x16x32_bf16 v[180:183], a[132:135], v[80:83], v[180:183]\n\t" \
    "v_mfma_f32_16x16x32_bf16 v[180:183], a[136:139], v[76:79], v[180:183]\n\tv_mfma_f32_16x16x32_bf16 v[180:183], a[140:143], v[72:75], v[180:183]\n\tv_mfma_f32_16x16x32_bf16 v[180:183], a[144:147], v[68:71], v[180:183]\n\tv_mfma_f32_16x16x32_bf16 v[184:187], a[128:131], v[88:91], v[184:187]\n\tv_mfma_f32_16x16x32_bf16 v[184:187], a[132:135], v[84:87], v[184:187]\n\tv_mfma_f32_16x16x32_bf16 v[184:187], a[136:139], v[80:83], v[184:187]\n\tv_mfma_f32_16x16x32_bf16 v[184:187], a[140:143], v[76:79], v[184:187]\n\tv_mfma_f32_16x16x32_bf16 v[184:187], a[144:147], v[72:75], v[184:187]\n\t" \
    "v_mfma_f32_16x16x32_bf16 v[184:187], a[148:151], v[68:71], v[184:187]\n\tv_mfma_f32_16x16x32_bf16 v[188:191], a[128:131], v[92:95], v[188:191]\n\tv_mfma_f32_16x16x32_bf16 v[188:191], a[132:135], v[88:91], v[188:191]\n\tv_mfma_f32_16x16x32_bf16 v[188:191], a[136:139], v[84:87], v[188:191]\n\tv_mfma_f32_16x16x32_bf16 v[188:191], a[140:143], v[80:83], v[188:191]\n\tv_mfma_f32_16x16x32_bf16 v[188:191], a[144:147], v[76:79], v[188:191]\n\tv_mfma_f32_16x16x32_bf16 v[188:191], a[148:151], v[72:75], v[188:191]\n\tv_mfma_f32_16x16x32_bf16 v[188:191], a[152:155], v[68:71], v[188:191]\n\t" \
    "v_mfma_f32_16x16x32_bf16 v[192:195], a[128:131], v[96:99], v[192:195]\n\tv_mfma_f32_16x16x32_bf16 v[192:195], a[132:135], v[92:95], v[192:195]\n\tv_mfma_f32_16x16x32_bf16 v[192:195], a[136:139], v[88:91], v[192:195]\n\tv_mfma_f32_16x16x32_bf16 v[192:195], a[140:143], v[84:87], v[192:195]\n\tv_mfma_f32_16x16x32_bf16 v[192:195], a[144:147], v[80:83], v[192:195]\n\tv_mfma_f32_16x16x32_bf16 v[192:195], a[148:151], v[76:79], v[192:195]\n\tv_mfma_f32_16x16x32_bf16 v[192:195], a[152:155], v[72:75], v[192:195]\n\tv_mfma_f32_16x16x32_bf16 v[192:195], a[156:159], v[68:71], v[192:195]\n\t" \
    "s_waitcnt lgkmcnt(0)\n\tv_mfma_f32_16x16x32_bf16 v[192:195], a[160:163], v[128:131], v[192:195]\n\tds_read_b128 v[68:71], v1 offset:55552\n\tv_mfma_f32_16x16x32_bf16 v[192:195], a[164:167], v[124:127], v[192:195]\n\tds_read_b128 v[72:75], v1 offset:56064\n\tv_mfma_f32_16x16x32_bf16 v[192:195], a[168:171], v[120:123], v[192:195]\n\tds_read_b128 v[76:79], v1 offset:56576\n\tv_mfma_f32_16x16x32_bf16 v[192:195], a[172:175], v[116:119], v[192:195]\n\t" \
    "ds_read_b128 v[80:83], v1 offset:57088\n\tv_mfma_f32_16x16x32_bf16 v[192:195], a[176:179], v[112:115], v[192:195]\n\tds_read_b128 v[84:87], v1 offset:57600\n\tv_mfma_f32_16x16x32_bf16 v[192:195], a[180:183], v[108:111], v[192:195]\n\tds_read_b128 v[88:91], v1 offset:58112\n\tv_mfma_f32_16x16x32_bf16 v[192:195], a[184:187], v[104:107], v[192:195]\n\tds_read_b128 v[92:95], v1 offset:58624\n\tv_mfma_f32_16x16x32_bf16 v[192:195], a[188:191], v[100:103], v[192:195]\n\t" \
    "ds_read_b128 v[96:99], v1 offset:59136\n\tv_mfma_f32_16x16x32_bf16 v[188:191], a[160:163], v[124:127], v[188:191]\n\tv_mfma_f32_16x16x32_bf16 v[188:191], a[164:167], v[120:123], v[188:191]\n\tv_mfma_f32_16x16x32_bf16 v[188:191], a[168:171], v[116:119], v[188:191]\n\tv_mfma_f32_16x16x32_bf16 v[188:191], a[172:175], v[112:115], v[188:191]\n\tv_mfma_f32_16x16x32_bf16 v[188:191], a[176:179], v[108:111], v[188:191]\n\tv_mfma_f32_16x16x32_bf16 v[188:191], a[180:183], v[104:107], v[188:191]\n\tv_mfma_f32_16x16x32_bf16 v[188:191], a[184:187], v[100:103], v[188:191]\n\t" \
    "v_mfma_f32_16x16x32_bf16 v[184:187], a[160:163], v[120:123], v[184:187]\n\tv_mfma_f32_16x16x32_bf16 v[184:187], a[164:167], v[116:119], v[184:187]\n\tv_mfma_f32_16x16x32_bf16 v[184:187], a[168:171], v[112:115], v[184:187]\n\tv_mfma_f32_16x16x32_bf16 v[184:187], a[172:175], v[108:111], v[184:187]\n\tv_mfma_f32_16x16x32_bf16 v[184:187], a[176:179], v[104:107], v[184:187]\n\tv_mfma_f32_16x16x32_bf16 v[184:187], a[180:183], v[100:103], v[184:187]\n\tv_mfma_f32_16x16x32_bf16 v[180:183], a[160:163], v[116:119], v[180:183]\n\tv_mfma_f32_16x16x32_bf16 v[180:183], a[164:167], v[112:115], v[180:183]\n\t" \
    "v_mfma_f32_16x16x32_bf16 v[180:183], a[168:171], v[108:111], v[180:183]\n\tv_mfma_f32_16x16x32_bf16 v[180:183], a[172:175], v[104:107], v[180:183]\n\tv_mfma_f32_16x16x32_bf16 v[180:183], a[176:179], v[100:103], v[180:183]\n\tv_mfma_f32_16x16x32_bf16 v[176:179], a[160:163], v[112:115], v[176:179]\n\tv_mfma_f32_16x16x32_bf16 v[176:179], a[164:167], v[108:111], v[176:179]\n\tv_mfma_f32_16x16x32_bf16 v[176:179], a[168:171], v[104:107], v[176:179]\n\tv_mfma_f32_16x16x32_bf16 v[176:179], a[172:175], v[100:103], v[176:179]\n\tv_mfma_f32_16x16x32_bf16 v[172:175], a[160:163], v[108:111], v[172:175]\n\t" \
    "v_mfma_f32_16x16x32_bf16 v[172:175], a[164:167], v[104:107], v[172:175]\n\tv_mfma_f32_16x16x32_bf16 v[172:175], a[168:171], v[100:103], v[172:175]\n\tv_mfma_f32_16x16x32_bf16 v[168:171], a[160:163], v[104:107], v[168:171]\n\tv_mfma_f32_16x16x32_bf16 v[168:171], a[164:167], v[100:103], v[168:171]\n\tv_mfma_f32_16x16x32_bf16 v[164:167], a[160:163], v[100:103], v[164:167]\n\ts_waitcnt lgkmcnt(0)\n\tv_mfma_f32_16x16x32_bf16 v[164:167], a[192:195], v[68:71], v[164:167]\n\tds_read_b128 v[100:103], v1 offset:55488\n\t" \
    "v_mfma_f32_16x16x32_bf16 v[168:171], a[192:195], v[72:75], v[168:171]\n\tds_read_b128 v[104:107], v1 offset:56000\n\tv_mfma_f32_16x16x32_bf16 v[168:171], a[196:199], v[68:71], v[168:171]\n\tds_read_b128 v[108:111], v1 offset:56512\n\tv_mfma_f32_16x16x32_bf16 v[172:175], a[192:195], v[76:79], v[172:175]\n\tds_read_b128 v[112:115], v1 offset:57024\n\tv_mfma_f32_16x16x32_bf16 v[172:175], a[196:199], v[72:75], v[172:175]\n\tds_read_b128 v[116:119], v1 offset:57536\n\t" \
    "v_mfma_f32_16x16x32_bf16 v[172:175], a[200:203], v[68:71], v[172:175]\n\tds_read_b128 v[120:123], v1 offset:58048\n\tv_mfma_f32_16x16x32_bf16 v[176:179], a[192:195], v[80:83], v[176:179]\n\tds_read_b128 v[124:127], v1 offset:58560\n\tv_mfma_f32_16x16x32_bf16 v[176:179], a[196:199], v[76:79], v[176:179]\n\tds_read_b128 v[128:131], v1 offset:59072\n\tv_mfma_f32_16x16x32_bf16 v[176:179], a[200:203], v[72:75], v[176:179]\n\tv_mfma_f32_16x16x32_bf16 v[176:179], a[204:207], v[68:71], v[176:179]\n\t" \
    "v_mfma_f32_16x16x32_bf16 v[180:183], a[192:195], v[84:87], v[180:183]\n\tv_mfma_f32_16x16x32_bf16 v[180:183], a[196:199], v[80:83], v[180:183]\n\tv_mfma_f32_16x16x32_bf16 v[180:183], a[200:203], v[76:79], v[180:183]\n\tv_mfma_f32_16x16x32_bf16 v[180:183], a[204:207], v[72:75], v[180:183]\n\tv_mfma_f32_16x16x32_bf16 v[180:183], a[208:211], v[68:71], v[180:183]\n\tv_mfma_f32_16x16x32_bf16 v[184:187], a[192:195], v[88:91], v[184:187]\n\tv_mfma_f32_16x16x32_bf16 v[184:187], a[196:199], v[84:87], v[184:187]\n\tv_mfma_f32_16x16x32_bf16 v[184:187], a[200:203], v[80:83], v[184:187]\n\t" \
    "v_mfma_f32_16x16x32_bf16 v[184:187], a[204:207], v[76:79], v[184:187]\n\tv_mfma_f32_16x16x32_bf16 v[184:187], a[208:211], v[72:75], v[184:187]\n\tv_mfma_f32_16x16x32_bf16 v[184:187], a[212:215], v[68:71], v[184:187]\n\tv_mfma_f32_16x16x32_bf16 v[188:191], a[192:195], v[92:95], v[188:191]\n\tv_mfma_f32_16x16x32_bf16 v[188:191], a[196:199], v[88:91], v[188:191]\n\tv_mfma_f32_16x16x32_bf16 v[188:191], a[200:203], v[84:87], v[188:191]\n\tv_mfma_f32_16x16x32_bf16 v[188:191], a[204:207], v[80:83], v[188:191]\n\tv_mfma_f32_16x16x32_bf16 v[188:191], a[208:211], v[76:79], v[188:191]\n\t" \
    "v_mfma_f32_16x16x32_bf16 v[188:191], a[212:215], v[72:75], v[188:191]\n\tv_mfma_f32_16x16x32_bf16 v[188:191], a[216:219], v[68:71], v[188:191]\n\tv_mfma_f32_16x16x32_bf16 v[192:195], a[192:195], v[96:99], v[192:195]\n\tv_mfma_f32_16x16x32_bf16 v[192:195], a[196:199], v[92:95], v[192:195]\n\tv_mfma_f32_16x16x32_bf16 v[192:195], a[200:203], v[88:91], v[192:195]\n\tv_mfma_f32_16x16x32_bf16 v[192:195], a[204:207], v[84:87], v[192:195]\n\tv_mfma_f32_16x16x32_bf16 v[192:195], a[208:211], v[80:83], v[192:195]\n\tv_mfma_f32_16x16x32_bf16 v[192:195], a[212:215], v[76:79], v[192:195]\n\t" \
    "v_mfma_f32_16x16x32_bf16 v[192:195], a[216:219], v[72:75], v[192:195]\n\tv_mfma_f32_16x16x32_bf16 v[192:195], a[220:223], v[68:71], v[192:195]\n\ts_waitcnt lgkmcnt(0)\n\tv_mfma_f32_16x16x32_bf16 v[192:195], a[224:227], v[128:131], v[192:195]\n\ts_waitcnt vmcnt(0)\n\tbuffer_store_dwordx2 v[248:249], off, s[8:11], s51 sc1\n\tv_mfma_f32_16x16x32_bf16 v[192:195], a[228:231], v[124:127], v[192:195]\n\tv_mfma_f32_16x16x32_bf16 v[192:195], a[232:235], v[120:123], v[192:195]\n\t" \
    "v_mfma_f32_16x16x32_bf16 v[192:195], a[236:239], v[116:119], v[192:195]\n\tv_mfma_f32_16x16x32_bf16 v[192:195], a[240:243], v[112:115], v[192:195]\n\tv_mfma_f32_16x16x32_bf16 v[192:195], a[244:247], v[108:111], v[192:195]\n\tv_mfma_f32_16x16x32_bf16 v[192:195], a[248:251], v[104:107], v[192:195]\n\tv_mfma_f32_16x16x32_bf16 v[192:195], a[252:255], v[100:103], v[192:195]\n\tv_mfma_f32_16x16x32_bf16 v[188:191], a[224:227], v[124:127], v[188:191]\n\tv_mfma_f32_16x16x32_bf16 v[188:191], a[228:231], v[120:123], v[188:191]\n\tv_mfma_f32_16x16x32_bf16 v[188:191], a[232:235], v[116:119], v[188:191]\n\t" \
    "v_mfma_f32_16x16x32_bf16 v[188:191], a[236:239], v[112:115], v[188:191]\n\tv_mfma_f32_16x16x32_bf16 v[188:191], a[240:243], v[108:111], v[188:191]\n\tv_mfma_f32_16x16x32_bf16 v[188:191], a[244:247], v[104:107], v[188:191]\n\tv_mfma_f32_16x16x32_bf16 v[188:191], a[248:251], v[100:103], v[188:191]\n\tv_mfma_f32_16x16x32_bf16 v[184:187], a[224:227], v[120:123], v[184:187]\n\tv_mfma_f32_16x16x32_bf16 v[184:187], a[228:231], v[116:119], v[184:187]\n\tv_mfma_f32_16x16x32_bf16 v[184:187], a[232:235], v[112:115], v[184:187]\n\tv_mfma_f32_16x16x32_bf16 v[184:187], a[236:239], v[108:111], v[184:187]\n\t" \
    "v_mfma_f32_16x16x32_bf16 v[184:187], a[240:243], v[104:107], v[184:187]\n\tv_mfma_f32_16x16x32_bf16 v[184:187], a[244:247], v[100:103], v[184:187]\n\tv_mfma_f32_16x16x32_bf16 v[180:183], a[224:227], v[116:119], v[180:183]\n\tv_mfma_f32_16x16x32_bf16 v[180:183], a[228:231], v[112:115], v[180:183]\n\tv_mfma_f32_16x16x32_bf16 v[180:183], a[232:235], v[108:111], v[180:183]\n\tv_mfma_f32_16x16x32_bf16 v[180:183], a[236:239], v[104:107], v[180:183]\n\tv_mfma_f32_16x16x32_bf16 v[180:183], a[240:243], v[100:103], v[180:183]\n\tv_mfma_f32_16x16x32_bf16 v[176:179], a[224:227], v[112:115], v[176:179]\n\t" \
    "v_mfma_f32_16x16x32_bf16 v[176:179], a[228:231], v[108:111], v[176:179]\n\tv_mfma_f32_16x16x32_bf16 v[176:179], a[232:235], v[104:107], v[176:179]\n\tv_mfma_f32_16x16x32_bf16 v[176:179], a[236:239], v[100:103], v[176:179]\n\tv_mfma_f32_16x16x32_bf16 v[172:175], a[224:227], v[108:111], v[172:175]\n\tv_mfma_f32_16x16x32_bf16 v[172:175], a[228:231], v[104:107], v[172:175]\n\tv_mfma_f32_16x16x32_bf16 v[172:175], a[232:235], v[100:103], v[172:175]\n\tv_mfma_f32_16x16x32_bf16 v[168:171], a[224:227], v[104:107], v[168:171]\n\tv_mfma_f32_16x16x32_bf16 v[168:171], a[228:231], v[100:103], v[168:171]\n\t" \
    "v_mfma_f32_16x16x32_bf16 v[164:167], a[224:227], v[100:103], v[164:167]\n\ts_nop 7\n\tv_cvt_pk_bf16_f32 v164, v164, v165\n\tv_cvt_pk_bf16_f32 v165, v166, v167\n\tds_write_b64 v3, v[164:165] offset:55488\n\tv_cvt_pk_bf16_f32 v168, v168, v169\n\tv_cvt_pk_bf16_f32 v169, v170, v171\n\tds_write_b64 v3, v[168:169] offset:56000\n\t" \
    "v_cvt_pk_bf16_f32 v172, v172, v173\n\tv_cvt_pk_bf16_f32 v173, v174, v175\n\tds_write_b64 v3, v[172:173] offset:56512\n\tv_cvt_pk_bf16_f32 v176, v176, v177\n\tv_cvt_pk_bf16_f32 v177, v178, v179\n\tds_write_b64 v3, v[176:177] offset:57024\n\tv_cvt_pk_bf16_f32 v180, v180, v181\n\tv_cvt_pk_bf16_f32 v181, v182, v183\n\t" \
    "ds_write_b64 v3, v[180:181] offset:57536\n\tv_cvt_pk_bf16_f32 v184, v184, v185\n\tv_cvt_pk_bf16_f32 v185, v186, v187\n\tds_write_b64 v3, v[184:185] offset:58048\n\tv_cvt_pk_bf16_f32 v188, v188, v189\n\tv_cvt_pk_bf16_f32 v189, v190, v191\n\tds_write_b64 v3, v[188:189] offset:58560\n\tv_cvt_pk_bf16_f32 v192, v192, v193\n\t" \
    "v_cvt_pk_bf16_f32 v193, v194, v195\n\tds_write_b64 v3, v[192:193] offset:59072\n\ts_waitcnt lgkmcnt(0)\n\ts_barrier\n\ts_add_u32 s51, s44, 12\n\ts_mul_i32 s65, s51, 1366\n\ts_lshr_b32 s65, s65, 16\n\ts_mul_i32 s56, s65, 48\n\t" \
    "s_sub_u32 s56, s51, s56\n\ts_lshl_b32 s56, s56, 6\n\ts_lshr_b32 s70, s65, 2\n\ts_mul_i32 s70, s70, 252\n\ts_add_u32 s65, s65, s70\n\ts_mul_i32 s65, s65, 3072\n\ts_add_u32 s56, s56, s65\n\ts_add_u32 s56, s56, 4849664\n\t" \
    "s_add_u32 s51, s51, s38\n\ts_lshl_b32 s51, s51, 3\n\ts_add_u32 s51, s51, 50176\n\ts_or_b32 s56, s56, s45\n\ts_or_b32 s51, s51, s45\n\tds_read_b64_tr_b16 v[204:205], v4 offset:55488\n\tds_read_b64_tr_b16 v[206:207], v4 offset:55496\n\tds_read_b64_tr_b16 v[208:209], v4 offset:55552\n\t" \
    "ds_read_b64_tr_b16 v[210:211], v4 offset:55560\n\tds_read_b64_tr_b16 v[212:213], v4 offset:56000\n\tds_read_b64_tr_b16 v[214:215], v4 offset:56008\n\tds_read_b64_tr_b16 v[216:217], v4 offset:56064\n\tds_read_b64_tr_b16 v[218:219], v4 offset:56072\n\ts_waitcnt lgkmcnt(6)\n\tbuffer_store_dwordx4 v[204:207], v5, s[8:11], s56 offen sc1\n\ts_add_u32 s56, s56, 49152\n\t" \
    "s_waitcnt lgkmcnt(4)\n\tbuffer_store_dwordx4 v[208:211], v5, s[8:11], s56 offen sc1\n\ts_add_u32 s56, s56, 49152\n\ts_waitcnt lgkmcnt(2)\n\tbuffer_store_dwordx4 v[212:215], v5, s[8:11], s56 offen sc1\n\ts_add_u32 s56, s56, 49152\n\ts_waitcnt lgkmcnt(0)\n\tbuffer_store_dwordx4 v[216:219], v5, s[8:11], s56 offen sc1\n\t" \
    "s_add_u32 s56, s56, 49152\n\ts_waitcnt vmcnt(0)\n\tbuffer_store_dwordx2 v[248:249], off, s[8:11], s51 sc1\n\ts_waitcnt vmcnt(0)\n\t103:\n\t98:\n\ts_nop 1\n\t" \
    ""
#define MAIN_CLOB "s2", "s3", "s4", "s5", "s6", "s7", "s8", "s9", "s10", "s11", "s12", "s13", "s14", "s15", "s16", "s17", "s18", "s19", "s20", "s21", "s22", "s23", "s24", "s25", "s26", "s27", "s28", "s29", "s30", "s31", "s33", "s34", "s35", "s36", "s37", "s39", "s41", "s42", "s43", "s44", "s45", "s46", "s47", "s48", "s49", "s50", "s51", "s52", "s53", "s54", "s55", "s56", "s57", "s58", "s59", "s60", "s61", "s62", "s63", "s64", "s65", "s66", "s67", "s68", "s69", "s70", "s71", "s72", "s73", "s74", "s75", "s76", "s77", "s78", "s79", "s80", "s81", "s82", "s83", "s84", "s85", "s86", "s87", "v0", "v1", "v2", "v3", "v4", "v5", "v6", "v7", "v8", "v9", "v10", "v11", "v12", "v13", "v14", "v15", "v16", "v17", "v18", "v19", "v20", "v21", "v22", "v23", "v24", "v25", "v26", "v27", "v28", "v29", "v30", "v31", "v32", "v33", "v34", "v35", "v36", "v37", "v38", "v39", "v40", "v41", "v42", "v43", "v44", "v45", "v46", "v47", "v48", "v49", "v50", "v51", "v52", "v53", "v54", "v55", "v56", "v57", "v58", "v59", "v60", "v61", "v62", "v63", "v64", "v65", "v66", "v67", "v68", "v69", "v70", "v71", "v72", "v73", "v74", "v75", "v76", "v77", "v78", "v79", "v80", "v81", "v82", "v83", "v84", "v85", "v86", "v87", "v88", "v89", "v90", "v91", "v92", "v93", "v94", "v95", "v96", "v97", "v98", "v99", "v100", "v101", "v102", "v103", "v104", "v105", "v106", "v107", "v108", "v109", "v110", "v111", "v112", "v113", "v114", "v115", "v116", "v117", "v118", "v119", "v120", "v121", "v122", "v123", "v124", "v125", "v126", "v127", "v128", "v129", "v130", "v131", "v132", "v133", "v134", "v135", "v136", "v137", "v138", "v139", "v140", "v141", "v142", "v143", "v144", "v145", "v146", "v147", "v148", "v149", "v150", "v151", "v152", "v153", "v154", "v155", "v156", "v157", "v158", "v159", "v160", "v161", "v162", "v163", "v164", "v165", "v166", "v167", "v168", "v169", "v170", "v171", "v172", "v173", "v174", "v175", "v176", "v177", "v178", "v179", "v180", "v181", "v182", "v183", "v184", "v185", "v186", "v187", "v188", "v189", "v190", "v191", "v192", "v193", "v194", "v195", "v196", "v197", "v198", "v199", "v200", "v201", "v202", "v203", "v204", "v205", "v206", "v207", "v208", "v209", "v210", "v211", "v212", "v213", "v214", "v215", "v216", "v217", "v218", "v219", "v220", "v221", "v222", "v223", "v224", "v225", "v226", "v227", "v228", "v229", "v230", "v231", "v232", "v233", "v234", "v235", "v236", "v237", "v238", "v239", "v241", "v242", "v243", "v244", "v245", "v246", "v247", "v248", "v249", "v250", "v251", "v252", "v253", "a0", "a1", "a2", "a3", "a4", "a5", "a6", "a7", "a8", "a9", "a10", "a11", "a12", "a13", "a14", "a15", "a16", "a17", "a18", "a19", "a20", "a21", "a22", "a23", "a24", "a25", "a26", "a27", "a28", "a29", "a30", "a31", "a32", "a33", "a34", "a35", "a36", "a37", "a38", "a39", "a40", "a41", "a42", "a43", "a44", "a45", "a46", "a47", "a48", "a49", "a50", "a51", "a52", "a53", "a54", "a55", "a56", "a57", "a58", "a59", "a60", "a61", "a62", "a63", "a64", "a65", "a66", "a67", "a68", "a69", "a70", "a71", "a72", "a73", "a74", "a75", "a76", "a77", "a78", "a79", "a80", "a81", "a82", "a83", "a84", "a85", "a86", "a87", "a88", "a89", "a90", "a91", "a92", "a93", "a94", "a95", "a96", "a97", "a98", "a99", "a100", "a101", "a102", "a103", "a104", "a105", "a106", "a107", "a108", "a109", "a110", "a111", "a112", "a113", "a114", "a115", "a116", "a117", "a118", "a119", "a120", "a121", "a122", "a123", "a124", "a125", "a126", "a127", "a128", "a129", "a130", "a131", "a132", "a133", "a134", "a135", "a136", "a137", "a138", "a139", "a140", "a141", "a142", "a143", "a144", "a145", "a146", "a147", "a148", "a149", "a150", "a151", "a152", "a153", "a154", "a155", "a156", "a157", "a158", "a159", "a160", "a161", "a162", "a163", "a164", "a165", "a166", "a167", "a168", "a169", "a170", "a171", "a172", "a173", "a174", "a175", "a176", "a177", "a178", "a179", "a180", "a181", "a182", "a183", "a184", "a185", "a186", "a187", "a188", "a189", "a190", "a191", "a192", "a193", "a194", "a195", "a196", "a197", "a198", "a199", "a200", "a201", "a202", "a203", "a204", "a205", "a206", "a207", "a208", "a209", "a210", "a211", "a212", "a213", "a214", "a215", "a216", "a217", "a218", "a219", "a220", "a221", "a222", "a223", "a224", "a225", "a226", "a227", "a228", "a229", "a230", "a231", "a232", "a233", "a234", "a235", "a236", "a237", "a238", "a239", "a240", "a241", "a242", "a243", "a244", "a245", "a246", "a247", "a248", "a249", "a250", "a251", "a252", "a253", "a254", "a255"

#define ARG(i) const void* p##i, unsigned n##i, unsigned f##i
__global__ __launch_bounds__(256, 1) void fused_kernel(ARG(0), ARG(1), ARG(2), ARG(3), ARG(4), ARG(5), ARG(6), ARG(7), ARG(8), ARG(9), ARG(10),
                                                       ARG(11)) {
#define MAIN_OUT
    const int b = blockIdx.x;
    unsigned tid = threadIdx.x;
    u64 ka = (u64)__builtin_amdgcn_kernarg_segment_ptr();
    int w;
    asm volatile(MAIN_ASM
                 : "=&{s38}"(w), "+{s[0:1]}"(ka), "+{v240}"(tid) MAIN_OUT
                 : "{s40}"(b)
                 : "memory", "scc", "vcc", MAIN_CLOB);
}

extern "C" void kernel_launch(void* const* d_in, const int* in_sizes, int n_in, void* d_out, int out_size,
                              void* d_ws, size_t ws_size, hipStream_t stream) {
    (void)hipFuncSetAttribute((const void*)fused_kernel, hipFuncAttributeMaxDynamicSharedMemorySize, LDS_BYTES);
    const unsigned F = 0x20000u, NX = S_LEN * D_DIM * 2, NW = S_LEN * F_DIM * 2;
    fused_kernel<<<256, 256, LDS_BYTES, stream>>>(d_ws, WS_END, F, d_in[7], NW, F, d_in[9], NW, F, d_in[0], NX, F, d_out, NX, F, d_in[8], F_DIM * 2, F,
                                                  d_in[10], S_LEN * 2, F, d_in[1], NX, F, d_in[2], NX, F, d_in[3], D_DIM * 8, F, d_in[4], NX, F,
                                                  d_in[5], D_DIM * 2, F);
}
```
